```python
import torch

def _softmax(x, axis=-1):
    return torch.softmax(x, dim=axis)
def _gelu(x):
    return torch.nn.functional.gelu(x, approximate="tanh")
def _silu(x):
    return torch.nn.functional.silu(x)
def _sigmoid(x):
    return torch.sigmoid(x)
def _layernorm(x, axis=-1, eps=1e-5):
    m = x.mean(dim=axis, keepdim=True)
    v = x.var(dim=axis, keepdim=True, unbiased=False)
    return (x - m) / torch.sqrt(v + eps)
def _scatter_add(tgt, idx, src, axis=0):
    return tgt.clone().index_add_(axis, idx, src)
def _segment_sum(src, seg, n):
    out = torch.zeros((n,) + src.shape[1:], dtype=src.dtype)
    return out.index_add_(0, seg, src)
def _pad(x, pw):
    flat = tuple(v for pair in reversed(pw) for v in pair)
    return torch.nn.functional.pad(x, flat)

kernel_name = 'standalone__modern_conv_block_convnext__vision'

def setup_inputs():

    _w = (lambda a: (lambda t: t.to(torch.bfloat16) if t.is_floating_point() else t)(torch.as_tensor(a)))
    H = W = 512
    C = 96
    K = 3
    return {
        "x": _w((lambda *s: torch.randn(*s, dtype=torch.bfloat16) * 0.05)(1, H, W, C)),
        "dw": _w((lambda *s: torch.randn(*s, dtype=torch.bfloat16) * 0.05)(K, K, C)),
        "w1": _w((lambda *s: torch.randn(*s, dtype=torch.bfloat16) * 0.05)(C, 4 * C)),
        "b1": _w((lambda *s: torch.randn(*s, dtype=torch.bfloat16) * 0.2)(4 * C)),
        "w2": _w((lambda *s: torch.randn(*s, dtype=torch.bfloat16) * 0.05)(4 * C, C)),
        "b2": _w((lambda *s: torch.randn(*s, dtype=torch.bfloat16) * 0.2)(C)),
        "gamma": _w((lambda *s: torch.randn(*s, dtype=torch.bfloat16) * 0.05)(C)),
    }

def reference(x, dw, w1, b1, w2, b2, gamma):
    B, H, W, C = x.shape
    K = dw.shape[0]
    pad = K // 2
    xp = _pad(x, ((0,0),(pad,pad),(pad,pad),(0,0)))
    out = torch.zeros_like(x)
    for i in range(K):
        for j in range(K):
            out = out + xp[:, i:i+H, j:j+W, :] * dw[i, j][None, None, None, :]
    h = _layernorm(out, axis=-1)
    h = torch.einsum('bhwc,ce->bhwe', h, w1) + b1
    h = _gelu(h)
    h = torch.einsum('bhwe,ec->bhwc', h, w2) + b2
    return x + gamma[None, None, None, :] * h
```

```cpp
#include <hip/hip_runtime.h>
#include <stdint.h>

constexpr int LDS_BYTES = 163584;

__device__ __forceinline__ float bf2f(unsigned short b) { return __uint_as_float((unsigned)b << 16); }
__device__ __forceinline__ unsigned short f2bf(float f) { unsigned r; asm("v_cvt_pk_bf16_f32 %0, %1, %1" : "=v"(r) : "v"(f)); return (unsigned short)r; }
__device__ __forceinline__ float rbf(float f) { return bf2f(f2bf(f)); }

__global__ __launch_bounds__(256, 1) __attribute__((aligned(4096))) void convnext_kernel(
    const unsigned short* __restrict__ x_, const unsigned short* __restrict__ dw_, const unsigned short* __restrict__ w1_,
    const unsigned short* __restrict__ b1_, const unsigned short* __restrict__ w2_, const unsigned short* __restrict__ b2_,
    const unsigned short* __restrict__ gamma_, unsigned short* __restrict__ out_) {
    const unsigned tid = threadIdx.x;
    unsigned long long ka = (unsigned long long)__builtin_amdgcn_kernarg_segment_ptr();
    asm volatile(
R"ASM(
s_load_dwordx16 s[20:35],%2,0
v_lshrrev_b32 v27,6,%1
v_and_b32 v24,63,%1
v_readfirstlane_b32 s52,v27
s_getpc_b64 s[14:15]
v_lshlrev_b32 v62,7,v24
global_load_dword v255,v62,s[14:15]
v_mul_u32_u24 v30,52,v27
v_add_u32 v30,v30,v24
v_subrev_u32 v29,4,v30
v_max_i32 v29,0xc0,v29
v_min_u32 v30,v30,v29
v_mul_u32_u24 v28,0xaaab,v30
s_and_b32 s64,%0,7
s_lshr_b32 s70,%0,3
v_lshrrev_b32 v28,19,v28
s_lshl_b32 s71,s64,6
s_mul_i32 s68,s70,3072
v_bfe_u32 v27,v28,2,2
s_sub_u32 s65,s71,1
s_mul_i32 s56,s65,98304
v_xor_b32 v27,v30,v27
s_add_u32 s56,s56,s68
s_cmp_lt_i32 s65,0
s_cselect_b32 s89,0x70000000,s56
v_lshlrev_b32 v27,4,v27
s_cmp_eq_u32 s70,0
s_cselect_b32 s67,0,99
s_cmp_eq_u32 s70,31
s_cselect_b32 s67,17,s67
v_mov_b32 v29,0x7fff0000
v_cmp_eq_u32_e32 vcc,s67,v28
s_mul_i32 s57,s52,832
s_add_u32 s57,s57,73728
v_cndmask_b32_e32 v6,v27,v29,vcc
s_mov_b32 s38,50331840
s_mov_b32 s39,0x00020000
s_mov_b32 s50,1728
s_mov_b32 s51,0x00020000
v_lshlrev_b32 v8,4,v24
v_min_u32 v28,191,%1
v_lshlrev_b32 v32,2,v28
v_min_u32 v34,95,%1
v_lshlrev_b32 v33,1,v34
s_waitcnt lgkmcnt(0)
s_sub_u32 s36,s20,192
s_subb_u32 s37,s21,0
s_and_b32 s37,s37,0xffff
s_add_u32 m0,s57,0
s_nop 0
buffer_load_dwordx4 v6,s[36:39],s89 offen lds
s_add_u32 m0,s57,3536
s_add_u32 s89,s56,98304
buffer_load_dwordx4 v6,s[36:39],s89 offen lds
s_add_u32 m0,s57,7080
s_add_u32 s89,s56,196608
buffer_load_dwordx4 v6,s[36:39],s89 offen lds
s_add_u32 m0,s57,10616
s_add_u32 s89,s56,294912
buffer_load_dwordx4 v6,s[36:39],s89 offen lds
s_mov_b32 s48,s22
s_and_b32 s49,s23,0xffff
s_mov_b32 m0,12288
global_load_ushort v37,v33,s[32:33]
global_load_dword v35,v32,s[26:27]
global_load_ushort v36,v33,s[30:31]
buffer_load_dwordx4 v8,s[48:51],0 offen offset:0 lds
buffer_load_dwordx4 v8,s[48:51],0 offen offset:1024 lds
v_and_b32 v27,15,v24
v_lshrrev_b32 v28,4,v24
s_mul_i32 s66,s52,192
v_min_u32 v27,11,v27
v_mul_u32_u24 v28,18432,v28
v_lshl_add_u32 v27,v27,4,s66
s_mul_i32 s66,s52,18432
v_add_u32 v172,v27,v28
v_add_u32 v173,s66,v8
global_load_dwordx4 v[160:163],v172,s[24:25]
global_load_dwordx4 v[164:167],v172,s[24:25] offset:3840
global_load_dwordx4 v[168:171],v173,s[28:29]
v_lshlrev_b32 v61,7,v24
s_bfe_u32 s64,%0,0x20003
s_lshl_b32 s65,s52,2
s_add_u32 s64,s64,s65
s_lshl_b32 s65,s64,13
v_add_u32 v62,s65,v61
s_getpc_b64 s[14:15]
N_pf_%=:
v_min_u32 v62,N_mainend_%=-N_pf_%=,v62
global_load_dword v255,v62,s[14:15]
s_and_b32 s64,s52,1
s_mul_i32 s65,s64,48
v_bfe_u32 v27,v24,3,2
v_and_b32 v28,4,v24
v_lshrrev_b32 v29,5,v24
v_add3_u32 v27,v27,v28,s65
v_lshl_add_u32 v27,v29,3,v27
v_mul_u32_u24 v27,768,v27
v_and_b32 v28,3,v24
v_xor_b32 v28,v28,v29
v_lshl_add_u32 v27,v28,4,v27
s_mul_i32 s65,s64,3072
v_add_u32 v28,s65,v8
s_mul_i32 s67,s64,3080
s_add_u32 s67,s67,40896
s_add_u32 s65,s65,16256
s_cmp_lt_u32 s52,2
s_cselect_b32 s58,s65,s67
s_cselect_b64 vcc,-1,0
s_movk_i32 s59,6144
s_cselect_b32 s59,-22464,s59
s_cselect_b32 s53,11264,0
s_cselect_b32 s88,0,67584
s_cselect_b32 s87,64,0
s_cselect_b32 s69,0,0x70000000
s_cselect_b64 s[44:45],s[24:25],s[28:29]
s_and_b32 s45,s45,0xffff
v_cndmask_b32_e32 v5,v28,v27,vcc
s_add_u32 m0,s57,14144
s_add_u32 s89,s56,393216
buffer_load_dwordx4 v6,s[36:39],s89 offen lds
s_mov_b32 s46,73728
s_mov_b32 s47,0x00020000
s_mov_b32 s99,0x72800000
s_mov_b32 s98,0xffff0000
s_mov_b64 s[90:91],0
v_mov_b32 v96,0x409d3ef8
v_lshlrev_b32 v102,4,%1
v_lshlrev_b32 v104,19,%1
v_cmp_eq_u32_e64 s[94:95],0,%1
v_add_u32 v103,0x8000,v102
s_mov_b32 s4,0x3f000000
s_mov_b32 s5,0x3fc00000
s_mov_b32 s6,0x40200000
s_mov_b32 s7,0x40600000
s_mov_b32 s8,0x40900000
s_mov_b32 s9,0x40b00000
s_mov_b32 s10,0x40d00000
s_mov_b32 s11,0x40f00000
s_mov_b32 s12,0x3f80222e
s_mov_b32 s13,0x13089abf
s_mov_b32 s96,0xebefe000
v_add_u32 v28,0x10000,v104
v_mul_f32 v98,s99,v104
v_fma_f32 v100,v28,s99,-v98
v_pk_fma_f32 v[64:65],v[100:101],s[4:5],v[98:99] op_sel_hi:[0,1,0]
s_add_u32 m0,s57,17680
s_add_u32 s89,s56,491520
buffer_load_dwordx4 v6,s[36:39],s89 offen lds
v_pk_fma_f32 v[66:67],v[100:101],s[6:7],v[98:99] op_sel_hi:[0,1,0]
v_pk_fma_f32 v[68:69],v[100:101],s[8:9],v[98:99] op_sel_hi:[0,1,0]
v_pk_fma_f32 v[70:71],v[100:101],s[10:11],v[98:99] op_sel_hi:[0,1,0]
v_cndmask_b32_e64 v64,v64,0,s[94:95]
v_pk_mul_f32 v[72:73],v[64:65],v[64:65]
v_pk_mul_f32 v[74:75],v[66:67],v[66:67]
v_pk_mul_f32 v[76:77],v[68:69],v[68:69]
v_pk_mul_f32 v[78:79],v[70:71],v[70:71]
v_pk_fma_f32 v[72:73],v[72:73],s[12:13],v[96:97] op_sel_hi:[1,0,0]
v_pk_fma_f32 v[74:75],v[74:75],s[12:13],v[96:97] op_sel_hi:[1,0,0]
v_pk_fma_f32 v[76:77],v[76:77],s[12:13],v[96:97] op_sel_hi:[1,0,0]
v_pk_fma_f32 v[78:79],v[78:79],s[12:13],v[96:97] op_sel_hi:[1,0,0]
v_pk_mul_f32 v[72:73],v[72:73],v[64:65]
v_pk_mul_f32 v[74:75],v[74:75],v[66:67]
v_pk_mul_f32 v[76:77],v[76:77],v[68:69]
v_pk_mul_f32 v[78:79],v[78:79],v[70:71]
v_exp_f32 v72,v72
v_exp_f32 v73,v73
v_exp_f32 v74,v74
v_exp_f32 v75,v75
v_exp_f32 v76,v76
s_add_u32 m0,s57,21224
s_add_u32 s89,s56,589824
buffer_load_dwordx4 v6,s[36:39],s89 offen lds
v_exp_f32 v77,v77
v_exp_f32 v78,v78
v_exp_f32 v79,v79
v_pk_fma_f32 v[72:73],v[72:73],s[96:97],s[96:97] op_sel_hi:[1,0,0]
v_pk_fma_f32 v[74:75],v[74:75],s[96:97],s[96:97] op_sel_hi:[1,0,0]
v_pk_fma_f32 v[76:77],v[76:77],s[96:97],s[96:97] op_sel_hi:[1,0,0]
v_pk_fma_f32 v[78:79],v[78:79],s[96:97],s[96:97] op_sel_hi:[1,0,0]
v_rcp_f32 v72,v72
v_rcp_f32 v73,v73
v_rcp_f32 v74,v74
v_rcp_f32 v75,v75
v_rcp_f32 v76,v76
v_rcp_f32 v77,v77
v_rcp_f32 v78,v78
v_rcp_f32 v79,v79
s_add_u32 m0,s57,24760
s_add_u32 s89,s56,688128
buffer_load_dwordx4 v6,s[36:39],s89 offen lds
v_pk_mul_f32 v[72:73],v[72:73],v[64:65]
v_pk_mul_f32 v[74:75],v[74:75],v[66:67]
v_pk_mul_f32 v[76:77],v[76:77],v[68:69]
v_pk_mul_f32 v[78:79],v[78:79],v[70:71]
v_pk_fma_f32 v[64:65],v[64:65],s[12:13],v[72:73] op_sel:[0,1,0] op_sel_hi:[1,1,1]
v_pk_fma_f32 v[66:67],v[66:67],s[12:13],v[74:75] op_sel:[0,1,0] op_sel_hi:[1,1,1]
v_pk_fma_f32 v[68:69],v[68:69],s[12:13],v[76:77] op_sel:[0,1,0] op_sel_hi:[1,1,1]
v_pk_fma_f32 v[70:71],v[70:71],s[12:13],v[78:79] op_sel:[0,1,0] op_sel_hi:[1,1,1]
v_cvt_pk_bf16_f32 v80,v72,v73
v_cvt_pk_bf16_f32 v81,v74,v75
v_cvt_pk_bf16_f32 v82,v76,v77
v_cvt_pk_bf16_f32 v83,v78,v79
v_cvt_pk_bf16_f32 v84,v64,v65
v_cvt_pk_bf16_f32 v85,v66,v67
v_cvt_pk_bf16_f32 v86,v68,v69
v_cvt_pk_bf16_f32 v87,v70,v71
ds_write_b128 v102,v[80:83] offset:0
ds_write_b128 v103,v[84:87] offset:32768
v_add_u32 v104,0x8000000,v104
v_add_u32 v28,0x10000,v104
v_mul_f32 v98,s99,v104
v_fma_f32 v100,v28,s99,-v98
v_pk_fma_f32 v[64:65],v[100:101],s[4:5],v[98:99] op_sel_hi:[0,1,0]
v_pk_fma_f32 v[66:67],v[100:101],s[6:7],v[98:99] op_sel_hi:[0,1,0]
v_pk_fma_f32 v[68:69],v[100:101],s[8:9],v[98:99] op_sel_hi:[0,1,0]
s_add_u32 m0,s57,28288
s_add_u32 s89,s56,786432
buffer_load_dwordx4 v6,s[36:39],s89 offen lds
v_pk_fma_f32 v[70:71],v[100:101],s[10:11],v[98:99] op_sel_hi:[0,1,0]
v_pk_mul_f32 v[72:73],v[64:65],v[64:65]
v_pk_mul_f32 v[74:75],v[66:67],v[66:67]
v_pk_mul_f32 v[76:77],v[68:69],v[68:69]
v_pk_mul_f32 v[78:79],v[70:71],v[70:71]
v_pk_fma_f32 v[72:73],v[72:73],s[12:13],v[96:97] op_sel_hi:[1,0,0]
v_pk_fma_f32 v[74:75],v[74:75],s[12:13],v[96:97] op_sel_hi:[1,0,0]
v_pk_fma_f32 v[76:77],v[76:77],s[12:13],v[96:97] op_sel_hi:[1,0,0]
v_pk_fma_f32 v[78:79],v[78:79],s[12:13],v[96:97] op_sel_hi:[1,0,0]
v_pk_mul_f32 v[72:73],v[72:73],v[64:65]
v_pk_mul_f32 v[74:75],v[74:75],v[66:67]
v_pk_mul_f32 v[76:77],v[76:77],v[68:69]
v_pk_mul_f32 v[78:79],v[78:79],v[70:71]
v_exp_f32 v72,v72
v_exp_f32 v73,v73
v_exp_f32 v74,v74
v_exp_f32 v75,v75
v_exp_f32 v76,v76
v_exp_f32 v77,v77
s_add_u32 m0,s57,31824
s_add_u32 s89,s56,884736
buffer_load_dwordx4 v6,s[36:39],s89 offen lds
v_exp_f32 v78,v78
v_exp_f32 v79,v79
v_pk_fma_f32 v[72:73],v[72:73],s[96:97],s[96:97] op_sel_hi:[1,0,0]
v_pk_fma_f32 v[74:75],v[74:75],s[96:97],s[96:97] op_sel_hi:[1,0,0]
v_pk_fma_f32 v[76:77],v[76:77],s[96:97],s[96:97] op_sel_hi:[1,0,0]
v_pk_fma_f32 v[78:79],v[78:79],s[96:97],s[96:97] op_sel_hi:[1,0,0]
v_rcp_f32 v72,v72
v_rcp_f32 v73,v73
v_rcp_f32 v74,v74
v_rcp_f32 v75,v75
v_rcp_f32 v76,v76
v_rcp_f32 v77,v77
v_rcp_f32 v78,v78
v_rcp_f32 v79,v79
v_pk_mul_f32 v[72:73],v[72:73],v[64:65]
s_add_u32 m0,s57,35368
s_add_u32 s89,s56,983040
buffer_load_dwordx4 v6,s[36:39],s89 offen lds
v_pk_mul_f32 v[74:75],v[74:75],v[66:67]
v_pk_mul_f32 v[76:77],v[76:77],v[68:69]
v_pk_mul_f32 v[78:79],v[78:79],v[70:71]
v_pk_fma_f32 v[64:65],v[64:65],s[12:13],v[72:73] op_sel:[0,1,0] op_sel_hi:[1,1,1]
v_pk_fma_f32 v[66:67],v[66:67],s[12:13],v[74:75] op_sel:[0,1,0] op_sel_hi:[1,1,1]
v_pk_fma_f32 v[68:69],v[68:69],s[12:13],v[76:77] op_sel:[0,1,0] op_sel_hi:[1,1,1]
v_pk_fma_f32 v[70:71],v[70:71],s[12:13],v[78:79] op_sel:[0,1,0] op_sel_hi:[1,1,1]
v_cvt_pk_bf16_f32 v88,v72,v73
v_cvt_pk_bf16_f32 v89,v74,v75
v_cvt_pk_bf16_f32 v90,v76,v77
v_cvt_pk_bf16_f32 v91,v78,v79
v_cvt_pk_bf16_f32 v92,v64,v65
v_cvt_pk_bf16_f32 v93,v66,v67
v_cvt_pk_bf16_f32 v94,v68,v69
v_cvt_pk_bf16_f32 v95,v70,v71
ds_write_b128 v102,v[88:91] offset:4096
ds_write_b128 v103,v[92:95] offset:36864
s_mov_b32 s96,0x8befe000
s_lshl_b32 s66,s52,2
s_mul_i32 s60,s66,3536
s_add_u32 s66,s66,s71
v_and_b32 v25,15,v24
v_lshrrev_b32 v26,4,v24
s_mul_i32 s61,s66,98304
s_add_u32 s61,s61,s68
s_mov_b32 s54,0x44ea44ea
s_mov_b32 s55,0x877f877f
s_mov_b64 s[40:41],s[34:35]
s_and_b32 s41,s41,0xffff
s_mov_b32 s42,50331648
s_mov_b32 s43,0x00020000
v_mov_b32 v41,0x70000
s_add_u32 m0,s57,38904
s_add_u32 s89,s56,1081344
buffer_load_dwordx4 v6,s[36:39],s89 offen lds
v_and_b32 v39,8,v24
v_and_b32 v40,7,v24
v_bfe_u32 v27,v24,1,1
v_sub_u32 v27,v26,v27
v_cmp_gt_u32_e64 s[92:93],3,v27
v_and_b32 v28,1,v24
v_mul_u32_u24 v27,3,v27
v_sub_u32 v27,v27,v28
v_cmp_eq_u32_e64 s[94:95],0,v28
v_lshrrev_b32 v29,2,v25
v_mul_i32_i24 v27,192,v27
v_lshl_add_u32 v27,v29,1,v27
v_mul_u32_u24 v10,0x5555,v29
v_add_u32 v27,12288,v27
v_cndmask_b32_e64 v45,v41,v27,s[92:93]
v_cndmask_b32_e64 v44,v41,v45,s[94:95]
v_cndmask_b32_e64 v47,v45,v41,s[94:95]
v_cmp_eq_u32_e64 s[92:93],v26,v29
v_lshlrev_b32 v27,4,v25
v_add_u32 v27,10112,v27
v_cndmask_b32_e64 v13,v41,v27,s[92:93]
v_lshrrev_b32 v27,2,v39
v_lshrrev_b32 v28,5,v24
v_add_u32 v27,v27,v28
v_mul_u32_u24 v27,3536,v27
s_add_u32 m0,s57,42432
s_add_u32 s89,s56,1179648
buffer_load_dwordx4 v6,s[36:39],s89 offen lds
v_lshl_or_b32 v10,v27,16,v10
v_and_b32 v27,1,v26
v_add_u32 v27,v27,v40
v_mul_u32_u24 v28,384,v27
v_lshrrev_b32 v27,1,v27
v_lshl_add_u32 v28,v27,4,v28
v_add_u32 v30,73728,v39
v_add_u32 v11,v28,v30
s_movk_i32 s66,16384
v_lshl_add_u32 v27,v40,1,1
v_lshrrev_b32 v28,2,v27
v_xor_b32 v28,v28,v26
v_mul_u32_u24 v27,192,v27
v_lshl_add_u32 v27,v28,4,v27
v_add3_u32 v12,v27,v39,s66
v_lshl_add_u32 v27,v40,1,2
v_lshrrev_b32 v28,2,v27
v_xor_b32 v28,v28,v26
v_mul_u32_u24 v27,192,v27
v_lshl_add_u32 v27,v28,4,v27
v_add3_u32 v27,v27,v39,s66
v_lshl_or_b32 v12,v27,16,v12
v_mul_u32_u24 v27,24576,v39
v_mul_u32_u24 v28,384,v40
v_lshl_add_u32 v27,v26,4,v27
s_add_u32 m0,s57,45968
s_add_u32 s89,s56,1277952
buffer_load_dwordx4 v6,s[36:39],s89 offen lds
v_add_u32 v7,v27,v28
v_lshrrev_b32 v27,3,v25
v_bfe_u32 v28,v25,2,1
v_lshl_add_u32 v28,v27,3,v28
v_lshl_add_u32 v28,v26,1,v28
v_lshlrev_b32 v28,6,v28
v_lshl_add_u32 v28,v27,4,v28
v_and_b32 v29,1,v25
v_lshl_add_u32 v28,v29,5,v28
v_bfe_u32 v29,v25,1,1
v_lshl_add_u32 v28,v29,3,v28
v_xor_b32 v29,16,v28
v_add_u32 v1,16256,v28
v_add_u32 v2,16256,v29
v_lshrrev_b32 v27,3,v25
v_bfe_u32 v28,v25,2,1
v_lshl_add_u32 v27,v28,1,v27
v_lshrrev_b32 v28,1,v26
v_lshl_add_u32 v27,v28,2,v27
v_mul_u32_u24 v27,192,v27
v_and_b32 v29,1,v26
v_mul_u32_u24 v30,3080,v29
v_add_u32 v27,v27,v30
v_and_b32 v30,3,v25
v_lshl_add_u32 v27,v30,4,v27
s_add_u32 m0,s57,49512
s_add_u32 s89,s56,1376256
buffer_load_dwordx4 v6,s[36:39],s89 offen lds
v_add_u32 v3,40896,v27
v_lshlrev_b32 v29,6,v29
v_lshl_add_u32 v29,v28,4,v29
v_add_u32 v4,8192,v29
v_lshrrev_b32 v27,5,v34
v_bfe_u32 v28,v34,2,1
v_and_b32 v29,3,v34
v_lshlrev_b32 v49,7,v27
v_lshl_add_u32 v49,v28,5,v49
v_lshl_add_u32 v49,v29,2,v49
v_bfe_u32 v30,v34,3,1
v_lshl_add_u32 v49,v30,6,v49
v_bfe_u32 v30,v34,4,1
v_lshl_add_u32 v49,v30,4,v49
v_add_u32 v49,11776,v49
v_lshrrev_b32 v55,6,v34
v_bfe_u32 v30,v34,3,2
v_lshl_add_u32 v55,v55,2,v30
v_and_b32 v30,1,v27
v_lshl_add_u32 v55,v55,1,v30
v_and_b32 v30,7,v34
v_lshl_add_u32 v55,v55,3,v30
v_lshlrev_b32 v55,2,v55
v_lshlrev_b32 v15,2,v24
v_lshl_add_u32 v50,v27,1,v28
s_add_u32 m0,s57,53048
s_add_u32 s89,s56,1474560
buffer_load_dwordx4 v6,s[36:39],s89 offen lds
v_lshlrev_b32 v50,4,v50
v_bfe_u32 v30,v34,3,2
v_lshl_add_u32 v50,v30,2,v50
v_add_u32 v50,v50,v29
v_lshlrev_b32 v50,4,v50
v_add_u32 v50,10112,v50
v_lshlrev_b32 v51,4,v29
v_cmp_eq_u32_e64 s[94:95],1,v28
v_lshlrev_b32 v48,1,v32
v_add_u32 v48,8192,v48
v_mov_b32 v53,0
s_waitcnt vmcnt(16)
v_lshlrev_b32 v37,16,v37
v_lshlrev_b32 v36,16,v36
v_mov_b32 v27,0x45800000
v_mov_b32 v28,0x53800000
v_cmp_nlt_f32_e64 s[90:91],|v37|,v27
v_cmp_nlt_f32_e64 vcc,|v36|,v28
v_mov_b32 v27,0x42800000
v_mul_f32 v37,0x6c800000,v37
v_mul_f32 v36,0x12800000,v36
v_rcp_f32 v56,v37
v_lshlrev_b32 v42,16,v35
v_and_b32 v43,s98,v35
s_or_b64 s[90:91],s[90:91],vcc
v_cmp_nlt_f32_e64 vcc,|v42|,v27
v_cmp_nlt_f32_e64 s[92:93],|v43|,v27
v_med3_f32 v56,v56,1.0,-1.0
v_pk_mul_f32 v[42:43],v[42:43],s[96:97] op_sel_hi:[1,0]
v_cvt_pk_bf16_f32 v52,v56,v53
ds_write_b64 v48,v[42:43]
ds_write_b32 v49,v36
v_lshlrev_b32 v54,16,v52
v_rcp_f32 v54,v54
v_lshlrev_b64 v[52:53],v51,v[52:53]
s_or_b64 s[90:91],s[90:91],vcc
s_or_b64 s[90:91],s[90:91],s[92:93]
v_cndmask_b32_e64 v56,v52,0,s[94:95]
v_cndmask_b32_e64 v58,0,v52,s[94:95]
v_cndmask_b32_e64 v57,v53,0,s[94:95]
v_cndmask_b32_e64 v59,0,v53,s[94:95]
ds_write_b32 v15,v7 offset:14848
ds_write_b32 v15,v13 offset:15104
s_mul_i32 s66,s52,1536
s_add_u32 s66,s66,16256
v_lshl_add_u32 v7,v24,3,s66
ds_write_b128 v50,v[56:59]
ds_write_b32 v55,v54 offset:14336
v_mov_b64 v[20:21],0
v_mov_b64 v[22:23],0
ds_read_u16 a96,v44 offset:0
ds_read_u16 a97,v45 offset:192
ds_read_u16 a98,v45 offset:384
ds_read_u16 a99,v47 offset:576
ds_read_u16 a100,v44 offset:8
ds_read_u16 a101,v45 offset:200
ds_read_u16 a102,v45 offset:392
ds_read_u16 a103,v47 offset:584
ds_read_u16 a104,v44 offset:16
v_mfma_f32_32x32x16_bf16 v[64:79],v[20:23],v[20:23],0
ds_read_u16 a105,v45 offset:208
ds_read_u16 a106,v45 offset:400
ds_read_u16 a107,v47 offset:592
ds_read_u16 a108,v44 offset:24
ds_read_u16 a109,v45 offset:216
ds_read_u16 a110,v45 offset:408
ds_read_u16 a111,v47 offset:600
ds_read_u16 a112,v44 offset:32
ds_read_u16 a113,v45 offset:224
ds_read_u16 a114,v45 offset:416
ds_read_u16 a115,v47 offset:608
ds_read_u16 a116,v44 offset:40
ds_read_u16 a117,v45 offset:232
ds_read_u16 a118,v45 offset:424
ds_read_u16 a119,v47 offset:616
s_add_u32 m0,s57,56576
s_add_u32 s89,s56,1572864
buffer_load_dwordx4 v6,s[36:39],s89 offen lds
ds_read_u16 a120,v44 offset:48
v_mfma_f32_32x32x16_bf16 v[80:95],v[20:23],v[20:23],0
ds_read_u16 a121,v45 offset:240
ds_read_u16 a122,v45 offset:432
ds_read_u16 a123,v47 offset:624
ds_read_u16 a124,v44 offset:56
ds_read_u16 a125,v45 offset:248
ds_read_u16 a126,v45 offset:440
ds_read_u16 a127,v47 offset:632
ds_read_u16 a128,v44 offset:64
ds_read_u16 a129,v45 offset:256
ds_read_u16 a130,v45 offset:448
ds_read_u16 a131,v47 offset:640
ds_read_u16 a132,v44 offset:72
ds_read_u16 a133,v45 offset:264
ds_read_u16 a134,v45 offset:456
ds_read_u16 a135,v47 offset:648
ds_read_u16 a136,v44 offset:80
v_mfma_f32_32x32x16_bf16 v[96:111],v[20:23],v[20:23],0
ds_read_u16 a137,v45 offset:272
ds_read_u16 a138,v45 offset:464
ds_read_u16 a139,v47 offset:656
ds_read_u16 a140,v44 offset:88
ds_read_u16 a141,v45 offset:280
ds_read_u16 a142,v45 offset:472
ds_read_u16 a143,v47 offset:664
s_add_u32 m0,s57,60112
s_add_u32 s89,s56,1671168
buffer_load_dwordx4 v6,s[36:39],s89 offen lds
ds_read_u16 a144,v44 offset:96
ds_read_u16 a145,v45 offset:288
ds_read_u16 a146,v45 offset:480
ds_read_u16 a147,v47 offset:672
ds_read_u16 a148,v44 offset:104
ds_read_u16 a149,v45 offset:296
ds_read_u16 a150,v45 offset:488
ds_read_u16 a151,v47 offset:680
ds_read_u16 a152,v44 offset:112
v_mfma_f32_32x32x16_bf16 v[112:127],v[20:23],v[20:23],0
ds_read_u16 a153,v45 offset:304
ds_read_u16 a154,v45 offset:496
ds_read_u16 a155,v47 offset:688
ds_read_u16 a156,v44 offset:120
ds_read_u16 a157,v45 offset:312
ds_read_u16 a158,v45 offset:504
ds_read_u16 a159,v47 offset:696
ds_read_u16 a160,v44 offset:128
ds_read_u16 a161,v45 offset:320
ds_read_u16 a162,v45 offset:512
ds_read_u16 a163,v47 offset:704
ds_read_u16 a164,v44 offset:136
ds_read_u16 a165,v45 offset:328
ds_read_u16 a166,v45 offset:520
ds_read_u16 a167,v47 offset:712
s_add_u32 m0,s58,18480
s_nop 0
buffer_load_dwordx4 v5,s[44:47],s69 offen offset:0 lds
ds_read_u16 a168,v44 offset:144
v_mfma_f32_32x32x16_bf16 v[128:143],v[20:23],v[20:23],0
ds_read_u16 a169,v45 offset:336
ds_read_u16 a170,v45 offset:528
ds_read_u16 a171,v47 offset:720
ds_read_u16 a172,v44 offset:152
ds_read_u16 a173,v45 offset:344
ds_read_u16 a174,v45 offset:536
ds_read_u16 a175,v47 offset:728
ds_read_u16 a176,v44 offset:160
ds_read_u16 a177,v45 offset:352
ds_read_u16 a178,v45 offset:544
ds_read_u16 a179,v47 offset:736
ds_read_u16 a180,v44 offset:168
ds_read_u16 a181,v45 offset:360
ds_read_u16 a182,v45 offset:552
ds_read_u16 a183,v47 offset:744
ds_read_u16 a184,v44 offset:176
v_mfma_f32_32x32x16_bf16 v[144:159],v[20:23],v[20:23],0
ds_read_u16 a185,v45 offset:368
ds_read_u16 a186,v45 offset:560
ds_read_u16 a187,v47 offset:752
ds_read_u16 a188,v44 offset:184
ds_read_u16 a189,v45 offset:376
ds_read_u16 a190,v45 offset:568
ds_read_u16 a191,v47 offset:760
s_add_u32 s69,s69,s53
buffer_load_dwordx4 v5,s[44:47],s69 offen offset:1024 lds
s_waitcnt vmcnt(17)
v_and_b32 v160,0x7fff7fff,v160
v_and_b32 v161,0x7fff7fff,v161
v_and_b32 v162,0x7fff7fff,v162
v_and_b32 v163,0x7fff7fff,v163
v_and_b32 v164,0x7fff7fff,v164
v_and_b32 v165,0x7fff7fff,v165
v_and_b32 v166,0x7fff7fff,v166
v_and_b32 v167,0x7fff7fff,v167
v_and_b32 v168,0x7fff7fff,v168
v_and_b32 v169,0x7fff7fff,v169
v_and_b32 v170,0x7fff7fff,v170
v_and_b32 v171,0x7fff7fff,v171
v_pk_max_u16 v160,v160,v164
v_pk_max_u16 v161,v161,v165
v_pk_max_u16 v162,v162,v166
v_pk_max_u16 v163,v163,v167
v_mov_b64 v[164:165],v[160:161]
v_mov_b64 v[166:167],v[162:163]
s_nop 0
v_permlane32_swap_b32 v160,v164
v_permlane32_swap_b32 v161,v165
v_permlane32_swap_b32 v162,v166
v_permlane32_swap_b32 v163,v167
v_pk_max_u16 v160,v160,v164
v_pk_max_u16 v161,v161,v165
v_pk_max_u16 v162,v162,v166
v_pk_max_u16 v163,v163,v167
v_mov_b64 v[164:165],v[160:161]
v_mov_b64 v[166:167],v[162:163]
s_nop 0
v_permlane16_swap_b32 v160,v164
v_permlane16_swap_b32 v161,v165
v_permlane16_swap_b32 v162,v166
v_permlane16_swap_b32 v163,v167
v_pk_max_u16 v160,v160,v164
v_pk_max_u16 v161,v161,v165
v_pk_max_u16 v162,v162,v166
v_pk_max_u16 v163,v163,v167
v_pk_min_u16 v160,v160,v161
v_pk_min_u16 v162,v162,v163
v_pk_min_u16 v160,v160,v162
v_pk_min_u16 v160,v160,v160 op_sel:[0,1] op_sel_hi:[1,0]
v_pk_max_u16 v168,v168,v169
v_pk_max_u16 v170,v170,v171
v_pk_max_u16 v168,v168,v170
v_pk_max_u16 v168,v168,v168 op_sel:[0,1] op_sel_hi:[1,0]
v_cmp_gt_u16_e32 vcc,0x3880,v160
s_nop 0
s_or_b64 s[90:91],s[90:91],vcc
v_cmp_le_u16_e32 vcc,0x5380,v168
s_nop 0
s_or_b64 s[90:91],s[90:91],vcc
s_waitcnt vmcnt(16)
v_mfma_f32_32x32x16_bf16 v[160:175],v[20:23],v[20:23],0
v_mfma_f32_32x32x16_bf16 v[176:191],v[20:23],v[20:23],0
v_mfma_f32_32x32x16_bf16 v[192:207],v[20:23],v[20:23],0
v_mfma_f32_32x32x16_bf16 v[208:223],v[20:23],v[20:23],0
v_mfma_f32_32x32x16_bf16 v[224:239],v[20:23],v[20:23],0
s_mov_b64 exec,0
s_cmp_eq_u32 s52,0
s_cbranch_scc1 TE00_%=
s_cmp_eq_u32 s52,1
s_cbranch_scc1 TE01_%=
s_cmp_eq_u32 s52,2
s_cbranch_scc1 TE02_%=
s_branch TE03_%=
TR0_%=:
s_mov_b64 exec,-1
s_add_u32 s69,s69,s53
buffer_load_dwordx4 v5,s[44:47],s69 offen offset:2048 lds
s_add_u32 m0,s58,0
s_mov_b32 s69,s87
buffer_load_dwordx4 v5,s[44:47],s69 offen offset:0 lds
s_add_u32 s69,s69,s53
buffer_load_dwordx4 v5,s[44:47],s69 offen offset:1024 lds
s_add_u32 s69,s69,s53
buffer_load_dwordx4 v5,s[44:47],s69 offen offset:2048 lds
v_accvgpr_write_b32 a88,0
v_accvgpr_write_b32 a89,0
v_accvgpr_write_b32 a90,0
v_accvgpr_write_b32 a91,0
s_waitcnt lgkmcnt(0)
s_waitcnt vmcnt(6)
s_barrier
v_add_u32_sdwa v28,s60,v10 dst_sel:DWORD dst_unused:UNUSED_PAD src0_sel:DWORD src1_sel:WORD_1
v_subrev_u32 v8,88400,v28
v_min_u32 v28,v28,v8
v_add_u32 v29,7072,v28
v_subrev_u32 v8,88400,v29
v_min_u32 v29,v29,v8
v_xad_u32 v24,v11,0,v28
v_xad_u32 v25,v11,8,v29
v_xad_u32 v26,v11,16,v28
v_xad_u32 v27,v11,24,v29
ds_read_b64 a[0:1],v24 offset:0
ds_read_b64 a[2:3],v24 offset:192
ds_read_b64 a[4:5],v25 offset:0
ds_read_b64 a[6:7],v25 offset:192
ds_read_b64 a[8:9],v24 offset:8
ds_read_b64 a[10:11],v24 offset:200
ds_read_b64 a[12:13],v25 offset:8
ds_read_b64 a[14:15],v25 offset:200
ds_read_b64 a[16:17],v26 offset:0
ds_read_b64 a[18:19],v26 offset:192
ds_read_b64 a[20:21],v27 offset:0
ds_read_b64 a[22:23],v27 offset:192
s_waitcnt lgkmcnt(8)
v_smfmac_f32_16x16x64_bf16 v[64:67],a[96:99],a[0:7],v10
s_waitcnt lgkmcnt(4)
s_add_u32 m0,s58,6160
s_add_u32 s69,s69,s59
buffer_load_dwordx4 v5,s[44:47],s69 offen offset:0 lds
ds_read_b64 a[0:1],v26 offset:8
ds_read_b64 a[2:3],v26 offset:200
ds_read_b64 a[4:5],v27 offset:8
ds_read_b64 a[6:7],v27 offset:200
v_smfmac_f32_16x16x64_bf16 v[68:71],a[100:103],a[8:15],v10
s_waitcnt lgkmcnt(4)
s_add_u32 m0,s57,63656
s_add_u32 s89,s56,1769472
buffer_load_dwordx4 v6,s[36:39],s89 offen lds
ds_read_b64 a[8:9],v24 offset:64
ds_read_b64 a[10:11],v24 offset:256
ds_read_b64 a[12:13],v25 offset:64
ds_read_b64 a[14:15],v25 offset:256
v_smfmac_f32_16x16x64_bf16 v[72:75],a[104:107],a[16:23],v10
s_waitcnt lgkmcnt(4)
ds_read_b64 a[16:17],v24 offset:72
ds_read_b64 a[18:19],v24 offset:264
ds_read_b64 a[20:21],v25 offset:72
ds_read_b64 a[22:23],v25 offset:264
v_and_b32 v248,8,v11
v_pk_mul_f32 v[40:41],v[64:65],v[64:65]
v_pk_mul_f32 v[42:43],v[66:67],v[66:67]
v_smfmac_f32_16x16x64_bf16 v[76:79],a[108:111],a[0:7],v10
s_waitcnt lgkmcnt(4)
ds_read_b64 a[0:1],v26 offset:64
ds_read_b64 a[2:3],v26 offset:256
ds_read_b64 a[4:5],v27 offset:64
ds_read_b64 a[6:7],v27 offset:256
v_mul_u32_u24 v248,884,v248
s_add_u32 s64,s60,3536
v_pk_add_f32 v[36:37],v[64:65],v[68:69]
v_pk_fma_f32 v[40:41],v[68:69],v[68:69],v[40:41]
v_pk_add_f32 v[38:39],v[66:67],v[70:71]
v_pk_fma_f32 v[42:43],v[70:71],v[70:71],v[42:43]
v_smfmac_f32_16x16x64_bf16 v[80:83],a[128:131],a[8:15],v10
s_waitcnt lgkmcnt(4)
s_add_u32 m0,s57,67192
s_add_u32 s89,s56,1867776
buffer_load_dwordx4 v6,s[36:39],s89 offen lds
ds_read_b64 a[8:9],v26 offset:72
ds_read_b64 a[10:11],v26 offset:264
ds_read_b64 a[12:13],v27 offset:72
ds_read_b64 a[14:15],v27 offset:264
v_add_u32 v251,s64,v248
v_pk_add_f32 v[36:37],v[36:37],v[72:73]
v_pk_fma_f32 v[40:41],v[72:73],v[72:73],v[40:41]
v_pk_add_f32 v[38:39],v[38:39],v[74:75]
v_pk_fma_f32 v[42:43],v[74:75],v[74:75],v[42:43]
v_smfmac_f32_16x16x64_bf16 v[84:87],a[132:135],a[16:23],v10
s_waitcnt lgkmcnt(4)
ds_read_b64 a[16:17],v24 offset:128
ds_read_b64 a[18:19],v24 offset:320
ds_read_b64 a[20:21],v25 offset:128
ds_read_b64 a[22:23],v25 offset:320
v_subrev_u32 v8,88400,v251
v_pk_add_f32 v[36:37],v[36:37],v[76:77]
v_pk_fma_f32 v[40:41],v[76:77],v[76:77],v[40:41]
v_pk_add_f32 v[38:39],v[38:39],v[78:79]
v_pk_fma_f32 v[42:43],v[78:79],v[78:79],v[42:43]
v_smfmac_f32_16x16x64_bf16 v[88:91],a[136:139],a[0:7],v10
s_waitcnt lgkmcnt(4)
ds_read_b64 a[0:1],v24 offset:136
ds_read_b64 a[2:3],v24 offset:328
ds_read_b64 a[4:5],v25 offset:136
ds_read_b64 a[6:7],v25 offset:328
v_min_u32 v251,v251,v8
v_pk_add_f32 v[36:37],v[36:37],v[80:81]
v_pk_fma_f32 v[40:41],v[80:81],v[80:81],v[40:41]
v_pk_add_f32 v[38:39],v[38:39],v[82:83]
v_pk_fma_f32 v[42:43],v[82:83],v[82:83],v[42:43]
v_smfmac_f32_16x16x64_bf16 v[92:95],a[140:143],a[8:15],v10
s_waitcnt lgkmcnt(4)
s_add_u32 m0,s58,6160
s_add_u32 s69,s69,s53
buffer_load_dwordx4 v5,s[44:47],s69 offen offset:1024 lds
ds_read_b64 a[8:9],v26 offset:128
ds_read_b64 a[10:11],v26 offset:320
ds_read_b64 a[12:13],v27 offset:128
ds_read_b64 a[14:15],v27 offset:320
v_xad_u32 v24,v11,32,v28
v_xad_u32 v25,v11,40,v29
v_pk_add_f32 v[36:37],v[36:37],v[84:85]
v_pk_fma_f32 v[40:41],v[84:85],v[84:85],v[40:41]
v_pk_add_f32 v[38:39],v[38:39],v[86:87]
v_pk_fma_f32 v[42:43],v[86:87],v[86:87],v[42:43]
v_smfmac_f32_16x16x64_bf16 v[96:99],a[160:163],a[16:23],v10
s_waitcnt lgkmcnt(4)
ds_read_b64 a[16:17],v26 offset:136
ds_read_b64 a[18:19],v26 offset:328
ds_read_b64 a[20:21],v27 offset:136
ds_read_b64 a[22:23],v27 offset:328
v_add_u32 v250,3536,v251
v_pk_add_f32 v[36:37],v[36:37],v[88:89]
v_pk_fma_f32 v[40:41],v[88:89],v[88:89],v[40:41]
v_pk_add_f32 v[38:39],v[38:39],v[90:91]
v_pk_fma_f32 v[42:43],v[90:91],v[90:91],v[42:43]
v_smfmac_f32_16x16x64_bf16 v[100:103],a[164:167],a[0:7],v10
s_waitcnt lgkmcnt(4)
s_add_u32 m0,s57,70720
s_add_u32 s89,s56,1966080
buffer_load_dwordx4 v6,s[36:39],s89 offen lds
ds_read_b64 a[0:1],v24 offset:0
ds_read_b64 a[2:3],v24 offset:192
ds_read_b64 a[4:5],v25 offset:0
ds_read_b64 a[6:7],v25 offset:192
v_xad_u32 v26,v11,48,v28
v_xad_u32 v27,v11,56,v29
v_pk_add_f32 v[36:37],v[36:37],v[92:93]
v_pk_fma_f32 v[40:41],v[92:93],v[92:93],v[40:41]
v_pk_add_f32 v[38:39],v[38:39],v[94:95]
v_pk_fma_f32 v[42:43],v[94:95],v[94:95],v[42:43]
v_smfmac_f32_16x16x64_bf16 v[104:107],a[168:171],a[8:15],v10
s_waitcnt lgkmcnt(4)
ds_read_b64 a[8:9],v24 offset:8
ds_read_b64 a[10:11],v24 offset:200
ds_read_b64 a[12:13],v25 offset:8
ds_read_b64 a[14:15],v25 offset:200
v_subrev_u32 v8,88400,v250
v_pk_add_f32 v[36:37],v[36:37],v[96:97]
v_pk_fma_f32 v[40:41],v[96:97],v[96:97],v[40:41]
v_pk_add_f32 v[38:39],v[38:39],v[98:99]
v_pk_fma_f32 v[42:43],v[98:99],v[98:99],v[42:43]
v_smfmac_f32_16x16x64_bf16 v[108:111],a[172:175],a[16:23],v10
s_waitcnt lgkmcnt(4)
ds_read_b64 a[16:17],v24 offset:64
ds_read_b64 a[18:19],v24 offset:256
ds_read_b64 a[20:21],v25 offset:64
ds_read_b64 a[22:23],v25 offset:256
v_min_u32 v250,v250,v8
v_pk_add_f32 v[36:37],v[36:37],v[100:101]
v_pk_fma_f32 v[40:41],v[100:101],v[100:101],v[40:41]
v_pk_add_f32 v[38:39],v[38:39],v[102:103]
v_pk_fma_f32 v[42:43],v[102:103],v[102:103],v[42:43]
v_smfmac_f32_16x16x64_bf16 v[112:115],a[112:115],a[0:7],v10
s_waitcnt lgkmcnt(4)
s_add_u32 m0,s58,6160
s_add_u32 s69,s69,s53
buffer_load_dwordx4 v5,s[44:47],s69 offen offset:2048 lds
ds_read_b64 a[0:1],v24 offset:72
ds_read_b64 a[2:3],v24 offset:264
ds_read_b64 a[4:5],v25 offset:72
ds_read_b64 a[6:7],v25 offset:264
v_xor_b32 v9,0x80008,v12
v_pk_add_f32 v[36:37],v[36:37],v[104:105]
v_pk_fma_f32 v[40:41],v[104:105],v[104:105],v[40:41]
v_pk_add_f32 v[38:39],v[38:39],v[106:107]
v_pk_fma_f32 v[42:43],v[106:107],v[106:107],v[42:43]
v_smfmac_f32_16x16x64_bf16 v[116:119],a[116:119],a[8:15],v10
s_waitcnt lgkmcnt(4)
ds_read_b64 a[8:9],v24 offset:128
ds_read_b64 a[10:11],v24 offset:320
ds_read_b64 a[12:13],v25 offset:128
ds_read_b64 a[14:15],v25 offset:320
v_add_u32_sdwa v248,v12,v251 dst_sel:DWORD dst_unused:UNUSED_PAD src0_sel:WORD_0 src1_sel:DWORD
v_pk_add_f32 v[36:37],v[36:37],v[108:109]
v_pk_fma_f32 v[40:41],v[108:109],v[108:109],v[40:41]
v_pk_add_f32 v[38:39],v[38:39],v[110:111]
v_pk_fma_f32 v[42:43],v[110:111],v[110:111],v[42:43]
v_smfmac_f32_16x16x64_bf16 v[120:123],a[144:147],a[16:23],v10
s_waitcnt lgkmcnt(4)
s_add_u32 m0,s57,74256
s_add_u32 s89,s56,2064384
buffer_load_dwordx4 v6,s[36:39],s89 offen lds
ds_read_b64 a[16:17],v24 offset:136
ds_read_b64 a[18:19],v24 offset:328
ds_read_b64 a[20:21],v25 offset:136
ds_read_b64 a[22:23],v25 offset:328
v_add_u32_sdwa v249,v12,v251 dst_sel:DWORD dst_unused:UNUSED_PAD src0_sel:WORD_1 src1_sel:DWORD
v_pk_add_f32 v[36:37],v[36:37],v[112:113]
v_pk_fma_f32 v[40:41],v[112:113],v[112:113],v[40:41]
v_pk_add_f32 v[38:39],v[38:39],v[114:115]
v_pk_fma_f32 v[42:43],v[114:115],v[114:115],v[42:43]
v_smfmac_f32_16x16x64_bf16 v[124:127],a[148:151],a[0:7],v10
s_waitcnt lgkmcnt(4)
ds_read_b64 a[0:1],v26 offset:0
ds_read_b64 a[2:3],v26 offset:192
ds_read_b64 a[4:5],v27 offset:0
ds_read_b64 a[6:7],v27 offset:192
v_add_u32_sdwa v251,v9,v250 dst_sel:DWORD dst_unused:UNUSED_PAD src0_sel:WORD_1 src1_sel:DWORD
v_pk_add_f32 v[36:37],v[36:37],v[116:117]
v_pk_fma_f32 v[40:41],v[116:117],v[116:117],v[40:41]
v_pk_add_f32 v[38:39],v[38:39],v[118:119]
v_pk_fma_f32 v[42:43],v[118:119],v[118:119],v[42:43]
v_smfmac_f32_16x16x64_bf16 v[128:131],a[176:179],a[8:15],v10
s_waitcnt lgkmcnt(4)
ds_read_b64 a[8:9],v26 offset:8
ds_read_b64 a[10:11],v26 offset:200
ds_read_b64 a[12:13],v27 offset:8
ds_read_b64 a[14:15],v27 offset:200
v_add_u32_sdwa v250,v9,v250 dst_sel:DWORD dst_unused:UNUSED_PAD src0_sel:WORD_0 src1_sel:DWORD
v_pk_add_f32 v[36:37],v[36:37],v[120:121]
v_pk_fma_f32 v[40:41],v[120:121],v[120:121],v[40:41]
v_pk_add_f32 v[38:39],v[38:39],v[122:123]
v_pk_fma_f32 v[42:43],v[122:123],v[122:123],v[42:43]
v_smfmac_f32_16x16x64_bf16 v[132:135],a[180:183],a[16:23],v10
s_waitcnt lgkmcnt(4)
s_add_u32 m0,s58,12320
s_add_u32 s69,s69,s59
buffer_load_dwordx4 v5,s[44:47],s69 offen offset:0 lds
ds_read_b64 a[16:17],v26 offset:64
ds_read_b64 a[18:19],v26 offset:256
ds_read_b64 a[20:21],v27 offset:64
ds_read_b64 a[22:23],v27 offset:256
v_pk_add_f32 v[36:37],v[36:37],v[124:125]
v_pk_fma_f32 v[40:41],v[124:125],v[124:125],v[40:41]
v_pk_add_f32 v[38:39],v[38:39],v[126:127]
v_pk_fma_f32 v[42:43],v[126:127],v[126:127],v[42:43]
v_smfmac_f32_16x16x64_bf16 v[136:139],a[120:123],a[0:7],v10
s_waitcnt lgkmcnt(4)
ds_read_b64 a[0:1],v26 offset:72
ds_read_b64 a[2:3],v26 offset:264
ds_read_b64 a[4:5],v27 offset:72
ds_read_b64 a[6:7],v27 offset:264
v_pk_add_f32 v[36:37],v[36:37],v[128:129]
v_pk_fma_f32 v[40:41],v[128:129],v[128:129],v[40:41]
v_pk_add_f32 v[38:39],v[38:39],v[130:131]
v_pk_fma_f32 v[42:43],v[130:131],v[130:131],v[42:43]
v_smfmac_f32_16x16x64_bf16 v[140:143],a[124:127],a[8:15],v10
s_waitcnt lgkmcnt(4)
s_add_u32 m0,s57,77800
s_add_u32 s89,s56,2162688
buffer_load_dwordx4 v6,s[36:39],s89 offen lds
ds_read_b64 a[8:9],v26 offset:128
ds_read_b64 a[10:11],v26 offset:320
ds_read_b64 a[12:13],v27 offset:128
ds_read_b64 a[14:15],v27 offset:320
v_pk_add_f32 v[36:37],v[36:37],v[132:133]
v_pk_fma_f32 v[40:41],v[132:133],v[132:133],v[40:41]
v_pk_add_f32 v[38:39],v[38:39],v[134:135]
v_pk_fma_f32 v[42:43],v[134:135],v[134:135],v[42:43]
v_smfmac_f32_16x16x64_bf16 v[144:147],a[152:155],a[16:23],v10
s_waitcnt lgkmcnt(4)
ds_read_b64 a[16:17],v26 offset:136
ds_read_b64 a[18:19],v26 offset:328
ds_read_b64 a[20:21],v27 offset:136
ds_read_b64 a[22:23],v27 offset:328
v_pk_add_f32 v[36:37],v[36:37],v[136:137]
v_pk_fma_f32 v[40:41],v[136:137],v[136:137],v[40:41]
v_pk_add_f32 v[38:39],v[38:39],v[138:139]
v_pk_fma_f32 v[42:43],v[138:139],v[138:139],v[42:43]
v_smfmac_f32_16x16x64_bf16 v[148:151],a[156:159],a[0:7],v10
s_waitcnt lgkmcnt(4)
ds_read_b64 a[224:225],v248 offset:57344
ds_read_b64 a[226:227],v248 offset:57352
ds_read_b64 a[228:229],v249 offset:57344
ds_read_b64 a[230:231],v249 offset:57352
ds_read_b128 a[240:243],v13 offset:0
v_pk_add_f32 v[36:37],v[36:37],v[140:141]
v_pk_fma_f32 v[40:41],v[140:141],v[140:141],v[40:41]
v_pk_add_f32 v[38:39],v[38:39],v[142:143]
v_pk_fma_f32 v[42:43],v[142:143],v[142:143],v[42:43]
v_smfmac_f32_16x16x64_bf16 v[152:155],a[184:187],a[8:15],v10
s_waitcnt lgkmcnt(5)
s_add_u32 m0,s58,12320
s_add_u32 s69,s69,s53
buffer_load_dwordx4 v5,s[44:47],s69 offen offset:1024 lds
ds_read_b64 a[232:233],v250 offset:57344
ds_read_b64 a[234:235],v250 offset:57352
ds_read_b64 a[236:237],v251 offset:57344
ds_read_b64 a[238:239],v251 offset:57352
ds_read_b128 a[244:247],v13 offset:256
v_pk_add_f32 v[36:37],v[36:37],v[144:145]
v_pk_fma_f32 v[40:41],v[144:145],v[144:145],v[40:41]
v_pk_add_f32 v[38:39],v[38:39],v[146:147]
v_pk_fma_f32 v[42:43],v[146:147],v[146:147],v[42:43]
v_smfmac_f32_16x16x64_bf16 v[156:159],a[188:191],a[16:23],v10
ds_read_b128 v[240:243],v4 offset:3584
ds_read_b128 v[244:247],v4 offset:3616
v_pk_add_f32 v[36:37],v[36:37],v[148:149]
v_pk_fma_f32 v[40:41],v[148:149],v[148:149],v[40:41]
v_pk_add_f32 v[38:39],v[38:39],v[150:151]
v_pk_fma_f32 v[42:43],v[150:151],v[150:151],v[42:43]
v_pk_add_f32 v[36:37],v[36:37],v[152:153]
v_pk_fma_f32 v[40:41],v[152:153],v[152:153],v[40:41]
v_pk_add_f32 v[38:39],v[38:39],v[154:155]
v_pk_fma_f32 v[42:43],v[154:155],v[154:155],v[42:43]
v_pk_add_f32 v[36:37],v[36:37],v[156:157]
v_pk_fma_f32 v[40:41],v[156:157],v[156:157],v[40:41]
v_pk_add_f32 v[38:39],v[38:39],v[158:159]
v_pk_fma_f32 v[42:43],v[158:159],v[158:159],v[42:43]
s_nop 0
v_permlane32_swap_b32 v36,v38
v_permlane32_swap_b32 v37,v39
v_permlane32_swap_b32 v40,v42
v_permlane32_swap_b32 v41,v43
v_pk_add_f32 v[36:37],v[36:37],v[38:39]
v_pk_add_f32 v[40:41],v[40:41],v[42:43]
s_add_u32 s60,s60,56576
s_sub_u32 s64,s60,88400
s_min_u32 s60,s60,s64
v_permlane16_swap_b32 v36,v37
v_permlane16_swap_b32 v40,v41
v_add_f32 v8,v36,v37
v_add_f32 v9,v40,v41
v_mul_f32 v8,0x3c2aaaab,v8
v_mul_f32 v9,0x3c2aaaab,v9
v_cmp_ngt_f32 vcc,0x5d800000,v9
v_fma_f32 v9,-v8,v8,v9
v_max_f32 v9,0,v9
s_or_b64 s[90:91],s[90:91],vcc
v_add_f32 v9,0x3727c5ac,v9
v_rsq_f32 v9,v9
s_nop 0
v_mul_f32 v36,s96,v9
v_mul_f32 v37,s96,v9
v_mul_f32 v40,-v8,v36
v_mul_f32 v41,-v8,v36
v_permlane16_swap_b32 v36,v37
s_nop 0
v_permlane16_swap_b32 v40,v41
v_mov_b64 v[38:39],v[36:37]
v_mov_b64 v[42:43],v[40:41]
s_nop 0
v_permlane32_swap_b32 v36,v38
v_permlane32_swap_b32 v37,v39
v_permlane32_swap_b32 v40,v42
v_permlane32_swap_b32 v41,v43
s_waitcnt vmcnt(10)
s_barrier
ds_read_b64_tr_b16 a[192:193],v1 offset:18480
ds_read_b64_tr_b16 a[194:195],v1 offset:19504
ds_read_b64_tr_b16 a[196:197],v1 offset:20528
ds_read_b64_tr_b16 a[198:199],v1 offset:21552
ds_read_b64_tr_b16 a[200:201],v1 offset:22576
ds_read_b64_tr_b16 a[202:203],v1 offset:23600
ds_read_b64_tr_b16 a[204:205],v2 offset:22576
ds_read_b64_tr_b16 a[206:207],v2 offset:23600
ds_read_b64_tr_b16 a[208:209],v2 offset:20528
ds_read_b64_tr_b16 a[210:211],v2 offset:21552
ds_read_b64_tr_b16 a[212:213],v2 offset:18480
ds_read_b64_tr_b16 a[214:215],v2 offset:19504
ds_read_b128 v[16:19],v4 offset:0
ds_read_b128 v[20:23],v4 offset:32
ds_read_b64 v[14:15],v7 offset:18480
v_pk_fma_f32 v[64:65],v[64:65],v[36:37],v[40:41]
v_pk_fma_f32 v[66:67],v[66:67],v[38:39],v[42:43]
v_pk_fma_f32 v[68:69],v[68:69],v[36:37],v[40:41]
v_pk_fma_f32 v[70:71],v[70:71],v[38:39],v[42:43]
v_pk_fma_f32 v[72:73],v[72:73],v[36:37],v[40:41]
v_pk_fma_f32 v[74:75],v[74:75],v[38:39],v[42:43]
v_pk_fma_f32 v[76:77],v[76:77],v[36:37],v[40:41]
v_pk_fma_f32 v[78:79],v[78:79],v[38:39],v[42:43]
v_pk_fma_f32 v[112:113],v[112:113],v[36:37],v[40:41]
v_pk_fma_f32 v[114:115],v[114:115],v[38:39],v[42:43]
v_pk_fma_f32 v[116:117],v[116:117],v[36:37],v[40:41]
v_pk_fma_f32 v[118:119],v[118:119],v[38:39],v[42:43]
s_add_u32 m0,s57,81336
s_add_u32 s89,s56,2260992
buffer_load_dwordx4 v6,s[36:39],s89 offen lds
v_pk_fma_f32 v[136:137],v[136:137],v[36:37],v[40:41]
v_pk_fma_f32 v[138:139],v[138:139],v[38:39],v[42:43]
v_pk_fma_f32 v[140:141],v[140:141],v[36:37],v[40:41]
v_pk_fma_f32 v[142:143],v[142:143],v[38:39],v[42:43]
v_pk_fma_f32 v[80:81],v[80:81],v[36:37],v[40:41]
v_pk_fma_f32 v[82:83],v[82:83],v[38:39],v[42:43]
v_pk_fma_f32 v[84:85],v[84:85],v[36:37],v[40:41]
v_pk_fma_f32 v[86:87],v[86:87],v[38:39],v[42:43]
v_pk_fma_f32 v[88:89],v[88:89],v[36:37],v[40:41]
v_pk_fma_f32 v[90:91],v[90:91],v[38:39],v[42:43]
v_pk_fma_f32 v[92:93],v[92:93],v[36:37],v[40:41]
v_pk_fma_f32 v[94:95],v[94:95],v[38:39],v[42:43]
s_add_u32 m0,s58,12320
s_add_u32 s69,s69,s53
buffer_load_dwordx4 v5,s[44:47],s69 offen offset:2048 lds
v_pk_fma_f32 v[120:121],v[120:121],v[36:37],v[40:41]
v_pk_fma_f32 v[122:123],v[122:123],v[38:39],v[42:43]
v_pk_fma_f32 v[124:125],v[124:125],v[36:37],v[40:41]
v_pk_fma_f32 v[126:127],v[126:127],v[38:39],v[42:43]
v_pk_fma_f32 v[144:145],v[144:145],v[36:37],v[40:41]
v_pk_fma_f32 v[146:147],v[146:147],v[38:39],v[42:43]
v_pk_fma_f32 v[148:149],v[148:149],v[36:37],v[40:41]
v_pk_fma_f32 v[150:151],v[150:151],v[38:39],v[42:43]
v_pk_fma_f32 v[96:97],v[96:97],v[36:37],v[40:41]
v_pk_fma_f32 v[98:99],v[98:99],v[38:39],v[42:43]
v_pk_fma_f32 v[100:101],v[100:101],v[36:37],v[40:41]
v_pk_fma_f32 v[102:103],v[102:103],v[38:39],v[42:43]
s_add_u32 m0,s57,84864
s_add_u32 s89,s56,2359296
buffer_load_dwordx4 v6,s[36:39],s89 offen lds
v_pk_fma_f32 v[104:105],v[104:105],v[36:37],v[40:41]
v_pk_fma_f32 v[106:107],v[106:107],v[38:39],v[42:43]
v_pk_fma_f32 v[108:109],v[108:109],v[36:37],v[40:41]
v_pk_fma_f32 v[110:111],v[110:111],v[38:39],v[42:43]
v_pk_fma_f32 v[128:129],v[128:129],v[36:37],v[40:41]
v_pk_fma_f32 v[130:131],v[130:131],v[38:39],v[42:43]
v_pk_fma_f32 v[132:133],v[132:133],v[36:37],v[40:41]
v_pk_fma_f32 v[134:135],v[134:135],v[38:39],v[42:43]
v_pk_fma_f32 v[152:153],v[152:153],v[36:37],v[40:41]
v_pk_fma_f32 v[154:155],v[154:155],v[38:39],v[42:43]
v_pk_fma_f32 v[156:157],v[156:157],v[36:37],v[40:41]
v_pk_fma_f32 v[158:159],v[158:159],v[38:39],v[42:43]
v_cvt_pk_bf16_f32 v64,v64,v68
s_waitcnt lgkmcnt(0)
v_or3_b32 v252,v14,v15,0
ds_read_b64 v[14:15],v7 offset:18992
v_cvt_pk_bf16_f32 v72,v72,v76
v_cvt_pk_bf16_f32 v112,v112,v116
v_accvgpr_write_b32 a0,v64
v_accvgpr_write_b32 a1,v72
v_cvt_pk_bf16_f32 v136,v136,v140
v_accvgpr_write_b32 a2,v112
v_cvt_pk_bf16_f32 v65,v65,v69
v_accvgpr_write_b32 a3,v136
v_cvt_pk_bf16_f32 v73,v73,v77
v_accvgpr_write_b32 a4,v65
v_cvt_pk_bf16_f32 v113,v113,v117
v_accvgpr_write_b32 a5,v73
v_cvt_pk_bf16_f32 v137,v137,v141
v_accvgpr_write_b32 a6,v113
v_cvt_pk_bf16_f32 v80,v80,v84
v_accvgpr_write_b32 a7,v137
v_cvt_pk_bf16_f32 v88,v88,v92
v_accvgpr_write_b32 a16,v80
v_cvt_pk_bf16_f32 v120,v120,v124
s_waitcnt lgkmcnt(0)
v_or3_b32 v252,v14,v15,v252
ds_read_b64 v[14:15],v7 offset:19504
v_accvgpr_write_b32 a17,v88
v_cvt_pk_bf16_f32 v144,v144,v148
v_accvgpr_write_b32 a18,v120
v_cvt_pk_bf16_f32 v81,v81,v85
v_accvgpr_write_b32 a19,v144
v_cvt_pk_bf16_f32 v89,v89,v93
v_accvgpr_write_b32 a20,v81
v_accvgpr_write_b32 a21,v89
v_cvt_pk_bf16_f32 v121,v121,v125
v_accvgpr_write_b32 a22,v121
v_cvt_pk_bf16_f32 v145,v145,v149
v_accvgpr_write_b32 a23,v145
v_cvt_pk_bf16_f32 v96,v96,v100
v_accvgpr_write_b32 a32,v96
v_cvt_pk_bf16_f32 v104,v104,v108
v_accvgpr_write_b32 a33,v104
v_cvt_pk_bf16_f32 v128,v128,v132
v_accvgpr_write_b32 a34,v128
v_cvt_pk_bf16_f32 v152,v152,v156
s_waitcnt lgkmcnt(0)
v_or3_b32 v252,v14,v15,v252
v_accvgpr_write_b32 a35,v152
v_cvt_pk_bf16_f32 v97,v97,v101
v_accvgpr_write_b32 a36,v97
v_cvt_pk_bf16_f32 v105,v105,v109
v_accvgpr_write_b32 a37,v105
v_cvt_pk_bf16_f32 v129,v129,v133
v_accvgpr_write_b32 a38,v129
v_cvt_pk_bf16_f32 v153,v153,v157
v_accvgpr_write_b32 a39,v153
v_mfma_f32_16x16x32_bf16 v[44:47],a[192:195],a[0:3],v[16:19]
v_cvt_pk_bf16_f32 v66,v66,v70
v_cvt_pk_bf16_f32 v74,v74,v78
v_cvt_pk_bf16_f32 v114,v114,v118
v_accvgpr_write_b32 a8,v66
v_mfma_f32_16x16x32_bf16 v[44:47],a[196:199],a[16:19],v[44:47]
v_accvgpr_write_b32 a9,v74
v_cvt_pk_bf16_f32 v138,v138,v142
v_accvgpr_write_b32 a10,v114
v_cvt_pk_bf16_f32 v67,v67,v71
v_mfma_f32_16x16x32_bf16 v[44:47],a[200:203],a[32:35],v[44:47]
v_accvgpr_write_b32 a11,v138
v_cvt_pk_bf16_f32 v75,v75,v79
v_accvgpr_write_b32 a12,v67
v_cvt_pk_bf16_f32 v115,v115,v119
v_mfma_f32_16x16x32_bf16 v[48:51],a[204:207],a[32:35],v[20:23]
v_accvgpr_write_b32 a13,v75
v_cvt_pk_bf16_f32 v139,v139,v143
v_accvgpr_write_b32 a14,v115
v_cvt_pk_bf16_f32 v82,v82,v86
v_mfma_f32_16x16x32_bf16 v[48:51],a[208:211],a[16:19],v[48:51]
v_accvgpr_write_b32 a15,v139
v_cvt_pk_bf16_f32 v90,v90,v94
v_accvgpr_write_b32 a24,v82
v_cvt_pk_bf16_f32 v122,v122,v126
v_mfma_f32_16x16x32_bf16 v[48:51],a[212:215],a[0:3],v[48:51]
v_accvgpr_write_b32 a25,v90
v_cvt_pk_bf16_f32 v146,v146,v150
v_accvgpr_write_b32 a26,v122
v_cvt_pk_bf16_f32 v83,v83,v87
v_mfma_f32_16x16x32_bf16 v[52:55],a[212:215],a[4:7],v[20:23]
TE00_%=:
s_cbranch_execz TP3326_%=
v_accvgpr_write_b32 a27,v146
v_cvt_pk_bf16_f32 v91,v91,v95
v_accvgpr_write_b32 a28,v83
v_accvgpr_write_b32 a29,v91
v_mfma_f32_16x16x32_bf16 v[52:55],a[208:211],a[20:23],v[52:55]
v_cvt_pk_bf16_f32 v123,v123,v127
v_accvgpr_write_b32 a30,v123
v_cvt_pk_bf16_f32 v147,v147,v151
v_accvgpr_write_b32 a31,v147
v_mfma_f32_16x16x32_bf16 v[52:55],a[204:207],a[36:39],v[52:55]
v_cvt_pk_bf16_f32 v98,v98,v102
v_accvgpr_write_b32 a40,v98
v_cvt_pk_bf16_f32 v106,v106,v110
v_accvgpr_write_b32 a41,v106
v_mfma_f32_16x16x32_bf16 v[56:59],a[200:203],a[36:39],v[16:19]
v_cvt_pk_bf16_f32 v130,v130,v134
v_accvgpr_write_b32 a42,v130
v_cvt_pk_bf16_f32 v154,v154,v158
v_accvgpr_write_b32 a43,v154
v_mfma_f32_16x16x32_bf16 v[56:59],a[196:199],a[20:23],v[56:59]
v_cvt_pk_bf16_f32 v99,v99,v103
v_accvgpr_write_b32 a44,v99
v_cvt_pk_bf16_f32 v107,v107,v111
v_accvgpr_write_b32 a45,v107
v_mfma_f32_16x16x32_bf16 v[56:59],a[192:195],a[4:7],v[56:59]
v_cvt_pk_bf16_f32 v131,v131,v135
v_accvgpr_write_b32 a46,v131
v_cvt_pk_bf16_f32 v155,v155,v159
v_accvgpr_write_b32 a47,v155
v_mfma_f32_16x16x32_bf16 v[60:63],a[192:195],a[8:11],v[16:19]
ds_read_b64 v[14:15],v7 offset:0
v_lshlrev_b32_sdwa v44,1,v44 dst_sel:DWORD dst_unused:UNUSED_PAD src0_sel:DWORD src1_sel:WORD_1
v_mfma_f32_16x16x32_bf16 v[60:63],a[196:199],a[24:27],v[60:63]
ds_read_b64_tr_b16 a[216:217],v1 offset:0
v_fma_mixlo_f16 v8,v53,s99,0
v_mfma_f32_16x16x32_bf16 v[60:63],a[200:203],a[40:43],v[60:63]
ds_read_b64_tr_b16 a[218:219],v1 offset:1024
v_lshlrev_b32_sdwa v46,1,v46 dst_sel:DWORD dst_unused:UNUSED_PAD src0_sel:DWORD src1_sel:WORD_1
v_mfma_f32_16x16x32_bf16 v[32:35],a[204:207],a[40:43],v[20:23]
ds_read_b64_tr_b16 a[220:221],v1 offset:2048
v_fma_mixhi_f16 v8,v55,s99,0
v_mfma_f32_16x16x32_bf16 v[32:35],a[208:211],a[24:27],v[32:35]
ds_read_b64_tr_b16 a[222:223],v1 offset:3072
v_lshlrev_b32_sdwa v45,1,v45 dst_sel:DWORD dst_unused:UNUSED_PAD src0_sel:DWORD src1_sel:WORD_1
v_mfma_f32_16x16x32_bf16 v[32:35],a[212:215],a[8:11],v[32:35]
ds_read_u16 v44,v44
v_pk_fma_f16 v9,v8,v8,s54
v_mfma_f32_16x16x32_bf16 v[36:39],a[212:215],a[12:15],v[20:23]
ds_read_b128 v[20:23],v4 offset:160
v_lshlrev_b32_sdwa v47,1,v47 dst_sel:DWORD dst_unused:UNUSED_PAD src0_sel:DWORD src1_sel:WORD_1
v_mfma_f32_16x16x32_bf16 v[36:39],a[208:211],a[28:31],v[36:39]
s_waitcnt lgkmcnt(6)
ds_read_b64_tr_b16 a[212:213],v1 offset:4096
ds_read_b64_tr_b16 a[214:215],v1 offset:5120
v_or3_b32 v252,v14,v15,v252
v_mfma_f32_16x16x32_bf16 v[36:39],a[204:207],a[44:47],v[36:39]
ds_read_b64 v[14:15],v7 offset:512
ds_read_b64_tr_b16 a[208:209],v2 offset:4096
v_mfma_f32_16x16x32_bf16 v[40:43],a[200:203],a[44:47],v[16:19]
ds_read_b128 v[16:19],v4 offset:128
ds_read_b64_tr_b16 a[210:211],v2 offset:5120
v_pk_mul_f16 v9,v8,v9
v_mfma_f32_16x16x32_bf16 v[40:43],a[196:199],a[28:31],v[40:43]
ds_read_b64_tr_b16 a[204:205],v2 offset:2048
ds_read_b64_tr_b16 a[206:207],v2 offset:3072
v_lshlrev_b32_sdwa v48,1,v48 dst_sel:DWORD dst_unused:UNUSED_PAD src0_sel:DWORD src1_sel:WORD_1
v_mfma_f32_16x16x32_bf16 v[40:43],a[192:195],a[12:15],v[40:43]
ds_read_b64_tr_b16 a[200:201],v2 offset:0
v_exp_f16 v8,v9
v_mfma_f32_16x16x32_bf16 v[136:139],a[240:243],a[224:227],v[240:243]
ds_read_b64_tr_b16 a[202:203],v2 offset:1024
v_lshlrev_b32_sdwa v50,1,v50 dst_sel:DWORD dst_unused:UNUSED_PAD src0_sel:DWORD src1_sel:WORD_1
v_mfma_f32_16x16x32_bf16 v[140:143],a[240:243],a[228:231],v[240:243]
ds_read_u16_d16_hi v46,v46
v_lshlrev_b32_sdwa v49,1,v49 dst_sel:DWORD dst_unused:UNUSED_PAD src0_sel:DWORD src1_sel:WORD_1
v_mfma_f32_16x16x32_bf16 v[112:115],a[240:243],a[232:235],v[240:243]
ds_read_u16 v45,v45
v_lshlrev_b32_sdwa v51,1,v51 dst_sel:DWORD dst_unused:UNUSED_PAD src0_sel:DWORD src1_sel:WORD_1
v_mfma_f32_16x16x32_bf16 v[116:119],a[240:243],a[236:239],v[240:243]
ds_read_b128 a[240:243],v13 offset:512
ds_read_b128 v[240:243],v4 offset:3712
v_mfma_f32_16x16x32_bf16 v[64:67],a[244:247],a[224:227],v[244:247]
s_waitcnt lgkmcnt(11)
ds_read_b64 a[224:225],v248 offset:57408
ds_read_b64 a[226:227],v248 offset:57416
v_or3_b32 v252,v14,v15,v252
v_mfma_f32_16x16x32_bf16 v[68:71],a[244:247],a[228:231],v[244:247]
ds_read_b64 v[14:15],v7 offset:1024
ds_read_b64 a[228:229],v249 offset:57408
v_exp_f16_sdwa v8,v9 dst_sel:WORD_1 dst_unused:UNUSED_PRESERVE src0_sel:WORD_1
v_mfma_f32_16x16x32_bf16 v[72:75],a[244:247],a[232:235],v[244:247]
ds_read_b64 a[230:231],v249 offset:57416
ds_read_b64 a[232:233],v250 offset:57408
v_mfma_f32_16x16x32_bf16 v[76:79],a[244:247],a[236:239],v[244:247]
s_waitcnt lgkmcnt(4)
ds_read_b64 a[234:235],v250 offset:57416
ds_read_b64 a[236:237],v251 offset:57408
v_mfma_f32_16x16x32_bf16 v[144:147],a[240:243],a[224:227],v[240:243]
s_waitcnt lgkmcnt(3)
ds_read_b64 a[238:239],v251 offset:57416
ds_read_b128 a[244:247],v13 offset:768
v_lshlrev_b32_sdwa v56,1,v56 dst_sel:DWORD dst_unused:UNUSED_PAD src0_sel:DWORD src1_sel:WORD_1
v_mfma_f32_16x16x32_bf16 v[148:151],a[240:243],a[228:231],v[240:243]
s_waitcnt lgkmcnt(3)
ds_read_b128 v[244:247],v4 offset:3744
ds_read_u16_d16_hi v47,v47
v_pk_fma_f16 v8,v8,s55,s55
v_mfma_f32_16x16x32_bf16 v[120:123],a[240:243],a[232:235],v[240:243]
s_waitcnt lgkmcnt(3)
ds_read_u16 v48,v48
v_lshlrev_b32_sdwa v58,1,v58 dst_sel:DWORD dst_unused:UNUSED_PAD src0_sel:DWORD src1_sel:WORD_1
v_mfma_f32_16x16x32_bf16 v[124:127],a[240:243],a[236:239],v[240:243]
s_waitcnt lgkmcnt(2)
ds_read_b128 a[240:243],v13 offset:1024
ds_read_b128 v[240:243],v4 offset:3840
v_rcp_f16 v9,v8
v_mfma_f32_16x16x32_bf16 v[80:83],a[244:247],a[224:227],v[244:247]
ds_read_b64 a[224:225],v248 offset:57472
ds_read_b64 a[226:227],v248 offset:57480
v_mfma_f32_16x16x32_bf16 v[84:87],a[244:247],a[228:231],v[244:247]
ds_read_b64 a[228:229],v249 offset:57472
ds_read_b64 a[230:231],v249 offset:57480
v_or3_b32 v252,v14,v15,v252
v_lshlrev_b32_sdwa v57,1,v57 dst_sel:DWORD dst_unused:UNUSED_PAD src0_sel:DWORD src1_sel:WORD_1
v_mfma_f32_16x16x32_bf16 v[88:91],a[244:247],a[232:235],v[244:247]
ds_read_b64 a[232:233],v250 offset:57472
ds_read_b64 a[234:235],v250 offset:57480
v_mfma_f32_16x16x32_bf16 v[92:95],a[244:247],a[236:239],v[244:247]
s_waitcnt lgkmcnt(4)
ds_read_b64 a[236:237],v251 offset:57472
ds_read_b64 a[238:239],v251 offset:57480
v_lshlrev_b32_sdwa v59,1,v59 dst_sel:DWORD dst_unused:UNUSED_PAD src0_sel:DWORD src1_sel:WORD_1
v_mfma_f32_16x16x32_bf16 v[152:155],a[240:243],a[224:227],v[240:243]
s_waitcnt lgkmcnt(4)
ds_read_b128 a[244:247],v13 offset:1280
ds_read_b128 v[244:247],v4 offset:3872
v_rcp_f16_sdwa v9,v8 dst_sel:WORD_1 dst_unused:UNUSED_PRESERVE src0_sel:WORD_1
v_mfma_f32_16x16x32_bf16 v[156:159],a[240:243],a[228:231],v[240:243]
s_waitcnt lgkmcnt(4)
ds_read_u16_d16_hi v50,v50
ds_read_u16 v49,v49
v_mfma_f32_16x16x32_bf16 v[128:131],a[240:243],a[232:235],v[240:243]
s_waitcnt lgkmcnt(4)
ds_read_u16_d16_hi v51,v51
ds_read_u16 v56,v56
v_lshlrev_b32_sdwa v52,1,v52 dst_sel:DWORD dst_unused:UNUSED_PAD src0_sel:DWORD src1_sel:WORD_1
v_mfma_f32_16x16x32_bf16 v[132:135],a[240:243],a[236:239],v[240:243]
s_waitcnt lgkmcnt(4)
ds_read_u16_d16_hi v58,v58
ds_read_u16 v57,v57
v_mfma_f32_16x16x32_bf16 v[96:99],a[244:247],a[224:227],v[244:247]
ds_read_u16_d16_hi v59,v59
ds_read_u16 v52,v52
v_lshlrev_b32_sdwa v54,1,v54 dst_sel:DWORD dst_unused:UNUSED_PAD src0_sel:DWORD src1_sel:WORD_1
v_mfma_f32_16x16x32_bf16 v[100:103],a[244:247],a[228:231],v[244:247]
ds_read_u16_d16_hi v54,v54
ds_read_b64_tr_b16 a[248:249],v3 offset:0
v_fma_mix_f32 v53,v53,v9,0 op_sel_hi:[0,1,0]
v_mfma_f32_16x16x32_bf16 v[104:107],a[244:247],a[232:235],v[244:247]
ds_read_b64_tr_b16 a[250:251],v3 offset:1536
ds_read_b64_tr_b16 a[252:253],v3 offset:8
v_mfma_f32_16x16x32_bf16 v[108:111],a[244:247],a[236:239],v[244:247]
s_barrier
ds_read_b64_tr_b16 a[254:255],v3 offset:1544
ds_read_b64_tr_b16 a[240:241],v3 offset:64
v_fma_mix_f32 v55,v55,v9,0 op_sel:[0,1,0] op_sel_hi:[0,1,0]
v_mfma_f32_16x16x32_bf16 v[24:27],a[216:219],a[0:3],v[16:19]
s_add_u32 m0,s57,0
ds_read_b64_tr_b16 a[242:243],v3 offset:1600
ds_read_b64_tr_b16 a[224:225],v3 offset:72
v_lshlrev_b32_sdwa v60,1,v60 dst_sel:DWORD dst_unused:UNUSED_PAD src0_sel:DWORD src1_sel:WORD_1
v_mfma_f32_16x16x32_bf16 v[24:27],a[220:223],a[16:19],v[24:27]
s_add_u32 s89,s56,2457600
ds_read_b64_tr_b16 a[226:227],v3 offset:1608
v_fma_mixlo_f16 v8,v37,s99,0
v_mfma_f32_16x16x32_bf16 v[24:27],a[212:215],a[32:35],v[24:27]
buffer_load_dwordx4 v6,s[36:39],s89 offen lds
v_lshl_add_u64 v[44:45],v[44:45],0,v[46:47]
v_lshlrev_b32_sdwa v62,1,v62 dst_sel:DWORD dst_unused:UNUSED_PAD src0_sel:DWORD src1_sel:WORD_1
v_mfma_f32_16x16x32_bf16 v[28:31],a[208:211],a[32:35],v[20:23]
s_waitcnt lgkmcnt(13)
ds_read_b64_tr_b16 a[228:229],v3 offset:128
v_lshl_add_u64 v[46:47],v[48:49],0,v[50:51]
v_fma_mixhi_f16 v8,v39,s99,0
v_mfma_f32_16x16x32_bf16 v[28:31],a[204:207],a[16:19],v[28:31]
s_waitcnt lgkmcnt(11)
ds_read_b64_tr_b16 a[230:231],v3 offset:1664
v_lshl_add_u64 v[48:49],v[56:57],0,v[58:59]
v_lshlrev_b32_sdwa v61,1,v61 dst_sel:DWORD dst_unused:UNUSED_PAD src0_sel:DWORD src1_sel:WORD_1
v_pk_fma_f16 v9,v8,v8,s54
v_mfma_f32_16x16x32_bf16 v[28:31],a[200:203],a[0:3],v[28:31]
s_add_u32 m0,s58,18480
ds_read_b64_tr_b16 a[232:233],v3 offset:136
v_lshlrev_b32_sdwa v63,1,v63 dst_sel:DWORD dst_unused:UNUSED_PAD src0_sel:DWORD src1_sel:WORD_1
v_mfma_f32_16x16x32_bf16 v[56:59],a[200:203],a[4:7],v[20:23]
s_waitcnt lgkmcnt(10)
ds_read_b64_tr_b16 a[234:235],v3 offset:1672
v_or_b32 v50,v52,v54
v_pk_mul_f16 v9,v8,v9
v_mfma_f32_16x16x32_bf16 v[56:59],a[204:207],a[20:23],v[56:59]
s_waitcnt vmcnt(8)
ds_read_u16 v60,v60
v_cvt_pk_bf16_f32 v51,v53,v55
v_lshlrev_b32_sdwa v32,1,v32 dst_sel:DWORD dst_unused:UNUSED_PAD src0_sel:DWORD src1_sel:WORD_1
v_mfma_f32_16x16x32_bf16 v[56:59],a[208:211],a[36:39],v[56:59]
s_barrier
v_exp_f16 v8,v9
v_mfma_f32_16x16x32_bf16 v[52:55],a[212:215],a[36:39],v[16:19]
s_add_u32 s69,s69,s59
ds_read_u16_d16_hi v62,v62
ds_read_u16 v61,v61
v_lshlrev_b32_sdwa v34,1,v34 dst_sel:DWORD dst_unused:UNUSED_PAD src0_sel:DWORD src1_sel:WORD_1
v_mfma_f32_16x16x32_bf16 v[52:55],a[220:223],a[20:23],v[52:55]
buffer_load_dwordx4 v5,s[44:47],s69 offen offset:0 lds
ds_read_u16_d16_hi v63,v63
v_lshlrev_b32_sdwa v33,1,v33 dst_sel:DWORD dst_unused:UNUSED_PAD src0_sel:DWORD src1_sel:WORD_1
v_mfma_f32_16x16x32_bf16 v[52:55],a[216:219],a[4:7],v[52:55]
s_waitcnt lgkmcnt(8)
ds_read_u16 v32,v32
v_exp_f16_sdwa v8,v9 dst_sel:WORD_1 dst_unused:UNUSED_PRESERVE src0_sel:WORD_1
v_mfma_f32_16x16x32_bf16 v[136:139],a[248:251],v[44:47],v[136:139]
s_add_u32 s69,s69,s53
ds_read_u16_d16_hi v34,v34
v_lshlrev_b32_sdwa v35,1,v35 dst_sel:DWORD dst_unused:UNUSED_PAD src0_sel:DWORD src1_sel:WORD_1
v_mfma_f32_16x16x32_bf16 v[140:143],a[248:251],v[48:51],v[140:143]
buffer_load_dwordx4 v5,s[44:47],s69 offen offset:1024 lds
ds_read_u16 v33,v33
v_lshlrev_b32_sdwa v40,1,v40 dst_sel:DWORD dst_unused:UNUSED_PAD src0_sel:DWORD src1_sel:WORD_1
v_mfma_f32_16x16x32_bf16 v[68:71],a[252:255],v[48:51],v[68:71]
s_add_u32 s69,s69,s53
ds_read_u16_d16_hi v35,v35
v_pk_fma_f16 v8,v8,s55,s55
v_mfma_f32_16x16x32_bf16 v[64:67],a[252:255],v[44:47],v[64:67]
v_rcp_f16 v9,v8
v_lshlrev_b32_sdwa v42,1,v42 dst_sel:DWORD dst_unused:UNUSED_PAD src0_sel:DWORD src1_sel:WORD_1
v_mfma_f32_16x16x32_bf16 v[144:147],a[240:243],v[44:47],v[144:147]
buffer_load_dwordx4 v5,s[44:47],s69 offen offset:2048 lds
ds_read_u16 v40,v40
v_mfma_f32_16x16x32_bf16 v[148:151],a[240:243],v[48:51],v[148:151]
ds_read_u16_d16_hi v42,v42
v_lshlrev_b32_sdwa v41,1,v41 dst_sel:DWORD dst_unused:UNUSED_PAD src0_sel:DWORD src1_sel:WORD_1
v_mfma_f32_16x16x32_bf16 v[84:87],a[224:227],v[48:51],v[84:87]
ds_read_u16 v41,v41
v_lshlrev_b32_sdwa v43,1,v43 dst_sel:DWORD dst_unused:UNUSED_PAD src0_sel:DWORD src1_sel:WORD_1
v_mfma_f32_16x16x32_bf16 v[80:83],a[224:227],v[44:47],v[80:83]
s_waitcnt lgkmcnt(7)
v_rcp_f16_sdwa v9,v8 dst_sel:WORD_1 dst_unused:UNUSED_PRESERVE src0_sel:WORD_1
v_mfma_f32_16x16x32_bf16 v[152:155],a[228:231],v[44:47],v[152:155]
s_add_u32 m0,s57,3544
ds_read_u16_d16_hi v43,v43
v_lshlrev_b32_sdwa v36,1,v36 dst_sel:DWORD dst_unused:UNUSED_PAD src0_sel:DWORD src1_sel:WORD_1
v_mfma_f32_16x16x32_bf16 v[156:159],a[228:231],v[48:51],v[156:159]
s_add_u32 s89,s56,2555904
ds_read_u16 v36,v36
v_lshlrev_b32_sdwa v38,1,v38 dst_sel:DWORD dst_unused:UNUSED_PAD src0_sel:DWORD src1_sel:WORD_1
v_mfma_f32_16x16x32_bf16 v[100:103],a[232:235],v[48:51],v[100:103]
buffer_load_dwordx4 v6,s[36:39],s89 offen lds
v_fma_mix_f32 v37,v37,v9,0 op_sel_hi:[0,1,0]
v_mfma_f32_16x16x32_bf16 v[96:99],a[232:235],v[44:47],v[96:99]
ds_read_u16_d16_hi v38,v38
v_fma_mix_f32 v39,v39,v9,0 op_sel:[0,1,0] op_sel_hi:[0,1,0]
v_mfma_f32_16x16x32_bf16 v[48:51],a[216:219],a[8:11],v[16:19]
ds_read_b64 v[14:15],v7 offset:6160
ds_read_b64_tr_b16 a[196:197],v1 offset:6160
v_lshlrev_b32_sdwa v24,1,v24 dst_sel:DWORD dst_unused:UNUSED_PAD src0_sel:DWORD src1_sel:WORD_1
v_mfma_f32_16x16x32_bf16 v[48:51],a[220:223],a[24:27],v[48:51]
ds_read_b64_tr_b16 a[198:199],v1 offset:7184
ds_read_b64_tr_b16 a[192:193],v1 offset:8208
v_fma_mixlo_f16 v8,v57,s99,0
v_mfma_f32_16x16x32_bf16 v[48:51],a[212:215],a[40:43],v[48:51]
ds_read_b64_tr_b16 a[194:195],v1 offset:9232
v_lshl_add_u64 v[60:61],v[60:61],0,v[62:63]
v_lshlrev_b32_sdwa v26,1,v26 dst_sel:DWORD dst_unused:UNUSED_PAD src0_sel:DWORD src1_sel:WORD_1
v_mfma_f32_16x16x32_bf16 v[44:47],a[208:211],a[40:43],v[20:23]
s_waitcnt lgkmcnt(8)
ds_read_u16 v24,v24
v_lshl_add_u64 v[62:63],v[32:33],0,v[34:35]
v_fma_mixhi_f16 v8,v59,s99,0
v_mfma_f32_16x16x32_bf16 v[44:47],a[204:207],a[24:27],v[44:47]
s_waitcnt lgkmcnt(8)
ds_read_u16_d16_hi v26,v26
v_lshl_add_u64 v[32:33],v[40:41],0,v[42:43]
v_lshlrev_b32_sdwa v25,1,v25 dst_sel:DWORD dst_unused:UNUSED_PAD src0_sel:DWORD src1_sel:WORD_1
v_pk_fma_f16 v9,v8,v8,s54
v_mfma_f32_16x16x32_bf16 v[44:47],a[200:203],a[8:11],v[44:47]
s_waitcnt lgkmcnt(6)
ds_read_u16 v25,v25
v_or3_b32 v252,v14,v15,v252
v_lshlrev_b32_sdwa v27,1,v27 dst_sel:DWORD dst_unused:UNUSED_PAD src0_sel:DWORD src1_sel:WORD_1
v_mfma_f32_16x16x32_bf16 v[40:43],a[200:203],a[12:15],v[20:23]
ds_read_b64 v[14:15],v7 offset:6672
ds_read_b128 v[20:23],v4 offset:288
v_or_b32 v34,v36,v38
v_pk_mul_f16 v9,v8,v9
v_mfma_f32_16x16x32_bf16 v[40:43],a[204:207],a[28:31],v[40:43]
ds_read_u16_d16_hi v27,v27
ds_read_b64_tr_b16 a[200:201],v1 offset:10256
v_cvt_pk_bf16_f32 v35,v37,v39
v_lshlrev_b32_sdwa v28,1,v28 dst_sel:DWORD dst_unused:UNUSED_PAD src0_sel:DWORD src1_sel:WORD_1
v_mfma_f32_16x16x32_bf16 v[40:43],a[208:211],a[44:47],v[40:43]
ds_read_b64_tr_b16 a[202:203],v1 offset:11280
v_exp_f16 v8,v9
v_mfma_f32_16x16x32_bf16 v[36:39],a[212:215],a[44:47],v[16:19]
ds_read_b128 v[16:19],v4 offset:256
ds_read_b64_tr_b16 a[204:205],v2 offset:10256
v_lshlrev_b32_sdwa v30,1,v30 dst_sel:DWORD dst_unused:UNUSED_PAD src0_sel:DWORD src1_sel:WORD_1
v_mfma_f32_16x16x32_bf16 v[36:39],a[220:223],a[28:31],v[36:39]
ds_read_b64_tr_b16 a[206:207],v2 offset:11280
ds_read_u16 v28,v28
v_lshlrev_b32_sdwa v29,1,v29 dst_sel:DWORD dst_unused:UNUSED_PAD src0_sel:DWORD src1_sel:WORD_1
v_mfma_f32_16x16x32_bf16 v[36:39],a[216:219],a[12:15],v[36:39]
s_waitcnt lgkmcnt(8)
ds_read_u16_d16_hi v30,v30
ds_read_u16 v29,v29
v_or3_b32 v252,v14,v15,v252
v_mfma_f32_16x16x32_bf16 v[112:115],a[248:251],v[60:63],v[112:115]
ds_read_b64 v[14:15],v7 offset:7184
ds_read_b64_tr_b16 a[208:209],v2 offset:8208
v_exp_f16_sdwa v8,v9 dst_sel:WORD_1 dst_unused:UNUSED_PRESERVE src0_sel:WORD_1
v_lshlrev_b32_sdwa v31,1,v31 dst_sel:DWORD dst_unused:UNUSED_PAD src0_sel:DWORD src1_sel:WORD_1
v_mfma_f32_16x16x32_bf16 v[116:119],a[248:251],v[32:35],v[116:119]
ds_read_u16_d16_hi v31,v31
ds_read_b64_tr_b16 a[210:211],v2 offset:9232
v_lshlrev_b32_sdwa v52,1,v52 dst_sel:DWORD dst_unused:UNUSED_PAD src0_sel:DWORD src1_sel:WORD_1
v_mfma_f32_16x16x32_bf16 v[76:79],a[252:255],v[32:35],v[76:79]
s_add_u32 m0,s57,7080
ds_read_u16 v52,v52
ds_read_b64_tr_b16 a[212:213],v2 offset:6160
v_pk_fma_f16 v8,v8,s55,s55
v_mfma_f32_16x16x32_bf16 v[72:75],a[252:255],v[60:63],v[72:75]
s_add_u32 s89,s56,2654208
ds_read_b64_tr_b16 a[214:215],v2 offset:7184
v_rcp_f16 v9,v8
v_lshlrev_b32_sdwa v54,1,v54 dst_sel:DWORD dst_unused:UNUSED_PAD src0_sel:DWORD src1_sel:WORD_1
v_mfma_f32_16x16x32_bf16 v[120:123],a[240:243],v[60:63],v[120:123]
buffer_load_dwordx4 v6,s[36:39],s89 offen lds
ds_read_u16_d16_hi v54,v54
v_mfma_f32_16x16x32_bf16 v[124:127],a[240:243],v[32:35],v[124:127]
s_waitcnt lgkmcnt(7)
ds_read_b64_tr_b16 a[236:237],v3 offset:6160
ds_read_b64_tr_b16 a[238:239],v3 offset:7696
v_or3_b32 v252,v14,v15,v252
v_lshlrev_b32_sdwa v53,1,v53 dst_sel:DWORD dst_unused:UNUSED_PAD src0_sel:DWORD src1_sel:WORD_1
v_mfma_f32_16x16x32_bf16 v[92:95],a[224:227],v[32:35],v[92:95]
ds_read_u16 v53,v53
ds_read_b64_tr_b16 a[244:245],v3 offset:6168
v_lshlrev_b32_sdwa v55,1,v55 dst_sel:DWORD dst_unused:UNUSED_PAD src0_sel:DWORD src1_sel:WORD_1
v_mfma_f32_16x16x32_bf16 v[88:91],a[224:227],v[60:63],v[88:91]
ds_read_u16_d16_hi v55,v55
v_rcp_f16_sdwa v9,v8 dst_sel:WORD_1 dst_unused:UNUSED_PRESERVE src0_sel:WORD_1
v_mfma_f32_16x16x32_bf16 v[128:131],a[228:231],v[60:63],v[128:131]
ds_read_b64_tr_b16 a[246:247],v3 offset:7704
ds_read_b64_tr_b16 a[248:249],v3 offset:6224
v_lshlrev_b32_sdwa v56,1,v56 dst_sel:DWORD dst_unused:UNUSED_PAD src0_sel:DWORD src1_sel:WORD_1
v_mfma_f32_16x16x32_bf16 v[132:135],a[228:231],v[32:35],v[132:135]
ds_read_u16 v56,v56
ds_read_b64_tr_b16 a[250:251],v3 offset:7760
v_lshlrev_b32_sdwa v58,1,v58 dst_sel:DWORD dst_unused:UNUSED_PAD src0_sel:DWORD src1_sel:WORD_1
v_mfma_f32_16x16x32_bf16 v[108:111],a[232:235],v[32:35],v[108:111]
ds_read_u16_d16_hi v58,v58
ds_read_b64_tr_b16 a[252:253],v3 offset:6232
v_fma_mix_f32 v57,v57,v9,0 op_sel_hi:[0,1,0]
v_mfma_f32_16x16x32_bf16 v[104:107],a[232:235],v[60:63],v[104:107]
ds_read_b64_tr_b16 a[254:255],v3 offset:7768
v_fma_mix_f32 v59,v59,v9,0 op_sel:[0,1,0] op_sel_hi:[0,1,0]
v_mfma_f32_16x16x32_bf16 v[32:35],a[196:199],a[0:3],v[16:19]
ds_read_b64_tr_b16 a[240:241],v3 offset:6288
v_lshlrev_b32_sdwa v48,1,v48 dst_sel:DWORD dst_unused:UNUSED_PAD src0_sel:DWORD src1_sel:WORD_1
v_mfma_f32_16x16x32_bf16 v[32:35],a[192:195],a[16:19],v[32:35]
ds_read_b64_tr_b16 a[242:243],v3 offset:7824
v_fma_mixlo_f16 v8,v41,s99,0
v_mfma_f32_16x16x32_bf16 v[32:35],a[200:203],a[32:35],v[32:35]
ds_read_b64_tr_b16 a[224:225],v3 offset:6296
v_lshl_add_u64 v[24:25],v[24:25],0,v[26:27]
v_lshlrev_b32_sdwa v50,1,v50 dst_sel:DWORD dst_unused:UNUSED_PAD src0_sel:DWORD src1_sel:WORD_1
v_mfma_f32_16x16x32_bf16 v[60:63],a[204:207],a[32:35],v[20:23]
s_waitcnt lgkmcnt(11)
ds_read_b64_tr_b16 a[226:227],v3 offset:7832
v_lshl_add_u64 v[26:27],v[28:29],0,v[30:31]
v_fma_mixhi_f16 v8,v43,s99,0
v_mfma_f32_16x16x32_bf16 v[60:63],a[208:211],a[16:19],v[60:63]
s_waitcnt lgkmcnt(11)
ds_read_u16 v48,v48
v_lshl_add_u64 v[28:29],v[52:53],0,v[54:55]
v_lshlrev_b32_sdwa v49,1,v49 dst_sel:DWORD dst_unused:UNUSED_PAD src0_sel:DWORD src1_sel:WORD_1
v_pk_fma_f16 v9,v8,v8,s54
v_mfma_f32_16x16x32_bf16 v[60:63],a[212:215],a[0:3],v[60:63]
ds_read_u16_d16_hi v50,v50
v_lshlrev_b32_sdwa v51,1,v51 dst_sel:DWORD dst_unused:UNUSED_PAD src0_sel:DWORD src1_sel:WORD_1
v_mfma_f32_16x16x32_bf16 v[52:55],a[212:215],a[4:7],v[20:23]
s_waitcnt lgkmcnt(8)
ds_read_u16 v49,v49
v_or_b32 v30,v56,v58
v_pk_mul_f16 v9,v8,v9
v_mfma_f32_16x16x32_bf16 v[52:55],a[208:211],a[20:23],v[52:55]
s_waitcnt vmcnt(7)
ds_read_u16_d16_hi v51,v51
v_cvt_pk_bf16_f32 v31,v57,v59
v_lshlrev_b32_sdwa v44,1,v44 dst_sel:DWORD dst_unused:UNUSED_PAD src0_sel:DWORD src1_sel:WORD_1
v_mfma_f32_16x16x32_bf16 v[52:55],a[204:207],a[36:39],v[52:55]
s_barrier
v_exp_f16 v8,v9
v_mfma_f32_16x16x32_bf16 v[56:59],a[200:203],a[36:39],v[16:19]
s_add_u32 m0,s57,10608
ds_read_u16 v44,v44
v_lshlrev_b32_sdwa v46,1,v46 dst_sel:DWORD dst_unused:UNUSED_PAD src0_sel:DWORD src1_sel:WORD_1
v_mfma_f32_16x16x32_bf16 v[56:59],a[192:195],a[20:23],v[56:59]
s_add_u32 s89,s56,2752512
ds_read_u16_d16_hi v46,v46
v_lshlrev_b32_sdwa v45,1,v45 dst_sel:DWORD dst_unused:UNUSED_PAD src0_sel:DWORD src1_sel:WORD_1
v_mfma_f32_16x16x32_bf16 v[56:59],a[196:199],a[4:7],v[56:59]
v_exp_f16_sdwa v8,v9 dst_sel:WORD_1 dst_unused:UNUSED_PRESERVE src0_sel:WORD_1
v_mfma_f32_16x16x32_bf16 v[136:139],a[236:239],v[24:27],v[136:139]
buffer_load_dwordx4 v6,s[36:39],s89 offen lds
v_lshlrev_b32_sdwa v47,1,v47 dst_sel:DWORD dst_unused:UNUSED_PAD src0_sel:DWORD src1_sel:WORD_1
v_mfma_f32_16x16x32_bf16 v[140:143],a[236:239],v[28:31],v[140:143]
s_add_u32 m0,s58,0
ds_read_u16 v45,v45
v_lshlrev_b32_sdwa v36,1,v36 dst_sel:DWORD dst_unused:UNUSED_PAD src0_sel:DWORD src1_sel:WORD_1
v_mfma_f32_16x16x32_bf16 v[68:71],a[244:247],v[28:31],v[68:71]
s_add_u32 s69,s69,s59
ds_read_u16_d16_hi v47,v47
v_pk_fma_f16 v8,v8,s55,s55
v_mfma_f32_16x16x32_bf16 v[64:67],a[244:247],v[24:27],v[64:67]
v_rcp_f16 v9,v8
v_lshlrev_b32_sdwa v38,1,v38 dst_sel:DWORD dst_unused:UNUSED_PAD src0_sel:DWORD src1_sel:WORD_1
v_mfma_f32_16x16x32_bf16 v[144:147],a[248:251],v[24:27],v[144:147]
buffer_load_dwordx4 v5,s[44:47],s69 offen offset:0 lds
ds_read_u16 v36,v36
v_mfma_f32_16x16x32_bf16 v[148:151],a[248:251],v[28:31],v[148:151]
s_waitcnt lgkmcnt(5)
ds_read_u16_d16_hi v38,v38
v_lshlrev_b32_sdwa v37,1,v37 dst_sel:DWORD dst_unused:UNUSED_PAD src0_sel:DWORD src1_sel:WORD_1
v_mfma_f32_16x16x32_bf16 v[84:87],a[252:255],v[28:31],v[84:87]
s_add_u32 s69,s69,s53
ds_read_u16 v37,v37
v_lshlrev_b32_sdwa v39,1,v39 dst_sel:DWORD dst_unused:UNUSED_PAD src0_sel:DWORD src1_sel:WORD_1
v_mfma_f32_16x16x32_bf16 v[80:83],a[252:255],v[24:27],v[80:83]
v_rcp_f16_sdwa v9,v8 dst_sel:WORD_1 dst_unused:UNUSED_PRESERVE src0_sel:WORD_1
v_mfma_f32_16x16x32_bf16 v[152:155],a[240:243],v[24:27],v[152:155]
buffer_load_dwordx4 v5,s[44:47],s69 offen offset:1024 lds
ds_read_u16_d16_hi v39,v39
v_lshlrev_b32_sdwa v40,1,v40 dst_sel:DWORD dst_unused:UNUSED_PAD src0_sel:DWORD src1_sel:WORD_1
v_mfma_f32_16x16x32_bf16 v[156:159],a[240:243],v[28:31],v[156:159]
s_add_u32 s69,s69,s53
ds_read_u16 v40,v40
v_lshlrev_b32_sdwa v42,1,v42 dst_sel:DWORD dst_unused:UNUSED_PAD src0_sel:DWORD src1_sel:WORD_1
v_mfma_f32_16x16x32_bf16 v[100:103],a[224:227],v[28:31],v[100:103]
buffer_load_dwordx4 v5,s[44:47],s69 offen offset:2048 lds
v_fma_mix_f32 v41,v41,v9,0 op_sel_hi:[0,1,0]
v_mfma_f32_16x16x32_bf16 v[96:99],a[224:227],v[24:27],v[96:99]
ds_read_u16_d16_hi v42,v42
v_fma_mix_f32 v43,v43,v9,0 op_sel:[0,1,0] op_sel_hi:[0,1,0]
v_mfma_f32_16x16x32_bf16 v[28:31],a[196:199],a[8:11],v[16:19]
ds_read_b64 v[14:15],v7 offset:12320
ds_read_b64_tr_b16 a[220:221],v1 offset:12320
v_lshlrev_b32_sdwa v32,1,v32 dst_sel:DWORD dst_unused:UNUSED_PAD src0_sel:DWORD src1_sel:WORD_1
v_mfma_f32_16x16x32_bf16 v[28:31],a[192:195],a[24:27],v[28:31]
ds_read_b64_tr_b16 a[222:223],v1 offset:13344
ds_read_b64_tr_b16 a[216:217],v1 offset:14368
v_fma_mixlo_f16 v8,v53,s99,0
v_mfma_f32_16x16x32_bf16 v[28:31],a[200:203],a[40:43],v[28:31]
ds_read_b64_tr_b16 a[218:219],v1 offset:15392
v_lshl_add_u64 v[48:49],v[48:49],0,v[50:51]
v_lshlrev_b32_sdwa v34,1,v34 dst_sel:DWORD dst_unused:UNUSED_PAD src0_sel:DWORD src1_sel:WORD_1
v_mfma_f32_16x16x32_bf16 v[24:27],a[204:207],a[40:43],v[20:23]
s_waitcnt lgkmcnt(8)
ds_read_u16 v32,v32
v_lshl_add_u64 v[50:51],v[44:45],0,v[46:47]
v_fma_mixhi_f16 v8,v55,s99,0
v_mfma_f32_16x16x32_bf16 v[24:27],a[208:211],a[24:27],v[24:27]
s_waitcnt lgkmcnt(8)
ds_read_u16_d16_hi v34,v34
v_lshl_add_u64 v[44:45],v[36:37],0,v[38:39]
v_lshlrev_b32_sdwa v33,1,v33 dst_sel:DWORD dst_unused:UNUSED_PAD src0_sel:DWORD src1_sel:WORD_1
v_pk_fma_f16 v9,v8,v8,s54
v_mfma_f32_16x16x32_bf16 v[24:27],a[212:215],a[8:11],v[24:27]
s_waitcnt lgkmcnt(6)
ds_read_u16 v33,v33
v_or3_b32 v252,v14,v15,v252
v_lshlrev_b32_sdwa v35,1,v35 dst_sel:DWORD dst_unused:UNUSED_PAD src0_sel:DWORD src1_sel:WORD_1
v_mfma_f32_16x16x32_bf16 v[36:39],a[212:215],a[12:15],v[20:23]
s_add_u32 m0,s57,14144
ds_read_b64 v[14:15],v7 offset:12832
ds_read_b128 v[20:23],v4 offset:416
v_or_b32 v46,v40,v42
v_pk_mul_f16 v9,v8,v9
v_mfma_f32_16x16x32_bf16 v[36:39],a[208:211],a[28:31],v[36:39]
s_add_u32 s89,s56,2850816
ds_read_u16_d16_hi v35,v35
ds_read_b64_tr_b16 a[212:213],v1 offset:16416
v_cvt_pk_bf16_f32 v47,v41,v43
v_lshlrev_b32_sdwa v60,1,v60 dst_sel:DWORD dst_unused:UNUSED_PAD src0_sel:DWORD src1_sel:WORD_1
v_mfma_f32_16x16x32_bf16 v[36:39],a[204:207],a[44:47],v[36:39]
ds_read_b64_tr_b16 a[214:215],v1 offset:17440
v_exp_f16 v8,v9
v_mfma_f32_16x16x32_bf16 v[40:43],a[200:203],a[44:47],v[16:19]
buffer_load_dwordx4 v6,s[36:39],s89 offen lds
ds_read_b128 v[16:19],v4 offset:384
v_lshlrev_b32_sdwa v62,1,v62 dst_sel:DWORD dst_unused:UNUSED_PAD src0_sel:DWORD src1_sel:WORD_1
v_mfma_f32_16x16x32_bf16 v[40:43],a[192:195],a[28:31],v[40:43]
ds_read_b64_tr_b16 a[208:209],v2 offset:16416
ds_read_b64_tr_b16 a[210:211],v2 offset:17440
v_lshlrev_b32_sdwa v61,1,v61 dst_sel:DWORD dst_unused:UNUSED_PAD src0_sel:DWORD src1_sel:WORD_1
v_mfma_f32_16x16x32_bf16 v[40:43],a[196:199],a[12:15],v[40:43]
s_waitcnt lgkmcnt(7)
ds_read_u16 v60,v60
ds_read_u16_d16_hi v62,v62
v_or3_b32 v252,v14,v15,v252
v_mfma_f32_16x16x32_bf16 v[112:115],a[236:239],v[48:51],v[112:115]
ds_read_b64 v[14:15],v7 offset:13344
ds_read_u16 v61,v61
v_exp_f16_sdwa v8,v9 dst_sel:WORD_1 dst_unused:UNUSED_PRESERVE src0_sel:WORD_1
v_lshlrev_b32_sdwa v63,1,v63 dst_sel:DWORD dst_unused:UNUSED_PAD src0_sel:DWORD src1_sel:WORD_1
v_mfma_f32_16x16x32_bf16 v[116:119],a[236:239],v[44:47],v[116:119]
ds_read_u16_d16_hi v63,v63
ds_read_b64_tr_b16 a[204:205],v2 offset:14368
v_lshlrev_b32_sdwa v56,1,v56 dst_sel:DWORD dst_unused:UNUSED_PAD src0_sel:DWORD src1_sel:WORD_1
v_mfma_f32_16x16x32_bf16 v[76:79],a[244:247],v[44:47],v[76:79]
ds_read_b64_tr_b16 a[206:207],v2 offset:15392
ds_read_u16 v56,v56
v_pk_fma_f16 v8,v8,s55,s55
v_mfma_f32_16x16x32_bf16 v[72:75],a[244:247],v[48:51],v[72:75]
ds_read_b64_tr_b16 a[200:201],v2 offset:12320
v_rcp_f16 v9,v8
v_lshlrev_b32_sdwa v58,1,v58 dst_sel:DWORD dst_unused:UNUSED_PAD src0_sel:DWORD src1_sel:WORD_1
v_mfma_f32_16x16x32_bf16 v[120:123],a[248:251],v[48:51],v[120:123]
ds_read_u16_d16_hi v58,v58
ds_read_b64_tr_b16 a[202:203],v2 offset:13344
v_mfma_f32_16x16x32_bf16 v[124:127],a[248:251],v[44:47],v[124:127]
s_waitcnt lgkmcnt(8)
ds_read_b64_tr_b16 a[228:229],v3 offset:12320
ds_read_b64_tr_b16 a[230:231],v3 offset:13856
v_or3_b32 v252,v14,v15,v252
v_lshlrev_b32_sdwa v57,1,v57 dst_sel:DWORD dst_unused:UNUSED_PAD src0_sel:DWORD src1_sel:WORD_1
v_mfma_f32_16x16x32_bf16 v[92:95],a[252:255],v[44:47],v[92:95]
ds_read_u16 v57,v57
ds_read_b64_tr_b16 a[232:233],v3 offset:12328
v_lshlrev_b32_sdwa v59,1,v59 dst_sel:DWORD dst_unused:UNUSED_PAD src0_sel:DWORD src1_sel:WORD_1
v_mfma_f32_16x16x32_bf16 v[88:91],a[252:255],v[48:51],v[88:91]
TE01_%=:
s_cbranch_execz TP3867_%=
ds_read_u16_d16_hi v59,v59
v_rcp_f16_sdwa v9,v8 dst_sel:WORD_1 dst_unused:UNUSED_PRESERVE src0_sel:WORD_1
v_mfma_f32_16x16x32_bf16 v[128:131],a[240:243],v[48:51],v[128:131]
ds_read_b64_tr_b16 a[234:235],v3 offset:13864
ds_read_b64_tr_b16 a[236:237],v3 offset:12384
v_lshlrev_b32_sdwa v52,1,v52 dst_sel:DWORD dst_unused:UNUSED_PAD src0_sel:DWORD src1_sel:WORD_1
v_mfma_f32_16x16x32_bf16 v[132:135],a[240:243],v[44:47],v[132:135]
ds_read_u16 v52,v52
ds_read_b64_tr_b16 a[238:239],v3 offset:13920
v_lshlrev_b32_sdwa v54,1,v54 dst_sel:DWORD dst_unused:UNUSED_PAD src0_sel:DWORD src1_sel:WORD_1
v_mfma_f32_16x16x32_bf16 v[108:111],a[224:227],v[44:47],v[108:111]
s_add_u32 m0,s57,17688
ds_read_u16_d16_hi v54,v54
ds_read_b64_tr_b16 a[244:245],v3 offset:12392
v_fma_mix_f32 v53,v53,v9,0 op_sel_hi:[0,1,0]
v_mfma_f32_16x16x32_bf16 v[104:107],a[224:227],v[48:51],v[104:107]
s_add_u32 s89,s56,2949120
ds_read_b64_tr_b16 a[246:247],v3 offset:13928
v_fma_mix_f32 v55,v55,v9,0 op_sel:[0,1,0] op_sel_hi:[0,1,0]
v_mfma_f32_16x16x32_bf16 v[44:47],a[220:223],a[0:3],v[16:19]
buffer_load_dwordx4 v6,s[36:39],s89 offen lds
v_lshlrev_b32_sdwa v28,1,v28 dst_sel:DWORD dst_unused:UNUSED_PAD src0_sel:DWORD src1_sel:WORD_1
v_mfma_f32_16x16x32_bf16 v[44:47],a[216:219],a[16:19],v[44:47]
ds_read_b64_tr_b16 a[248:249],v3 offset:12448
v_fma_mixlo_f16 v8,v37,s99,0
v_mfma_f32_16x16x32_bf16 v[44:47],a[212:215],a[32:35],v[44:47]
ds_read_b64_tr_b16 a[250:251],v3 offset:13984
v_lshl_add_u64 v[32:33],v[32:33],0,v[34:35]
v_lshlrev_b32_sdwa v30,1,v30 dst_sel:DWORD dst_unused:UNUSED_PAD src0_sel:DWORD src1_sel:WORD_1
v_mfma_f32_16x16x32_bf16 v[48:51],a[208:211],a[32:35],v[20:23]
s_waitcnt lgkmcnt(10)
ds_read_b64_tr_b16 a[252:253],v3 offset:12456
v_lshl_add_u64 v[34:35],v[60:61],0,v[62:63]
v_fma_mixhi_f16 v8,v39,s99,0
v_mfma_f32_16x16x32_bf16 v[48:51],a[204:207],a[16:19],v[48:51]
s_waitcnt lgkmcnt(10)
ds_read_b64_tr_b16 a[254:255],v3 offset:13992
v_lshl_add_u64 v[60:61],v[56:57],0,v[58:59]
v_lshlrev_b32_sdwa v29,1,v29 dst_sel:DWORD dst_unused:UNUSED_PAD src0_sel:DWORD src1_sel:WORD_1
v_pk_fma_f16 v9,v8,v8,s54
v_mfma_f32_16x16x32_bf16 v[48:51],a[200:203],a[0:3],v[48:51]
ds_read_u16 v28,v28
v_lshlrev_b32_sdwa v31,1,v31 dst_sel:DWORD dst_unused:UNUSED_PAD src0_sel:DWORD src1_sel:WORD_1
v_mfma_f32_16x16x32_bf16 v[56:59],a[200:203],a[4:7],v[20:23]
s_waitcnt lgkmcnt(7)
ds_read_u16_d16_hi v30,v30
v_or_b32 v62,v52,v54
v_pk_mul_f16 v9,v8,v9
v_mfma_f32_16x16x32_bf16 v[56:59],a[204:207],a[20:23],v[56:59]
s_waitcnt vmcnt(8)
ds_read_u16 v29,v29
v_cvt_pk_bf16_f32 v63,v53,v55
v_lshlrev_b32_sdwa v24,1,v24 dst_sel:DWORD dst_unused:UNUSED_PAD src0_sel:DWORD src1_sel:WORD_1
v_mfma_f32_16x16x32_bf16 v[56:59],a[208:211],a[36:39],v[56:59]
s_barrier
v_exp_f16 v8,v9
v_mfma_f32_16x16x32_bf16 v[52:55],a[212:215],a[36:39],v[16:19]
ds_read_u16_d16_hi v31,v31
v_lshlrev_b32_sdwa v26,1,v26 dst_sel:DWORD dst_unused:UNUSED_PAD src0_sel:DWORD src1_sel:WORD_1
v_mfma_f32_16x16x32_bf16 v[52:55],a[216:219],a[20:23],v[52:55]
ds_read_u16 v24,v24
v_lshlrev_b32_sdwa v25,1,v25 dst_sel:DWORD dst_unused:UNUSED_PAD src0_sel:DWORD src1_sel:WORD_1
v_mfma_f32_16x16x32_bf16 v[52:55],a[220:223],a[4:7],v[52:55]
s_add_u32 m0,s58,6160
ds_read_u16_d16_hi v26,v26
v_exp_f16_sdwa v8,v9 dst_sel:WORD_1 dst_unused:UNUSED_PRESERVE src0_sel:WORD_1
v_mfma_f32_16x16x32_bf16 v[136:139],a[228:231],v[32:35],v[136:139]
s_add_u32 s69,s69,s59
ds_read_u16 v25,v25
v_lshlrev_b32_sdwa v27,1,v27 dst_sel:DWORD dst_unused:UNUSED_PAD src0_sel:DWORD src1_sel:WORD_1
v_mfma_f32_16x16x32_bf16 v[140:143],a[228:231],v[60:63],v[140:143]
buffer_load_dwordx4 v5,s[44:47],s69 offen offset:0 lds
v_lshlrev_b32_sdwa v40,1,v40 dst_sel:DWORD dst_unused:UNUSED_PAD src0_sel:DWORD src1_sel:WORD_1
v_mfma_f32_16x16x32_bf16 v[68:71],a[232:235],v[60:63],v[68:71]
ds_read_u16_d16_hi v27,v27
v_pk_fma_f16 v8,v8,s55,s55
v_mfma_f32_16x16x32_bf16 v[64:67],a[232:235],v[32:35],v[64:67]
v_rcp_f16 v9,v8
v_lshlrev_b32_sdwa v42,1,v42 dst_sel:DWORD dst_unused:UNUSED_PAD src0_sel:DWORD src1_sel:WORD_1
v_mfma_f32_16x16x32_bf16 v[144:147],a[236:239],v[32:35],v[144:147]
s_add_u32 s69,s69,s53
ds_read_u16 v40,v40
v_lshlrev_b32_sdwa v41,1,v41 dst_sel:DWORD dst_unused:UNUSED_PAD src0_sel:DWORD src1_sel:WORD_1
v_mfma_f32_16x16x32_bf16 v[148:151],a[236:239],v[60:63],v[148:151]
s_waitcnt lgkmcnt(6)
buffer_load_dwordx4 v5,s[44:47],s69 offen offset:1024 lds
ds_read_u16_d16_hi v42,v42
v_mfma_f32_16x16x32_bf16 v[84:87],a[244:247],v[60:63],v[84:87]
s_add_u32 m0,s57,21224
ds_read_u16 v41,v41
v_lshlrev_b32_sdwa v43,1,v43 dst_sel:DWORD dst_unused:UNUSED_PAD src0_sel:DWORD src1_sel:WORD_1
v_mfma_f32_16x16x32_bf16 v[80:83],a[244:247],v[32:35],v[80:83]
s_add_u32 s89,s56,3047424
v_rcp_f16_sdwa v9,v8 dst_sel:WORD_1 dst_unused:UNUSED_PRESERVE src0_sel:WORD_1
v_mfma_f32_16x16x32_bf16 v[152:155],a[248:251],v[32:35],v[152:155]
buffer_load_dwordx4 v6,s[36:39],s89 offen lds
ds_read_u16_d16_hi v43,v43
v_lshlrev_b32_sdwa v36,1,v36 dst_sel:DWORD dst_unused:UNUSED_PAD src0_sel:DWORD src1_sel:WORD_1
v_mfma_f32_16x16x32_bf16 v[156:159],a[248:251],v[60:63],v[156:159]
s_add_u32 m0,s58,6160
ds_read_u16 v36,v36
v_lshlrev_b32_sdwa v38,1,v38 dst_sel:DWORD dst_unused:UNUSED_PAD src0_sel:DWORD src1_sel:WORD_1
v_mfma_f32_16x16x32_bf16 v[100:103],a[252:255],v[60:63],v[100:103]
s_add_u32 s69,s69,s53
ds_read_u16_d16_hi v38,v38
v_fma_mix_f32 v37,v37,v9,0 op_sel_hi:[0,1,0]
v_mfma_f32_16x16x32_bf16 v[96:99],a[252:255],v[32:35],v[96:99]
buffer_load_dwordx4 v5,s[44:47],s69 offen offset:2048 lds
v_fma_mix_f32 v39,v39,v9,0 op_sel:[0,1,0] op_sel_hi:[0,1,0]
v_mfma_f32_16x16x32_bf16 v[60:63],a[220:223],a[8:11],v[16:19]
ds_read_b64 v[14:15],v7 offset:18480
ds_read_b64_tr_b16 a[192:193],v1 offset:18480
v_lshlrev_b32_sdwa v44,1,v44 dst_sel:DWORD dst_unused:UNUSED_PAD src0_sel:DWORD src1_sel:WORD_1
v_mfma_f32_16x16x32_bf16 v[60:63],a[216:219],a[24:27],v[60:63]
ds_read_b64_tr_b16 a[194:195],v1 offset:19504
ds_read_b64_tr_b16 a[196:197],v1 offset:20528
v_fma_mixlo_f16 v8,v57,s99,0
v_mfma_f32_16x16x32_bf16 v[60:63],a[212:215],a[40:43],v[60:63]
s_waitcnt lgkmcnt(8)
ds_read_b64_tr_b16 a[198:199],v1 offset:21552
v_lshl_add_u64 v[28:29],v[28:29],0,v[30:31]
v_lshlrev_b32_sdwa v46,1,v46 dst_sel:DWORD dst_unused:UNUSED_PAD src0_sel:DWORD src1_sel:WORD_1
v_mfma_f32_16x16x32_bf16 v[32:35],a[208:211],a[40:43],v[20:23]
ds_read_u16 v44,v44
v_lshl_add_u64 v[30:31],v[24:25],0,v[26:27]
v_fma_mixhi_f16 v8,v59,s99,0
v_mfma_f32_16x16x32_bf16 v[32:35],a[204:207],a[24:27],v[32:35]
s_waitcnt lgkmcnt(8)
ds_read_u16_d16_hi v46,v46
v_lshl_add_u64 v[24:25],v[40:41],0,v[42:43]
v_lshlrev_b32_sdwa v45,1,v45 dst_sel:DWORD dst_unused:UNUSED_PAD src0_sel:DWORD src1_sel:WORD_1
v_pk_fma_f16 v9,v8,v8,s54
v_mfma_f32_16x16x32_bf16 v[32:35],a[200:203],a[8:11],v[32:35]
s_waitcnt lgkmcnt(6)
ds_read_u16 v45,v45
v_or3_b32 v252,v14,v15,v252
v_lshlrev_b32_sdwa v47,1,v47 dst_sel:DWORD dst_unused:UNUSED_PAD src0_sel:DWORD src1_sel:WORD_1
v_mfma_f32_16x16x32_bf16 v[40:43],a[200:203],a[12:15],v[20:23]
ds_read_b64 v[14:15],v7 offset:18992
ds_read_b128 v[20:23],v4 offset:544
v_or_b32 v26,v36,v38
v_pk_mul_f16 v9,v8,v9
v_mfma_f32_16x16x32_bf16 v[40:43],a[204:207],a[28:31],v[40:43]
ds_read_u16_d16_hi v47,v47
ds_read_b64_tr_b16 a[200:201],v1 offset:22576
v_cvt_pk_bf16_f32 v27,v37,v39
v_lshlrev_b32_sdwa v48,1,v48 dst_sel:DWORD dst_unused:UNUSED_PAD src0_sel:DWORD src1_sel:WORD_1
v_mfma_f32_16x16x32_bf16 v[40:43],a[208:211],a[44:47],v[40:43]
ds_read_b64_tr_b16 a[202:203],v1 offset:23600
v_exp_f16 v8,v9
v_mfma_f32_16x16x32_bf16 v[36:39],a[212:215],a[44:47],v[16:19]
ds_read_b128 v[16:19],v4 offset:512
ds_read_b64_tr_b16 a[204:205],v2 offset:22576
v_lshlrev_b32_sdwa v50,1,v50 dst_sel:DWORD dst_unused:UNUSED_PAD src0_sel:DWORD src1_sel:WORD_1
v_mfma_f32_16x16x32_bf16 v[36:39],a[216:219],a[28:31],v[36:39]
ds_read_b64_tr_b16 a[206:207],v2 offset:23600
ds_read_u16 v48,v48
v_lshlrev_b32_sdwa v49,1,v49 dst_sel:DWORD dst_unused:UNUSED_PAD src0_sel:DWORD src1_sel:WORD_1
v_mfma_f32_16x16x32_bf16 v[36:39],a[220:223],a[12:15],v[36:39]
s_waitcnt lgkmcnt(8)
ds_read_u16_d16_hi v50,v50
ds_read_u16 v49,v49
v_or3_b32 v252,v14,v15,v252
v_mfma_f32_16x16x32_bf16 v[112:115],a[228:231],v[28:31],v[112:115]
ds_read_b64 v[14:15],v7 offset:19504
ds_read_b64_tr_b16 a[208:209],v2 offset:20528
v_exp_f16_sdwa v8,v9 dst_sel:WORD_1 dst_unused:UNUSED_PRESERVE src0_sel:WORD_1
v_lshlrev_b32_sdwa v51,1,v51 dst_sel:DWORD dst_unused:UNUSED_PAD src0_sel:DWORD src1_sel:WORD_1
v_mfma_f32_16x16x32_bf16 v[116:119],a[228:231],v[24:27],v[116:119]
s_add_u32 m0,s57,24752
ds_read_u16_d16_hi v51,v51
ds_read_b64_tr_b16 a[210:211],v2 offset:21552
v_lshlrev_b32_sdwa v52,1,v52 dst_sel:DWORD dst_unused:UNUSED_PAD src0_sel:DWORD src1_sel:WORD_1
v_mfma_f32_16x16x32_bf16 v[76:79],a[232:235],v[24:27],v[76:79]
s_add_u32 s89,s56,3145728
ds_read_u16 v52,v52
ds_read_b64_tr_b16 a[212:213],v2 offset:18480
v_pk_fma_f16 v8,v8,s55,s55
v_mfma_f32_16x16x32_bf16 v[72:75],a[232:235],v[28:31],v[72:75]
ds_read_b64_tr_b16 a[214:215],v2 offset:19504
v_rcp_f16 v9,v8
v_lshlrev_b32_sdwa v54,1,v54 dst_sel:DWORD dst_unused:UNUSED_PAD src0_sel:DWORD src1_sel:WORD_1
v_mfma_f32_16x16x32_bf16 v[120:123],a[236:239],v[28:31],v[120:123]
buffer_load_dwordx4 v6,s[36:39],s89 offen lds
ds_read_u16_d16_hi v54,v54
v_mfma_f32_16x16x32_bf16 v[124:127],a[236:239],v[24:27],v[124:127]
s_waitcnt lgkmcnt(7)
ds_read_b64_tr_b16 a[240:241],v3 offset:18480
ds_read_b64_tr_b16 a[242:243],v3 offset:20016
v_or3_b32 v252,v14,v15,v252
v_lshlrev_b32_sdwa v53,1,v53 dst_sel:DWORD dst_unused:UNUSED_PAD src0_sel:DWORD src1_sel:WORD_1
v_mfma_f32_16x16x32_bf16 v[92:95],a[244:247],v[24:27],v[92:95]
ds_read_u16 v53,v53
ds_read_b64_tr_b16 a[224:225],v3 offset:18488
v_lshlrev_b32_sdwa v55,1,v55 dst_sel:DWORD dst_unused:UNUSED_PAD src0_sel:DWORD src1_sel:WORD_1
v_mfma_f32_16x16x32_bf16 v[88:91],a[244:247],v[28:31],v[88:91]
ds_read_u16_d16_hi v55,v55
v_rcp_f16_sdwa v9,v8 dst_sel:WORD_1 dst_unused:UNUSED_PRESERVE src0_sel:WORD_1
v_mfma_f32_16x16x32_bf16 v[128:131],a[248:251],v[28:31],v[128:131]
ds_read_b64_tr_b16 a[226:227],v3 offset:20024
ds_read_b64_tr_b16 a[228:229],v3 offset:18544
v_lshlrev_b32_sdwa v56,1,v56 dst_sel:DWORD dst_unused:UNUSED_PAD src0_sel:DWORD src1_sel:WORD_1
v_mfma_f32_16x16x32_bf16 v[132:135],a[248:251],v[24:27],v[132:135]
ds_read_u16 v56,v56
ds_read_b64_tr_b16 a[230:231],v3 offset:20080
v_lshlrev_b32_sdwa v58,1,v58 dst_sel:DWORD dst_unused:UNUSED_PAD src0_sel:DWORD src1_sel:WORD_1
v_mfma_f32_16x16x32_bf16 v[108:111],a[252:255],v[24:27],v[108:111]
ds_read_u16_d16_hi v58,v58
ds_read_b64_tr_b16 a[232:233],v3 offset:18552
v_fma_mix_f32 v57,v57,v9,0 op_sel_hi:[0,1,0]
v_mfma_f32_16x16x32_bf16 v[104:107],a[252:255],v[28:31],v[104:107]
ds_read_b64_tr_b16 a[234:235],v3 offset:20088
v_fma_mix_f32 v59,v59,v9,0 op_sel:[0,1,0] op_sel_hi:[0,1,0]
v_mfma_f32_16x16x32_bf16 v[24:27],a[192:195],a[0:3],v[16:19]
ds_read_b64_tr_b16 a[236:237],v3 offset:18608
v_lshlrev_b32_sdwa v60,1,v60 dst_sel:DWORD dst_unused:UNUSED_PAD src0_sel:DWORD src1_sel:WORD_1
v_mfma_f32_16x16x32_bf16 v[24:27],a[196:199],a[16:19],v[24:27]
ds_read_b64_tr_b16 a[238:239],v3 offset:20144
v_fma_mixlo_f16 v8,v41,s99,0
v_mfma_f32_16x16x32_bf16 v[24:27],a[200:203],a[32:35],v[24:27]
ds_read_b64_tr_b16 a[244:245],v3 offset:18616
v_lshl_add_u64 v[44:45],v[44:45],0,v[46:47]
v_lshlrev_b32_sdwa v62,1,v62 dst_sel:DWORD dst_unused:UNUSED_PAD src0_sel:DWORD src1_sel:WORD_1
v_mfma_f32_16x16x32_bf16 v[28:31],a[204:207],a[32:35],v[20:23]
s_waitcnt lgkmcnt(11)
ds_read_b64_tr_b16 a[246:247],v3 offset:20152
v_lshl_add_u64 v[46:47],v[48:49],0,v[50:51]
v_fma_mixhi_f16 v8,v43,s99,0
v_mfma_f32_16x16x32_bf16 v[28:31],a[208:211],a[16:19],v[28:31]
s_waitcnt lgkmcnt(11)
ds_read_u16 v60,v60
v_lshl_add_u64 v[48:49],v[52:53],0,v[54:55]
v_lshlrev_b32_sdwa v61,1,v61 dst_sel:DWORD dst_unused:UNUSED_PAD src0_sel:DWORD src1_sel:WORD_1
v_pk_fma_f16 v9,v8,v8,s54
v_mfma_f32_16x16x32_bf16 v[28:31],a[212:215],a[0:3],v[28:31]
ds_read_u16_d16_hi v62,v62
v_lshlrev_b32_sdwa v63,1,v63 dst_sel:DWORD dst_unused:UNUSED_PAD src0_sel:DWORD src1_sel:WORD_1
v_mfma_f32_16x16x32_bf16 v[52:55],a[212:215],a[4:7],v[20:23]
s_waitcnt lgkmcnt(8)
ds_read_u16 v61,v61
v_or_b32 v50,v56,v58
v_pk_mul_f16 v9,v8,v9
v_mfma_f32_16x16x32_bf16 v[52:55],a[208:211],a[20:23],v[52:55]
s_waitcnt vmcnt(7)
ds_read_u16_d16_hi v63,v63
v_cvt_pk_bf16_f32 v51,v57,v59
v_lshlrev_b32_sdwa v32,1,v32 dst_sel:DWORD dst_unused:UNUSED_PAD src0_sel:DWORD src1_sel:WORD_1
v_mfma_f32_16x16x32_bf16 v[52:55],a[204:207],a[36:39],v[52:55]
s_barrier
v_exp_f16 v8,v9
v_mfma_f32_16x16x32_bf16 v[56:59],a[200:203],a[36:39],v[16:19]
s_add_u32 m0,s57,28288
ds_read_u16 v32,v32
v_lshlrev_b32_sdwa v34,1,v34 dst_sel:DWORD dst_unused:UNUSED_PAD src0_sel:DWORD src1_sel:WORD_1
v_mfma_f32_16x16x32_bf16 v[56:59],a[196:199],a[20:23],v[56:59]
s_add_u32 s89,s56,3244032
ds_read_u16_d16_hi v34,v34
v_lshlrev_b32_sdwa v33,1,v33 dst_sel:DWORD dst_unused:UNUSED_PAD src0_sel:DWORD src1_sel:WORD_1
v_mfma_f32_16x16x32_bf16 v[56:59],a[192:195],a[4:7],v[56:59]
v_exp_f16_sdwa v8,v9 dst_sel:WORD_1 dst_unused:UNUSED_PRESERVE src0_sel:WORD_1
v_mfma_f32_16x16x32_bf16 v[136:139],a[240:243],v[44:47],v[136:139]
buffer_load_dwordx4 v6,s[36:39],s89 offen lds
v_lshlrev_b32_sdwa v35,1,v35 dst_sel:DWORD dst_unused:UNUSED_PAD src0_sel:DWORD src1_sel:WORD_1
v_mfma_f32_16x16x32_bf16 v[140:143],a[240:243],v[48:51],v[140:143]
s_add_u32 m0,s58,12320
ds_read_u16 v33,v33
v_lshlrev_b32_sdwa v36,1,v36 dst_sel:DWORD dst_unused:UNUSED_PAD src0_sel:DWORD src1_sel:WORD_1
v_mfma_f32_16x16x32_bf16 v[68:71],a[224:227],v[48:51],v[68:71]
s_add_u32 s69,s69,s59
ds_read_u16_d16_hi v35,v35
v_pk_fma_f16 v8,v8,s55,s55
v_mfma_f32_16x16x32_bf16 v[64:67],a[224:227],v[44:47],v[64:67]
v_rcp_f16 v9,v8
v_lshlrev_b32_sdwa v38,1,v38 dst_sel:DWORD dst_unused:UNUSED_PAD src0_sel:DWORD src1_sel:WORD_1
v_mfma_f32_16x16x32_bf16 v[144:147],a[228:231],v[44:47],v[144:147]
buffer_load_dwordx4 v5,s[44:47],s69 offen offset:0 lds
ds_read_u16 v36,v36
v_mfma_f32_16x16x32_bf16 v[148:151],a[228:231],v[48:51],v[148:151]
s_waitcnt lgkmcnt(5)
ds_read_u16_d16_hi v38,v38
v_lshlrev_b32_sdwa v37,1,v37 dst_sel:DWORD dst_unused:UNUSED_PAD src0_sel:DWORD src1_sel:WORD_1
v_mfma_f32_16x16x32_bf16 v[84:87],a[232:235],v[48:51],v[84:87]
s_add_u32 s69,s69,s53
ds_read_u16 v37,v37
v_lshlrev_b32_sdwa v39,1,v39 dst_sel:DWORD dst_unused:UNUSED_PAD src0_sel:DWORD src1_sel:WORD_1
v_mfma_f32_16x16x32_bf16 v[80:83],a[232:235],v[44:47],v[80:83]
v_rcp_f16_sdwa v9,v8 dst_sel:WORD_1 dst_unused:UNUSED_PRESERVE src0_sel:WORD_1
v_mfma_f32_16x16x32_bf16 v[152:155],a[236:239],v[44:47],v[152:155]
buffer_load_dwordx4 v5,s[44:47],s69 offen offset:1024 lds
ds_read_u16_d16_hi v39,v39
v_lshlrev_b32_sdwa v40,1,v40 dst_sel:DWORD dst_unused:UNUSED_PAD src0_sel:DWORD src1_sel:WORD_1
v_mfma_f32_16x16x32_bf16 v[156:159],a[236:239],v[48:51],v[156:159]
s_add_u32 s69,s69,s53
ds_read_u16 v40,v40
v_lshlrev_b32_sdwa v42,1,v42 dst_sel:DWORD dst_unused:UNUSED_PAD src0_sel:DWORD src1_sel:WORD_1
v_mfma_f32_16x16x32_bf16 v[100:103],a[244:247],v[48:51],v[100:103]
buffer_load_dwordx4 v5,s[44:47],s69 offen offset:2048 lds
v_fma_mix_f32 v41,v41,v9,0 op_sel_hi:[0,1,0]
v_mfma_f32_16x16x32_bf16 v[96:99],a[244:247],v[44:47],v[96:99]
ds_read_u16_d16_hi v42,v42
v_fma_mix_f32 v43,v43,v9,0 op_sel:[0,1,0] op_sel_hi:[0,1,0]
v_mfma_f32_16x16x32_bf16 v[48:51],a[192:195],a[8:11],v[16:19]
ds_read_b64 v[14:15],v7 offset:0
ds_read_b64_tr_b16 a[216:217],v1 offset:0
v_lshlrev_b32_sdwa v24,1,v24 dst_sel:DWORD dst_unused:UNUSED_PAD src0_sel:DWORD src1_sel:WORD_1
v_mfma_f32_16x16x32_bf16 v[48:51],a[196:199],a[24:27],v[48:51]
ds_read_b64_tr_b16 a[218:219],v1 offset:1024
ds_read_b64_tr_b16 a[220:221],v1 offset:2048
v_fma_mixlo_f16 v8,v53,s99,0
v_mfma_f32_16x16x32_bf16 v[48:51],a[200:203],a[40:43],v[48:51]
ds_read_b64_tr_b16 a[222:223],v1 offset:3072
v_lshl_add_u64 v[60:61],v[60:61],0,v[62:63]
v_lshlrev_b32_sdwa v26,1,v26 dst_sel:DWORD dst_unused:UNUSED_PAD src0_sel:DWORD src1_sel:WORD_1
v_mfma_f32_16x16x32_bf16 v[44:47],a[204:207],a[40:43],v[20:23]
s_waitcnt lgkmcnt(8)
ds_read_u16 v24,v24
v_lshl_add_u64 v[62:63],v[32:33],0,v[34:35]
v_fma_mixhi_f16 v8,v55,s99,0
v_mfma_f32_16x16x32_bf16 v[44:47],a[208:211],a[24:27],v[44:47]
s_waitcnt lgkmcnt(8)
ds_read_u16_d16_hi v26,v26
v_lshl_add_u64 v[32:33],v[36:37],0,v[38:39]
v_lshlrev_b32_sdwa v25,1,v25 dst_sel:DWORD dst_unused:UNUSED_PAD src0_sel:DWORD src1_sel:WORD_1
v_pk_fma_f16 v9,v8,v8,s54
v_mfma_f32_16x16x32_bf16 v[44:47],a[212:215],a[8:11],v[44:47]
s_waitcnt lgkmcnt(6)
ds_read_u16 v25,v25
v_or3_b32 v252,v14,v15,v252
v_lshlrev_b32_sdwa v27,1,v27 dst_sel:DWORD dst_unused:UNUSED_PAD src0_sel:DWORD src1_sel:WORD_1
v_mfma_f32_16x16x32_bf16 v[36:39],a[212:215],a[12:15],v[20:23]
s_add_u32 m0,s57,31832
ds_read_b64 v[14:15],v7 offset:512
ds_read_b128 v[20:23],v4 offset:672
v_or_b32 v34,v40,v42
v_pk_mul_f16 v9,v8,v9
v_mfma_f32_16x16x32_bf16 v[36:39],a[208:211],a[28:31],v[36:39]
s_add_u32 s89,s56,3342336
ds_read_u16_d16_hi v27,v27
ds_read_b64_tr_b16 a[212:213],v1 offset:4096
v_cvt_pk_bf16_f32 v35,v41,v43
v_lshlrev_b32_sdwa v28,1,v28 dst_sel:DWORD dst_unused:UNUSED_PAD src0_sel:DWORD src1_sel:WORD_1
v_mfma_f32_16x16x32_bf16 v[36:39],a[204:207],a[44:47],v[36:39]
ds_read_b64_tr_b16 a[214:215],v1 offset:5120
v_exp_f16 v8,v9
v_mfma_f32_16x16x32_bf16 v[40:43],a[200:203],a[44:47],v[16:19]
buffer_load_dwordx4 v6,s[36:39],s89 offen lds
ds_read_b128 v[16:19],v4 offset:640
v_lshlrev_b32_sdwa v30,1,v30 dst_sel:DWORD dst_unused:UNUSED_PAD src0_sel:DWORD src1_sel:WORD_1
v_mfma_f32_16x16x32_bf16 v[40:43],a[196:199],a[28:31],v[40:43]
ds_read_b64_tr_b16 a[208:209],v2 offset:4096
ds_read_b64_tr_b16 a[210:211],v2 offset:5120
v_lshlrev_b32_sdwa v29,1,v29 dst_sel:DWORD dst_unused:UNUSED_PAD src0_sel:DWORD src1_sel:WORD_1
v_mfma_f32_16x16x32_bf16 v[40:43],a[192:195],a[12:15],v[40:43]
s_waitcnt lgkmcnt(7)
ds_read_u16 v28,v28
ds_read_u16_d16_hi v30,v30
v_or3_b32 v252,v14,v15,v252
v_mfma_f32_16x16x32_bf16 v[112:115],a[240:243],v[60:63],v[112:115]
ds_read_b64 v[14:15],v7 offset:1024
ds_read_u16 v29,v29
v_exp_f16_sdwa v8,v9 dst_sel:WORD_1 dst_unused:UNUSED_PRESERVE src0_sel:WORD_1
v_lshlrev_b32_sdwa v31,1,v31 dst_sel:DWORD dst_unused:UNUSED_PAD src0_sel:DWORD src1_sel:WORD_1
v_mfma_f32_16x16x32_bf16 v[116:119],a[240:243],v[32:35],v[116:119]
ds_read_u16_d16_hi v31,v31
ds_read_b64_tr_b16 a[204:205],v2 offset:2048
v_lshlrev_b32_sdwa v56,1,v56 dst_sel:DWORD dst_unused:UNUSED_PAD src0_sel:DWORD src1_sel:WORD_1
v_mfma_f32_16x16x32_bf16 v[76:79],a[224:227],v[32:35],v[76:79]
ds_read_b64_tr_b16 a[206:207],v2 offset:3072
ds_read_u16 v56,v56
v_pk_fma_f16 v8,v8,s55,s55
v_mfma_f32_16x16x32_bf16 v[72:75],a[224:227],v[60:63],v[72:75]
ds_read_b64_tr_b16 a[200:201],v2 offset:0
v_rcp_f16 v9,v8
v_lshlrev_b32_sdwa v58,1,v58 dst_sel:DWORD dst_unused:UNUSED_PAD src0_sel:DWORD src1_sel:WORD_1
v_mfma_f32_16x16x32_bf16 v[120:123],a[228:231],v[60:63],v[120:123]
ds_read_u16_d16_hi v58,v58
ds_read_b64_tr_b16 a[202:203],v2 offset:1024
v_mfma_f32_16x16x32_bf16 v[124:127],a[228:231],v[32:35],v[124:127]
s_waitcnt lgkmcnt(8)
ds_read_b64_tr_b16 a[248:249],v3 offset:0
ds_read_b64_tr_b16 a[250:251],v3 offset:1536
v_or3_b32 v252,v14,v15,v252
v_lshlrev_b32_sdwa v57,1,v57 dst_sel:DWORD dst_unused:UNUSED_PAD src0_sel:DWORD src1_sel:WORD_1
v_mfma_f32_16x16x32_bf16 v[92:95],a[232:235],v[32:35],v[92:95]
ds_read_u16 v57,v57
ds_read_b64_tr_b16 a[252:253],v3 offset:8
v_and_b32 v252,0x40004000,v252
v_lshlrev_b32_sdwa v59,1,v59 dst_sel:DWORD dst_unused:UNUSED_PAD src0_sel:DWORD src1_sel:WORD_1
v_mfma_f32_16x16x32_bf16 v[88:91],a[232:235],v[60:63],v[88:91]
ds_read_u16_d16_hi v59,v59
ds_read_b64_tr_b16 a[254:255],v3 offset:1544
v_cmp_ne_u32_e32 vcc,0,v252
s_nop 0
s_or_b64 s[90:91],s[90:91],vcc
v_mfma_f32_16x16x32_bf16 v[128:131],a[236:239],v[60:63],v[128:131]
s_add_u32 m0,s57,35368
ds_read_b64_tr_b16 a[240:241],v3 offset:64
v_rcp_f16_sdwa v9,v8 dst_sel:WORD_1 dst_unused:UNUSED_PRESERVE src0_sel:WORD_1
v_lshlrev_b32_sdwa v52,1,v52 dst_sel:DWORD dst_unused:UNUSED_PAD src0_sel:DWORD src1_sel:WORD_1
v_mfma_f32_16x16x32_bf16 v[132:135],a[236:239],v[32:35],v[132:135]
s_add_u32 s89,s56,3440640
ds_read_u16 v52,v52
ds_read_b64_tr_b16 a[242:243],v3 offset:1600
v_lshlrev_b32_sdwa v54,1,v54 dst_sel:DWORD dst_unused:UNUSED_PAD src0_sel:DWORD src1_sel:WORD_1
v_mfma_f32_16x16x32_bf16 v[108:111],a[244:247],v[32:35],v[108:111]
buffer_load_dwordx4 v6,s[36:39],s89 offen lds
ds_read_u16_d16_hi v54,v54
v_fma_mix_f32 v53,v53,v9,0 op_sel_hi:[0,1,0]
v_mfma_f32_16x16x32_bf16 v[104:107],a[244:247],v[60:63],v[104:107]
ds_read_b64_tr_b16 a[224:225],v3 offset:72
v_fma_mix_f32 v55,v55,v9,0 op_sel:[0,1,0] op_sel_hi:[0,1,0]
v_mfma_f32_16x16x32_bf16 v[32:35],a[216:219],a[0:3],v[16:19]
ds_read_b64_tr_b16 a[226:227],v3 offset:1608
v_lshlrev_b32_sdwa v48,1,v48 dst_sel:DWORD dst_unused:UNUSED_PAD src0_sel:DWORD src1_sel:WORD_1
v_mfma_f32_16x16x32_bf16 v[32:35],a[220:223],a[16:19],v[32:35]
ds_read_b64_tr_b16 a[228:229],v3 offset:128
v_fma_mixlo_f16 v8,v37,s99,0
v_mfma_f32_16x16x32_bf16 v[32:35],a[212:215],a[32:35],v[32:35]
ds_read_b64_tr_b16 a[230:231],v3 offset:1664
v_lshl_add_u64 v[24:25],v[24:25],0,v[26:27]
v_lshlrev_b32_sdwa v50,1,v50 dst_sel:DWORD dst_unused:UNUSED_PAD src0_sel:DWORD src1_sel:WORD_1
v_mfma_f32_16x16x32_bf16 v[60:63],a[208:211],a[32:35],v[20:23]
s_waitcnt lgkmcnt(10)
ds_read_b64_tr_b16 a[232:233],v3 offset:136
v_lshl_add_u64 v[26:27],v[28:29],0,v[30:31]
v_fma_mixhi_f16 v8,v39,s99,0
v_mfma_f32_16x16x32_bf16 v[60:63],a[204:207],a[16:19],v[60:63]
s_waitcnt lgkmcnt(9)
ds_read_b64_tr_b16 a[234:235],v3 offset:1672
v_lshl_add_u64 v[28:29],v[56:57],0,v[58:59]
v_lshlrev_b32_sdwa v49,1,v49 dst_sel:DWORD dst_unused:UNUSED_PAD src0_sel:DWORD src1_sel:WORD_1
v_pk_fma_f16 v9,v8,v8,s54
v_mfma_f32_16x16x32_bf16 v[60:63],a[200:203],a[0:3],v[60:63]
ds_read_u16 v48,v48
v_lshlrev_b32_sdwa v51,1,v51 dst_sel:DWORD dst_unused:UNUSED_PAD src0_sel:DWORD src1_sel:WORD_1
v_mfma_f32_16x16x32_bf16 v[56:59],a[200:203],a[4:7],v[20:23]
s_waitcnt lgkmcnt(7)
ds_read_u16_d16_hi v50,v50
v_or_b32 v30,v52,v54
v_pk_mul_f16 v9,v8,v9
v_mfma_f32_16x16x32_bf16 v[56:59],a[204:207],a[20:23],v[56:59]
s_waitcnt vmcnt(7)
ds_read_u16 v49,v49
v_cvt_pk_bf16_f32 v31,v53,v55
v_lshlrev_b32_sdwa v44,1,v44 dst_sel:DWORD dst_unused:UNUSED_PAD src0_sel:DWORD src1_sel:WORD_1
v_mfma_f32_16x16x32_bf16 v[56:59],a[208:211],a[36:39],v[56:59]
s_barrier
v_exp_f16 v8,v9
v_mfma_f32_16x16x32_bf16 v[52:55],a[212:215],a[36:39],v[16:19]
ds_read_u16_d16_hi v51,v51
v_lshlrev_b32_sdwa v46,1,v46 dst_sel:DWORD dst_unused:UNUSED_PAD src0_sel:DWORD src1_sel:WORD_1
v_mfma_f32_16x16x32_bf16 v[52:55],a[220:223],a[20:23],v[52:55]
ds_read_u16 v44,v44
v_lshlrev_b32_sdwa v45,1,v45 dst_sel:DWORD dst_unused:UNUSED_PAD src0_sel:DWORD src1_sel:WORD_1
v_mfma_f32_16x16x32_bf16 v[52:55],a[216:219],a[4:7],v[52:55]
s_add_u32 m0,s58,18480
ds_read_u16_d16_hi v46,v46
v_exp_f16_sdwa v8,v9 dst_sel:WORD_1 dst_unused:UNUSED_PRESERVE src0_sel:WORD_1
v_mfma_f32_16x16x32_bf16 v[136:139],a[248:251],v[24:27],v[136:139]
s_add_u32 s69,s69,s59
ds_read_u16 v45,v45
v_lshlrev_b32_sdwa v47,1,v47 dst_sel:DWORD dst_unused:UNUSED_PAD src0_sel:DWORD src1_sel:WORD_1
v_mfma_f32_16x16x32_bf16 v[140:143],a[248:251],v[28:31],v[140:143]
buffer_load_dwordx4 v5,s[44:47],s69 offen offset:0 lds
v_lshlrev_b32_sdwa v40,1,v40 dst_sel:DWORD dst_unused:UNUSED_PAD src0_sel:DWORD src1_sel:WORD_1
v_mfma_f32_16x16x32_bf16 v[68:71],a[252:255],v[28:31],v[68:71]
ds_read_u16_d16_hi v47,v47
v_pk_fma_f16 v8,v8,s55,s55
v_mfma_f32_16x16x32_bf16 v[64:67],a[252:255],v[24:27],v[64:67]
v_rcp_f16 v9,v8
v_lshlrev_b32_sdwa v42,1,v42 dst_sel:DWORD dst_unused:UNUSED_PAD src0_sel:DWORD src1_sel:WORD_1
v_mfma_f32_16x16x32_bf16 v[144:147],a[240:243],v[24:27],v[144:147]
s_add_u32 s69,s69,s53
ds_read_u16 v40,v40
v_lshlrev_b32_sdwa v41,1,v41 dst_sel:DWORD dst_unused:UNUSED_PAD src0_sel:DWORD src1_sel:WORD_1
v_mfma_f32_16x16x32_bf16 v[148:151],a[240:243],v[28:31],v[148:151]
s_waitcnt lgkmcnt(6)
buffer_load_dwordx4 v5,s[44:47],s69 offen offset:1024 lds
ds_read_u16_d16_hi v42,v42
v_mfma_f32_16x16x32_bf16 v[84:87],a[224:227],v[28:31],v[84:87]
s_add_u32 m0,s57,38896
ds_read_u16 v41,v41
v_lshlrev_b32_sdwa v43,1,v43 dst_sel:DWORD dst_unused:UNUSED_PAD src0_sel:DWORD src1_sel:WORD_1
v_mfma_f32_16x16x32_bf16 v[80:83],a[224:227],v[24:27],v[80:83]
s_add_u32 s89,s56,3538944
v_rcp_f16_sdwa v9,v8 dst_sel:WORD_1 dst_unused:UNUSED_PRESERVE src0_sel:WORD_1
v_mfma_f32_16x16x32_bf16 v[152:155],a[228:231],v[24:27],v[152:155]
buffer_load_dwordx4 v6,s[36:39],s89 offen lds
ds_read_u16_d16_hi v43,v43
v_lshlrev_b32_sdwa v36,1,v36 dst_sel:DWORD dst_unused:UNUSED_PAD src0_sel:DWORD src1_sel:WORD_1
v_mfma_f32_16x16x32_bf16 v[156:159],a[228:231],v[28:31],v[156:159]
s_add_u32 m0,s58,18480
ds_read_u16 v36,v36
v_lshlrev_b32_sdwa v38,1,v38 dst_sel:DWORD dst_unused:UNUSED_PAD src0_sel:DWORD src1_sel:WORD_1
v_mfma_f32_16x16x32_bf16 v[100:103],a[232:235],v[28:31],v[100:103]
s_add_u32 s69,s69,s53
ds_read_u16_d16_hi v38,v38
v_fma_mix_f32 v37,v37,v9,0 op_sel_hi:[0,1,0]
v_mfma_f32_16x16x32_bf16 v[96:99],a[232:235],v[24:27],v[96:99]
buffer_load_dwordx4 v5,s[44:47],s69 offen offset:2048 lds
v_mfma_f32_32x32x16_bf16 v[240:255],a[88:91],a[88:91],0
v_fma_mix_f32 v39,v39,v9,0 op_sel:[0,1,0] op_sel_hi:[0,1,0]
ds_read_b64_tr_b16 a[236:237],v3 offset:6160
ds_read_b64_tr_b16 a[238:239],v3 offset:7696
ds_read_b64_tr_b16 a[244:245],v3 offset:6168
ds_read_b64_tr_b16 a[246:247],v3 offset:7704
s_waitcnt vmcnt(9)
s_barrier
v_add_u32_sdwa v24,s60,v10 dst_sel:DWORD dst_unused:UNUSED_PAD src0_sel:DWORD src1_sel:WORD_1
v_subrev_u32 v8,88400,v24
v_min_u32 v24,v24,v8
v_add_u32 v25,7072,v24
v_subrev_u32 v8,88400,v25
v_min_u32 v25,v25,v8
s_waitcnt lgkmcnt(14)
v_lshl_add_u64 v[48:49],v[48:49],0,v[50:51]
s_waitcnt lgkmcnt(10)
v_lshl_add_u64 v[50:51],v[44:45],0,v[46:47]
s_waitcnt lgkmcnt(6)
v_lshl_add_u64 v[44:45],v[40:41],0,v[42:43]
s_waitcnt lgkmcnt(4)
v_or_b32 v46,v36,v38
v_cvt_pk_bf16_f32 v47,v37,v39
v_mfma_f32_16x16x32_bf16 v[112:115],a[248:251],v[48:51],v[112:115]
s_add_u32 m0,s58,0
s_add_u32 s69,s69,s59
buffer_load_dwordx4 v5,s[44:47],s69 offen offset:0 lds
v_xad_u32 v28,v11,0,v24
v_xad_u32 v29,v11,8,v25
v_mfma_f32_16x16x32_bf16 v[116:119],a[248:251],v[44:47],v[116:119]
v_xad_u32 v30,v11,16,v24
v_xad_u32 v31,v11,24,v25
v_mfma_f32_16x16x32_bf16 v[76:79],a[252:255],v[44:47],v[76:79]
ds_read_b64 a[48:49],v28 offset:0
ds_read_b64 a[50:51],v28 offset:192
v_mfma_f32_16x16x32_bf16 v[72:75],a[252:255],v[48:51],v[72:75]
ds_read_b64 a[52:53],v29 offset:0
ds_read_b64 a[54:55],v29 offset:192
v_mfma_f32_16x16x32_bf16 v[120:123],a[240:243],v[48:51],v[120:123]
s_add_u32 s69,s69,s53
buffer_load_dwordx4 v5,s[44:47],s69 offen offset:1024 lds
ds_read_b64 a[56:57],v28 offset:8
ds_read_b64 a[58:59],v28 offset:200
v_mfma_f32_16x16x32_bf16 v[124:127],a[240:243],v[44:47],v[124:127]
TE02_%=:
s_cbranch_execz TP4402_%=
ds_read_b64 a[60:61],v29 offset:8
ds_read_b64 a[62:63],v29 offset:200
v_mfma_f32_16x16x32_bf16 v[92:95],a[224:227],v[44:47],v[92:95]
v_mfma_f32_16x16x32_bf16 v[88:91],a[224:227],v[48:51],v[88:91]
v_mfma_f32_16x16x32_bf16 v[128:131],a[228:231],v[48:51],v[128:131]
v_mfma_f32_16x16x32_bf16 v[132:135],a[228:231],v[44:47],v[132:135]
v_mfma_f32_16x16x32_bf16 v[108:111],a[232:235],v[44:47],v[108:111]
v_mfma_f32_16x16x32_bf16 v[104:107],a[232:235],v[48:51],v[104:107]
ds_read_b64 a[64:65],v30 offset:0
ds_read_b64 a[66:67],v30 offset:192
ds_read_b64 a[68:69],v31 offset:0
ds_read_b64 a[70:71],v31 offset:192
s_waitcnt lgkmcnt(8)
v_smfmac_f32_16x16x64_bf16 v[160:163],a[96:99],a[48:55],v10
s_waitcnt lgkmcnt(4)
s_add_u32 s69,s69,s53
buffer_load_dwordx4 v5,s[44:47],s69 offen offset:2048 lds
ds_read_b64 a[48:49],v30 offset:8
ds_read_b64 a[50:51],v30 offset:200
ds_read_b64 a[52:53],v31 offset:8
ds_read_b64 a[54:55],v31 offset:200
v_smfmac_f32_16x16x64_bf16 v[164:167],a[100:103],a[56:63],v10
s_waitcnt lgkmcnt(4)
s_add_u32 m0,s57,42432
s_add_u32 s89,s56,3637248
buffer_load_dwordx4 v6,s[36:39],s89 offen lds
ds_read_b64 a[56:57],v28 offset:64
ds_read_b64 a[58:59],v28 offset:256
ds_read_b64 a[60:61],v29 offset:64
ds_read_b64 a[62:63],v29 offset:256
v_smfmac_f32_16x16x64_bf16 v[168:171],a[104:107],a[64:71],v10
s_waitcnt lgkmcnt(4)
ds_read_b64 a[64:65],v28 offset:72
ds_read_b64 a[66:67],v28 offset:264
ds_read_b64 a[68:69],v29 offset:72
ds_read_b64 a[70:71],v29 offset:264
v_and_b32 v48,8,v11
v_pk_mul_f32 v[44:45],v[160:161],v[160:161]
v_pk_mul_f32 v[46:47],v[162:163],v[162:163]
v_smfmac_f32_16x16x64_bf16 v[172:175],a[108:111],a[48:55],v10
s_waitcnt lgkmcnt(4)
ds_read_b64 a[48:49],v30 offset:64
ds_read_b64 a[50:51],v30 offset:256
ds_read_b64 a[52:53],v31 offset:64
ds_read_b64 a[54:55],v31 offset:256
v_mul_u32_u24 v48,884,v48
s_add_u32 s64,s60,3536
v_pk_add_f32 v[36:37],v[160:161],v[164:165]
v_pk_fma_f32 v[44:45],v[164:165],v[164:165],v[44:45]
v_pk_add_f32 v[38:39],v[162:163],v[166:167]
v_pk_fma_f32 v[46:47],v[166:167],v[166:167],v[46:47]
v_smfmac_f32_16x16x64_bf16 v[176:179],a[128:131],a[56:63],v10
s_waitcnt lgkmcnt(4)
ds_read_b64 a[56:57],v30 offset:72
ds_read_b64 a[58:59],v30 offset:264
ds_read_b64 a[60:61],v31 offset:72
ds_read_b64 a[62:63],v31 offset:264
v_add_u32 v51,s64,v48
v_pk_add_f32 v[36:37],v[36:37],v[168:169]
v_pk_fma_f32 v[44:45],v[168:169],v[168:169],v[44:45]
v_pk_add_f32 v[38:39],v[38:39],v[170:171]
v_pk_fma_f32 v[46:47],v[170:171],v[170:171],v[46:47]
v_smfmac_f32_16x16x64_bf16 v[180:183],a[132:135],a[64:71],v10
s_waitcnt lgkmcnt(4)
ds_read_b64 a[64:65],v28 offset:128
ds_read_b64 a[66:67],v28 offset:320
ds_read_b64 a[68:69],v29 offset:128
ds_read_b64 a[70:71],v29 offset:320
v_subrev_u32 v8,88400,v51
v_pk_add_f32 v[36:37],v[36:37],v[172:173]
v_pk_fma_f32 v[44:45],v[172:173],v[172:173],v[44:45]
v_pk_add_f32 v[38:39],v[38:39],v[174:175]
v_pk_fma_f32 v[46:47],v[174:175],v[174:175],v[46:47]
v_smfmac_f32_16x16x64_bf16 v[184:187],a[136:139],a[48:55],v10
s_waitcnt lgkmcnt(4)
ds_read_b64 a[48:49],v28 offset:136
ds_read_b64 a[50:51],v28 offset:328
ds_read_b64 a[52:53],v29 offset:136
ds_read_b64 a[54:55],v29 offset:328
v_min_u32 v51,v51,v8
v_pk_add_f32 v[36:37],v[36:37],v[176:177]
v_pk_fma_f32 v[44:45],v[176:177],v[176:177],v[44:45]
v_pk_add_f32 v[38:39],v[38:39],v[178:179]
v_pk_fma_f32 v[46:47],v[178:179],v[178:179],v[46:47]
v_smfmac_f32_16x16x64_bf16 v[188:191],a[140:143],a[56:63],v10
s_waitcnt lgkmcnt(4)
ds_read_b64 a[56:57],v30 offset:128
ds_read_b64 a[58:59],v30 offset:320
ds_read_b64 a[60:61],v31 offset:128
ds_read_b64 a[62:63],v31 offset:320
v_xad_u32 v28,v11,32,v24
v_xad_u32 v29,v11,40,v25
v_pk_add_f32 v[36:37],v[36:37],v[180:181]
v_pk_fma_f32 v[44:45],v[180:181],v[180:181],v[44:45]
v_pk_add_f32 v[38:39],v[38:39],v[182:183]
v_pk_fma_f32 v[46:47],v[182:183],v[182:183],v[46:47]
v_smfmac_f32_16x16x64_bf16 v[192:195],a[160:163],a[64:71],v10
s_waitcnt lgkmcnt(4)
s_add_u32 m0,s57,45976
s_add_u32 s89,s56,3735552
buffer_load_dwordx4 v6,s[36:39],s89 offen lds
ds_read_b64 a[64:65],v30 offset:136
ds_read_b64 a[66:67],v30 offset:328
ds_read_b64 a[68:69],v31 offset:136
ds_read_b64 a[70:71],v31 offset:328
v_add_u32 v50,3536,v51
v_pk_add_f32 v[36:37],v[36:37],v[184:185]
v_pk_fma_f32 v[44:45],v[184:185],v[184:185],v[44:45]
v_pk_add_f32 v[38:39],v[38:39],v[186:187]
v_pk_fma_f32 v[46:47],v[186:187],v[186:187],v[46:47]
v_smfmac_f32_16x16x64_bf16 v[196:199],a[164:167],a[48:55],v10
s_waitcnt lgkmcnt(4)
ds_read_b64 a[48:49],v28 offset:0
ds_read_b64 a[50:51],v28 offset:192
ds_read_b64 a[52:53],v29 offset:0
ds_read_b64 a[54:55],v29 offset:192
v_xad_u32 v30,v11,48,v24
v_xad_u32 v31,v11,56,v25
v_pk_add_f32 v[36:37],v[36:37],v[188:189]
v_pk_fma_f32 v[44:45],v[188:189],v[188:189],v[44:45]
v_pk_add_f32 v[38:39],v[38:39],v[190:191]
v_pk_fma_f32 v[46:47],v[190:191],v[190:191],v[46:47]
v_smfmac_f32_16x16x64_bf16 v[200:203],a[168:171],a[56:63],v10
s_waitcnt lgkmcnt(4)
ds_read_b64 a[56:57],v28 offset:8
ds_read_b64 a[58:59],v28 offset:200
ds_read_b64 a[60:61],v29 offset:8
ds_read_b64 a[62:63],v29 offset:200
v_subrev_u32 v8,88400,v50
v_pk_add_f32 v[36:37],v[36:37],v[192:193]
v_pk_fma_f32 v[44:45],v[192:193],v[192:193],v[44:45]
v_pk_add_f32 v[38:39],v[38:39],v[194:195]
v_pk_fma_f32 v[46:47],v[194:195],v[194:195],v[46:47]
v_smfmac_f32_16x16x64_bf16 v[204:207],a[172:175],a[64:71],v10
s_waitcnt lgkmcnt(4)
ds_read_b64 a[64:65],v28 offset:64
ds_read_b64 a[66:67],v28 offset:256
ds_read_b64 a[68:69],v29 offset:64
ds_read_b64 a[70:71],v29 offset:256
v_min_u32 v50,v50,v8
v_pk_add_f32 v[36:37],v[36:37],v[196:197]
v_pk_fma_f32 v[44:45],v[196:197],v[196:197],v[44:45]
v_pk_add_f32 v[38:39],v[38:39],v[198:199]
v_pk_fma_f32 v[46:47],v[198:199],v[198:199],v[46:47]
v_smfmac_f32_16x16x64_bf16 v[208:211],a[112:115],a[48:55],v10
s_waitcnt lgkmcnt(4)
ds_read_b64 a[48:49],v28 offset:72
ds_read_b64 a[50:51],v28 offset:264
ds_read_b64 a[52:53],v29 offset:72
ds_read_b64 a[54:55],v29 offset:264
v_xor_b32 v9,0x80008,v12
v_pk_add_f32 v[36:37],v[36:37],v[200:201]
v_pk_fma_f32 v[44:45],v[200:201],v[200:201],v[44:45]
v_pk_add_f32 v[38:39],v[38:39],v[202:203]
v_pk_fma_f32 v[46:47],v[202:203],v[202:203],v[46:47]
v_smfmac_f32_16x16x64_bf16 v[212:215],a[116:119],a[56:63],v10
s_waitcnt lgkmcnt(4)
ds_read_b64 a[56:57],v28 offset:128
ds_read_b64 a[58:59],v28 offset:320
ds_read_b64 a[60:61],v29 offset:128
ds_read_b64 a[62:63],v29 offset:320
v_add_u32_sdwa v48,v12,v51 dst_sel:DWORD dst_unused:UNUSED_PAD src0_sel:WORD_0 src1_sel:DWORD
v_pk_add_f32 v[36:37],v[36:37],v[204:205]
v_pk_fma_f32 v[44:45],v[204:205],v[204:205],v[44:45]
v_pk_add_f32 v[38:39],v[38:39],v[206:207]
v_pk_fma_f32 v[46:47],v[206:207],v[206:207],v[46:47]
v_smfmac_f32_16x16x64_bf16 v[216:219],a[144:147],a[64:71],v10
s_waitcnt lgkmcnt(4)
ds_read_b64 a[64:65],v28 offset:136
ds_read_b64 a[66:67],v28 offset:328
ds_read_b64 a[68:69],v29 offset:136
ds_read_b64 a[70:71],v29 offset:328
v_add_u32_sdwa v49,v12,v51 dst_sel:DWORD dst_unused:UNUSED_PAD src0_sel:WORD_1 src1_sel:DWORD
v_pk_add_f32 v[36:37],v[36:37],v[208:209]
v_pk_fma_f32 v[44:45],v[208:209],v[208:209],v[44:45]
v_pk_add_f32 v[38:39],v[38:39],v[210:211]
v_pk_fma_f32 v[46:47],v[210:211],v[210:211],v[46:47]
v_smfmac_f32_16x16x64_bf16 v[220:223],a[148:151],a[48:55],v10
s_waitcnt lgkmcnt(4)
ds_read_b64 a[48:49],v30 offset:0
ds_read_b64 a[50:51],v30 offset:192
ds_read_b64 a[52:53],v31 offset:0
ds_read_b64 a[54:55],v31 offset:192
v_add_u32_sdwa v51,v9,v50 dst_sel:DWORD dst_unused:UNUSED_PAD src0_sel:WORD_1 src1_sel:DWORD
v_pk_add_f32 v[36:37],v[36:37],v[212:213]
v_pk_fma_f32 v[44:45],v[212:213],v[212:213],v[44:45]
v_pk_add_f32 v[38:39],v[38:39],v[214:215]
v_pk_fma_f32 v[46:47],v[214:215],v[214:215],v[46:47]
v_smfmac_f32_16x16x64_bf16 v[224:227],a[176:179],a[56:63],v10
s_waitcnt lgkmcnt(4)
s_add_u32 m0,s57,49512
s_add_u32 s89,s56,3833856
buffer_load_dwordx4 v6,s[36:39],s89 offen lds
ds_read_b64 a[56:57],v30 offset:8
ds_read_b64 a[58:59],v30 offset:200
ds_read_b64 a[60:61],v31 offset:8
ds_read_b64 a[62:63],v31 offset:200
v_add_u32_sdwa v50,v9,v50 dst_sel:DWORD dst_unused:UNUSED_PAD src0_sel:WORD_0 src1_sel:DWORD
v_pk_add_f32 v[36:37],v[36:37],v[216:217]
v_pk_fma_f32 v[44:45],v[216:217],v[216:217],v[44:45]
v_pk_add_f32 v[38:39],v[38:39],v[218:219]
v_pk_fma_f32 v[46:47],v[218:219],v[218:219],v[46:47]
v_smfmac_f32_16x16x64_bf16 v[228:231],a[180:183],a[64:71],v10
s_waitcnt lgkmcnt(4)
ds_read_b64 a[64:65],v30 offset:64
ds_read_b64 a[66:67],v30 offset:256
ds_read_b64 a[68:69],v31 offset:64
ds_read_b64 a[70:71],v31 offset:256
v_pk_add_f32 v[36:37],v[36:37],v[220:221]
v_pk_fma_f32 v[44:45],v[220:221],v[220:221],v[44:45]
v_pk_add_f32 v[38:39],v[38:39],v[222:223]
v_pk_fma_f32 v[46:47],v[222:223],v[222:223],v[46:47]
v_smfmac_f32_16x16x64_bf16 v[232:235],a[120:123],a[48:55],v10
s_waitcnt lgkmcnt(4)
ds_read_b64 a[48:49],v30 offset:72
ds_read_b64 a[50:51],v30 offset:264
ds_read_b64 a[52:53],v31 offset:72
ds_read_b64 a[54:55],v31 offset:264
v_pk_add_f32 v[36:37],v[36:37],v[224:225]
v_pk_fma_f32 v[44:45],v[224:225],v[224:225],v[44:45]
v_pk_add_f32 v[38:39],v[38:39],v[226:227]
v_pk_fma_f32 v[46:47],v[226:227],v[226:227],v[46:47]
v_smfmac_f32_16x16x64_bf16 v[236:239],a[124:127],a[56:63],v10
s_waitcnt lgkmcnt(4)
ds_read_b64 a[56:57],v30 offset:128
ds_read_b64 a[58:59],v30 offset:320
ds_read_b64 a[60:61],v31 offset:128
ds_read_b64 a[62:63],v31 offset:320
v_pk_add_f32 v[36:37],v[36:37],v[228:229]
v_pk_fma_f32 v[44:45],v[228:229],v[228:229],v[44:45]
v_pk_add_f32 v[38:39],v[38:39],v[230:231]
v_pk_fma_f32 v[46:47],v[230:231],v[230:231],v[46:47]
v_smfmac_f32_16x16x64_bf16 v[240:243],a[152:155],a[64:71],v10
s_waitcnt lgkmcnt(4)
ds_read_b64 a[64:65],v30 offset:136
ds_read_b64 a[66:67],v30 offset:328
ds_read_b64 a[68:69],v31 offset:136
ds_read_b64 a[70:71],v31 offset:328
v_pk_add_f32 v[36:37],v[36:37],v[232:233]
v_pk_fma_f32 v[44:45],v[232:233],v[232:233],v[44:45]
v_pk_add_f32 v[38:39],v[38:39],v[234:235]
v_pk_fma_f32 v[46:47],v[234:235],v[234:235],v[46:47]
v_smfmac_f32_16x16x64_bf16 v[244:247],a[156:159],a[48:55],v10
s_waitcnt lgkmcnt(4)
ds_read_b64 a[248:249],v48 offset:57344
ds_read_b64 a[250:251],v48 offset:57352
ds_read_b64 a[252:253],v49 offset:57344
ds_read_b64 a[254:255],v49 offset:57352
ds_read_b128 a[228:231],v13 offset:0
v_pk_add_f32 v[36:37],v[36:37],v[236:237]
v_pk_fma_f32 v[44:45],v[236:237],v[236:237],v[44:45]
v_pk_add_f32 v[38:39],v[38:39],v[238:239]
v_pk_fma_f32 v[46:47],v[238:239],v[238:239],v[46:47]
v_smfmac_f32_16x16x64_bf16 v[248:251],a[184:187],a[56:63],v10
s_waitcnt lgkmcnt(5)
ds_read_b64 a[240:241],v50 offset:57344
ds_read_b64 a[242:243],v50 offset:57352
ds_read_b64 a[224:225],v51 offset:57344
ds_read_b64 a[226:227],v51 offset:57352
ds_read_b128 a[232:235],v13 offset:256
v_pk_add_f32 v[36:37],v[36:37],v[240:241]
v_pk_fma_f32 v[44:45],v[240:241],v[240:241],v[44:45]
v_pk_add_f32 v[38:39],v[38:39],v[242:243]
v_pk_fma_f32 v[46:47],v[242:243],v[242:243],v[46:47]
v_smfmac_f32_16x16x64_bf16 v[252:255],a[188:191],a[64:71],v10
s_add_u32 m0,s57,53040
s_add_u32 s89,s56,3932160
buffer_load_dwordx4 v6,s[36:39],s89 offen lds
ds_read_b128 v[28:31],v4 offset:3584
ds_read_b128 v[24:27],v4 offset:3616
v_pk_add_f32 v[36:37],v[36:37],v[244:245]
v_pk_fma_f32 v[44:45],v[244:245],v[244:245],v[44:45]
v_pk_add_f32 v[38:39],v[38:39],v[246:247]
v_pk_fma_f32 v[46:47],v[246:247],v[246:247],v[46:47]
v_pk_add_f32 v[36:37],v[36:37],v[248:249]
v_pk_fma_f32 v[44:45],v[248:249],v[248:249],v[44:45]
v_pk_add_f32 v[38:39],v[38:39],v[250:251]
v_pk_fma_f32 v[46:47],v[250:251],v[250:251],v[46:47]
v_pk_add_f32 v[36:37],v[36:37],v[252:253]
v_pk_fma_f32 v[44:45],v[252:253],v[252:253],v[44:45]
v_pk_add_f32 v[38:39],v[38:39],v[254:255]
v_pk_fma_f32 v[46:47],v[254:255],v[254:255],v[46:47]
s_nop 0
v_permlane32_swap_b32 v36,v38
v_permlane32_swap_b32 v37,v39
v_permlane32_swap_b32 v44,v46
v_permlane32_swap_b32 v45,v47
v_pk_add_f32 v[36:37],v[36:37],v[38:39]
v_pk_add_f32 v[44:45],v[44:45],v[46:47]
s_add_u32 s60,s60,56576
s_sub_u32 s64,s60,88400
s_min_u32 s60,s60,s64
v_permlane16_swap_b32 v36,v37
v_permlane16_swap_b32 v44,v45
v_add_f32 v8,v36,v37
v_add_f32 v9,v44,v45
v_mul_f32 v8,0x3c2aaaab,v8
v_mul_f32 v9,0x3c2aaaab,v9
v_cmp_ngt_f32 vcc,0x5d800000,v9
v_fma_f32 v9,-v8,v8,v9
v_max_f32 v9,0,v9
s_or_b64 s[90:91],s[90:91],vcc
v_add_f32 v9,0x3727c5ac,v9
v_rsq_f32 v9,v9
s_nop 0
v_mul_f32 v36,s96,v9
v_mul_f32 v37,s96,v9
v_mul_f32 v44,-v8,v36
v_mul_f32 v45,-v8,v36
v_permlane16_swap_b32 v36,v37
s_nop 0
v_permlane16_swap_b32 v44,v45
v_mov_b64 v[38:39],v[36:37]
v_mov_b64 v[46:47],v[44:45]
s_nop 0
v_permlane32_swap_b32 v36,v38
v_permlane32_swap_b32 v37,v39
v_permlane32_swap_b32 v44,v46
v_permlane32_swap_b32 v45,v47
v_pk_fma_f32 v[160:161],v[160:161],v[36:37],v[44:45]
v_pk_fma_f32 v[162:163],v[162:163],v[38:39],v[46:47]
v_pk_fma_f32 v[164:165],v[164:165],v[36:37],v[44:45]
v_pk_fma_f32 v[166:167],v[166:167],v[38:39],v[46:47]
v_pk_fma_f32 v[168:169],v[168:169],v[36:37],v[44:45]
v_pk_fma_f32 v[170:171],v[170:171],v[38:39],v[46:47]
v_pk_fma_f32 v[172:173],v[172:173],v[36:37],v[44:45]
v_pk_fma_f32 v[174:175],v[174:175],v[38:39],v[46:47]
v_pk_fma_f32 v[208:209],v[208:209],v[36:37],v[44:45]
v_pk_fma_f32 v[210:211],v[210:211],v[38:39],v[46:47]
v_pk_fma_f32 v[212:213],v[212:213],v[36:37],v[44:45]
v_pk_fma_f32 v[214:215],v[214:215],v[38:39],v[46:47]
v_pk_fma_f32 v[232:233],v[232:233],v[36:37],v[44:45]
v_pk_fma_f32 v[234:235],v[234:235],v[38:39],v[46:47]
v_pk_fma_f32 v[236:237],v[236:237],v[36:37],v[44:45]
v_pk_fma_f32 v[238:239],v[238:239],v[38:39],v[46:47]
v_pk_fma_f32 v[176:177],v[176:177],v[36:37],v[44:45]
v_pk_fma_f32 v[178:179],v[178:179],v[38:39],v[46:47]
v_pk_fma_f32 v[180:181],v[180:181],v[36:37],v[44:45]
v_pk_fma_f32 v[182:183],v[182:183],v[38:39],v[46:47]
v_pk_fma_f32 v[184:185],v[184:185],v[36:37],v[44:45]
v_pk_fma_f32 v[186:187],v[186:187],v[38:39],v[46:47]
v_pk_fma_f32 v[188:189],v[188:189],v[36:37],v[44:45]
v_pk_fma_f32 v[190:191],v[190:191],v[38:39],v[46:47]
v_pk_fma_f32 v[216:217],v[216:217],v[36:37],v[44:45]
v_pk_fma_f32 v[218:219],v[218:219],v[38:39],v[46:47]
v_pk_fma_f32 v[220:221],v[220:221],v[36:37],v[44:45]
v_pk_fma_f32 v[222:223],v[222:223],v[38:39],v[46:47]
v_pk_fma_f32 v[240:241],v[240:241],v[36:37],v[44:45]
v_pk_fma_f32 v[242:243],v[242:243],v[38:39],v[46:47]
v_pk_fma_f32 v[244:245],v[244:245],v[36:37],v[44:45]
v_pk_fma_f32 v[246:247],v[246:247],v[38:39],v[46:47]
v_pk_fma_f32 v[192:193],v[192:193],v[36:37],v[44:45]
v_pk_fma_f32 v[194:195],v[194:195],v[38:39],v[46:47]
v_pk_fma_f32 v[196:197],v[196:197],v[36:37],v[44:45]
v_pk_fma_f32 v[198:199],v[198:199],v[38:39],v[46:47]
v_pk_fma_f32 v[200:201],v[200:201],v[36:37],v[44:45]
v_pk_fma_f32 v[202:203],v[202:203],v[38:39],v[46:47]
v_pk_fma_f32 v[204:205],v[204:205],v[36:37],v[44:45]
v_pk_fma_f32 v[206:207],v[206:207],v[38:39],v[46:47]
v_pk_fma_f32 v[224:225],v[224:225],v[36:37],v[44:45]
v_pk_fma_f32 v[226:227],v[226:227],v[38:39],v[46:47]
v_pk_fma_f32 v[228:229],v[228:229],v[36:37],v[44:45]
v_pk_fma_f32 v[230:231],v[230:231],v[38:39],v[46:47]
v_pk_fma_f32 v[248:249],v[248:249],v[36:37],v[44:45]
v_pk_fma_f32 v[250:251],v[250:251],v[38:39],v[46:47]
v_pk_fma_f32 v[252:253],v[252:253],v[36:37],v[44:45]
v_pk_fma_f32 v[254:255],v[254:255],v[38:39],v[46:47]
v_mfma_f32_16x16x32_bf16 v[40:43],a[216:219],a[8:11],v[16:19]
ds_read_b64_tr_b16 a[196:197],v1 offset:6160
v_cvt_pk_bf16_f32 v160,v160,v164
v_cvt_pk_bf16_f32 v168,v168,v172
v_cvt_pk_bf16_f32 v208,v208,v212
v_lshlrev_b32_sdwa v32,1,v32 dst_sel:DWORD dst_unused:UNUSED_PAD src0_sel:DWORD src1_sel:WORD_1
v_mfma_f32_16x16x32_bf16 v[40:43],a[220:223],a[24:27],v[40:43]
ds_read_u16 v32,v32
v_accvgpr_write_b32 a48,v160
v_accvgpr_write_b32 a49,v168
v_cvt_pk_bf16_f32 v232,v232,v236
v_accvgpr_write_b32 a50,v208
v_mfma_f32_16x16x32_bf16 v[40:43],a[212:215],a[40:43],v[40:43]
ds_read_b64_tr_b16 a[198:199],v1 offset:7184
v_cvt_pk_bf16_f32 v161,v161,v165
v_accvgpr_write_b32 a51,v232
v_cvt_pk_bf16_f32 v169,v169,v173
v_accvgpr_write_b32 a52,v161
v_fma_mixlo_f16 v8,v57,s99,0
v_mfma_f32_16x16x32_bf16 v[36:39],a[208:211],a[40:43],v[20:23]
ds_read_b64_tr_b16 a[192:193],v1 offset:8208
v_cvt_pk_bf16_f32 v209,v209,v213
v_accvgpr_write_b32 a53,v169
v_cvt_pk_bf16_f32 v233,v233,v237
v_accvgpr_write_b32 a54,v209
v_lshlrev_b32_sdwa v34,1,v34 dst_sel:DWORD dst_unused:UNUSED_PAD src0_sel:DWORD src1_sel:WORD_1
v_mfma_f32_16x16x32_bf16 v[36:39],a[204:207],a[24:27],v[36:39]
ds_read_u16_d16_hi v34,v34
v_cvt_pk_bf16_f32 v162,v162,v166
v_accvgpr_write_b32 a55,v233
v_cvt_pk_bf16_f32 v170,v170,v174
v_accvgpr_write_b32 a56,v162
v_fma_mixhi_f16 v8,v59,s99,0
v_mfma_f32_16x16x32_bf16 v[36:39],a[200:203],a[8:11],v[36:39]
ds_read_b64_tr_b16 a[194:195],v1 offset:9232
v_accvgpr_write_b32 a57,v170
v_cvt_pk_bf16_f32 v210,v210,v214
v_accvgpr_write_b32 a58,v210
v_lshlrev_b32_sdwa v33,1,v33 dst_sel:DWORD dst_unused:UNUSED_PAD src0_sel:DWORD src1_sel:WORD_1
v_cvt_pk_bf16_f32 v234,v234,v238
v_mfma_f32_16x16x32_bf16 v[44:47],a[200:203],a[12:15],v[20:23]
ds_read_u16 v33,v33
v_accvgpr_write_b32 a59,v234
v_cvt_pk_bf16_f32 v163,v163,v167
v_accvgpr_write_b32 a60,v163
v_mfma_f32_16x16x32_bf16 v[44:47],a[204:207],a[28:31],v[44:47]
v_cvt_pk_bf16_f32 v171,v171,v175
v_accvgpr_write_b32 a61,v171
v_cvt_pk_bf16_f32 v211,v211,v215
v_accvgpr_write_b32 a62,v211
v_pk_fma_f16 v9,v8,v8,s54
v_mfma_f32_16x16x32_bf16 v[44:47],a[208:211],a[44:47],v[44:47]
s_waitcnt lgkmcnt(7)
v_cvt_pk_bf16_f32 v235,v235,v239
v_accvgpr_write_b32 a63,v235
v_cvt_pk_bf16_f32 v176,v176,v180
v_cvt_pk_bf16_f32 v184,v184,v188
v_lshlrev_b32_sdwa v35,1,v35 dst_sel:DWORD dst_unused:UNUSED_PAD src0_sel:DWORD src1_sel:WORD_1
v_mfma_f32_16x16x32_bf16 v[232:235],a[228:231],a[248:251],v[28:31]
ds_read_u16_d16_hi v35,v35
v_cvt_pk_bf16_f32 v216,v216,v220
v_accvgpr_write_b32 a64,v176
v_accvgpr_write_b32 a65,v184
v_cvt_pk_bf16_f32 v240,v240,v244
v_pk_mul_f16 v9,v8,v9
v_mfma_f32_16x16x32_bf16 v[236:239],a[228:231],a[252:255],v[28:31]
v_accvgpr_write_b32 a66,v216
v_cvt_pk_bf16_f32 v177,v177,v181
v_accvgpr_write_b32 a67,v240
v_cvt_pk_bf16_f32 v185,v185,v189
v_lshlrev_b32_sdwa v60,1,v60 dst_sel:DWORD dst_unused:UNUSED_PAD src0_sel:DWORD src1_sel:WORD_1
v_mfma_f32_16x16x32_bf16 v[208:211],a[228:231],a[240:243],v[28:31]
v_exp_f16 v8,v9
v_accvgpr_write_b32 a68,v177
v_cvt_pk_bf16_f32 v217,v217,v221
v_accvgpr_write_b32 a69,v185
v_cvt_pk_bf16_f32 v241,v241,v245
v_mfma_f32_16x16x32_bf16 v[212:215],a[228:231],a[224:227],v[28:31]
ds_read_b128 a[228:231],v13 offset:512
ds_read_b128 v[28:31],v4 offset:3712
v_accvgpr_write_b32 a70,v217
v_cvt_pk_bf16_f32 v178,v178,v182
v_accvgpr_write_b32 a71,v241
v_mfma_f32_16x16x32_bf16 v[160:163],a[232:235],a[248:251],v[24:27]
ds_read_b64 a[248:249],v48 offset:57408
ds_read_b64 a[250:251],v48 offset:57416
v_cvt_pk_bf16_f32 v186,v186,v190
v_accvgpr_write_b32 a72,v178
v_accvgpr_write_b32 a73,v186
v_cvt_pk_bf16_f32 v218,v218,v222
v_lshlrev_b32_sdwa v62,1,v62 dst_sel:DWORD dst_unused:UNUSED_PAD src0_sel:DWORD src1_sel:WORD_1
v_mfma_f32_16x16x32_bf16 v[164:167],a[232:235],a[252:255],v[24:27]
ds_read_b64 a[252:253],v49 offset:57408
ds_read_b64 a[254:255],v49 offset:57416
v_accvgpr_write_b32 a74,v218
v_cvt_pk_bf16_f32 v242,v242,v246
v_accvgpr_write_b32 a75,v242
v_cvt_pk_bf16_f32 v179,v179,v183
v_mfma_f32_16x16x32_bf16 v[168:171],a[232:235],a[240:243],v[24:27]
ds_read_b64 a[240:241],v50 offset:57408
ds_read_b64 a[242:243],v50 offset:57416
v_accvgpr_write_b32 a76,v179
v_cvt_pk_bf16_f32 v187,v187,v191
v_accvgpr_write_b32 a77,v187
v_cvt_pk_bf16_f32 v219,v219,v223
v_lshlrev_b32_sdwa v61,1,v61 dst_sel:DWORD dst_unused:UNUSED_PAD src0_sel:DWORD src1_sel:WORD_1
v_mfma_f32_16x16x32_bf16 v[172:175],a[232:235],a[224:227],v[24:27]
s_waitcnt lgkmcnt(4)
ds_read_b64 a[224:225],v51 offset:57408
ds_read_b64 a[226:227],v51 offset:57416
v_exp_f16_sdwa v8,v9 dst_sel:WORD_1 dst_unused:UNUSED_PRESERVE src0_sel:WORD_1
v_accvgpr_write_b32 a78,v219
v_cvt_pk_bf16_f32 v243,v243,v247
v_accvgpr_write_b32 a79,v243
v_cvt_pk_bf16_f32 v192,v192,v196
v_mfma_f32_16x16x32_bf16 v[240:243],a[228:231],a[248:251],v[28:31]
s_waitcnt lgkmcnt(4)
ds_read_b128 a[232:235],v13 offset:768
ds_read_b128 v[24:27],v4 offset:3744
v_cvt_pk_bf16_f32 v200,v200,v204
v_cvt_pk_bf16_f32 v224,v224,v228
v_accvgpr_write_b32 a80,v192
v_accvgpr_write_b32 a81,v200
v_lshlrev_b32_sdwa v63,1,v63 dst_sel:DWORD dst_unused:UNUSED_PAD src0_sel:DWORD src1_sel:WORD_1
v_mfma_f32_16x16x32_bf16 v[244:247],a[228:231],a[252:255],v[28:31]
s_waitcnt lgkmcnt(4)
ds_read_u16 v60,v60
v_cvt_pk_bf16_f32 v248,v248,v252
v_accvgpr_write_b32 a82,v224
v_cvt_pk_bf16_f32 v193,v193,v197
v_mfma_f32_16x16x32_bf16 v[216:219],a[228:231],a[240:243],v[28:31]
s_waitcnt lgkmcnt(3)
ds_read_u16_d16_hi v62,v62
v_accvgpr_write_b32 a83,v248
v_cvt_pk_bf16_f32 v201,v201,v205
v_accvgpr_write_b32 a84,v193
v_cvt_pk_bf16_f32 v225,v225,v229
v_mfma_f32_16x16x32_bf16 v[220:223],a[228:231],a[224:227],v[28:31]
s_waitcnt lgkmcnt(2)
ds_read_b128 a[228:231],v13 offset:1024
ds_read_b128 v[28:31],v4 offset:3840
v_accvgpr_write_b32 a85,v201
v_cvt_pk_bf16_f32 v249,v249,v253
v_accvgpr_write_b32 a86,v225
v_cvt_pk_bf16_f32 v194,v194,v198
v_lshlrev_b32_sdwa v52,1,v52 dst_sel:DWORD dst_unused:UNUSED_PAD src0_sel:DWORD src1_sel:WORD_1
v_mfma_f32_16x16x32_bf16 v[176:179],a[232:235],a[248:251],v[24:27]
ds_read_b64 a[248:249],v48 offset:57472
ds_read_b64 a[250:251],v48 offset:57480
v_accvgpr_write_b32 a87,v249
v_cvt_pk_bf16_f32 v202,v202,v206
v_accvgpr_write_b32 a88,v194
v_accvgpr_write_b32 a89,v202
v_pk_fma_f16 v8,v8,s55,s55
v_mfma_f32_16x16x32_bf16 v[180:183],a[232:235],a[252:255],v[24:27]
ds_read_b64 a[252:253],v49 offset:57472
ds_read_b64 a[254:255],v49 offset:57480
v_cvt_pk_bf16_f32 v226,v226,v230
v_accvgpr_write_b32 a90,v226
v_cvt_pk_bf16_f32 v250,v250,v254
v_accvgpr_write_b32 a91,v250
v_lshlrev_b32_sdwa v54,1,v54 dst_sel:DWORD dst_unused:UNUSED_PAD src0_sel:DWORD src1_sel:WORD_1
v_mfma_f32_16x16x32_bf16 v[184:187],a[232:235],a[240:243],v[24:27]
ds_read_b64 a[240:241],v50 offset:57472
ds_read_b64 a[242:243],v50 offset:57480
v_rcp_f16 v9,v8
v_cvt_pk_bf16_f32 v195,v195,v199
v_accvgpr_write_b32 a92,v195
v_cvt_pk_bf16_f32 v203,v203,v207
v_accvgpr_write_b32 a93,v203
v_mfma_f32_16x16x32_bf16 v[188:191],a[232:235],a[224:227],v[24:27]
s_waitcnt lgkmcnt(4)
ds_read_b64 a[224:225],v51 offset:57472
ds_read_b64 a[226:227],v51 offset:57480
v_cvt_pk_bf16_f32 v227,v227,v231
v_accvgpr_write_b32 a94,v227
TE03_%=:
s_cbranch_execz TP4943_%=
v_cvt_pk_bf16_f32 v251,v251,v255
v_accvgpr_write_b32 a95,v251
v_mfma_f32_16x16x32_bf16 v[248:251],a[228:231],a[248:251],v[28:31]
s_waitcnt lgkmcnt(4)
ds_read_b128 a[232:235],v13 offset:1280
ds_read_b128 v[24:27],v4 offset:3872
v_lshlrev_b32_sdwa v53,1,v53 dst_sel:DWORD dst_unused:UNUSED_PAD src0_sel:DWORD src1_sel:WORD_1
v_mfma_f32_16x16x32_bf16 v[252:255],a[228:231],a[252:255],v[28:31]
s_waitcnt lgkmcnt(4)
ds_read_u16 v61,v61
v_lshlrev_b32_sdwa v55,1,v55 dst_sel:DWORD dst_unused:UNUSED_PAD src0_sel:DWORD src1_sel:WORD_1
v_mfma_f32_16x16x32_bf16 v[224:227],a[228:231],a[240:243],v[28:31]
s_waitcnt lgkmcnt(3)
ds_read_u16_d16_hi v63,v63
v_lshlrev_b32_sdwa v56,1,v56 dst_sel:DWORD dst_unused:UNUSED_PAD src0_sel:DWORD src1_sel:WORD_1
v_mfma_f32_16x16x32_bf16 v[228:231],a[228:231],a[224:227],v[28:31]
s_waitcnt lgkmcnt(2)
ds_read_b64_tr_b16 a[228:229],v3 offset:6224
ds_read_b64_tr_b16 a[230:231],v3 offset:7760
v_rcp_f16_sdwa v9,v8 dst_sel:WORD_1 dst_unused:UNUSED_PRESERVE src0_sel:WORD_1
v_mfma_f32_16x16x32_bf16 v[192:195],a[232:235],a[248:251],v[24:27]
ds_read_b64_tr_b16 a[248:249],v3 offset:6232
ds_read_b64_tr_b16 a[250:251],v3 offset:7768
v_mfma_f32_16x16x32_bf16 v[196:199],a[232:235],a[252:255],v[24:27]
ds_read_b64_tr_b16 a[252:253],v3 offset:6288
ds_read_b64_tr_b16 a[254:255],v3 offset:7824
v_mfma_f32_16x16x32_bf16 v[200:203],a[232:235],a[240:243],v[24:27]
ds_read_b64_tr_b16 a[240:241],v3 offset:6296
ds_read_b64_tr_b16 a[242:243],v3 offset:7832
v_mfma_f32_16x16x32_bf16 v[204:207],a[232:235],a[224:227],v[24:27]
ds_read_u16 v52,v52
ds_read_u16_d16_hi v54,v54
v_lshlrev_b32_sdwa v58,1,v58 dst_sel:DWORD dst_unused:UNUSED_PAD src0_sel:DWORD src1_sel:WORD_1
v_mfma_f32_16x16x32_bf16 v[48:51],a[212:215],a[44:47],v[16:19]
ds_read_u16 v53,v53
ds_read_u16_d16_hi v55,v55
v_fma_mix_f32 v57,v57,v9,0 op_sel_hi:[0,1,0]
v_mfma_f32_16x16x32_bf16 v[48:51],a[220:223],a[28:31],v[48:51]
ds_read_u16 v56,v56
v_fma_mix_f32 v59,v59,v9,0 op_sel:[0,1,0] op_sel_hi:[0,1,0]
v_mfma_f32_16x16x32_bf16 v[48:51],a[216:219],a[12:15],v[48:51]
s_barrier
ds_read_u16_d16_hi v58,v58
v_mfma_f32_16x16x32_bf16 v[28:31],a[216:219],a[48:51],v[16:19]
s_add_u32 m0,s57,56576
ds_read_b64 v[14:15],v7 offset:6160
v_lshlrev_b32_sdwa v40,1,v40 dst_sel:DWORD dst_unused:UNUSED_PAD src0_sel:DWORD src1_sel:WORD_1
v_mfma_f32_16x16x32_bf16 v[28:31],a[220:223],a[64:67],v[28:31]
s_add_u32 s89,s56,4030464
ds_read_u16 v40,v40
v_fma_mixlo_f16 v8,v45,s99,0
v_mfma_f32_16x16x32_bf16 v[28:31],a[212:215],a[80:83],v[28:31]
buffer_load_dwordx4 v6,s[36:39],s89 offen lds
v_lshl_add_u64 v[32:33],v[32:33],0,v[34:35]
v_lshlrev_b32_sdwa v42,1,v42 dst_sel:DWORD dst_unused:UNUSED_PAD src0_sel:DWORD src1_sel:WORD_1
v_mfma_f32_16x16x32_bf16 v[24:27],a[208:211],a[80:83],v[20:23]
s_waitcnt lgkmcnt(10)
ds_read_u16_d16_hi v42,v42
v_lshl_add_u64 v[34:35],v[60:61],0,v[62:63]
v_fma_mixhi_f16 v8,v47,s99,0
v_mfma_f32_16x16x32_bf16 v[24:27],a[204:207],a[64:67],v[24:27]
s_waitcnt lgkmcnt(5)
v_lshl_add_u64 v[60:61],v[52:53],0,v[54:55]
v_lshlrev_b32_sdwa v41,1,v41 dst_sel:DWORD dst_unused:UNUSED_PAD src0_sel:DWORD src1_sel:WORD_1
v_pk_fma_f16 v9,v8,v8,s54
v_mfma_f32_16x16x32_bf16 v[24:27],a[200:203],a[48:51],v[24:27]
s_waitcnt lgkmcnt(2)
ds_read_u16 v41,v41
v_or3_b32 v13,v14,v15,0
v_lshlrev_b32_sdwa v43,1,v43 dst_sel:DWORD dst_unused:UNUSED_PAD src0_sel:DWORD src1_sel:WORD_1
v_mfma_f32_16x16x32_bf16 v[52:55],a[200:203],a[52:55],v[20:23]
ds_read_b64 v[14:15],v7 offset:6672
v_or_b32 v62,v56,v58
v_pk_mul_f16 v9,v8,v9
v_mfma_f32_16x16x32_bf16 v[52:55],a[204:207],a[68:71],v[52:55]
ds_read_u16_d16_hi v43,v43
v_cvt_pk_bf16_f32 v63,v57,v59
v_lshlrev_b32_sdwa v36,1,v36 dst_sel:DWORD dst_unused:UNUSED_PAD src0_sel:DWORD src1_sel:WORD_1
v_mfma_f32_16x16x32_bf16 v[52:55],a[208:211],a[84:87],v[52:55]
v_exp_f16 v8,v9
v_mfma_f32_16x16x32_bf16 v[56:59],a[212:215],a[84:87],v[16:19]
ds_read_u16 v36,v36
v_lshlrev_b32_sdwa v38,1,v38 dst_sel:DWORD dst_unused:UNUSED_PAD src0_sel:DWORD src1_sel:WORD_1
v_mfma_f32_16x16x32_bf16 v[56:59],a[220:223],a[68:71],v[56:59]
ds_read_u16_d16_hi v38,v38
v_lshlrev_b32_sdwa v37,1,v37 dst_sel:DWORD dst_unused:UNUSED_PAD src0_sel:DWORD src1_sel:WORD_1
v_mfma_f32_16x16x32_bf16 v[56:59],a[216:219],a[52:55],v[56:59]
s_waitcnt lgkmcnt(3)
ds_read_u16 v37,v37
v_or3_b32 v13,v14,v15,v13
v_mfma_f32_16x16x32_bf16 v[136:139],a[236:239],v[32:35],v[136:139]
ds_read_b64 v[14:15],v7 offset:7184
v_exp_f16_sdwa v8,v9 dst_sel:WORD_1 dst_unused:UNUSED_PRESERVE src0_sel:WORD_1
v_lshlrev_b32_sdwa v39,1,v39 dst_sel:DWORD dst_unused:UNUSED_PAD src0_sel:DWORD src1_sel:WORD_1
v_mfma_f32_16x16x32_bf16 v[140:143],a[236:239],v[60:63],v[140:143]
ds_read_u16_d16_hi v39,v39
v_lshlrev_b32_sdwa v48,1,v48 dst_sel:DWORD dst_unused:UNUSED_PAD src0_sel:DWORD src1_sel:WORD_1
v_mfma_f32_16x16x32_bf16 v[68:71],a[244:247],v[60:63],v[68:71]
ds_read_u16 v48,v48
v_pk_fma_f16 v8,v8,s55,s55
v_mfma_f32_16x16x32_bf16 v[64:67],a[244:247],v[32:35],v[64:67]
v_rcp_f16 v9,v8
v_lshlrev_b32_sdwa v50,1,v50 dst_sel:DWORD dst_unused:UNUSED_PAD src0_sel:DWORD src1_sel:WORD_1
v_mfma_f32_16x16x32_bf16 v[144:147],a[228:231],v[32:35],v[144:147]
ds_read_u16_d16_hi v50,v50
v_lshlrev_b32_sdwa v49,1,v49 dst_sel:DWORD dst_unused:UNUSED_PAD src0_sel:DWORD src1_sel:WORD_1
v_mfma_f32_16x16x32_bf16 v[148:151],a[228:231],v[60:63],v[148:151]
s_waitcnt lgkmcnt(3)
ds_read_u16 v49,v49
v_or3_b32 v13,v14,v15,v13
v_mfma_f32_16x16x32_bf16 v[84:87],a[248:251],v[60:63],v[84:87]
v_lshlrev_b32_sdwa v51,1,v51 dst_sel:DWORD dst_unused:UNUSED_PAD src0_sel:DWORD src1_sel:WORD_1
v_lshlrev_b32_sdwa v44,1,v44 dst_sel:DWORD dst_unused:UNUSED_PAD src0_sel:DWORD src1_sel:WORD_1
v_mfma_f32_16x16x32_bf16 v[80:83],a[248:251],v[32:35],v[80:83]
v_rcp_f16_sdwa v9,v8 dst_sel:WORD_1 dst_unused:UNUSED_PRESERVE src0_sel:WORD_1
v_mfma_f32_16x16x32_bf16 v[152:155],a[252:255],v[32:35],v[152:155]
ds_read_u16_d16_hi v51,v51
v_lshlrev_b32_sdwa v46,1,v46 dst_sel:DWORD dst_unused:UNUSED_PAD src0_sel:DWORD src1_sel:WORD_1
v_mfma_f32_16x16x32_bf16 v[156:159],a[252:255],v[60:63],v[156:159]
ds_read_u16 v44,v44
v_fma_mix_f32 v45,v45,v9,0 op_sel_hi:[0,1,0]
v_mfma_f32_16x16x32_bf16 v[100:103],a[240:243],v[60:63],v[100:103]
ds_read_u16_d16_hi v46,v46
v_fma_mix_f32 v47,v47,v9,0 op_sel:[0,1,0] op_sel_hi:[0,1,0]
v_mfma_f32_16x16x32_bf16 v[96:99],a[240:243],v[32:35],v[96:99]
v_mfma_f32_16x16x32_bf16 v[60:63],a[216:219],a[56:59],v[16:19]
v_lshlrev_b32_sdwa v28,1,v28 dst_sel:DWORD dst_unused:UNUSED_PAD src0_sel:DWORD src1_sel:WORD_1
v_fma_mixlo_f16 v8,v53,s99,0
v_mfma_f32_16x16x32_bf16 v[60:63],a[220:223],a[72:75],v[60:63]
ds_read_u16 v28,v28
v_lshlrev_b32_sdwa v30,1,v30 dst_sel:DWORD dst_unused:UNUSED_PAD src0_sel:DWORD src1_sel:WORD_1
v_mfma_f32_16x16x32_bf16 v[60:63],a[212:215],a[88:91],v[60:63]
ds_read_u16_d16_hi v30,v30
v_lshl_add_u64 v[40:41],v[40:41],0,v[42:43]
v_mfma_f32_16x16x32_bf16 v[32:35],a[208:211],a[88:91],v[20:23]
s_waitcnt lgkmcnt(5)
v_lshl_add_u64 v[42:43],v[36:37],0,v[38:39]
v_fma_mixhi_f16 v8,v55,s99,0
v_mfma_f32_16x16x32_bf16 v[32:35],a[204:207],a[72:75],v[32:35]
s_waitcnt lgkmcnt(4)
v_lshl_add_u64 v[36:37],v[48:49],0,v[50:51]
v_lshlrev_b32_sdwa v29,1,v29 dst_sel:DWORD dst_unused:UNUSED_PAD src0_sel:DWORD src1_sel:WORD_1
v_pk_fma_f16 v9,v8,v8,s54
v_mfma_f32_16x16x32_bf16 v[32:35],a[200:203],a[56:59],v[32:35]
ds_read_u16 v29,v29
v_lshlrev_b32_sdwa v31,1,v31 dst_sel:DWORD dst_unused:UNUSED_PAD src0_sel:DWORD src1_sel:WORD_1
v_mfma_f32_16x16x32_bf16 v[48:51],a[200:203],a[60:63],v[20:23]
s_waitcnt lgkmcnt(3)
ds_read_b64_tr_b16 a[200:201],v1 offset:10256
ds_read_b64_tr_b16 a[202:203],v1 offset:11280
v_or_b32 v38,v44,v46
v_pk_mul_f16 v9,v8,v9
v_mfma_f32_16x16x32_bf16 v[48:51],a[204:207],a[76:79],v[48:51]
ds_read_b64_tr_b16 a[204:205],v2 offset:10256
ds_read_b64_tr_b16 a[206:207],v2 offset:11280
v_cvt_pk_bf16_f32 v39,v45,v47
v_lshlrev_b32_sdwa v24,1,v24 dst_sel:DWORD dst_unused:UNUSED_PAD src0_sel:DWORD src1_sel:WORD_1
v_mfma_f32_16x16x32_bf16 v[48:51],a[208:211],a[92:95],v[48:51]
ds_read_b64_tr_b16 a[208:209],v2 offset:8208
ds_read_b64_tr_b16 a[210:211],v2 offset:9232
v_exp_f16 v8,v9
v_mfma_f32_16x16x32_bf16 v[44:47],a[212:215],a[92:95],v[16:19]
ds_read_b64_tr_b16 a[212:213],v2 offset:6160
ds_read_b64_tr_b16 a[214:215],v2 offset:7184
v_lshlrev_b32_sdwa v26,1,v26 dst_sel:DWORD dst_unused:UNUSED_PAD src0_sel:DWORD src1_sel:WORD_1
v_mfma_f32_16x16x32_bf16 v[44:47],a[220:223],a[76:79],v[44:47]
ds_read_b128 v[20:23],v4 offset:800
v_lshlrev_b32_sdwa v25,1,v25 dst_sel:DWORD dst_unused:UNUSED_PAD src0_sel:DWORD src1_sel:WORD_1
v_mfma_f32_16x16x32_bf16 v[44:47],a[216:219],a[60:63],v[44:47]
v_exp_f16_sdwa v8,v9 dst_sel:WORD_1 dst_unused:UNUSED_PRESERVE src0_sel:WORD_1
v_mfma_f32_16x16x32_bf16 v[112:115],a[236:239],v[40:43],v[112:115]
ds_read_b128 v[16:19],v4 offset:768
v_lshlrev_b32_sdwa v27,1,v27 dst_sel:DWORD dst_unused:UNUSED_PAD src0_sel:DWORD src1_sel:WORD_1
v_mfma_f32_16x16x32_bf16 v[116:119],a[236:239],v[36:39],v[116:119]
ds_read_u16_d16_hi v31,v31
v_lshlrev_b32_sdwa v56,1,v56 dst_sel:DWORD dst_unused:UNUSED_PAD src0_sel:DWORD src1_sel:WORD_1
v_mfma_f32_16x16x32_bf16 v[76:79],a[244:247],v[36:39],v[76:79]
ds_read_u16 v24,v24
v_pk_fma_f16 v8,v8,s55,s55
v_mfma_f32_16x16x32_bf16 v[72:75],a[244:247],v[40:43],v[72:75]
v_rcp_f16 v9,v8
v_lshlrev_b32_sdwa v58,1,v58 dst_sel:DWORD dst_unused:UNUSED_PAD src0_sel:DWORD src1_sel:WORD_1
v_mfma_f32_16x16x32_bf16 v[120:123],a[228:231],v[40:43],v[120:123]
ds_read_u16_d16_hi v26,v26
v_lshlrev_b32_sdwa v57,1,v57 dst_sel:DWORD dst_unused:UNUSED_PAD src0_sel:DWORD src1_sel:WORD_1
v_mfma_f32_16x16x32_bf16 v[124:127],a[228:231],v[36:39],v[124:127]
ds_read_u16 v25,v25
v_lshlrev_b32_sdwa v59,1,v59 dst_sel:DWORD dst_unused:UNUSED_PAD src0_sel:DWORD src1_sel:WORD_1
v_mfma_f32_16x16x32_bf16 v[92:95],a[248:251],v[36:39],v[92:95]
ds_read_u16_d16_hi v27,v27
v_lshlrev_b32_sdwa v52,1,v52 dst_sel:DWORD dst_unused:UNUSED_PAD src0_sel:DWORD src1_sel:WORD_1
v_mfma_f32_16x16x32_bf16 v[88:91],a[248:251],v[40:43],v[88:91]
v_rcp_f16_sdwa v9,v8 dst_sel:WORD_1 dst_unused:UNUSED_PRESERVE src0_sel:WORD_1
v_mfma_f32_16x16x32_bf16 v[128:131],a[252:255],v[40:43],v[128:131]
ds_read_u16 v56,v56
ds_read_u16_d16_hi v58,v58
v_mfma_f32_16x16x32_bf16 v[132:135],a[252:255],v[36:39],v[132:135]
ds_read_u16 v57,v57
ds_read_u16_d16_hi v59,v59
v_lshlrev_b32_sdwa v54,1,v54 dst_sel:DWORD dst_unused:UNUSED_PAD src0_sel:DWORD src1_sel:WORD_1
v_mfma_f32_16x16x32_bf16 v[108:111],a[240:243],v[36:39],v[108:111]
ds_read_u16 v52,v52
v_fma_mix_f32 v53,v53,v9,0 op_sel_hi:[0,1,0]
v_mfma_f32_16x16x32_bf16 v[104:107],a[240:243],v[40:43],v[104:107]
s_waitcnt lgkmcnt(8)
ds_read_u16_d16_hi v54,v54
v_fma_mix_f32 v55,v55,v9,0 op_sel:[0,1,0] op_sel_hi:[0,1,0]
v_mfma_f32_16x16x32_bf16 v[36:39],a[196:199],a[0:3],v[16:19]
s_add_u32 m0,s57,60120
v_lshlrev_b32_sdwa v60,1,v60 dst_sel:DWORD dst_unused:UNUSED_PAD src0_sel:DWORD src1_sel:WORD_1
v_fma_mixlo_f16 v8,v49,s99,0
v_mfma_f32_16x16x32_bf16 v[36:39],a[192:195],a[16:19],v[36:39]
s_add_u32 s89,s56,4128768
ds_read_u16 v60,v60
v_lshlrev_b32_sdwa v62,1,v62 dst_sel:DWORD dst_unused:UNUSED_PAD src0_sel:DWORD src1_sel:WORD_1
v_mfma_f32_16x16x32_bf16 v[36:39],a[200:203],a[32:35],v[36:39]
buffer_load_dwordx4 v6,s[36:39],s89 offen lds
v_lshl_add_u64 v[28:29],v[28:29],0,v[30:31]
v_mfma_f32_16x16x32_bf16 v[40:43],a[204:207],a[32:35],v[20:23]
s_waitcnt lgkmcnt(7)
ds_read_u16_d16_hi v62,v62
v_lshl_add_u64 v[30:31],v[24:25],0,v[26:27]
v_fma_mixhi_f16 v8,v51,s99,0
v_mfma_f32_16x16x32_bf16 v[40:43],a[208:211],a[16:19],v[40:43]
s_waitcnt lgkmcnt(4)
v_lshl_add_u64 v[24:25],v[56:57],0,v[58:59]
v_lshlrev_b32_sdwa v61,1,v61 dst_sel:DWORD dst_unused:UNUSED_PAD src0_sel:DWORD src1_sel:WORD_1
v_pk_fma_f16 v9,v8,v8,s54
v_mfma_f32_16x16x32_bf16 v[40:43],a[212:215],a[0:3],v[40:43]
ds_read_u16 v61,v61
v_lshlrev_b32_sdwa v63,1,v63 dst_sel:DWORD dst_unused:UNUSED_PAD src0_sel:DWORD src1_sel:WORD_1
v_mfma_f32_16x16x32_bf16 v[56:59],a[212:215],a[4:7],v[20:23]
s_waitcnt lgkmcnt(3)
ds_read_u16_d16_hi v63,v63
v_or_b32 v26,v52,v54
v_pk_mul_f16 v9,v8,v9
v_mfma_f32_16x16x32_bf16 v[56:59],a[208:211],a[20:23],v[56:59]
s_waitcnt vmcnt(15)
v_cvt_pk_bf16_f32 v27,v53,v55
v_lshlrev_b32_sdwa v32,1,v32 dst_sel:DWORD dst_unused:UNUSED_PAD src0_sel:DWORD src1_sel:WORD_1
v_mfma_f32_16x16x32_bf16 v[56:59],a[204:207],a[36:39],v[56:59]
s_barrier
v_exp_f16 v8,v9
v_mfma_f32_16x16x32_bf16 v[52:55],a[200:203],a[36:39],v[16:19]
ds_read_u16 v32,v32
v_lshlrev_b32_sdwa v34,1,v34 dst_sel:DWORD dst_unused:UNUSED_PAD src0_sel:DWORD src1_sel:WORD_1
v_mfma_f32_16x16x32_bf16 v[52:55],a[192:195],a[20:23],v[52:55]
s_add_u32 m0,s58,6160
ds_read_u16_d16_hi v34,v34
v_lshlrev_b32_sdwa v33,1,v33 dst_sel:DWORD dst_unused:UNUSED_PAD src0_sel:DWORD src1_sel:WORD_1
v_mfma_f32_16x16x32_bf16 v[52:55],a[196:199],a[4:7],v[52:55]
s_add_u32 s69,s69,s59
v_exp_f16_sdwa v8,v9 dst_sel:WORD_1 dst_unused:UNUSED_PRESERVE src0_sel:WORD_1
v_mfma_f32_16x16x32_bf16 v[232:235],a[236:239],v[28:31],v[232:235]
buffer_load_dwordx4 v5,s[44:47],s69 offen offset:0 lds
v_lshlrev_b32_sdwa v35,1,v35 dst_sel:DWORD dst_unused:UNUSED_PAD src0_sel:DWORD src1_sel:WORD_1
v_mfma_f32_16x16x32_bf16 v[236:239],a[236:239],v[24:27],v[236:239]
ds_read_u16 v33,v33
v_lshlrev_b32_sdwa v44,1,v44 dst_sel:DWORD dst_unused:UNUSED_PAD src0_sel:DWORD src1_sel:WORD_1
v_mfma_f32_16x16x32_bf16 v[164:167],a[244:247],v[24:27],v[164:167]
s_add_u32 m0,s57,63656
ds_read_u16_d16_hi v35,v35
v_pk_fma_f16 v8,v8,s55,s55
v_mfma_f32_16x16x32_bf16 v[160:163],a[244:247],v[28:31],v[160:163]
s_add_u32 s89,s56,4227072
v_rcp_f16 v9,v8
v_lshlrev_b32_sdwa v46,1,v46 dst_sel:DWORD dst_unused:UNUSED_PAD src0_sel:DWORD src1_sel:WORD_1
v_mfma_f32_16x16x32_bf16 v[240:243],a[228:231],v[28:31],v[240:243]
buffer_load_dwordx4 v6,s[36:39],s89 offen lds
ds_read_u16 v44,v44
v_mfma_f32_16x16x32_bf16 v[244:247],a[228:231],v[24:27],v[244:247]
s_add_u32 m0,s58,6160
ds_read_u16_d16_hi v46,v46
v_lshlrev_b32_sdwa v45,1,v45 dst_sel:DWORD dst_unused:UNUSED_PAD src0_sel:DWORD src1_sel:WORD_1
v_mfma_f32_16x16x32_bf16 v[180:183],a[248:251],v[24:27],v[180:183]
s_add_u32 s69,s69,s53
ds_read_u16 v45,v45
v_lshlrev_b32_sdwa v47,1,v47 dst_sel:DWORD dst_unused:UNUSED_PAD src0_sel:DWORD src1_sel:WORD_1
v_mfma_f32_16x16x32_bf16 v[176:179],a[248:251],v[28:31],v[176:179]
v_rcp_f16_sdwa v9,v8 dst_sel:WORD_1 dst_unused:UNUSED_PRESERVE src0_sel:WORD_1
v_mfma_f32_16x16x32_bf16 v[248:251],a[252:255],v[28:31],v[248:251]
buffer_load_dwordx4 v5,s[44:47],s69 offen offset:1024 lds
ds_read_u16_d16_hi v47,v47
v_lshlrev_b32_sdwa v48,1,v48 dst_sel:DWORD dst_unused:UNUSED_PAD src0_sel:DWORD src1_sel:WORD_1
v_mfma_f32_16x16x32_bf16 v[252:255],a[252:255],v[24:27],v[252:255]
s_add_u32 s69,s69,s53
ds_read_u16 v48,v48
v_lshlrev_b32_sdwa v50,1,v50 dst_sel:DWORD dst_unused:UNUSED_PAD src0_sel:DWORD src1_sel:WORD_1
v_mfma_f32_16x16x32_bf16 v[196:199],a[240:243],v[24:27],v[196:199]
buffer_load_dwordx4 v5,s[44:47],s69 offen offset:2048 lds
v_fma_mix_f32 v49,v49,v9,0 op_sel_hi:[0,1,0]
v_mfma_f32_16x16x32_bf16 v[192:195],a[240:243],v[28:31],v[192:195]
ds_read_u16_d16_hi v50,v50
v_fma_mix_f32 v51,v51,v9,0 op_sel:[0,1,0] op_sel_hi:[0,1,0]
v_mfma_f32_16x16x32_bf16 v[24:27],a[196:199],a[8:11],v[16:19]
ds_read_b64_tr_b16 a[224:225],v3 offset:12320
ds_read_b64_tr_b16 a[226:227],v3 offset:13856
v_lshlrev_b32_sdwa v36,1,v36 dst_sel:DWORD dst_unused:UNUSED_PAD src0_sel:DWORD src1_sel:WORD_1
v_mfma_f32_16x16x32_bf16 v[24:27],a[192:195],a[24:27],v[24:27]
ds_read_u16 v36,v36
ds_read_b64_tr_b16 a[232:233],v3 offset:12328
v_fma_mixlo_f16 v8,v57,s99,0
v_mfma_f32_16x16x32_bf16 v[24:27],a[200:203],a[40:43],v[24:27]
s_waitcnt lgkmcnt(8)
ds_read_b64_tr_b16 a[234:235],v3 offset:13864
v_lshl_add_u64 v[60:61],v[60:61],0,v[62:63]
v_lshlrev_b32_sdwa v38,1,v38 dst_sel:DWORD dst_unused:UNUSED_PAD src0_sel:DWORD src1_sel:WORD_1
v_mfma_f32_16x16x32_bf16 v[28:31],a[204:207],a[40:43],v[20:23]
ds_read_u16_d16_hi v38,v38
v_lshl_add_u64 v[62:63],v[32:33],0,v[34:35]
v_fma_mixhi_f16 v8,v59,s99,0
v_mfma_f32_16x16x32_bf16 v[28:31],a[208:211],a[24:27],v[28:31]
s_waitcnt lgkmcnt(8)
ds_read_b64_tr_b16 a[220:221],v1 offset:12320
v_lshl_add_u64 v[32:33],v[44:45],0,v[46:47]
v_lshlrev_b32_sdwa v37,1,v37 dst_sel:DWORD dst_unused:UNUSED_PAD src0_sel:DWORD src1_sel:WORD_1
v_pk_fma_f16 v9,v8,v8,s54
v_mfma_f32_16x16x32_bf16 v[28:31],a[212:215],a[8:11],v[28:31]
ds_read_u16 v37,v37
v_lshlrev_b32_sdwa v39,1,v39 dst_sel:DWORD dst_unused:UNUSED_PAD src0_sel:DWORD src1_sel:WORD_1
v_mfma_f32_16x16x32_bf16 v[44:47],a[212:215],a[12:15],v[20:23]
s_waitcnt lgkmcnt(8)
ds_read_u16_d16_hi v39,v39
v_or_b32 v34,v48,v50
v_pk_mul_f16 v9,v8,v9
v_mfma_f32_16x16x32_bf16 v[44:47],a[208:211],a[28:31],v[44:47]
ds_read_b64_tr_b16 a[222:223],v1 offset:13344
v_cvt_pk_bf16_f32 v35,v49,v51
v_lshlrev_b32_sdwa v40,1,v40 dst_sel:DWORD dst_unused:UNUSED_PAD src0_sel:DWORD src1_sel:WORD_1
v_mfma_f32_16x16x32_bf16 v[44:47],a[204:207],a[44:47],v[44:47]
v_exp_f16 v8,v9
v_mfma_f32_16x16x32_bf16 v[48:51],a[200:203],a[44:47],v[16:19]
ds_read_u16 v40,v40
v_lshlrev_b32_sdwa v42,1,v42 dst_sel:DWORD dst_unused:UNUSED_PAD src0_sel:DWORD src1_sel:WORD_1
v_mfma_f32_16x16x32_bf16 v[48:51],a[192:195],a[28:31],v[48:51]
ds_read_u16_d16_hi v42,v42
v_lshlrev_b32_sdwa v41,1,v41 dst_sel:DWORD dst_unused:UNUSED_PAD src0_sel:DWORD src1_sel:WORD_1
v_mfma_f32_16x16x32_bf16 v[48:51],a[196:199],a[12:15],v[48:51]
v_exp_f16_sdwa v8,v9 dst_sel:WORD_1 dst_unused:UNUSED_PRESERVE src0_sel:WORD_1
v_mfma_f32_16x16x32_bf16 v[208:211],a[236:239],v[60:63],v[208:211]
ds_read_u16 v41,v41
v_lshlrev_b32_sdwa v43,1,v43 dst_sel:DWORD dst_unused:UNUSED_PAD src0_sel:DWORD src1_sel:WORD_1
v_mfma_f32_16x16x32_bf16 v[212:215],a[236:239],v[32:35],v[212:215]
ds_read_u16_d16_hi v43,v43
v_lshlrev_b32_sdwa v52,1,v52 dst_sel:DWORD dst_unused:UNUSED_PAD src0_sel:DWORD src1_sel:WORD_1
v_mfma_f32_16x16x32_bf16 v[172:175],a[244:247],v[32:35],v[172:175]
ds_read_u16 v52,v52
v_pk_fma_f16 v8,v8,s55,s55
v_mfma_f32_16x16x32_bf16 v[168:171],a[244:247],v[60:63],v[168:171]
ds_read_b64_tr_b16 a[236:237],v3 offset:12384
v_rcp_f16 v9,v8
v_lshlrev_b32_sdwa v54,1,v54 dst_sel:DWORD dst_unused:UNUSED_PAD src0_sel:DWORD src1_sel:WORD_1
v_mfma_f32_16x16x32_bf16 v[216:219],a[228:231],v[60:63],v[216:219]
ds_read_u16_d16_hi v54,v54
v_lshlrev_b32_sdwa v53,1,v53 dst_sel:DWORD dst_unused:UNUSED_PAD src0_sel:DWORD src1_sel:WORD_1
v_mfma_f32_16x16x32_bf16 v[220:223],a[228:231],v[32:35],v[220:223]
ds_read_u16 v53,v53
ds_read_b64_tr_b16 a[238:239],v3 offset:13920
v_mfma_f32_16x16x32_bf16 v[188:191],a[248:251],v[32:35],v[188:191]
ds_read_b64_tr_b16 a[244:245],v3 offset:12392
v_lshlrev_b32_sdwa v55,1,v55 dst_sel:DWORD dst_unused:UNUSED_PAD src0_sel:DWORD src1_sel:WORD_1
v_mfma_f32_16x16x32_bf16 v[184:187],a[248:251],v[60:63],v[184:187]
ds_read_u16_d16_hi v55,v55
v_rcp_f16_sdwa v9,v8 dst_sel:WORD_1 dst_unused:UNUSED_PRESERVE src0_sel:WORD_1
v_mfma_f32_16x16x32_bf16 v[224:227],a[252:255],v[60:63],v[224:227]
ds_read_b64_tr_b16 a[246:247],v3 offset:13928
ds_read_b64_tr_b16 a[228:229],v3 offset:12448
v_lshlrev_b32_sdwa v56,1,v56 dst_sel:DWORD dst_unused:UNUSED_PAD src0_sel:DWORD src1_sel:WORD_1
v_mfma_f32_16x16x32_bf16 v[228:231],a[252:255],v[32:35],v[228:231]
ds_read_u16 v56,v56
v_lshlrev_b32_sdwa v58,1,v58 dst_sel:DWORD dst_unused:UNUSED_PAD src0_sel:DWORD src1_sel:WORD_1
v_mfma_f32_16x16x32_bf16 v[204:207],a[240:243],v[32:35],v[204:207]
ds_read_u16_d16_hi v58,v58
v_fma_mix_f32 v57,v57,v9,0 op_sel_hi:[0,1,0]
v_mfma_f32_16x16x32_bf16 v[200:203],a[240:243],v[60:63],v[200:203]
ds_read_b64_tr_b16 a[230:231],v3 offset:13984
v_fma_mix_f32 v59,v59,v9,0 op_sel:[0,1,0] op_sel_hi:[0,1,0]
v_mfma_f32_16x16x32_bf16 v[32:35],a[196:199],a[48:51],v[16:19]
s_add_u32 m0,s57,67184
ds_read_b64 v[14:15],v7 offset:12320
v_lshlrev_b32_sdwa v24,1,v24 dst_sel:DWORD dst_unused:UNUSED_PAD src0_sel:DWORD src1_sel:WORD_1
v_mfma_f32_16x16x32_bf16 v[32:35],a[192:195],a[64:67],v[32:35]
s_add_u32 s89,s56,4325376
ds_read_b64_tr_b16 a[248:249],v3 offset:12456
v_fma_mixlo_f16 v8,v45,s99,0
v_mfma_f32_16x16x32_bf16 v[32:35],a[200:203],a[80:83],v[32:35]
s_waitcnt lgkmcnt(9)
buffer_load_dwordx4 v6,s[36:39],s89 offen lds
v_lshl_add_u64 v[36:37],v[36:37],0,v[38:39]
v_lshlrev_b32_sdwa v26,1,v26 dst_sel:DWORD dst_unused:UNUSED_PAD src0_sel:DWORD src1_sel:WORD_1
v_mfma_f32_16x16x32_bf16 v[60:63],a[204:207],a[80:83],v[20:23]
ds_read_b64_tr_b16 a[250:251],v3 offset:13992
v_lshl_add_u64 v[38:39],v[40:41],0,v[42:43]
v_fma_mixhi_f16 v8,v47,s99,0
v_mfma_f32_16x16x32_bf16 v[60:63],a[208:211],a[64:67],v[60:63]
s_waitcnt lgkmcnt(8)
ds_read_u16 v24,v24
v_lshl_add_u64 v[40:41],v[52:53],0,v[54:55]
v_lshlrev_b32_sdwa v25,1,v25 dst_sel:DWORD dst_unused:UNUSED_PAD src0_sel:DWORD src1_sel:WORD_1
v_pk_fma_f16 v9,v8,v8,s54
v_mfma_f32_16x16x32_bf16 v[60:63],a[212:215],a[48:51],v[60:63]
s_waitcnt lgkmcnt(3)
ds_read_u16_d16_hi v26,v26
v_or3_b32 v13,v14,v15,v13
v_lshlrev_b32_sdwa v27,1,v27 dst_sel:DWORD dst_unused:UNUSED_PAD src0_sel:DWORD src1_sel:WORD_1
v_mfma_f32_16x16x32_bf16 v[52:55],a[212:215],a[52:55],v[20:23]
ds_read_b64 v[14:15],v7 offset:12832
v_or_b32 v42,v56,v58
v_pk_mul_f16 v9,v8,v9
v_mfma_f32_16x16x32_bf16 v[52:55],a[208:211],a[68:71],v[52:55]
ds_read_u16 v25,v25
v_cvt_pk_bf16_f32 v43,v57,v59
v_lshlrev_b32_sdwa v28,1,v28 dst_sel:DWORD dst_unused:UNUSED_PAD src0_sel:DWORD src1_sel:WORD_1
v_mfma_f32_16x16x32_bf16 v[52:55],a[204:207],a[84:87],v[52:55]
v_exp_f16 v8,v9
v_mfma_f32_16x16x32_bf16 v[56:59],a[200:203],a[84:87],v[16:19]
ds_read_u16_d16_hi v27,v27
v_lshlrev_b32_sdwa v30,1,v30 dst_sel:DWORD dst_unused:UNUSED_PAD src0_sel:DWORD src1_sel:WORD_1
v_mfma_f32_16x16x32_bf16 v[56:59],a[192:195],a[68:71],v[56:59]
ds_read_u16 v28,v28
v_lshlrev_b32_sdwa v29,1,v29 dst_sel:DWORD dst_unused:UNUSED_PAD src0_sel:DWORD src1_sel:WORD_1
v_mfma_f32_16x16x32_bf16 v[56:59],a[196:199],a[52:55],v[56:59]
s_waitcnt lgkmcnt(3)
ds_read_u16_d16_hi v30,v30
v_or3_b32 v13,v14,v15,v13
v_mfma_f32_16x16x32_bf16 v[136:139],a[224:227],v[36:39],v[136:139]
ds_read_b64 v[14:15],v7 offset:13344
v_exp_f16_sdwa v8,v9 dst_sel:WORD_1 dst_unused:UNUSED_PRESERVE src0_sel:WORD_1
v_lshlrev_b32_sdwa v31,1,v31 dst_sel:DWORD dst_unused:UNUSED_PAD src0_sel:DWORD src1_sel:WORD_1
v_mfma_f32_16x16x32_bf16 v[140:143],a[224:227],v[40:43],v[140:143]
ds_read_u16 v29,v29
v_lshlrev_b32_sdwa v48,1,v48 dst_sel:DWORD dst_unused:UNUSED_PAD src0_sel:DWORD src1_sel:WORD_1
v_mfma_f32_16x16x32_bf16 v[68:71],a[232:235],v[40:43],v[68:71]
ds_read_u16_d16_hi v31,v31
v_pk_fma_f16 v8,v8,s55,s55
v_mfma_f32_16x16x32_bf16 v[64:67],a[232:235],v[36:39],v[64:67]
v_rcp_f16 v9,v8
v_lshlrev_b32_sdwa v50,1,v50 dst_sel:DWORD dst_unused:UNUSED_PAD src0_sel:DWORD src1_sel:WORD_1
v_mfma_f32_16x16x32_bf16 v[144:147],a[236:239],v[36:39],v[144:147]
ds_read_u16 v48,v48
v_lshlrev_b32_sdwa v49,1,v49 dst_sel:DWORD dst_unused:UNUSED_PAD src0_sel:DWORD src1_sel:WORD_1
v_mfma_f32_16x16x32_bf16 v[148:151],a[236:239],v[40:43],v[148:151]
s_waitcnt lgkmcnt(3)
ds_read_u16_d16_hi v50,v50
v_or3_b32 v13,v14,v15,v13
v_mfma_f32_16x16x32_bf16 v[84:87],a[244:247],v[40:43],v[84:87]
ds_read_u16 v49,v49
v_lshlrev_b32_sdwa v51,1,v51 dst_sel:DWORD dst_unused:UNUSED_PAD src0_sel:DWORD src1_sel:WORD_1
v_mfma_f32_16x16x32_bf16 v[80:83],a[244:247],v[36:39],v[80:83]
v_rcp_f16_sdwa v9,v8 dst_sel:WORD_1 dst_unused:UNUSED_PRESERVE src0_sel:WORD_1
v_mfma_f32_16x16x32_bf16 v[152:155],a[228:231],v[36:39],v[152:155]
ds_read_u16_d16_hi v51,v51
v_lshlrev_b32_sdwa v44,1,v44 dst_sel:DWORD dst_unused:UNUSED_PAD src0_sel:DWORD src1_sel:WORD_1
v_mfma_f32_16x16x32_bf16 v[156:159],a[228:231],v[40:43],v[156:159]
ds_read_u16 v44,v44
v_lshlrev_b32_sdwa v46,1,v46 dst_sel:DWORD dst_unused:UNUSED_PAD src0_sel:DWORD src1_sel:WORD_1
v_mfma_f32_16x16x32_bf16 v[100:103],a[248:251],v[40:43],v[100:103]
ds_read_u16_d16_hi v46,v46
v_fma_mix_f32 v45,v45,v9,0 op_sel_hi:[0,1,0]
v_mfma_f32_16x16x32_bf16 v[96:99],a[248:251],v[36:39],v[96:99]
ds_read_b64_tr_b16 a[216:217],v1 offset:14368
v_fma_mix_f32 v47,v47,v9,0 op_sel:[0,1,0] op_sel_hi:[0,1,0]
v_mfma_f32_16x16x32_bf16 v[40:43],a[196:199],a[56:59],v[16:19]
ds_read_b64_tr_b16 a[218:219],v1 offset:15392
v_lshlrev_b32_sdwa v32,1,v32 dst_sel:DWORD dst_unused:UNUSED_PAD src0_sel:DWORD src1_sel:WORD_1
v_mfma_f32_16x16x32_bf16 v[40:43],a[192:195],a[72:75],v[40:43]
ds_read_u16 v32,v32
v_fma_mixlo_f16 v8,v53,s99,0
v_mfma_f32_16x16x32_bf16 v[40:43],a[200:203],a[88:91],v[40:43]
v_lshl_add_u64 v[24:25],v[24:25],0,v[26:27]
v_lshlrev_b32_sdwa v34,1,v34 dst_sel:DWORD dst_unused:UNUSED_PAD src0_sel:DWORD src1_sel:WORD_1
v_mfma_f32_16x16x32_bf16 v[36:39],a[204:207],a[88:91],v[20:23]
s_waitcnt lgkmcnt(6)
ds_read_u16_d16_hi v34,v34
v_lshl_add_u64 v[26:27],v[28:29],0,v[30:31]
v_fma_mixhi_f16 v8,v55,s99,0
v_mfma_f32_16x16x32_bf16 v[36:39],a[208:211],a[72:75],v[36:39]
s_waitcnt lgkmcnt(6)
v_lshl_add_u64 v[28:29],v[48:49],0,v[50:51]
v_lshlrev_b32_sdwa v33,1,v33 dst_sel:DWORD dst_unused:UNUSED_PAD src0_sel:DWORD src1_sel:WORD_1
v_pk_fma_f16 v9,v8,v8,s54
v_mfma_f32_16x16x32_bf16 v[36:39],a[212:215],a[56:59],v[36:39]
ds_read_u16 v33,v33
v_lshlrev_b32_sdwa v35,1,v35 dst_sel:DWORD dst_unused:UNUSED_PAD src0_sel:DWORD src1_sel:WORD_1
v_mfma_f32_16x16x32_bf16 v[48:51],a[212:215],a[60:63],v[20:23]
s_waitcnt lgkmcnt(5)
ds_read_b128 v[20:23],v4 offset:928
v_or_b32 v30,v44,v46
v_pk_mul_f16 v9,v8,v9
v_mfma_f32_16x16x32_bf16 v[48:51],a[208:211],a[76:79],v[48:51]
ds_read_u16_d16_hi v35,v35
v_cvt_pk_bf16_f32 v31,v45,v47
v_lshlrev_b32_sdwa v60,1,v60 dst_sel:DWORD dst_unused:UNUSED_PAD src0_sel:DWORD src1_sel:WORD_1
v_mfma_f32_16x16x32_bf16 v[48:51],a[204:207],a[92:95],v[48:51]
ds_read_b64_tr_b16 a[212:213],v1 offset:16416
v_exp_f16 v8,v9
v_mfma_f32_16x16x32_bf16 v[44:47],a[200:203],a[92:95],v[16:19]
ds_read_b128 v[16:19],v4 offset:896
ds_read_b64_tr_b16 a[214:215],v1 offset:17440
v_lshlrev_b32_sdwa v62,1,v62 dst_sel:DWORD dst_unused:UNUSED_PAD src0_sel:DWORD src1_sel:WORD_1
v_mfma_f32_16x16x32_bf16 v[44:47],a[192:195],a[76:79],v[44:47]
ds_read_b64_tr_b16 a[208:209],v2 offset:16416
ds_read_b64_tr_b16 a[210:211],v2 offset:17440
v_lshlrev_b32_sdwa v61,1,v61 dst_sel:DWORD dst_unused:UNUSED_PAD src0_sel:DWORD src1_sel:WORD_1
v_mfma_f32_16x16x32_bf16 v[44:47],a[196:199],a[60:63],v[44:47]
ds_read_u16 v60,v60
v_exp_f16_sdwa v8,v9 dst_sel:WORD_1 dst_unused:UNUSED_PRESERVE src0_sel:WORD_1
v_mfma_f32_16x16x32_bf16 v[112:115],a[224:227],v[24:27],v[112:115]
ds_read_u16_d16_hi v62,v62
v_lshlrev_b32_sdwa v63,1,v63 dst_sel:DWORD dst_unused:UNUSED_PAD src0_sel:DWORD src1_sel:WORD_1
v_mfma_f32_16x16x32_bf16 v[116:119],a[224:227],v[28:31],v[116:119]
ds_read_u16 v61,v61
v_lshlrev_b32_sdwa v56,1,v56 dst_sel:DWORD dst_unused:UNUSED_PAD src0_sel:DWORD src1_sel:WORD_1
v_mfma_f32_16x16x32_bf16 v[76:79],a[232:235],v[28:31],v[76:79]
ds_read_u16_d16_hi v63,v63
v_pk_fma_f16 v8,v8,s55,s55
v_mfma_f32_16x16x32_bf16 v[72:75],a[232:235],v[24:27],v[72:75]
v_rcp_f16 v9,v8
v_lshlrev_b32_sdwa v58,1,v58 dst_sel:DWORD dst_unused:UNUSED_PAD src0_sel:DWORD src1_sel:WORD_1
v_mfma_f32_16x16x32_bf16 v[120:123],a[236:239],v[24:27],v[120:123]
ds_read_b64_tr_b16 a[204:205],v2 offset:14368
v_lshlrev_b32_sdwa v57,1,v57 dst_sel:DWORD dst_unused:UNUSED_PAD src0_sel:DWORD src1_sel:WORD_1
v_mfma_f32_16x16x32_bf16 v[124:127],a[236:239],v[28:31],v[124:127]
ds_read_b64_tr_b16 a[206:207],v2 offset:15392
v_lshlrev_b32_sdwa v59,1,v59 dst_sel:DWORD dst_unused:UNUSED_PAD src0_sel:DWORD src1_sel:WORD_1
v_mfma_f32_16x16x32_bf16 v[92:95],a[244:247],v[28:31],v[92:95]
ds_read_u16 v56,v56
v_lshlrev_b32_sdwa v52,1,v52 dst_sel:DWORD dst_unused:UNUSED_PAD src0_sel:DWORD src1_sel:WORD_1
v_mfma_f32_16x16x32_bf16 v[88:91],a[244:247],v[24:27],v[88:91]
ds_read_u16_d16_hi v58,v58
v_rcp_f16_sdwa v9,v8 dst_sel:WORD_1 dst_unused:UNUSED_PRESERVE src0_sel:WORD_1
v_mfma_f32_16x16x32_bf16 v[128:131],a[228:231],v[24:27],v[128:131]
ds_read_u16 v57,v57
ds_read_u16_d16_hi v59,v59
v_mfma_f32_16x16x32_bf16 v[132:135],a[228:231],v[28:31],v[132:135]
ds_read_b64_tr_b16 a[200:201],v2 offset:12320
ds_read_b64_tr_b16 a[202:203],v2 offset:13344
v_lshlrev_b32_sdwa v54,1,v54 dst_sel:DWORD dst_unused:UNUSED_PAD src0_sel:DWORD src1_sel:WORD_1
v_mfma_f32_16x16x32_bf16 v[108:111],a[248:251],v[28:31],v[108:111]
TP3326_%=:
s_cbranch_execz TP5478_%=
ds_read_u16 v52,v52
v_fma_mix_f32 v53,v53,v9,0 op_sel_hi:[0,1,0]
v_mfma_f32_16x16x32_bf16 v[104:107],a[248:251],v[24:27],v[104:107]
s_waitcnt lgkmcnt(9)
ds_read_u16_d16_hi v54,v54
v_fma_mix_f32 v55,v55,v9,0 op_sel:[0,1,0] op_sel_hi:[0,1,0]
v_mfma_f32_16x16x32_bf16 v[28:31],a[220:223],a[0:3],v[16:19]
s_add_u32 m0,s57,70720
v_lshlrev_b32_sdwa v40,1,v40 dst_sel:DWORD dst_unused:UNUSED_PAD src0_sel:DWORD src1_sel:WORD_1
v_fma_mixlo_f16 v8,v49,s99,0
v_mfma_f32_16x16x32_bf16 v[28:31],a[216:219],a[16:19],v[28:31]
s_add_u32 s89,s56,4423680
ds_read_u16 v40,v40
v_lshlrev_b32_sdwa v42,1,v42 dst_sel:DWORD dst_unused:UNUSED_PAD src0_sel:DWORD src1_sel:WORD_1
v_mfma_f32_16x16x32_bf16 v[28:31],a[212:215],a[32:35],v[28:31]
buffer_load_dwordx4 v6,s[36:39],s89 offen lds
v_lshl_add_u64 v[32:33],v[32:33],0,v[34:35]
v_mfma_f32_16x16x32_bf16 v[24:27],a[208:211],a[32:35],v[20:23]
s_waitcnt lgkmcnt(8)
ds_read_u16_d16_hi v42,v42
v_lshl_add_u64 v[34:35],v[60:61],0,v[62:63]
v_fma_mixhi_f16 v8,v51,s99,0
v_mfma_f32_16x16x32_bf16 v[24:27],a[204:207],a[16:19],v[24:27]
s_waitcnt lgkmcnt(4)
v_lshl_add_u64 v[60:61],v[56:57],0,v[58:59]
v_lshlrev_b32_sdwa v41,1,v41 dst_sel:DWORD dst_unused:UNUSED_PAD src0_sel:DWORD src1_sel:WORD_1
v_pk_fma_f16 v9,v8,v8,s54
v_mfma_f32_16x16x32_bf16 v[24:27],a[200:203],a[0:3],v[24:27]
ds_read_u16 v41,v41
v_lshlrev_b32_sdwa v43,1,v43 dst_sel:DWORD dst_unused:UNUSED_PAD src0_sel:DWORD src1_sel:WORD_1
v_mfma_f32_16x16x32_bf16 v[56:59],a[200:203],a[4:7],v[20:23]
s_waitcnt lgkmcnt(3)
ds_read_u16_d16_hi v43,v43
v_or_b32 v62,v52,v54
v_pk_mul_f16 v9,v8,v9
v_mfma_f32_16x16x32_bf16 v[56:59],a[204:207],a[20:23],v[56:59]
s_waitcnt vmcnt(15)
v_cvt_pk_bf16_f32 v63,v53,v55
v_lshlrev_b32_sdwa v36,1,v36 dst_sel:DWORD dst_unused:UNUSED_PAD src0_sel:DWORD src1_sel:WORD_1
v_mfma_f32_16x16x32_bf16 v[56:59],a[208:211],a[36:39],v[56:59]
s_barrier
v_exp_f16 v8,v9
v_mfma_f32_16x16x32_bf16 v[52:55],a[212:215],a[36:39],v[16:19]
s_add_u32 m0,s57,74264
ds_read_u16 v36,v36
v_lshlrev_b32_sdwa v38,1,v38 dst_sel:DWORD dst_unused:UNUSED_PAD src0_sel:DWORD src1_sel:WORD_1
v_mfma_f32_16x16x32_bf16 v[52:55],a[216:219],a[20:23],v[52:55]
s_add_u32 s89,s56,4521984
ds_read_u16_d16_hi v38,v38
v_lshlrev_b32_sdwa v37,1,v37 dst_sel:DWORD dst_unused:UNUSED_PAD src0_sel:DWORD src1_sel:WORD_1
v_mfma_f32_16x16x32_bf16 v[52:55],a[220:223],a[4:7],v[52:55]
v_exp_f16_sdwa v8,v9 dst_sel:WORD_1 dst_unused:UNUSED_PRESERVE src0_sel:WORD_1
v_mfma_f32_16x16x32_bf16 v[232:235],a[224:227],v[32:35],v[232:235]
buffer_load_dwordx4 v6,s[36:39],s89 offen lds
v_lshlrev_b32_sdwa v39,1,v39 dst_sel:DWORD dst_unused:UNUSED_PAD src0_sel:DWORD src1_sel:WORD_1
v_mfma_f32_16x16x32_bf16 v[236:239],a[224:227],v[60:63],v[236:239]
s_add_u32 m0,s58,12320
ds_read_u16 v37,v37
v_lshlrev_b32_sdwa v44,1,v44 dst_sel:DWORD dst_unused:UNUSED_PAD src0_sel:DWORD src1_sel:WORD_1
v_mfma_f32_16x16x32_bf16 v[164:167],a[232:235],v[60:63],v[164:167]
s_add_u32 s69,s69,s59
ds_read_u16_d16_hi v39,v39
v_pk_fma_f16 v8,v8,s55,s55
v_mfma_f32_16x16x32_bf16 v[160:163],a[232:235],v[32:35],v[160:163]
v_rcp_f16 v9,v8
v_lshlrev_b32_sdwa v46,1,v46 dst_sel:DWORD dst_unused:UNUSED_PAD src0_sel:DWORD src1_sel:WORD_1
v_mfma_f32_16x16x32_bf16 v[240:243],a[236:239],v[32:35],v[240:243]
buffer_load_dwordx4 v5,s[44:47],s69 offen offset:0 lds
ds_read_u16 v44,v44
v_mfma_f32_16x16x32_bf16 v[244:247],a[236:239],v[60:63],v[244:247]
s_add_u32 s69,s69,s53
ds_read_u16_d16_hi v46,v46
v_lshlrev_b32_sdwa v45,1,v45 dst_sel:DWORD dst_unused:UNUSED_PAD src0_sel:DWORD src1_sel:WORD_1
v_mfma_f32_16x16x32_bf16 v[180:183],a[244:247],v[60:63],v[180:183]
buffer_load_dwordx4 v5,s[44:47],s69 offen offset:1024 lds
v_lshlrev_b32_sdwa v47,1,v47 dst_sel:DWORD dst_unused:UNUSED_PAD src0_sel:DWORD src1_sel:WORD_1
v_mfma_f32_16x16x32_bf16 v[176:179],a[244:247],v[32:35],v[176:179]
ds_read_u16 v45,v45
v_rcp_f16_sdwa v9,v8 dst_sel:WORD_1 dst_unused:UNUSED_PRESERVE src0_sel:WORD_1
v_mfma_f32_16x16x32_bf16 v[248:251],a[228:231],v[32:35],v[248:251]
ds_read_u16_d16_hi v47,v47
v_lshlrev_b32_sdwa v48,1,v48 dst_sel:DWORD dst_unused:UNUSED_PAD src0_sel:DWORD src1_sel:WORD_1
v_mfma_f32_16x16x32_bf16 v[252:255],a[228:231],v[60:63],v[252:255]
s_add_u32 s69,s69,s53
ds_read_u16 v48,v48
v_lshlrev_b32_sdwa v50,1,v50 dst_sel:DWORD dst_unused:UNUSED_PAD src0_sel:DWORD src1_sel:WORD_1
v_mfma_f32_16x16x32_bf16 v[196:199],a[248:251],v[60:63],v[196:199]
buffer_load_dwordx4 v5,s[44:47],s69 offen offset:2048 lds
v_fma_mix_f32 v49,v49,v9,0 op_sel_hi:[0,1,0]
v_mfma_f32_16x16x32_bf16 v[192:195],a[248:251],v[32:35],v[192:195]
ds_read_u16_d16_hi v50,v50
v_fma_mix_f32 v51,v51,v9,0 op_sel:[0,1,0] op_sel_hi:[0,1,0]
v_mfma_f32_16x16x32_bf16 v[60:63],a[220:223],a[8:11],v[16:19]
ds_read_b64_tr_b16 a[252:253],v3 offset:18480
ds_read_b64_tr_b16 a[254:255],v3 offset:20016
v_lshlrev_b32_sdwa v28,1,v28 dst_sel:DWORD dst_unused:UNUSED_PAD src0_sel:DWORD src1_sel:WORD_1
v_mfma_f32_16x16x32_bf16 v[60:63],a[216:219],a[24:27],v[60:63]
ds_read_u16 v28,v28
ds_read_b64_tr_b16 a[240:241],v3 offset:18488
v_fma_mixlo_f16 v8,v57,s99,0
v_mfma_f32_16x16x32_bf16 v[60:63],a[212:215],a[40:43],v[60:63]
s_waitcnt lgkmcnt(8)
ds_read_b64_tr_b16 a[242:243],v3 offset:20024
v_lshl_add_u64 v[40:41],v[40:41],0,v[42:43]
v_lshlrev_b32_sdwa v30,1,v30 dst_sel:DWORD dst_unused:UNUSED_PAD src0_sel:DWORD src1_sel:WORD_1
v_mfma_f32_16x16x32_bf16 v[32:35],a[208:211],a[40:43],v[20:23]
ds_read_u16_d16_hi v30,v30
v_lshl_add_u64 v[42:43],v[36:37],0,v[38:39]
v_fma_mixhi_f16 v8,v59,s99,0
v_mfma_f32_16x16x32_bf16 v[32:35],a[204:207],a[24:27],v[32:35]
s_waitcnt lgkmcnt(8)
ds_read_b64_tr_b16 a[192:193],v1 offset:18480
v_lshl_add_u64 v[36:37],v[44:45],0,v[46:47]
v_lshlrev_b32_sdwa v29,1,v29 dst_sel:DWORD dst_unused:UNUSED_PAD src0_sel:DWORD src1_sel:WORD_1
v_pk_fma_f16 v9,v8,v8,s54
v_mfma_f32_16x16x32_bf16 v[32:35],a[200:203],a[8:11],v[32:35]
ds_read_u16 v29,v29
v_lshlrev_b32_sdwa v31,1,v31 dst_sel:DWORD dst_unused:UNUSED_PAD src0_sel:DWORD src1_sel:WORD_1
v_mfma_f32_16x16x32_bf16 v[44:47],a[200:203],a[12:15],v[20:23]
s_waitcnt lgkmcnt(8)
ds_read_u16_d16_hi v31,v31
v_or_b32 v38,v48,v50
v_pk_mul_f16 v9,v8,v9
v_mfma_f32_16x16x32_bf16 v[44:47],a[204:207],a[28:31],v[44:47]
ds_read_b64_tr_b16 a[194:195],v1 offset:19504
v_cvt_pk_bf16_f32 v39,v49,v51
v_lshlrev_b32_sdwa v24,1,v24 dst_sel:DWORD dst_unused:UNUSED_PAD src0_sel:DWORD src1_sel:WORD_1
v_mfma_f32_16x16x32_bf16 v[44:47],a[208:211],a[44:47],v[44:47]
v_exp_f16 v8,v9
v_mfma_f32_16x16x32_bf16 v[48:51],a[212:215],a[44:47],v[16:19]
ds_read_u16 v24,v24
v_lshlrev_b32_sdwa v26,1,v26 dst_sel:DWORD dst_unused:UNUSED_PAD src0_sel:DWORD src1_sel:WORD_1
v_mfma_f32_16x16x32_bf16 v[48:51],a[216:219],a[28:31],v[48:51]
ds_read_u16_d16_hi v26,v26
v_lshlrev_b32_sdwa v25,1,v25 dst_sel:DWORD dst_unused:UNUSED_PAD src0_sel:DWORD src1_sel:WORD_1
v_mfma_f32_16x16x32_bf16 v[48:51],a[220:223],a[12:15],v[48:51]
v_exp_f16_sdwa v8,v9 dst_sel:WORD_1 dst_unused:UNUSED_PRESERVE src0_sel:WORD_1
v_mfma_f32_16x16x32_bf16 v[208:211],a[224:227],v[40:43],v[208:211]
ds_read_u16 v25,v25
v_lshlrev_b32_sdwa v27,1,v27 dst_sel:DWORD dst_unused:UNUSED_PAD src0_sel:DWORD src1_sel:WORD_1
v_mfma_f32_16x16x32_bf16 v[212:215],a[224:227],v[36:39],v[212:215]
ds_read_u16_d16_hi v27,v27
v_lshlrev_b32_sdwa v52,1,v52 dst_sel:DWORD dst_unused:UNUSED_PAD src0_sel:DWORD src1_sel:WORD_1
v_mfma_f32_16x16x32_bf16 v[172:175],a[232:235],v[36:39],v[172:175]
ds_read_u16 v52,v52
v_pk_fma_f16 v8,v8,s55,s55
v_mfma_f32_16x16x32_bf16 v[168:171],a[232:235],v[40:43],v[168:171]
ds_read_b64_tr_b16 a[224:225],v3 offset:18544
v_rcp_f16 v9,v8
v_lshlrev_b32_sdwa v54,1,v54 dst_sel:DWORD dst_unused:UNUSED_PAD src0_sel:DWORD src1_sel:WORD_1
v_mfma_f32_16x16x32_bf16 v[216:219],a[236:239],v[40:43],v[216:219]
ds_read_u16_d16_hi v54,v54
v_lshlrev_b32_sdwa v53,1,v53 dst_sel:DWORD dst_unused:UNUSED_PAD src0_sel:DWORD src1_sel:WORD_1
v_mfma_f32_16x16x32_bf16 v[220:223],a[236:239],v[36:39],v[220:223]
ds_read_u16 v53,v53
ds_read_b64_tr_b16 a[226:227],v3 offset:20080
v_mfma_f32_16x16x32_bf16 v[188:191],a[244:247],v[36:39],v[188:191]
ds_read_b64_tr_b16 a[232:233],v3 offset:18552
v_lshlrev_b32_sdwa v55,1,v55 dst_sel:DWORD dst_unused:UNUSED_PAD src0_sel:DWORD src1_sel:WORD_1
v_mfma_f32_16x16x32_bf16 v[184:187],a[244:247],v[40:43],v[184:187]
ds_read_u16_d16_hi v55,v55
v_rcp_f16_sdwa v9,v8 dst_sel:WORD_1 dst_unused:UNUSED_PRESERVE src0_sel:WORD_1
v_mfma_f32_16x16x32_bf16 v[224:227],a[228:231],v[40:43],v[224:227]
ds_read_b64_tr_b16 a[234:235],v3 offset:20088
ds_read_b64_tr_b16 a[236:237],v3 offset:18608
v_lshlrev_b32_sdwa v56,1,v56 dst_sel:DWORD dst_unused:UNUSED_PAD src0_sel:DWORD src1_sel:WORD_1
v_mfma_f32_16x16x32_bf16 v[228:231],a[228:231],v[36:39],v[228:231]
ds_read_u16 v56,v56
v_lshlrev_b32_sdwa v58,1,v58 dst_sel:DWORD dst_unused:UNUSED_PAD src0_sel:DWORD src1_sel:WORD_1
v_mfma_f32_16x16x32_bf16 v[204:207],a[248:251],v[36:39],v[204:207]
ds_read_u16_d16_hi v58,v58
v_fma_mix_f32 v57,v57,v9,0 op_sel_hi:[0,1,0]
v_mfma_f32_16x16x32_bf16 v[200:203],a[248:251],v[40:43],v[200:203]
ds_read_b64_tr_b16 a[238:239],v3 offset:20144
v_fma_mix_f32 v59,v59,v9,0 op_sel:[0,1,0] op_sel_hi:[0,1,0]
v_mfma_f32_16x16x32_bf16 v[36:39],a[220:223],a[48:51],v[16:19]
s_add_u32 m0,s57,77800
ds_read_b64 v[14:15],v7 offset:18480
v_lshlrev_b32_sdwa v60,1,v60 dst_sel:DWORD dst_unused:UNUSED_PAD src0_sel:DWORD src1_sel:WORD_1
v_mfma_f32_16x16x32_bf16 v[36:39],a[216:219],a[64:67],v[36:39]
s_add_u32 s89,s56,4620288
ds_read_b64_tr_b16 a[244:245],v3 offset:18616
v_fma_mixlo_f16 v8,v45,s99,0
v_mfma_f32_16x16x32_bf16 v[36:39],a[212:215],a[80:83],v[36:39]
s_waitcnt lgkmcnt(9)
buffer_load_dwordx4 v6,s[36:39],s89 offen lds
v_lshl_add_u64 v[28:29],v[28:29],0,v[30:31]
v_lshlrev_b32_sdwa v62,1,v62 dst_sel:DWORD dst_unused:UNUSED_PAD src0_sel:DWORD src1_sel:WORD_1
v_mfma_f32_16x16x32_bf16 v[40:43],a[208:211],a[80:83],v[20:23]
ds_read_b64_tr_b16 a[246:247],v3 offset:20152
v_lshl_add_u64 v[30:31],v[24:25],0,v[26:27]
v_fma_mixhi_f16 v8,v47,s99,0
v_mfma_f32_16x16x32_bf16 v[40:43],a[204:207],a[64:67],v[40:43]
s_waitcnt lgkmcnt(8)
ds_read_u16 v60,v60
v_lshl_add_u64 v[24:25],v[52:53],0,v[54:55]
v_lshlrev_b32_sdwa v61,1,v61 dst_sel:DWORD dst_unused:UNUSED_PAD src0_sel:DWORD src1_sel:WORD_1
v_pk_fma_f16 v9,v8,v8,s54
v_mfma_f32_16x16x32_bf16 v[40:43],a[200:203],a[48:51],v[40:43]
s_waitcnt lgkmcnt(3)
ds_read_u16_d16_hi v62,v62
v_or3_b32 v13,v14,v15,v13
v_lshlrev_b32_sdwa v63,1,v63 dst_sel:DWORD dst_unused:UNUSED_PAD src0_sel:DWORD src1_sel:WORD_1
v_mfma_f32_16x16x32_bf16 v[52:55],a[200:203],a[52:55],v[20:23]
ds_read_b64 v[14:15],v7 offset:18992
v_or_b32 v26,v56,v58
v_pk_mul_f16 v9,v8,v9
v_mfma_f32_16x16x32_bf16 v[52:55],a[204:207],a[68:71],v[52:55]
ds_read_u16 v61,v61
v_cvt_pk_bf16_f32 v27,v57,v59
v_lshlrev_b32_sdwa v32,1,v32 dst_sel:DWORD dst_unused:UNUSED_PAD src0_sel:DWORD src1_sel:WORD_1
v_mfma_f32_16x16x32_bf16 v[52:55],a[208:211],a[84:87],v[52:55]
v_exp_f16 v8,v9
v_mfma_f32_16x16x32_bf16 v[56:59],a[212:215],a[84:87],v[16:19]
ds_read_u16_d16_hi v63,v63
v_lshlrev_b32_sdwa v34,1,v34 dst_sel:DWORD dst_unused:UNUSED_PAD src0_sel:DWORD src1_sel:WORD_1
v_mfma_f32_16x16x32_bf16 v[56:59],a[216:219],a[68:71],v[56:59]
ds_read_u16 v32,v32
v_lshlrev_b32_sdwa v33,1,v33 dst_sel:DWORD dst_unused:UNUSED_PAD src0_sel:DWORD src1_sel:WORD_1
v_mfma_f32_16x16x32_bf16 v[56:59],a[220:223],a[52:55],v[56:59]
s_waitcnt lgkmcnt(3)
ds_read_u16_d16_hi v34,v34
v_or3_b32 v13,v14,v15,v13
v_mfma_f32_16x16x32_bf16 v[136:139],a[252:255],v[28:31],v[136:139]
ds_read_b64 v[14:15],v7 offset:19504
v_exp_f16_sdwa v8,v9 dst_sel:WORD_1 dst_unused:UNUSED_PRESERVE src0_sel:WORD_1
v_lshlrev_b32_sdwa v35,1,v35 dst_sel:DWORD dst_unused:UNUSED_PAD src0_sel:DWORD src1_sel:WORD_1
v_mfma_f32_16x16x32_bf16 v[140:143],a[252:255],v[24:27],v[140:143]
ds_read_u16 v33,v33
v_lshlrev_b32_sdwa v48,1,v48 dst_sel:DWORD dst_unused:UNUSED_PAD src0_sel:DWORD src1_sel:WORD_1
v_mfma_f32_16x16x32_bf16 v[68:71],a[240:243],v[24:27],v[68:71]
ds_read_u16_d16_hi v35,v35
v_pk_fma_f16 v8,v8,s55,s55
v_mfma_f32_16x16x32_bf16 v[64:67],a[240:243],v[28:31],v[64:67]
v_rcp_f16 v9,v8
v_lshlrev_b32_sdwa v50,1,v50 dst_sel:DWORD dst_unused:UNUSED_PAD src0_sel:DWORD src1_sel:WORD_1
v_mfma_f32_16x16x32_bf16 v[144:147],a[224:227],v[28:31],v[144:147]
ds_read_u16 v48,v48
v_lshlrev_b32_sdwa v49,1,v49 dst_sel:DWORD dst_unused:UNUSED_PAD src0_sel:DWORD src1_sel:WORD_1
v_mfma_f32_16x16x32_bf16 v[148:151],a[224:227],v[24:27],v[148:151]
s_waitcnt lgkmcnt(3)
ds_read_u16_d16_hi v50,v50
v_or3_b32 v13,v14,v15,v13
v_mfma_f32_16x16x32_bf16 v[84:87],a[232:235],v[24:27],v[84:87]
ds_read_u16 v49,v49
v_lshlrev_b32_sdwa v51,1,v51 dst_sel:DWORD dst_unused:UNUSED_PAD src0_sel:DWORD src1_sel:WORD_1
v_mfma_f32_16x16x32_bf16 v[80:83],a[232:235],v[28:31],v[80:83]
v_rcp_f16_sdwa v9,v8 dst_sel:WORD_1 dst_unused:UNUSED_PRESERVE src0_sel:WORD_1
v_mfma_f32_16x16x32_bf16 v[152:155],a[236:239],v[28:31],v[152:155]
ds_read_u16_d16_hi v51,v51
v_lshlrev_b32_sdwa v44,1,v44 dst_sel:DWORD dst_unused:UNUSED_PAD src0_sel:DWORD src1_sel:WORD_1
v_mfma_f32_16x16x32_bf16 v[156:159],a[236:239],v[24:27],v[156:159]
ds_read_u16 v44,v44
v_lshlrev_b32_sdwa v46,1,v46 dst_sel:DWORD dst_unused:UNUSED_PAD src0_sel:DWORD src1_sel:WORD_1
v_mfma_f32_16x16x32_bf16 v[100:103],a[244:247],v[24:27],v[100:103]
ds_read_u16_d16_hi v46,v46
v_fma_mix_f32 v45,v45,v9,0 op_sel_hi:[0,1,0]
v_mfma_f32_16x16x32_bf16 v[96:99],a[244:247],v[28:31],v[96:99]
s_add_u32 m0,s57,81328
ds_read_b64_tr_b16 a[196:197],v1 offset:20528
v_fma_mix_f32 v47,v47,v9,0 op_sel:[0,1,0] op_sel_hi:[0,1,0]
v_mfma_f32_16x16x32_bf16 v[24:27],a[220:223],a[56:59],v[16:19]
s_add_u32 s89,s56,4718592
ds_read_b64_tr_b16 a[198:199],v1 offset:21552
v_lshlrev_b32_sdwa v36,1,v36 dst_sel:DWORD dst_unused:UNUSED_PAD src0_sel:DWORD src1_sel:WORD_1
v_mfma_f32_16x16x32_bf16 v[24:27],a[216:219],a[72:75],v[24:27]
buffer_load_dwordx4 v6,s[36:39],s89 offen lds
v_fma_mixlo_f16 v8,v53,s99,0
v_mfma_f32_16x16x32_bf16 v[24:27],a[212:215],a[88:91],v[24:27]
ds_read_u16 v36,v36
v_lshl_add_u64 v[60:61],v[60:61],0,v[62:63]
v_lshlrev_b32_sdwa v38,1,v38 dst_sel:DWORD dst_unused:UNUSED_PAD src0_sel:DWORD src1_sel:WORD_1
v_mfma_f32_16x16x32_bf16 v[28:31],a[208:211],a[88:91],v[20:23]
s_waitcnt lgkmcnt(6)
ds_read_u16_d16_hi v38,v38
v_lshl_add_u64 v[62:63],v[32:33],0,v[34:35]
v_fma_mixhi_f16 v8,v55,s99,0
v_mfma_f32_16x16x32_bf16 v[28:31],a[204:207],a[72:75],v[28:31]
s_waitcnt lgkmcnt(6)
v_lshl_add_u64 v[32:33],v[48:49],0,v[50:51]
v_lshlrev_b32_sdwa v37,1,v37 dst_sel:DWORD dst_unused:UNUSED_PAD src0_sel:DWORD src1_sel:WORD_1
v_pk_fma_f16 v9,v8,v8,s54
v_mfma_f32_16x16x32_bf16 v[28:31],a[200:203],a[56:59],v[28:31]
ds_read_u16 v37,v37
v_lshlrev_b32_sdwa v39,1,v39 dst_sel:DWORD dst_unused:UNUSED_PAD src0_sel:DWORD src1_sel:WORD_1
v_mfma_f32_16x16x32_bf16 v[48:51],a[200:203],a[60:63],v[20:23]
s_waitcnt lgkmcnt(5)
ds_read_b128 v[20:23],v4 offset:1056
v_or_b32 v34,v44,v46
v_pk_mul_f16 v9,v8,v9
v_mfma_f32_16x16x32_bf16 v[48:51],a[204:207],a[76:79],v[48:51]
ds_read_u16_d16_hi v39,v39
v_cvt_pk_bf16_f32 v35,v45,v47
v_lshlrev_b32_sdwa v40,1,v40 dst_sel:DWORD dst_unused:UNUSED_PAD src0_sel:DWORD src1_sel:WORD_1
v_mfma_f32_16x16x32_bf16 v[48:51],a[208:211],a[92:95],v[48:51]
ds_read_b64_tr_b16 a[200:201],v1 offset:22576
v_exp_f16 v8,v9
v_mfma_f32_16x16x32_bf16 v[44:47],a[212:215],a[92:95],v[16:19]
ds_read_b128 v[16:19],v4 offset:1024
ds_read_b64_tr_b16 a[202:203],v1 offset:23600
v_lshlrev_b32_sdwa v42,1,v42 dst_sel:DWORD dst_unused:UNUSED_PAD src0_sel:DWORD src1_sel:WORD_1
v_mfma_f32_16x16x32_bf16 v[44:47],a[216:219],a[76:79],v[44:47]
ds_read_b64_tr_b16 a[204:205],v2 offset:22576
ds_read_b64_tr_b16 a[206:207],v2 offset:23600
v_lshlrev_b32_sdwa v41,1,v41 dst_sel:DWORD dst_unused:UNUSED_PAD src0_sel:DWORD src1_sel:WORD_1
v_mfma_f32_16x16x32_bf16 v[44:47],a[220:223],a[60:63],v[44:47]
ds_read_u16 v40,v40
v_exp_f16_sdwa v8,v9 dst_sel:WORD_1 dst_unused:UNUSED_PRESERVE src0_sel:WORD_1
v_mfma_f32_16x16x32_bf16 v[112:115],a[252:255],v[60:63],v[112:115]
ds_read_u16_d16_hi v42,v42
v_lshlrev_b32_sdwa v43,1,v43 dst_sel:DWORD dst_unused:UNUSED_PAD src0_sel:DWORD src1_sel:WORD_1
v_mfma_f32_16x16x32_bf16 v[116:119],a[252:255],v[32:35],v[116:119]
ds_read_u16 v41,v41
v_lshlrev_b32_sdwa v56,1,v56 dst_sel:DWORD dst_unused:UNUSED_PAD src0_sel:DWORD src1_sel:WORD_1
v_mfma_f32_16x16x32_bf16 v[76:79],a[240:243],v[32:35],v[76:79]
ds_read_u16_d16_hi v43,v43
v_pk_fma_f16 v8,v8,s55,s55
v_mfma_f32_16x16x32_bf16 v[72:75],a[240:243],v[60:63],v[72:75]
v_rcp_f16 v9,v8
v_lshlrev_b32_sdwa v58,1,v58 dst_sel:DWORD dst_unused:UNUSED_PAD src0_sel:DWORD src1_sel:WORD_1
v_mfma_f32_16x16x32_bf16 v[120:123],a[224:227],v[60:63],v[120:123]
ds_read_b64_tr_b16 a[208:209],v2 offset:20528
v_lshlrev_b32_sdwa v57,1,v57 dst_sel:DWORD dst_unused:UNUSED_PAD src0_sel:DWORD src1_sel:WORD_1
v_mfma_f32_16x16x32_bf16 v[124:127],a[224:227],v[32:35],v[124:127]
ds_read_b64_tr_b16 a[210:211],v2 offset:21552
v_lshlrev_b32_sdwa v59,1,v59 dst_sel:DWORD dst_unused:UNUSED_PAD src0_sel:DWORD src1_sel:WORD_1
v_mfma_f32_16x16x32_bf16 v[92:95],a[232:235],v[32:35],v[92:95]
ds_read_u16 v56,v56
v_lshlrev_b32_sdwa v52,1,v52 dst_sel:DWORD dst_unused:UNUSED_PAD src0_sel:DWORD src1_sel:WORD_1
v_mfma_f32_16x16x32_bf16 v[88:91],a[232:235],v[60:63],v[88:91]
ds_read_u16_d16_hi v58,v58
v_rcp_f16_sdwa v9,v8 dst_sel:WORD_1 dst_unused:UNUSED_PRESERVE src0_sel:WORD_1
v_mfma_f32_16x16x32_bf16 v[128:131],a[236:239],v[60:63],v[128:131]
ds_read_u16 v57,v57
ds_read_u16_d16_hi v59,v59
v_mfma_f32_16x16x32_bf16 v[132:135],a[236:239],v[32:35],v[132:135]
ds_read_b64_tr_b16 a[212:213],v2 offset:18480
ds_read_b64_tr_b16 a[214:215],v2 offset:19504
v_lshlrev_b32_sdwa v54,1,v54 dst_sel:DWORD dst_unused:UNUSED_PAD src0_sel:DWORD src1_sel:WORD_1
v_mfma_f32_16x16x32_bf16 v[108:111],a[244:247],v[32:35],v[108:111]
ds_read_u16 v52,v52
v_fma_mix_f32 v53,v53,v9,0 op_sel_hi:[0,1,0]
v_mfma_f32_16x16x32_bf16 v[104:107],a[244:247],v[60:63],v[104:107]
s_waitcnt lgkmcnt(9)
ds_read_u16_d16_hi v54,v54
v_fma_mix_f32 v55,v55,v9,0 op_sel:[0,1,0] op_sel_hi:[0,1,0]
v_mfma_f32_16x16x32_bf16 v[32:35],a[192:195],a[0:3],v[16:19]
v_lshlrev_b32_sdwa v24,1,v24 dst_sel:DWORD dst_unused:UNUSED_PAD src0_sel:DWORD src1_sel:WORD_1
v_fma_mixlo_f16 v8,v49,s99,0
v_mfma_f32_16x16x32_bf16 v[32:35],a[196:199],a[16:19],v[32:35]
ds_read_u16 v24,v24
v_lshlrev_b32_sdwa v26,1,v26 dst_sel:DWORD dst_unused:UNUSED_PAD src0_sel:DWORD src1_sel:WORD_1
v_mfma_f32_16x16x32_bf16 v[32:35],a[200:203],a[32:35],v[32:35]
ds_read_u16_d16_hi v26,v26
v_lshl_add_u64 v[36:37],v[36:37],0,v[38:39]
v_mfma_f32_16x16x32_bf16 v[60:63],a[204:207],a[32:35],v[20:23]
s_waitcnt lgkmcnt(9)
v_lshl_add_u64 v[38:39],v[40:41],0,v[42:43]
v_fma_mixhi_f16 v8,v51,s99,0
v_mfma_f32_16x16x32_bf16 v[60:63],a[208:211],a[16:19],v[60:63]
s_waitcnt lgkmcnt(4)
v_lshl_add_u64 v[40:41],v[56:57],0,v[58:59]
v_lshlrev_b32_sdwa v25,1,v25 dst_sel:DWORD dst_unused:UNUSED_PAD src0_sel:DWORD src1_sel:WORD_1
v_pk_fma_f16 v9,v8,v8,s54
v_mfma_f32_16x16x32_bf16 v[60:63],a[212:215],a[0:3],v[60:63]
s_add_u32 m0,s57,84864
ds_read_u16 v25,v25
v_lshlrev_b32_sdwa v27,1,v27 dst_sel:DWORD dst_unused:UNUSED_PAD src0_sel:DWORD src1_sel:WORD_1
v_mfma_f32_16x16x32_bf16 v[56:59],a[212:215],a[4:7],v[20:23]
s_waitcnt lgkmcnt(3)
ds_read_u16_d16_hi v27,v27
v_or_b32 v42,v52,v54
v_pk_mul_f16 v9,v8,v9
v_mfma_f32_16x16x32_bf16 v[56:59],a[208:211],a[20:23],v[56:59]
s_waitcnt vmcnt(18)
v_cvt_pk_bf16_f32 v43,v53,v55
v_lshlrev_b32_sdwa v28,1,v28 dst_sel:DWORD dst_unused:UNUSED_PAD src0_sel:DWORD src1_sel:WORD_1
v_mfma_f32_16x16x32_bf16 v[56:59],a[204:207],a[36:39],v[56:59]
s_barrier
v_exp_f16 v8,v9
v_mfma_f32_16x16x32_bf16 v[52:55],a[200:203],a[36:39],v[16:19]
s_add_u32 s89,s56,4816896
ds_read_u16 v28,v28
v_lshlrev_b32_sdwa v30,1,v30 dst_sel:DWORD dst_unused:UNUSED_PAD src0_sel:DWORD src1_sel:WORD_1
v_mfma_f32_16x16x32_bf16 v[52:55],a[196:199],a[20:23],v[52:55]
buffer_load_dwordx4 v6,s[36:39],s89 offen lds
v_lshlrev_b32_sdwa v29,1,v29 dst_sel:DWORD dst_unused:UNUSED_PAD src0_sel:DWORD src1_sel:WORD_1
v_mfma_f32_16x16x32_bf16 v[52:55],a[192:195],a[4:7],v[52:55]
s_add_u32 m0,s58,18480
ds_read_u16_d16_hi v30,v30
v_exp_f16_sdwa v8,v9 dst_sel:WORD_1 dst_unused:UNUSED_PRESERVE src0_sel:WORD_1
v_mfma_f32_16x16x32_bf16 v[232:235],a[252:255],v[36:39],v[232:235]
s_mov_b32 s69,s88
ds_read_u16 v29,v29
v_lshlrev_b32_sdwa v31,1,v31 dst_sel:DWORD dst_unused:UNUSED_PAD src0_sel:DWORD src1_sel:WORD_1
v_mfma_f32_16x16x32_bf16 v[236:239],a[252:255],v[40:43],v[236:239]
buffer_load_dwordx4 v5,s[44:47],s69 offen offset:0 lds
v_lshlrev_b32_sdwa v44,1,v44 dst_sel:DWORD dst_unused:UNUSED_PAD src0_sel:DWORD src1_sel:WORD_1
v_mfma_f32_16x16x32_bf16 v[164:167],a[240:243],v[40:43],v[164:167]
ds_read_u16_d16_hi v31,v31
v_pk_fma_f16 v8,v8,s55,s55
v_mfma_f32_16x16x32_bf16 v[160:163],a[240:243],v[36:39],v[160:163]
v_rcp_f16 v9,v8
v_lshlrev_b32_sdwa v46,1,v46 dst_sel:DWORD dst_unused:UNUSED_PAD src0_sel:DWORD src1_sel:WORD_1
v_mfma_f32_16x16x32_bf16 v[240:243],a[224:227],v[36:39],v[240:243]
s_add_u32 s69,s69,s53
ds_read_u16 v44,v44
v_lshlrev_b32_sdwa v45,1,v45 dst_sel:DWORD dst_unused:UNUSED_PAD src0_sel:DWORD src1_sel:WORD_1
v_mfma_f32_16x16x32_bf16 v[244:247],a[224:227],v[40:43],v[244:247]
buffer_load_dwordx4 v5,s[44:47],s69 offen offset:1024 lds
ds_read_u16_d16_hi v46,v46
v_mfma_f32_16x16x32_bf16 v[180:183],a[232:235],v[40:43],v[180:183]
ds_read_u16 v45,v45
v_lshlrev_b32_sdwa v47,1,v47 dst_sel:DWORD dst_unused:UNUSED_PAD src0_sel:DWORD src1_sel:WORD_1
v_mfma_f32_16x16x32_bf16 v[176:179],a[232:235],v[36:39],v[176:179]
v_rcp_f16_sdwa v9,v8 dst_sel:WORD_1 dst_unused:UNUSED_PRESERVE src0_sel:WORD_1
v_mfma_f32_16x16x32_bf16 v[248:251],a[236:239],v[36:39],v[248:251]
ds_read_u16_d16_hi v47,v47
v_lshlrev_b32_sdwa v48,1,v48 dst_sel:DWORD dst_unused:UNUSED_PAD src0_sel:DWORD src1_sel:WORD_1
v_mfma_f32_16x16x32_bf16 v[252:255],a[236:239],v[40:43],v[252:255]
s_add_u32 s69,s69,s53
ds_read_u16 v48,v48
v_lshlrev_b32_sdwa v50,1,v50 dst_sel:DWORD dst_unused:UNUSED_PAD src0_sel:DWORD src1_sel:WORD_1
v_mfma_f32_16x16x32_bf16 v[196:199],a[244:247],v[40:43],v[196:199]
buffer_load_dwordx4 v5,s[44:47],s69 offen offset:2048 lds
v_fma_mix_f32 v49,v49,v9,0 op_sel_hi:[0,1,0]
v_mfma_f32_16x16x32_bf16 v[192:195],a[244:247],v[36:39],v[192:195]
ds_read_u16_d16_hi v50,v50
v_fma_mix_f32 v51,v51,v9,0 op_sel:[0,1,0] op_sel_hi:[0,1,0]
v_mfma_f32_16x16x32_bf16 v[40:43],a[192:195],a[8:11],v[16:19]
ds_read_b64_tr_b16 a[228:229],v3 offset:0
ds_read_b64_tr_b16 a[230:231],v3 offset:1536
v_lshlrev_b32_sdwa v32,1,v32 dst_sel:DWORD dst_unused:UNUSED_PAD src0_sel:DWORD src1_sel:WORD_1
v_mfma_f32_16x16x32_bf16 v[40:43],a[196:199],a[24:27],v[40:43]
ds_read_u16 v32,v32
ds_read_b64_tr_b16 a[248:249],v3 offset:8
v_fma_mixlo_f16 v8,v57,s99,0
v_mfma_f32_16x16x32_bf16 v[40:43],a[200:203],a[40:43],v[40:43]
s_waitcnt lgkmcnt(8)
ds_read_b64_tr_b16 a[250:251],v3 offset:1544
v_lshl_add_u64 v[24:25],v[24:25],0,v[26:27]
v_lshlrev_b32_sdwa v34,1,v34 dst_sel:DWORD dst_unused:UNUSED_PAD src0_sel:DWORD src1_sel:WORD_1
v_mfma_f32_16x16x32_bf16 v[36:39],a[204:207],a[40:43],v[20:23]
ds_read_u16_d16_hi v34,v34
v_lshl_add_u64 v[26:27],v[28:29],0,v[30:31]
v_fma_mixhi_f16 v8,v59,s99,0
v_mfma_f32_16x16x32_bf16 v[36:39],a[208:211],a[24:27],v[36:39]
s_waitcnt lgkmcnt(8)
ds_read_b64_tr_b16 a[216:217],v1 offset:0
v_lshl_add_u64 v[28:29],v[44:45],0,v[46:47]
v_lshlrev_b32_sdwa v33,1,v33 dst_sel:DWORD dst_unused:UNUSED_PAD src0_sel:DWORD src1_sel:WORD_1
v_pk_fma_f16 v9,v8,v8,s54
v_mfma_f32_16x16x32_bf16 v[36:39],a[212:215],a[8:11],v[36:39]
ds_read_u16 v33,v33
v_lshlrev_b32_sdwa v35,1,v35 dst_sel:DWORD dst_unused:UNUSED_PAD src0_sel:DWORD src1_sel:WORD_1
v_mfma_f32_16x16x32_bf16 v[44:47],a[212:215],a[12:15],v[20:23]
s_waitcnt lgkmcnt(8)
ds_read_u16_d16_hi v35,v35
v_or_b32 v30,v48,v50
v_pk_mul_f16 v9,v8,v9
v_mfma_f32_16x16x32_bf16 v[44:47],a[208:211],a[28:31],v[44:47]
ds_read_b64_tr_b16 a[218:219],v1 offset:1024
v_cvt_pk_bf16_f32 v31,v49,v51
v_lshlrev_b32_sdwa v60,1,v60 dst_sel:DWORD dst_unused:UNUSED_PAD src0_sel:DWORD src1_sel:WORD_1
v_mfma_f32_16x16x32_bf16 v[44:47],a[204:207],a[44:47],v[44:47]
v_exp_f16 v8,v9
v_mfma_f32_16x16x32_bf16 v[48:51],a[200:203],a[44:47],v[16:19]
ds_read_u16 v60,v60
v_lshlrev_b32_sdwa v62,1,v62 dst_sel:DWORD dst_unused:UNUSED_PAD src0_sel:DWORD src1_sel:WORD_1
v_mfma_f32_16x16x32_bf16 v[48:51],a[196:199],a[28:31],v[48:51]
ds_read_u16_d16_hi v62,v62
v_lshlrev_b32_sdwa v61,1,v61 dst_sel:DWORD dst_unused:UNUSED_PAD src0_sel:DWORD src1_sel:WORD_1
v_mfma_f32_16x16x32_bf16 v[48:51],a[192:195],a[12:15],v[48:51]
v_exp_f16_sdwa v8,v9 dst_sel:WORD_1 dst_unused:UNUSED_PRESERVE src0_sel:WORD_1
v_mfma_f32_16x16x32_bf16 v[208:211],a[252:255],v[24:27],v[208:211]
ds_read_u16 v61,v61
v_lshlrev_b32_sdwa v63,1,v63 dst_sel:DWORD dst_unused:UNUSED_PAD src0_sel:DWORD src1_sel:WORD_1
v_mfma_f32_16x16x32_bf16 v[212:215],a[252:255],v[28:31],v[212:215]
ds_read_u16_d16_hi v63,v63
v_lshlrev_b32_sdwa v52,1,v52 dst_sel:DWORD dst_unused:UNUSED_PAD src0_sel:DWORD src1_sel:WORD_1
v_mfma_f32_16x16x32_bf16 v[172:175],a[240:243],v[28:31],v[172:175]
ds_read_u16 v52,v52
v_pk_fma_f16 v8,v8,s55,s55
v_mfma_f32_16x16x32_bf16 v[168:171],a[240:243],v[24:27],v[168:171]
ds_read_b64_tr_b16 a[252:253],v3 offset:64
v_rcp_f16 v9,v8
v_lshlrev_b32_sdwa v54,1,v54 dst_sel:DWORD dst_unused:UNUSED_PAD src0_sel:DWORD src1_sel:WORD_1
v_mfma_f32_16x16x32_bf16 v[216:219],a[224:227],v[24:27],v[216:219]
ds_read_u16_d16_hi v54,v54
v_lshlrev_b32_sdwa v53,1,v53 dst_sel:DWORD dst_unused:UNUSED_PAD src0_sel:DWORD src1_sel:WORD_1
v_mfma_f32_16x16x32_bf16 v[220:223],a[224:227],v[28:31],v[220:223]
ds_read_u16 v53,v53
ds_read_b64_tr_b16 a[254:255],v3 offset:1600
v_mfma_f32_16x16x32_bf16 v[188:191],a[232:235],v[28:31],v[188:191]
ds_read_b64_tr_b16 a[240:241],v3 offset:72
v_lshlrev_b32_sdwa v55,1,v55 dst_sel:DWORD dst_unused:UNUSED_PAD src0_sel:DWORD src1_sel:WORD_1
v_mfma_f32_16x16x32_bf16 v[184:187],a[232:235],v[24:27],v[184:187]
ds_read_u16_d16_hi v55,v55
v_rcp_f16_sdwa v9,v8 dst_sel:WORD_1 dst_unused:UNUSED_PRESERVE src0_sel:WORD_1
v_mfma_f32_16x16x32_bf16 v[224:227],a[236:239],v[24:27],v[224:227]
ds_read_b64_tr_b16 a[242:243],v3 offset:1608
ds_read_b64_tr_b16 a[224:225],v3 offset:128
v_lshlrev_b32_sdwa v56,1,v56 dst_sel:DWORD dst_unused:UNUSED_PAD src0_sel:DWORD src1_sel:WORD_1
v_mfma_f32_16x16x32_bf16 v[228:231],a[236:239],v[28:31],v[228:231]
ds_read_u16 v56,v56
v_lshlrev_b32_sdwa v58,1,v58 dst_sel:DWORD dst_unused:UNUSED_PAD src0_sel:DWORD src1_sel:WORD_1
v_mfma_f32_16x16x32_bf16 v[204:207],a[244:247],v[28:31],v[204:207]
ds_read_u16_d16_hi v58,v58
v_fma_mix_f32 v57,v57,v9,0 op_sel_hi:[0,1,0]
v_mfma_f32_16x16x32_bf16 v[200:203],a[244:247],v[24:27],v[200:203]
ds_read_b64_tr_b16 a[226:227],v3 offset:1664
v_fma_mix_f32 v59,v59,v9,0 op_sel:[0,1,0] op_sel_hi:[0,1,0]
v_mfma_f32_16x16x32_bf16 v[28:31],a[192:195],a[48:51],v[16:19]
s_add_u32 m0,s57,8
ds_read_b64 v[14:15],v7 offset:0
v_lshlrev_b32_sdwa v40,1,v40 dst_sel:DWORD dst_unused:UNUSED_PAD src0_sel:DWORD src1_sel:WORD_1
v_mfma_f32_16x16x32_bf16 v[28:31],a[196:199],a[64:67],v[28:31]
s_add_u32 s89,s56,4915200
ds_read_b64_tr_b16 a[232:233],v3 offset:136
v_fma_mixlo_f16 v8,v45,s99,0
v_mfma_f32_16x16x32_bf16 v[28:31],a[200:203],a[80:83],v[28:31]
s_waitcnt lgkmcnt(9)
buffer_load_dwordx4 v6,s[36:39],s89 offen lds
v_lshl_add_u64 v[32:33],v[32:33],0,v[34:35]
v_lshlrev_b32_sdwa v42,1,v42 dst_sel:DWORD dst_unused:UNUSED_PAD src0_sel:DWORD src1_sel:WORD_1
v_mfma_f32_16x16x32_bf16 v[24:27],a[204:207],a[80:83],v[20:23]
ds_read_b64_tr_b16 a[234:235],v3 offset:1672
v_lshl_add_u64 v[34:35],v[60:61],0,v[62:63]
v_fma_mixhi_f16 v8,v47,s99,0
v_mfma_f32_16x16x32_bf16 v[24:27],a[208:211],a[64:67],v[24:27]
s_waitcnt lgkmcnt(8)
ds_read_u16 v40,v40
v_lshl_add_u64 v[60:61],v[52:53],0,v[54:55]
v_lshlrev_b32_sdwa v41,1,v41 dst_sel:DWORD dst_unused:UNUSED_PAD src0_sel:DWORD src1_sel:WORD_1
v_pk_fma_f16 v9,v8,v8,s54
v_mfma_f32_16x16x32_bf16 v[24:27],a[212:215],a[48:51],v[24:27]
s_waitcnt lgkmcnt(3)
ds_read_u16_d16_hi v42,v42
v_or3_b32 v13,v14,v15,v13
v_lshlrev_b32_sdwa v43,1,v43 dst_sel:DWORD dst_unused:UNUSED_PAD src0_sel:DWORD src1_sel:WORD_1
v_mfma_f32_16x16x32_bf16 v[52:55],a[212:215],a[52:55],v[20:23]
TP3867_%=:
s_cbranch_execz TP6020_%=
ds_read_b64 v[14:15],v7 offset:512
v_or_b32 v62,v56,v58
v_pk_mul_f16 v9,v8,v9
v_mfma_f32_16x16x32_bf16 v[52:55],a[208:211],a[68:71],v[52:55]
ds_read_u16 v41,v41
v_cvt_pk_bf16_f32 v63,v57,v59
v_lshlrev_b32_sdwa v36,1,v36 dst_sel:DWORD dst_unused:UNUSED_PAD src0_sel:DWORD src1_sel:WORD_1
v_mfma_f32_16x16x32_bf16 v[52:55],a[204:207],a[84:87],v[52:55]
v_exp_f16 v8,v9
v_mfma_f32_16x16x32_bf16 v[56:59],a[200:203],a[84:87],v[16:19]
ds_read_u16_d16_hi v43,v43
v_lshlrev_b32_sdwa v38,1,v38 dst_sel:DWORD dst_unused:UNUSED_PAD src0_sel:DWORD src1_sel:WORD_1
v_mfma_f32_16x16x32_bf16 v[56:59],a[196:199],a[68:71],v[56:59]
ds_read_u16 v36,v36
v_lshlrev_b32_sdwa v37,1,v37 dst_sel:DWORD dst_unused:UNUSED_PAD src0_sel:DWORD src1_sel:WORD_1
v_mfma_f32_16x16x32_bf16 v[56:59],a[192:195],a[52:55],v[56:59]
s_waitcnt lgkmcnt(3)
ds_read_u16_d16_hi v38,v38
v_or3_b32 v13,v14,v15,v13
v_mfma_f32_16x16x32_bf16 v[136:139],a[228:231],v[32:35],v[136:139]
ds_read_b64 v[14:15],v7 offset:1024
v_exp_f16_sdwa v8,v9 dst_sel:WORD_1 dst_unused:UNUSED_PRESERVE src0_sel:WORD_1
v_lshlrev_b32_sdwa v39,1,v39 dst_sel:DWORD dst_unused:UNUSED_PAD src0_sel:DWORD src1_sel:WORD_1
v_mfma_f32_16x16x32_bf16 v[140:143],a[228:231],v[60:63],v[140:143]
ds_read_u16 v37,v37
v_lshlrev_b32_sdwa v48,1,v48 dst_sel:DWORD dst_unused:UNUSED_PAD src0_sel:DWORD src1_sel:WORD_1
v_mfma_f32_16x16x32_bf16 v[68:71],a[248:251],v[60:63],v[68:71]
ds_read_u16_d16_hi v39,v39
v_pk_fma_f16 v8,v8,s55,s55
v_mfma_f32_16x16x32_bf16 v[64:67],a[248:251],v[32:35],v[64:67]
v_rcp_f16 v9,v8
v_lshlrev_b32_sdwa v50,1,v50 dst_sel:DWORD dst_unused:UNUSED_PAD src0_sel:DWORD src1_sel:WORD_1
v_mfma_f32_16x16x32_bf16 v[144:147],a[252:255],v[32:35],v[144:147]
ds_read_u16 v48,v48
v_lshlrev_b32_sdwa v49,1,v49 dst_sel:DWORD dst_unused:UNUSED_PAD src0_sel:DWORD src1_sel:WORD_1
v_mfma_f32_16x16x32_bf16 v[148:151],a[252:255],v[60:63],v[148:151]
s_waitcnt lgkmcnt(3)
ds_read_u16_d16_hi v50,v50
v_or3_b32 v13,v14,v15,v13
v_mfma_f32_16x16x32_bf16 v[84:87],a[240:243],v[60:63],v[84:87]
s_add_u32 m0,s57,3544
ds_read_u16 v49,v49
v_lshlrev_b32_sdwa v51,1,v51 dst_sel:DWORD dst_unused:UNUSED_PAD src0_sel:DWORD src1_sel:WORD_1
v_mfma_f32_16x16x32_bf16 v[80:83],a[240:243],v[32:35],v[80:83]
s_add_u32 s89,s56,5013504
v_rcp_f16_sdwa v9,v8 dst_sel:WORD_1 dst_unused:UNUSED_PRESERVE src0_sel:WORD_1
v_mfma_f32_16x16x32_bf16 v[152:155],a[224:227],v[32:35],v[152:155]
buffer_load_dwordx4 v6,s[36:39],s89 offen lds
ds_read_u16_d16_hi v51,v51
v_lshlrev_b32_sdwa v44,1,v44 dst_sel:DWORD dst_unused:UNUSED_PAD src0_sel:DWORD src1_sel:WORD_1
v_mfma_f32_16x16x32_bf16 v[156:159],a[224:227],v[60:63],v[156:159]
ds_read_u16 v44,v44
v_lshlrev_b32_sdwa v46,1,v46 dst_sel:DWORD dst_unused:UNUSED_PAD src0_sel:DWORD src1_sel:WORD_1
v_mfma_f32_16x16x32_bf16 v[100:103],a[232:235],v[60:63],v[100:103]
ds_read_u16_d16_hi v46,v46
v_fma_mix_f32 v45,v45,v9,0 op_sel_hi:[0,1,0]
v_mfma_f32_16x16x32_bf16 v[96:99],a[232:235],v[32:35],v[96:99]
ds_read_b64_tr_b16 a[220:221],v1 offset:2048
v_fma_mix_f32 v47,v47,v9,0 op_sel:[0,1,0] op_sel_hi:[0,1,0]
v_mfma_f32_16x16x32_bf16 v[60:63],a[192:195],a[56:59],v[16:19]
ds_read_b64_tr_b16 a[222:223],v1 offset:3072
v_lshlrev_b32_sdwa v28,1,v28 dst_sel:DWORD dst_unused:UNUSED_PAD src0_sel:DWORD src1_sel:WORD_1
v_mfma_f32_16x16x32_bf16 v[60:63],a[196:199],a[72:75],v[60:63]
ds_read_u16 v28,v28
v_fma_mixlo_f16 v8,v53,s99,0
v_mfma_f32_16x16x32_bf16 v[60:63],a[200:203],a[88:91],v[60:63]
v_lshl_add_u64 v[40:41],v[40:41],0,v[42:43]
v_lshlrev_b32_sdwa v30,1,v30 dst_sel:DWORD dst_unused:UNUSED_PAD src0_sel:DWORD src1_sel:WORD_1
v_mfma_f32_16x16x32_bf16 v[32:35],a[204:207],a[88:91],v[20:23]
s_waitcnt lgkmcnt(6)
ds_read_u16_d16_hi v30,v30
v_lshl_add_u64 v[42:43],v[36:37],0,v[38:39]
v_fma_mixhi_f16 v8,v55,s99,0
v_mfma_f32_16x16x32_bf16 v[32:35],a[208:211],a[72:75],v[32:35]
s_waitcnt lgkmcnt(6)
v_lshl_add_u64 v[36:37],v[48:49],0,v[50:51]
v_lshlrev_b32_sdwa v29,1,v29 dst_sel:DWORD dst_unused:UNUSED_PAD src0_sel:DWORD src1_sel:WORD_1
v_pk_fma_f16 v9,v8,v8,s54
v_mfma_f32_16x16x32_bf16 v[32:35],a[212:215],a[56:59],v[32:35]
ds_read_u16 v29,v29
v_lshlrev_b32_sdwa v31,1,v31 dst_sel:DWORD dst_unused:UNUSED_PAD src0_sel:DWORD src1_sel:WORD_1
v_mfma_f32_16x16x32_bf16 v[48:51],a[212:215],a[60:63],v[20:23]
s_waitcnt lgkmcnt(5)
ds_read_b128 v[20:23],v4 offset:1184
v_or_b32 v38,v44,v46
v_pk_mul_f16 v9,v8,v9
v_mfma_f32_16x16x32_bf16 v[48:51],a[208:211],a[76:79],v[48:51]
ds_read_u16_d16_hi v31,v31
v_cvt_pk_bf16_f32 v39,v45,v47
v_lshlrev_b32_sdwa v24,1,v24 dst_sel:DWORD dst_unused:UNUSED_PAD src0_sel:DWORD src1_sel:WORD_1
v_mfma_f32_16x16x32_bf16 v[48:51],a[204:207],a[92:95],v[48:51]
ds_read_b64_tr_b16 a[212:213],v1 offset:4096
v_exp_f16 v8,v9
v_mfma_f32_16x16x32_bf16 v[44:47],a[200:203],a[92:95],v[16:19]
ds_read_b128 v[16:19],v4 offset:1152
ds_read_b64_tr_b16 a[214:215],v1 offset:5120
v_lshlrev_b32_sdwa v26,1,v26 dst_sel:DWORD dst_unused:UNUSED_PAD src0_sel:DWORD src1_sel:WORD_1
v_mfma_f32_16x16x32_bf16 v[44:47],a[196:199],a[76:79],v[44:47]
ds_read_b64_tr_b16 a[208:209],v2 offset:4096
ds_read_b64_tr_b16 a[210:211],v2 offset:5120
v_lshlrev_b32_sdwa v25,1,v25 dst_sel:DWORD dst_unused:UNUSED_PAD src0_sel:DWORD src1_sel:WORD_1
v_mfma_f32_16x16x32_bf16 v[44:47],a[192:195],a[60:63],v[44:47]
ds_read_u16 v24,v24
v_exp_f16_sdwa v8,v9 dst_sel:WORD_1 dst_unused:UNUSED_PRESERVE src0_sel:WORD_1
v_mfma_f32_16x16x32_bf16 v[112:115],a[228:231],v[40:43],v[112:115]
ds_read_u16_d16_hi v26,v26
v_lshlrev_b32_sdwa v27,1,v27 dst_sel:DWORD dst_unused:UNUSED_PAD src0_sel:DWORD src1_sel:WORD_1
v_mfma_f32_16x16x32_bf16 v[116:119],a[228:231],v[36:39],v[116:119]
ds_read_u16 v25,v25
v_lshlrev_b32_sdwa v56,1,v56 dst_sel:DWORD dst_unused:UNUSED_PAD src0_sel:DWORD src1_sel:WORD_1
v_mfma_f32_16x16x32_bf16 v[76:79],a[248:251],v[36:39],v[76:79]
ds_read_u16_d16_hi v27,v27
v_pk_fma_f16 v8,v8,s55,s55
v_mfma_f32_16x16x32_bf16 v[72:75],a[248:251],v[40:43],v[72:75]
v_rcp_f16 v9,v8
v_lshlrev_b32_sdwa v58,1,v58 dst_sel:DWORD dst_unused:UNUSED_PAD src0_sel:DWORD src1_sel:WORD_1
v_mfma_f32_16x16x32_bf16 v[120:123],a[252:255],v[40:43],v[120:123]
ds_read_b64_tr_b16 a[204:205],v2 offset:2048
v_lshlrev_b32_sdwa v57,1,v57 dst_sel:DWORD dst_unused:UNUSED_PAD src0_sel:DWORD src1_sel:WORD_1
v_mfma_f32_16x16x32_bf16 v[124:127],a[252:255],v[36:39],v[124:127]
ds_read_b64_tr_b16 a[206:207],v2 offset:3072
v_lshlrev_b32_sdwa v59,1,v59 dst_sel:DWORD dst_unused:UNUSED_PAD src0_sel:DWORD src1_sel:WORD_1
v_mfma_f32_16x16x32_bf16 v[92:95],a[240:243],v[36:39],v[92:95]
ds_read_u16 v56,v56
v_lshlrev_b32_sdwa v52,1,v52 dst_sel:DWORD dst_unused:UNUSED_PAD src0_sel:DWORD src1_sel:WORD_1
v_mfma_f32_16x16x32_bf16 v[88:91],a[240:243],v[40:43],v[88:91]
ds_read_u16_d16_hi v58,v58
v_rcp_f16_sdwa v9,v8 dst_sel:WORD_1 dst_unused:UNUSED_PRESERVE src0_sel:WORD_1
v_mfma_f32_16x16x32_bf16 v[128:131],a[224:227],v[40:43],v[128:131]
ds_read_u16 v57,v57
ds_read_u16_d16_hi v59,v59
v_mfma_f32_16x16x32_bf16 v[132:135],a[224:227],v[36:39],v[132:135]
ds_read_b64_tr_b16 a[200:201],v2 offset:0
ds_read_b64_tr_b16 a[202:203],v2 offset:1024
v_lshlrev_b32_sdwa v54,1,v54 dst_sel:DWORD dst_unused:UNUSED_PAD src0_sel:DWORD src1_sel:WORD_1
v_mfma_f32_16x16x32_bf16 v[108:111],a[232:235],v[36:39],v[108:111]
ds_read_u16 v52,v52
v_fma_mix_f32 v53,v53,v9,0 op_sel_hi:[0,1,0]
v_mfma_f32_16x16x32_bf16 v[104:107],a[232:235],v[40:43],v[104:107]
s_waitcnt lgkmcnt(9)
ds_read_u16_d16_hi v54,v54
v_fma_mix_f32 v55,v55,v9,0 op_sel:[0,1,0] op_sel_hi:[0,1,0]
v_mfma_f32_16x16x32_bf16 v[36:39],a[216:219],a[0:3],v[16:19]
v_lshlrev_b32_sdwa v60,1,v60 dst_sel:DWORD dst_unused:UNUSED_PAD src0_sel:DWORD src1_sel:WORD_1
v_fma_mixlo_f16 v8,v49,s99,0
v_mfma_f32_16x16x32_bf16 v[36:39],a[220:223],a[16:19],v[36:39]
s_add_u32 m0,s57,7072
ds_read_u16 v60,v60
v_lshlrev_b32_sdwa v62,1,v62 dst_sel:DWORD dst_unused:UNUSED_PAD src0_sel:DWORD src1_sel:WORD_1
v_mfma_f32_16x16x32_bf16 v[36:39],a[212:215],a[32:35],v[36:39]
s_add_u32 s89,s56,5111808
ds_read_u16_d16_hi v62,v62
v_lshl_add_u64 v[28:29],v[28:29],0,v[30:31]
v_mfma_f32_16x16x32_bf16 v[40:43],a[208:211],a[32:35],v[20:23]
s_waitcnt lgkmcnt(9)
buffer_load_dwordx4 v6,s[36:39],s89 offen lds
v_lshl_add_u64 v[30:31],v[24:25],0,v[26:27]
v_fma_mixhi_f16 v8,v51,s99,0
v_mfma_f32_16x16x32_bf16 v[40:43],a[204:207],a[16:19],v[40:43]
s_waitcnt lgkmcnt(4)
v_lshl_add_u64 v[24:25],v[56:57],0,v[58:59]
v_lshlrev_b32_sdwa v61,1,v61 dst_sel:DWORD dst_unused:UNUSED_PAD src0_sel:DWORD src1_sel:WORD_1
v_pk_fma_f16 v9,v8,v8,s54
v_mfma_f32_16x16x32_bf16 v[40:43],a[200:203],a[0:3],v[40:43]
ds_read_u16 v61,v61
v_lshlrev_b32_sdwa v63,1,v63 dst_sel:DWORD dst_unused:UNUSED_PAD src0_sel:DWORD src1_sel:WORD_1
v_mfma_f32_16x16x32_bf16 v[56:59],a[200:203],a[4:7],v[20:23]
s_waitcnt lgkmcnt(3)
ds_read_u16_d16_hi v63,v63
v_or_b32 v26,v52,v54
v_pk_mul_f16 v9,v8,v9
v_mfma_f32_16x16x32_bf16 v[56:59],a[204:207],a[20:23],v[56:59]
s_waitcnt vmcnt(15)
v_cvt_pk_bf16_f32 v27,v53,v55
v_lshlrev_b32_sdwa v32,1,v32 dst_sel:DWORD dst_unused:UNUSED_PAD src0_sel:DWORD src1_sel:WORD_1
v_mfma_f32_16x16x32_bf16 v[56:59],a[208:211],a[36:39],v[56:59]
s_barrier
v_exp_f16 v8,v9
v_mfma_f32_16x16x32_bf16 v[52:55],a[212:215],a[36:39],v[16:19]
ds_read_u16 v32,v32
v_lshlrev_b32_sdwa v34,1,v34 dst_sel:DWORD dst_unused:UNUSED_PAD src0_sel:DWORD src1_sel:WORD_1
v_mfma_f32_16x16x32_bf16 v[52:55],a[220:223],a[20:23],v[52:55]
s_add_u32 m0,s58,0
ds_read_u16_d16_hi v34,v34
v_lshlrev_b32_sdwa v33,1,v33 dst_sel:DWORD dst_unused:UNUSED_PAD src0_sel:DWORD src1_sel:WORD_1
v_mfma_f32_16x16x32_bf16 v[52:55],a[216:219],a[4:7],v[52:55]
s_mov_b32 s69,s87
v_exp_f16_sdwa v8,v9 dst_sel:WORD_1 dst_unused:UNUSED_PRESERVE src0_sel:WORD_1
v_mfma_f32_16x16x32_bf16 v[232:235],a[228:231],v[28:31],v[232:235]
buffer_load_dwordx4 v5,s[44:47],s69 offen offset:0 lds
v_lshlrev_b32_sdwa v35,1,v35 dst_sel:DWORD dst_unused:UNUSED_PAD src0_sel:DWORD src1_sel:WORD_1
v_mfma_f32_16x16x32_bf16 v[236:239],a[228:231],v[24:27],v[236:239]
ds_read_u16 v33,v33
v_lshlrev_b32_sdwa v44,1,v44 dst_sel:DWORD dst_unused:UNUSED_PAD src0_sel:DWORD src1_sel:WORD_1
v_mfma_f32_16x16x32_bf16 v[164:167],a[248:251],v[24:27],v[164:167]
ds_read_u16_d16_hi v35,v35
v_pk_fma_f16 v8,v8,s55,s55
v_mfma_f32_16x16x32_bf16 v[160:163],a[248:251],v[28:31],v[160:163]
v_rcp_f16 v9,v8
v_lshlrev_b32_sdwa v46,1,v46 dst_sel:DWORD dst_unused:UNUSED_PAD src0_sel:DWORD src1_sel:WORD_1
v_mfma_f32_16x16x32_bf16 v[240:243],a[252:255],v[28:31],v[240:243]
s_add_u32 s69,s69,s53
ds_read_u16 v44,v44
v_lshlrev_b32_sdwa v45,1,v45 dst_sel:DWORD dst_unused:UNUSED_PAD src0_sel:DWORD src1_sel:WORD_1
v_mfma_f32_16x16x32_bf16 v[244:247],a[252:255],v[24:27],v[244:247]
buffer_load_dwordx4 v5,s[44:47],s69 offen offset:1024 lds
ds_read_u16_d16_hi v46,v46
v_mfma_f32_16x16x32_bf16 v[180:183],a[240:243],v[24:27],v[180:183]
ds_read_u16 v45,v45
v_lshlrev_b32_sdwa v47,1,v47 dst_sel:DWORD dst_unused:UNUSED_PAD src0_sel:DWORD src1_sel:WORD_1
v_mfma_f32_16x16x32_bf16 v[176:179],a[240:243],v[28:31],v[176:179]
v_rcp_f16_sdwa v9,v8 dst_sel:WORD_1 dst_unused:UNUSED_PRESERVE src0_sel:WORD_1
v_mfma_f32_16x16x32_bf16 v[248:251],a[224:227],v[28:31],v[248:251]
ds_read_u16_d16_hi v47,v47
v_lshlrev_b32_sdwa v48,1,v48 dst_sel:DWORD dst_unused:UNUSED_PAD src0_sel:DWORD src1_sel:WORD_1
v_mfma_f32_16x16x32_bf16 v[252:255],a[224:227],v[24:27],v[252:255]
s_add_u32 s69,s69,s53
ds_read_u16 v48,v48
v_lshlrev_b32_sdwa v50,1,v50 dst_sel:DWORD dst_unused:UNUSED_PAD src0_sel:DWORD src1_sel:WORD_1
v_mfma_f32_16x16x32_bf16 v[196:199],a[232:235],v[24:27],v[196:199]
buffer_load_dwordx4 v5,s[44:47],s69 offen offset:2048 lds
v_fma_mix_f32 v49,v49,v9,0 op_sel_hi:[0,1,0]
v_mfma_f32_16x16x32_bf16 v[192:195],a[232:235],v[28:31],v[192:195]
ds_read_u16_d16_hi v50,v50
v_fma_mix_f32 v51,v51,v9,0 op_sel:[0,1,0] op_sel_hi:[0,1,0]
v_mfma_f32_16x16x32_bf16 v[24:27],a[216:219],a[8:11],v[16:19]
ds_read_b64_tr_b16 a[236:237],v3 offset:6160
ds_read_b64_tr_b16 a[238:239],v3 offset:7696
v_lshlrev_b32_sdwa v36,1,v36 dst_sel:DWORD dst_unused:UNUSED_PAD src0_sel:DWORD src1_sel:WORD_1
v_mfma_f32_16x16x32_bf16 v[24:27],a[220:223],a[24:27],v[24:27]
ds_read_u16 v36,v36
ds_read_b64_tr_b16 a[244:245],v3 offset:6168
v_fma_mixlo_f16 v8,v57,s99,0
v_mfma_f32_16x16x32_bf16 v[24:27],a[212:215],a[40:43],v[24:27]
s_waitcnt lgkmcnt(8)
ds_read_b64_tr_b16 a[246:247],v3 offset:7704
v_lshl_add_u64 v[60:61],v[60:61],0,v[62:63]
v_lshlrev_b32_sdwa v38,1,v38 dst_sel:DWORD dst_unused:UNUSED_PAD src0_sel:DWORD src1_sel:WORD_1
v_mfma_f32_16x16x32_bf16 v[28:31],a[208:211],a[40:43],v[20:23]
ds_read_u16_d16_hi v38,v38
v_lshl_add_u64 v[62:63],v[32:33],0,v[34:35]
v_fma_mixhi_f16 v8,v59,s99,0
v_mfma_f32_16x16x32_bf16 v[28:31],a[204:207],a[24:27],v[28:31]
s_waitcnt lgkmcnt(8)
ds_read_b64_tr_b16 a[196:197],v1 offset:6160
v_lshl_add_u64 v[32:33],v[44:45],0,v[46:47]
v_lshlrev_b32_sdwa v37,1,v37 dst_sel:DWORD dst_unused:UNUSED_PAD src0_sel:DWORD src1_sel:WORD_1
v_pk_fma_f16 v9,v8,v8,s54
v_mfma_f32_16x16x32_bf16 v[28:31],a[200:203],a[8:11],v[28:31]
ds_read_u16 v37,v37
v_lshlrev_b32_sdwa v39,1,v39 dst_sel:DWORD dst_unused:UNUSED_PAD src0_sel:DWORD src1_sel:WORD_1
v_mfma_f32_16x16x32_bf16 v[44:47],a[200:203],a[12:15],v[20:23]
s_waitcnt lgkmcnt(8)
ds_read_u16_d16_hi v39,v39
v_or_b32 v34,v48,v50
v_pk_mul_f16 v9,v8,v9
v_mfma_f32_16x16x32_bf16 v[44:47],a[204:207],a[28:31],v[44:47]
ds_read_b64_tr_b16 a[198:199],v1 offset:7184
v_cvt_pk_bf16_f32 v35,v49,v51
v_lshlrev_b32_sdwa v40,1,v40 dst_sel:DWORD dst_unused:UNUSED_PAD src0_sel:DWORD src1_sel:WORD_1
v_mfma_f32_16x16x32_bf16 v[44:47],a[208:211],a[44:47],v[44:47]
v_exp_f16 v8,v9
v_mfma_f32_16x16x32_bf16 v[48:51],a[212:215],a[44:47],v[16:19]
ds_read_u16 v40,v40
v_lshlrev_b32_sdwa v42,1,v42 dst_sel:DWORD dst_unused:UNUSED_PAD src0_sel:DWORD src1_sel:WORD_1
v_mfma_f32_16x16x32_bf16 v[48:51],a[220:223],a[28:31],v[48:51]
ds_read_u16_d16_hi v42,v42
v_lshlrev_b32_sdwa v41,1,v41 dst_sel:DWORD dst_unused:UNUSED_PAD src0_sel:DWORD src1_sel:WORD_1
v_mfma_f32_16x16x32_bf16 v[48:51],a[216:219],a[12:15],v[48:51]
v_exp_f16_sdwa v8,v9 dst_sel:WORD_1 dst_unused:UNUSED_PRESERVE src0_sel:WORD_1
v_mfma_f32_16x16x32_bf16 v[208:211],a[228:231],v[60:63],v[208:211]
ds_read_u16 v41,v41
v_lshlrev_b32_sdwa v43,1,v43 dst_sel:DWORD dst_unused:UNUSED_PAD src0_sel:DWORD src1_sel:WORD_1
v_mfma_f32_16x16x32_bf16 v[212:215],a[228:231],v[32:35],v[212:215]
ds_read_u16_d16_hi v43,v43
v_lshlrev_b32_sdwa v52,1,v52 dst_sel:DWORD dst_unused:UNUSED_PAD src0_sel:DWORD src1_sel:WORD_1
v_mfma_f32_16x16x32_bf16 v[172:175],a[248:251],v[32:35],v[172:175]
ds_read_u16 v52,v52
v_pk_fma_f16 v8,v8,s55,s55
v_mfma_f32_16x16x32_bf16 v[168:171],a[248:251],v[60:63],v[168:171]
ds_read_b64_tr_b16 a[228:229],v3 offset:6224
v_rcp_f16 v9,v8
v_lshlrev_b32_sdwa v54,1,v54 dst_sel:DWORD dst_unused:UNUSED_PAD src0_sel:DWORD src1_sel:WORD_1
v_mfma_f32_16x16x32_bf16 v[216:219],a[252:255],v[60:63],v[216:219]
ds_read_u16_d16_hi v54,v54
v_lshlrev_b32_sdwa v53,1,v53 dst_sel:DWORD dst_unused:UNUSED_PAD src0_sel:DWORD src1_sel:WORD_1
v_mfma_f32_16x16x32_bf16 v[220:223],a[252:255],v[32:35],v[220:223]
ds_read_u16 v53,v53
ds_read_b64_tr_b16 a[230:231],v3 offset:7760
v_mfma_f32_16x16x32_bf16 v[188:191],a[240:243],v[32:35],v[188:191]
ds_read_b64_tr_b16 a[248:249],v3 offset:6232
v_lshlrev_b32_sdwa v55,1,v55 dst_sel:DWORD dst_unused:UNUSED_PAD src0_sel:DWORD src1_sel:WORD_1
v_mfma_f32_16x16x32_bf16 v[184:187],a[240:243],v[60:63],v[184:187]
ds_read_u16_d16_hi v55,v55
v_rcp_f16_sdwa v9,v8 dst_sel:WORD_1 dst_unused:UNUSED_PRESERVE src0_sel:WORD_1
v_mfma_f32_16x16x32_bf16 v[224:227],a[224:227],v[60:63],v[224:227]
ds_read_b64_tr_b16 a[250:251],v3 offset:7768
ds_read_b64_tr_b16 a[252:253],v3 offset:6288
v_lshlrev_b32_sdwa v56,1,v56 dst_sel:DWORD dst_unused:UNUSED_PAD src0_sel:DWORD src1_sel:WORD_1
v_mfma_f32_16x16x32_bf16 v[228:231],a[224:227],v[32:35],v[228:231]
ds_read_u16 v56,v56
v_lshlrev_b32_sdwa v58,1,v58 dst_sel:DWORD dst_unused:UNUSED_PAD src0_sel:DWORD src1_sel:WORD_1
v_mfma_f32_16x16x32_bf16 v[204:207],a[232:235],v[32:35],v[204:207]
ds_read_u16_d16_hi v58,v58
v_fma_mix_f32 v57,v57,v9,0 op_sel_hi:[0,1,0]
v_mfma_f32_16x16x32_bf16 v[200:203],a[232:235],v[60:63],v[200:203]
ds_read_b64_tr_b16 a[254:255],v3 offset:7824
v_fma_mix_f32 v59,v59,v9,0 op_sel:[0,1,0] op_sel_hi:[0,1,0]
v_mfma_f32_16x16x32_bf16 v[32:35],a[216:219],a[48:51],v[16:19]
s_add_u32 m0,s57,10608
ds_read_b64 v[14:15],v7 offset:6160
v_lshlrev_b32_sdwa v24,1,v24 dst_sel:DWORD dst_unused:UNUSED_PAD src0_sel:DWORD src1_sel:WORD_1
v_mfma_f32_16x16x32_bf16 v[32:35],a[220:223],a[64:67],v[32:35]
s_add_u32 s89,s56,5210112
ds_read_b64_tr_b16 a[240:241],v3 offset:6296
v_fma_mixlo_f16 v8,v45,s99,0
v_mfma_f32_16x16x32_bf16 v[32:35],a[212:215],a[80:83],v[32:35]
s_waitcnt lgkmcnt(9)
buffer_load_dwordx4 v6,s[36:39],s89 offen lds
v_lshl_add_u64 v[36:37],v[36:37],0,v[38:39]
v_lshlrev_b32_sdwa v26,1,v26 dst_sel:DWORD dst_unused:UNUSED_PAD src0_sel:DWORD src1_sel:WORD_1
v_mfma_f32_16x16x32_bf16 v[60:63],a[208:211],a[80:83],v[20:23]
ds_read_b64_tr_b16 a[242:243],v3 offset:7832
v_lshl_add_u64 v[38:39],v[40:41],0,v[42:43]
v_fma_mixhi_f16 v8,v47,s99,0
v_mfma_f32_16x16x32_bf16 v[60:63],a[204:207],a[64:67],v[60:63]
s_waitcnt lgkmcnt(8)
ds_read_u16 v24,v24
v_lshl_add_u64 v[40:41],v[52:53],0,v[54:55]
v_lshlrev_b32_sdwa v25,1,v25 dst_sel:DWORD dst_unused:UNUSED_PAD src0_sel:DWORD src1_sel:WORD_1
v_pk_fma_f16 v9,v8,v8,s54
v_mfma_f32_16x16x32_bf16 v[60:63],a[200:203],a[48:51],v[60:63]
s_waitcnt lgkmcnt(3)
ds_read_u16_d16_hi v26,v26
v_or3_b32 v13,v14,v15,v13
v_lshlrev_b32_sdwa v27,1,v27 dst_sel:DWORD dst_unused:UNUSED_PAD src0_sel:DWORD src1_sel:WORD_1
v_mfma_f32_16x16x32_bf16 v[52:55],a[200:203],a[52:55],v[20:23]
ds_read_b64 v[14:15],v7 offset:6672
v_or_b32 v42,v56,v58
v_pk_mul_f16 v9,v8,v9
v_mfma_f32_16x16x32_bf16 v[52:55],a[204:207],a[68:71],v[52:55]
ds_read_u16 v25,v25
v_cvt_pk_bf16_f32 v43,v57,v59
v_lshlrev_b32_sdwa v28,1,v28 dst_sel:DWORD dst_unused:UNUSED_PAD src0_sel:DWORD src1_sel:WORD_1
v_mfma_f32_16x16x32_bf16 v[52:55],a[208:211],a[84:87],v[52:55]
v_exp_f16 v8,v9
v_mfma_f32_16x16x32_bf16 v[56:59],a[212:215],a[84:87],v[16:19]
ds_read_u16_d16_hi v27,v27
v_lshlrev_b32_sdwa v30,1,v30 dst_sel:DWORD dst_unused:UNUSED_PAD src0_sel:DWORD src1_sel:WORD_1
v_mfma_f32_16x16x32_bf16 v[56:59],a[220:223],a[68:71],v[56:59]
ds_read_u16 v28,v28
v_lshlrev_b32_sdwa v29,1,v29 dst_sel:DWORD dst_unused:UNUSED_PAD src0_sel:DWORD src1_sel:WORD_1
v_mfma_f32_16x16x32_bf16 v[56:59],a[216:219],a[52:55],v[56:59]
s_waitcnt lgkmcnt(3)
ds_read_u16_d16_hi v30,v30
v_or3_b32 v13,v14,v15,v13
v_mfma_f32_16x16x32_bf16 v[136:139],a[236:239],v[36:39],v[136:139]
ds_read_b64 v[14:15],v7 offset:7184
v_exp_f16_sdwa v8,v9 dst_sel:WORD_1 dst_unused:UNUSED_PRESERVE src0_sel:WORD_1
v_lshlrev_b32_sdwa v31,1,v31 dst_sel:DWORD dst_unused:UNUSED_PAD src0_sel:DWORD src1_sel:WORD_1
v_mfma_f32_16x16x32_bf16 v[140:143],a[236:239],v[40:43],v[140:143]
ds_read_u16 v29,v29
v_lshlrev_b32_sdwa v48,1,v48 dst_sel:DWORD dst_unused:UNUSED_PAD src0_sel:DWORD src1_sel:WORD_1
v_mfma_f32_16x16x32_bf16 v[68:71],a[244:247],v[40:43],v[68:71]
s_add_u32 m0,s57,14152
ds_read_u16_d16_hi v31,v31
v_pk_fma_f16 v8,v8,s55,s55
v_mfma_f32_16x16x32_bf16 v[64:67],a[244:247],v[36:39],v[64:67]
s_add_u32 s89,s56,5308416
v_rcp_f16 v9,v8
v_lshlrev_b32_sdwa v50,1,v50 dst_sel:DWORD dst_unused:UNUSED_PAD src0_sel:DWORD src1_sel:WORD_1
v_mfma_f32_16x16x32_bf16 v[144:147],a[228:231],v[36:39],v[144:147]
buffer_load_dwordx4 v6,s[36:39],s89 offen lds
v_lshlrev_b32_sdwa v49,1,v49 dst_sel:DWORD dst_unused:UNUSED_PAD src0_sel:DWORD src1_sel:WORD_1
v_mfma_f32_16x16x32_bf16 v[148:151],a[228:231],v[40:43],v[148:151]
s_waitcnt lgkmcnt(2)
ds_read_u16 v48,v48
v_or3_b32 v13,v14,v15,v13
v_mfma_f32_16x16x32_bf16 v[84:87],a[248:251],v[40:43],v[84:87]
ds_read_u16_d16_hi v50,v50
v_lshlrev_b32_sdwa v51,1,v51 dst_sel:DWORD dst_unused:UNUSED_PAD src0_sel:DWORD src1_sel:WORD_1
v_mfma_f32_16x16x32_bf16 v[80:83],a[248:251],v[36:39],v[80:83]
v_rcp_f16_sdwa v9,v8 dst_sel:WORD_1 dst_unused:UNUSED_PRESERVE src0_sel:WORD_1
v_mfma_f32_16x16x32_bf16 v[152:155],a[252:255],v[36:39],v[152:155]
ds_read_u16 v49,v49
ds_read_u16_d16_hi v51,v51
v_lshlrev_b32_sdwa v44,1,v44 dst_sel:DWORD dst_unused:UNUSED_PAD src0_sel:DWORD src1_sel:WORD_1
v_mfma_f32_16x16x32_bf16 v[156:159],a[252:255],v[40:43],v[156:159]
ds_read_u16 v44,v44
v_lshlrev_b32_sdwa v46,1,v46 dst_sel:DWORD dst_unused:UNUSED_PAD src0_sel:DWORD src1_sel:WORD_1
v_mfma_f32_16x16x32_bf16 v[100:103],a[240:243],v[40:43],v[100:103]
ds_read_u16_d16_hi v46,v46
v_fma_mix_f32 v45,v45,v9,0 op_sel_hi:[0,1,0]
v_mfma_f32_16x16x32_bf16 v[96:99],a[240:243],v[36:39],v[96:99]
ds_read_b64_tr_b16 a[192:193],v1 offset:8208
v_fma_mix_f32 v47,v47,v9,0 op_sel:[0,1,0] op_sel_hi:[0,1,0]
v_mfma_f32_16x16x32_bf16 v[40:43],a[216:219],a[56:59],v[16:19]
ds_read_b64_tr_b16 a[194:195],v1 offset:9232
v_lshlrev_b32_sdwa v32,1,v32 dst_sel:DWORD dst_unused:UNUSED_PAD src0_sel:DWORD src1_sel:WORD_1
v_mfma_f32_16x16x32_bf16 v[40:43],a[220:223],a[72:75],v[40:43]
ds_read_u16 v32,v32
v_fma_mixlo_f16 v8,v53,s99,0
v_mfma_f32_16x16x32_bf16 v[40:43],a[212:215],a[88:91],v[40:43]
v_lshl_add_u64 v[24:25],v[24:25],0,v[26:27]
v_lshlrev_b32_sdwa v34,1,v34 dst_sel:DWORD dst_unused:UNUSED_PAD src0_sel:DWORD src1_sel:WORD_1
v_mfma_f32_16x16x32_bf16 v[36:39],a[208:211],a[88:91],v[20:23]
s_waitcnt lgkmcnt(7)
ds_read_u16_d16_hi v34,v34
v_lshl_add_u64 v[26:27],v[28:29],0,v[30:31]
v_fma_mixhi_f16 v8,v55,s99,0
v_mfma_f32_16x16x32_bf16 v[36:39],a[204:207],a[72:75],v[36:39]
s_waitcnt lgkmcnt(6)
v_lshl_add_u64 v[28:29],v[48:49],0,v[50:51]
v_lshlrev_b32_sdwa v33,1,v33 dst_sel:DWORD dst_unused:UNUSED_PAD src0_sel:DWORD src1_sel:WORD_1
v_pk_fma_f16 v9,v8,v8,s54
v_mfma_f32_16x16x32_bf16 v[36:39],a[200:203],a[56:59],v[36:39]
ds_read_u16 v33,v33
v_lshlrev_b32_sdwa v35,1,v35 dst_sel:DWORD dst_unused:UNUSED_PAD src0_sel:DWORD src1_sel:WORD_1
v_mfma_f32_16x16x32_bf16 v[48:51],a[200:203],a[60:63],v[20:23]
s_waitcnt lgkmcnt(5)
ds_read_b128 v[20:23],v4 offset:1312
v_or_b32 v30,v44,v46
v_pk_mul_f16 v9,v8,v9
v_mfma_f32_16x16x32_bf16 v[48:51],a[204:207],a[76:79],v[48:51]
ds_read_u16_d16_hi v35,v35
v_cvt_pk_bf16_f32 v31,v45,v47
v_lshlrev_b32_sdwa v60,1,v60 dst_sel:DWORD dst_unused:UNUSED_PAD src0_sel:DWORD src1_sel:WORD_1
v_mfma_f32_16x16x32_bf16 v[48:51],a[208:211],a[92:95],v[48:51]
ds_read_b64_tr_b16 a[200:201],v1 offset:10256
v_exp_f16 v8,v9
v_mfma_f32_16x16x32_bf16 v[44:47],a[212:215],a[92:95],v[16:19]
ds_read_b128 v[16:19],v4 offset:1280
ds_read_b64_tr_b16 a[202:203],v1 offset:11280
v_lshlrev_b32_sdwa v62,1,v62 dst_sel:DWORD dst_unused:UNUSED_PAD src0_sel:DWORD src1_sel:WORD_1
v_mfma_f32_16x16x32_bf16 v[44:47],a[220:223],a[76:79],v[44:47]
ds_read_b64_tr_b16 a[204:205],v2 offset:10256
ds_read_b64_tr_b16 a[206:207],v2 offset:11280
v_lshlrev_b32_sdwa v61,1,v61 dst_sel:DWORD dst_unused:UNUSED_PAD src0_sel:DWORD src1_sel:WORD_1
v_mfma_f32_16x16x32_bf16 v[44:47],a[216:219],a[60:63],v[44:47]
ds_read_u16 v60,v60
v_exp_f16_sdwa v8,v9 dst_sel:WORD_1 dst_unused:UNUSED_PRESERVE src0_sel:WORD_1
v_mfma_f32_16x16x32_bf16 v[112:115],a[236:239],v[24:27],v[112:115]
ds_read_u16_d16_hi v62,v62
v_lshlrev_b32_sdwa v63,1,v63 dst_sel:DWORD dst_unused:UNUSED_PAD src0_sel:DWORD src1_sel:WORD_1
v_mfma_f32_16x16x32_bf16 v[116:119],a[236:239],v[28:31],v[116:119]
ds_read_u16 v61,v61
v_lshlrev_b32_sdwa v56,1,v56 dst_sel:DWORD dst_unused:UNUSED_PAD src0_sel:DWORD src1_sel:WORD_1
v_mfma_f32_16x16x32_bf16 v[76:79],a[244:247],v[28:31],v[76:79]
ds_read_u16_d16_hi v63,v63
v_pk_fma_f16 v8,v8,s55,s55
v_mfma_f32_16x16x32_bf16 v[72:75],a[244:247],v[24:27],v[72:75]
v_rcp_f16 v9,v8
v_lshlrev_b32_sdwa v58,1,v58 dst_sel:DWORD dst_unused:UNUSED_PAD src0_sel:DWORD src1_sel:WORD_1
v_mfma_f32_16x16x32_bf16 v[120:123],a[228:231],v[24:27],v[120:123]
ds_read_b64_tr_b16 a[208:209],v2 offset:8208
v_lshlrev_b32_sdwa v57,1,v57 dst_sel:DWORD dst_unused:UNUSED_PAD src0_sel:DWORD src1_sel:WORD_1
v_mfma_f32_16x16x32_bf16 v[124:127],a[228:231],v[28:31],v[124:127]
ds_read_b64_tr_b16 a[210:211],v2 offset:9232
v_lshlrev_b32_sdwa v59,1,v59 dst_sel:DWORD dst_unused:UNUSED_PAD src0_sel:DWORD src1_sel:WORD_1
v_mfma_f32_16x16x32_bf16 v[92:95],a[248:251],v[28:31],v[92:95]
ds_read_u16 v56,v56
v_lshlrev_b32_sdwa v52,1,v52 dst_sel:DWORD dst_unused:UNUSED_PAD src0_sel:DWORD src1_sel:WORD_1
v_mfma_f32_16x16x32_bf16 v[88:91],a[248:251],v[24:27],v[88:91]
ds_read_u16_d16_hi v58,v58
v_rcp_f16_sdwa v9,v8 dst_sel:WORD_1 dst_unused:UNUSED_PRESERVE src0_sel:WORD_1
v_mfma_f32_16x16x32_bf16 v[128:131],a[252:255],v[24:27],v[128:131]
ds_read_u16 v57,v57
ds_read_u16_d16_hi v59,v59
v_mfma_f32_16x16x32_bf16 v[132:135],a[252:255],v[28:31],v[132:135]
ds_read_b64_tr_b16 a[212:213],v2 offset:6160
ds_read_b64_tr_b16 a[214:215],v2 offset:7184
v_lshlrev_b32_sdwa v54,1,v54 dst_sel:DWORD dst_unused:UNUSED_PAD src0_sel:DWORD src1_sel:WORD_1
v_mfma_f32_16x16x32_bf16 v[108:111],a[240:243],v[28:31],v[108:111]
ds_read_u16 v52,v52
v_fma_mix_f32 v53,v53,v9,0 op_sel_hi:[0,1,0]
v_mfma_f32_16x16x32_bf16 v[104:107],a[240:243],v[24:27],v[104:107]
s_waitcnt lgkmcnt(9)
ds_read_u16_d16_hi v54,v54
v_fma_mix_f32 v55,v55,v9,0 op_sel:[0,1,0] op_sel_hi:[0,1,0]
v_mfma_f32_16x16x32_bf16 v[28:31],a[196:199],a[0:3],v[16:19]
s_add_u32 m0,s57,17688
v_lshlrev_b32_sdwa v40,1,v40 dst_sel:DWORD dst_unused:UNUSED_PAD src0_sel:DWORD src1_sel:WORD_1
v_fma_mixlo_f16 v8,v49,s99,0
v_mfma_f32_16x16x32_bf16 v[28:31],a[192:195],a[16:19],v[28:31]
s_add_u32 s89,s56,5406720
ds_read_u16 v40,v40
v_lshlrev_b32_sdwa v42,1,v42 dst_sel:DWORD dst_unused:UNUSED_PAD src0_sel:DWORD src1_sel:WORD_1
v_mfma_f32_16x16x32_bf16 v[28:31],a[200:203],a[32:35],v[28:31]
buffer_load_dwordx4 v6,s[36:39],s89 offen lds
v_lshl_add_u64 v[32:33],v[32:33],0,v[34:35]
v_mfma_f32_16x16x32_bf16 v[24:27],a[204:207],a[32:35],v[20:23]
s_waitcnt lgkmcnt(8)
ds_read_u16_d16_hi v42,v42
v_lshl_add_u64 v[34:35],v[60:61],0,v[62:63]
v_fma_mixhi_f16 v8,v51,s99,0
v_mfma_f32_16x16x32_bf16 v[24:27],a[208:211],a[16:19],v[24:27]
s_waitcnt lgkmcnt(4)
v_lshl_add_u64 v[60:61],v[56:57],0,v[58:59]
v_lshlrev_b32_sdwa v41,1,v41 dst_sel:DWORD dst_unused:UNUSED_PAD src0_sel:DWORD src1_sel:WORD_1
v_pk_fma_f16 v9,v8,v8,s54
v_mfma_f32_16x16x32_bf16 v[24:27],a[212:215],a[0:3],v[24:27]
ds_read_u16 v41,v41
v_lshlrev_b32_sdwa v43,1,v43 dst_sel:DWORD dst_unused:UNUSED_PAD src0_sel:DWORD src1_sel:WORD_1
v_mfma_f32_16x16x32_bf16 v[56:59],a[212:215],a[4:7],v[20:23]
s_waitcnt lgkmcnt(3)
ds_read_u16_d16_hi v43,v43
v_or_b32 v62,v52,v54
v_pk_mul_f16 v9,v8,v9
v_mfma_f32_16x16x32_bf16 v[56:59],a[208:211],a[20:23],v[56:59]
s_waitcnt vmcnt(15)
v_cvt_pk_bf16_f32 v63,v53,v55
v_lshlrev_b32_sdwa v36,1,v36 dst_sel:DWORD dst_unused:UNUSED_PAD src0_sel:DWORD src1_sel:WORD_1
v_mfma_f32_16x16x32_bf16 v[56:59],a[204:207],a[36:39],v[56:59]
s_barrier
v_exp_f16 v8,v9
v_mfma_f32_16x16x32_bf16 v[52:55],a[200:203],a[36:39],v[16:19]
ds_read_u16 v36,v36
v_lshlrev_b32_sdwa v38,1,v38 dst_sel:DWORD dst_unused:UNUSED_PAD src0_sel:DWORD src1_sel:WORD_1
v_mfma_f32_16x16x32_bf16 v[52:55],a[192:195],a[20:23],v[52:55]
s_add_u32 m0,s58,6160
ds_read_u16_d16_hi v38,v38
v_lshlrev_b32_sdwa v37,1,v37 dst_sel:DWORD dst_unused:UNUSED_PAD src0_sel:DWORD src1_sel:WORD_1
v_mfma_f32_16x16x32_bf16 v[52:55],a[196:199],a[4:7],v[52:55]
s_add_u32 s69,s69,s59
v_exp_f16_sdwa v8,v9 dst_sel:WORD_1 dst_unused:UNUSED_PRESERVE src0_sel:WORD_1
v_mfma_f32_16x16x32_bf16 v[232:235],a[236:239],v[32:35],v[232:235]
buffer_load_dwordx4 v5,s[44:47],s69 offen offset:0 lds
v_lshlrev_b32_sdwa v39,1,v39 dst_sel:DWORD dst_unused:UNUSED_PAD src0_sel:DWORD src1_sel:WORD_1
v_mfma_f32_16x16x32_bf16 v[236:239],a[236:239],v[60:63],v[236:239]
ds_read_u16 v37,v37
v_lshlrev_b32_sdwa v44,1,v44 dst_sel:DWORD dst_unused:UNUSED_PAD src0_sel:DWORD src1_sel:WORD_1
v_mfma_f32_16x16x32_bf16 v[164:167],a[244:247],v[60:63],v[164:167]
ds_read_u16_d16_hi v39,v39
v_pk_fma_f16 v8,v8,s55,s55
v_mfma_f32_16x16x32_bf16 v[160:163],a[244:247],v[32:35],v[160:163]
TP4402_%=:
s_cbranch_execz TP6558_%=
v_rcp_f16 v9,v8
v_lshlrev_b32_sdwa v46,1,v46 dst_sel:DWORD dst_unused:UNUSED_PAD src0_sel:DWORD src1_sel:WORD_1
v_mfma_f32_16x16x32_bf16 v[240:243],a[228:231],v[32:35],v[240:243]
s_add_u32 s69,s69,s53
ds_read_u16 v44,v44
v_lshlrev_b32_sdwa v45,1,v45 dst_sel:DWORD dst_unused:UNUSED_PAD src0_sel:DWORD src1_sel:WORD_1
v_mfma_f32_16x16x32_bf16 v[244:247],a[228:231],v[60:63],v[244:247]
buffer_load_dwordx4 v5,s[44:47],s69 offen offset:1024 lds
ds_read_u16_d16_hi v46,v46
v_mfma_f32_16x16x32_bf16 v[180:183],a[248:251],v[60:63],v[180:183]
ds_read_u16 v45,v45
v_lshlrev_b32_sdwa v47,1,v47 dst_sel:DWORD dst_unused:UNUSED_PAD src0_sel:DWORD src1_sel:WORD_1
v_mfma_f32_16x16x32_bf16 v[176:179],a[248:251],v[32:35],v[176:179]
v_rcp_f16_sdwa v9,v8 dst_sel:WORD_1 dst_unused:UNUSED_PRESERVE src0_sel:WORD_1
v_mfma_f32_16x16x32_bf16 v[248:251],a[252:255],v[32:35],v[248:251]
ds_read_u16_d16_hi v47,v47
v_lshlrev_b32_sdwa v48,1,v48 dst_sel:DWORD dst_unused:UNUSED_PAD src0_sel:DWORD src1_sel:WORD_1
v_mfma_f32_16x16x32_bf16 v[252:255],a[252:255],v[60:63],v[252:255]
s_add_u32 s69,s69,s53
ds_read_u16 v48,v48
v_lshlrev_b32_sdwa v50,1,v50 dst_sel:DWORD dst_unused:UNUSED_PAD src0_sel:DWORD src1_sel:WORD_1
v_mfma_f32_16x16x32_bf16 v[196:199],a[240:243],v[60:63],v[196:199]
buffer_load_dwordx4 v5,s[44:47],s69 offen offset:2048 lds
v_fma_mix_f32 v49,v49,v9,0 op_sel_hi:[0,1,0]
v_mfma_f32_16x16x32_bf16 v[192:195],a[240:243],v[32:35],v[192:195]
ds_read_u16_d16_hi v50,v50
v_fma_mix_f32 v51,v51,v9,0 op_sel:[0,1,0] op_sel_hi:[0,1,0]
v_mfma_f32_16x16x32_bf16 v[60:63],a[196:199],a[8:11],v[16:19]
ds_read_b64_tr_b16 a[224:225],v3 offset:12320
ds_read_b64_tr_b16 a[226:227],v3 offset:13856
v_lshlrev_b32_sdwa v28,1,v28 dst_sel:DWORD dst_unused:UNUSED_PAD src0_sel:DWORD src1_sel:WORD_1
v_mfma_f32_16x16x32_bf16 v[60:63],a[192:195],a[24:27],v[60:63]
ds_read_u16 v28,v28
ds_read_b64_tr_b16 a[232:233],v3 offset:12328
v_fma_mixlo_f16 v8,v57,s99,0
v_mfma_f32_16x16x32_bf16 v[60:63],a[200:203],a[40:43],v[60:63]
s_waitcnt lgkmcnt(8)
ds_read_b64_tr_b16 a[234:235],v3 offset:13864
v_lshl_add_u64 v[40:41],v[40:41],0,v[42:43]
v_lshlrev_b32_sdwa v30,1,v30 dst_sel:DWORD dst_unused:UNUSED_PAD src0_sel:DWORD src1_sel:WORD_1
v_mfma_f32_16x16x32_bf16 v[32:35],a[204:207],a[40:43],v[20:23]
ds_read_u16_d16_hi v30,v30
v_lshl_add_u64 v[42:43],v[36:37],0,v[38:39]
v_fma_mixhi_f16 v8,v59,s99,0
v_mfma_f32_16x16x32_bf16 v[32:35],a[208:211],a[24:27],v[32:35]
s_waitcnt lgkmcnt(8)
ds_read_b64_tr_b16 a[220:221],v1 offset:12320
v_lshl_add_u64 v[36:37],v[44:45],0,v[46:47]
v_lshlrev_b32_sdwa v29,1,v29 dst_sel:DWORD dst_unused:UNUSED_PAD src0_sel:DWORD src1_sel:WORD_1
v_pk_fma_f16 v9,v8,v8,s54
v_mfma_f32_16x16x32_bf16 v[32:35],a[212:215],a[8:11],v[32:35]
ds_read_u16 v29,v29
v_lshlrev_b32_sdwa v31,1,v31 dst_sel:DWORD dst_unused:UNUSED_PAD src0_sel:DWORD src1_sel:WORD_1
v_mfma_f32_16x16x32_bf16 v[44:47],a[212:215],a[12:15],v[20:23]
s_waitcnt lgkmcnt(8)
ds_read_u16_d16_hi v31,v31
v_or_b32 v38,v48,v50
v_pk_mul_f16 v9,v8,v9
v_mfma_f32_16x16x32_bf16 v[44:47],a[208:211],a[28:31],v[44:47]
ds_read_b64_tr_b16 a[222:223],v1 offset:13344
v_cvt_pk_bf16_f32 v39,v49,v51
v_lshlrev_b32_sdwa v24,1,v24 dst_sel:DWORD dst_unused:UNUSED_PAD src0_sel:DWORD src1_sel:WORD_1
v_mfma_f32_16x16x32_bf16 v[44:47],a[204:207],a[44:47],v[44:47]
v_exp_f16 v8,v9
v_mfma_f32_16x16x32_bf16 v[48:51],a[200:203],a[44:47],v[16:19]
ds_read_u16 v24,v24
v_lshlrev_b32_sdwa v26,1,v26 dst_sel:DWORD dst_unused:UNUSED_PAD src0_sel:DWORD src1_sel:WORD_1
v_mfma_f32_16x16x32_bf16 v[48:51],a[192:195],a[28:31],v[48:51]
ds_read_u16_d16_hi v26,v26
v_lshlrev_b32_sdwa v25,1,v25 dst_sel:DWORD dst_unused:UNUSED_PAD src0_sel:DWORD src1_sel:WORD_1
v_mfma_f32_16x16x32_bf16 v[48:51],a[196:199],a[12:15],v[48:51]
v_exp_f16_sdwa v8,v9 dst_sel:WORD_1 dst_unused:UNUSED_PRESERVE src0_sel:WORD_1
v_mfma_f32_16x16x32_bf16 v[208:211],a[236:239],v[40:43],v[208:211]
ds_read_u16 v25,v25
v_lshlrev_b32_sdwa v27,1,v27 dst_sel:DWORD dst_unused:UNUSED_PAD src0_sel:DWORD src1_sel:WORD_1
v_mfma_f32_16x16x32_bf16 v[212:215],a[236:239],v[36:39],v[212:215]
ds_read_u16_d16_hi v27,v27
v_lshlrev_b32_sdwa v52,1,v52 dst_sel:DWORD dst_unused:UNUSED_PAD src0_sel:DWORD src1_sel:WORD_1
v_mfma_f32_16x16x32_bf16 v[172:175],a[244:247],v[36:39],v[172:175]
ds_read_u16 v52,v52
v_pk_fma_f16 v8,v8,s55,s55
v_mfma_f32_16x16x32_bf16 v[168:171],a[244:247],v[40:43],v[168:171]
ds_read_b64_tr_b16 a[236:237],v3 offset:12384
v_rcp_f16 v9,v8
v_lshlrev_b32_sdwa v54,1,v54 dst_sel:DWORD dst_unused:UNUSED_PAD src0_sel:DWORD src1_sel:WORD_1
v_mfma_f32_16x16x32_bf16 v[216:219],a[228:231],v[40:43],v[216:219]
ds_read_u16_d16_hi v54,v54
v_lshlrev_b32_sdwa v53,1,v53 dst_sel:DWORD dst_unused:UNUSED_PAD src0_sel:DWORD src1_sel:WORD_1
v_mfma_f32_16x16x32_bf16 v[220:223],a[228:231],v[36:39],v[220:223]
ds_read_u16 v53,v53
ds_read_b64_tr_b16 a[238:239],v3 offset:13920
v_mfma_f32_16x16x32_bf16 v[188:191],a[248:251],v[36:39],v[188:191]
ds_read_b64_tr_b16 a[244:245],v3 offset:12392
v_lshlrev_b32_sdwa v55,1,v55 dst_sel:DWORD dst_unused:UNUSED_PAD src0_sel:DWORD src1_sel:WORD_1
v_mfma_f32_16x16x32_bf16 v[184:187],a[248:251],v[40:43],v[184:187]
ds_read_u16_d16_hi v55,v55
v_rcp_f16_sdwa v9,v8 dst_sel:WORD_1 dst_unused:UNUSED_PRESERVE src0_sel:WORD_1
v_mfma_f32_16x16x32_bf16 v[224:227],a[252:255],v[40:43],v[224:227]
ds_read_b64_tr_b16 a[246:247],v3 offset:13928
ds_read_b64_tr_b16 a[228:229],v3 offset:12448
v_lshlrev_b32_sdwa v56,1,v56 dst_sel:DWORD dst_unused:UNUSED_PAD src0_sel:DWORD src1_sel:WORD_1
v_mfma_f32_16x16x32_bf16 v[228:231],a[252:255],v[36:39],v[228:231]
ds_read_u16 v56,v56
v_lshlrev_b32_sdwa v58,1,v58 dst_sel:DWORD dst_unused:UNUSED_PAD src0_sel:DWORD src1_sel:WORD_1
v_mfma_f32_16x16x32_bf16 v[204:207],a[240:243],v[36:39],v[204:207]
ds_read_u16_d16_hi v58,v58
v_fma_mix_f32 v57,v57,v9,0 op_sel_hi:[0,1,0]
v_mfma_f32_16x16x32_bf16 v[200:203],a[240:243],v[40:43],v[200:203]
ds_read_b64_tr_b16 a[230:231],v3 offset:13984
v_fma_mix_f32 v59,v59,v9,0 op_sel:[0,1,0] op_sel_hi:[0,1,0]
v_mfma_f32_16x16x32_bf16 v[36:39],a[196:199],a[48:51],v[16:19]
s_add_u32 m0,s57,21216
ds_read_b64 v[14:15],v7 offset:12320
v_lshlrev_b32_sdwa v60,1,v60 dst_sel:DWORD dst_unused:UNUSED_PAD src0_sel:DWORD src1_sel:WORD_1
v_mfma_f32_16x16x32_bf16 v[36:39],a[192:195],a[64:67],v[36:39]
s_add_u32 s89,s56,5505024
ds_read_b64_tr_b16 a[248:249],v3 offset:12456
v_fma_mixlo_f16 v8,v45,s99,0
v_mfma_f32_16x16x32_bf16 v[36:39],a[200:203],a[80:83],v[36:39]
s_waitcnt lgkmcnt(9)
buffer_load_dwordx4 v6,s[36:39],s89 offen lds
v_lshl_add_u64 v[28:29],v[28:29],0,v[30:31]
v_lshlrev_b32_sdwa v62,1,v62 dst_sel:DWORD dst_unused:UNUSED_PAD src0_sel:DWORD src1_sel:WORD_1
v_mfma_f32_16x16x32_bf16 v[40:43],a[204:207],a[80:83],v[20:23]
ds_read_b64_tr_b16 a[250:251],v3 offset:13992
v_lshl_add_u64 v[30:31],v[24:25],0,v[26:27]
v_fma_mixhi_f16 v8,v47,s99,0
v_mfma_f32_16x16x32_bf16 v[40:43],a[208:211],a[64:67],v[40:43]
s_waitcnt lgkmcnt(8)
ds_read_u16 v60,v60
v_lshl_add_u64 v[24:25],v[52:53],0,v[54:55]
v_lshlrev_b32_sdwa v61,1,v61 dst_sel:DWORD dst_unused:UNUSED_PAD src0_sel:DWORD src1_sel:WORD_1
v_pk_fma_f16 v9,v8,v8,s54
v_mfma_f32_16x16x32_bf16 v[40:43],a[212:215],a[48:51],v[40:43]
s_waitcnt lgkmcnt(3)
ds_read_u16_d16_hi v62,v62
v_or3_b32 v13,v14,v15,v13
v_lshlrev_b32_sdwa v63,1,v63 dst_sel:DWORD dst_unused:UNUSED_PAD src0_sel:DWORD src1_sel:WORD_1
v_mfma_f32_16x16x32_bf16 v[52:55],a[212:215],a[52:55],v[20:23]
ds_read_b64 v[14:15],v7 offset:12832
v_or_b32 v26,v56,v58
v_pk_mul_f16 v9,v8,v9
v_mfma_f32_16x16x32_bf16 v[52:55],a[208:211],a[68:71],v[52:55]
ds_read_u16 v61,v61
v_cvt_pk_bf16_f32 v27,v57,v59
v_lshlrev_b32_sdwa v32,1,v32 dst_sel:DWORD dst_unused:UNUSED_PAD src0_sel:DWORD src1_sel:WORD_1
v_mfma_f32_16x16x32_bf16 v[52:55],a[204:207],a[84:87],v[52:55]
v_exp_f16 v8,v9
v_mfma_f32_16x16x32_bf16 v[56:59],a[200:203],a[84:87],v[16:19]
ds_read_u16_d16_hi v63,v63
v_lshlrev_b32_sdwa v34,1,v34 dst_sel:DWORD dst_unused:UNUSED_PAD src0_sel:DWORD src1_sel:WORD_1
v_mfma_f32_16x16x32_bf16 v[56:59],a[192:195],a[68:71],v[56:59]
ds_read_u16 v32,v32
v_lshlrev_b32_sdwa v33,1,v33 dst_sel:DWORD dst_unused:UNUSED_PAD src0_sel:DWORD src1_sel:WORD_1
v_mfma_f32_16x16x32_bf16 v[56:59],a[196:199],a[52:55],v[56:59]
s_waitcnt lgkmcnt(3)
ds_read_u16_d16_hi v34,v34
v_or3_b32 v13,v14,v15,v13
v_mfma_f32_16x16x32_bf16 v[136:139],a[224:227],v[28:31],v[136:139]
ds_read_b64 v[14:15],v7 offset:13344
v_exp_f16_sdwa v8,v9 dst_sel:WORD_1 dst_unused:UNUSED_PRESERVE src0_sel:WORD_1
v_lshlrev_b32_sdwa v35,1,v35 dst_sel:DWORD dst_unused:UNUSED_PAD src0_sel:DWORD src1_sel:WORD_1
v_mfma_f32_16x16x32_bf16 v[140:143],a[224:227],v[24:27],v[140:143]
ds_read_u16 v33,v33
v_lshlrev_b32_sdwa v48,1,v48 dst_sel:DWORD dst_unused:UNUSED_PAD src0_sel:DWORD src1_sel:WORD_1
v_mfma_f32_16x16x32_bf16 v[68:71],a[232:235],v[24:27],v[68:71]
ds_read_u16_d16_hi v35,v35
v_pk_fma_f16 v8,v8,s55,s55
v_mfma_f32_16x16x32_bf16 v[64:67],a[232:235],v[28:31],v[64:67]
v_rcp_f16 v9,v8
v_lshlrev_b32_sdwa v50,1,v50 dst_sel:DWORD dst_unused:UNUSED_PAD src0_sel:DWORD src1_sel:WORD_1
v_mfma_f32_16x16x32_bf16 v[144:147],a[236:239],v[28:31],v[144:147]
ds_read_u16 v48,v48
v_lshlrev_b32_sdwa v49,1,v49 dst_sel:DWORD dst_unused:UNUSED_PAD src0_sel:DWORD src1_sel:WORD_1
v_mfma_f32_16x16x32_bf16 v[148:151],a[236:239],v[24:27],v[148:151]
s_waitcnt lgkmcnt(3)
ds_read_u16_d16_hi v50,v50
v_or3_b32 v13,v14,v15,v13
v_mfma_f32_16x16x32_bf16 v[84:87],a[244:247],v[24:27],v[84:87]
ds_read_u16 v49,v49
v_and_b32 v13,0x40004000,v13
v_lshlrev_b32_sdwa v51,1,v51 dst_sel:DWORD dst_unused:UNUSED_PAD src0_sel:DWORD src1_sel:WORD_1
v_mfma_f32_16x16x32_bf16 v[80:83],a[244:247],v[28:31],v[80:83]
ds_read_u16_d16_hi v51,v51
v_cmp_ne_u32_e32 vcc,0,v13
s_nop 0
s_or_b64 s[90:91],s[90:91],vcc
v_mfma_f32_16x16x32_bf16 v[152:155],a[228:231],v[28:31],v[152:155]
ds_read_b64_tr_b16 a[216:217],v1 offset:14368
v_mbcnt_lo_u32_b32 v15,-1,0
v_lshlrev_b32_sdwa v44,1,v44 dst_sel:DWORD dst_unused:UNUSED_PAD src0_sel:DWORD src1_sel:WORD_1
v_mfma_f32_16x16x32_bf16 v[156:159],a[228:231],v[24:27],v[156:159]
ds_read_u16 v44,v44
v_mbcnt_hi_u32_b32 v15,-1,v15
v_lshlrev_b32_sdwa v46,1,v46 dst_sel:DWORD dst_unused:UNUSED_PAD src0_sel:DWORD src1_sel:WORD_1
v_mfma_f32_16x16x32_bf16 v[100:103],a[248:251],v[24:27],v[100:103]
ds_read_u16_d16_hi v46,v46
v_lshlrev_b32 v15,2,v15
v_rcp_f16_sdwa v9,v8 dst_sel:WORD_1 dst_unused:UNUSED_PRESERVE src0_sel:WORD_1
v_mfma_f32_16x16x32_bf16 v[96:99],a[248:251],v[28:31],v[96:99]
ds_read_b32 v7,v15 offset:14848
ds_read_b32 v13,v15 offset:15104
v_fma_mix_f32 v45,v45,v9,0 op_sel_hi:[0,1,0]
v_fma_mix_f32 v47,v47,v9,0 op_sel:[0,1,0] op_sel_hi:[0,1,0]
v_mfma_f32_16x16x32_bf16 v[24:27],a[196:199],a[56:59],v[16:19]
ds_read_b32 v14,v15 offset:14336
ds_read_b32 v15,v15 offset:14592
v_lshlrev_b32_sdwa v36,1,v36 dst_sel:DWORD dst_unused:UNUSED_PAD src0_sel:DWORD src1_sel:WORD_1
v_mfma_f32_16x16x32_bf16 v[24:27],a[192:195],a[72:75],v[24:27]
ds_read_b64_tr_b16 a[218:219],v1 offset:15392
v_fma_mixlo_f16 v8,v53,s99,0
v_mfma_f32_16x16x32_bf16 v[24:27],a[200:203],a[88:91],v[24:27]
ds_read_u16 v36,v36
v_lshl_add_u64 v[60:61],v[60:61],0,v[62:63]
v_lshlrev_b32_sdwa v38,1,v38 dst_sel:DWORD dst_unused:UNUSED_PAD src0_sel:DWORD src1_sel:WORD_1
v_mfma_f32_16x16x32_bf16 v[28:31],a[204:207],a[88:91],v[20:23]
s_waitcnt lgkmcnt(10)
ds_read_u16_d16_hi v38,v38
v_lshl_add_u64 v[62:63],v[32:33],0,v[34:35]
v_fma_mixhi_f16 v8,v55,s99,0
v_mfma_f32_16x16x32_bf16 v[28:31],a[208:211],a[72:75],v[28:31]
s_waitcnt lgkmcnt(10)
v_lshl_add_u64 v[32:33],v[48:49],0,v[50:51]
v_lshlrev_b32_sdwa v37,1,v37 dst_sel:DWORD dst_unused:UNUSED_PAD src0_sel:DWORD src1_sel:WORD_1
v_pk_fma_f16 v9,v8,v8,s54
v_mfma_f32_16x16x32_bf16 v[28:31],a[212:215],a[56:59],v[28:31]
ds_read_u16 v37,v37
v_lshlrev_b32_sdwa v39,1,v39 dst_sel:DWORD dst_unused:UNUSED_PAD src0_sel:DWORD src1_sel:WORD_1
v_mfma_f32_16x16x32_bf16 v[48:51],a[212:215],a[60:63],v[20:23]
s_waitcnt lgkmcnt(8)
ds_read_b128 v[20:23],v4 offset:1440
v_or_b32 v34,v44,v46
v_pk_mul_f16 v9,v8,v9
v_mfma_f32_16x16x32_bf16 v[48:51],a[208:211],a[76:79],v[48:51]
ds_read_u16_d16_hi v39,v39
v_cvt_pk_bf16_f32 v35,v45,v47
v_lshlrev_b32_sdwa v40,1,v40 dst_sel:DWORD dst_unused:UNUSED_PAD src0_sel:DWORD src1_sel:WORD_1
v_mfma_f32_16x16x32_bf16 v[48:51],a[204:207],a[92:95],v[48:51]
ds_read_b64_tr_b16 a[212:213],v1 offset:16416
v_exp_f16 v8,v9
v_mfma_f32_16x16x32_bf16 v[44:47],a[200:203],a[92:95],v[16:19]
ds_read_b128 v[16:19],v4 offset:1408
ds_read_b64_tr_b16 a[214:215],v1 offset:17440
v_lshlrev_b32_sdwa v42,1,v42 dst_sel:DWORD dst_unused:UNUSED_PAD src0_sel:DWORD src1_sel:WORD_1
v_mfma_f32_16x16x32_bf16 v[44:47],a[192:195],a[76:79],v[44:47]
ds_read_b64_tr_b16 a[208:209],v2 offset:16416
ds_read_b64_tr_b16 a[210:211],v2 offset:17440
v_lshlrev_b32_sdwa v41,1,v41 dst_sel:DWORD dst_unused:UNUSED_PAD src0_sel:DWORD src1_sel:WORD_1
v_mfma_f32_16x16x32_bf16 v[44:47],a[196:199],a[60:63],v[44:47]
ds_read_u16 v40,v40
v_exp_f16_sdwa v8,v9 dst_sel:WORD_1 dst_unused:UNUSED_PRESERVE src0_sel:WORD_1
v_mfma_f32_16x16x32_bf16 v[112:115],a[224:227],v[60:63],v[112:115]
ds_read_u16_d16_hi v42,v42
v_lshlrev_b32_sdwa v43,1,v43 dst_sel:DWORD dst_unused:UNUSED_PAD src0_sel:DWORD src1_sel:WORD_1
v_mfma_f32_16x16x32_bf16 v[116:119],a[224:227],v[32:35],v[116:119]
ds_read_u16 v41,v41
v_lshlrev_b32_sdwa v56,1,v56 dst_sel:DWORD dst_unused:UNUSED_PAD src0_sel:DWORD src1_sel:WORD_1
v_mfma_f32_16x16x32_bf16 v[76:79],a[232:235],v[32:35],v[76:79]
ds_read_u16_d16_hi v43,v43
v_pk_fma_f16 v8,v8,s55,s55
v_mfma_f32_16x16x32_bf16 v[72:75],a[232:235],v[60:63],v[72:75]
v_rcp_f16 v9,v8
v_lshlrev_b32_sdwa v58,1,v58 dst_sel:DWORD dst_unused:UNUSED_PAD src0_sel:DWORD src1_sel:WORD_1
v_mfma_f32_16x16x32_bf16 v[120:123],a[236:239],v[60:63],v[120:123]
ds_read_b64_tr_b16 a[204:205],v2 offset:14368
v_lshlrev_b32_sdwa v57,1,v57 dst_sel:DWORD dst_unused:UNUSED_PAD src0_sel:DWORD src1_sel:WORD_1
v_mfma_f32_16x16x32_bf16 v[124:127],a[236:239],v[32:35],v[124:127]
ds_read_b64_tr_b16 a[206:207],v2 offset:15392
v_lshlrev_b32_sdwa v59,1,v59 dst_sel:DWORD dst_unused:UNUSED_PAD src0_sel:DWORD src1_sel:WORD_1
v_mfma_f32_16x16x32_bf16 v[92:95],a[244:247],v[32:35],v[92:95]
ds_read_u16 v56,v56
v_lshlrev_b32_sdwa v52,1,v52 dst_sel:DWORD dst_unused:UNUSED_PAD src0_sel:DWORD src1_sel:WORD_1
v_mfma_f32_16x16x32_bf16 v[88:91],a[244:247],v[60:63],v[88:91]
ds_read_u16_d16_hi v58,v58
v_rcp_f16_sdwa v9,v8 dst_sel:WORD_1 dst_unused:UNUSED_PRESERVE src0_sel:WORD_1
v_mfma_f32_16x16x32_bf16 v[128:131],a[228:231],v[60:63],v[128:131]
ds_read_u16 v57,v57
ds_read_u16_d16_hi v59,v59
v_mfma_f32_16x16x32_bf16 v[132:135],a[228:231],v[32:35],v[132:135]
ds_read_b64_tr_b16 a[200:201],v2 offset:12320
ds_read_b64_tr_b16 a[202:203],v2 offset:13344
v_lshlrev_b32_sdwa v54,1,v54 dst_sel:DWORD dst_unused:UNUSED_PAD src0_sel:DWORD src1_sel:WORD_1
v_mfma_f32_16x16x32_bf16 v[108:111],a[248:251],v[32:35],v[108:111]
ds_read_u16 v52,v52
v_fma_mix_f32 v53,v53,v9,0 op_sel_hi:[0,1,0]
v_mfma_f32_16x16x32_bf16 v[104:107],a[248:251],v[60:63],v[104:107]
s_waitcnt lgkmcnt(9)
ds_read_u16_d16_hi v54,v54
v_fma_mix_f32 v55,v55,v9,0 op_sel:[0,1,0] op_sel_hi:[0,1,0]
v_mfma_f32_16x16x32_bf16 v[32:35],a[220:223],a[0:3],v[16:19]
v_lshlrev_b32_sdwa v24,1,v24 dst_sel:DWORD dst_unused:UNUSED_PAD src0_sel:DWORD src1_sel:WORD_1
v_fma_mixlo_f16 v8,v49,s99,0
v_mfma_f32_16x16x32_bf16 v[32:35],a[216:219],a[16:19],v[32:35]
ds_read_u16 v24,v24
v_lshlrev_b32_sdwa v26,1,v26 dst_sel:DWORD dst_unused:UNUSED_PAD src0_sel:DWORD src1_sel:WORD_1
v_mfma_f32_16x16x32_bf16 v[32:35],a[212:215],a[32:35],v[32:35]
ds_read_u16_d16_hi v26,v26
v_lshl_add_u64 v[36:37],v[36:37],0,v[38:39]
v_mfma_f32_16x16x32_bf16 v[60:63],a[208:211],a[32:35],v[20:23]
s_waitcnt lgkmcnt(9)
v_lshl_add_u64 v[38:39],v[40:41],0,v[42:43]
v_fma_mixhi_f16 v8,v51,s99,0
v_mfma_f32_16x16x32_bf16 v[60:63],a[204:207],a[16:19],v[60:63]
s_waitcnt lgkmcnt(4)
v_lshl_add_u64 v[40:41],v[56:57],0,v[58:59]
v_lshlrev_b32_sdwa v25,1,v25 dst_sel:DWORD dst_unused:UNUSED_PAD src0_sel:DWORD src1_sel:WORD_1
v_pk_fma_f16 v9,v8,v8,s54
v_mfma_f32_16x16x32_bf16 v[60:63],a[200:203],a[0:3],v[60:63]
ds_read_u16 v25,v25
v_lshlrev_b32_sdwa v27,1,v27 dst_sel:DWORD dst_unused:UNUSED_PAD src0_sel:DWORD src1_sel:WORD_1
v_mfma_f32_16x16x32_bf16 v[56:59],a[200:203],a[4:7],v[20:23]
s_waitcnt lgkmcnt(3)
ds_read_u16_d16_hi v27,v27
v_or_b32 v42,v52,v54
v_pk_mul_f16 v9,v8,v9
v_mfma_f32_16x16x32_bf16 v[56:59],a[204:207],a[20:23],v[56:59]
s_waitcnt vmcnt(13)
v_cvt_pk_bf16_f32 v43,v53,v55
v_lshlrev_b32_sdwa v28,1,v28 dst_sel:DWORD dst_unused:UNUSED_PAD src0_sel:DWORD src1_sel:WORD_1
v_mfma_f32_16x16x32_bf16 v[56:59],a[208:211],a[36:39],v[56:59]
s_barrier
v_exp_f16 v8,v9
v_mfma_f32_16x16x32_bf16 v[52:55],a[212:215],a[36:39],v[16:19]
ds_read_u16 v28,v28
v_lshlrev_b32_sdwa v30,1,v30 dst_sel:DWORD dst_unused:UNUSED_PAD src0_sel:DWORD src1_sel:WORD_1
v_mfma_f32_16x16x32_bf16 v[52:55],a[216:219],a[20:23],v[52:55]
s_add_u32 m0,s58,12320
ds_read_u16_d16_hi v30,v30
v_lshlrev_b32_sdwa v29,1,v29 dst_sel:DWORD dst_unused:UNUSED_PAD src0_sel:DWORD src1_sel:WORD_1
v_mfma_f32_16x16x32_bf16 v[52:55],a[220:223],a[4:7],v[52:55]
s_add_u32 s69,s69,s59
v_exp_f16_sdwa v8,v9 dst_sel:WORD_1 dst_unused:UNUSED_PRESERVE src0_sel:WORD_1
v_mfma_f32_16x16x32_bf16 v[232:235],a[224:227],v[36:39],v[232:235]
buffer_load_dwordx4 v5,s[44:47],s69 offen offset:0 lds
v_lshlrev_b32_sdwa v31,1,v31 dst_sel:DWORD dst_unused:UNUSED_PAD src0_sel:DWORD src1_sel:WORD_1
v_mfma_f32_16x16x32_bf16 v[236:239],a[224:227],v[40:43],v[236:239]
ds_read_u16 v29,v29
v_lshlrev_b32_sdwa v44,1,v44 dst_sel:DWORD dst_unused:UNUSED_PAD src0_sel:DWORD src1_sel:WORD_1
v_mfma_f32_16x16x32_bf16 v[164:167],a[232:235],v[40:43],v[164:167]
ds_read_u16_d16_hi v31,v31
v_pk_fma_f16 v8,v8,s55,s55
v_mfma_f32_16x16x32_bf16 v[160:163],a[232:235],v[36:39],v[160:163]
v_rcp_f16 v9,v8
v_lshlrev_b32_sdwa v46,1,v46 dst_sel:DWORD dst_unused:UNUSED_PAD src0_sel:DWORD src1_sel:WORD_1
v_mfma_f32_16x16x32_bf16 v[240:243],a[236:239],v[36:39],v[240:243]
s_add_u32 s69,s69,s53
ds_read_u16 v44,v44
v_lshlrev_b32_sdwa v45,1,v45 dst_sel:DWORD dst_unused:UNUSED_PAD src0_sel:DWORD src1_sel:WORD_1
v_mfma_f32_16x16x32_bf16 v[244:247],a[236:239],v[40:43],v[244:247]
buffer_load_dwordx4 v5,s[44:47],s69 offen offset:1024 lds
ds_read_u16_d16_hi v46,v46
v_mfma_f32_16x16x32_bf16 v[180:183],a[244:247],v[40:43],v[180:183]
ds_read_u16 v45,v45
v_lshlrev_b32_sdwa v47,1,v47 dst_sel:DWORD dst_unused:UNUSED_PAD src0_sel:DWORD src1_sel:WORD_1
v_mfma_f32_16x16x32_bf16 v[176:179],a[244:247],v[36:39],v[176:179]
v_rcp_f16_sdwa v9,v8 dst_sel:WORD_1 dst_unused:UNUSED_PRESERVE src0_sel:WORD_1
v_mfma_f32_16x16x32_bf16 v[248:251],a[228:231],v[36:39],v[248:251]
ds_read_u16_d16_hi v47,v47
v_lshlrev_b32_sdwa v48,1,v48 dst_sel:DWORD dst_unused:UNUSED_PAD src0_sel:DWORD src1_sel:WORD_1
v_mfma_f32_16x16x32_bf16 v[252:255],a[228:231],v[40:43],v[252:255]
s_add_u32 s69,s69,s53
ds_read_u16 v48,v48
v_lshlrev_b32_sdwa v50,1,v50 dst_sel:DWORD dst_unused:UNUSED_PAD src0_sel:DWORD src1_sel:WORD_1
v_mfma_f32_16x16x32_bf16 v[196:199],a[248:251],v[40:43],v[196:199]
buffer_load_dwordx4 v5,s[44:47],s69 offen offset:2048 lds
v_fma_mix_f32 v49,v49,v9,0 op_sel_hi:[0,1,0]
v_mfma_f32_16x16x32_bf16 v[192:195],a[248:251],v[36:39],v[192:195]
ds_read_u16_d16_hi v50,v50
v_fma_mix_f32 v51,v51,v9,0 op_sel:[0,1,0] op_sel_hi:[0,1,0]
v_mfma_f32_16x16x32_bf16 v[40:43],a[220:223],a[8:11],v[16:19]
ds_read_b64_tr_b16 a[252:253],v3 offset:18480
ds_read_b64_tr_b16 a[254:255],v3 offset:20016
v_lshlrev_b32_sdwa v32,1,v32 dst_sel:DWORD dst_unused:UNUSED_PAD src0_sel:DWORD src1_sel:WORD_1
v_mfma_f32_16x16x32_bf16 v[40:43],a[216:219],a[24:27],v[40:43]
ds_read_u16 v32,v32
ds_read_b64_tr_b16 a[240:241],v3 offset:18488
v_fma_mixlo_f16 v8,v57,s99,0
v_mfma_f32_16x16x32_bf16 v[40:43],a[212:215],a[40:43],v[40:43]
s_waitcnt lgkmcnt(8)
ds_read_b64_tr_b16 a[242:243],v3 offset:20024
v_lshl_add_u64 v[24:25],v[24:25],0,v[26:27]
v_lshlrev_b32_sdwa v34,1,v34 dst_sel:DWORD dst_unused:UNUSED_PAD src0_sel:DWORD src1_sel:WORD_1
v_mfma_f32_16x16x32_bf16 v[36:39],a[208:211],a[40:43],v[20:23]
ds_read_u16_d16_hi v34,v34
v_lshl_add_u64 v[26:27],v[28:29],0,v[30:31]
v_fma_mixhi_f16 v8,v59,s99,0
v_mfma_f32_16x16x32_bf16 v[36:39],a[204:207],a[24:27],v[36:39]
s_waitcnt lgkmcnt(8)
ds_read_b64_tr_b16 a[192:193],v1 offset:18480
v_lshl_add_u64 v[28:29],v[44:45],0,v[46:47]
v_lshlrev_b32_sdwa v33,1,v33 dst_sel:DWORD dst_unused:UNUSED_PAD src0_sel:DWORD src1_sel:WORD_1
v_pk_fma_f16 v9,v8,v8,s54
v_mfma_f32_16x16x32_bf16 v[36:39],a[200:203],a[8:11],v[36:39]
ds_read_u16 v33,v33
v_lshlrev_b32_sdwa v35,1,v35 dst_sel:DWORD dst_unused:UNUSED_PAD src0_sel:DWORD src1_sel:WORD_1
v_mfma_f32_16x16x32_bf16 v[44:47],a[200:203],a[12:15],v[20:23]
s_waitcnt lgkmcnt(8)
ds_read_u16_d16_hi v35,v35
v_or_b32 v30,v48,v50
v_pk_mul_f16 v9,v8,v9
v_mfma_f32_16x16x32_bf16 v[44:47],a[204:207],a[28:31],v[44:47]
ds_read_b64_tr_b16 a[194:195],v1 offset:19504
v_cvt_pk_bf16_f32 v31,v49,v51
v_lshlrev_b32_sdwa v60,1,v60 dst_sel:DWORD dst_unused:UNUSED_PAD src0_sel:DWORD src1_sel:WORD_1
v_mfma_f32_16x16x32_bf16 v[44:47],a[208:211],a[44:47],v[44:47]
v_exp_f16 v8,v9
v_mfma_f32_16x16x32_bf16 v[48:51],a[212:215],a[44:47],v[16:19]
ds_read_u16 v60,v60
v_lshlrev_b32_sdwa v62,1,v62 dst_sel:DWORD dst_unused:UNUSED_PAD src0_sel:DWORD src1_sel:WORD_1
v_mfma_f32_16x16x32_bf16 v[48:51],a[216:219],a[28:31],v[48:51]
ds_read_u16_d16_hi v62,v62
v_lshlrev_b32_sdwa v61,1,v61 dst_sel:DWORD dst_unused:UNUSED_PAD src0_sel:DWORD src1_sel:WORD_1
v_mfma_f32_16x16x32_bf16 v[48:51],a[220:223],a[12:15],v[48:51]
v_exp_f16_sdwa v8,v9 dst_sel:WORD_1 dst_unused:UNUSED_PRESERVE src0_sel:WORD_1
v_mfma_f32_16x16x32_bf16 v[208:211],a[224:227],v[24:27],v[208:211]
ds_read_u16 v61,v61
v_lshlrev_b32_sdwa v63,1,v63 dst_sel:DWORD dst_unused:UNUSED_PAD src0_sel:DWORD src1_sel:WORD_1
v_mfma_f32_16x16x32_bf16 v[212:215],a[224:227],v[28:31],v[212:215]
ds_read_u16_d16_hi v63,v63
v_lshlrev_b32_sdwa v52,1,v52 dst_sel:DWORD dst_unused:UNUSED_PAD src0_sel:DWORD src1_sel:WORD_1
v_mfma_f32_16x16x32_bf16 v[172:175],a[232:235],v[28:31],v[172:175]
ds_read_u16 v52,v52
v_pk_fma_f16 v8,v8,s55,s55
v_mfma_f32_16x16x32_bf16 v[168:171],a[232:235],v[24:27],v[168:171]
ds_read_b64_tr_b16 a[224:225],v3 offset:18544
v_rcp_f16 v9,v8
v_lshlrev_b32_sdwa v54,1,v54 dst_sel:DWORD dst_unused:UNUSED_PAD src0_sel:DWORD src1_sel:WORD_1
v_mfma_f32_16x16x32_bf16 v[216:219],a[236:239],v[24:27],v[216:219]
ds_read_u16_d16_hi v54,v54
v_lshlrev_b32_sdwa v53,1,v53 dst_sel:DWORD dst_unused:UNUSED_PAD src0_sel:DWORD src1_sel:WORD_1
v_mfma_f32_16x16x32_bf16 v[220:223],a[236:239],v[28:31],v[220:223]
ds_read_u16 v53,v53
ds_read_b64_tr_b16 a[226:227],v3 offset:20080
v_mfma_f32_16x16x32_bf16 v[188:191],a[244:247],v[28:31],v[188:191]
ds_read_b64_tr_b16 a[232:233],v3 offset:18552
v_lshlrev_b32_sdwa v55,1,v55 dst_sel:DWORD dst_unused:UNUSED_PAD src0_sel:DWORD src1_sel:WORD_1
v_mfma_f32_16x16x32_bf16 v[184:187],a[244:247],v[24:27],v[184:187]
ds_read_u16_d16_hi v55,v55
v_rcp_f16_sdwa v9,v8 dst_sel:WORD_1 dst_unused:UNUSED_PRESERVE src0_sel:WORD_1
v_mfma_f32_16x16x32_bf16 v[224:227],a[228:231],v[24:27],v[224:227]
ds_read_b64_tr_b16 a[234:235],v3 offset:20088
ds_read_b64_tr_b16 a[236:237],v3 offset:18608
v_lshlrev_b32_sdwa v56,1,v56 dst_sel:DWORD dst_unused:UNUSED_PAD src0_sel:DWORD src1_sel:WORD_1
v_mfma_f32_16x16x32_bf16 v[228:231],a[228:231],v[28:31],v[228:231]
ds_read_u16 v56,v56
v_lshlrev_b32_sdwa v58,1,v58 dst_sel:DWORD dst_unused:UNUSED_PAD src0_sel:DWORD src1_sel:WORD_1
v_mfma_f32_16x16x32_bf16 v[204:207],a[248:251],v[28:31],v[204:207]
ds_read_u16_d16_hi v58,v58
v_fma_mix_f32 v57,v57,v9,0 op_sel_hi:[0,1,0]
v_mfma_f32_16x16x32_bf16 v[200:203],a[248:251],v[24:27],v[200:203]
ds_read_b64_tr_b16 a[238:239],v3 offset:20144
v_fma_mix_f32 v59,v59,v9,0 op_sel:[0,1,0] op_sel_hi:[0,1,0]
v_mfma_f32_16x16x32_bf16 v[28:31],a[220:223],a[48:51],v[16:19]
ds_read_b64_tr_b16 a[244:245],v3 offset:18616
v_lshlrev_b32_sdwa v40,1,v40 dst_sel:DWORD dst_unused:UNUSED_PAD src0_sel:DWORD src1_sel:WORD_1
v_mfma_f32_16x16x32_bf16 v[28:31],a[216:219],a[64:67],v[28:31]
ds_read_b64_tr_b16 a[246:247],v3 offset:20152
v_fma_mixlo_f16 v8,v45,s99,0
v_mfma_f32_16x16x32_bf16 v[28:31],a[212:215],a[80:83],v[28:31]
s_waitcnt lgkmcnt(9)
ds_read_u16 v40,v40
v_lshl_add_u64 v[32:33],v[32:33],0,v[34:35]
v_lshlrev_b32_sdwa v42,1,v42 dst_sel:DWORD dst_unused:UNUSED_PAD src0_sel:DWORD src1_sel:WORD_1
v_mfma_f32_16x16x32_bf16 v[24:27],a[208:211],a[80:83],v[20:23]
ds_read_u16_d16_hi v42,v42
v_lshl_add_u64 v[34:35],v[60:61],0,v[62:63]
v_fma_mixhi_f16 v8,v47,s99,0
v_mfma_f32_16x16x32_bf16 v[24:27],a[204:207],a[64:67],v[24:27]
s_waitcnt lgkmcnt(9)
ds_read_b64_tr_b16 a[196:197],v1 offset:20528
v_lshl_add_u64 v[60:61],v[52:53],0,v[54:55]
v_lshlrev_b32_sdwa v41,1,v41 dst_sel:DWORD dst_unused:UNUSED_PAD src0_sel:DWORD src1_sel:WORD_1
v_pk_fma_f16 v9,v8,v8,s54
v_mfma_f32_16x16x32_bf16 v[24:27],a[200:203],a[48:51],v[24:27]
ds_read_u16 v41,v41
v_lshlrev_b32_sdwa v43,1,v43 dst_sel:DWORD dst_unused:UNUSED_PAD src0_sel:DWORD src1_sel:WORD_1
v_mfma_f32_16x16x32_bf16 v[52:55],a[200:203],a[52:55],v[20:23]
s_waitcnt lgkmcnt(7)
ds_read_u16_d16_hi v43,v43
v_or_b32 v62,v56,v58
v_pk_mul_f16 v9,v8,v9
v_mfma_f32_16x16x32_bf16 v[52:55],a[204:207],a[68:71],v[52:55]
ds_read_b64_tr_b16 a[198:199],v1 offset:21552
v_cvt_pk_bf16_f32 v63,v57,v59
v_lshlrev_b32_sdwa v36,1,v36 dst_sel:DWORD dst_unused:UNUSED_PAD src0_sel:DWORD src1_sel:WORD_1
v_mfma_f32_16x16x32_bf16 v[52:55],a[208:211],a[84:87],v[52:55]
v_exp_f16 v8,v9
v_mfma_f32_16x16x32_bf16 v[56:59],a[212:215],a[84:87],v[16:19]
ds_read_u16 v36,v36
v_lshlrev_b32_sdwa v38,1,v38 dst_sel:DWORD dst_unused:UNUSED_PAD src0_sel:DWORD src1_sel:WORD_1
v_mfma_f32_16x16x32_bf16 v[56:59],a[216:219],a[68:71],v[56:59]
ds_read_u16_d16_hi v38,v38
v_lshlrev_b32_sdwa v37,1,v37 dst_sel:DWORD dst_unused:UNUSED_PAD src0_sel:DWORD src1_sel:WORD_1
v_mfma_f32_16x16x32_bf16 v[56:59],a[220:223],a[52:55],v[56:59]
v_exp_f16_sdwa v8,v9 dst_sel:WORD_1 dst_unused:UNUSED_PRESERVE src0_sel:WORD_1
v_mfma_f32_16x16x32_bf16 v[136:139],a[252:255],v[32:35],v[136:139]
ds_read_u16 v37,v37
v_lshlrev_b32_sdwa v39,1,v39 dst_sel:DWORD dst_unused:UNUSED_PAD src0_sel:DWORD src1_sel:WORD_1
v_mfma_f32_16x16x32_bf16 v[140:143],a[252:255],v[60:63],v[140:143]
ds_read_u16_d16_hi v39,v39
v_lshlrev_b32_sdwa v48,1,v48 dst_sel:DWORD dst_unused:UNUSED_PAD src0_sel:DWORD src1_sel:WORD_1
v_mfma_f32_16x16x32_bf16 v[68:71],a[240:243],v[60:63],v[68:71]
ds_read_u16 v48,v48
v_pk_fma_f16 v8,v8,s55,s55
v_mfma_f32_16x16x32_bf16 v[64:67],a[240:243],v[32:35],v[64:67]
v_rcp_f16 v9,v8
v_lshlrev_b32_sdwa v50,1,v50 dst_sel:DWORD dst_unused:UNUSED_PAD src0_sel:DWORD src1_sel:WORD_1
v_mfma_f32_16x16x32_bf16 v[144:147],a[224:227],v[32:35],v[144:147]
ds_read_u16_d16_hi v50,v50
v_lshlrev_b32_sdwa v49,1,v49 dst_sel:DWORD dst_unused:UNUSED_PAD src0_sel:DWORD src1_sel:WORD_1
v_mfma_f32_16x16x32_bf16 v[148:151],a[224:227],v[60:63],v[148:151]
ds_read_u16 v49,v49
v_lshlrev_b32_sdwa v51,1,v51 dst_sel:DWORD dst_unused:UNUSED_PAD src0_sel:DWORD src1_sel:WORD_1
v_mfma_f32_16x16x32_bf16 v[84:87],a[232:235],v[60:63],v[84:87]
ds_read_u16_d16_hi v51,v51
v_lshlrev_b32_sdwa v44,1,v44 dst_sel:DWORD dst_unused:UNUSED_PAD src0_sel:DWORD src1_sel:WORD_1
v_mfma_f32_16x16x32_bf16 v[80:83],a[232:235],v[32:35],v[80:83]
s_waitcnt lgkmcnt(6)
v_rcp_f16_sdwa v9,v8 dst_sel:WORD_1 dst_unused:UNUSED_PRESERVE src0_sel:WORD_1
v_mfma_f32_16x16x32_bf16 v[152:155],a[236:239],v[32:35],v[152:155]
ds_read_u16 v44,v44
v_lshlrev_b32_sdwa v46,1,v46 dst_sel:DWORD dst_unused:UNUSED_PAD src0_sel:DWORD src1_sel:WORD_1
v_mfma_f32_16x16x32_bf16 v[156:159],a[236:239],v[60:63],v[156:159]
ds_read_u16_d16_hi v46,v46
v_fma_mix_f32 v45,v45,v9,0 op_sel_hi:[0,1,0]
v_mfma_f32_16x16x32_bf16 v[100:103],a[244:247],v[60:63],v[100:103]
v_fma_mix_f32 v47,v47,v9,0 op_sel:[0,1,0] op_sel_hi:[0,1,0]
v_mfma_f32_16x16x32_bf16 v[96:99],a[244:247],v[32:35],v[96:99]
v_mfma_f32_16x16x32_bf16 v[60:63],a[220:223],a[56:59],v[16:19]
v_mul_f32_dpp v136,v14,v136 row_newbcast:0 row_mask:0xf bank_mask:0xf
s_add_u32 s62,s61,0
v_mul_f32_dpp v137,v14,v137 row_newbcast:1 row_mask:0xf bank_mask:0xf
v_mul_f32_dpp v138,v14,v138 row_newbcast:2 row_mask:0xf bank_mask:0xf
v_lshlrev_b32_sdwa v28,1,v28 dst_sel:DWORD dst_unused:UNUSED_PAD src0_sel:DWORD src1_sel:WORD_1
v_mfma_f32_16x16x32_bf16 v[60:63],a[216:219],a[72:75],v[60:63]
ds_read_u16 v28,v28
v_mul_f32_dpp v139,v14,v139 row_newbcast:3 row_mask:0xf bank_mask:0xf
v_mul_f32_dpp v64,v14,v64 row_newbcast:4 row_mask:0xf bank_mask:0xf
v_fma_mixlo_f16 v8,v53,s99,0
v_mul_f32_dpp v65,v14,v65 row_newbcast:5 row_mask:0xf bank_mask:0xf
v_mfma_f32_16x16x32_bf16 v[60:63],a[212:215],a[88:91],v[60:63]
TP4943_%=:
s_cbranch_execz TP7094_%=
v_lshl_add_u64 v[40:41],v[40:41],0,v[42:43]
v_mul_f32_dpp v66,v14,v66 row_newbcast:6 row_mask:0xf bank_mask:0xf
v_mul_f32_dpp v67,v14,v67 row_newbcast:7 row_mask:0xf bank_mask:0xf
v_lshlrev_b32_sdwa v30,1,v30 dst_sel:DWORD dst_unused:UNUSED_PAD src0_sel:DWORD src1_sel:WORD_1
v_cvt_pk_bf16_f32 v136,v136,v137
v_mfma_f32_16x16x32_bf16 v[32:35],a[208:211],a[88:91],v[20:23]
s_waitcnt lgkmcnt(3)
ds_read_u16_d16_hi v30,v30
v_lshl_add_u64 v[42:43],v[36:37],0,v[38:39]
v_cvt_pk_bf16_f32 v137,v138,v139
v_cvt_pk_bf16_f32 v138,v64,v65
v_fma_mixhi_f16 v8,v55,s99,0
v_cvt_pk_bf16_f32 v139,v66,v67
v_mfma_f32_16x16x32_bf16 v[32:35],a[204:207],a[72:75],v[32:35]
buffer_store_dwordx4 v[136:139],v7,s[40:43],s62 offen offset:0 sc1
v_lshl_add_u64 v[36:37],v[48:49],0,v[50:51]
v_mul_f32_dpp v144,v14,v144 row_newbcast:8 row_mask:0xf bank_mask:0xf
v_mul_f32_dpp v145,v14,v145 row_newbcast:9 row_mask:0xf bank_mask:0xf
v_lshlrev_b32_sdwa v29,1,v29 dst_sel:DWORD dst_unused:UNUSED_PAD src0_sel:DWORD src1_sel:WORD_1
v_pk_fma_f16 v9,v8,v8,s54
v_mul_f32_dpp v146,v14,v146 row_newbcast:10 row_mask:0xf bank_mask:0xf
v_mfma_f32_16x16x32_bf16 v[32:35],a[200:203],a[56:59],v[32:35]
ds_read_u16 v29,v29
v_mul_f32_dpp v147,v14,v147 row_newbcast:11 row_mask:0xf bank_mask:0xf
v_mul_f32_dpp v80,v14,v80 row_newbcast:12 row_mask:0xf bank_mask:0xf
v_lshlrev_b32_sdwa v31,1,v31 dst_sel:DWORD dst_unused:UNUSED_PAD src0_sel:DWORD src1_sel:WORD_1
v_mul_f32_dpp v81,v14,v81 row_newbcast:13 row_mask:0xf bank_mask:0xf
v_mfma_f32_16x16x32_bf16 v[48:51],a[200:203],a[60:63],v[20:23]
s_waitcnt lgkmcnt(3)
ds_read_b128 v[20:23],v4 offset:32
v_or_b32 v38,v44,v46
v_mul_f32_dpp v82,v14,v82 row_newbcast:14 row_mask:0xf bank_mask:0xf
v_mul_f32_dpp v83,v14,v83 row_newbcast:15 row_mask:0xf bank_mask:0xf
v_pk_mul_f16 v9,v8,v9
v_cvt_pk_bf16_f32 v144,v144,v145
v_mfma_f32_16x16x32_bf16 v[48:51],a[204:207],a[76:79],v[48:51]
ds_read_u16_d16_hi v31,v31
v_cvt_pk_bf16_f32 v39,v45,v47
v_cvt_pk_bf16_f32 v145,v146,v147
v_cvt_pk_bf16_f32 v146,v80,v81
v_lshlrev_b32_sdwa v24,1,v24 dst_sel:DWORD dst_unused:UNUSED_PAD src0_sel:DWORD src1_sel:WORD_1
v_cvt_pk_bf16_f32 v147,v82,v83
v_mfma_f32_16x16x32_bf16 v[48:51],a[208:211],a[92:95],v[48:51]
ds_read_b64_tr_b16 a[200:201],v1 offset:22576
v_exp_f16 v8,v9
v_mul_f32_dpp v152,v15,v152 row_newbcast:0 row_mask:0xf bank_mask:0xf
v_mul_f32_dpp v153,v15,v153 row_newbcast:1 row_mask:0xf bank_mask:0xf
v_mul_f32_dpp v154,v15,v154 row_newbcast:2 row_mask:0xf bank_mask:0xf
v_mfma_f32_16x16x32_bf16 v[44:47],a[212:215],a[92:95],v[16:19]
ds_read_b128 v[16:19],v4 offset:0
ds_read_b64_tr_b16 a[202:203],v1 offset:23600
v_mul_f32_dpp v155,v15,v155 row_newbcast:3 row_mask:0xf bank_mask:0xf
v_mul_f32_dpp v96,v15,v96 row_newbcast:4 row_mask:0xf bank_mask:0xf
v_lshlrev_b32_sdwa v26,1,v26 dst_sel:DWORD dst_unused:UNUSED_PAD src0_sel:DWORD src1_sel:WORD_1
v_mul_f32_dpp v97,v15,v97 row_newbcast:5 row_mask:0xf bank_mask:0xf
v_mfma_f32_16x16x32_bf16 v[44:47],a[216:219],a[76:79],v[44:47]
ds_read_b64_tr_b16 a[204:205],v2 offset:22576
ds_read_b64_tr_b16 a[206:207],v2 offset:23600
v_mul_f32_dpp v98,v15,v98 row_newbcast:6 row_mask:0xf bank_mask:0xf
v_mul_f32_dpp v99,v15,v99 row_newbcast:7 row_mask:0xf bank_mask:0xf
v_lshlrev_b32_sdwa v25,1,v25 dst_sel:DWORD dst_unused:UNUSED_PAD src0_sel:DWORD src1_sel:WORD_1
v_cvt_pk_bf16_f32 v152,v152,v153
v_mfma_f32_16x16x32_bf16 v[44:47],a[220:223],a[60:63],v[44:47]
ds_read_u16 v24,v24
v_exp_f16_sdwa v8,v9 dst_sel:WORD_1 dst_unused:UNUSED_PRESERVE src0_sel:WORD_1
v_cvt_pk_bf16_f32 v153,v154,v155
v_cvt_pk_bf16_f32 v154,v96,v97
v_cvt_pk_bf16_f32 v155,v98,v99
v_mfma_f32_16x16x32_bf16 v[112:115],a[252:255],v[40:43],v[112:115]
ds_read_u16_d16_hi v26,v26
v_mul_f32_dpp v140,v14,v140 row_newbcast:0 row_mask:0xf bank_mask:0xf
v_mul_f32_dpp v141,v14,v141 row_newbcast:1 row_mask:0xf bank_mask:0xf
v_lshlrev_b32_sdwa v27,1,v27 dst_sel:DWORD dst_unused:UNUSED_PAD src0_sel:DWORD src1_sel:WORD_1
v_mul_f32_dpp v142,v14,v142 row_newbcast:2 row_mask:0xf bank_mask:0xf
v_mfma_f32_16x16x32_bf16 v[116:119],a[252:255],v[36:39],v[116:119]
ds_read_u16 v25,v25
v_mul_f32_dpp v143,v14,v143 row_newbcast:3 row_mask:0xf bank_mask:0xf
v_mul_f32_dpp v68,v14,v68 row_newbcast:4 row_mask:0xf bank_mask:0xf
v_lshlrev_b32_sdwa v56,1,v56 dst_sel:DWORD dst_unused:UNUSED_PAD src0_sel:DWORD src1_sel:WORD_1
v_mul_f32_dpp v69,v14,v69 row_newbcast:5 row_mask:0xf bank_mask:0xf
v_mfma_f32_16x16x32_bf16 v[76:79],a[240:243],v[36:39],v[76:79]
ds_read_u16_d16_hi v27,v27
v_mul_f32_dpp v70,v14,v70 row_newbcast:6 row_mask:0xf bank_mask:0xf
v_mul_f32_dpp v71,v14,v71 row_newbcast:7 row_mask:0xf bank_mask:0xf
v_pk_fma_f16 v8,v8,s55,s55
v_cvt_pk_bf16_f32 v140,v140,v141
v_mfma_f32_16x16x32_bf16 v[72:75],a[240:243],v[40:43],v[72:75]
v_rcp_f16 v9,v8
v_cvt_pk_bf16_f32 v141,v142,v143
v_cvt_pk_bf16_f32 v142,v68,v69
v_lshlrev_b32_sdwa v58,1,v58 dst_sel:DWORD dst_unused:UNUSED_PAD src0_sel:DWORD src1_sel:WORD_1
v_cvt_pk_bf16_f32 v143,v70,v71
v_mfma_f32_16x16x32_bf16 v[120:123],a[224:227],v[40:43],v[120:123]
buffer_store_dwordx4 v[144:147],v7,s[40:43],s62 offen offset:64 sc1
ds_read_b64_tr_b16 a[208:209],v2 offset:20528
v_mul_f32_dpp v148,v14,v148 row_newbcast:8 row_mask:0xf bank_mask:0xf
v_mul_f32_dpp v149,v14,v149 row_newbcast:9 row_mask:0xf bank_mask:0xf
v_mul_f32_dpp v150,v14,v150 row_newbcast:10 row_mask:0xf bank_mask:0xf
v_mfma_f32_16x16x32_bf16 v[124:127],a[224:227],v[36:39],v[124:127]
ds_read_b64_tr_b16 a[210:211],v2 offset:21552
v_mul_f32_dpp v151,v14,v151 row_newbcast:11 row_mask:0xf bank_mask:0xf
v_mul_f32_dpp v84,v14,v84 row_newbcast:12 row_mask:0xf bank_mask:0xf
v_lshlrev_b32_sdwa v57,1,v57 dst_sel:DWORD dst_unused:UNUSED_PAD src0_sel:DWORD src1_sel:WORD_1
v_mul_f32_dpp v85,v14,v85 row_newbcast:13 row_mask:0xf bank_mask:0xf
v_mfma_f32_16x16x32_bf16 v[92:95],a[232:235],v[36:39],v[92:95]
ds_read_u16 v56,v56
v_mul_f32_dpp v86,v14,v86 row_newbcast:14 row_mask:0xf bank_mask:0xf
v_mul_f32_dpp v87,v14,v87 row_newbcast:15 row_mask:0xf bank_mask:0xf
v_lshlrev_b32_sdwa v59,1,v59 dst_sel:DWORD dst_unused:UNUSED_PAD src0_sel:DWORD src1_sel:WORD_1
v_cvt_pk_bf16_f32 v148,v148,v149
v_mfma_f32_16x16x32_bf16 v[88:91],a[232:235],v[40:43],v[88:91]
v_rcp_f16_sdwa v9,v8 dst_sel:WORD_1 dst_unused:UNUSED_PRESERVE src0_sel:WORD_1
v_cvt_pk_bf16_f32 v149,v150,v151
v_cvt_pk_bf16_f32 v150,v84,v85
v_cvt_pk_bf16_f32 v151,v86,v87
v_mfma_f32_16x16x32_bf16 v[128:131],a[236:239],v[40:43],v[128:131]
ds_read_u16_d16_hi v58,v58
ds_read_u16 v57,v57
v_mul_f32_dpp v156,v15,v156 row_newbcast:0 row_mask:0xf bank_mask:0xf
v_mul_f32_dpp v157,v15,v157 row_newbcast:1 row_mask:0xf bank_mask:0xf
v_lshlrev_b32_sdwa v52,1,v52 dst_sel:DWORD dst_unused:UNUSED_PAD src0_sel:DWORD src1_sel:WORD_1
v_mul_f32_dpp v158,v15,v158 row_newbcast:2 row_mask:0xf bank_mask:0xf
v_mfma_f32_16x16x32_bf16 v[132:135],a[236:239],v[36:39],v[132:135]
ds_read_u16_d16_hi v59,v59
ds_read_b64_tr_b16 a[212:213],v2 offset:18480
v_mul_f32_dpp v159,v15,v159 row_newbcast:3 row_mask:0xf bank_mask:0xf
v_mul_f32_dpp v100,v15,v100 row_newbcast:4 row_mask:0xf bank_mask:0xf
v_mul_f32_dpp v101,v15,v101 row_newbcast:5 row_mask:0xf bank_mask:0xf
v_lshlrev_b32_sdwa v54,1,v54 dst_sel:DWORD dst_unused:UNUSED_PAD src0_sel:DWORD src1_sel:WORD_1
v_mfma_f32_16x16x32_bf16 v[108:111],a[244:247],v[36:39],v[108:111]
ds_read_b64_tr_b16 a[214:215],v2 offset:19504
v_mul_f32_dpp v102,v15,v102 row_newbcast:6 row_mask:0xf bank_mask:0xf
v_mul_f32_dpp v103,v15,v103 row_newbcast:7 row_mask:0xf bank_mask:0xf
v_fma_mix_f32 v53,v53,v9,0 op_sel_hi:[0,1,0]
v_cvt_pk_bf16_f32 v156,v156,v157
v_mfma_f32_16x16x32_bf16 v[104:107],a[244:247],v[40:43],v[104:107]
s_waitcnt lgkmcnt(8)
ds_read_u16 v52,v52
ds_read_u16_d16_hi v54,v54
v_cvt_pk_bf16_f32 v157,v158,v159
v_cvt_pk_bf16_f32 v158,v100,v101
v_cvt_pk_bf16_f32 v159,v102,v103
v_fma_mix_f32 v55,v55,v9,0 op_sel:[0,1,0] op_sel_hi:[0,1,0]
v_mfma_f32_16x16x32_bf16 v[36:39],a[192:195],a[48:51],v[16:19]
v_mul_f32_dpp v112,v14,v112 row_newbcast:0 row_mask:0xf bank_mask:0xf
s_add_u32 s63,s61,98304
v_accvgpr_write_b32 a16,0
v_accvgpr_write_b32 a17,0
v_lshlrev_b32_sdwa v60,1,v60 dst_sel:DWORD dst_unused:UNUSED_PAD src0_sel:DWORD src1_sel:WORD_1
v_mfma_f32_16x16x32_bf16 v[36:39],a[196:199],a[64:67],v[36:39]
ds_read_u16 v60,v60
v_accvgpr_write_b32 a18,0
v_accvgpr_write_b32 a19,0
v_fma_mixlo_f16 v8,v49,s99,0
v_mul_f32_dpp v113,v14,v113 row_newbcast:1 row_mask:0xf bank_mask:0xf
v_mfma_f32_16x16x32_bf16 v[36:39],a[200:203],a[80:83],v[36:39]
buffer_store_dwordx4 v[152:155],v7,s[40:43],s62 offen offset:128 sc1
v_lshl_add_u64 v[28:29],v[28:29],0,v[30:31]
v_mul_f32_dpp v114,v14,v114 row_newbcast:2 row_mask:0xf bank_mask:0xf
v_mul_f32_dpp v115,v14,v115 row_newbcast:3 row_mask:0xf bank_mask:0xf
v_lshlrev_b32_sdwa v62,1,v62 dst_sel:DWORD dst_unused:UNUSED_PAD src0_sel:DWORD src1_sel:WORD_1
v_mul_f32_dpp v72,v14,v72 row_newbcast:4 row_mask:0xf bank_mask:0xf
v_mfma_f32_16x16x32_bf16 v[40:43],a[204:207],a[80:83],v[20:23]
s_waitcnt lgkmcnt(8)
ds_read_u16_d16_hi v62,v62
v_lshl_add_u64 v[30:31],v[24:25],0,v[26:27]
v_mul_f32_dpp v73,v14,v73 row_newbcast:5 row_mask:0xf bank_mask:0xf
v_mul_f32_dpp v74,v14,v74 row_newbcast:6 row_mask:0xf bank_mask:0xf
v_fma_mixhi_f16 v8,v51,s99,0
v_mul_f32_dpp v75,v14,v75 row_newbcast:7 row_mask:0xf bank_mask:0xf
v_mfma_f32_16x16x32_bf16 v[40:43],a[208:211],a[64:67],v[40:43]
s_waitcnt lgkmcnt(4)
v_lshl_add_u64 v[24:25],v[56:57],0,v[58:59]
v_cvt_pk_bf16_f32 v112,v112,v113
v_cvt_pk_bf16_f32 v113,v114,v115
v_lshlrev_b32_sdwa v61,1,v61 dst_sel:DWORD dst_unused:UNUSED_PAD src0_sel:DWORD src1_sel:WORD_1
v_pk_fma_f16 v9,v8,v8,s54
v_cvt_pk_bf16_f32 v114,v72,v73
v_mfma_f32_16x16x32_bf16 v[40:43],a[212:215],a[48:51],v[40:43]
ds_read_u16 v61,v61
v_cvt_pk_bf16_f32 v115,v74,v75
v_mul_f32_dpp v120,v14,v120 row_newbcast:8 row_mask:0xf bank_mask:0xf
v_lshlrev_b32_sdwa v63,1,v63 dst_sel:DWORD dst_unused:UNUSED_PAD src0_sel:DWORD src1_sel:WORD_1
v_accvgpr_write_b32 a24,v112
v_mfma_f32_16x16x32_bf16 v[56:59],a[212:215],a[52:55],v[20:23]
s_waitcnt lgkmcnt(3)
ds_read_u16_d16_hi v63,v63
v_or_b32 v26,v52,v54
v_mul_f32_dpp v121,v14,v121 row_newbcast:9 row_mask:0xf bank_mask:0xf
v_mul_f32_dpp v122,v14,v122 row_newbcast:10 row_mask:0xf bank_mask:0xf
v_pk_mul_f16 v9,v8,v9
v_accvgpr_write_b32 a25,v113
v_mfma_f32_16x16x32_bf16 v[56:59],a[208:211],a[68:71],v[56:59]
s_waitcnt vmcnt(13)
v_cvt_pk_bf16_f32 v27,v53,v55
v_mul_f32_dpp v123,v14,v123 row_newbcast:11 row_mask:0xf bank_mask:0xf
v_mul_f32_dpp v88,v14,v88 row_newbcast:12 row_mask:0xf bank_mask:0xf
v_lshlrev_b32_sdwa v32,1,v32 dst_sel:DWORD dst_unused:UNUSED_PAD src0_sel:DWORD src1_sel:WORD_1
v_accvgpr_write_b32 a26,v114
v_mfma_f32_16x16x32_bf16 v[56:59],a[204:207],a[84:87],v[56:59]
s_barrier
v_exp_f16 v8,v9
v_mul_f32_dpp v89,v14,v89 row_newbcast:13 row_mask:0xf bank_mask:0xf
v_mul_f32_dpp v90,v14,v90 row_newbcast:14 row_mask:0xf bank_mask:0xf
v_accvgpr_write_b32 a27,v115
v_mfma_f32_16x16x32_bf16 v[52:55],a[200:203],a[84:87],v[16:19]
ds_read_u16 v32,v32
v_mul_f32_dpp v91,v14,v91 row_newbcast:15 row_mask:0xf bank_mask:0xf
v_cvt_pk_bf16_f32 v120,v120,v121
v_lshlrev_b32_sdwa v34,1,v34 dst_sel:DWORD dst_unused:UNUSED_PAD src0_sel:DWORD src1_sel:WORD_1
v_cvt_pk_bf16_f32 v121,v122,v123
v_mfma_f32_16x16x32_bf16 v[52:55],a[196:199],a[68:71],v[52:55]
s_add_u32 m0,s58,18480
ds_read_u16_d16_hi v34,v34
v_cvt_pk_bf16_f32 v122,v88,v89
v_cvt_pk_bf16_f32 v123,v90,v91
v_lshlrev_b32_sdwa v33,1,v33 dst_sel:DWORD dst_unused:UNUSED_PAD src0_sel:DWORD src1_sel:WORD_1
v_mul_f32_dpp v128,v15,v128 row_newbcast:0 row_mask:0xf bank_mask:0xf
v_mfma_f32_16x16x32_bf16 v[52:55],a[192:195],a[52:55],v[52:55]
s_add_u32 s69,s69,s59
v_exp_f16_sdwa v8,v9 dst_sel:WORD_1 dst_unused:UNUSED_PRESERVE src0_sel:WORD_1
v_accvgpr_write_b32 a28,v120
v_mul_f32_dpp v129,v15,v129 row_newbcast:1 row_mask:0xf bank_mask:0xf
v_mul_f32_dpp v130,v15,v130 row_newbcast:2 row_mask:0xf bank_mask:0xf
v_mfma_f32_16x16x32_bf16 v[232:235],a[252:255],v[28:31],v[232:235]
buffer_load_dwordx4 v5,s[44:47],s69 offen offset:0 lds
v_accvgpr_write_b32 a29,v121
v_mul_f32_dpp v131,v15,v131 row_newbcast:3 row_mask:0xf bank_mask:0xf
v_lshlrev_b32_sdwa v35,1,v35 dst_sel:DWORD dst_unused:UNUSED_PAD src0_sel:DWORD src1_sel:WORD_1
v_mul_f32_dpp v104,v15,v104 row_newbcast:4 row_mask:0xf bank_mask:0xf
v_mfma_f32_16x16x32_bf16 v[236:239],a[252:255],v[24:27],v[236:239]
s_add_u32 s69,s69,s53
ds_read_u16 v33,v33
v_accvgpr_write_b32 a30,v122
v_mul_f32_dpp v105,v15,v105 row_newbcast:5 row_mask:0xf bank_mask:0xf
v_lshlrev_b32_sdwa v44,1,v44 dst_sel:DWORD dst_unused:UNUSED_PAD src0_sel:DWORD src1_sel:WORD_1
v_mul_f32_dpp v106,v15,v106 row_newbcast:6 row_mask:0xf bank_mask:0xf
v_mfma_f32_16x16x32_bf16 v[164:167],a[240:243],v[24:27],v[164:167]
buffer_load_dwordx4 v5,s[44:47],s69 offen offset:1024 lds
ds_read_u16_d16_hi v35,v35
v_accvgpr_write_b32 a31,v123
v_mul_f32_dpp v107,v15,v107 row_newbcast:7 row_mask:0xf bank_mask:0xf
v_pk_fma_f16 v8,v8,s55,s55
v_cvt_pk_bf16_f32 v128,v128,v129
v_mfma_f32_16x16x32_bf16 v[160:163],a[240:243],v[28:31],v[160:163]
v_rcp_f16 v9,v8
v_cvt_pk_bf16_f32 v129,v130,v131
v_cvt_pk_bf16_f32 v130,v104,v105
v_lshlrev_b32_sdwa v46,1,v46 dst_sel:DWORD dst_unused:UNUSED_PAD src0_sel:DWORD src1_sel:WORD_1
v_cvt_pk_bf16_f32 v131,v106,v107
v_mfma_f32_16x16x32_bf16 v[240:243],a[224:227],v[28:31],v[240:243]
buffer_store_dwordx4 v[140:143],v7,s[40:43],s62 offen offset:192 sc1
ds_read_u16 v44,v44
v_mul_f32_dpp v116,v14,v116 row_newbcast:0 row_mask:0xf bank_mask:0xf
v_accvgpr_write_b32 a32,v128
v_mul_f32_dpp v117,v14,v117 row_newbcast:1 row_mask:0xf bank_mask:0xf
v_mfma_f32_16x16x32_bf16 v[244:247],a[224:227],v[24:27],v[244:247]
s_add_u32 s69,s69,s53
ds_read_u16_d16_hi v46,v46
v_mul_f32_dpp v118,v14,v118 row_newbcast:2 row_mask:0xf bank_mask:0xf
v_accvgpr_write_b32 a33,v129
v_lshlrev_b32_sdwa v45,1,v45 dst_sel:DWORD dst_unused:UNUSED_PAD src0_sel:DWORD src1_sel:WORD_1
v_mul_f32_dpp v119,v14,v119 row_newbcast:3 row_mask:0xf bank_mask:0xf
v_mfma_f32_16x16x32_bf16 v[180:183],a[232:235],v[24:27],v[180:183]
buffer_load_dwordx4 v5,s[44:47],s69 offen offset:2048 lds
v_mul_f32_dpp v76,v14,v76 row_newbcast:4 row_mask:0xf bank_mask:0xf
v_accvgpr_write_b32 a34,v130
v_lshlrev_b32_sdwa v47,1,v47 dst_sel:DWORD dst_unused:UNUSED_PAD src0_sel:DWORD src1_sel:WORD_1
v_mul_f32_dpp v77,v14,v77 row_newbcast:5 row_mask:0xf bank_mask:0xf
v_mfma_f32_16x16x32_bf16 v[176:179],a[232:235],v[28:31],v[176:179]
ds_read_u16 v45,v45
v_rcp_f16_sdwa v9,v8 dst_sel:WORD_1 dst_unused:UNUSED_PRESERVE src0_sel:WORD_1
v_mul_f32_dpp v78,v14,v78 row_newbcast:6 row_mask:0xf bank_mask:0xf
v_accvgpr_write_b32 a35,v131
v_mul_f32_dpp v79,v14,v79 row_newbcast:7 row_mask:0xf bank_mask:0xf
v_mfma_f32_16x16x32_bf16 v[248:251],a[236:239],v[28:31],v[248:251]
ds_read_u16_d16_hi v47,v47
v_cvt_pk_bf16_f32 v116,v116,v117
v_cvt_pk_bf16_f32 v117,v118,v119
v_lshlrev_b32_sdwa v48,1,v48 dst_sel:DWORD dst_unused:UNUSED_PAD src0_sel:DWORD src1_sel:WORD_1
v_cvt_pk_bf16_f32 v118,v76,v77
v_mfma_f32_16x16x32_bf16 v[252:255],a[236:239],v[24:27],v[252:255]
ds_read_u16 v48,v48
v_cvt_pk_bf16_f32 v119,v78,v79
v_mfma_f32_32x32x16_bf16 v[64:79],a[16:19],a[16:19],0
v_mul_f32_dpp v124,v14,v124 row_newbcast:8 row_mask:0xf bank_mask:0xf
v_lshlrev_b32_sdwa v50,1,v50 dst_sel:DWORD dst_unused:UNUSED_PAD src0_sel:DWORD src1_sel:WORD_1
v_fma_mix_f32 v49,v49,v9,0 op_sel_hi:[0,1,0]
v_accvgpr_write_b32 a36,v116
v_fma_mix_f32 v51,v51,v9,0 op_sel:[0,1,0] op_sel_hi:[0,1,0]
v_mfma_f32_16x16x32_bf16 v[196:199],a[244:247],v[24:27],v[196:199]
ds_read_u16_d16_hi v50,v50
v_mul_f32_dpp v125,v14,v125 row_newbcast:9 row_mask:0xf bank_mask:0xf
v_mul_f32_dpp v126,v14,v126 row_newbcast:10 row_mask:0xf bank_mask:0xf
v_mfma_f32_16x16x32_bf16 v[192:195],a[244:247],v[28:31],v[192:195]
ds_read_b64_tr_b16 a[228:229],v3 offset:0
v_accvgpr_write_b32 a37,v117
v_mul_f32_dpp v127,v14,v127 row_newbcast:11 row_mask:0xf bank_mask:0xf
v_mul_f32_dpp v92,v14,v92 row_newbcast:12 row_mask:0xf bank_mask:0xf
ds_read_b64_tr_b16 a[230:231],v3 offset:1536
ds_read_b64_tr_b16 a[248:249],v3 offset:8
ds_read_b64_tr_b16 a[250:251],v3 offset:1544
s_mov_b64 exec,0
s_cmp_eq_u32 s52,0
s_cbranch_scc1 TE10_%=
s_cmp_eq_u32 s52,1
s_cbranch_scc1 TE11_%=
s_cmp_eq_u32 s52,2
s_cbranch_scc1 TE12_%=
s_branch TE13_%=
TR1_%=:
s_mov_b64 exec,-1
v_add_u32_sdwa v28,s60,v10 dst_sel:DWORD dst_unused:UNUSED_PAD src0_sel:DWORD src1_sel:WORD_1
v_subrev_u32 v8,88400,v28
v_min_u32 v28,v28,v8
v_add_u32 v29,7072,v28
v_subrev_u32 v8,88400,v29
v_min_u32 v29,v29,v8
s_waitcnt lgkmcnt(14)
v_lshl_add_u64 v[60:61],v[60:61],0,v[62:63]
s_waitcnt lgkmcnt(10)
v_lshl_add_u64 v[62:63],v[32:33],0,v[34:35]
s_waitcnt lgkmcnt(6)
v_lshl_add_u64 v[32:33],v[44:45],0,v[46:47]
s_waitcnt lgkmcnt(4)
v_or_b32 v34,v48,v50
v_cvt_pk_bf16_f32 v35,v49,v51
v_mfma_f32_16x16x32_bf16 v[208:211],a[252:255],v[60:63],v[208:211]
buffer_store_dwordx4 v[148:151],v7,s[40:43],s62 offen offset:256 sc1
v_xad_u32 v24,v11,0,v28
v_xad_u32 v25,v11,8,v29
v_xad_u32 v26,v11,16,v28
v_xad_u32 v27,v11,24,v29
v_mfma_f32_16x16x32_bf16 v[212:215],a[252:255],v[32:35],v[212:215]
ds_read_b64 a[0:1],v24 offset:0
v_accvgpr_write_b32 a38,v118
v_mul_f32_dpp v93,v14,v93 row_newbcast:13 row_mask:0xf bank_mask:0xf
v_mul_f32_dpp v94,v14,v94 row_newbcast:14 row_mask:0xf bank_mask:0xf
v_accvgpr_write_b32 a39,v119
v_mfma_f32_16x16x32_bf16 v[172:175],a[240:243],v[32:35],v[172:175]
ds_read_b64 a[2:3],v24 offset:192
v_mul_f32_dpp v95,v14,v95 row_newbcast:15 row_mask:0xf bank_mask:0xf
v_cvt_pk_bf16_f32 v124,v124,v125
v_cvt_pk_bf16_f32 v125,v126,v127
v_mfma_f32_16x16x32_bf16 v[168:171],a[240:243],v[60:63],v[168:171]
ds_read_b64 a[4:5],v25 offset:0
v_cvt_pk_bf16_f32 v126,v92,v93
v_cvt_pk_bf16_f32 v127,v94,v95
v_mfma_f32_32x32x16_bf16 v[80:95],a[16:19],a[16:19],0
v_mfma_f32_16x16x32_bf16 v[216:219],a[224:227],v[60:63],v[216:219]
ds_read_b64 a[6:7],v25 offset:192
v_mul_f32_dpp v132,v15,v132 row_newbcast:0 row_mask:0xf bank_mask:0xf
v_accvgpr_write_b32 a40,v124
v_mul_f32_dpp v133,v15,v133 row_newbcast:1 row_mask:0xf bank_mask:0xf
v_mfma_f32_16x16x32_bf16 v[220:223],a[224:227],v[32:35],v[220:223]
ds_read_b64 a[8:9],v24 offset:8
v_mul_f32_dpp v134,v15,v134 row_newbcast:2 row_mask:0xf bank_mask:0xf
v_accvgpr_write_b32 a41,v125
v_mul_f32_dpp v135,v15,v135 row_newbcast:3 row_mask:0xf bank_mask:0xf
v_mfma_f32_16x16x32_bf16 v[188:191],a[232:235],v[32:35],v[188:191]
ds_read_b64 a[10:11],v24 offset:200
v_mul_f32_dpp v108,v15,v108 row_newbcast:4 row_mask:0xf bank_mask:0xf
v_accvgpr_write_b32 a42,v126
v_mul_f32_dpp v109,v15,v109 row_newbcast:5 row_mask:0xf bank_mask:0xf
v_mfma_f32_16x16x32_bf16 v[184:187],a[232:235],v[60:63],v[184:187]
ds_read_b64 a[12:13],v25 offset:8
v_mul_f32_dpp v110,v15,v110 row_newbcast:6 row_mask:0xf bank_mask:0xf
v_accvgpr_write_b32 a43,v127
v_mul_f32_dpp v111,v15,v111 row_newbcast:7 row_mask:0xf bank_mask:0xf
v_mfma_f32_16x16x32_bf16 v[224:227],a[236:239],v[60:63],v[224:227]
ds_read_b64 a[14:15],v25 offset:200
v_cvt_pk_bf16_f32 v132,v132,v133
v_cvt_pk_bf16_f32 v133,v134,v135
v_cvt_pk_bf16_f32 v134,v108,v109
v_mfma_f32_16x16x32_bf16 v[228:231],a[236:239],v[32:35],v[228:231]
v_cvt_pk_bf16_f32 v135,v110,v111
v_mfma_f32_32x32x16_bf16 v[96:111],a[16:19],a[16:19],0
v_accvgpr_write_b32 a44,v132
v_mfma_f32_16x16x32_bf16 v[204:207],a[244:247],v[32:35],v[204:207]
v_accvgpr_write_b32 a45,v133
v_accvgpr_write_b32 a46,v134
v_accvgpr_write_b32 a47,v135
v_mfma_f32_16x16x32_bf16 v[200:203],a[244:247],v[60:63],v[200:203]
buffer_store_dwordx4 v[156:159],v7,s[40:43],s62 offen offset:320 sc1
v_mfma_f32_32x32x16_bf16 v[112:127],a[16:19],a[16:19],0
v_mov_b64 v[44:45],0
v_mov_b64 v[46:47],0
ds_read_b64 a[16:17],v26 offset:0
ds_read_b64 a[18:19],v26 offset:192
ds_read_b64 a[20:21],v27 offset:0
ds_read_b64 a[22:23],v27 offset:192
s_waitcnt lgkmcnt(8)
v_smfmac_f32_16x16x64_bf16 v[64:67],a[96:99],a[0:7],v10
s_waitcnt lgkmcnt(4)
ds_read_b64 a[0:1],v26 offset:8
ds_read_b64 a[2:3],v26 offset:200
ds_read_b64 a[4:5],v27 offset:8
ds_read_b64 a[6:7],v27 offset:200
v_smfmac_f32_16x16x64_bf16 v[68:71],a[100:103],a[8:15],v10
s_waitcnt lgkmcnt(4)
ds_read_b64 a[8:9],v24 offset:64
ds_read_b64 a[10:11],v24 offset:256
ds_read_b64 a[12:13],v25 offset:64
ds_read_b64 a[14:15],v25 offset:256
v_smfmac_f32_16x16x64_bf16 v[72:75],a[104:107],a[16:23],v10
s_waitcnt lgkmcnt(4)
ds_read_b64 a[16:17],v24 offset:72
ds_read_b64 a[18:19],v24 offset:264
ds_read_b64 a[20:21],v25 offset:72
ds_read_b64 a[22:23],v25 offset:264
v_and_b32 v60,8,v11
v_pk_mul_f32 v[32:33],v[64:65],v[64:65]
v_pk_mul_f32 v[34:35],v[66:67],v[66:67]
v_smfmac_f32_16x16x64_bf16 v[76:79],a[108:111],a[0:7],v10
s_waitcnt lgkmcnt(4)
ds_read_b64 a[0:1],v26 offset:64
ds_read_b64 a[2:3],v26 offset:256
ds_read_b64 a[4:5],v27 offset:64
ds_read_b64 a[6:7],v27 offset:256
v_mul_u32_u24 v60,884,v60
s_add_u32 s64,s60,3536
v_pk_add_f32 v[48:49],v[64:65],v[68:69]
v_pk_fma_f32 v[32:33],v[68:69],v[68:69],v[32:33]
v_pk_add_f32 v[50:51],v[66:67],v[70:71]
v_pk_fma_f32 v[34:35],v[70:71],v[70:71],v[34:35]
v_smfmac_f32_16x16x64_bf16 v[80:83],a[128:131],a[8:15],v10
s_waitcnt lgkmcnt(4)
ds_read_b64 a[8:9],v26 offset:72
ds_read_b64 a[10:11],v26 offset:264
ds_read_b64 a[12:13],v27 offset:72
ds_read_b64 a[14:15],v27 offset:264
v_add_u32 v63,s64,v60
v_pk_add_f32 v[48:49],v[48:49],v[72:73]
v_pk_fma_f32 v[32:33],v[72:73],v[72:73],v[32:33]
v_pk_add_f32 v[50:51],v[50:51],v[74:75]
v_pk_fma_f32 v[34:35],v[74:75],v[74:75],v[34:35]
v_smfmac_f32_16x16x64_bf16 v[84:87],a[132:135],a[16:23],v10
s_waitcnt lgkmcnt(4)
ds_read_b64 a[16:17],v24 offset:128
ds_read_b64 a[18:19],v24 offset:320
ds_read_b64 a[20:21],v25 offset:128
ds_read_b64 a[22:23],v25 offset:320
v_subrev_u32 v8,88400,v63
v_pk_add_f32 v[48:49],v[48:49],v[76:77]
v_pk_fma_f32 v[32:33],v[76:77],v[76:77],v[32:33]
v_pk_add_f32 v[50:51],v[50:51],v[78:79]
v_pk_fma_f32 v[34:35],v[78:79],v[78:79],v[34:35]
v_smfmac_f32_16x16x64_bf16 v[88:91],a[136:139],a[0:7],v10
s_waitcnt lgkmcnt(4)
ds_read_b64 a[0:1],v24 offset:136
ds_read_b64 a[2:3],v24 offset:328
ds_read_b64 a[4:5],v25 offset:136
ds_read_b64 a[6:7],v25 offset:328
v_min_u32 v63,v63,v8
v_pk_add_f32 v[48:49],v[48:49],v[80:81]
v_pk_fma_f32 v[32:33],v[80:81],v[80:81],v[32:33]
v_pk_add_f32 v[50:51],v[50:51],v[82:83]
v_pk_fma_f32 v[34:35],v[82:83],v[82:83],v[34:35]
v_smfmac_f32_16x16x64_bf16 v[92:95],a[140:143],a[8:15],v10
s_waitcnt lgkmcnt(4)
buffer_store_dwordx4 a[24:27],v7,s[40:43],s63 offen offset:0 sc1
ds_read_b64 a[8:9],v26 offset:128
ds_read_b64 a[10:11],v26 offset:320
ds_read_b64 a[12:13],v27 offset:128
ds_read_b64 a[14:15],v27 offset:320
v_xad_u32 v24,v11,32,v28
v_xad_u32 v25,v11,40,v29
v_pk_add_f32 v[48:49],v[48:49],v[84:85]
v_pk_fma_f32 v[32:33],v[84:85],v[84:85],v[32:33]
v_pk_add_f32 v[50:51],v[50:51],v[86:87]
v_pk_fma_f32 v[34:35],v[86:87],v[86:87],v[34:35]
v_smfmac_f32_16x16x64_bf16 v[96:99],a[160:163],a[16:23],v10
s_waitcnt lgkmcnt(4)
ds_read_b64 a[16:17],v26 offset:136
ds_read_b64 a[18:19],v26 offset:328
ds_read_b64 a[20:21],v27 offset:136
ds_read_b64 a[22:23],v27 offset:328
v_add_u32 v62,3536,v63
v_pk_add_f32 v[48:49],v[48:49],v[88:89]
v_pk_fma_f32 v[32:33],v[88:89],v[88:89],v[32:33]
v_pk_add_f32 v[50:51],v[50:51],v[90:91]
v_pk_fma_f32 v[34:35],v[90:91],v[90:91],v[34:35]
v_smfmac_f32_16x16x64_bf16 v[100:103],a[164:167],a[0:7],v10
s_waitcnt lgkmcnt(4)
ds_read_b64 a[0:1],v24 offset:0
ds_read_b64 a[2:3],v24 offset:192
ds_read_b64 a[4:5],v25 offset:0
ds_read_b64 a[6:7],v25 offset:192
v_xad_u32 v26,v11,48,v28
v_xad_u32 v27,v11,56,v29
v_pk_add_f32 v[48:49],v[48:49],v[92:93]
v_pk_fma_f32 v[32:33],v[92:93],v[92:93],v[32:33]
v_pk_add_f32 v[50:51],v[50:51],v[94:95]
v_pk_fma_f32 v[34:35],v[94:95],v[94:95],v[34:35]
v_smfmac_f32_16x16x64_bf16 v[104:107],a[168:171],a[8:15],v10
s_waitcnt lgkmcnt(4)
ds_read_b64 a[8:9],v24 offset:8
ds_read_b64 a[10:11],v24 offset:200
ds_read_b64 a[12:13],v25 offset:8
ds_read_b64 a[14:15],v25 offset:200
v_subrev_u32 v8,88400,v62
v_pk_add_f32 v[48:49],v[48:49],v[96:97]
v_pk_fma_f32 v[32:33],v[96:97],v[96:97],v[32:33]
v_pk_add_f32 v[50:51],v[50:51],v[98:99]
v_pk_fma_f32 v[34:35],v[98:99],v[98:99],v[34:35]
v_smfmac_f32_16x16x64_bf16 v[108:111],a[172:175],a[16:23],v10
s_waitcnt lgkmcnt(4)
ds_read_b64 a[16:17],v24 offset:64
ds_read_b64 a[18:19],v24 offset:256
ds_read_b64 a[20:21],v25 offset:64
ds_read_b64 a[22:23],v25 offset:256
v_min_u32 v62,v62,v8
v_pk_add_f32 v[48:49],v[48:49],v[100:101]
v_pk_fma_f32 v[32:33],v[100:101],v[100:101],v[32:33]
v_pk_add_f32 v[50:51],v[50:51],v[102:103]
v_pk_fma_f32 v[34:35],v[102:103],v[102:103],v[34:35]
v_smfmac_f32_16x16x64_bf16 v[112:115],a[112:115],a[0:7],v10
s_waitcnt lgkmcnt(4)
ds_read_b64 a[0:1],v24 offset:72
ds_read_b64 a[2:3],v24 offset:264
ds_read_b64 a[4:5],v25 offset:72
ds_read_b64 a[6:7],v25 offset:264
v_xor_b32 v9,0x80008,v12
v_pk_add_f32 v[48:49],v[48:49],v[104:105]
v_pk_fma_f32 v[32:33],v[104:105],v[104:105],v[32:33]
v_pk_add_f32 v[50:51],v[50:51],v[106:107]
v_pk_fma_f32 v[34:35],v[106:107],v[106:107],v[34:35]
v_mfma_f32_32x32x16_bf16 v[128:143],v[44:47],v[44:47],0
v_smfmac_f32_16x16x64_bf16 v[116:119],a[116:119],a[8:15],v10
s_waitcnt lgkmcnt(4)
buffer_store_dwordx4 a[28:31],v7,s[40:43],s63 offen offset:64 sc1
ds_read_b64 a[8:9],v24 offset:128
ds_read_b64 a[10:11],v24 offset:320
ds_read_b64 a[12:13],v25 offset:128
ds_read_b64 a[14:15],v25 offset:320
TP5478_%=:
s_cbranch_execz TR0_%=
v_add_u32_sdwa v60,v12,v63 dst_sel:DWORD dst_unused:UNUSED_PAD src0_sel:WORD_0 src1_sel:DWORD
v_pk_add_f32 v[48:49],v[48:49],v[108:109]
v_pk_fma_f32 v[32:33],v[108:109],v[108:109],v[32:33]
v_pk_add_f32 v[50:51],v[50:51],v[110:111]
v_pk_fma_f32 v[34:35],v[110:111],v[110:111],v[34:35]
v_smfmac_f32_16x16x64_bf16 v[120:123],a[144:147],a[16:23],v10
s_waitcnt lgkmcnt(4)
ds_read_b64 a[16:17],v24 offset:136
ds_read_b64 a[18:19],v24 offset:328
ds_read_b64 a[20:21],v25 offset:136
ds_read_b64 a[22:23],v25 offset:328
v_add_u32_sdwa v61,v12,v63 dst_sel:DWORD dst_unused:UNUSED_PAD src0_sel:WORD_1 src1_sel:DWORD
v_pk_add_f32 v[48:49],v[48:49],v[112:113]
v_pk_fma_f32 v[32:33],v[112:113],v[112:113],v[32:33]
v_pk_add_f32 v[50:51],v[50:51],v[114:115]
v_pk_fma_f32 v[34:35],v[114:115],v[114:115],v[34:35]
v_smfmac_f32_16x16x64_bf16 v[124:127],a[148:151],a[0:7],v10
s_waitcnt lgkmcnt(4)
ds_read_b64 a[0:1],v26 offset:0
ds_read_b64 a[2:3],v26 offset:192
ds_read_b64 a[4:5],v27 offset:0
ds_read_b64 a[6:7],v27 offset:192
v_add_u32_sdwa v63,v9,v62 dst_sel:DWORD dst_unused:UNUSED_PAD src0_sel:WORD_1 src1_sel:DWORD
v_pk_add_f32 v[48:49],v[48:49],v[116:117]
v_pk_fma_f32 v[32:33],v[116:117],v[116:117],v[32:33]
v_pk_add_f32 v[50:51],v[50:51],v[118:119]
v_pk_fma_f32 v[34:35],v[118:119],v[118:119],v[34:35]
v_smfmac_f32_16x16x64_bf16 v[128:131],a[176:179],a[8:15],v10
s_waitcnt lgkmcnt(4)
ds_read_b64 a[8:9],v26 offset:8
ds_read_b64 a[10:11],v26 offset:200
ds_read_b64 a[12:13],v27 offset:8
ds_read_b64 a[14:15],v27 offset:200
v_add_u32_sdwa v62,v9,v62 dst_sel:DWORD dst_unused:UNUSED_PAD src0_sel:WORD_0 src1_sel:DWORD
v_pk_add_f32 v[48:49],v[48:49],v[120:121]
v_pk_fma_f32 v[32:33],v[120:121],v[120:121],v[32:33]
v_pk_add_f32 v[50:51],v[50:51],v[122:123]
v_pk_fma_f32 v[34:35],v[122:123],v[122:123],v[34:35]
v_mfma_f32_32x32x16_bf16 v[144:159],v[44:47],v[44:47],0
v_smfmac_f32_16x16x64_bf16 v[132:135],a[180:183],a[16:23],v10
s_waitcnt lgkmcnt(4)
ds_read_b64 a[16:17],v26 offset:64
ds_read_b64 a[18:19],v26 offset:256
ds_read_b64 a[20:21],v27 offset:64
ds_read_b64 a[22:23],v27 offset:256
v_pk_add_f32 v[48:49],v[48:49],v[124:125]
v_pk_fma_f32 v[32:33],v[124:125],v[124:125],v[32:33]
v_pk_add_f32 v[50:51],v[50:51],v[126:127]
v_pk_fma_f32 v[34:35],v[126:127],v[126:127],v[34:35]
v_smfmac_f32_16x16x64_bf16 v[136:139],a[120:123],a[0:7],v10
s_waitcnt lgkmcnt(4)
ds_read_b64 a[0:1],v26 offset:72
ds_read_b64 a[2:3],v26 offset:264
ds_read_b64 a[4:5],v27 offset:72
ds_read_b64 a[6:7],v27 offset:264
v_pk_add_f32 v[48:49],v[48:49],v[128:129]
v_pk_fma_f32 v[32:33],v[128:129],v[128:129],v[32:33]
v_pk_add_f32 v[50:51],v[50:51],v[130:131]
v_pk_fma_f32 v[34:35],v[130:131],v[130:131],v[34:35]
v_smfmac_f32_16x16x64_bf16 v[140:143],a[124:127],a[8:15],v10
s_waitcnt lgkmcnt(4)
ds_read_b64 a[8:9],v26 offset:128
ds_read_b64 a[10:11],v26 offset:320
ds_read_b64 a[12:13],v27 offset:128
ds_read_b64 a[14:15],v27 offset:320
v_pk_add_f32 v[48:49],v[48:49],v[132:133]
v_pk_fma_f32 v[32:33],v[132:133],v[132:133],v[32:33]
v_pk_add_f32 v[50:51],v[50:51],v[134:135]
v_pk_fma_f32 v[34:35],v[134:135],v[134:135],v[34:35]
v_smfmac_f32_16x16x64_bf16 v[144:147],a[152:155],a[16:23],v10
s_waitcnt lgkmcnt(4)
buffer_store_dwordx4 a[32:35],v7,s[40:43],s63 offen offset:128 sc1
ds_read_b64 a[16:17],v26 offset:136
ds_read_b64 a[18:19],v26 offset:328
ds_read_b64 a[20:21],v27 offset:136
ds_read_b64 a[22:23],v27 offset:328
v_pk_add_f32 v[48:49],v[48:49],v[136:137]
v_pk_fma_f32 v[32:33],v[136:137],v[136:137],v[32:33]
v_pk_add_f32 v[50:51],v[50:51],v[138:139]
v_pk_fma_f32 v[34:35],v[138:139],v[138:139],v[34:35]
v_smfmac_f32_16x16x64_bf16 v[148:151],a[156:159],a[0:7],v10
s_waitcnt lgkmcnt(4)
ds_read_b64 a[252:253],v60 offset:57344
ds_read_b64 a[254:255],v60 offset:57352
ds_read_b64 a[240:241],v61 offset:57344
ds_read_b64 a[242:243],v61 offset:57352
ds_read_b128 a[236:239],v13 offset:0
v_pk_add_f32 v[48:49],v[48:49],v[140:141]
v_pk_fma_f32 v[32:33],v[140:141],v[140:141],v[32:33]
v_pk_add_f32 v[50:51],v[50:51],v[142:143]
v_pk_fma_f32 v[34:35],v[142:143],v[142:143],v[34:35]
v_smfmac_f32_16x16x64_bf16 v[152:155],a[184:187],a[8:15],v10
s_waitcnt lgkmcnt(5)
ds_read_b64 a[224:225],v62 offset:57344
ds_read_b64 a[226:227],v62 offset:57352
ds_read_b64 a[232:233],v63 offset:57344
ds_read_b64 a[234:235],v63 offset:57352
ds_read_b128 a[244:247],v13 offset:256
v_pk_add_f32 v[48:49],v[48:49],v[144:145]
v_pk_fma_f32 v[32:33],v[144:145],v[144:145],v[32:33]
v_pk_add_f32 v[50:51],v[50:51],v[146:147]
v_pk_fma_f32 v[34:35],v[146:147],v[146:147],v[34:35]
v_smfmac_f32_16x16x64_bf16 v[156:159],a[188:191],a[16:23],v10
ds_read_b128 v[24:27],v4 offset:3584
ds_read_b128 v[28:31],v4 offset:3616
v_pk_add_f32 v[48:49],v[48:49],v[148:149]
v_pk_fma_f32 v[32:33],v[148:149],v[148:149],v[32:33]
v_pk_add_f32 v[50:51],v[50:51],v[150:151]
v_pk_fma_f32 v[34:35],v[150:151],v[150:151],v[34:35]
v_pk_add_f32 v[48:49],v[48:49],v[152:153]
v_pk_fma_f32 v[32:33],v[152:153],v[152:153],v[32:33]
v_pk_add_f32 v[50:51],v[50:51],v[154:155]
v_pk_fma_f32 v[34:35],v[154:155],v[154:155],v[34:35]
v_pk_add_f32 v[48:49],v[48:49],v[156:157]
v_pk_fma_f32 v[32:33],v[156:157],v[156:157],v[32:33]
v_pk_add_f32 v[50:51],v[50:51],v[158:159]
v_pk_fma_f32 v[34:35],v[158:159],v[158:159],v[34:35]
s_nop 0
v_permlane32_swap_b32 v48,v50
v_permlane32_swap_b32 v49,v51
v_permlane32_swap_b32 v32,v34
v_permlane32_swap_b32 v33,v35
v_pk_add_f32 v[48:49],v[48:49],v[50:51]
v_pk_add_f32 v[32:33],v[32:33],v[34:35]
s_add_u32 s60,s60,56576
s_sub_u32 s64,s60,88400
s_min_u32 s60,s60,s64
v_permlane16_swap_b32 v48,v49
v_permlane16_swap_b32 v32,v33
v_add_f32 v8,v48,v49
v_add_f32 v9,v32,v33
v_mul_f32 v8,0x3c2aaaab,v8
v_mul_f32 v9,0x3c2aaaab,v9
v_cmp_ngt_f32 vcc,0x5d800000,v9
v_fma_f32 v9,-v8,v8,v9
v_max_f32 v9,0,v9
s_or_b64 s[90:91],s[90:91],vcc
v_add_f32 v9,0x3727c5ac,v9
v_rsq_f32 v9,v9
s_nop 0
v_mul_f32 v48,s96,v9
v_mul_f32 v49,s96,v9
v_mul_f32 v32,-v8,v48
v_mul_f32 v33,-v8,v48
v_permlane16_swap_b32 v48,v49
s_nop 0
v_permlane16_swap_b32 v32,v33
v_mov_b64 v[50:51],v[48:49]
v_mov_b64 v[34:35],v[32:33]
s_nop 0
v_permlane32_swap_b32 v48,v50
v_permlane32_swap_b32 v49,v51
v_permlane32_swap_b32 v32,v34
v_permlane32_swap_b32 v33,v35
v_pk_fma_f32 v[64:65],v[64:65],v[48:49],v[32:33]
v_pk_fma_f32 v[66:67],v[66:67],v[50:51],v[34:35]
v_pk_fma_f32 v[68:69],v[68:69],v[48:49],v[32:33]
v_pk_fma_f32 v[70:71],v[70:71],v[50:51],v[34:35]
v_pk_fma_f32 v[72:73],v[72:73],v[48:49],v[32:33]
v_pk_fma_f32 v[74:75],v[74:75],v[50:51],v[34:35]
v_pk_fma_f32 v[76:77],v[76:77],v[48:49],v[32:33]
v_pk_fma_f32 v[78:79],v[78:79],v[50:51],v[34:35]
v_pk_fma_f32 v[112:113],v[112:113],v[48:49],v[32:33]
v_pk_fma_f32 v[114:115],v[114:115],v[50:51],v[34:35]
v_pk_fma_f32 v[116:117],v[116:117],v[48:49],v[32:33]
v_pk_fma_f32 v[118:119],v[118:119],v[50:51],v[34:35]
buffer_store_dwordx4 a[36:39],v7,s[40:43],s63 offen offset:192 sc1
v_pk_fma_f32 v[136:137],v[136:137],v[48:49],v[32:33]
v_pk_fma_f32 v[138:139],v[138:139],v[50:51],v[34:35]
v_pk_fma_f32 v[140:141],v[140:141],v[48:49],v[32:33]
v_pk_fma_f32 v[142:143],v[142:143],v[50:51],v[34:35]
v_pk_fma_f32 v[80:81],v[80:81],v[48:49],v[32:33]
v_pk_fma_f32 v[82:83],v[82:83],v[50:51],v[34:35]
v_pk_fma_f32 v[84:85],v[84:85],v[48:49],v[32:33]
v_pk_fma_f32 v[86:87],v[86:87],v[50:51],v[34:35]
v_pk_fma_f32 v[88:89],v[88:89],v[48:49],v[32:33]
v_pk_fma_f32 v[90:91],v[90:91],v[50:51],v[34:35]
v_pk_fma_f32 v[92:93],v[92:93],v[48:49],v[32:33]
v_pk_fma_f32 v[94:95],v[94:95],v[50:51],v[34:35]
v_pk_fma_f32 v[120:121],v[120:121],v[48:49],v[32:33]
v_pk_fma_f32 v[122:123],v[122:123],v[50:51],v[34:35]
v_pk_fma_f32 v[124:125],v[124:125],v[48:49],v[32:33]
v_pk_fma_f32 v[126:127],v[126:127],v[50:51],v[34:35]
v_pk_fma_f32 v[144:145],v[144:145],v[48:49],v[32:33]
v_pk_fma_f32 v[146:147],v[146:147],v[50:51],v[34:35]
v_pk_fma_f32 v[148:149],v[148:149],v[48:49],v[32:33]
v_pk_fma_f32 v[150:151],v[150:151],v[50:51],v[34:35]
v_pk_fma_f32 v[96:97],v[96:97],v[48:49],v[32:33]
v_pk_fma_f32 v[98:99],v[98:99],v[50:51],v[34:35]
v_pk_fma_f32 v[100:101],v[100:101],v[48:49],v[32:33]
v_pk_fma_f32 v[102:103],v[102:103],v[50:51],v[34:35]
v_pk_fma_f32 v[104:105],v[104:105],v[48:49],v[32:33]
v_pk_fma_f32 v[106:107],v[106:107],v[50:51],v[34:35]
v_pk_fma_f32 v[108:109],v[108:109],v[48:49],v[32:33]
v_pk_fma_f32 v[110:111],v[110:111],v[50:51],v[34:35]
v_pk_fma_f32 v[128:129],v[128:129],v[48:49],v[32:33]
v_pk_fma_f32 v[130:131],v[130:131],v[50:51],v[34:35]
v_pk_fma_f32 v[132:133],v[132:133],v[48:49],v[32:33]
v_pk_fma_f32 v[134:135],v[134:135],v[50:51],v[34:35]
v_pk_fma_f32 v[152:153],v[152:153],v[48:49],v[32:33]
v_pk_fma_f32 v[154:155],v[154:155],v[50:51],v[34:35]
v_pk_fma_f32 v[156:157],v[156:157],v[48:49],v[32:33]
v_pk_fma_f32 v[158:159],v[158:159],v[50:51],v[34:35]
v_mfma_f32_16x16x32_bf16 v[44:47],a[192:195],a[56:59],v[16:19]
ds_read_b64_tr_b16 a[216:217],v1 offset:0
v_cvt_pk_bf16_f32 v64,v64,v68
v_cvt_pk_bf16_f32 v72,v72,v76
v_cvt_pk_bf16_f32 v112,v112,v116
v_lshlrev_b32_sdwa v36,1,v36 dst_sel:DWORD dst_unused:UNUSED_PAD src0_sel:DWORD src1_sel:WORD_1
v_mfma_f32_16x16x32_bf16 v[44:47],a[196:199],a[72:75],v[44:47]
ds_read_u16 v36,v36
v_accvgpr_write_b32 a0,v64
v_accvgpr_write_b32 a1,v72
v_cvt_pk_bf16_f32 v136,v136,v140
v_accvgpr_write_b32 a2,v112
v_mfma_f32_16x16x32_bf16 v[44:47],a[200:203],a[88:91],v[44:47]
buffer_store_dwordx4 a[40:43],v7,s[40:43],s63 offen offset:256 sc1
v_cvt_pk_bf16_f32 v65,v65,v69
v_accvgpr_write_b32 a3,v136
v_cvt_pk_bf16_f32 v73,v73,v77
v_accvgpr_write_b32 a4,v65
v_fma_mixlo_f16 v8,v57,s99,0
v_mfma_f32_16x16x32_bf16 v[48:51],a[204:207],a[88:91],v[20:23]
ds_read_b64_tr_b16 a[218:219],v1 offset:1024
v_cvt_pk_bf16_f32 v113,v113,v117
v_accvgpr_write_b32 a5,v73
v_cvt_pk_bf16_f32 v137,v137,v141
v_accvgpr_write_b32 a6,v113
v_lshlrev_b32_sdwa v38,1,v38 dst_sel:DWORD dst_unused:UNUSED_PAD src0_sel:DWORD src1_sel:WORD_1
v_mfma_f32_16x16x32_bf16 v[48:51],a[208:211],a[72:75],v[48:51]
ds_read_u16_d16_hi v38,v38
v_cvt_pk_bf16_f32 v66,v66,v70
v_accvgpr_write_b32 a7,v137
v_cvt_pk_bf16_f32 v74,v74,v78
v_accvgpr_write_b32 a8,v66
v_fma_mixhi_f16 v8,v59,s99,0
v_mfma_f32_16x16x32_bf16 v[48:51],a[212:215],a[56:59],v[48:51]
ds_read_b64_tr_b16 a[220:221],v1 offset:2048
v_accvgpr_write_b32 a9,v74
v_cvt_pk_bf16_f32 v114,v114,v118
v_accvgpr_write_b32 a10,v114
v_lshlrev_b32_sdwa v37,1,v37 dst_sel:DWORD dst_unused:UNUSED_PAD src0_sel:DWORD src1_sel:WORD_1
v_cvt_pk_bf16_f32 v138,v138,v142
v_mfma_f32_16x16x32_bf16 v[32:35],a[212:215],a[60:63],v[20:23]
ds_read_u16 v37,v37
v_accvgpr_write_b32 a11,v138
v_cvt_pk_bf16_f32 v67,v67,v71
v_accvgpr_write_b32 a12,v67
v_mfma_f32_16x16x32_bf16 v[32:35],a[208:211],a[76:79],v[32:35]
ds_read_b64_tr_b16 a[222:223],v1 offset:3072
v_cvt_pk_bf16_f32 v75,v75,v79
v_accvgpr_write_b32 a13,v75
v_cvt_pk_bf16_f32 v115,v115,v119
v_accvgpr_write_b32 a14,v115
v_pk_fma_f16 v9,v8,v8,s54
v_mfma_f32_16x16x32_bf16 v[32:35],a[204:207],a[92:95],v[32:35]
s_waitcnt lgkmcnt(7)
v_cvt_pk_bf16_f32 v139,v139,v143
v_accvgpr_write_b32 a15,v139
v_cvt_pk_bf16_f32 v80,v80,v84
v_cvt_pk_bf16_f32 v88,v88,v92
v_lshlrev_b32_sdwa v39,1,v39 dst_sel:DWORD dst_unused:UNUSED_PAD src0_sel:DWORD src1_sel:WORD_1
v_mfma_f32_16x16x32_bf16 v[136:139],a[236:239],a[252:255],v[24:27]
ds_read_u16_d16_hi v39,v39
v_cvt_pk_bf16_f32 v120,v120,v124
v_accvgpr_write_b32 a16,v80
v_accvgpr_write_b32 a17,v88
v_cvt_pk_bf16_f32 v144,v144,v148
v_pk_mul_f16 v9,v8,v9
v_mfma_f32_16x16x32_bf16 v[140:143],a[236:239],a[240:243],v[24:27]
v_accvgpr_write_b32 a18,v120
v_cvt_pk_bf16_f32 v81,v81,v85
v_accvgpr_write_b32 a19,v144
v_cvt_pk_bf16_f32 v89,v89,v93
v_lshlrev_b32_sdwa v40,1,v40 dst_sel:DWORD dst_unused:UNUSED_PAD src0_sel:DWORD src1_sel:WORD_1
v_mfma_f32_16x16x32_bf16 v[112:115],a[236:239],a[224:227],v[24:27]
v_exp_f16 v8,v9
v_accvgpr_write_b32 a20,v81
v_cvt_pk_bf16_f32 v121,v121,v125
v_accvgpr_write_b32 a21,v89
v_cvt_pk_bf16_f32 v145,v145,v149
v_mfma_f32_16x16x32_bf16 v[116:119],a[236:239],a[232:235],v[24:27]
ds_read_b128 a[236:239],v13 offset:512
ds_read_b128 v[24:27],v4 offset:3712
v_accvgpr_write_b32 a22,v121
v_cvt_pk_bf16_f32 v82,v82,v86
v_accvgpr_write_b32 a23,v145
v_mfma_f32_16x16x32_bf16 v[64:67],a[244:247],a[252:255],v[28:31]
buffer_store_dwordx4 a[44:47],v7,s[40:43],s63 offen offset:320 sc1
ds_read_b64 a[252:253],v60 offset:57408
v_cvt_pk_bf16_f32 v90,v90,v94
v_accvgpr_write_b32 a24,v82
v_accvgpr_write_b32 a25,v90
v_cvt_pk_bf16_f32 v122,v122,v126
v_lshlrev_b32_sdwa v42,1,v42 dst_sel:DWORD dst_unused:UNUSED_PAD src0_sel:DWORD src1_sel:WORD_1
v_mfma_f32_16x16x32_bf16 v[68:71],a[244:247],a[240:243],v[28:31]
ds_read_b64 a[254:255],v60 offset:57416
ds_read_b64 a[240:241],v61 offset:57408
v_accvgpr_write_b32 a26,v122
v_cvt_pk_bf16_f32 v146,v146,v150
v_accvgpr_write_b32 a27,v146
v_cvt_pk_bf16_f32 v83,v83,v87
v_mfma_f32_16x16x32_bf16 v[72:75],a[244:247],a[224:227],v[28:31]
ds_read_b64 a[242:243],v61 offset:57416
ds_read_b64 a[224:225],v62 offset:57408
v_accvgpr_write_b32 a28,v83
v_cvt_pk_bf16_f32 v91,v91,v95
v_accvgpr_write_b32 a29,v91
v_cvt_pk_bf16_f32 v123,v123,v127
v_lshlrev_b32_sdwa v41,1,v41 dst_sel:DWORD dst_unused:UNUSED_PAD src0_sel:DWORD src1_sel:WORD_1
v_mfma_f32_16x16x32_bf16 v[76:79],a[244:247],a[232:235],v[28:31]
s_waitcnt lgkmcnt(3)
ds_read_b64 a[226:227],v62 offset:57416
ds_read_b64 a[232:233],v63 offset:57408
v_exp_f16_sdwa v8,v9 dst_sel:WORD_1 dst_unused:UNUSED_PRESERVE src0_sel:WORD_1
v_accvgpr_write_b32 a30,v123
v_cvt_pk_bf16_f32 v147,v147,v151
v_accvgpr_write_b32 a31,v147
v_cvt_pk_bf16_f32 v96,v96,v100
v_mfma_f32_16x16x32_bf16 v[144:147],a[236:239],a[252:255],v[24:27]
s_waitcnt lgkmcnt(3)
ds_read_b64 a[234:235],v63 offset:57416
ds_read_b128 a[244:247],v13 offset:768
v_cvt_pk_bf16_f32 v104,v104,v108
v_cvt_pk_bf16_f32 v128,v128,v132
v_accvgpr_write_b32 a32,v96
v_accvgpr_write_b32 a33,v104
v_lshlrev_b32_sdwa v43,1,v43 dst_sel:DWORD dst_unused:UNUSED_PAD src0_sel:DWORD src1_sel:WORD_1
v_mfma_f32_16x16x32_bf16 v[148:151],a[236:239],a[240:243],v[24:27]
s_waitcnt lgkmcnt(3)
ds_read_b128 v[28:31],v4 offset:3744
v_cvt_pk_bf16_f32 v152,v152,v156
v_accvgpr_write_b32 a34,v128
v_cvt_pk_bf16_f32 v97,v97,v101
v_mfma_f32_16x16x32_bf16 v[120:123],a[236:239],a[224:227],v[24:27]
s_waitcnt lgkmcnt(2)
ds_read_u16 v40,v40
v_accvgpr_write_b32 a35,v152
v_cvt_pk_bf16_f32 v105,v105,v109
v_accvgpr_write_b32 a36,v97
v_cvt_pk_bf16_f32 v129,v129,v133
v_mfma_f32_16x16x32_bf16 v[124:127],a[236:239],a[232:235],v[24:27]
s_waitcnt lgkmcnt(1)
ds_read_b128 a[236:239],v13 offset:1024
ds_read_b128 v[24:27],v4 offset:3840
v_accvgpr_write_b32 a37,v105
v_cvt_pk_bf16_f32 v153,v153,v157
v_accvgpr_write_b32 a38,v129
v_cvt_pk_bf16_f32 v98,v98,v102
v_lshlrev_b32_sdwa v52,1,v52 dst_sel:DWORD dst_unused:UNUSED_PAD src0_sel:DWORD src1_sel:WORD_1
v_mfma_f32_16x16x32_bf16 v[80:83],a[244:247],a[252:255],v[28:31]
ds_read_b64 a[252:253],v60 offset:57472
ds_read_b64 a[254:255],v60 offset:57480
v_accvgpr_write_b32 a39,v153
v_cvt_pk_bf16_f32 v106,v106,v110
v_accvgpr_write_b32 a40,v98
v_accvgpr_write_b32 a41,v106
v_pk_fma_f16 v8,v8,s55,s55
v_mfma_f32_16x16x32_bf16 v[84:87],a[244:247],a[240:243],v[28:31]
ds_read_b64 a[240:241],v61 offset:57472
ds_read_b64 a[242:243],v61 offset:57480
v_cvt_pk_bf16_f32 v130,v130,v134
v_accvgpr_write_b32 a42,v130
v_cvt_pk_bf16_f32 v154,v154,v158
v_accvgpr_write_b32 a43,v154
v_lshlrev_b32_sdwa v54,1,v54 dst_sel:DWORD dst_unused:UNUSED_PAD src0_sel:DWORD src1_sel:WORD_1
v_mfma_f32_16x16x32_bf16 v[88:91],a[244:247],a[224:227],v[28:31]
ds_read_b64 a[224:225],v62 offset:57472
ds_read_b64 a[226:227],v62 offset:57480
v_rcp_f16 v9,v8
v_cvt_pk_bf16_f32 v99,v99,v103
v_accvgpr_write_b32 a44,v99
v_cvt_pk_bf16_f32 v107,v107,v111
v_accvgpr_write_b32 a45,v107
v_mfma_f32_16x16x32_bf16 v[92:95],a[244:247],a[232:235],v[28:31]
s_waitcnt lgkmcnt(4)
ds_read_b64 a[232:233],v63 offset:57472
ds_read_b64 a[234:235],v63 offset:57480
v_cvt_pk_bf16_f32 v131,v131,v135
v_accvgpr_write_b32 a46,v131
v_cvt_pk_bf16_f32 v155,v155,v159
v_accvgpr_write_b32 a47,v155
v_mfma_f32_16x16x32_bf16 v[152:155],a[236:239],a[252:255],v[24:27]
s_waitcnt lgkmcnt(4)
ds_read_b128 a[244:247],v13 offset:1280
ds_read_b128 v[28:31],v4 offset:3872
v_lshlrev_b32_sdwa v53,1,v53 dst_sel:DWORD dst_unused:UNUSED_PAD src0_sel:DWORD src1_sel:WORD_1
v_mfma_f32_16x16x32_bf16 v[156:159],a[236:239],a[240:243],v[24:27]
s_waitcnt lgkmcnt(4)
ds_read_u16_d16_hi v42,v42
v_lshlrev_b32_sdwa v55,1,v55 dst_sel:DWORD dst_unused:UNUSED_PAD src0_sel:DWORD src1_sel:WORD_1
v_mfma_f32_16x16x32_bf16 v[128:131],a[236:239],a[224:227],v[24:27]
s_waitcnt lgkmcnt(3)
ds_read_u16 v41,v41
v_lshlrev_b32_sdwa v56,1,v56 dst_sel:DWORD dst_unused:UNUSED_PAD src0_sel:DWORD src1_sel:WORD_1
v_mfma_f32_16x16x32_bf16 v[132:135],a[236:239],a[232:235],v[24:27]
s_waitcnt lgkmcnt(2)
ds_read_b64_tr_b16 a[236:237],v3 offset:64
ds_read_b64_tr_b16 a[238:239],v3 offset:1600
v_rcp_f16_sdwa v9,v8 dst_sel:WORD_1 dst_unused:UNUSED_PRESERVE src0_sel:WORD_1
v_mfma_f32_16x16x32_bf16 v[96:99],a[244:247],a[252:255],v[28:31]
ds_read_b64_tr_b16 a[252:253],v3 offset:72
ds_read_b64_tr_b16 a[254:255],v3 offset:1608
v_mfma_f32_16x16x32_bf16 v[100:103],a[244:247],a[240:243],v[28:31]
ds_read_b64_tr_b16 a[240:241],v3 offset:128
ds_read_b64_tr_b16 a[242:243],v3 offset:1664
v_mfma_f32_16x16x32_bf16 v[104:107],a[244:247],a[224:227],v[28:31]
ds_read_b64_tr_b16 a[224:225],v3 offset:136
ds_read_b64_tr_b16 a[226:227],v3 offset:1672
v_mfma_f32_16x16x32_bf16 v[108:111],a[244:247],a[232:235],v[28:31]
ds_read_u16_d16_hi v43,v43
ds_read_u16 v52,v52
v_lshlrev_b32_sdwa v58,1,v58 dst_sel:DWORD dst_unused:UNUSED_PAD src0_sel:DWORD src1_sel:WORD_1
v_mfma_f32_16x16x32_bf16 v[60:63],a[200:203],a[92:95],v[16:19]
ds_read_u16_d16_hi v54,v54
ds_read_u16 v53,v53
v_fma_mix_f32 v57,v57,v9,0 op_sel_hi:[0,1,0]
v_mfma_f32_16x16x32_bf16 v[60:63],a[196:199],a[76:79],v[60:63]
ds_read_u16_d16_hi v55,v55
v_fma_mix_f32 v59,v59,v9,0 op_sel:[0,1,0] op_sel_hi:[0,1,0]
v_mfma_f32_16x16x32_bf16 v[60:63],a[192:195],a[60:63],v[60:63]
s_barrier
ds_read_u16 v56,v56
ds_read_u16_d16_hi v58,v58
v_mfma_f32_16x16x32_bf16 v[24:27],a[192:195],a[0:3],v[16:19]
s_add_u32 m0,s57,24752
v_lshlrev_b32_sdwa v44,1,v44 dst_sel:DWORD dst_unused:UNUSED_PAD src0_sel:DWORD src1_sel:WORD_1
v_fma_mixlo_f16 v8,v33,s99,0
v_mfma_f32_16x16x32_bf16 v[24:27],a[196:199],a[16:19],v[24:27]
s_add_u32 s89,s56,5603328
ds_read_u16 v44,v44
v_lshlrev_b32_sdwa v46,1,v46 dst_sel:DWORD dst_unused:UNUSED_PAD src0_sel:DWORD src1_sel:WORD_1
v_mfma_f32_16x16x32_bf16 v[24:27],a[200:203],a[32:35],v[24:27]
buffer_load_dwordx4 v6,s[36:39],s89 offen lds
v_lshl_add_u64 v[36:37],v[36:37],0,v[38:39]
v_mfma_f32_16x16x32_bf16 v[28:31],a[204:207],a[32:35],v[20:23]
s_waitcnt lgkmcnt(7)
ds_read_u16_d16_hi v46,v46
v_lshl_add_u64 v[38:39],v[40:41],0,v[42:43]
v_fma_mixhi_f16 v8,v35,s99,0
v_mfma_f32_16x16x32_bf16 v[28:31],a[208:211],a[16:19],v[28:31]
s_waitcnt lgkmcnt(4)
v_lshl_add_u64 v[40:41],v[52:53],0,v[54:55]
v_lshlrev_b32_sdwa v45,1,v45 dst_sel:DWORD dst_unused:UNUSED_PAD src0_sel:DWORD src1_sel:WORD_1
v_pk_fma_f16 v9,v8,v8,s54
v_mfma_f32_16x16x32_bf16 v[28:31],a[212:215],a[0:3],v[28:31]
ds_read_u16 v45,v45
v_lshlrev_b32_sdwa v47,1,v47 dst_sel:DWORD dst_unused:UNUSED_PAD src0_sel:DWORD src1_sel:WORD_1
v_mfma_f32_16x16x32_bf16 v[52:55],a[212:215],a[4:7],v[20:23]
s_waitcnt lgkmcnt(3)
ds_read_u16_d16_hi v47,v47
v_or_b32 v42,v56,v58
v_pk_mul_f16 v9,v8,v9
v_mfma_f32_16x16x32_bf16 v[52:55],a[208:211],a[20:23],v[52:55]
v_cvt_pk_bf16_f32 v43,v57,v59
v_lshlrev_b32_sdwa v48,1,v48 dst_sel:DWORD dst_unused:UNUSED_PAD src0_sel:DWORD src1_sel:WORD_1
v_mfma_f32_16x16x32_bf16 v[52:55],a[204:207],a[36:39],v[52:55]
v_exp_f16 v8,v9
v_mfma_f32_16x16x32_bf16 v[56:59],a[200:203],a[36:39],v[16:19]
ds_read_u16 v48,v48
v_lshlrev_b32_sdwa v50,1,v50 dst_sel:DWORD dst_unused:UNUSED_PAD src0_sel:DWORD src1_sel:WORD_1
v_mfma_f32_16x16x32_bf16 v[56:59],a[196:199],a[20:23],v[56:59]
ds_read_u16_d16_hi v50,v50
v_lshlrev_b32_sdwa v49,1,v49 dst_sel:DWORD dst_unused:UNUSED_PAD src0_sel:DWORD src1_sel:WORD_1
v_mfma_f32_16x16x32_bf16 v[56:59],a[192:195],a[4:7],v[56:59]
v_exp_f16_sdwa v8,v9 dst_sel:WORD_1 dst_unused:UNUSED_PRESERVE src0_sel:WORD_1
v_mfma_f32_16x16x32_bf16 v[232:235],a[228:231],v[36:39],v[232:235]
ds_read_u16 v49,v49
v_lshlrev_b32_sdwa v51,1,v51 dst_sel:DWORD dst_unused:UNUSED_PAD src0_sel:DWORD src1_sel:WORD_1
v_mfma_f32_16x16x32_bf16 v[236:239],a[228:231],v[40:43],v[236:239]
ds_read_u16_d16_hi v51,v51
v_lshlrev_b32_sdwa v60,1,v60 dst_sel:DWORD dst_unused:UNUSED_PAD src0_sel:DWORD src1_sel:WORD_1
v_mfma_f32_16x16x32_bf16 v[164:167],a[248:251],v[40:43],v[164:167]
ds_read_u16 v60,v60
v_pk_fma_f16 v8,v8,s55,s55
v_mfma_f32_16x16x32_bf16 v[160:163],a[248:251],v[36:39],v[160:163]
v_rcp_f16 v9,v8
v_lshlrev_b32_sdwa v62,1,v62 dst_sel:DWORD dst_unused:UNUSED_PAD src0_sel:DWORD src1_sel:WORD_1
v_mfma_f32_16x16x32_bf16 v[240:243],a[236:239],v[36:39],v[240:243]
ds_read_u16_d16_hi v62,v62
v_lshlrev_b32_sdwa v61,1,v61 dst_sel:DWORD dst_unused:UNUSED_PAD src0_sel:DWORD src1_sel:WORD_1
v_mfma_f32_16x16x32_bf16 v[244:247],a[236:239],v[40:43],v[244:247]
ds_read_u16 v61,v61
v_lshlrev_b32_sdwa v63,1,v63 dst_sel:DWORD dst_unused:UNUSED_PAD src0_sel:DWORD src1_sel:WORD_1
v_mfma_f32_16x16x32_bf16 v[180:183],a[252:255],v[40:43],v[180:183]
ds_read_u16_d16_hi v63,v63
v_lshlrev_b32_sdwa v32,1,v32 dst_sel:DWORD dst_unused:UNUSED_PAD src0_sel:DWORD src1_sel:WORD_1
v_mfma_f32_16x16x32_bf16 v[176:179],a[252:255],v[36:39],v[176:179]
v_rcp_f16_sdwa v9,v8 dst_sel:WORD_1 dst_unused:UNUSED_PRESERVE src0_sel:WORD_1
v_mfma_f32_16x16x32_bf16 v[248:251],a[240:243],v[36:39],v[248:251]
ds_read_u16 v32,v32
v_lshlrev_b32_sdwa v34,1,v34 dst_sel:DWORD dst_unused:UNUSED_PAD src0_sel:DWORD src1_sel:WORD_1
v_mfma_f32_16x16x32_bf16 v[252:255],a[240:243],v[40:43],v[252:255]
ds_read_u16_d16_hi v34,v34
v_fma_mix_f32 v33,v33,v9,0 op_sel_hi:[0,1,0]
v_mfma_f32_16x16x32_bf16 v[196:199],a[224:227],v[40:43],v[196:199]
v_fma_mix_f32 v35,v35,v9,0 op_sel:[0,1,0] op_sel_hi:[0,1,0]
v_mfma_f32_16x16x32_bf16 v[192:195],a[224:227],v[36:39],v[192:195]
v_mfma_f32_16x16x32_bf16 v[40:43],a[192:195],a[8:11],v[16:19]
v_lshlrev_b32_sdwa v24,1,v24 dst_sel:DWORD dst_unused:UNUSED_PAD src0_sel:DWORD src1_sel:WORD_1
v_fma_mixlo_f16 v8,v53,s99,0
v_mfma_f32_16x16x32_bf16 v[40:43],a[196:199],a[24:27],v[40:43]
ds_read_u16 v24,v24
v_lshlrev_b32_sdwa v26,1,v26 dst_sel:DWORD dst_unused:UNUSED_PAD src0_sel:DWORD src1_sel:WORD_1
v_mfma_f32_16x16x32_bf16 v[40:43],a[200:203],a[40:43],v[40:43]
s_waitcnt lgkmcnt(4)
ds_read_u16_d16_hi v26,v26
v_lshl_add_u64 v[44:45],v[44:45],0,v[46:47]
v_mfma_f32_16x16x32_bf16 v[36:39],a[204:207],a[40:43],v[20:23]
v_lshl_add_u64 v[46:47],v[48:49],0,v[50:51]
v_fma_mixhi_f16 v8,v55,s99,0
v_mfma_f32_16x16x32_bf16 v[36:39],a[208:211],a[24:27],v[36:39]
s_waitcnt lgkmcnt(4)
v_lshl_add_u64 v[48:49],v[60:61],0,v[62:63]
v_lshlrev_b32_sdwa v25,1,v25 dst_sel:DWORD dst_unused:UNUSED_PAD src0_sel:DWORD src1_sel:WORD_1
v_pk_fma_f16 v9,v8,v8,s54
v_mfma_f32_16x16x32_bf16 v[36:39],a[212:215],a[8:11],v[36:39]
ds_read_u16 v25,v25
v_lshlrev_b32_sdwa v27,1,v27 dst_sel:DWORD dst_unused:UNUSED_PAD src0_sel:DWORD src1_sel:WORD_1
v_mfma_f32_16x16x32_bf16 v[60:63],a[212:215],a[12:15],v[20:23]
s_waitcnt lgkmcnt(3)
ds_read_b64_tr_b16 a[212:213],v1 offset:4096
ds_read_b64_tr_b16 a[214:215],v1 offset:5120
v_or_b32 v50,v32,v34
v_pk_mul_f16 v9,v8,v9
v_mfma_f32_16x16x32_bf16 v[60:63],a[208:211],a[28:31],v[60:63]
ds_read_b64_tr_b16 a[208:209],v2 offset:4096
ds_read_b64_tr_b16 a[210:211],v2 offset:5120
v_cvt_pk_bf16_f32 v51,v33,v35
v_lshlrev_b32_sdwa v28,1,v28 dst_sel:DWORD dst_unused:UNUSED_PAD src0_sel:DWORD src1_sel:WORD_1
v_mfma_f32_16x16x32_bf16 v[60:63],a[204:207],a[44:47],v[60:63]
ds_read_b64_tr_b16 a[204:205],v2 offset:2048
ds_read_b64_tr_b16 a[206:207],v2 offset:3072
v_exp_f16 v8,v9
v_mfma_f32_16x16x32_bf16 v[32:35],a[200:203],a[44:47],v[16:19]
ds_read_b64_tr_b16 a[200:201],v2 offset:0
ds_read_b64_tr_b16 a[202:203],v2 offset:1024
v_lshlrev_b32_sdwa v30,1,v30 dst_sel:DWORD dst_unused:UNUSED_PAD src0_sel:DWORD src1_sel:WORD_1
v_mfma_f32_16x16x32_bf16 v[32:35],a[196:199],a[28:31],v[32:35]
ds_read_b128 v[20:23],v4 offset:160
v_lshlrev_b32_sdwa v29,1,v29 dst_sel:DWORD dst_unused:UNUSED_PAD src0_sel:DWORD src1_sel:WORD_1
v_mfma_f32_16x16x32_bf16 v[32:35],a[192:195],a[12:15],v[32:35]
TP6020_%=:
s_cbranch_execz TR0_%=
v_exp_f16_sdwa v8,v9 dst_sel:WORD_1 dst_unused:UNUSED_PRESERVE src0_sel:WORD_1
v_mfma_f32_16x16x32_bf16 v[208:211],a[228:231],v[44:47],v[208:211]
ds_read_b128 v[16:19],v4 offset:128
v_lshlrev_b32_sdwa v31,1,v31 dst_sel:DWORD dst_unused:UNUSED_PAD src0_sel:DWORD src1_sel:WORD_1
v_mfma_f32_16x16x32_bf16 v[212:215],a[228:231],v[48:51],v[212:215]
ds_read_u16_d16_hi v27,v27
v_lshlrev_b32_sdwa v56,1,v56 dst_sel:DWORD dst_unused:UNUSED_PAD src0_sel:DWORD src1_sel:WORD_1
v_mfma_f32_16x16x32_bf16 v[172:175],a[248:251],v[48:51],v[172:175]
ds_read_u16 v28,v28
v_pk_fma_f16 v8,v8,s55,s55
v_mfma_f32_16x16x32_bf16 v[168:171],a[248:251],v[44:47],v[168:171]
v_rcp_f16 v9,v8
v_lshlrev_b32_sdwa v58,1,v58 dst_sel:DWORD dst_unused:UNUSED_PAD src0_sel:DWORD src1_sel:WORD_1
v_mfma_f32_16x16x32_bf16 v[216:219],a[236:239],v[44:47],v[216:219]
ds_read_u16_d16_hi v30,v30
v_lshlrev_b32_sdwa v57,1,v57 dst_sel:DWORD dst_unused:UNUSED_PAD src0_sel:DWORD src1_sel:WORD_1
v_mfma_f32_16x16x32_bf16 v[220:223],a[236:239],v[48:51],v[220:223]
ds_read_u16 v29,v29
v_lshlrev_b32_sdwa v59,1,v59 dst_sel:DWORD dst_unused:UNUSED_PAD src0_sel:DWORD src1_sel:WORD_1
v_mfma_f32_16x16x32_bf16 v[188:191],a[252:255],v[48:51],v[188:191]
ds_read_u16_d16_hi v31,v31
v_lshlrev_b32_sdwa v52,1,v52 dst_sel:DWORD dst_unused:UNUSED_PAD src0_sel:DWORD src1_sel:WORD_1
v_mfma_f32_16x16x32_bf16 v[184:187],a[252:255],v[44:47],v[184:187]
v_rcp_f16_sdwa v9,v8 dst_sel:WORD_1 dst_unused:UNUSED_PRESERVE src0_sel:WORD_1
v_mfma_f32_16x16x32_bf16 v[224:227],a[240:243],v[44:47],v[224:227]
ds_read_u16 v56,v56
ds_read_u16_d16_hi v58,v58
v_mfma_f32_16x16x32_bf16 v[228:231],a[240:243],v[48:51],v[228:231]
ds_read_u16 v57,v57
ds_read_u16_d16_hi v59,v59
v_lshlrev_b32_sdwa v54,1,v54 dst_sel:DWORD dst_unused:UNUSED_PAD src0_sel:DWORD src1_sel:WORD_1
v_mfma_f32_16x16x32_bf16 v[204:207],a[224:227],v[48:51],v[204:207]
ds_read_u16 v52,v52
v_fma_mix_f32 v53,v53,v9,0 op_sel_hi:[0,1,0]
v_mfma_f32_16x16x32_bf16 v[200:203],a[224:227],v[44:47],v[200:203]
s_waitcnt lgkmcnt(8)
ds_read_u16_d16_hi v54,v54
v_fma_mix_f32 v55,v55,v9,0 op_sel:[0,1,0] op_sel_hi:[0,1,0]
v_mfma_f32_16x16x32_bf16 v[48:51],a[216:219],a[48:51],v[16:19]
s_add_u32 m0,s57,28296
v_lshlrev_b32_sdwa v40,1,v40 dst_sel:DWORD dst_unused:UNUSED_PAD src0_sel:DWORD src1_sel:WORD_1
v_fma_mixlo_f16 v8,v61,s99,0
v_mfma_f32_16x16x32_bf16 v[48:51],a[220:223],a[64:67],v[48:51]
s_add_u32 s89,s56,5701632
ds_read_u16 v40,v40
v_lshlrev_b32_sdwa v42,1,v42 dst_sel:DWORD dst_unused:UNUSED_PAD src0_sel:DWORD src1_sel:WORD_1
v_mfma_f32_16x16x32_bf16 v[48:51],a[212:215],a[80:83],v[48:51]
buffer_load_dwordx4 v6,s[36:39],s89 offen lds
v_lshl_add_u64 v[24:25],v[24:25],0,v[26:27]
v_mfma_f32_16x16x32_bf16 v[44:47],a[208:211],a[80:83],v[20:23]
s_waitcnt lgkmcnt(7)
ds_read_u16_d16_hi v42,v42
v_lshl_add_u64 v[26:27],v[28:29],0,v[30:31]
v_fma_mixhi_f16 v8,v63,s99,0
v_mfma_f32_16x16x32_bf16 v[44:47],a[204:207],a[64:67],v[44:47]
s_waitcnt lgkmcnt(4)
v_lshl_add_u64 v[28:29],v[56:57],0,v[58:59]
v_lshlrev_b32_sdwa v41,1,v41 dst_sel:DWORD dst_unused:UNUSED_PAD src0_sel:DWORD src1_sel:WORD_1
v_pk_fma_f16 v9,v8,v8,s54
v_mfma_f32_16x16x32_bf16 v[44:47],a[200:203],a[48:51],v[44:47]
ds_read_u16 v41,v41
v_lshlrev_b32_sdwa v43,1,v43 dst_sel:DWORD dst_unused:UNUSED_PAD src0_sel:DWORD src1_sel:WORD_1
v_mfma_f32_16x16x32_bf16 v[56:59],a[200:203],a[52:55],v[20:23]
s_waitcnt lgkmcnt(3)
ds_read_u16_d16_hi v43,v43
v_or_b32 v30,v52,v54
v_pk_mul_f16 v9,v8,v9
v_mfma_f32_16x16x32_bf16 v[56:59],a[204:207],a[68:71],v[56:59]
s_waitcnt vmcnt(21)
v_cvt_pk_bf16_f32 v31,v53,v55
v_lshlrev_b32_sdwa v36,1,v36 dst_sel:DWORD dst_unused:UNUSED_PAD src0_sel:DWORD src1_sel:WORD_1
v_mfma_f32_16x16x32_bf16 v[56:59],a[208:211],a[84:87],v[56:59]
s_barrier
v_exp_f16 v8,v9
v_mfma_f32_16x16x32_bf16 v[52:55],a[212:215],a[84:87],v[16:19]
ds_read_u16 v36,v36
v_lshlrev_b32_sdwa v38,1,v38 dst_sel:DWORD dst_unused:UNUSED_PAD src0_sel:DWORD src1_sel:WORD_1
v_mfma_f32_16x16x32_bf16 v[52:55],a[220:223],a[68:71],v[52:55]
s_add_u32 m0,s58,0
ds_read_u16_d16_hi v38,v38
v_lshlrev_b32_sdwa v37,1,v37 dst_sel:DWORD dst_unused:UNUSED_PAD src0_sel:DWORD src1_sel:WORD_1
v_mfma_f32_16x16x32_bf16 v[52:55],a[216:219],a[52:55],v[52:55]
s_add_u32 s69,s69,s59
v_exp_f16_sdwa v8,v9 dst_sel:WORD_1 dst_unused:UNUSED_PRESERVE src0_sel:WORD_1
v_mfma_f32_16x16x32_bf16 v[136:139],a[228:231],v[24:27],v[136:139]
buffer_load_dwordx4 v5,s[44:47],s69 offen offset:0 lds
v_lshlrev_b32_sdwa v39,1,v39 dst_sel:DWORD dst_unused:UNUSED_PAD src0_sel:DWORD src1_sel:WORD_1
v_mfma_f32_16x16x32_bf16 v[140:143],a[228:231],v[28:31],v[140:143]
ds_read_u16 v37,v37
v_lshlrev_b32_sdwa v32,1,v32 dst_sel:DWORD dst_unused:UNUSED_PAD src0_sel:DWORD src1_sel:WORD_1
v_mfma_f32_16x16x32_bf16 v[68:71],a[248:251],v[28:31],v[68:71]
ds_read_u16_d16_hi v39,v39
v_pk_fma_f16 v8,v8,s55,s55
v_mfma_f32_16x16x32_bf16 v[64:67],a[248:251],v[24:27],v[64:67]
v_rcp_f16 v9,v8
v_lshlrev_b32_sdwa v34,1,v34 dst_sel:DWORD dst_unused:UNUSED_PAD src0_sel:DWORD src1_sel:WORD_1
v_mfma_f32_16x16x32_bf16 v[144:147],a[236:239],v[24:27],v[144:147]
s_add_u32 s69,s69,s53
ds_read_u16 v32,v32
v_lshlrev_b32_sdwa v33,1,v33 dst_sel:DWORD dst_unused:UNUSED_PAD src0_sel:DWORD src1_sel:WORD_1
v_mfma_f32_16x16x32_bf16 v[148:151],a[236:239],v[28:31],v[148:151]
buffer_load_dwordx4 v5,s[44:47],s69 offen offset:1024 lds
ds_read_u16_d16_hi v34,v34
v_mfma_f32_16x16x32_bf16 v[84:87],a[252:255],v[28:31],v[84:87]
ds_read_u16 v33,v33
v_lshlrev_b32_sdwa v35,1,v35 dst_sel:DWORD dst_unused:UNUSED_PAD src0_sel:DWORD src1_sel:WORD_1
v_mfma_f32_16x16x32_bf16 v[80:83],a[252:255],v[24:27],v[80:83]
v_rcp_f16_sdwa v9,v8 dst_sel:WORD_1 dst_unused:UNUSED_PRESERVE src0_sel:WORD_1
v_mfma_f32_16x16x32_bf16 v[152:155],a[240:243],v[24:27],v[152:155]
ds_read_u16_d16_hi v35,v35
v_lshlrev_b32_sdwa v60,1,v60 dst_sel:DWORD dst_unused:UNUSED_PAD src0_sel:DWORD src1_sel:WORD_1
v_mfma_f32_16x16x32_bf16 v[156:159],a[240:243],v[28:31],v[156:159]
s_add_u32 s69,s69,s53
ds_read_u16 v60,v60
v_lshlrev_b32_sdwa v62,1,v62 dst_sel:DWORD dst_unused:UNUSED_PAD src0_sel:DWORD src1_sel:WORD_1
v_mfma_f32_16x16x32_bf16 v[100:103],a[224:227],v[28:31],v[100:103]
buffer_load_dwordx4 v5,s[44:47],s69 offen offset:2048 lds
v_fma_mix_f32 v61,v61,v9,0 op_sel_hi:[0,1,0]
v_mfma_f32_16x16x32_bf16 v[96:99],a[224:227],v[24:27],v[96:99]
ds_read_u16_d16_hi v62,v62
v_fma_mix_f32 v63,v63,v9,0 op_sel:[0,1,0] op_sel_hi:[0,1,0]
v_mfma_f32_16x16x32_bf16 v[28:31],a[216:219],a[56:59],v[16:19]
ds_read_b64_tr_b16 a[232:233],v3 offset:6160
ds_read_b64_tr_b16 a[234:235],v3 offset:7696
v_lshlrev_b32_sdwa v48,1,v48 dst_sel:DWORD dst_unused:UNUSED_PAD src0_sel:DWORD src1_sel:WORD_1
v_mfma_f32_16x16x32_bf16 v[28:31],a[220:223],a[72:75],v[28:31]
ds_read_u16 v48,v48
ds_read_b64_tr_b16 a[244:245],v3 offset:6168
v_fma_mixlo_f16 v8,v57,s99,0
v_mfma_f32_16x16x32_bf16 v[28:31],a[212:215],a[88:91],v[28:31]
s_waitcnt lgkmcnt(8)
ds_read_b64_tr_b16 a[246:247],v3 offset:7704
v_lshl_add_u64 v[40:41],v[40:41],0,v[42:43]
v_lshlrev_b32_sdwa v50,1,v50 dst_sel:DWORD dst_unused:UNUSED_PAD src0_sel:DWORD src1_sel:WORD_1
v_mfma_f32_16x16x32_bf16 v[24:27],a[208:211],a[88:91],v[20:23]
ds_read_u16_d16_hi v50,v50
v_lshl_add_u64 v[42:43],v[36:37],0,v[38:39]
v_fma_mixhi_f16 v8,v59,s99,0
v_mfma_f32_16x16x32_bf16 v[24:27],a[204:207],a[72:75],v[24:27]
s_waitcnt lgkmcnt(8)
ds_read_b64_tr_b16 a[196:197],v1 offset:6160
v_lshl_add_u64 v[36:37],v[32:33],0,v[34:35]
v_lshlrev_b32_sdwa v49,1,v49 dst_sel:DWORD dst_unused:UNUSED_PAD src0_sel:DWORD src1_sel:WORD_1
v_pk_fma_f16 v9,v8,v8,s54
v_mfma_f32_16x16x32_bf16 v[24:27],a[200:203],a[56:59],v[24:27]
ds_read_u16 v49,v49
v_lshlrev_b32_sdwa v51,1,v51 dst_sel:DWORD dst_unused:UNUSED_PAD src0_sel:DWORD src1_sel:WORD_1
v_mfma_f32_16x16x32_bf16 v[32:35],a[200:203],a[60:63],v[20:23]
s_waitcnt lgkmcnt(8)
ds_read_u16_d16_hi v51,v51
v_or_b32 v38,v60,v62
v_pk_mul_f16 v9,v8,v9
v_mfma_f32_16x16x32_bf16 v[32:35],a[204:207],a[76:79],v[32:35]
ds_read_b64_tr_b16 a[198:199],v1 offset:7184
v_cvt_pk_bf16_f32 v39,v61,v63
v_lshlrev_b32_sdwa v44,1,v44 dst_sel:DWORD dst_unused:UNUSED_PAD src0_sel:DWORD src1_sel:WORD_1
v_mfma_f32_16x16x32_bf16 v[32:35],a[208:211],a[92:95],v[32:35]
v_exp_f16 v8,v9
v_mfma_f32_16x16x32_bf16 v[60:63],a[212:215],a[92:95],v[16:19]
ds_read_u16 v44,v44
v_lshlrev_b32_sdwa v46,1,v46 dst_sel:DWORD dst_unused:UNUSED_PAD src0_sel:DWORD src1_sel:WORD_1
v_mfma_f32_16x16x32_bf16 v[60:63],a[220:223],a[76:79],v[60:63]
ds_read_u16_d16_hi v46,v46
v_lshlrev_b32_sdwa v45,1,v45 dst_sel:DWORD dst_unused:UNUSED_PAD src0_sel:DWORD src1_sel:WORD_1
v_mfma_f32_16x16x32_bf16 v[60:63],a[216:219],a[60:63],v[60:63]
v_exp_f16_sdwa v8,v9 dst_sel:WORD_1 dst_unused:UNUSED_PRESERVE src0_sel:WORD_1
v_mfma_f32_16x16x32_bf16 v[112:115],a[228:231],v[40:43],v[112:115]
ds_read_u16 v45,v45
v_lshlrev_b32_sdwa v47,1,v47 dst_sel:DWORD dst_unused:UNUSED_PAD src0_sel:DWORD src1_sel:WORD_1
v_mfma_f32_16x16x32_bf16 v[116:119],a[228:231],v[36:39],v[116:119]
ds_read_u16_d16_hi v47,v47
v_lshlrev_b32_sdwa v52,1,v52 dst_sel:DWORD dst_unused:UNUSED_PAD src0_sel:DWORD src1_sel:WORD_1
v_mfma_f32_16x16x32_bf16 v[76:79],a[248:251],v[36:39],v[76:79]
ds_read_u16 v52,v52
v_pk_fma_f16 v8,v8,s55,s55
v_mfma_f32_16x16x32_bf16 v[72:75],a[248:251],v[40:43],v[72:75]
ds_read_b64_tr_b16 a[228:229],v3 offset:6224
v_rcp_f16 v9,v8
v_lshlrev_b32_sdwa v54,1,v54 dst_sel:DWORD dst_unused:UNUSED_PAD src0_sel:DWORD src1_sel:WORD_1
v_mfma_f32_16x16x32_bf16 v[120:123],a[236:239],v[40:43],v[120:123]
ds_read_u16_d16_hi v54,v54
v_lshlrev_b32_sdwa v53,1,v53 dst_sel:DWORD dst_unused:UNUSED_PAD src0_sel:DWORD src1_sel:WORD_1
v_mfma_f32_16x16x32_bf16 v[124:127],a[236:239],v[36:39],v[124:127]
ds_read_u16 v53,v53
ds_read_b64_tr_b16 a[230:231],v3 offset:7760
v_mfma_f32_16x16x32_bf16 v[92:95],a[252:255],v[36:39],v[92:95]
ds_read_b64_tr_b16 a[248:249],v3 offset:6232
v_lshlrev_b32_sdwa v55,1,v55 dst_sel:DWORD dst_unused:UNUSED_PAD src0_sel:DWORD src1_sel:WORD_1
v_mfma_f32_16x16x32_bf16 v[88:91],a[252:255],v[40:43],v[88:91]
ds_read_u16_d16_hi v55,v55
v_rcp_f16_sdwa v9,v8 dst_sel:WORD_1 dst_unused:UNUSED_PRESERVE src0_sel:WORD_1
v_mfma_f32_16x16x32_bf16 v[128:131],a[240:243],v[40:43],v[128:131]
ds_read_b64_tr_b16 a[250:251],v3 offset:7768
ds_read_b64_tr_b16 a[236:237],v3 offset:6288
v_lshlrev_b32_sdwa v56,1,v56 dst_sel:DWORD dst_unused:UNUSED_PAD src0_sel:DWORD src1_sel:WORD_1
v_mfma_f32_16x16x32_bf16 v[132:135],a[240:243],v[36:39],v[132:135]
ds_read_u16 v56,v56
v_lshlrev_b32_sdwa v58,1,v58 dst_sel:DWORD dst_unused:UNUSED_PAD src0_sel:DWORD src1_sel:WORD_1
v_mfma_f32_16x16x32_bf16 v[108:111],a[224:227],v[36:39],v[108:111]
ds_read_u16_d16_hi v58,v58
v_fma_mix_f32 v57,v57,v9,0 op_sel_hi:[0,1,0]
v_mfma_f32_16x16x32_bf16 v[104:107],a[224:227],v[40:43],v[104:107]
ds_read_b64_tr_b16 a[238:239],v3 offset:7824
v_fma_mix_f32 v59,v59,v9,0 op_sel:[0,1,0] op_sel_hi:[0,1,0]
v_mfma_f32_16x16x32_bf16 v[36:39],a[216:219],a[0:3],v[16:19]
s_add_u32 m0,s57,31832
ds_read_b64_tr_b16 a[252:253],v3 offset:6296
v_lshlrev_b32_sdwa v28,1,v28 dst_sel:DWORD dst_unused:UNUSED_PAD src0_sel:DWORD src1_sel:WORD_1
v_mfma_f32_16x16x32_bf16 v[36:39],a[220:223],a[16:19],v[36:39]
s_add_u32 s89,s56,5799936
ds_read_b64_tr_b16 a[254:255],v3 offset:7832
v_fma_mixlo_f16 v8,v33,s99,0
v_mfma_f32_16x16x32_bf16 v[36:39],a[212:215],a[32:35],v[36:39]
s_waitcnt lgkmcnt(9)
buffer_load_dwordx4 v6,s[36:39],s89 offen lds
v_lshl_add_u64 v[48:49],v[48:49],0,v[50:51]
v_lshlrev_b32_sdwa v30,1,v30 dst_sel:DWORD dst_unused:UNUSED_PAD src0_sel:DWORD src1_sel:WORD_1
v_mfma_f32_16x16x32_bf16 v[40:43],a[208:211],a[32:35],v[20:23]
ds_read_u16 v28,v28
v_lshl_add_u64 v[50:51],v[44:45],0,v[46:47]
v_fma_mixhi_f16 v8,v35,s99,0
v_mfma_f32_16x16x32_bf16 v[40:43],a[204:207],a[16:19],v[40:43]
s_waitcnt lgkmcnt(8)
ds_read_u16_d16_hi v30,v30
v_lshl_add_u64 v[44:45],v[52:53],0,v[54:55]
v_lshlrev_b32_sdwa v29,1,v29 dst_sel:DWORD dst_unused:UNUSED_PAD src0_sel:DWORD src1_sel:WORD_1
v_pk_fma_f16 v9,v8,v8,s54
v_mfma_f32_16x16x32_bf16 v[40:43],a[200:203],a[0:3],v[40:43]
ds_read_u16 v29,v29
v_lshlrev_b32_sdwa v31,1,v31 dst_sel:DWORD dst_unused:UNUSED_PAD src0_sel:DWORD src1_sel:WORD_1
v_mfma_f32_16x16x32_bf16 v[52:55],a[200:203],a[4:7],v[20:23]
s_waitcnt lgkmcnt(6)
ds_read_u16_d16_hi v31,v31
v_or_b32 v46,v56,v58
v_pk_mul_f16 v9,v8,v9
v_mfma_f32_16x16x32_bf16 v[52:55],a[204:207],a[20:23],v[52:55]
ds_read_b64_tr_b16 a[192:193],v1 offset:8208
v_cvt_pk_bf16_f32 v47,v57,v59
v_lshlrev_b32_sdwa v24,1,v24 dst_sel:DWORD dst_unused:UNUSED_PAD src0_sel:DWORD src1_sel:WORD_1
v_mfma_f32_16x16x32_bf16 v[52:55],a[208:211],a[36:39],v[52:55]
v_exp_f16 v8,v9
v_mfma_f32_16x16x32_bf16 v[56:59],a[212:215],a[36:39],v[16:19]
ds_read_u16 v24,v24
v_lshlrev_b32_sdwa v26,1,v26 dst_sel:DWORD dst_unused:UNUSED_PAD src0_sel:DWORD src1_sel:WORD_1
v_mfma_f32_16x16x32_bf16 v[56:59],a[220:223],a[20:23],v[56:59]
ds_read_u16_d16_hi v26,v26
v_lshlrev_b32_sdwa v25,1,v25 dst_sel:DWORD dst_unused:UNUSED_PAD src0_sel:DWORD src1_sel:WORD_1
v_mfma_f32_16x16x32_bf16 v[56:59],a[216:219],a[4:7],v[56:59]
v_exp_f16_sdwa v8,v9 dst_sel:WORD_1 dst_unused:UNUSED_PRESERVE src0_sel:WORD_1
v_mfma_f32_16x16x32_bf16 v[232:235],a[232:235],v[48:51],v[232:235]
ds_read_u16 v25,v25
v_lshlrev_b32_sdwa v27,1,v27 dst_sel:DWORD dst_unused:UNUSED_PAD src0_sel:DWORD src1_sel:WORD_1
v_mfma_f32_16x16x32_bf16 v[236:239],a[232:235],v[44:47],v[236:239]
ds_read_u16_d16_hi v27,v27
v_lshlrev_b32_sdwa v60,1,v60 dst_sel:DWORD dst_unused:UNUSED_PAD src0_sel:DWORD src1_sel:WORD_1
v_mfma_f32_16x16x32_bf16 v[164:167],a[244:247],v[44:47],v[164:167]
ds_read_u16 v60,v60
v_pk_fma_f16 v8,v8,s55,s55
v_mfma_f32_16x16x32_bf16 v[160:163],a[244:247],v[48:51],v[160:163]
v_rcp_f16 v9,v8
v_lshlrev_b32_sdwa v62,1,v62 dst_sel:DWORD dst_unused:UNUSED_PAD src0_sel:DWORD src1_sel:WORD_1
v_mfma_f32_16x16x32_bf16 v[240:243],a[228:231],v[48:51],v[240:243]
ds_read_u16_d16_hi v62,v62
v_lshlrev_b32_sdwa v61,1,v61 dst_sel:DWORD dst_unused:UNUSED_PAD src0_sel:DWORD src1_sel:WORD_1
v_mfma_f32_16x16x32_bf16 v[244:247],a[228:231],v[44:47],v[244:247]
ds_read_u16 v61,v61
v_lshlrev_b32_sdwa v63,1,v63 dst_sel:DWORD dst_unused:UNUSED_PAD src0_sel:DWORD src1_sel:WORD_1
v_mfma_f32_16x16x32_bf16 v[180:183],a[248:251],v[44:47],v[180:183]
ds_read_u16_d16_hi v63,v63
v_lshlrev_b32_sdwa v32,1,v32 dst_sel:DWORD dst_unused:UNUSED_PAD src0_sel:DWORD src1_sel:WORD_1
v_mfma_f32_16x16x32_bf16 v[176:179],a[248:251],v[48:51],v[176:179]
s_waitcnt lgkmcnt(6)
v_rcp_f16_sdwa v9,v8 dst_sel:WORD_1 dst_unused:UNUSED_PRESERVE src0_sel:WORD_1
v_mfma_f32_16x16x32_bf16 v[248:251],a[236:239],v[48:51],v[248:251]
ds_read_u16 v32,v32
v_lshlrev_b32_sdwa v34,1,v34 dst_sel:DWORD dst_unused:UNUSED_PAD src0_sel:DWORD src1_sel:WORD_1
v_mfma_f32_16x16x32_bf16 v[252:255],a[236:239],v[44:47],v[252:255]
ds_read_u16_d16_hi v34,v34
v_fma_mix_f32 v33,v33,v9,0 op_sel_hi:[0,1,0]
v_mfma_f32_16x16x32_bf16 v[196:199],a[252:255],v[44:47],v[196:199]
ds_read_b64_tr_b16 a[194:195],v1 offset:9232
v_fma_mix_f32 v35,v35,v9,0 op_sel:[0,1,0] op_sel_hi:[0,1,0]
v_mfma_f32_16x16x32_bf16 v[192:195],a[252:255],v[48:51],v[192:195]
v_mfma_f32_16x16x32_bf16 v[44:47],a[216:219],a[8:11],v[16:19]
v_lshlrev_b32_sdwa v36,1,v36 dst_sel:DWORD dst_unused:UNUSED_PAD src0_sel:DWORD src1_sel:WORD_1
v_fma_mixlo_f16 v8,v53,s99,0
v_mfma_f32_16x16x32_bf16 v[44:47],a[220:223],a[24:27],v[44:47]
ds_read_u16 v36,v36
v_lshlrev_b32_sdwa v38,1,v38 dst_sel:DWORD dst_unused:UNUSED_PAD src0_sel:DWORD src1_sel:WORD_1
v_mfma_f32_16x16x32_bf16 v[44:47],a[212:215],a[40:43],v[44:47]
ds_read_u16_d16_hi v38,v38
v_lshl_add_u64 v[28:29],v[28:29],0,v[30:31]
v_mfma_f32_16x16x32_bf16 v[48:51],a[208:211],a[40:43],v[20:23]
s_waitcnt lgkmcnt(5)
v_lshl_add_u64 v[30:31],v[24:25],0,v[26:27]
v_fma_mixhi_f16 v8,v55,s99,0
v_mfma_f32_16x16x32_bf16 v[48:51],a[204:207],a[24:27],v[48:51]
v_lshl_add_u64 v[24:25],v[60:61],0,v[62:63]
v_lshlrev_b32_sdwa v37,1,v37 dst_sel:DWORD dst_unused:UNUSED_PAD src0_sel:DWORD src1_sel:WORD_1
v_pk_fma_f16 v9,v8,v8,s54
v_mfma_f32_16x16x32_bf16 v[48:51],a[200:203],a[8:11],v[48:51]
ds_read_u16 v37,v37
v_lshlrev_b32_sdwa v39,1,v39 dst_sel:DWORD dst_unused:UNUSED_PAD src0_sel:DWORD src1_sel:WORD_1
v_mfma_f32_16x16x32_bf16 v[60:63],a[200:203],a[12:15],v[20:23]
s_waitcnt lgkmcnt(4)
ds_read_b128 v[20:23],v4 offset:288
v_or_b32 v26,v32,v34
v_pk_mul_f16 v9,v8,v9
v_mfma_f32_16x16x32_bf16 v[60:63],a[204:207],a[28:31],v[60:63]
ds_read_u16_d16_hi v39,v39
v_cvt_pk_bf16_f32 v27,v33,v35
v_lshlrev_b32_sdwa v40,1,v40 dst_sel:DWORD dst_unused:UNUSED_PAD src0_sel:DWORD src1_sel:WORD_1
v_mfma_f32_16x16x32_bf16 v[60:63],a[208:211],a[44:47],v[60:63]
ds_read_b64_tr_b16 a[200:201],v1 offset:10256
v_exp_f16 v8,v9
v_mfma_f32_16x16x32_bf16 v[32:35],a[212:215],a[44:47],v[16:19]
ds_read_b128 v[16:19],v4 offset:256
ds_read_b64_tr_b16 a[202:203],v1 offset:11280
v_lshlrev_b32_sdwa v42,1,v42 dst_sel:DWORD dst_unused:UNUSED_PAD src0_sel:DWORD src1_sel:WORD_1
v_mfma_f32_16x16x32_bf16 v[32:35],a[220:223],a[28:31],v[32:35]
ds_read_b64_tr_b16 a[204:205],v2 offset:10256
ds_read_b64_tr_b16 a[206:207],v2 offset:11280
v_lshlrev_b32_sdwa v41,1,v41 dst_sel:DWORD dst_unused:UNUSED_PAD src0_sel:DWORD src1_sel:WORD_1
v_mfma_f32_16x16x32_bf16 v[32:35],a[216:219],a[12:15],v[32:35]
ds_read_u16 v40,v40
v_exp_f16_sdwa v8,v9 dst_sel:WORD_1 dst_unused:UNUSED_PRESERVE src0_sel:WORD_1
v_mfma_f32_16x16x32_bf16 v[208:211],a[232:235],v[28:31],v[208:211]
ds_read_u16_d16_hi v42,v42
v_lshlrev_b32_sdwa v43,1,v43 dst_sel:DWORD dst_unused:UNUSED_PAD src0_sel:DWORD src1_sel:WORD_1
v_mfma_f32_16x16x32_bf16 v[212:215],a[232:235],v[24:27],v[212:215]
ds_read_u16 v41,v41
v_lshlrev_b32_sdwa v56,1,v56 dst_sel:DWORD dst_unused:UNUSED_PAD src0_sel:DWORD src1_sel:WORD_1
v_mfma_f32_16x16x32_bf16 v[172:175],a[244:247],v[24:27],v[172:175]
ds_read_u16_d16_hi v43,v43
v_pk_fma_f16 v8,v8,s55,s55
v_mfma_f32_16x16x32_bf16 v[168:171],a[244:247],v[28:31],v[168:171]
v_rcp_f16 v9,v8
v_lshlrev_b32_sdwa v58,1,v58 dst_sel:DWORD dst_unused:UNUSED_PAD src0_sel:DWORD src1_sel:WORD_1
v_mfma_f32_16x16x32_bf16 v[216:219],a[228:231],v[28:31],v[216:219]
ds_read_b64_tr_b16 a[208:209],v2 offset:8208
v_lshlrev_b32_sdwa v57,1,v57 dst_sel:DWORD dst_unused:UNUSED_PAD src0_sel:DWORD src1_sel:WORD_1
v_mfma_f32_16x16x32_bf16 v[220:223],a[228:231],v[24:27],v[220:223]
ds_read_b64_tr_b16 a[210:211],v2 offset:9232
v_lshlrev_b32_sdwa v59,1,v59 dst_sel:DWORD dst_unused:UNUSED_PAD src0_sel:DWORD src1_sel:WORD_1
v_mfma_f32_16x16x32_bf16 v[188:191],a[248:251],v[24:27],v[188:191]
ds_read_u16 v56,v56
v_lshlrev_b32_sdwa v52,1,v52 dst_sel:DWORD dst_unused:UNUSED_PAD src0_sel:DWORD src1_sel:WORD_1
v_mfma_f32_16x16x32_bf16 v[184:187],a[248:251],v[28:31],v[184:187]
ds_read_u16_d16_hi v58,v58
v_rcp_f16_sdwa v9,v8 dst_sel:WORD_1 dst_unused:UNUSED_PRESERVE src0_sel:WORD_1
v_mfma_f32_16x16x32_bf16 v[224:227],a[236:239],v[28:31],v[224:227]
ds_read_u16 v57,v57
ds_read_u16_d16_hi v59,v59
v_mfma_f32_16x16x32_bf16 v[228:231],a[236:239],v[24:27],v[228:231]
ds_read_b64_tr_b16 a[212:213],v2 offset:6160
ds_read_b64_tr_b16 a[214:215],v2 offset:7184
v_lshlrev_b32_sdwa v54,1,v54 dst_sel:DWORD dst_unused:UNUSED_PAD src0_sel:DWORD src1_sel:WORD_1
v_mfma_f32_16x16x32_bf16 v[204:207],a[252:255],v[24:27],v[204:207]
ds_read_u16 v52,v52
v_fma_mix_f32 v53,v53,v9,0 op_sel_hi:[0,1,0]
v_mfma_f32_16x16x32_bf16 v[200:203],a[252:255],v[28:31],v[200:203]
s_waitcnt lgkmcnt(9)
ds_read_u16_d16_hi v54,v54
v_fma_mix_f32 v55,v55,v9,0 op_sel:[0,1,0] op_sel_hi:[0,1,0]
v_mfma_f32_16x16x32_bf16 v[24:27],a[196:199],a[48:51],v[16:19]
s_add_u32 m0,s57,35360
v_lshlrev_b32_sdwa v44,1,v44 dst_sel:DWORD dst_unused:UNUSED_PAD src0_sel:DWORD src1_sel:WORD_1
v_fma_mixlo_f16 v8,v61,s99,0
v_mfma_f32_16x16x32_bf16 v[24:27],a[192:195],a[64:67],v[24:27]
s_add_u32 s89,s56,5898240
ds_read_u16 v44,v44
v_lshlrev_b32_sdwa v46,1,v46 dst_sel:DWORD dst_unused:UNUSED_PAD src0_sel:DWORD src1_sel:WORD_1
v_mfma_f32_16x16x32_bf16 v[24:27],a[200:203],a[80:83],v[24:27]
buffer_load_dwordx4 v6,s[36:39],s89 offen lds
v_lshl_add_u64 v[36:37],v[36:37],0,v[38:39]
v_mfma_f32_16x16x32_bf16 v[28:31],a[204:207],a[80:83],v[20:23]
s_waitcnt lgkmcnt(8)
ds_read_u16_d16_hi v46,v46
v_lshl_add_u64 v[38:39],v[40:41],0,v[42:43]
v_fma_mixhi_f16 v8,v63,s99,0
v_mfma_f32_16x16x32_bf16 v[28:31],a[208:211],a[64:67],v[28:31]
s_waitcnt lgkmcnt(4)
v_lshl_add_u64 v[40:41],v[56:57],0,v[58:59]
v_lshlrev_b32_sdwa v45,1,v45 dst_sel:DWORD dst_unused:UNUSED_PAD src0_sel:DWORD src1_sel:WORD_1
v_pk_fma_f16 v9,v8,v8,s54
v_mfma_f32_16x16x32_bf16 v[28:31],a[212:215],a[48:51],v[28:31]
ds_read_u16 v45,v45
v_lshlrev_b32_sdwa v47,1,v47 dst_sel:DWORD dst_unused:UNUSED_PAD src0_sel:DWORD src1_sel:WORD_1
v_mfma_f32_16x16x32_bf16 v[56:59],a[212:215],a[52:55],v[20:23]
s_waitcnt lgkmcnt(3)
ds_read_u16_d16_hi v47,v47
v_or_b32 v42,v52,v54
v_pk_mul_f16 v9,v8,v9
v_mfma_f32_16x16x32_bf16 v[56:59],a[208:211],a[68:71],v[56:59]
s_waitcnt vmcnt(22)
v_cvt_pk_bf16_f32 v43,v53,v55
v_lshlrev_b32_sdwa v48,1,v48 dst_sel:DWORD dst_unused:UNUSED_PAD src0_sel:DWORD src1_sel:WORD_1
v_mfma_f32_16x16x32_bf16 v[56:59],a[204:207],a[84:87],v[56:59]
s_barrier
v_exp_f16 v8,v9
v_mfma_f32_16x16x32_bf16 v[52:55],a[200:203],a[84:87],v[16:19]
ds_read_u16 v48,v48
v_lshlrev_b32_sdwa v50,1,v50 dst_sel:DWORD dst_unused:UNUSED_PAD src0_sel:DWORD src1_sel:WORD_1
v_mfma_f32_16x16x32_bf16 v[52:55],a[192:195],a[68:71],v[52:55]
s_add_u32 m0,s58,6160
ds_read_u16_d16_hi v50,v50
v_lshlrev_b32_sdwa v49,1,v49 dst_sel:DWORD dst_unused:UNUSED_PAD src0_sel:DWORD src1_sel:WORD_1
v_mfma_f32_16x16x32_bf16 v[52:55],a[196:199],a[52:55],v[52:55]
s_add_u32 s69,s69,s59
v_exp_f16_sdwa v8,v9 dst_sel:WORD_1 dst_unused:UNUSED_PRESERVE src0_sel:WORD_1
v_mfma_f32_16x16x32_bf16 v[136:139],a[232:235],v[36:39],v[136:139]
buffer_load_dwordx4 v5,s[44:47],s69 offen offset:0 lds
v_lshlrev_b32_sdwa v51,1,v51 dst_sel:DWORD dst_unused:UNUSED_PAD src0_sel:DWORD src1_sel:WORD_1
v_mfma_f32_16x16x32_bf16 v[140:143],a[232:235],v[40:43],v[140:143]
ds_read_u16 v49,v49
v_lshlrev_b32_sdwa v32,1,v32 dst_sel:DWORD dst_unused:UNUSED_PAD src0_sel:DWORD src1_sel:WORD_1
v_mfma_f32_16x16x32_bf16 v[68:71],a[244:247],v[40:43],v[68:71]
ds_read_u16_d16_hi v51,v51
v_pk_fma_f16 v8,v8,s55,s55
v_mfma_f32_16x16x32_bf16 v[64:67],a[244:247],v[36:39],v[64:67]
v_rcp_f16 v9,v8
v_lshlrev_b32_sdwa v34,1,v34 dst_sel:DWORD dst_unused:UNUSED_PAD src0_sel:DWORD src1_sel:WORD_1
v_mfma_f32_16x16x32_bf16 v[144:147],a[228:231],v[36:39],v[144:147]
s_add_u32 s69,s69,s53
ds_read_u16 v32,v32
v_lshlrev_b32_sdwa v33,1,v33 dst_sel:DWORD dst_unused:UNUSED_PAD src0_sel:DWORD src1_sel:WORD_1
v_mfma_f32_16x16x32_bf16 v[148:151],a[228:231],v[40:43],v[148:151]
buffer_load_dwordx4 v5,s[44:47],s69 offen offset:1024 lds
ds_read_u16_d16_hi v34,v34
v_mfma_f32_16x16x32_bf16 v[84:87],a[248:251],v[40:43],v[84:87]
ds_read_u16 v33,v33
v_lshlrev_b32_sdwa v35,1,v35 dst_sel:DWORD dst_unused:UNUSED_PAD src0_sel:DWORD src1_sel:WORD_1
v_mfma_f32_16x16x32_bf16 v[80:83],a[248:251],v[36:39],v[80:83]
v_rcp_f16_sdwa v9,v8 dst_sel:WORD_1 dst_unused:UNUSED_PRESERVE src0_sel:WORD_1
v_mfma_f32_16x16x32_bf16 v[152:155],a[236:239],v[36:39],v[152:155]
s_add_u32 m0,s57,38896
ds_read_u16_d16_hi v35,v35
v_lshlrev_b32_sdwa v60,1,v60 dst_sel:DWORD dst_unused:UNUSED_PAD src0_sel:DWORD src1_sel:WORD_1
v_mfma_f32_16x16x32_bf16 v[156:159],a[236:239],v[40:43],v[156:159]
s_add_u32 s89,s56,5996544
ds_read_u16 v60,v60
v_lshlrev_b32_sdwa v62,1,v62 dst_sel:DWORD dst_unused:UNUSED_PAD src0_sel:DWORD src1_sel:WORD_1
v_mfma_f32_16x16x32_bf16 v[100:103],a[252:255],v[40:43],v[100:103]
buffer_load_dwordx4 v6,s[36:39],s89 offen lds
v_fma_mix_f32 v61,v61,v9,0 op_sel_hi:[0,1,0]
v_mfma_f32_16x16x32_bf16 v[96:99],a[252:255],v[36:39],v[96:99]
s_add_u32 m0,s58,6160
ds_read_u16_d16_hi v62,v62
v_fma_mix_f32 v63,v63,v9,0 op_sel:[0,1,0] op_sel_hi:[0,1,0]
v_mfma_f32_16x16x32_bf16 v[40:43],a[196:199],a[56:59],v[16:19]
s_add_u32 s69,s69,s53
ds_read_b64_tr_b16 a[240:241],v3 offset:12320
ds_read_b64_tr_b16 a[242:243],v3 offset:13856
v_lshlrev_b32_sdwa v24,1,v24 dst_sel:DWORD dst_unused:UNUSED_PAD src0_sel:DWORD src1_sel:WORD_1
v_mfma_f32_16x16x32_bf16 v[40:43],a[192:195],a[72:75],v[40:43]
buffer_load_dwordx4 v5,s[44:47],s69 offen offset:2048 lds
ds_read_u16 v24,v24
v_fma_mixlo_f16 v8,v57,s99,0
v_mfma_f32_16x16x32_bf16 v[40:43],a[200:203],a[88:91],v[40:43]
s_waitcnt lgkmcnt(7)
ds_read_b64_tr_b16 a[224:225],v3 offset:12328
v_lshl_add_u64 v[44:45],v[44:45],0,v[46:47]
v_lshlrev_b32_sdwa v26,1,v26 dst_sel:DWORD dst_unused:UNUSED_PAD src0_sel:DWORD src1_sel:WORD_1
v_mfma_f32_16x16x32_bf16 v[36:39],a[204:207],a[88:91],v[20:23]
ds_read_u16_d16_hi v26,v26
v_lshl_add_u64 v[46:47],v[48:49],0,v[50:51]
v_fma_mixhi_f16 v8,v59,s99,0
v_mfma_f32_16x16x32_bf16 v[36:39],a[208:211],a[72:75],v[36:39]
s_waitcnt lgkmcnt(7)
ds_read_b64_tr_b16 a[226:227],v3 offset:13864
v_lshl_add_u64 v[48:49],v[32:33],0,v[34:35]
v_lshlrev_b32_sdwa v25,1,v25 dst_sel:DWORD dst_unused:UNUSED_PAD src0_sel:DWORD src1_sel:WORD_1
v_pk_fma_f16 v9,v8,v8,s54
v_mfma_f32_16x16x32_bf16 v[36:39],a[212:215],a[56:59],v[36:39]
ds_read_u16 v25,v25
v_lshlrev_b32_sdwa v27,1,v27 dst_sel:DWORD dst_unused:UNUSED_PAD src0_sel:DWORD src1_sel:WORD_1
v_mfma_f32_16x16x32_bf16 v[32:35],a[212:215],a[60:63],v[20:23]
s_waitcnt lgkmcnt(7)
ds_read_u16_d16_hi v27,v27
v_or_b32 v50,v60,v62
v_pk_mul_f16 v9,v8,v9
v_mfma_f32_16x16x32_bf16 v[32:35],a[208:211],a[76:79],v[32:35]
ds_read_b64_tr_b16 a[220:221],v1 offset:12320
v_cvt_pk_bf16_f32 v51,v61,v63
v_lshlrev_b32_sdwa v28,1,v28 dst_sel:DWORD dst_unused:UNUSED_PAD src0_sel:DWORD src1_sel:WORD_1
v_mfma_f32_16x16x32_bf16 v[32:35],a[204:207],a[92:95],v[32:35]
v_exp_f16 v8,v9
v_mfma_f32_16x16x32_bf16 v[60:63],a[200:203],a[92:95],v[16:19]
ds_read_u16 v28,v28
v_lshlrev_b32_sdwa v30,1,v30 dst_sel:DWORD dst_unused:UNUSED_PAD src0_sel:DWORD src1_sel:WORD_1
v_mfma_f32_16x16x32_bf16 v[60:63],a[192:195],a[76:79],v[60:63]
ds_read_u16_d16_hi v30,v30
v_lshlrev_b32_sdwa v29,1,v29 dst_sel:DWORD dst_unused:UNUSED_PAD src0_sel:DWORD src1_sel:WORD_1
v_mfma_f32_16x16x32_bf16 v[60:63],a[196:199],a[60:63],v[60:63]
v_exp_f16_sdwa v8,v9 dst_sel:WORD_1 dst_unused:UNUSED_PRESERVE src0_sel:WORD_1
v_mfma_f32_16x16x32_bf16 v[112:115],a[232:235],v[44:47],v[112:115]
ds_read_u16 v29,v29
v_lshlrev_b32_sdwa v31,1,v31 dst_sel:DWORD dst_unused:UNUSED_PAD src0_sel:DWORD src1_sel:WORD_1
v_mfma_f32_16x16x32_bf16 v[116:119],a[232:235],v[48:51],v[116:119]
ds_read_u16_d16_hi v31,v31
v_lshlrev_b32_sdwa v52,1,v52 dst_sel:DWORD dst_unused:UNUSED_PAD src0_sel:DWORD src1_sel:WORD_1
v_mfma_f32_16x16x32_bf16 v[76:79],a[244:247],v[48:51],v[76:79]
ds_read_u16 v52,v52
v_pk_fma_f16 v8,v8,s55,s55
v_mfma_f32_16x16x32_bf16 v[72:75],a[244:247],v[44:47],v[72:75]
ds_read_b64_tr_b16 a[232:233],v3 offset:12384
v_rcp_f16 v9,v8
v_lshlrev_b32_sdwa v54,1,v54 dst_sel:DWORD dst_unused:UNUSED_PAD src0_sel:DWORD src1_sel:WORD_1
v_mfma_f32_16x16x32_bf16 v[120:123],a[228:231],v[44:47],v[120:123]
ds_read_u16_d16_hi v54,v54
ds_read_b64_tr_b16 a[234:235],v3 offset:13920
v_mfma_f32_16x16x32_bf16 v[124:127],a[228:231],v[48:51],v[124:127]
ds_read_b64_tr_b16 a[244:245],v3 offset:12392
ds_read_b64_tr_b16 a[246:247],v3 offset:13928
v_lshlrev_b32_sdwa v53,1,v53 dst_sel:DWORD dst_unused:UNUSED_PAD src0_sel:DWORD src1_sel:WORD_1
v_mfma_f32_16x16x32_bf16 v[92:95],a[248:251],v[48:51],v[92:95]
ds_read_u16 v53,v53
v_lshlrev_b32_sdwa v55,1,v55 dst_sel:DWORD dst_unused:UNUSED_PAD src0_sel:DWORD src1_sel:WORD_1
v_mfma_f32_16x16x32_bf16 v[88:91],a[248:251],v[44:47],v[88:91]
ds_read_u16_d16_hi v55,v55
v_rcp_f16_sdwa v9,v8 dst_sel:WORD_1 dst_unused:UNUSED_PRESERVE src0_sel:WORD_1
v_mfma_f32_16x16x32_bf16 v[128:131],a[236:239],v[44:47],v[128:131]
ds_read_b64_tr_b16 a[228:229],v3 offset:12448
ds_read_b64_tr_b16 a[230:231],v3 offset:13984
v_lshlrev_b32_sdwa v56,1,v56 dst_sel:DWORD dst_unused:UNUSED_PAD src0_sel:DWORD src1_sel:WORD_1
v_mfma_f32_16x16x32_bf16 v[132:135],a[236:239],v[48:51],v[132:135]
ds_read_u16 v56,v56
v_lshlrev_b32_sdwa v58,1,v58 dst_sel:DWORD dst_unused:UNUSED_PAD src0_sel:DWORD src1_sel:WORD_1
v_mfma_f32_16x16x32_bf16 v[108:111],a[252:255],v[48:51],v[108:111]
ds_read_u16_d16_hi v58,v58
v_fma_mix_f32 v57,v57,v9,0 op_sel_hi:[0,1,0]
v_mfma_f32_16x16x32_bf16 v[104:107],a[252:255],v[44:47],v[104:107]
ds_read_b64_tr_b16 a[248:249],v3 offset:12456
v_fma_mix_f32 v59,v59,v9,0 op_sel:[0,1,0] op_sel_hi:[0,1,0]
v_mfma_f32_16x16x32_bf16 v[48:51],a[196:199],a[0:3],v[16:19]
ds_read_b64_tr_b16 a[250:251],v3 offset:13992
v_lshlrev_b32_sdwa v40,1,v40 dst_sel:DWORD dst_unused:UNUSED_PAD src0_sel:DWORD src1_sel:WORD_1
v_mfma_f32_16x16x32_bf16 v[48:51],a[192:195],a[16:19],v[48:51]
TP6558_%=:
s_cbranch_execz TR0_%=
ds_read_u16 v40,v40
v_fma_mixlo_f16 v8,v33,s99,0
v_mfma_f32_16x16x32_bf16 v[48:51],a[200:203],a[32:35],v[48:51]
s_waitcnt lgkmcnt(9)
ds_read_b64_tr_b16 a[222:223],v1 offset:13344
v_lshl_add_u64 v[24:25],v[24:25],0,v[26:27]
v_lshlrev_b32_sdwa v42,1,v42 dst_sel:DWORD dst_unused:UNUSED_PAD src0_sel:DWORD src1_sel:WORD_1
v_mfma_f32_16x16x32_bf16 v[44:47],a[204:207],a[32:35],v[20:23]
ds_read_u16_d16_hi v42,v42
v_lshl_add_u64 v[26:27],v[28:29],0,v[30:31]
v_fma_mixhi_f16 v8,v35,s99,0
v_mfma_f32_16x16x32_bf16 v[44:47],a[208:211],a[16:19],v[44:47]
s_waitcnt lgkmcnt(9)
ds_read_b64_tr_b16 a[216:217],v1 offset:14368
v_lshl_add_u64 v[28:29],v[52:53],0,v[54:55]
v_lshlrev_b32_sdwa v41,1,v41 dst_sel:DWORD dst_unused:UNUSED_PAD src0_sel:DWORD src1_sel:WORD_1
v_pk_fma_f16 v9,v8,v8,s54
v_mfma_f32_16x16x32_bf16 v[44:47],a[212:215],a[0:3],v[44:47]
ds_read_u16 v41,v41
v_lshlrev_b32_sdwa v43,1,v43 dst_sel:DWORD dst_unused:UNUSED_PAD src0_sel:DWORD src1_sel:WORD_1
v_mfma_f32_16x16x32_bf16 v[52:55],a[212:215],a[4:7],v[20:23]
s_waitcnt lgkmcnt(7)
ds_read_u16_d16_hi v43,v43
v_or_b32 v30,v56,v58
v_pk_mul_f16 v9,v8,v9
v_mfma_f32_16x16x32_bf16 v[52:55],a[208:211],a[20:23],v[52:55]
ds_read_b64_tr_b16 a[218:219],v1 offset:15392
v_cvt_pk_bf16_f32 v31,v57,v59
v_lshlrev_b32_sdwa v36,1,v36 dst_sel:DWORD dst_unused:UNUSED_PAD src0_sel:DWORD src1_sel:WORD_1
v_mfma_f32_16x16x32_bf16 v[52:55],a[204:207],a[36:39],v[52:55]
v_exp_f16 v8,v9
v_mfma_f32_16x16x32_bf16 v[56:59],a[200:203],a[36:39],v[16:19]
ds_read_u16 v36,v36
v_lshlrev_b32_sdwa v38,1,v38 dst_sel:DWORD dst_unused:UNUSED_PAD src0_sel:DWORD src1_sel:WORD_1
v_mfma_f32_16x16x32_bf16 v[56:59],a[192:195],a[20:23],v[56:59]
s_add_u32 m0,s57,42440
ds_read_u16_d16_hi v38,v38
v_lshlrev_b32_sdwa v37,1,v37 dst_sel:DWORD dst_unused:UNUSED_PAD src0_sel:DWORD src1_sel:WORD_1
v_mfma_f32_16x16x32_bf16 v[56:59],a[196:199],a[4:7],v[56:59]
s_add_u32 s89,s56,6094848
v_exp_f16_sdwa v8,v9 dst_sel:WORD_1 dst_unused:UNUSED_PRESERVE src0_sel:WORD_1
v_mfma_f32_16x16x32_bf16 v[232:235],a[240:243],v[24:27],v[232:235]
buffer_load_dwordx4 v6,s[36:39],s89 offen lds
v_lshlrev_b32_sdwa v39,1,v39 dst_sel:DWORD dst_unused:UNUSED_PAD src0_sel:DWORD src1_sel:WORD_1
v_mfma_f32_16x16x32_bf16 v[236:239],a[240:243],v[28:31],v[236:239]
ds_read_u16 v37,v37
v_lshlrev_b32_sdwa v60,1,v60 dst_sel:DWORD dst_unused:UNUSED_PAD src0_sel:DWORD src1_sel:WORD_1
v_mfma_f32_16x16x32_bf16 v[164:167],a[224:227],v[28:31],v[164:167]
ds_read_u16_d16_hi v39,v39
v_pk_fma_f16 v8,v8,s55,s55
v_mfma_f32_16x16x32_bf16 v[160:163],a[224:227],v[24:27],v[160:163]
v_rcp_f16 v9,v8
v_lshlrev_b32_sdwa v62,1,v62 dst_sel:DWORD dst_unused:UNUSED_PAD src0_sel:DWORD src1_sel:WORD_1
v_mfma_f32_16x16x32_bf16 v[240:243],a[232:235],v[24:27],v[240:243]
ds_read_u16 v60,v60
v_lshlrev_b32_sdwa v61,1,v61 dst_sel:DWORD dst_unused:UNUSED_PAD src0_sel:DWORD src1_sel:WORD_1
v_mfma_f32_16x16x32_bf16 v[244:247],a[232:235],v[28:31],v[244:247]
ds_read_u16_d16_hi v62,v62
v_lshlrev_b32_sdwa v63,1,v63 dst_sel:DWORD dst_unused:UNUSED_PAD src0_sel:DWORD src1_sel:WORD_1
v_mfma_f32_16x16x32_bf16 v[180:183],a[244:247],v[28:31],v[180:183]
ds_read_u16 v61,v61
v_lshlrev_b32_sdwa v32,1,v32 dst_sel:DWORD dst_unused:UNUSED_PAD src0_sel:DWORD src1_sel:WORD_1
v_mfma_f32_16x16x32_bf16 v[176:179],a[244:247],v[24:27],v[176:179]
v_rcp_f16_sdwa v9,v8 dst_sel:WORD_1 dst_unused:UNUSED_PRESERVE src0_sel:WORD_1
v_mfma_f32_16x16x32_bf16 v[248:251],a[228:231],v[24:27],v[248:251]
ds_read_u16_d16_hi v63,v63
v_lshlrev_b32_sdwa v34,1,v34 dst_sel:DWORD dst_unused:UNUSED_PAD src0_sel:DWORD src1_sel:WORD_1
v_mfma_f32_16x16x32_bf16 v[252:255],a[228:231],v[28:31],v[252:255]
s_waitcnt lgkmcnt(6)
ds_read_u16 v32,v32
v_fma_mix_f32 v33,v33,v9,0 op_sel_hi:[0,1,0]
v_mfma_f32_16x16x32_bf16 v[196:199],a[248:251],v[28:31],v[196:199]
ds_read_u16_d16_hi v34,v34
v_fma_mix_f32 v35,v35,v9,0 op_sel:[0,1,0] op_sel_hi:[0,1,0]
v_mfma_f32_16x16x32_bf16 v[192:195],a[248:251],v[24:27],v[192:195]
v_mfma_f32_16x16x32_bf16 v[28:31],a[196:199],a[8:11],v[16:19]
v_lshlrev_b32_sdwa v48,1,v48 dst_sel:DWORD dst_unused:UNUSED_PAD src0_sel:DWORD src1_sel:WORD_1
v_fma_mixlo_f16 v8,v53,s99,0
v_mfma_f32_16x16x32_bf16 v[28:31],a[192:195],a[24:27],v[28:31]
ds_read_u16 v48,v48
v_lshlrev_b32_sdwa v50,1,v50 dst_sel:DWORD dst_unused:UNUSED_PAD src0_sel:DWORD src1_sel:WORD_1
v_mfma_f32_16x16x32_bf16 v[28:31],a[200:203],a[40:43],v[28:31]
ds_read_u16_d16_hi v50,v50
v_lshl_add_u64 v[40:41],v[40:41],0,v[42:43]
v_mfma_f32_16x16x32_bf16 v[24:27],a[204:207],a[40:43],v[20:23]
s_waitcnt lgkmcnt(5)
v_lshl_add_u64 v[42:43],v[36:37],0,v[38:39]
v_fma_mixhi_f16 v8,v55,s99,0
v_mfma_f32_16x16x32_bf16 v[24:27],a[208:211],a[24:27],v[24:27]
s_waitcnt lgkmcnt(4)
v_lshl_add_u64 v[36:37],v[60:61],0,v[62:63]
v_lshlrev_b32_sdwa v49,1,v49 dst_sel:DWORD dst_unused:UNUSED_PAD src0_sel:DWORD src1_sel:WORD_1
v_pk_fma_f16 v9,v8,v8,s54
v_mfma_f32_16x16x32_bf16 v[24:27],a[212:215],a[8:11],v[24:27]
ds_read_u16 v49,v49
v_lshlrev_b32_sdwa v51,1,v51 dst_sel:DWORD dst_unused:UNUSED_PAD src0_sel:DWORD src1_sel:WORD_1
v_mfma_f32_16x16x32_bf16 v[60:63],a[212:215],a[12:15],v[20:23]
s_waitcnt lgkmcnt(3)
ds_read_b128 v[20:23],v4 offset:416
v_or_b32 v38,v32,v34
v_pk_mul_f16 v9,v8,v9
v_mfma_f32_16x16x32_bf16 v[60:63],a[208:211],a[28:31],v[60:63]
ds_read_u16_d16_hi v51,v51
v_cvt_pk_bf16_f32 v39,v33,v35
v_lshlrev_b32_sdwa v44,1,v44 dst_sel:DWORD dst_unused:UNUSED_PAD src0_sel:DWORD src1_sel:WORD_1
v_mfma_f32_16x16x32_bf16 v[60:63],a[204:207],a[44:47],v[60:63]
ds_read_b64_tr_b16 a[212:213],v1 offset:16416
v_exp_f16 v8,v9
v_mfma_f32_16x16x32_bf16 v[32:35],a[200:203],a[44:47],v[16:19]
ds_read_b128 v[16:19],v4 offset:384
ds_read_b64_tr_b16 a[214:215],v1 offset:17440
v_lshlrev_b32_sdwa v46,1,v46 dst_sel:DWORD dst_unused:UNUSED_PAD src0_sel:DWORD src1_sel:WORD_1
v_mfma_f32_16x16x32_bf16 v[32:35],a[192:195],a[28:31],v[32:35]
ds_read_b64_tr_b16 a[208:209],v2 offset:16416
ds_read_b64_tr_b16 a[210:211],v2 offset:17440
v_lshlrev_b32_sdwa v45,1,v45 dst_sel:DWORD dst_unused:UNUSED_PAD src0_sel:DWORD src1_sel:WORD_1
v_mfma_f32_16x16x32_bf16 v[32:35],a[196:199],a[12:15],v[32:35]
ds_read_u16 v44,v44
v_exp_f16_sdwa v8,v9 dst_sel:WORD_1 dst_unused:UNUSED_PRESERVE src0_sel:WORD_1
v_mfma_f32_16x16x32_bf16 v[208:211],a[240:243],v[40:43],v[208:211]
ds_read_u16_d16_hi v46,v46
v_lshlrev_b32_sdwa v47,1,v47 dst_sel:DWORD dst_unused:UNUSED_PAD src0_sel:DWORD src1_sel:WORD_1
v_mfma_f32_16x16x32_bf16 v[212:215],a[240:243],v[36:39],v[212:215]
ds_read_u16 v45,v45
v_lshlrev_b32_sdwa v56,1,v56 dst_sel:DWORD dst_unused:UNUSED_PAD src0_sel:DWORD src1_sel:WORD_1
v_mfma_f32_16x16x32_bf16 v[172:175],a[224:227],v[36:39],v[172:175]
ds_read_u16_d16_hi v47,v47
v_pk_fma_f16 v8,v8,s55,s55
v_mfma_f32_16x16x32_bf16 v[168:171],a[224:227],v[40:43],v[168:171]
v_rcp_f16 v9,v8
v_lshlrev_b32_sdwa v58,1,v58 dst_sel:DWORD dst_unused:UNUSED_PAD src0_sel:DWORD src1_sel:WORD_1
v_mfma_f32_16x16x32_bf16 v[216:219],a[232:235],v[40:43],v[216:219]
ds_read_b64_tr_b16 a[204:205],v2 offset:14368
v_lshlrev_b32_sdwa v57,1,v57 dst_sel:DWORD dst_unused:UNUSED_PAD src0_sel:DWORD src1_sel:WORD_1
v_mfma_f32_16x16x32_bf16 v[220:223],a[232:235],v[36:39],v[220:223]
ds_read_b64_tr_b16 a[206:207],v2 offset:15392
v_lshlrev_b32_sdwa v59,1,v59 dst_sel:DWORD dst_unused:UNUSED_PAD src0_sel:DWORD src1_sel:WORD_1
v_mfma_f32_16x16x32_bf16 v[188:191],a[244:247],v[36:39],v[188:191]
ds_read_u16 v56,v56
v_lshlrev_b32_sdwa v52,1,v52 dst_sel:DWORD dst_unused:UNUSED_PAD src0_sel:DWORD src1_sel:WORD_1
v_mfma_f32_16x16x32_bf16 v[184:187],a[244:247],v[40:43],v[184:187]
ds_read_u16_d16_hi v58,v58
v_rcp_f16_sdwa v9,v8 dst_sel:WORD_1 dst_unused:UNUSED_PRESERVE src0_sel:WORD_1
v_mfma_f32_16x16x32_bf16 v[224:227],a[228:231],v[40:43],v[224:227]
ds_read_u16 v57,v57
ds_read_u16_d16_hi v59,v59
v_mfma_f32_16x16x32_bf16 v[228:231],a[228:231],v[36:39],v[228:231]
ds_read_b64_tr_b16 a[200:201],v2 offset:12320
ds_read_b64_tr_b16 a[202:203],v2 offset:13344
v_lshlrev_b32_sdwa v54,1,v54 dst_sel:DWORD dst_unused:UNUSED_PAD src0_sel:DWORD src1_sel:WORD_1
v_mfma_f32_16x16x32_bf16 v[204:207],a[248:251],v[36:39],v[204:207]
ds_read_u16 v52,v52
v_fma_mix_f32 v53,v53,v9,0 op_sel_hi:[0,1,0]
v_mfma_f32_16x16x32_bf16 v[200:203],a[248:251],v[40:43],v[200:203]
s_waitcnt lgkmcnt(9)
ds_read_u16_d16_hi v54,v54
v_fma_mix_f32 v55,v55,v9,0 op_sel:[0,1,0] op_sel_hi:[0,1,0]
v_mfma_f32_16x16x32_bf16 v[36:39],a[220:223],a[48:51],v[16:19]
v_lshlrev_b32_sdwa v28,1,v28 dst_sel:DWORD dst_unused:UNUSED_PAD src0_sel:DWORD src1_sel:WORD_1
v_fma_mixlo_f16 v8,v61,s99,0
v_mfma_f32_16x16x32_bf16 v[36:39],a[216:219],a[64:67],v[36:39]
ds_read_u16 v28,v28
v_lshlrev_b32_sdwa v30,1,v30 dst_sel:DWORD dst_unused:UNUSED_PAD src0_sel:DWORD src1_sel:WORD_1
v_mfma_f32_16x16x32_bf16 v[36:39],a[212:215],a[80:83],v[36:39]
ds_read_u16_d16_hi v30,v30
v_lshl_add_u64 v[48:49],v[48:49],0,v[50:51]
v_mfma_f32_16x16x32_bf16 v[40:43],a[208:211],a[80:83],v[20:23]
s_waitcnt lgkmcnt(9)
v_lshl_add_u64 v[50:51],v[44:45],0,v[46:47]
v_fma_mixhi_f16 v8,v63,s99,0
v_mfma_f32_16x16x32_bf16 v[40:43],a[204:207],a[64:67],v[40:43]
s_waitcnt lgkmcnt(4)
v_lshl_add_u64 v[44:45],v[56:57],0,v[58:59]
v_lshlrev_b32_sdwa v29,1,v29 dst_sel:DWORD dst_unused:UNUSED_PAD src0_sel:DWORD src1_sel:WORD_1
v_pk_fma_f16 v9,v8,v8,s54
v_mfma_f32_16x16x32_bf16 v[40:43],a[200:203],a[48:51],v[40:43]
s_add_u32 m0,s57,45976
ds_read_u16 v29,v29
v_lshlrev_b32_sdwa v31,1,v31 dst_sel:DWORD dst_unused:UNUSED_PAD src0_sel:DWORD src1_sel:WORD_1
v_mfma_f32_16x16x32_bf16 v[56:59],a[200:203],a[52:55],v[20:23]
s_waitcnt lgkmcnt(3)
ds_read_u16_d16_hi v31,v31
v_or_b32 v46,v52,v54
v_pk_mul_f16 v9,v8,v9
v_mfma_f32_16x16x32_bf16 v[56:59],a[204:207],a[68:71],v[56:59]
s_waitcnt vmcnt(20)
v_cvt_pk_bf16_f32 v47,v53,v55
v_lshlrev_b32_sdwa v24,1,v24 dst_sel:DWORD dst_unused:UNUSED_PAD src0_sel:DWORD src1_sel:WORD_1
v_mfma_f32_16x16x32_bf16 v[56:59],a[208:211],a[84:87],v[56:59]
s_barrier
v_exp_f16 v8,v9
v_mfma_f32_16x16x32_bf16 v[52:55],a[212:215],a[84:87],v[16:19]
s_add_u32 s89,s56,6193152
ds_read_u16 v24,v24
v_lshlrev_b32_sdwa v26,1,v26 dst_sel:DWORD dst_unused:UNUSED_PAD src0_sel:DWORD src1_sel:WORD_1
v_mfma_f32_16x16x32_bf16 v[52:55],a[216:219],a[68:71],v[52:55]
buffer_load_dwordx4 v6,s[36:39],s89 offen lds
v_lshlrev_b32_sdwa v25,1,v25 dst_sel:DWORD dst_unused:UNUSED_PAD src0_sel:DWORD src1_sel:WORD_1
v_mfma_f32_16x16x32_bf16 v[52:55],a[220:223],a[52:55],v[52:55]
s_add_u32 m0,s58,12320
ds_read_u16_d16_hi v26,v26
v_exp_f16_sdwa v8,v9 dst_sel:WORD_1 dst_unused:UNUSED_PRESERVE src0_sel:WORD_1
v_mfma_f32_16x16x32_bf16 v[136:139],a[240:243],v[48:51],v[136:139]
s_add_u32 s69,s69,s59
ds_read_u16 v25,v25
v_lshlrev_b32_sdwa v27,1,v27 dst_sel:DWORD dst_unused:UNUSED_PAD src0_sel:DWORD src1_sel:WORD_1
v_mfma_f32_16x16x32_bf16 v[140:143],a[240:243],v[44:47],v[140:143]
buffer_load_dwordx4 v5,s[44:47],s69 offen offset:0 lds
v_lshlrev_b32_sdwa v32,1,v32 dst_sel:DWORD dst_unused:UNUSED_PAD src0_sel:DWORD src1_sel:WORD_1
v_mfma_f32_16x16x32_bf16 v[68:71],a[224:227],v[44:47],v[68:71]
ds_read_u16_d16_hi v27,v27
v_pk_fma_f16 v8,v8,s55,s55
v_mfma_f32_16x16x32_bf16 v[64:67],a[224:227],v[48:51],v[64:67]
v_rcp_f16 v9,v8
v_lshlrev_b32_sdwa v34,1,v34 dst_sel:DWORD dst_unused:UNUSED_PAD src0_sel:DWORD src1_sel:WORD_1
v_mfma_f32_16x16x32_bf16 v[144:147],a[232:235],v[48:51],v[144:147]
s_add_u32 s69,s69,s53
ds_read_u16 v32,v32
v_lshlrev_b32_sdwa v33,1,v33 dst_sel:DWORD dst_unused:UNUSED_PAD src0_sel:DWORD src1_sel:WORD_1
v_mfma_f32_16x16x32_bf16 v[148:151],a[232:235],v[44:47],v[148:151]
buffer_load_dwordx4 v5,s[44:47],s69 offen offset:1024 lds
ds_read_u16_d16_hi v34,v34
v_mfma_f32_16x16x32_bf16 v[84:87],a[244:247],v[44:47],v[84:87]
ds_read_u16 v33,v33
v_lshlrev_b32_sdwa v35,1,v35 dst_sel:DWORD dst_unused:UNUSED_PAD src0_sel:DWORD src1_sel:WORD_1
v_mfma_f32_16x16x32_bf16 v[80:83],a[244:247],v[48:51],v[80:83]
v_rcp_f16_sdwa v9,v8 dst_sel:WORD_1 dst_unused:UNUSED_PRESERVE src0_sel:WORD_1
v_mfma_f32_16x16x32_bf16 v[152:155],a[228:231],v[48:51],v[152:155]
ds_read_u16_d16_hi v35,v35
v_lshlrev_b32_sdwa v60,1,v60 dst_sel:DWORD dst_unused:UNUSED_PAD src0_sel:DWORD src1_sel:WORD_1
v_mfma_f32_16x16x32_bf16 v[156:159],a[228:231],v[44:47],v[156:159]
s_add_u32 s69,s69,s53
ds_read_u16 v60,v60
v_lshlrev_b32_sdwa v62,1,v62 dst_sel:DWORD dst_unused:UNUSED_PAD src0_sel:DWORD src1_sel:WORD_1
v_mfma_f32_16x16x32_bf16 v[100:103],a[248:251],v[44:47],v[100:103]
buffer_load_dwordx4 v5,s[44:47],s69 offen offset:2048 lds
v_fma_mix_f32 v61,v61,v9,0 op_sel_hi:[0,1,0]
v_mfma_f32_16x16x32_bf16 v[96:99],a[248:251],v[48:51],v[96:99]
ds_read_u16_d16_hi v62,v62
v_fma_mix_f32 v63,v63,v9,0 op_sel:[0,1,0] op_sel_hi:[0,1,0]
v_mfma_f32_16x16x32_bf16 v[44:47],a[220:223],a[56:59],v[16:19]
ds_read_b64_tr_b16 a[236:237],v3 offset:18480
ds_read_b64_tr_b16 a[238:239],v3 offset:20016
v_lshlrev_b32_sdwa v36,1,v36 dst_sel:DWORD dst_unused:UNUSED_PAD src0_sel:DWORD src1_sel:WORD_1
v_mfma_f32_16x16x32_bf16 v[44:47],a[216:219],a[72:75],v[44:47]
ds_read_u16 v36,v36
ds_read_b64_tr_b16 a[252:253],v3 offset:18488
v_fma_mixlo_f16 v8,v57,s99,0
v_mfma_f32_16x16x32_bf16 v[44:47],a[212:215],a[88:91],v[44:47]
s_waitcnt lgkmcnt(8)
ds_read_b64_tr_b16 a[254:255],v3 offset:20024
v_lshl_add_u64 v[28:29],v[28:29],0,v[30:31]
v_lshlrev_b32_sdwa v38,1,v38 dst_sel:DWORD dst_unused:UNUSED_PAD src0_sel:DWORD src1_sel:WORD_1
v_mfma_f32_16x16x32_bf16 v[48:51],a[208:211],a[88:91],v[20:23]
ds_read_u16_d16_hi v38,v38
v_lshl_add_u64 v[30:31],v[24:25],0,v[26:27]
v_fma_mixhi_f16 v8,v59,s99,0
v_mfma_f32_16x16x32_bf16 v[48:51],a[204:207],a[72:75],v[48:51]
s_waitcnt lgkmcnt(8)
ds_read_b64_tr_b16 a[192:193],v1 offset:18480
v_lshl_add_u64 v[24:25],v[32:33],0,v[34:35]
v_lshlrev_b32_sdwa v37,1,v37 dst_sel:DWORD dst_unused:UNUSED_PAD src0_sel:DWORD src1_sel:WORD_1
v_pk_fma_f16 v9,v8,v8,s54
v_mfma_f32_16x16x32_bf16 v[48:51],a[200:203],a[56:59],v[48:51]
ds_read_u16 v37,v37
v_lshlrev_b32_sdwa v39,1,v39 dst_sel:DWORD dst_unused:UNUSED_PAD src0_sel:DWORD src1_sel:WORD_1
v_mfma_f32_16x16x32_bf16 v[32:35],a[200:203],a[60:63],v[20:23]
s_waitcnt lgkmcnt(8)
ds_read_u16_d16_hi v39,v39
v_or_b32 v26,v60,v62
v_pk_mul_f16 v9,v8,v9
v_mfma_f32_16x16x32_bf16 v[32:35],a[204:207],a[76:79],v[32:35]
ds_read_b64_tr_b16 a[194:195],v1 offset:19504
v_cvt_pk_bf16_f32 v27,v61,v63
v_lshlrev_b32_sdwa v40,1,v40 dst_sel:DWORD dst_unused:UNUSED_PAD src0_sel:DWORD src1_sel:WORD_1
v_mfma_f32_16x16x32_bf16 v[32:35],a[208:211],a[92:95],v[32:35]
v_exp_f16 v8,v9
v_mfma_f32_16x16x32_bf16 v[60:63],a[212:215],a[92:95],v[16:19]
ds_read_u16 v40,v40
v_lshlrev_b32_sdwa v42,1,v42 dst_sel:DWORD dst_unused:UNUSED_PAD src0_sel:DWORD src1_sel:WORD_1
v_mfma_f32_16x16x32_bf16 v[60:63],a[216:219],a[76:79],v[60:63]
ds_read_u16_d16_hi v42,v42
v_lshlrev_b32_sdwa v41,1,v41 dst_sel:DWORD dst_unused:UNUSED_PAD src0_sel:DWORD src1_sel:WORD_1
v_mfma_f32_16x16x32_bf16 v[60:63],a[220:223],a[60:63],v[60:63]
v_exp_f16_sdwa v8,v9 dst_sel:WORD_1 dst_unused:UNUSED_PRESERVE src0_sel:WORD_1
v_mfma_f32_16x16x32_bf16 v[112:115],a[240:243],v[28:31],v[112:115]
ds_read_u16 v41,v41
v_lshlrev_b32_sdwa v43,1,v43 dst_sel:DWORD dst_unused:UNUSED_PAD src0_sel:DWORD src1_sel:WORD_1
v_mfma_f32_16x16x32_bf16 v[116:119],a[240:243],v[24:27],v[116:119]
ds_read_u16_d16_hi v43,v43
v_lshlrev_b32_sdwa v52,1,v52 dst_sel:DWORD dst_unused:UNUSED_PAD src0_sel:DWORD src1_sel:WORD_1
v_mfma_f32_16x16x32_bf16 v[76:79],a[224:227],v[24:27],v[76:79]
ds_read_u16 v52,v52
v_pk_fma_f16 v8,v8,s55,s55
v_mfma_f32_16x16x32_bf16 v[72:75],a[224:227],v[28:31],v[72:75]
ds_read_b64_tr_b16 a[240:241],v3 offset:18544
v_rcp_f16 v9,v8
v_lshlrev_b32_sdwa v54,1,v54 dst_sel:DWORD dst_unused:UNUSED_PAD src0_sel:DWORD src1_sel:WORD_1
v_mfma_f32_16x16x32_bf16 v[120:123],a[232:235],v[28:31],v[120:123]
ds_read_u16_d16_hi v54,v54
v_lshlrev_b32_sdwa v53,1,v53 dst_sel:DWORD dst_unused:UNUSED_PAD src0_sel:DWORD src1_sel:WORD_1
v_mfma_f32_16x16x32_bf16 v[124:127],a[232:235],v[24:27],v[124:127]
ds_read_u16 v53,v53
ds_read_b64_tr_b16 a[242:243],v3 offset:20080
v_mfma_f32_16x16x32_bf16 v[92:95],a[244:247],v[24:27],v[92:95]
ds_read_b64_tr_b16 a[224:225],v3 offset:18552
v_lshlrev_b32_sdwa v55,1,v55 dst_sel:DWORD dst_unused:UNUSED_PAD src0_sel:DWORD src1_sel:WORD_1
v_mfma_f32_16x16x32_bf16 v[88:91],a[244:247],v[28:31],v[88:91]
ds_read_u16_d16_hi v55,v55
v_rcp_f16_sdwa v9,v8 dst_sel:WORD_1 dst_unused:UNUSED_PRESERVE src0_sel:WORD_1
v_mfma_f32_16x16x32_bf16 v[128:131],a[228:231],v[28:31],v[128:131]
ds_read_b64_tr_b16 a[226:227],v3 offset:20088
ds_read_b64_tr_b16 a[232:233],v3 offset:18608
v_lshlrev_b32_sdwa v56,1,v56 dst_sel:DWORD dst_unused:UNUSED_PAD src0_sel:DWORD src1_sel:WORD_1
v_mfma_f32_16x16x32_bf16 v[132:135],a[228:231],v[24:27],v[132:135]
ds_read_u16 v56,v56
v_lshlrev_b32_sdwa v58,1,v58 dst_sel:DWORD dst_unused:UNUSED_PAD src0_sel:DWORD src1_sel:WORD_1
v_mfma_f32_16x16x32_bf16 v[108:111],a[248:251],v[24:27],v[108:111]
ds_read_u16_d16_hi v58,v58
v_fma_mix_f32 v57,v57,v9,0 op_sel_hi:[0,1,0]
v_mfma_f32_16x16x32_bf16 v[104:107],a[248:251],v[28:31],v[104:107]
ds_read_b64_tr_b16 a[234:235],v3 offset:20144
v_fma_mix_f32 v59,v59,v9,0 op_sel:[0,1,0] op_sel_hi:[0,1,0]
v_mfma_f32_16x16x32_bf16 v[24:27],a[220:223],a[0:3],v[16:19]
s_add_u32 m0,s57,49504
ds_read_b64_tr_b16 a[244:245],v3 offset:18616
v_lshlrev_b32_sdwa v44,1,v44 dst_sel:DWORD dst_unused:UNUSED_PAD src0_sel:DWORD src1_sel:WORD_1
v_mfma_f32_16x16x32_bf16 v[24:27],a[216:219],a[16:19],v[24:27]
s_add_u32 s89,s56,6291456
ds_read_b64_tr_b16 a[246:247],v3 offset:20152
v_fma_mixlo_f16 v8,v33,s99,0
v_mfma_f32_16x16x32_bf16 v[24:27],a[212:215],a[32:35],v[24:27]
s_waitcnt lgkmcnt(9)
buffer_load_dwordx4 v6,s[36:39],s89 offen lds
v_lshl_add_u64 v[36:37],v[36:37],0,v[38:39]
v_lshlrev_b32_sdwa v46,1,v46 dst_sel:DWORD dst_unused:UNUSED_PAD src0_sel:DWORD src1_sel:WORD_1
v_mfma_f32_16x16x32_bf16 v[28:31],a[208:211],a[32:35],v[20:23]
ds_read_u16 v44,v44
v_lshl_add_u64 v[38:39],v[40:41],0,v[42:43]
v_fma_mixhi_f16 v8,v35,s99,0
v_mfma_f32_16x16x32_bf16 v[28:31],a[204:207],a[16:19],v[28:31]
s_waitcnt lgkmcnt(8)
ds_read_u16_d16_hi v46,v46
v_lshl_add_u64 v[40:41],v[52:53],0,v[54:55]
v_lshlrev_b32_sdwa v45,1,v45 dst_sel:DWORD dst_unused:UNUSED_PAD src0_sel:DWORD src1_sel:WORD_1
v_pk_fma_f16 v9,v8,v8,s54
v_mfma_f32_16x16x32_bf16 v[28:31],a[200:203],a[0:3],v[28:31]
ds_read_u16 v45,v45
v_lshlrev_b32_sdwa v47,1,v47 dst_sel:DWORD dst_unused:UNUSED_PAD src0_sel:DWORD src1_sel:WORD_1
v_mfma_f32_16x16x32_bf16 v[52:55],a[200:203],a[4:7],v[20:23]
s_waitcnt lgkmcnt(6)
ds_read_u16_d16_hi v47,v47
v_or_b32 v42,v56,v58
v_pk_mul_f16 v9,v8,v9
v_mfma_f32_16x16x32_bf16 v[52:55],a[204:207],a[20:23],v[52:55]
ds_read_b64_tr_b16 a[196:197],v1 offset:20528
v_cvt_pk_bf16_f32 v43,v57,v59
v_lshlrev_b32_sdwa v48,1,v48 dst_sel:DWORD dst_unused:UNUSED_PAD src0_sel:DWORD src1_sel:WORD_1
v_mfma_f32_16x16x32_bf16 v[52:55],a[208:211],a[36:39],v[52:55]
v_exp_f16 v8,v9
v_mfma_f32_16x16x32_bf16 v[56:59],a[212:215],a[36:39],v[16:19]
ds_read_u16 v48,v48
v_lshlrev_b32_sdwa v50,1,v50 dst_sel:DWORD dst_unused:UNUSED_PAD src0_sel:DWORD src1_sel:WORD_1
v_mfma_f32_16x16x32_bf16 v[56:59],a[216:219],a[20:23],v[56:59]
ds_read_u16_d16_hi v50,v50
v_lshlrev_b32_sdwa v49,1,v49 dst_sel:DWORD dst_unused:UNUSED_PAD src0_sel:DWORD src1_sel:WORD_1
v_mfma_f32_16x16x32_bf16 v[56:59],a[220:223],a[4:7],v[56:59]
v_exp_f16_sdwa v8,v9 dst_sel:WORD_1 dst_unused:UNUSED_PRESERVE src0_sel:WORD_1
v_mfma_f32_16x16x32_bf16 v[232:235],a[236:239],v[36:39],v[232:235]
ds_read_u16 v49,v49
v_lshlrev_b32_sdwa v51,1,v51 dst_sel:DWORD dst_unused:UNUSED_PAD src0_sel:DWORD src1_sel:WORD_1
v_mfma_f32_16x16x32_bf16 v[236:239],a[236:239],v[40:43],v[236:239]
ds_read_u16_d16_hi v51,v51
v_lshlrev_b32_sdwa v60,1,v60 dst_sel:DWORD dst_unused:UNUSED_PAD src0_sel:DWORD src1_sel:WORD_1
v_mfma_f32_16x16x32_bf16 v[164:167],a[252:255],v[40:43],v[164:167]
ds_read_u16 v60,v60
v_pk_fma_f16 v8,v8,s55,s55
v_mfma_f32_16x16x32_bf16 v[160:163],a[252:255],v[36:39],v[160:163]
v_rcp_f16 v9,v8
v_lshlrev_b32_sdwa v62,1,v62 dst_sel:DWORD dst_unused:UNUSED_PAD src0_sel:DWORD src1_sel:WORD_1
v_mfma_f32_16x16x32_bf16 v[240:243],a[240:243],v[36:39],v[240:243]
ds_read_u16_d16_hi v62,v62
v_lshlrev_b32_sdwa v61,1,v61 dst_sel:DWORD dst_unused:UNUSED_PAD src0_sel:DWORD src1_sel:WORD_1
v_mfma_f32_16x16x32_bf16 v[244:247],a[240:243],v[40:43],v[244:247]
ds_read_u16 v61,v61
v_lshlrev_b32_sdwa v63,1,v63 dst_sel:DWORD dst_unused:UNUSED_PAD src0_sel:DWORD src1_sel:WORD_1
v_mfma_f32_16x16x32_bf16 v[180:183],a[224:227],v[40:43],v[180:183]
ds_read_u16_d16_hi v63,v63
v_lshlrev_b32_sdwa v32,1,v32 dst_sel:DWORD dst_unused:UNUSED_PAD src0_sel:DWORD src1_sel:WORD_1
v_mfma_f32_16x16x32_bf16 v[176:179],a[224:227],v[36:39],v[176:179]
s_waitcnt lgkmcnt(6)
v_rcp_f16_sdwa v9,v8 dst_sel:WORD_1 dst_unused:UNUSED_PRESERVE src0_sel:WORD_1
v_mfma_f32_16x16x32_bf16 v[248:251],a[232:235],v[36:39],v[248:251]
ds_read_u16 v32,v32
v_lshlrev_b32_sdwa v34,1,v34 dst_sel:DWORD dst_unused:UNUSED_PAD src0_sel:DWORD src1_sel:WORD_1
v_mfma_f32_16x16x32_bf16 v[252:255],a[232:235],v[40:43],v[252:255]
ds_read_u16_d16_hi v34,v34
v_fma_mix_f32 v33,v33,v9,0 op_sel_hi:[0,1,0]
v_mfma_f32_16x16x32_bf16 v[196:199],a[244:247],v[40:43],v[196:199]
ds_read_b64_tr_b16 a[198:199],v1 offset:21552
v_fma_mix_f32 v35,v35,v9,0 op_sel:[0,1,0] op_sel_hi:[0,1,0]
v_mfma_f32_16x16x32_bf16 v[192:195],a[244:247],v[36:39],v[192:195]
v_mfma_f32_16x16x32_bf16 v[40:43],a[220:223],a[8:11],v[16:19]
v_lshlrev_b32_sdwa v24,1,v24 dst_sel:DWORD dst_unused:UNUSED_PAD src0_sel:DWORD src1_sel:WORD_1
v_fma_mixlo_f16 v8,v53,s99,0
v_mfma_f32_16x16x32_bf16 v[40:43],a[216:219],a[24:27],v[40:43]
ds_read_u16 v24,v24
v_lshlrev_b32_sdwa v26,1,v26 dst_sel:DWORD dst_unused:UNUSED_PAD src0_sel:DWORD src1_sel:WORD_1
v_mfma_f32_16x16x32_bf16 v[40:43],a[212:215],a[40:43],v[40:43]
ds_read_u16_d16_hi v26,v26
v_lshl_add_u64 v[44:45],v[44:45],0,v[46:47]
v_mfma_f32_16x16x32_bf16 v[36:39],a[208:211],a[40:43],v[20:23]
s_waitcnt lgkmcnt(5)
v_lshl_add_u64 v[46:47],v[48:49],0,v[50:51]
v_fma_mixhi_f16 v8,v55,s99,0
v_mfma_f32_16x16x32_bf16 v[36:39],a[204:207],a[24:27],v[36:39]
v_lshl_add_u64 v[48:49],v[60:61],0,v[62:63]
v_lshlrev_b32_sdwa v25,1,v25 dst_sel:DWORD dst_unused:UNUSED_PAD src0_sel:DWORD src1_sel:WORD_1
v_pk_fma_f16 v9,v8,v8,s54
v_mfma_f32_16x16x32_bf16 v[36:39],a[200:203],a[8:11],v[36:39]
ds_read_u16 v25,v25
v_lshlrev_b32_sdwa v27,1,v27 dst_sel:DWORD dst_unused:UNUSED_PAD src0_sel:DWORD src1_sel:WORD_1
v_mfma_f32_16x16x32_bf16 v[60:63],a[200:203],a[12:15],v[20:23]
s_waitcnt lgkmcnt(4)
ds_read_b128 v[20:23],v4 offset:544
v_or_b32 v50,v32,v34
v_pk_mul_f16 v9,v8,v9
v_mfma_f32_16x16x32_bf16 v[60:63],a[204:207],a[28:31],v[60:63]
ds_read_u16_d16_hi v27,v27
v_cvt_pk_bf16_f32 v51,v33,v35
v_lshlrev_b32_sdwa v28,1,v28 dst_sel:DWORD dst_unused:UNUSED_PAD src0_sel:DWORD src1_sel:WORD_1
v_mfma_f32_16x16x32_bf16 v[60:63],a[208:211],a[44:47],v[60:63]
ds_read_b64_tr_b16 a[200:201],v1 offset:22576
v_exp_f16 v8,v9
v_mfma_f32_16x16x32_bf16 v[32:35],a[212:215],a[44:47],v[16:19]
ds_read_b128 v[16:19],v4 offset:512
ds_read_b64_tr_b16 a[202:203],v1 offset:23600
v_lshlrev_b32_sdwa v30,1,v30 dst_sel:DWORD dst_unused:UNUSED_PAD src0_sel:DWORD src1_sel:WORD_1
v_mfma_f32_16x16x32_bf16 v[32:35],a[216:219],a[28:31],v[32:35]
ds_read_b64_tr_b16 a[204:205],v2 offset:22576
ds_read_b64_tr_b16 a[206:207],v2 offset:23600
v_lshlrev_b32_sdwa v29,1,v29 dst_sel:DWORD dst_unused:UNUSED_PAD src0_sel:DWORD src1_sel:WORD_1
v_mfma_f32_16x16x32_bf16 v[32:35],a[220:223],a[12:15],v[32:35]
ds_read_u16 v28,v28
v_exp_f16_sdwa v8,v9 dst_sel:WORD_1 dst_unused:UNUSED_PRESERVE src0_sel:WORD_1
v_mfma_f32_16x16x32_bf16 v[208:211],a[236:239],v[44:47],v[208:211]
ds_read_u16_d16_hi v30,v30
v_lshlrev_b32_sdwa v31,1,v31 dst_sel:DWORD dst_unused:UNUSED_PAD src0_sel:DWORD src1_sel:WORD_1
v_mfma_f32_16x16x32_bf16 v[212:215],a[236:239],v[48:51],v[212:215]
ds_read_u16 v29,v29
v_lshlrev_b32_sdwa v56,1,v56 dst_sel:DWORD dst_unused:UNUSED_PAD src0_sel:DWORD src1_sel:WORD_1
v_mfma_f32_16x16x32_bf16 v[172:175],a[252:255],v[48:51],v[172:175]
ds_read_u16_d16_hi v31,v31
v_pk_fma_f16 v8,v8,s55,s55
v_mfma_f32_16x16x32_bf16 v[168:171],a[252:255],v[44:47],v[168:171]
v_rcp_f16 v9,v8
v_lshlrev_b32_sdwa v58,1,v58 dst_sel:DWORD dst_unused:UNUSED_PAD src0_sel:DWORD src1_sel:WORD_1
v_mfma_f32_16x16x32_bf16 v[216:219],a[240:243],v[44:47],v[216:219]
ds_read_b64_tr_b16 a[208:209],v2 offset:20528
v_lshlrev_b32_sdwa v57,1,v57 dst_sel:DWORD dst_unused:UNUSED_PAD src0_sel:DWORD src1_sel:WORD_1
v_mfma_f32_16x16x32_bf16 v[220:223],a[240:243],v[48:51],v[220:223]
ds_read_b64_tr_b16 a[210:211],v2 offset:21552
v_lshlrev_b32_sdwa v59,1,v59 dst_sel:DWORD dst_unused:UNUSED_PAD src0_sel:DWORD src1_sel:WORD_1
v_mfma_f32_16x16x32_bf16 v[188:191],a[224:227],v[48:51],v[188:191]
ds_read_u16 v56,v56
v_lshlrev_b32_sdwa v52,1,v52 dst_sel:DWORD dst_unused:UNUSED_PAD src0_sel:DWORD src1_sel:WORD_1
v_mfma_f32_16x16x32_bf16 v[184:187],a[224:227],v[44:47],v[184:187]
ds_read_u16_d16_hi v58,v58
v_rcp_f16_sdwa v9,v8 dst_sel:WORD_1 dst_unused:UNUSED_PRESERVE src0_sel:WORD_1
v_mfma_f32_16x16x32_bf16 v[224:227],a[232:235],v[44:47],v[224:227]
ds_read_u16 v57,v57
ds_read_u16_d16_hi v59,v59
v_mfma_f32_16x16x32_bf16 v[228:231],a[232:235],v[48:51],v[228:231]
ds_read_b64_tr_b16 a[212:213],v2 offset:18480
ds_read_b64_tr_b16 a[214:215],v2 offset:19504
v_lshlrev_b32_sdwa v54,1,v54 dst_sel:DWORD dst_unused:UNUSED_PAD src0_sel:DWORD src1_sel:WORD_1
v_mfma_f32_16x16x32_bf16 v[204:207],a[244:247],v[48:51],v[204:207]
ds_read_u16 v52,v52
v_fma_mix_f32 v53,v53,v9,0 op_sel_hi:[0,1,0]
v_mfma_f32_16x16x32_bf16 v[200:203],a[244:247],v[44:47],v[200:203]
s_waitcnt lgkmcnt(9)
ds_read_u16_d16_hi v54,v54
v_fma_mix_f32 v55,v55,v9,0 op_sel:[0,1,0] op_sel_hi:[0,1,0]
v_mfma_f32_16x16x32_bf16 v[48:51],a[192:195],a[48:51],v[16:19]
s_add_u32 m0,s57,53040
v_lshlrev_b32_sdwa v40,1,v40 dst_sel:DWORD dst_unused:UNUSED_PAD src0_sel:DWORD src1_sel:WORD_1
v_fma_mixlo_f16 v8,v61,s99,0
v_mfma_f32_16x16x32_bf16 v[48:51],a[196:199],a[64:67],v[48:51]
s_add_u32 s89,s56,6389760
ds_read_u16 v40,v40
v_lshlrev_b32_sdwa v42,1,v42 dst_sel:DWORD dst_unused:UNUSED_PAD src0_sel:DWORD src1_sel:WORD_1
v_mfma_f32_16x16x32_bf16 v[48:51],a[200:203],a[80:83],v[48:51]
buffer_load_dwordx4 v6,s[36:39],s89 offen lds
v_lshl_add_u64 v[24:25],v[24:25],0,v[26:27]
v_mfma_f32_16x16x32_bf16 v[44:47],a[204:207],a[80:83],v[20:23]
s_waitcnt lgkmcnt(8)
ds_read_u16_d16_hi v42,v42
v_lshl_add_u64 v[26:27],v[28:29],0,v[30:31]
v_fma_mixhi_f16 v8,v63,s99,0
v_mfma_f32_16x16x32_bf16 v[44:47],a[208:211],a[64:67],v[44:47]
s_waitcnt lgkmcnt(4)
v_lshl_add_u64 v[28:29],v[56:57],0,v[58:59]
v_lshlrev_b32_sdwa v41,1,v41 dst_sel:DWORD dst_unused:UNUSED_PAD src0_sel:DWORD src1_sel:WORD_1
v_pk_fma_f16 v9,v8,v8,s54
v_mfma_f32_16x16x32_bf16 v[44:47],a[212:215],a[48:51],v[44:47]
ds_read_u16 v41,v41
v_lshlrev_b32_sdwa v43,1,v43 dst_sel:DWORD dst_unused:UNUSED_PAD src0_sel:DWORD src1_sel:WORD_1
v_mfma_f32_16x16x32_bf16 v[56:59],a[212:215],a[52:55],v[20:23]
s_waitcnt lgkmcnt(3)
ds_read_u16_d16_hi v43,v43
v_or_b32 v30,v52,v54
v_pk_mul_f16 v9,v8,v9
v_mfma_f32_16x16x32_bf16 v[56:59],a[208:211],a[68:71],v[56:59]
s_waitcnt vmcnt(13)
v_cvt_pk_bf16_f32 v31,v53,v55
v_lshlrev_b32_sdwa v36,1,v36 dst_sel:DWORD dst_unused:UNUSED_PAD src0_sel:DWORD src1_sel:WORD_1
v_mfma_f32_16x16x32_bf16 v[56:59],a[204:207],a[84:87],v[56:59]
s_barrier
v_exp_f16 v8,v9
v_mfma_f32_16x16x32_bf16 v[52:55],a[200:203],a[84:87],v[16:19]
ds_read_u16 v36,v36
v_lshlrev_b32_sdwa v38,1,v38 dst_sel:DWORD dst_unused:UNUSED_PAD src0_sel:DWORD src1_sel:WORD_1
v_mfma_f32_16x16x32_bf16 v[52:55],a[196:199],a[68:71],v[52:55]
s_add_u32 m0,s58,18480
ds_read_u16_d16_hi v38,v38
v_lshlrev_b32_sdwa v37,1,v37 dst_sel:DWORD dst_unused:UNUSED_PAD src0_sel:DWORD src1_sel:WORD_1
v_mfma_f32_16x16x32_bf16 v[52:55],a[192:195],a[52:55],v[52:55]
s_add_u32 s69,s69,s59
v_exp_f16_sdwa v8,v9 dst_sel:WORD_1 dst_unused:UNUSED_PRESERVE src0_sel:WORD_1
v_mfma_f32_16x16x32_bf16 v[136:139],a[236:239],v[24:27],v[136:139]
buffer_load_dwordx4 v5,s[44:47],s69 offen offset:0 lds
v_lshlrev_b32_sdwa v39,1,v39 dst_sel:DWORD dst_unused:UNUSED_PAD src0_sel:DWORD src1_sel:WORD_1
v_mfma_f32_16x16x32_bf16 v[140:143],a[236:239],v[28:31],v[140:143]
ds_read_u16 v37,v37
v_lshlrev_b32_sdwa v32,1,v32 dst_sel:DWORD dst_unused:UNUSED_PAD src0_sel:DWORD src1_sel:WORD_1
v_mfma_f32_16x16x32_bf16 v[68:71],a[252:255],v[28:31],v[68:71]
TP7094_%=:
s_cbranch_execz TR0_%=
ds_read_u16_d16_hi v39,v39
v_pk_fma_f16 v8,v8,s55,s55
v_mfma_f32_16x16x32_bf16 v[64:67],a[252:255],v[24:27],v[64:67]
v_rcp_f16 v9,v8
v_lshlrev_b32_sdwa v34,1,v34 dst_sel:DWORD dst_unused:UNUSED_PAD src0_sel:DWORD src1_sel:WORD_1
v_mfma_f32_16x16x32_bf16 v[144:147],a[240:243],v[24:27],v[144:147]
s_add_u32 s69,s69,s53
ds_read_u16 v32,v32
v_lshlrev_b32_sdwa v33,1,v33 dst_sel:DWORD dst_unused:UNUSED_PAD src0_sel:DWORD src1_sel:WORD_1
v_mfma_f32_16x16x32_bf16 v[148:151],a[240:243],v[28:31],v[148:151]
buffer_load_dwordx4 v5,s[44:47],s69 offen offset:1024 lds
ds_read_u16_d16_hi v34,v34
v_mfma_f32_16x16x32_bf16 v[84:87],a[224:227],v[28:31],v[84:87]
ds_read_u16 v33,v33
v_lshlrev_b32_sdwa v35,1,v35 dst_sel:DWORD dst_unused:UNUSED_PAD src0_sel:DWORD src1_sel:WORD_1
v_mfma_f32_16x16x32_bf16 v[80:83],a[224:227],v[24:27],v[80:83]
v_rcp_f16_sdwa v9,v8 dst_sel:WORD_1 dst_unused:UNUSED_PRESERVE src0_sel:WORD_1
v_mfma_f32_16x16x32_bf16 v[152:155],a[232:235],v[24:27],v[152:155]
ds_read_u16_d16_hi v35,v35
v_lshlrev_b32_sdwa v60,1,v60 dst_sel:DWORD dst_unused:UNUSED_PAD src0_sel:DWORD src1_sel:WORD_1
v_mfma_f32_16x16x32_bf16 v[156:159],a[232:235],v[28:31],v[156:159]
s_add_u32 s69,s69,s53
ds_read_u16 v60,v60
v_lshlrev_b32_sdwa v62,1,v62 dst_sel:DWORD dst_unused:UNUSED_PAD src0_sel:DWORD src1_sel:WORD_1
v_mfma_f32_16x16x32_bf16 v[100:103],a[244:247],v[28:31],v[100:103]
buffer_load_dwordx4 v5,s[44:47],s69 offen offset:2048 lds
v_fma_mix_f32 v61,v61,v9,0 op_sel_hi:[0,1,0]
v_mfma_f32_16x16x32_bf16 v[96:99],a[244:247],v[24:27],v[96:99]
ds_read_u16_d16_hi v62,v62
v_fma_mix_f32 v63,v63,v9,0 op_sel:[0,1,0] op_sel_hi:[0,1,0]
v_mfma_f32_16x16x32_bf16 v[28:31],a[192:195],a[56:59],v[16:19]
ds_read_b64_tr_b16 a[228:229],v3 offset:0
ds_read_b64_tr_b16 a[230:231],v3 offset:1536
v_lshlrev_b32_sdwa v48,1,v48 dst_sel:DWORD dst_unused:UNUSED_PAD src0_sel:DWORD src1_sel:WORD_1
v_mfma_f32_16x16x32_bf16 v[28:31],a[196:199],a[72:75],v[28:31]
ds_read_u16 v48,v48
ds_read_b64_tr_b16 a[248:249],v3 offset:8
v_fma_mixlo_f16 v8,v57,s99,0
v_mfma_f32_16x16x32_bf16 v[28:31],a[200:203],a[88:91],v[28:31]
s_waitcnt lgkmcnt(8)
ds_read_b64_tr_b16 a[250:251],v3 offset:1544
v_lshl_add_u64 v[40:41],v[40:41],0,v[42:43]
v_lshlrev_b32_sdwa v50,1,v50 dst_sel:DWORD dst_unused:UNUSED_PAD src0_sel:DWORD src1_sel:WORD_1
v_mfma_f32_16x16x32_bf16 v[24:27],a[204:207],a[88:91],v[20:23]
ds_read_u16_d16_hi v50,v50
v_lshl_add_u64 v[42:43],v[36:37],0,v[38:39]
v_fma_mixhi_f16 v8,v59,s99,0
v_mfma_f32_16x16x32_bf16 v[24:27],a[208:211],a[72:75],v[24:27]
s_waitcnt lgkmcnt(8)
ds_read_b64_tr_b16 a[216:217],v1 offset:0
v_lshl_add_u64 v[36:37],v[32:33],0,v[34:35]
v_lshlrev_b32_sdwa v49,1,v49 dst_sel:DWORD dst_unused:UNUSED_PAD src0_sel:DWORD src1_sel:WORD_1
v_pk_fma_f16 v9,v8,v8,s54
v_mfma_f32_16x16x32_bf16 v[24:27],a[212:215],a[56:59],v[24:27]
ds_read_u16 v49,v49
v_lshlrev_b32_sdwa v51,1,v51 dst_sel:DWORD dst_unused:UNUSED_PAD src0_sel:DWORD src1_sel:WORD_1
v_mfma_f32_16x16x32_bf16 v[32:35],a[212:215],a[60:63],v[20:23]
s_waitcnt lgkmcnt(8)
ds_read_u16_d16_hi v51,v51
v_or_b32 v38,v60,v62
v_pk_mul_f16 v9,v8,v9
v_mfma_f32_16x16x32_bf16 v[32:35],a[208:211],a[76:79],v[32:35]
ds_read_b64_tr_b16 a[218:219],v1 offset:1024
v_cvt_pk_bf16_f32 v39,v61,v63
v_lshlrev_b32_sdwa v44,1,v44 dst_sel:DWORD dst_unused:UNUSED_PAD src0_sel:DWORD src1_sel:WORD_1
v_mfma_f32_16x16x32_bf16 v[32:35],a[204:207],a[92:95],v[32:35]
v_exp_f16 v8,v9
v_mfma_f32_16x16x32_bf16 v[60:63],a[200:203],a[92:95],v[16:19]
ds_read_u16 v44,v44
v_lshlrev_b32_sdwa v46,1,v46 dst_sel:DWORD dst_unused:UNUSED_PAD src0_sel:DWORD src1_sel:WORD_1
v_mfma_f32_16x16x32_bf16 v[60:63],a[196:199],a[76:79],v[60:63]
ds_read_u16_d16_hi v46,v46
v_lshlrev_b32_sdwa v45,1,v45 dst_sel:DWORD dst_unused:UNUSED_PAD src0_sel:DWORD src1_sel:WORD_1
v_mfma_f32_16x16x32_bf16 v[60:63],a[192:195],a[60:63],v[60:63]
v_exp_f16_sdwa v8,v9 dst_sel:WORD_1 dst_unused:UNUSED_PRESERVE src0_sel:WORD_1
v_mfma_f32_16x16x32_bf16 v[112:115],a[236:239],v[40:43],v[112:115]
ds_read_u16 v45,v45
v_lshlrev_b32_sdwa v47,1,v47 dst_sel:DWORD dst_unused:UNUSED_PAD src0_sel:DWORD src1_sel:WORD_1
v_mfma_f32_16x16x32_bf16 v[116:119],a[236:239],v[36:39],v[116:119]
ds_read_u16_d16_hi v47,v47
v_lshlrev_b32_sdwa v52,1,v52 dst_sel:DWORD dst_unused:UNUSED_PAD src0_sel:DWORD src1_sel:WORD_1
v_mfma_f32_16x16x32_bf16 v[76:79],a[252:255],v[36:39],v[76:79]
ds_read_u16 v52,v52
v_pk_fma_f16 v8,v8,s55,s55
v_mfma_f32_16x16x32_bf16 v[72:75],a[252:255],v[40:43],v[72:75]
ds_read_b64_tr_b16 a[236:237],v3 offset:64
v_rcp_f16 v9,v8
v_lshlrev_b32_sdwa v54,1,v54 dst_sel:DWORD dst_unused:UNUSED_PAD src0_sel:DWORD src1_sel:WORD_1
v_mfma_f32_16x16x32_bf16 v[120:123],a[240:243],v[40:43],v[120:123]
ds_read_u16_d16_hi v54,v54
v_lshlrev_b32_sdwa v53,1,v53 dst_sel:DWORD dst_unused:UNUSED_PAD src0_sel:DWORD src1_sel:WORD_1
v_mfma_f32_16x16x32_bf16 v[124:127],a[240:243],v[36:39],v[124:127]
ds_read_u16 v53,v53
ds_read_b64_tr_b16 a[238:239],v3 offset:1600
v_mfma_f32_16x16x32_bf16 v[92:95],a[224:227],v[36:39],v[92:95]
ds_read_b64_tr_b16 a[252:253],v3 offset:72
v_lshlrev_b32_sdwa v55,1,v55 dst_sel:DWORD dst_unused:UNUSED_PAD src0_sel:DWORD src1_sel:WORD_1
v_mfma_f32_16x16x32_bf16 v[88:91],a[224:227],v[40:43],v[88:91]
ds_read_u16_d16_hi v55,v55
v_rcp_f16_sdwa v9,v8 dst_sel:WORD_1 dst_unused:UNUSED_PRESERVE src0_sel:WORD_1
v_mfma_f32_16x16x32_bf16 v[128:131],a[232:235],v[40:43],v[128:131]
ds_read_b64_tr_b16 a[254:255],v3 offset:1608
ds_read_b64_tr_b16 a[240:241],v3 offset:128
v_lshlrev_b32_sdwa v56,1,v56 dst_sel:DWORD dst_unused:UNUSED_PAD src0_sel:DWORD src1_sel:WORD_1
v_mfma_f32_16x16x32_bf16 v[132:135],a[232:235],v[36:39],v[132:135]
ds_read_u16 v56,v56
v_lshlrev_b32_sdwa v58,1,v58 dst_sel:DWORD dst_unused:UNUSED_PAD src0_sel:DWORD src1_sel:WORD_1
v_mfma_f32_16x16x32_bf16 v[108:111],a[244:247],v[36:39],v[108:111]
ds_read_u16_d16_hi v58,v58
v_fma_mix_f32 v57,v57,v9,0 op_sel_hi:[0,1,0]
v_mfma_f32_16x16x32_bf16 v[104:107],a[244:247],v[40:43],v[104:107]
ds_read_b64_tr_b16 a[242:243],v3 offset:1664
v_fma_mix_f32 v59,v59,v9,0 op_sel:[0,1,0] op_sel_hi:[0,1,0]
v_mfma_f32_16x16x32_bf16 v[36:39],a[192:195],a[0:3],v[16:19]
ds_read_b64_tr_b16 a[224:225],v3 offset:136
v_lshlrev_b32_sdwa v28,1,v28 dst_sel:DWORD dst_unused:UNUSED_PAD src0_sel:DWORD src1_sel:WORD_1
v_mfma_f32_16x16x32_bf16 v[36:39],a[196:199],a[16:19],v[36:39]
ds_read_b64_tr_b16 a[226:227],v3 offset:1672
v_fma_mixlo_f16 v8,v33,s99,0
v_mfma_f32_16x16x32_bf16 v[36:39],a[200:203],a[32:35],v[36:39]
s_waitcnt lgkmcnt(9)
ds_read_u16 v28,v28
v_lshl_add_u64 v[48:49],v[48:49],0,v[50:51]
v_lshlrev_b32_sdwa v30,1,v30 dst_sel:DWORD dst_unused:UNUSED_PAD src0_sel:DWORD src1_sel:WORD_1
v_mfma_f32_16x16x32_bf16 v[40:43],a[204:207],a[32:35],v[20:23]
ds_read_u16_d16_hi v30,v30
v_lshl_add_u64 v[50:51],v[44:45],0,v[46:47]
v_fma_mixhi_f16 v8,v35,s99,0
v_mfma_f32_16x16x32_bf16 v[40:43],a[208:211],a[16:19],v[40:43]
s_waitcnt lgkmcnt(9)
ds_read_b64_tr_b16 a[220:221],v1 offset:2048
v_lshl_add_u64 v[44:45],v[52:53],0,v[54:55]
v_lshlrev_b32_sdwa v29,1,v29 dst_sel:DWORD dst_unused:UNUSED_PAD src0_sel:DWORD src1_sel:WORD_1
v_pk_fma_f16 v9,v8,v8,s54
v_mfma_f32_16x16x32_bf16 v[40:43],a[212:215],a[0:3],v[40:43]
ds_read_u16 v29,v29
v_lshlrev_b32_sdwa v31,1,v31 dst_sel:DWORD dst_unused:UNUSED_PAD src0_sel:DWORD src1_sel:WORD_1
v_mfma_f32_16x16x32_bf16 v[52:55],a[212:215],a[4:7],v[20:23]
s_waitcnt lgkmcnt(7)
ds_read_u16_d16_hi v31,v31
v_or_b32 v46,v56,v58
v_pk_mul_f16 v9,v8,v9
v_mfma_f32_16x16x32_bf16 v[52:55],a[208:211],a[20:23],v[52:55]
ds_read_b64_tr_b16 a[222:223],v1 offset:3072
v_cvt_pk_bf16_f32 v47,v57,v59
v_lshlrev_b32_sdwa v24,1,v24 dst_sel:DWORD dst_unused:UNUSED_PAD src0_sel:DWORD src1_sel:WORD_1
v_mfma_f32_16x16x32_bf16 v[52:55],a[204:207],a[36:39],v[52:55]
v_exp_f16 v8,v9
v_mfma_f32_16x16x32_bf16 v[56:59],a[200:203],a[36:39],v[16:19]
ds_read_u16 v24,v24
v_lshlrev_b32_sdwa v26,1,v26 dst_sel:DWORD dst_unused:UNUSED_PAD src0_sel:DWORD src1_sel:WORD_1
v_mfma_f32_16x16x32_bf16 v[56:59],a[196:199],a[20:23],v[56:59]
ds_read_u16_d16_hi v26,v26
v_lshlrev_b32_sdwa v25,1,v25 dst_sel:DWORD dst_unused:UNUSED_PAD src0_sel:DWORD src1_sel:WORD_1
v_mfma_f32_16x16x32_bf16 v[56:59],a[192:195],a[4:7],v[56:59]
v_exp_f16_sdwa v8,v9 dst_sel:WORD_1 dst_unused:UNUSED_PRESERVE src0_sel:WORD_1
v_mfma_f32_16x16x32_bf16 v[232:235],a[228:231],v[48:51],v[232:235]
ds_read_u16 v25,v25
v_lshlrev_b32_sdwa v27,1,v27 dst_sel:DWORD dst_unused:UNUSED_PAD src0_sel:DWORD src1_sel:WORD_1
v_mfma_f32_16x16x32_bf16 v[236:239],a[228:231],v[44:47],v[236:239]
ds_read_u16_d16_hi v27,v27
v_lshlrev_b32_sdwa v60,1,v60 dst_sel:DWORD dst_unused:UNUSED_PAD src0_sel:DWORD src1_sel:WORD_1
v_mfma_f32_16x16x32_bf16 v[164:167],a[248:251],v[44:47],v[164:167]
ds_read_u16 v60,v60
v_pk_fma_f16 v8,v8,s55,s55
v_mfma_f32_16x16x32_bf16 v[160:163],a[248:251],v[48:51],v[160:163]
v_rcp_f16 v9,v8
v_lshlrev_b32_sdwa v62,1,v62 dst_sel:DWORD dst_unused:UNUSED_PAD src0_sel:DWORD src1_sel:WORD_1
v_mfma_f32_16x16x32_bf16 v[240:243],a[236:239],v[48:51],v[240:243]
ds_read_u16_d16_hi v62,v62
v_lshlrev_b32_sdwa v61,1,v61 dst_sel:DWORD dst_unused:UNUSED_PAD src0_sel:DWORD src1_sel:WORD_1
v_mfma_f32_16x16x32_bf16 v[244:247],a[236:239],v[44:47],v[244:247]
ds_read_u16 v61,v61
v_lshlrev_b32_sdwa v63,1,v63 dst_sel:DWORD dst_unused:UNUSED_PAD src0_sel:DWORD src1_sel:WORD_1
v_mfma_f32_16x16x32_bf16 v[180:183],a[252:255],v[44:47],v[180:183]
ds_read_u16_d16_hi v63,v63
v_lshlrev_b32_sdwa v32,1,v32 dst_sel:DWORD dst_unused:UNUSED_PAD src0_sel:DWORD src1_sel:WORD_1
v_mfma_f32_16x16x32_bf16 v[176:179],a[252:255],v[48:51],v[176:179]
s_waitcnt lgkmcnt(6)
v_rcp_f16_sdwa v9,v8 dst_sel:WORD_1 dst_unused:UNUSED_PRESERVE src0_sel:WORD_1
v_mfma_f32_16x16x32_bf16 v[248:251],a[240:243],v[48:51],v[248:251]
ds_read_u16 v32,v32
v_lshlrev_b32_sdwa v34,1,v34 dst_sel:DWORD dst_unused:UNUSED_PAD src0_sel:DWORD src1_sel:WORD_1
v_mfma_f32_16x16x32_bf16 v[252:255],a[240:243],v[44:47],v[252:255]
ds_read_u16_d16_hi v34,v34
v_fma_mix_f32 v33,v33,v9,0 op_sel_hi:[0,1,0]
v_mfma_f32_16x16x32_bf16 v[196:199],a[224:227],v[44:47],v[196:199]
v_fma_mix_f32 v35,v35,v9,0 op_sel:[0,1,0] op_sel_hi:[0,1,0]
v_mfma_f32_16x16x32_bf16 v[192:195],a[224:227],v[48:51],v[192:195]
v_mfma_f32_16x16x32_bf16 v[44:47],a[192:195],a[8:11],v[16:19]
v_mul_f32_dpp v232,v14,v232 row_newbcast:0 row_mask:0xf bank_mask:0xf
s_add_u32 s62,s61,1572864
v_mul_f32_dpp v233,v14,v233 row_newbcast:1 row_mask:0xf bank_mask:0xf
v_mul_f32_dpp v234,v14,v234 row_newbcast:2 row_mask:0xf bank_mask:0xf
v_lshlrev_b32_sdwa v36,1,v36 dst_sel:DWORD dst_unused:UNUSED_PAD src0_sel:DWORD src1_sel:WORD_1
v_mfma_f32_16x16x32_bf16 v[44:47],a[196:199],a[24:27],v[44:47]
ds_read_u16 v36,v36
v_mul_f32_dpp v235,v14,v235 row_newbcast:3 row_mask:0xf bank_mask:0xf
v_mul_f32_dpp v160,v14,v160 row_newbcast:4 row_mask:0xf bank_mask:0xf
v_fma_mixlo_f16 v8,v53,s99,0
v_mul_f32_dpp v161,v14,v161 row_newbcast:5 row_mask:0xf bank_mask:0xf
v_mfma_f32_16x16x32_bf16 v[44:47],a[200:203],a[40:43],v[44:47]
v_lshl_add_u64 v[28:29],v[28:29],0,v[30:31]
v_mul_f32_dpp v162,v14,v162 row_newbcast:6 row_mask:0xf bank_mask:0xf
v_mul_f32_dpp v163,v14,v163 row_newbcast:7 row_mask:0xf bank_mask:0xf
v_lshlrev_b32_sdwa v38,1,v38 dst_sel:DWORD dst_unused:UNUSED_PAD src0_sel:DWORD src1_sel:WORD_1
v_cvt_pk_bf16_f32 v232,v232,v233
v_mfma_f32_16x16x32_bf16 v[48:51],a[204:207],a[40:43],v[20:23]
s_waitcnt lgkmcnt(3)
ds_read_u16_d16_hi v38,v38
v_lshl_add_u64 v[30:31],v[24:25],0,v[26:27]
v_cvt_pk_bf16_f32 v233,v234,v235
v_cvt_pk_bf16_f32 v234,v160,v161
v_fma_mixhi_f16 v8,v55,s99,0
v_cvt_pk_bf16_f32 v235,v162,v163
v_mfma_f32_16x16x32_bf16 v[48:51],a[208:211],a[24:27],v[48:51]
buffer_store_dwordx4 v[232:235],v7,s[40:43],s62 offen offset:0 sc1
v_lshl_add_u64 v[24:25],v[60:61],0,v[62:63]
v_mul_f32_dpp v240,v14,v240 row_newbcast:8 row_mask:0xf bank_mask:0xf
v_mul_f32_dpp v241,v14,v241 row_newbcast:9 row_mask:0xf bank_mask:0xf
v_lshlrev_b32_sdwa v37,1,v37 dst_sel:DWORD dst_unused:UNUSED_PAD src0_sel:DWORD src1_sel:WORD_1
v_pk_fma_f16 v9,v8,v8,s54
v_mul_f32_dpp v242,v14,v242 row_newbcast:10 row_mask:0xf bank_mask:0xf
v_mfma_f32_16x16x32_bf16 v[48:51],a[212:215],a[8:11],v[48:51]
ds_read_u16 v37,v37
v_mul_f32_dpp v243,v14,v243 row_newbcast:11 row_mask:0xf bank_mask:0xf
v_mul_f32_dpp v176,v14,v176 row_newbcast:12 row_mask:0xf bank_mask:0xf
v_lshlrev_b32_sdwa v39,1,v39 dst_sel:DWORD dst_unused:UNUSED_PAD src0_sel:DWORD src1_sel:WORD_1
v_mul_f32_dpp v177,v14,v177 row_newbcast:13 row_mask:0xf bank_mask:0xf
v_mfma_f32_16x16x32_bf16 v[60:63],a[212:215],a[12:15],v[20:23]
s_waitcnt lgkmcnt(3)
ds_read_b128 v[20:23],v4 offset:672
v_or_b32 v26,v32,v34
v_mul_f32_dpp v178,v14,v178 row_newbcast:14 row_mask:0xf bank_mask:0xf
v_mul_f32_dpp v179,v14,v179 row_newbcast:15 row_mask:0xf bank_mask:0xf
v_pk_mul_f16 v9,v8,v9
v_cvt_pk_bf16_f32 v240,v240,v241
v_mfma_f32_16x16x32_bf16 v[60:63],a[208:211],a[28:31],v[60:63]
ds_read_u16_d16_hi v39,v39
v_cvt_pk_bf16_f32 v27,v33,v35
v_cvt_pk_bf16_f32 v241,v242,v243
v_cvt_pk_bf16_f32 v242,v176,v177
v_lshlrev_b32_sdwa v40,1,v40 dst_sel:DWORD dst_unused:UNUSED_PAD src0_sel:DWORD src1_sel:WORD_1
v_cvt_pk_bf16_f32 v243,v178,v179
v_mfma_f32_16x16x32_bf16 v[60:63],a[204:207],a[44:47],v[60:63]
ds_read_b64_tr_b16 a[212:213],v1 offset:4096
v_exp_f16 v8,v9
v_mul_f32_dpp v248,v15,v248 row_newbcast:0 row_mask:0xf bank_mask:0xf
v_mul_f32_dpp v249,v15,v249 row_newbcast:1 row_mask:0xf bank_mask:0xf
v_mul_f32_dpp v250,v15,v250 row_newbcast:2 row_mask:0xf bank_mask:0xf
v_mfma_f32_16x16x32_bf16 v[32:35],a[200:203],a[44:47],v[16:19]
ds_read_b128 v[16:19],v4 offset:640
ds_read_b64_tr_b16 a[214:215],v1 offset:5120
v_mul_f32_dpp v251,v15,v251 row_newbcast:3 row_mask:0xf bank_mask:0xf
v_mul_f32_dpp v192,v15,v192 row_newbcast:4 row_mask:0xf bank_mask:0xf
v_lshlrev_b32_sdwa v42,1,v42 dst_sel:DWORD dst_unused:UNUSED_PAD src0_sel:DWORD src1_sel:WORD_1
v_mul_f32_dpp v193,v15,v193 row_newbcast:5 row_mask:0xf bank_mask:0xf
v_mfma_f32_16x16x32_bf16 v[32:35],a[196:199],a[28:31],v[32:35]
ds_read_b64_tr_b16 a[208:209],v2 offset:4096
ds_read_b64_tr_b16 a[210:211],v2 offset:5120
v_mul_f32_dpp v194,v15,v194 row_newbcast:6 row_mask:0xf bank_mask:0xf
v_mul_f32_dpp v195,v15,v195 row_newbcast:7 row_mask:0xf bank_mask:0xf
v_lshlrev_b32_sdwa v41,1,v41 dst_sel:DWORD dst_unused:UNUSED_PAD src0_sel:DWORD src1_sel:WORD_1
v_cvt_pk_bf16_f32 v248,v248,v249
v_mfma_f32_16x16x32_bf16 v[32:35],a[192:195],a[12:15],v[32:35]
ds_read_u16 v40,v40
v_exp_f16_sdwa v8,v9 dst_sel:WORD_1 dst_unused:UNUSED_PRESERVE src0_sel:WORD_1
v_cvt_pk_bf16_f32 v249,v250,v251
v_cvt_pk_bf16_f32 v250,v192,v193
v_cvt_pk_bf16_f32 v251,v194,v195
v_mfma_f32_16x16x32_bf16 v[208:211],a[228:231],v[28:31],v[208:211]
ds_read_u16_d16_hi v42,v42
v_mul_f32_dpp v236,v14,v236 row_newbcast:0 row_mask:0xf bank_mask:0xf
v_mul_f32_dpp v237,v14,v237 row_newbcast:1 row_mask:0xf bank_mask:0xf
v_lshlrev_b32_sdwa v43,1,v43 dst_sel:DWORD dst_unused:UNUSED_PAD src0_sel:DWORD src1_sel:WORD_1
v_mul_f32_dpp v238,v14,v238 row_newbcast:2 row_mask:0xf bank_mask:0xf
v_mfma_f32_16x16x32_bf16 v[212:215],a[228:231],v[24:27],v[212:215]
ds_read_u16 v41,v41
v_mul_f32_dpp v239,v14,v239 row_newbcast:3 row_mask:0xf bank_mask:0xf
v_mul_f32_dpp v164,v14,v164 row_newbcast:4 row_mask:0xf bank_mask:0xf
v_lshlrev_b32_sdwa v56,1,v56 dst_sel:DWORD dst_unused:UNUSED_PAD src0_sel:DWORD src1_sel:WORD_1
v_mul_f32_dpp v165,v14,v165 row_newbcast:5 row_mask:0xf bank_mask:0xf
v_mfma_f32_16x16x32_bf16 v[172:175],a[248:251],v[24:27],v[172:175]
ds_read_u16_d16_hi v43,v43
v_mul_f32_dpp v166,v14,v166 row_newbcast:6 row_mask:0xf bank_mask:0xf
v_mul_f32_dpp v167,v14,v167 row_newbcast:7 row_mask:0xf bank_mask:0xf
v_pk_fma_f16 v8,v8,s55,s55
v_cvt_pk_bf16_f32 v236,v236,v237
v_mfma_f32_16x16x32_bf16 v[168:171],a[248:251],v[28:31],v[168:171]
v_rcp_f16 v9,v8
v_cvt_pk_bf16_f32 v237,v238,v239
v_cvt_pk_bf16_f32 v238,v164,v165
v_lshlrev_b32_sdwa v58,1,v58 dst_sel:DWORD dst_unused:UNUSED_PAD src0_sel:DWORD src1_sel:WORD_1
v_cvt_pk_bf16_f32 v239,v166,v167
v_mfma_f32_16x16x32_bf16 v[216:219],a[236:239],v[28:31],v[216:219]
buffer_store_dwordx4 v[240:243],v7,s[40:43],s62 offen offset:64 sc1
ds_read_b64_tr_b16 a[204:205],v2 offset:2048
v_mul_f32_dpp v244,v14,v244 row_newbcast:8 row_mask:0xf bank_mask:0xf
v_mul_f32_dpp v245,v14,v245 row_newbcast:9 row_mask:0xf bank_mask:0xf
v_mul_f32_dpp v246,v14,v246 row_newbcast:10 row_mask:0xf bank_mask:0xf
v_mfma_f32_16x16x32_bf16 v[220:223],a[236:239],v[24:27],v[220:223]
ds_read_b64_tr_b16 a[206:207],v2 offset:3072
v_mul_f32_dpp v247,v14,v247 row_newbcast:11 row_mask:0xf bank_mask:0xf
v_mul_f32_dpp v180,v14,v180 row_newbcast:12 row_mask:0xf bank_mask:0xf
v_lshlrev_b32_sdwa v57,1,v57 dst_sel:DWORD dst_unused:UNUSED_PAD src0_sel:DWORD src1_sel:WORD_1
v_mul_f32_dpp v181,v14,v181 row_newbcast:13 row_mask:0xf bank_mask:0xf
v_mfma_f32_16x16x32_bf16 v[188:191],a[252:255],v[24:27],v[188:191]
ds_read_u16 v56,v56
v_mul_f32_dpp v182,v14,v182 row_newbcast:14 row_mask:0xf bank_mask:0xf
v_mul_f32_dpp v183,v14,v183 row_newbcast:15 row_mask:0xf bank_mask:0xf
v_lshlrev_b32_sdwa v59,1,v59 dst_sel:DWORD dst_unused:UNUSED_PAD src0_sel:DWORD src1_sel:WORD_1
v_cvt_pk_bf16_f32 v244,v244,v245
v_mfma_f32_16x16x32_bf16 v[184:187],a[252:255],v[28:31],v[184:187]
v_rcp_f16_sdwa v9,v8 dst_sel:WORD_1 dst_unused:UNUSED_PRESERVE src0_sel:WORD_1
v_cvt_pk_bf16_f32 v245,v246,v247
v_cvt_pk_bf16_f32 v246,v180,v181
v_cvt_pk_bf16_f32 v247,v182,v183
v_mfma_f32_16x16x32_bf16 v[224:227],a[240:243],v[28:31],v[224:227]
ds_read_u16_d16_hi v58,v58
ds_read_u16 v57,v57
v_mul_f32_dpp v252,v15,v252 row_newbcast:0 row_mask:0xf bank_mask:0xf
v_mul_f32_dpp v253,v15,v253 row_newbcast:1 row_mask:0xf bank_mask:0xf
v_lshlrev_b32_sdwa v52,1,v52 dst_sel:DWORD dst_unused:UNUSED_PAD src0_sel:DWORD src1_sel:WORD_1
v_mul_f32_dpp v254,v15,v254 row_newbcast:2 row_mask:0xf bank_mask:0xf
v_mfma_f32_16x16x32_bf16 v[228:231],a[240:243],v[24:27],v[228:231]
ds_read_u16_d16_hi v59,v59
ds_read_b64_tr_b16 a[200:201],v2 offset:0
v_mul_f32_dpp v255,v15,v255 row_newbcast:3 row_mask:0xf bank_mask:0xf
v_mul_f32_dpp v196,v15,v196 row_newbcast:4 row_mask:0xf bank_mask:0xf
v_mul_f32_dpp v197,v15,v197 row_newbcast:5 row_mask:0xf bank_mask:0xf
v_lshlrev_b32_sdwa v54,1,v54 dst_sel:DWORD dst_unused:UNUSED_PAD src0_sel:DWORD src1_sel:WORD_1
v_mfma_f32_16x16x32_bf16 v[204:207],a[224:227],v[24:27],v[204:207]
ds_read_b64_tr_b16 a[202:203],v2 offset:1024
v_mul_f32_dpp v198,v15,v198 row_newbcast:6 row_mask:0xf bank_mask:0xf
v_mul_f32_dpp v199,v15,v199 row_newbcast:7 row_mask:0xf bank_mask:0xf
v_fma_mix_f32 v53,v53,v9,0 op_sel_hi:[0,1,0]
v_cvt_pk_bf16_f32 v252,v252,v253
v_mfma_f32_16x16x32_bf16 v[200:203],a[224:227],v[28:31],v[200:203]
s_waitcnt lgkmcnt(8)
ds_read_u16 v52,v52
ds_read_u16_d16_hi v54,v54
v_cvt_pk_bf16_f32 v253,v254,v255
v_cvt_pk_bf16_f32 v254,v196,v197
v_cvt_pk_bf16_f32 v255,v198,v199
v_fma_mix_f32 v55,v55,v9,0 op_sel:[0,1,0] op_sel_hi:[0,1,0]
v_mfma_f32_16x16x32_bf16 v[24:27],a[216:219],a[0:3],v[16:19]
v_mul_f32_dpp v208,v14,v208 row_newbcast:0 row_mask:0xf bank_mask:0xf
s_add_u32 s63,s61,1671168
v_accvgpr_write_b32 a64,0
v_accvgpr_write_b32 a65,0
v_lshlrev_b32_sdwa v44,1,v44 dst_sel:DWORD dst_unused:UNUSED_PAD src0_sel:DWORD src1_sel:WORD_1
v_mfma_f32_16x16x32_bf16 v[24:27],a[220:223],a[16:19],v[24:27]
ds_read_u16 v44,v44
v_accvgpr_write_b32 a66,0
v_accvgpr_write_b32 a67,0
v_fma_mixlo_f16 v8,v61,s99,0
v_mul_f32_dpp v209,v14,v209 row_newbcast:1 row_mask:0xf bank_mask:0xf
v_mfma_f32_16x16x32_bf16 v[24:27],a[212:215],a[32:35],v[24:27]
buffer_store_dwordx4 v[248:251],v7,s[40:43],s62 offen offset:128 sc1
v_lshl_add_u64 v[36:37],v[36:37],0,v[38:39]
v_mul_f32_dpp v210,v14,v210 row_newbcast:2 row_mask:0xf bank_mask:0xf
v_mul_f32_dpp v211,v14,v211 row_newbcast:3 row_mask:0xf bank_mask:0xf
v_lshlrev_b32_sdwa v46,1,v46 dst_sel:DWORD dst_unused:UNUSED_PAD src0_sel:DWORD src1_sel:WORD_1
v_mul_f32_dpp v168,v14,v168 row_newbcast:4 row_mask:0xf bank_mask:0xf
v_mfma_f32_16x16x32_bf16 v[28:31],a[208:211],a[32:35],v[20:23]
s_waitcnt lgkmcnt(8)
ds_read_u16_d16_hi v46,v46
v_lshl_add_u64 v[38:39],v[40:41],0,v[42:43]
v_mul_f32_dpp v169,v14,v169 row_newbcast:5 row_mask:0xf bank_mask:0xf
v_mul_f32_dpp v170,v14,v170 row_newbcast:6 row_mask:0xf bank_mask:0xf
v_fma_mixhi_f16 v8,v63,s99,0
v_mul_f32_dpp v171,v14,v171 row_newbcast:7 row_mask:0xf bank_mask:0xf
v_mfma_f32_16x16x32_bf16 v[28:31],a[204:207],a[16:19],v[28:31]
s_waitcnt lgkmcnt(4)
v_lshl_add_u64 v[40:41],v[56:57],0,v[58:59]
v_cvt_pk_bf16_f32 v208,v208,v209
v_cvt_pk_bf16_f32 v209,v210,v211
v_lshlrev_b32_sdwa v45,1,v45 dst_sel:DWORD dst_unused:UNUSED_PAD src0_sel:DWORD src1_sel:WORD_1
v_pk_fma_f16 v9,v8,v8,s54
v_cvt_pk_bf16_f32 v210,v168,v169
v_mfma_f32_16x16x32_bf16 v[28:31],a[200:203],a[0:3],v[28:31]
ds_read_u16 v45,v45
v_cvt_pk_bf16_f32 v211,v170,v171
v_mul_f32_dpp v216,v14,v216 row_newbcast:8 row_mask:0xf bank_mask:0xf
v_lshlrev_b32_sdwa v47,1,v47 dst_sel:DWORD dst_unused:UNUSED_PAD src0_sel:DWORD src1_sel:WORD_1
v_accvgpr_write_b32 a72,v208
v_mfma_f32_16x16x32_bf16 v[56:59],a[200:203],a[4:7],v[20:23]
s_waitcnt lgkmcnt(3)
ds_read_u16_d16_hi v47,v47
v_or_b32 v42,v52,v54
v_mul_f32_dpp v217,v14,v217 row_newbcast:9 row_mask:0xf bank_mask:0xf
v_mul_f32_dpp v218,v14,v218 row_newbcast:10 row_mask:0xf bank_mask:0xf
v_pk_mul_f16 v9,v8,v9
v_accvgpr_write_b32 a73,v209
v_mfma_f32_16x16x32_bf16 v[56:59],a[204:207],a[20:23],v[56:59]
s_waitcnt vmcnt(6)
v_cvt_pk_bf16_f32 v43,v53,v55
v_mul_f32_dpp v219,v14,v219 row_newbcast:11 row_mask:0xf bank_mask:0xf
v_mul_f32_dpp v184,v14,v184 row_newbcast:12 row_mask:0xf bank_mask:0xf
v_lshlrev_b32_sdwa v48,1,v48 dst_sel:DWORD dst_unused:UNUSED_PAD src0_sel:DWORD src1_sel:WORD_1
v_accvgpr_write_b32 a74,v210
v_mfma_f32_16x16x32_bf16 v[56:59],a[208:211],a[36:39],v[56:59]
s_barrier
v_exp_f16 v8,v9
v_mul_f32_dpp v185,v14,v185 row_newbcast:13 row_mask:0xf bank_mask:0xf
v_mul_f32_dpp v186,v14,v186 row_newbcast:14 row_mask:0xf bank_mask:0xf
v_accvgpr_write_b32 a75,v211
v_mfma_f32_16x16x32_bf16 v[52:55],a[212:215],a[36:39],v[16:19]
ds_read_u16 v48,v48
v_mul_f32_dpp v187,v14,v187 row_newbcast:15 row_mask:0xf bank_mask:0xf
v_cvt_pk_bf16_f32 v216,v216,v217
v_lshlrev_b32_sdwa v50,1,v50 dst_sel:DWORD dst_unused:UNUSED_PAD src0_sel:DWORD src1_sel:WORD_1
v_cvt_pk_bf16_f32 v217,v218,v219
v_mfma_f32_16x16x32_bf16 v[52:55],a[220:223],a[20:23],v[52:55]
s_add_u32 m0,s58,0
ds_read_u16_d16_hi v50,v50
v_cvt_pk_bf16_f32 v218,v184,v185
v_cvt_pk_bf16_f32 v219,v186,v187
v_lshlrev_b32_sdwa v49,1,v49 dst_sel:DWORD dst_unused:UNUSED_PAD src0_sel:DWORD src1_sel:WORD_1
v_mul_f32_dpp v224,v15,v224 row_newbcast:0 row_mask:0xf bank_mask:0xf
v_mfma_f32_16x16x32_bf16 v[52:55],a[216:219],a[4:7],v[52:55]
s_add_u32 s69,s69,s59
v_exp_f16_sdwa v8,v9 dst_sel:WORD_1 dst_unused:UNUSED_PRESERVE src0_sel:WORD_1
v_accvgpr_write_b32 a76,v216
v_mul_f32_dpp v225,v15,v225 row_newbcast:1 row_mask:0xf bank_mask:0xf
v_mul_f32_dpp v226,v15,v226 row_newbcast:2 row_mask:0xf bank_mask:0xf
v_mfma_f32_16x16x32_bf16 v[136:139],a[228:231],v[36:39],v[136:139]
buffer_load_dwordx4 v5,s[44:47],s69 offen offset:0 lds
v_accvgpr_write_b32 a77,v217
v_mul_f32_dpp v227,v15,v227 row_newbcast:3 row_mask:0xf bank_mask:0xf
v_lshlrev_b32_sdwa v51,1,v51 dst_sel:DWORD dst_unused:UNUSED_PAD src0_sel:DWORD src1_sel:WORD_1
v_mul_f32_dpp v200,v15,v200 row_newbcast:4 row_mask:0xf bank_mask:0xf
v_mfma_f32_16x16x32_bf16 v[140:143],a[228:231],v[40:43],v[140:143]
s_add_u32 s69,s69,s53
ds_read_u16 v49,v49
v_accvgpr_write_b32 a78,v218
v_mul_f32_dpp v201,v15,v201 row_newbcast:5 row_mask:0xf bank_mask:0xf
v_lshlrev_b32_sdwa v32,1,v32 dst_sel:DWORD dst_unused:UNUSED_PAD src0_sel:DWORD src1_sel:WORD_1
v_mul_f32_dpp v202,v15,v202 row_newbcast:6 row_mask:0xf bank_mask:0xf
v_mfma_f32_16x16x32_bf16 v[68:71],a[248:251],v[40:43],v[68:71]
buffer_load_dwordx4 v5,s[44:47],s69 offen offset:1024 lds
ds_read_u16_d16_hi v51,v51
v_accvgpr_write_b32 a79,v219
v_mul_f32_dpp v203,v15,v203 row_newbcast:7 row_mask:0xf bank_mask:0xf
v_pk_fma_f16 v8,v8,s55,s55
v_cvt_pk_bf16_f32 v224,v224,v225
v_mfma_f32_16x16x32_bf16 v[64:67],a[248:251],v[36:39],v[64:67]
v_rcp_f16 v9,v8
v_cvt_pk_bf16_f32 v225,v226,v227
v_cvt_pk_bf16_f32 v226,v200,v201
v_lshlrev_b32_sdwa v34,1,v34 dst_sel:DWORD dst_unused:UNUSED_PAD src0_sel:DWORD src1_sel:WORD_1
v_cvt_pk_bf16_f32 v227,v202,v203
v_mfma_f32_16x16x32_bf16 v[144:147],a[236:239],v[36:39],v[144:147]
buffer_store_dwordx4 v[236:239],v7,s[40:43],s62 offen offset:192 sc1
ds_read_u16 v32,v32
v_mul_f32_dpp v212,v14,v212 row_newbcast:0 row_mask:0xf bank_mask:0xf
v_accvgpr_write_b32 a80,v224
v_mul_f32_dpp v213,v14,v213 row_newbcast:1 row_mask:0xf bank_mask:0xf
v_mfma_f32_16x16x32_bf16 v[148:151],a[236:239],v[40:43],v[148:151]
s_add_u32 s69,s69,s53
ds_read_u16_d16_hi v34,v34
v_mul_f32_dpp v214,v14,v214 row_newbcast:2 row_mask:0xf bank_mask:0xf
v_accvgpr_write_b32 a81,v225
v_lshlrev_b32_sdwa v33,1,v33 dst_sel:DWORD dst_unused:UNUSED_PAD src0_sel:DWORD src1_sel:WORD_1
v_mul_f32_dpp v215,v14,v215 row_newbcast:3 row_mask:0xf bank_mask:0xf
v_mfma_f32_16x16x32_bf16 v[84:87],a[252:255],v[40:43],v[84:87]
buffer_load_dwordx4 v5,s[44:47],s69 offen offset:2048 lds
v_mul_f32_dpp v172,v14,v172 row_newbcast:4 row_mask:0xf bank_mask:0xf
v_accvgpr_write_b32 a82,v226
v_lshlrev_b32_sdwa v35,1,v35 dst_sel:DWORD dst_unused:UNUSED_PAD src0_sel:DWORD src1_sel:WORD_1
v_mul_f32_dpp v173,v14,v173 row_newbcast:5 row_mask:0xf bank_mask:0xf
v_mfma_f32_16x16x32_bf16 v[80:83],a[252:255],v[36:39],v[80:83]
ds_read_u16 v33,v33
v_rcp_f16_sdwa v9,v8 dst_sel:WORD_1 dst_unused:UNUSED_PRESERVE src0_sel:WORD_1
v_mul_f32_dpp v174,v14,v174 row_newbcast:6 row_mask:0xf bank_mask:0xf
v_accvgpr_write_b32 a83,v227
v_mul_f32_dpp v175,v14,v175 row_newbcast:7 row_mask:0xf bank_mask:0xf
v_mfma_f32_16x16x32_bf16 v[152:155],a[240:243],v[36:39],v[152:155]
ds_read_u16_d16_hi v35,v35
v_cvt_pk_bf16_f32 v212,v212,v213
v_cvt_pk_bf16_f32 v213,v214,v215
v_lshlrev_b32_sdwa v60,1,v60 dst_sel:DWORD dst_unused:UNUSED_PAD src0_sel:DWORD src1_sel:WORD_1
v_cvt_pk_bf16_f32 v214,v172,v173
v_mfma_f32_16x16x32_bf16 v[156:159],a[240:243],v[40:43],v[156:159]
ds_read_u16 v60,v60
v_cvt_pk_bf16_f32 v215,v174,v175
v_mfma_f32_32x32x16_bf16 v[160:175],a[64:67],a[64:67],0
v_mul_f32_dpp v220,v14,v220 row_newbcast:8 row_mask:0xf bank_mask:0xf
v_lshlrev_b32_sdwa v62,1,v62 dst_sel:DWORD dst_unused:UNUSED_PAD src0_sel:DWORD src1_sel:WORD_1
v_fma_mix_f32 v61,v61,v9,0 op_sel_hi:[0,1,0]
v_accvgpr_write_b32 a84,v212
v_fma_mix_f32 v63,v63,v9,0 op_sel:[0,1,0] op_sel_hi:[0,1,0]
v_mfma_f32_16x16x32_bf16 v[100:103],a[224:227],v[40:43],v[100:103]
ds_read_u16_d16_hi v62,v62
v_mul_f32_dpp v221,v14,v221 row_newbcast:9 row_mask:0xf bank_mask:0xf
v_mul_f32_dpp v222,v14,v222 row_newbcast:10 row_mask:0xf bank_mask:0xf
v_mfma_f32_16x16x32_bf16 v[96:99],a[224:227],v[36:39],v[96:99]
ds_read_b64_tr_b16 a[232:233],v3 offset:6160
v_accvgpr_write_b32 a85,v213
v_mul_f32_dpp v223,v14,v223 row_newbcast:11 row_mask:0xf bank_mask:0xf
v_mul_f32_dpp v188,v14,v188 row_newbcast:12 row_mask:0xf bank_mask:0xf
ds_read_b64_tr_b16 a[234:235],v3 offset:7696
ds_read_b64_tr_b16 a[244:245],v3 offset:6168
ds_read_b64_tr_b16 a[246:247],v3 offset:7704
v_add_u32_sdwa v36,s60,v10 dst_sel:DWORD dst_unused:UNUSED_PAD src0_sel:DWORD src1_sel:WORD_1
v_subrev_u32 v8,88400,v36
v_min_u32 v36,v36,v8
v_add_u32 v37,7072,v36
v_subrev_u32 v8,88400,v37
v_min_u32 v37,v37,v8
s_waitcnt lgkmcnt(14)
v_lshl_add_u64 v[44:45],v[44:45],0,v[46:47]
s_waitcnt lgkmcnt(10)
v_lshl_add_u64 v[46:47],v[48:49],0,v[50:51]
s_waitcnt lgkmcnt(6)
v_lshl_add_u64 v[48:49],v[32:33],0,v[34:35]
s_waitcnt lgkmcnt(4)
v_or_b32 v50,v60,v62
v_cvt_pk_bf16_f32 v51,v61,v63
v_mfma_f32_16x16x32_bf16 v[112:115],a[228:231],v[44:47],v[112:115]
buffer_store_dwordx4 v[244:247],v7,s[40:43],s62 offen offset:256 sc1
v_xad_u32 v40,v11,0,v36
v_xad_u32 v41,v11,8,v37
v_xad_u32 v42,v11,16,v36
v_xad_u32 v43,v11,24,v37
v_mfma_f32_16x16x32_bf16 v[116:119],a[228:231],v[48:51],v[116:119]
ds_read_b64 a[48:49],v40 offset:0
v_accvgpr_write_b32 a86,v214
v_mul_f32_dpp v189,v14,v189 row_newbcast:13 row_mask:0xf bank_mask:0xf
v_mul_f32_dpp v190,v14,v190 row_newbcast:14 row_mask:0xf bank_mask:0xf
v_accvgpr_write_b32 a87,v215
v_mfma_f32_16x16x32_bf16 v[76:79],a[248:251],v[48:51],v[76:79]
TE10_%=:
s_cbranch_execz TP9784_%=
ds_read_b64 a[50:51],v40 offset:192
v_mul_f32_dpp v191,v14,v191 row_newbcast:15 row_mask:0xf bank_mask:0xf
v_cvt_pk_bf16_f32 v220,v220,v221
v_cvt_pk_bf16_f32 v221,v222,v223
v_mfma_f32_16x16x32_bf16 v[72:75],a[248:251],v[44:47],v[72:75]
ds_read_b64 a[52:53],v41 offset:0
v_cvt_pk_bf16_f32 v222,v188,v189
v_cvt_pk_bf16_f32 v223,v190,v191
v_mfma_f32_32x32x16_bf16 v[176:191],a[64:67],a[64:67],0
v_mfma_f32_16x16x32_bf16 v[120:123],a[236:239],v[44:47],v[120:123]
ds_read_b64 a[54:55],v41 offset:192
v_mul_f32_dpp v228,v15,v228 row_newbcast:0 row_mask:0xf bank_mask:0xf
v_accvgpr_write_b32 a88,v220
v_mul_f32_dpp v229,v15,v229 row_newbcast:1 row_mask:0xf bank_mask:0xf
v_mfma_f32_16x16x32_bf16 v[124:127],a[236:239],v[48:51],v[124:127]
ds_read_b64 a[56:57],v40 offset:8
v_mul_f32_dpp v230,v15,v230 row_newbcast:2 row_mask:0xf bank_mask:0xf
v_accvgpr_write_b32 a89,v221
v_mul_f32_dpp v231,v15,v231 row_newbcast:3 row_mask:0xf bank_mask:0xf
v_mfma_f32_16x16x32_bf16 v[92:95],a[252:255],v[48:51],v[92:95]
ds_read_b64 a[58:59],v40 offset:200
v_mul_f32_dpp v204,v15,v204 row_newbcast:4 row_mask:0xf bank_mask:0xf
v_accvgpr_write_b32 a90,v222
v_mul_f32_dpp v205,v15,v205 row_newbcast:5 row_mask:0xf bank_mask:0xf
v_mfma_f32_16x16x32_bf16 v[88:91],a[252:255],v[44:47],v[88:91]
ds_read_b64 a[60:61],v41 offset:8
v_mul_f32_dpp v206,v15,v206 row_newbcast:6 row_mask:0xf bank_mask:0xf
v_accvgpr_write_b32 a91,v223
v_mul_f32_dpp v207,v15,v207 row_newbcast:7 row_mask:0xf bank_mask:0xf
v_mfma_f32_16x16x32_bf16 v[128:131],a[240:243],v[44:47],v[128:131]
ds_read_b64 a[62:63],v41 offset:200
v_cvt_pk_bf16_f32 v228,v228,v229
v_cvt_pk_bf16_f32 v229,v230,v231
v_cvt_pk_bf16_f32 v230,v204,v205
v_mfma_f32_16x16x32_bf16 v[132:135],a[240:243],v[48:51],v[132:135]
v_cvt_pk_bf16_f32 v231,v206,v207
v_mfma_f32_32x32x16_bf16 v[192:207],a[64:67],a[64:67],0
v_accvgpr_write_b32 a92,v228
v_mfma_f32_16x16x32_bf16 v[108:111],a[224:227],v[48:51],v[108:111]
v_accvgpr_write_b32 a93,v229
v_accvgpr_write_b32 a94,v230
v_accvgpr_write_b32 a95,v231
v_mfma_f32_16x16x32_bf16 v[104:107],a[224:227],v[44:47],v[104:107]
buffer_store_dwordx4 v[252:255],v7,s[40:43],s62 offen offset:320 sc1
v_mfma_f32_32x32x16_bf16 v[208:223],a[64:67],a[64:67],0
v_mov_b64 v[32:33],0
v_mov_b64 v[34:35],0
ds_read_b64 a[64:65],v42 offset:0
ds_read_b64 a[66:67],v42 offset:192
ds_read_b64 a[68:69],v43 offset:0
ds_read_b64 a[70:71],v43 offset:192
s_waitcnt lgkmcnt(8)
v_smfmac_f32_16x16x64_bf16 v[160:163],a[96:99],a[48:55],v10
s_waitcnt lgkmcnt(4)
ds_read_b64 a[48:49],v42 offset:8
ds_read_b64 a[50:51],v42 offset:200
ds_read_b64 a[52:53],v43 offset:8
ds_read_b64 a[54:55],v43 offset:200
v_smfmac_f32_16x16x64_bf16 v[164:167],a[100:103],a[56:63],v10
s_waitcnt lgkmcnt(4)
ds_read_b64 a[56:57],v40 offset:64
ds_read_b64 a[58:59],v40 offset:256
ds_read_b64 a[60:61],v41 offset:64
ds_read_b64 a[62:63],v41 offset:256
v_smfmac_f32_16x16x64_bf16 v[168:171],a[104:107],a[64:71],v10
s_waitcnt lgkmcnt(4)
ds_read_b64 a[64:65],v40 offset:72
ds_read_b64 a[66:67],v40 offset:264
ds_read_b64 a[68:69],v41 offset:72
ds_read_b64 a[70:71],v41 offset:264
v_and_b32 v44,8,v11
v_pk_mul_f32 v[48:49],v[160:161],v[160:161]
v_pk_mul_f32 v[50:51],v[162:163],v[162:163]
v_smfmac_f32_16x16x64_bf16 v[172:175],a[108:111],a[48:55],v10
s_waitcnt lgkmcnt(4)
ds_read_b64 a[48:49],v42 offset:64
ds_read_b64 a[50:51],v42 offset:256
ds_read_b64 a[52:53],v43 offset:64
ds_read_b64 a[54:55],v43 offset:256
v_mul_u32_u24 v44,884,v44
s_add_u32 s64,s60,3536
v_pk_add_f32 v[60:61],v[160:161],v[164:165]
v_pk_fma_f32 v[48:49],v[164:165],v[164:165],v[48:49]
v_pk_add_f32 v[62:63],v[162:163],v[166:167]
v_pk_fma_f32 v[50:51],v[166:167],v[166:167],v[50:51]
v_smfmac_f32_16x16x64_bf16 v[176:179],a[128:131],a[56:63],v10
s_waitcnt lgkmcnt(4)
ds_read_b64 a[56:57],v42 offset:72
ds_read_b64 a[58:59],v42 offset:264
ds_read_b64 a[60:61],v43 offset:72
ds_read_b64 a[62:63],v43 offset:264
v_add_u32 v47,s64,v44
v_pk_add_f32 v[60:61],v[60:61],v[168:169]
v_pk_fma_f32 v[48:49],v[168:169],v[168:169],v[48:49]
v_pk_add_f32 v[62:63],v[62:63],v[170:171]
v_pk_fma_f32 v[50:51],v[170:171],v[170:171],v[50:51]
v_smfmac_f32_16x16x64_bf16 v[180:183],a[132:135],a[64:71],v10
s_waitcnt lgkmcnt(4)
ds_read_b64 a[64:65],v40 offset:128
ds_read_b64 a[66:67],v40 offset:320
ds_read_b64 a[68:69],v41 offset:128
ds_read_b64 a[70:71],v41 offset:320
v_subrev_u32 v8,88400,v47
v_pk_add_f32 v[60:61],v[60:61],v[172:173]
v_pk_fma_f32 v[48:49],v[172:173],v[172:173],v[48:49]
v_pk_add_f32 v[62:63],v[62:63],v[174:175]
v_pk_fma_f32 v[50:51],v[174:175],v[174:175],v[50:51]
v_smfmac_f32_16x16x64_bf16 v[184:187],a[136:139],a[48:55],v10
s_waitcnt lgkmcnt(4)
ds_read_b64 a[48:49],v40 offset:136
ds_read_b64 a[50:51],v40 offset:328
ds_read_b64 a[52:53],v41 offset:136
ds_read_b64 a[54:55],v41 offset:328
v_min_u32 v47,v47,v8
v_pk_add_f32 v[60:61],v[60:61],v[176:177]
v_pk_fma_f32 v[48:49],v[176:177],v[176:177],v[48:49]
v_pk_add_f32 v[62:63],v[62:63],v[178:179]
v_pk_fma_f32 v[50:51],v[178:179],v[178:179],v[50:51]
v_smfmac_f32_16x16x64_bf16 v[188:191],a[140:143],a[56:63],v10
s_waitcnt lgkmcnt(4)
buffer_store_dwordx4 a[72:75],v7,s[40:43],s63 offen offset:0 sc1
ds_read_b64 a[56:57],v42 offset:128
ds_read_b64 a[58:59],v42 offset:320
ds_read_b64 a[60:61],v43 offset:128
ds_read_b64 a[62:63],v43 offset:320
v_xad_u32 v40,v11,32,v36
v_xad_u32 v41,v11,40,v37
v_pk_add_f32 v[60:61],v[60:61],v[180:181]
v_pk_fma_f32 v[48:49],v[180:181],v[180:181],v[48:49]
v_pk_add_f32 v[62:63],v[62:63],v[182:183]
v_pk_fma_f32 v[50:51],v[182:183],v[182:183],v[50:51]
v_smfmac_f32_16x16x64_bf16 v[192:195],a[160:163],a[64:71],v10
s_waitcnt lgkmcnt(4)
ds_read_b64 a[64:65],v42 offset:136
ds_read_b64 a[66:67],v42 offset:328
ds_read_b64 a[68:69],v43 offset:136
ds_read_b64 a[70:71],v43 offset:328
v_add_u32 v46,3536,v47
v_pk_add_f32 v[60:61],v[60:61],v[184:185]
v_pk_fma_f32 v[48:49],v[184:185],v[184:185],v[48:49]
v_pk_add_f32 v[62:63],v[62:63],v[186:187]
v_pk_fma_f32 v[50:51],v[186:187],v[186:187],v[50:51]
v_smfmac_f32_16x16x64_bf16 v[196:199],a[164:167],a[48:55],v10
s_waitcnt lgkmcnt(4)
ds_read_b64 a[48:49],v40 offset:0
ds_read_b64 a[50:51],v40 offset:192
ds_read_b64 a[52:53],v41 offset:0
ds_read_b64 a[54:55],v41 offset:192
v_xad_u32 v42,v11,48,v36
v_xad_u32 v43,v11,56,v37
v_pk_add_f32 v[60:61],v[60:61],v[188:189]
v_pk_fma_f32 v[48:49],v[188:189],v[188:189],v[48:49]
v_pk_add_f32 v[62:63],v[62:63],v[190:191]
v_pk_fma_f32 v[50:51],v[190:191],v[190:191],v[50:51]
v_smfmac_f32_16x16x64_bf16 v[200:203],a[168:171],a[56:63],v10
s_waitcnt lgkmcnt(4)
ds_read_b64 a[56:57],v40 offset:8
ds_read_b64 a[58:59],v40 offset:200
ds_read_b64 a[60:61],v41 offset:8
ds_read_b64 a[62:63],v41 offset:200
v_subrev_u32 v8,88400,v46
v_pk_add_f32 v[60:61],v[60:61],v[192:193]
v_pk_fma_f32 v[48:49],v[192:193],v[192:193],v[48:49]
v_pk_add_f32 v[62:63],v[62:63],v[194:195]
v_pk_fma_f32 v[50:51],v[194:195],v[194:195],v[50:51]
v_smfmac_f32_16x16x64_bf16 v[204:207],a[172:175],a[64:71],v10
s_waitcnt lgkmcnt(4)
ds_read_b64 a[64:65],v40 offset:64
ds_read_b64 a[66:67],v40 offset:256
ds_read_b64 a[68:69],v41 offset:64
ds_read_b64 a[70:71],v41 offset:256
v_min_u32 v46,v46,v8
v_pk_add_f32 v[60:61],v[60:61],v[196:197]
v_pk_fma_f32 v[48:49],v[196:197],v[196:197],v[48:49]
v_pk_add_f32 v[62:63],v[62:63],v[198:199]
v_pk_fma_f32 v[50:51],v[198:199],v[198:199],v[50:51]
v_smfmac_f32_16x16x64_bf16 v[208:211],a[112:115],a[48:55],v10
s_waitcnt lgkmcnt(4)
ds_read_b64 a[48:49],v40 offset:72
ds_read_b64 a[50:51],v40 offset:264
ds_read_b64 a[52:53],v41 offset:72
ds_read_b64 a[54:55],v41 offset:264
v_xor_b32 v9,0x80008,v12
v_pk_add_f32 v[60:61],v[60:61],v[200:201]
v_pk_fma_f32 v[48:49],v[200:201],v[200:201],v[48:49]
v_pk_add_f32 v[62:63],v[62:63],v[202:203]
v_pk_fma_f32 v[50:51],v[202:203],v[202:203],v[50:51]
v_mfma_f32_32x32x16_bf16 v[224:239],v[32:35],v[32:35],0
v_smfmac_f32_16x16x64_bf16 v[212:215],a[116:119],a[56:63],v10
s_waitcnt lgkmcnt(4)
buffer_store_dwordx4 a[76:79],v7,s[40:43],s63 offen offset:64 sc1
ds_read_b64 a[56:57],v40 offset:128
ds_read_b64 a[58:59],v40 offset:320
ds_read_b64 a[60:61],v41 offset:128
ds_read_b64 a[62:63],v41 offset:320
v_add_u32_sdwa v44,v12,v47 dst_sel:DWORD dst_unused:UNUSED_PAD src0_sel:WORD_0 src1_sel:DWORD
v_pk_add_f32 v[60:61],v[60:61],v[204:205]
v_pk_fma_f32 v[48:49],v[204:205],v[204:205],v[48:49]
v_pk_add_f32 v[62:63],v[62:63],v[206:207]
v_pk_fma_f32 v[50:51],v[206:207],v[206:207],v[50:51]
v_smfmac_f32_16x16x64_bf16 v[216:219],a[144:147],a[64:71],v10
s_waitcnt lgkmcnt(4)
ds_read_b64 a[64:65],v40 offset:136
ds_read_b64 a[66:67],v40 offset:328
ds_read_b64 a[68:69],v41 offset:136
ds_read_b64 a[70:71],v41 offset:328
v_add_u32_sdwa v45,v12,v47 dst_sel:DWORD dst_unused:UNUSED_PAD src0_sel:WORD_1 src1_sel:DWORD
v_pk_add_f32 v[60:61],v[60:61],v[208:209]
v_pk_fma_f32 v[48:49],v[208:209],v[208:209],v[48:49]
v_pk_add_f32 v[62:63],v[62:63],v[210:211]
v_pk_fma_f32 v[50:51],v[210:211],v[210:211],v[50:51]
v_smfmac_f32_16x16x64_bf16 v[220:223],a[148:151],a[48:55],v10
s_waitcnt lgkmcnt(4)
ds_read_b64 a[48:49],v42 offset:0
ds_read_b64 a[50:51],v42 offset:192
ds_read_b64 a[52:53],v43 offset:0
ds_read_b64 a[54:55],v43 offset:192
v_add_u32_sdwa v47,v9,v46 dst_sel:DWORD dst_unused:UNUSED_PAD src0_sel:WORD_1 src1_sel:DWORD
v_pk_add_f32 v[60:61],v[60:61],v[212:213]
v_pk_fma_f32 v[48:49],v[212:213],v[212:213],v[48:49]
v_pk_add_f32 v[62:63],v[62:63],v[214:215]
v_pk_fma_f32 v[50:51],v[214:215],v[214:215],v[50:51]
v_smfmac_f32_16x16x64_bf16 v[224:227],a[176:179],a[56:63],v10
s_waitcnt lgkmcnt(4)
ds_read_b64 a[56:57],v42 offset:8
ds_read_b64 a[58:59],v42 offset:200
ds_read_b64 a[60:61],v43 offset:8
ds_read_b64 a[62:63],v43 offset:200
v_add_u32_sdwa v46,v9,v46 dst_sel:DWORD dst_unused:UNUSED_PAD src0_sel:WORD_0 src1_sel:DWORD
v_pk_add_f32 v[60:61],v[60:61],v[216:217]
v_pk_fma_f32 v[48:49],v[216:217],v[216:217],v[48:49]
v_pk_add_f32 v[62:63],v[62:63],v[218:219]
v_pk_fma_f32 v[50:51],v[218:219],v[218:219],v[50:51]
v_mfma_f32_32x32x16_bf16 v[240:255],v[32:35],v[32:35],0
v_smfmac_f32_16x16x64_bf16 v[228:231],a[180:183],a[64:71],v10
s_waitcnt lgkmcnt(4)
ds_read_b64 a[64:65],v42 offset:64
ds_read_b64 a[66:67],v42 offset:256
ds_read_b64 a[68:69],v43 offset:64
ds_read_b64 a[70:71],v43 offset:256
v_pk_add_f32 v[60:61],v[60:61],v[220:221]
v_pk_fma_f32 v[48:49],v[220:221],v[220:221],v[48:49]
v_pk_add_f32 v[62:63],v[62:63],v[222:223]
v_pk_fma_f32 v[50:51],v[222:223],v[222:223],v[50:51]
v_smfmac_f32_16x16x64_bf16 v[232:235],a[120:123],a[48:55],v10
s_waitcnt lgkmcnt(4)
ds_read_b64 a[48:49],v42 offset:72
ds_read_b64 a[50:51],v42 offset:264
ds_read_b64 a[52:53],v43 offset:72
ds_read_b64 a[54:55],v43 offset:264
v_pk_add_f32 v[60:61],v[60:61],v[224:225]
v_pk_fma_f32 v[48:49],v[224:225],v[224:225],v[48:49]
v_pk_add_f32 v[62:63],v[62:63],v[226:227]
v_pk_fma_f32 v[50:51],v[226:227],v[226:227],v[50:51]
v_smfmac_f32_16x16x64_bf16 v[236:239],a[124:127],a[56:63],v10
s_waitcnt lgkmcnt(4)
ds_read_b64 a[56:57],v42 offset:128
ds_read_b64 a[58:59],v42 offset:320
ds_read_b64 a[60:61],v43 offset:128
ds_read_b64 a[62:63],v43 offset:320
v_pk_add_f32 v[60:61],v[60:61],v[228:229]
v_pk_fma_f32 v[48:49],v[228:229],v[228:229],v[48:49]
v_pk_add_f32 v[62:63],v[62:63],v[230:231]
v_pk_fma_f32 v[50:51],v[230:231],v[230:231],v[50:51]
v_smfmac_f32_16x16x64_bf16 v[240:243],a[152:155],a[64:71],v10
s_waitcnt lgkmcnt(4)
buffer_store_dwordx4 a[80:83],v7,s[40:43],s63 offen offset:128 sc1
ds_read_b64 a[64:65],v42 offset:136
ds_read_b64 a[66:67],v42 offset:328
ds_read_b64 a[68:69],v43 offset:136
ds_read_b64 a[70:71],v43 offset:328
v_pk_add_f32 v[60:61],v[60:61],v[232:233]
v_pk_fma_f32 v[48:49],v[232:233],v[232:233],v[48:49]
v_pk_add_f32 v[62:63],v[62:63],v[234:235]
v_pk_fma_f32 v[50:51],v[234:235],v[234:235],v[50:51]
v_smfmac_f32_16x16x64_bf16 v[244:247],a[156:159],a[48:55],v10
s_waitcnt lgkmcnt(4)
ds_read_b64 a[228:229],v44 offset:57344
ds_read_b64 a[230:231],v44 offset:57352
ds_read_b64 a[248:249],v45 offset:57344
ds_read_b64 a[250:251],v45 offset:57352
ds_read_b128 a[240:243],v13 offset:0
v_pk_add_f32 v[60:61],v[60:61],v[236:237]
v_pk_fma_f32 v[48:49],v[236:237],v[236:237],v[48:49]
v_pk_add_f32 v[62:63],v[62:63],v[238:239]
v_pk_fma_f32 v[50:51],v[238:239],v[238:239],v[50:51]
v_smfmac_f32_16x16x64_bf16 v[248:251],a[184:187],a[56:63],v10
s_waitcnt lgkmcnt(5)
ds_read_b64 a[236:237],v46 offset:57344
ds_read_b64 a[238:239],v46 offset:57352
ds_read_b64 a[252:253],v47 offset:57344
ds_read_b64 a[254:255],v47 offset:57352
ds_read_b128 a[224:227],v13 offset:256
v_pk_add_f32 v[60:61],v[60:61],v[240:241]
v_pk_fma_f32 v[48:49],v[240:241],v[240:241],v[48:49]
v_pk_add_f32 v[62:63],v[62:63],v[242:243]
v_pk_fma_f32 v[50:51],v[242:243],v[242:243],v[50:51]
v_smfmac_f32_16x16x64_bf16 v[252:255],a[188:191],a[64:71],v10
ds_read_b128 v[40:43],v4 offset:3584
ds_read_b128 v[36:39],v4 offset:3616
v_pk_add_f32 v[60:61],v[60:61],v[244:245]
v_pk_fma_f32 v[48:49],v[244:245],v[244:245],v[48:49]
v_pk_add_f32 v[62:63],v[62:63],v[246:247]
v_pk_fma_f32 v[50:51],v[246:247],v[246:247],v[50:51]
v_pk_add_f32 v[60:61],v[60:61],v[248:249]
v_pk_fma_f32 v[48:49],v[248:249],v[248:249],v[48:49]
v_pk_add_f32 v[62:63],v[62:63],v[250:251]
v_pk_fma_f32 v[50:51],v[250:251],v[250:251],v[50:51]
v_pk_add_f32 v[60:61],v[60:61],v[252:253]
v_pk_fma_f32 v[48:49],v[252:253],v[252:253],v[48:49]
v_pk_add_f32 v[62:63],v[62:63],v[254:255]
v_pk_fma_f32 v[50:51],v[254:255],v[254:255],v[50:51]
s_nop 0
v_permlane32_swap_b32 v60,v62
v_permlane32_swap_b32 v61,v63
v_permlane32_swap_b32 v48,v50
v_permlane32_swap_b32 v49,v51
v_pk_add_f32 v[60:61],v[60:61],v[62:63]
v_pk_add_f32 v[48:49],v[48:49],v[50:51]
s_nop 0
v_permlane16_swap_b32 v60,v61
v_permlane16_swap_b32 v48,v49
v_add_f32 v8,v60,v61
v_add_f32 v9,v48,v49
v_mul_f32 v8,0x3c2aaaab,v8
v_mul_f32 v9,0x3c2aaaab,v9
v_cmp_ngt_f32 vcc,0x5d800000,v9
v_fma_f32 v9,-v8,v8,v9
v_max_f32 v9,0,v9
s_or_b64 s[90:91],s[90:91],vcc
v_add_f32 v9,0x3727c5ac,v9
v_rsq_f32 v9,v9
s_nop 0
v_mul_f32 v60,s96,v9
v_mul_f32 v61,s96,v9
v_mul_f32 v48,-v8,v60
v_mul_f32 v49,-v8,v60
v_permlane16_swap_b32 v60,v61
s_nop 0
v_permlane16_swap_b32 v48,v49
v_mov_b64 v[62:63],v[60:61]
v_mov_b64 v[50:51],v[48:49]
s_nop 0
v_permlane32_swap_b32 v60,v62
v_permlane32_swap_b32 v61,v63
v_permlane32_swap_b32 v48,v50
v_permlane32_swap_b32 v49,v51
s_or_b32 s64,s90,s91
s_lshl_b32 s65,s52,2
v_mov_b32 v8,s64
v_mov_b32 v9,s65
ds_write_b32 v9,v8 offset:11648
v_pk_fma_f32 v[160:161],v[160:161],v[60:61],v[48:49]
v_pk_fma_f32 v[162:163],v[162:163],v[62:63],v[50:51]
v_pk_fma_f32 v[164:165],v[164:165],v[60:61],v[48:49]
v_pk_fma_f32 v[166:167],v[166:167],v[62:63],v[50:51]
v_pk_fma_f32 v[168:169],v[168:169],v[60:61],v[48:49]
v_pk_fma_f32 v[170:171],v[170:171],v[62:63],v[50:51]
v_pk_fma_f32 v[172:173],v[172:173],v[60:61],v[48:49]
v_pk_fma_f32 v[174:175],v[174:175],v[62:63],v[50:51]
v_pk_fma_f32 v[208:209],v[208:209],v[60:61],v[48:49]
v_pk_fma_f32 v[210:211],v[210:211],v[62:63],v[50:51]
v_pk_fma_f32 v[212:213],v[212:213],v[60:61],v[48:49]
v_pk_fma_f32 v[214:215],v[214:215],v[62:63],v[50:51]
buffer_store_dwordx4 a[84:87],v7,s[40:43],s63 offen offset:192 sc1
v_pk_fma_f32 v[232:233],v[232:233],v[60:61],v[48:49]
v_pk_fma_f32 v[234:235],v[234:235],v[62:63],v[50:51]
v_pk_fma_f32 v[236:237],v[236:237],v[60:61],v[48:49]
v_pk_fma_f32 v[238:239],v[238:239],v[62:63],v[50:51]
v_pk_fma_f32 v[176:177],v[176:177],v[60:61],v[48:49]
v_pk_fma_f32 v[178:179],v[178:179],v[62:63],v[50:51]
v_pk_fma_f32 v[180:181],v[180:181],v[60:61],v[48:49]
v_pk_fma_f32 v[182:183],v[182:183],v[62:63],v[50:51]
v_pk_fma_f32 v[184:185],v[184:185],v[60:61],v[48:49]
v_pk_fma_f32 v[186:187],v[186:187],v[62:63],v[50:51]
v_pk_fma_f32 v[188:189],v[188:189],v[60:61],v[48:49]
v_pk_fma_f32 v[190:191],v[190:191],v[62:63],v[50:51]
v_pk_fma_f32 v[216:217],v[216:217],v[60:61],v[48:49]
v_pk_fma_f32 v[218:219],v[218:219],v[62:63],v[50:51]
v_pk_fma_f32 v[220:221],v[220:221],v[60:61],v[48:49]
v_pk_fma_f32 v[222:223],v[222:223],v[62:63],v[50:51]
v_pk_fma_f32 v[240:241],v[240:241],v[60:61],v[48:49]
v_pk_fma_f32 v[242:243],v[242:243],v[62:63],v[50:51]
v_pk_fma_f32 v[244:245],v[244:245],v[60:61],v[48:49]
v_pk_fma_f32 v[246:247],v[246:247],v[62:63],v[50:51]
v_pk_fma_f32 v[192:193],v[192:193],v[60:61],v[48:49]
v_pk_fma_f32 v[194:195],v[194:195],v[62:63],v[50:51]
v_pk_fma_f32 v[196:197],v[196:197],v[60:61],v[48:49]
v_pk_fma_f32 v[198:199],v[198:199],v[62:63],v[50:51]
v_pk_fma_f32 v[200:201],v[200:201],v[60:61],v[48:49]
v_pk_fma_f32 v[202:203],v[202:203],v[62:63],v[50:51]
v_pk_fma_f32 v[204:205],v[204:205],v[60:61],v[48:49]
v_pk_fma_f32 v[206:207],v[206:207],v[62:63],v[50:51]
v_pk_fma_f32 v[224:225],v[224:225],v[60:61],v[48:49]
v_pk_fma_f32 v[226:227],v[226:227],v[62:63],v[50:51]
v_pk_fma_f32 v[228:229],v[228:229],v[60:61],v[48:49]
v_pk_fma_f32 v[230:231],v[230:231],v[62:63],v[50:51]
v_pk_fma_f32 v[248:249],v[248:249],v[60:61],v[48:49]
v_pk_fma_f32 v[250:251],v[250:251],v[62:63],v[50:51]
v_pk_fma_f32 v[252:253],v[252:253],v[60:61],v[48:49]
v_pk_fma_f32 v[254:255],v[254:255],v[62:63],v[50:51]
v_mfma_f32_16x16x32_bf16 v[32:35],a[216:219],a[8:11],v[16:19]
ds_read_b64_tr_b16 a[196:197],v1 offset:6160
v_cvt_pk_bf16_f32 v160,v160,v164
v_cvt_pk_bf16_f32 v168,v168,v172
v_cvt_pk_bf16_f32 v208,v208,v212
v_lshlrev_b32_sdwa v24,1,v24 dst_sel:DWORD dst_unused:UNUSED_PAD src0_sel:DWORD src1_sel:WORD_1
v_mfma_f32_16x16x32_bf16 v[32:35],a[220:223],a[24:27],v[32:35]
ds_read_u16 v24,v24
v_accvgpr_write_b32 a48,v160
v_accvgpr_write_b32 a49,v168
v_cvt_pk_bf16_f32 v232,v232,v236
v_accvgpr_write_b32 a50,v208
v_mfma_f32_16x16x32_bf16 v[32:35],a[212:215],a[40:43],v[32:35]
buffer_store_dwordx4 a[88:91],v7,s[40:43],s63 offen offset:256 sc1
v_cvt_pk_bf16_f32 v161,v161,v165
v_accvgpr_write_b32 a51,v232
v_cvt_pk_bf16_f32 v169,v169,v173
v_accvgpr_write_b32 a52,v161
v_fma_mixlo_f16 v8,v57,s99,0
v_mfma_f32_16x16x32_bf16 v[60:63],a[208:211],a[40:43],v[20:23]
ds_read_b64_tr_b16 a[198:199],v1 offset:7184
v_cvt_pk_bf16_f32 v209,v209,v213
v_accvgpr_write_b32 a53,v169
v_cvt_pk_bf16_f32 v233,v233,v237
v_accvgpr_write_b32 a54,v209
v_lshlrev_b32_sdwa v26,1,v26 dst_sel:DWORD dst_unused:UNUSED_PAD src0_sel:DWORD src1_sel:WORD_1
v_mfma_f32_16x16x32_bf16 v[60:63],a[204:207],a[24:27],v[60:63]
ds_read_u16_d16_hi v26,v26
v_cvt_pk_bf16_f32 v162,v162,v166
v_accvgpr_write_b32 a55,v233
v_cvt_pk_bf16_f32 v170,v170,v174
v_accvgpr_write_b32 a56,v162
v_fma_mixhi_f16 v8,v59,s99,0
v_mfma_f32_16x16x32_bf16 v[60:63],a[200:203],a[8:11],v[60:63]
ds_read_b64_tr_b16 a[192:193],v1 offset:8208
v_accvgpr_write_b32 a57,v170
v_cvt_pk_bf16_f32 v210,v210,v214
v_accvgpr_write_b32 a58,v210
v_lshlrev_b32_sdwa v25,1,v25 dst_sel:DWORD dst_unused:UNUSED_PAD src0_sel:DWORD src1_sel:WORD_1
v_cvt_pk_bf16_f32 v234,v234,v238
v_mfma_f32_16x16x32_bf16 v[48:51],a[200:203],a[12:15],v[20:23]
ds_read_u16 v25,v25
v_accvgpr_write_b32 a59,v234
v_cvt_pk_bf16_f32 v163,v163,v167
v_accvgpr_write_b32 a60,v163
v_mfma_f32_16x16x32_bf16 v[48:51],a[204:207],a[28:31],v[48:51]
ds_read_b64_tr_b16 a[194:195],v1 offset:9232
v_cvt_pk_bf16_f32 v171,v171,v175
v_accvgpr_write_b32 a61,v171
v_cvt_pk_bf16_f32 v211,v211,v215
v_accvgpr_write_b32 a62,v211
v_pk_fma_f16 v9,v8,v8,s54
v_mfma_f32_16x16x32_bf16 v[48:51],a[208:211],a[44:47],v[48:51]
s_waitcnt lgkmcnt(7)
v_cvt_pk_bf16_f32 v235,v235,v239
v_accvgpr_write_b32 a63,v235
v_cvt_pk_bf16_f32 v176,v176,v180
v_cvt_pk_bf16_f32 v184,v184,v188
v_lshlrev_b32_sdwa v27,1,v27 dst_sel:DWORD dst_unused:UNUSED_PAD src0_sel:DWORD src1_sel:WORD_1
v_mfma_f32_16x16x32_bf16 v[232:235],a[240:243],a[228:231],v[40:43]
ds_read_u16_d16_hi v27,v27
v_cvt_pk_bf16_f32 v216,v216,v220
v_accvgpr_write_b32 a64,v176
v_accvgpr_write_b32 a65,v184
v_cvt_pk_bf16_f32 v240,v240,v244
v_pk_mul_f16 v9,v8,v9
v_mfma_f32_16x16x32_bf16 v[236:239],a[240:243],a[248:251],v[40:43]
v_accvgpr_write_b32 a66,v216
v_cvt_pk_bf16_f32 v177,v177,v181
v_accvgpr_write_b32 a67,v240
v_cvt_pk_bf16_f32 v185,v185,v189
v_lshlrev_b32_sdwa v28,1,v28 dst_sel:DWORD dst_unused:UNUSED_PAD src0_sel:DWORD src1_sel:WORD_1
v_mfma_f32_16x16x32_bf16 v[208:211],a[240:243],a[236:239],v[40:43]
v_exp_f16 v8,v9
v_accvgpr_write_b32 a68,v177
v_cvt_pk_bf16_f32 v217,v217,v221
v_accvgpr_write_b32 a69,v185
v_cvt_pk_bf16_f32 v241,v241,v245
v_mfma_f32_16x16x32_bf16 v[212:215],a[240:243],a[252:255],v[40:43]
ds_read_b128 a[240:243],v13 offset:512
ds_read_b128 v[40:43],v4 offset:3712
v_accvgpr_write_b32 a70,v217
v_cvt_pk_bf16_f32 v178,v178,v182
v_accvgpr_write_b32 a71,v241
v_mfma_f32_16x16x32_bf16 v[160:163],a[224:227],a[228:231],v[36:39]
buffer_store_dwordx4 a[92:95],v7,s[40:43],s63 offen offset:320 sc1
ds_read_b64 a[228:229],v44 offset:57408
v_cvt_pk_bf16_f32 v186,v186,v190
v_accvgpr_write_b32 a72,v178
v_accvgpr_write_b32 a73,v186
v_cvt_pk_bf16_f32 v218,v218,v222
v_lshlrev_b32_sdwa v30,1,v30 dst_sel:DWORD dst_unused:UNUSED_PAD src0_sel:DWORD src1_sel:WORD_1
v_mfma_f32_16x16x32_bf16 v[164:167],a[224:227],a[248:251],v[36:39]
ds_read_b64 a[230:231],v44 offset:57416
ds_read_b64 a[248:249],v45 offset:57408
v_accvgpr_write_b32 a74,v218
v_cvt_pk_bf16_f32 v242,v242,v246
v_accvgpr_write_b32 a75,v242
v_cvt_pk_bf16_f32 v179,v179,v183
v_mfma_f32_16x16x32_bf16 v[168:171],a[224:227],a[236:239],v[36:39]
ds_read_b64 a[250:251],v45 offset:57416
ds_read_b64 a[236:237],v46 offset:57408
v_accvgpr_write_b32 a76,v179
v_cvt_pk_bf16_f32 v187,v187,v191
v_accvgpr_write_b32 a77,v187
v_cvt_pk_bf16_f32 v219,v219,v223
v_lshlrev_b32_sdwa v29,1,v29 dst_sel:DWORD dst_unused:UNUSED_PAD src0_sel:DWORD src1_sel:WORD_1
v_mfma_f32_16x16x32_bf16 v[172:175],a[224:227],a[252:255],v[36:39]
s_waitcnt lgkmcnt(3)
ds_read_b64 a[238:239],v46 offset:57416
ds_read_b64 a[252:253],v47 offset:57408
v_exp_f16_sdwa v8,v9 dst_sel:WORD_1 dst_unused:UNUSED_PRESERVE src0_sel:WORD_1
v_accvgpr_write_b32 a78,v219
v_cvt_pk_bf16_f32 v243,v243,v247
v_accvgpr_write_b32 a79,v243
v_cvt_pk_bf16_f32 v192,v192,v196
v_mfma_f32_16x16x32_bf16 v[240:243],a[240:243],a[228:231],v[40:43]
s_waitcnt lgkmcnt(3)
ds_read_b64 a[254:255],v47 offset:57416
ds_read_b128 a[224:227],v13 offset:768
v_cvt_pk_bf16_f32 v200,v200,v204
v_cvt_pk_bf16_f32 v224,v224,v228
v_accvgpr_write_b32 a80,v192
v_accvgpr_write_b32 a81,v200
v_lshlrev_b32_sdwa v31,1,v31 dst_sel:DWORD dst_unused:UNUSED_PAD src0_sel:DWORD src1_sel:WORD_1
v_mfma_f32_16x16x32_bf16 v[244:247],a[240:243],a[248:251],v[40:43]
s_waitcnt lgkmcnt(3)
ds_read_b128 v[36:39],v4 offset:3744
v_cvt_pk_bf16_f32 v248,v248,v252
v_accvgpr_write_b32 a82,v224
v_cvt_pk_bf16_f32 v193,v193,v197
v_mfma_f32_16x16x32_bf16 v[216:219],a[240:243],a[236:239],v[40:43]
s_waitcnt lgkmcnt(2)
ds_read_u16 v28,v28
v_accvgpr_write_b32 a83,v248
v_cvt_pk_bf16_f32 v201,v201,v205
v_accvgpr_write_b32 a84,v193
v_cvt_pk_bf16_f32 v225,v225,v229
v_mfma_f32_16x16x32_bf16 v[220:223],a[240:243],a[252:255],v[40:43]
s_waitcnt lgkmcnt(1)
ds_read_b128 a[240:243],v13 offset:1024
ds_read_b128 v[40:43],v4 offset:3840
v_accvgpr_write_b32 a85,v201
v_cvt_pk_bf16_f32 v249,v249,v253
v_accvgpr_write_b32 a86,v225
v_cvt_pk_bf16_f32 v194,v194,v198
v_lshlrev_b32_sdwa v52,1,v52 dst_sel:DWORD dst_unused:UNUSED_PAD src0_sel:DWORD src1_sel:WORD_1
v_mfma_f32_16x16x32_bf16 v[176:179],a[224:227],a[228:231],v[36:39]
TE11_%=:
s_cbranch_execz TP10316_%=
ds_read_b64 a[228:229],v44 offset:57472
ds_read_b64 a[230:231],v44 offset:57480
v_accvgpr_write_b32 a87,v249
v_cvt_pk_bf16_f32 v202,v202,v206
v_accvgpr_write_b32 a88,v194
v_accvgpr_write_b32 a89,v202
v_pk_fma_f16 v8,v8,s55,s55
v_mfma_f32_16x16x32_bf16 v[180:183],a[224:227],a[248:251],v[36:39]
ds_read_b64 a[248:249],v45 offset:57472
ds_read_b64 a[250:251],v45 offset:57480
v_cvt_pk_bf16_f32 v226,v226,v230
v_accvgpr_write_b32 a90,v226
v_cvt_pk_bf16_f32 v250,v250,v254
v_accvgpr_write_b32 a91,v250
v_lshlrev_b32_sdwa v54,1,v54 dst_sel:DWORD dst_unused:UNUSED_PAD src0_sel:DWORD src1_sel:WORD_1
v_mfma_f32_16x16x32_bf16 v[184:187],a[224:227],a[236:239],v[36:39]
ds_read_b64 a[236:237],v46 offset:57472
ds_read_b64 a[238:239],v46 offset:57480
v_rcp_f16 v9,v8
v_cvt_pk_bf16_f32 v195,v195,v199
v_accvgpr_write_b32 a92,v195
v_cvt_pk_bf16_f32 v203,v203,v207
v_accvgpr_write_b32 a93,v203
v_mfma_f32_16x16x32_bf16 v[188:191],a[224:227],a[252:255],v[36:39]
s_waitcnt lgkmcnt(4)
ds_read_b64 a[252:253],v47 offset:57472
ds_read_b64 a[254:255],v47 offset:57480
v_cvt_pk_bf16_f32 v227,v227,v231
v_accvgpr_write_b32 a94,v227
v_cvt_pk_bf16_f32 v251,v251,v255
v_accvgpr_write_b32 a95,v251
v_mfma_f32_16x16x32_bf16 v[248:251],a[240:243],a[228:231],v[40:43]
s_waitcnt lgkmcnt(4)
ds_read_b128 a[224:227],v13 offset:1280
ds_read_b128 v[36:39],v4 offset:3872
v_lshlrev_b32_sdwa v53,1,v53 dst_sel:DWORD dst_unused:UNUSED_PAD src0_sel:DWORD src1_sel:WORD_1
v_mfma_f32_16x16x32_bf16 v[252:255],a[240:243],a[248:251],v[40:43]
s_waitcnt lgkmcnt(4)
ds_read_u16_d16_hi v30,v30
v_lshlrev_b32_sdwa v55,1,v55 dst_sel:DWORD dst_unused:UNUSED_PAD src0_sel:DWORD src1_sel:WORD_1
v_mfma_f32_16x16x32_bf16 v[224:227],a[240:243],a[236:239],v[40:43]
s_waitcnt lgkmcnt(3)
ds_read_u16 v29,v29
v_lshlrev_b32_sdwa v56,1,v56 dst_sel:DWORD dst_unused:UNUSED_PAD src0_sel:DWORD src1_sel:WORD_1
v_mfma_f32_16x16x32_bf16 v[228:231],a[240:243],a[252:255],v[40:43]
s_waitcnt lgkmcnt(2)
ds_read_b64_tr_b16 a[240:241],v3 offset:6224
ds_read_b64_tr_b16 a[242:243],v3 offset:7760
v_rcp_f16_sdwa v9,v8 dst_sel:WORD_1 dst_unused:UNUSED_PRESERVE src0_sel:WORD_1
v_mfma_f32_16x16x32_bf16 v[192:195],a[224:227],a[228:231],v[36:39]
ds_read_b64_tr_b16 a[228:229],v3 offset:6232
ds_read_b64_tr_b16 a[230:231],v3 offset:7768
v_mfma_f32_16x16x32_bf16 v[196:199],a[224:227],a[248:251],v[36:39]
ds_read_b64_tr_b16 a[248:249],v3 offset:6288
ds_read_b64_tr_b16 a[250:251],v3 offset:7824
v_mfma_f32_16x16x32_bf16 v[200:203],a[224:227],a[236:239],v[36:39]
ds_read_b64_tr_b16 a[236:237],v3 offset:6296
ds_read_b64_tr_b16 a[238:239],v3 offset:7832
v_mfma_f32_16x16x32_bf16 v[204:207],a[224:227],a[252:255],v[36:39]
ds_read_u16_d16_hi v31,v31
ds_read_u16 v52,v52
v_lshlrev_b32_sdwa v58,1,v58 dst_sel:DWORD dst_unused:UNUSED_PAD src0_sel:DWORD src1_sel:WORD_1
v_mfma_f32_16x16x32_bf16 v[44:47],a[212:215],a[44:47],v[16:19]
ds_read_u16_d16_hi v54,v54
ds_read_u16 v53,v53
v_fma_mix_f32 v57,v57,v9,0 op_sel_hi:[0,1,0]
v_mfma_f32_16x16x32_bf16 v[44:47],a[220:223],a[28:31],v[44:47]
ds_read_u16_d16_hi v55,v55
v_fma_mix_f32 v59,v59,v9,0 op_sel:[0,1,0] op_sel_hi:[0,1,0]
v_mfma_f32_16x16x32_bf16 v[44:47],a[216:219],a[12:15],v[44:47]
s_barrier
ds_read_u16 v56,v56
ds_read_u16_d16_hi v58,v58
v_mfma_f32_16x16x32_bf16 v[40:43],a[216:219],a[48:51],v[16:19]
v_lshlrev_b32_sdwa v32,1,v32 dst_sel:DWORD dst_unused:UNUSED_PAD src0_sel:DWORD src1_sel:WORD_1
v_fma_mixlo_f16 v8,v49,s99,0
v_mfma_f32_16x16x32_bf16 v[40:43],a[220:223],a[64:67],v[40:43]
ds_read_u16 v32,v32
v_lshlrev_b32_sdwa v34,1,v34 dst_sel:DWORD dst_unused:UNUSED_PAD src0_sel:DWORD src1_sel:WORD_1
v_mfma_f32_16x16x32_bf16 v[40:43],a[212:215],a[80:83],v[40:43]
ds_read_u16_d16_hi v34,v34
v_lshl_add_u64 v[24:25],v[24:25],0,v[26:27]
v_mfma_f32_16x16x32_bf16 v[36:39],a[208:211],a[80:83],v[20:23]
s_waitcnt lgkmcnt(8)
v_lshl_add_u64 v[26:27],v[28:29],0,v[30:31]
v_fma_mixhi_f16 v8,v51,s99,0
v_mfma_f32_16x16x32_bf16 v[36:39],a[204:207],a[64:67],v[36:39]
s_waitcnt lgkmcnt(4)
v_lshl_add_u64 v[28:29],v[52:53],0,v[54:55]
v_lshlrev_b32_sdwa v33,1,v33 dst_sel:DWORD dst_unused:UNUSED_PAD src0_sel:DWORD src1_sel:WORD_1
v_pk_fma_f16 v9,v8,v8,s54
v_mfma_f32_16x16x32_bf16 v[36:39],a[200:203],a[48:51],v[36:39]
ds_read_u16 v33,v33
v_lshlrev_b32_sdwa v35,1,v35 dst_sel:DWORD dst_unused:UNUSED_PAD src0_sel:DWORD src1_sel:WORD_1
v_mfma_f32_16x16x32_bf16 v[52:55],a[200:203],a[52:55],v[20:23]
s_waitcnt lgkmcnt(3)
ds_read_u16_d16_hi v35,v35
v_or_b32 v30,v56,v58
v_pk_mul_f16 v9,v8,v9
v_mfma_f32_16x16x32_bf16 v[52:55],a[204:207],a[68:71],v[52:55]
v_cvt_pk_bf16_f32 v31,v57,v59
v_lshlrev_b32_sdwa v60,1,v60 dst_sel:DWORD dst_unused:UNUSED_PAD src0_sel:DWORD src1_sel:WORD_1
v_mfma_f32_16x16x32_bf16 v[52:55],a[208:211],a[84:87],v[52:55]
v_exp_f16 v8,v9
v_mfma_f32_16x16x32_bf16 v[56:59],a[212:215],a[84:87],v[16:19]
ds_read_u16 v60,v60
v_lshlrev_b32_sdwa v62,1,v62 dst_sel:DWORD dst_unused:UNUSED_PAD src0_sel:DWORD src1_sel:WORD_1
v_mfma_f32_16x16x32_bf16 v[56:59],a[220:223],a[68:71],v[56:59]
ds_read_u16_d16_hi v62,v62
v_lshlrev_b32_sdwa v61,1,v61 dst_sel:DWORD dst_unused:UNUSED_PAD src0_sel:DWORD src1_sel:WORD_1
v_mfma_f32_16x16x32_bf16 v[56:59],a[216:219],a[52:55],v[56:59]
v_exp_f16_sdwa v8,v9 dst_sel:WORD_1 dst_unused:UNUSED_PRESERVE src0_sel:WORD_1
v_mfma_f32_16x16x32_bf16 v[136:139],a[232:235],v[24:27],v[136:139]
ds_read_u16 v61,v61
v_lshlrev_b32_sdwa v63,1,v63 dst_sel:DWORD dst_unused:UNUSED_PAD src0_sel:DWORD src1_sel:WORD_1
v_mfma_f32_16x16x32_bf16 v[140:143],a[232:235],v[28:31],v[140:143]
ds_read_u16_d16_hi v63,v63
v_lshlrev_b32_sdwa v44,1,v44 dst_sel:DWORD dst_unused:UNUSED_PAD src0_sel:DWORD src1_sel:WORD_1
v_mfma_f32_16x16x32_bf16 v[68:71],a[244:247],v[28:31],v[68:71]
ds_read_u16 v44,v44
v_pk_fma_f16 v8,v8,s55,s55
v_mfma_f32_16x16x32_bf16 v[64:67],a[244:247],v[24:27],v[64:67]
v_rcp_f16 v9,v8
v_lshlrev_b32_sdwa v46,1,v46 dst_sel:DWORD dst_unused:UNUSED_PAD src0_sel:DWORD src1_sel:WORD_1
v_mfma_f32_16x16x32_bf16 v[144:147],a[240:243],v[24:27],v[144:147]
ds_read_u16_d16_hi v46,v46
v_lshlrev_b32_sdwa v45,1,v45 dst_sel:DWORD dst_unused:UNUSED_PAD src0_sel:DWORD src1_sel:WORD_1
v_mfma_f32_16x16x32_bf16 v[148:151],a[240:243],v[28:31],v[148:151]
ds_read_u16 v45,v45
v_lshlrev_b32_sdwa v47,1,v47 dst_sel:DWORD dst_unused:UNUSED_PAD src0_sel:DWORD src1_sel:WORD_1
v_mfma_f32_16x16x32_bf16 v[84:87],a[228:231],v[28:31],v[84:87]
ds_read_u16_d16_hi v47,v47
v_lshlrev_b32_sdwa v48,1,v48 dst_sel:DWORD dst_unused:UNUSED_PAD src0_sel:DWORD src1_sel:WORD_1
v_mfma_f32_16x16x32_bf16 v[80:83],a[228:231],v[24:27],v[80:83]
v_rcp_f16_sdwa v9,v8 dst_sel:WORD_1 dst_unused:UNUSED_PRESERVE src0_sel:WORD_1
v_mfma_f32_16x16x32_bf16 v[152:155],a[248:251],v[24:27],v[152:155]
ds_read_u16 v48,v48
v_lshlrev_b32_sdwa v50,1,v50 dst_sel:DWORD dst_unused:UNUSED_PAD src0_sel:DWORD src1_sel:WORD_1
v_mfma_f32_16x16x32_bf16 v[156:159],a[248:251],v[28:31],v[156:159]
ds_read_u16_d16_hi v50,v50
v_fma_mix_f32 v49,v49,v9,0 op_sel_hi:[0,1,0]
v_mfma_f32_16x16x32_bf16 v[100:103],a[236:239],v[28:31],v[100:103]
v_fma_mix_f32 v51,v51,v9,0 op_sel:[0,1,0] op_sel_hi:[0,1,0]
v_mfma_f32_16x16x32_bf16 v[96:99],a[236:239],v[24:27],v[96:99]
v_mfma_f32_16x16x32_bf16 v[28:31],a[216:219],a[56:59],v[16:19]
v_lshlrev_b32_sdwa v40,1,v40 dst_sel:DWORD dst_unused:UNUSED_PAD src0_sel:DWORD src1_sel:WORD_1
v_fma_mixlo_f16 v8,v53,s99,0
v_mfma_f32_16x16x32_bf16 v[28:31],a[220:223],a[72:75],v[28:31]
ds_read_u16 v40,v40
v_lshlrev_b32_sdwa v42,1,v42 dst_sel:DWORD dst_unused:UNUSED_PAD src0_sel:DWORD src1_sel:WORD_1
v_mfma_f32_16x16x32_bf16 v[28:31],a[212:215],a[88:91],v[28:31]
s_waitcnt lgkmcnt(4)
ds_read_u16_d16_hi v42,v42
v_lshl_add_u64 v[32:33],v[32:33],0,v[34:35]
v_mfma_f32_16x16x32_bf16 v[24:27],a[208:211],a[88:91],v[20:23]
v_lshl_add_u64 v[34:35],v[60:61],0,v[62:63]
v_fma_mixhi_f16 v8,v55,s99,0
v_mfma_f32_16x16x32_bf16 v[24:27],a[204:207],a[72:75],v[24:27]
s_waitcnt lgkmcnt(4)
v_lshl_add_u64 v[60:61],v[44:45],0,v[46:47]
v_lshlrev_b32_sdwa v41,1,v41 dst_sel:DWORD dst_unused:UNUSED_PAD src0_sel:DWORD src1_sel:WORD_1
v_pk_fma_f16 v9,v8,v8,s54
v_mfma_f32_16x16x32_bf16 v[24:27],a[200:203],a[56:59],v[24:27]
ds_read_u16 v41,v41
v_lshlrev_b32_sdwa v43,1,v43 dst_sel:DWORD dst_unused:UNUSED_PAD src0_sel:DWORD src1_sel:WORD_1
v_mfma_f32_16x16x32_bf16 v[44:47],a[200:203],a[60:63],v[20:23]
s_waitcnt lgkmcnt(3)
ds_read_b64_tr_b16 a[200:201],v1 offset:10256
ds_read_b64_tr_b16 a[202:203],v1 offset:11280
v_or_b32 v62,v48,v50
v_pk_mul_f16 v9,v8,v9
v_mfma_f32_16x16x32_bf16 v[44:47],a[204:207],a[76:79],v[44:47]
ds_read_b64_tr_b16 a[204:205],v2 offset:10256
ds_read_b64_tr_b16 a[206:207],v2 offset:11280
v_cvt_pk_bf16_f32 v63,v49,v51
v_lshlrev_b32_sdwa v36,1,v36 dst_sel:DWORD dst_unused:UNUSED_PAD src0_sel:DWORD src1_sel:WORD_1
v_mfma_f32_16x16x32_bf16 v[44:47],a[208:211],a[92:95],v[44:47]
ds_read_b64_tr_b16 a[208:209],v2 offset:8208
ds_read_b64_tr_b16 a[210:211],v2 offset:9232
v_exp_f16 v8,v9
v_mfma_f32_16x16x32_bf16 v[48:51],a[212:215],a[92:95],v[16:19]
ds_read_b64_tr_b16 a[212:213],v2 offset:6160
ds_read_b64_tr_b16 a[214:215],v2 offset:7184
v_lshlrev_b32_sdwa v38,1,v38 dst_sel:DWORD dst_unused:UNUSED_PAD src0_sel:DWORD src1_sel:WORD_1
v_mfma_f32_16x16x32_bf16 v[48:51],a[220:223],a[76:79],v[48:51]
ds_read_b128 v[20:23],v4 offset:800
v_lshlrev_b32_sdwa v37,1,v37 dst_sel:DWORD dst_unused:UNUSED_PAD src0_sel:DWORD src1_sel:WORD_1
v_mfma_f32_16x16x32_bf16 v[48:51],a[216:219],a[60:63],v[48:51]
v_exp_f16_sdwa v8,v9 dst_sel:WORD_1 dst_unused:UNUSED_PRESERVE src0_sel:WORD_1
v_mfma_f32_16x16x32_bf16 v[112:115],a[232:235],v[32:35],v[112:115]
ds_read_b128 v[16:19],v4 offset:768
v_lshlrev_b32_sdwa v39,1,v39 dst_sel:DWORD dst_unused:UNUSED_PAD src0_sel:DWORD src1_sel:WORD_1
v_mfma_f32_16x16x32_bf16 v[116:119],a[232:235],v[60:63],v[116:119]
ds_read_u16_d16_hi v43,v43
v_lshlrev_b32_sdwa v56,1,v56 dst_sel:DWORD dst_unused:UNUSED_PAD src0_sel:DWORD src1_sel:WORD_1
v_mfma_f32_16x16x32_bf16 v[76:79],a[244:247],v[60:63],v[76:79]
ds_read_u16 v36,v36
v_pk_fma_f16 v8,v8,s55,s55
v_mfma_f32_16x16x32_bf16 v[72:75],a[244:247],v[32:35],v[72:75]
v_rcp_f16 v9,v8
v_lshlrev_b32_sdwa v58,1,v58 dst_sel:DWORD dst_unused:UNUSED_PAD src0_sel:DWORD src1_sel:WORD_1
v_mfma_f32_16x16x32_bf16 v[120:123],a[240:243],v[32:35],v[120:123]
ds_read_u16_d16_hi v38,v38
v_lshlrev_b32_sdwa v57,1,v57 dst_sel:DWORD dst_unused:UNUSED_PAD src0_sel:DWORD src1_sel:WORD_1
v_mfma_f32_16x16x32_bf16 v[124:127],a[240:243],v[60:63],v[124:127]
ds_read_u16 v37,v37
v_lshlrev_b32_sdwa v59,1,v59 dst_sel:DWORD dst_unused:UNUSED_PAD src0_sel:DWORD src1_sel:WORD_1
v_mfma_f32_16x16x32_bf16 v[92:95],a[228:231],v[60:63],v[92:95]
ds_read_u16_d16_hi v39,v39
v_lshlrev_b32_sdwa v52,1,v52 dst_sel:DWORD dst_unused:UNUSED_PAD src0_sel:DWORD src1_sel:WORD_1
v_mfma_f32_16x16x32_bf16 v[88:91],a[228:231],v[32:35],v[88:91]
v_rcp_f16_sdwa v9,v8 dst_sel:WORD_1 dst_unused:UNUSED_PRESERVE src0_sel:WORD_1
v_mfma_f32_16x16x32_bf16 v[128:131],a[248:251],v[32:35],v[128:131]
ds_read_u16 v56,v56
ds_read_u16_d16_hi v58,v58
v_mfma_f32_16x16x32_bf16 v[132:135],a[248:251],v[60:63],v[132:135]
ds_read_u16 v57,v57
ds_read_u16_d16_hi v59,v59
v_lshlrev_b32_sdwa v54,1,v54 dst_sel:DWORD dst_unused:UNUSED_PAD src0_sel:DWORD src1_sel:WORD_1
v_mfma_f32_16x16x32_bf16 v[108:111],a[236:239],v[60:63],v[108:111]
ds_read_u16 v52,v52
v_fma_mix_f32 v53,v53,v9,0 op_sel_hi:[0,1,0]
v_mfma_f32_16x16x32_bf16 v[104:107],a[236:239],v[32:35],v[104:107]
s_waitcnt lgkmcnt(8)
ds_read_u16_d16_hi v54,v54
v_fma_mix_f32 v55,v55,v9,0 op_sel:[0,1,0] op_sel_hi:[0,1,0]
v_mfma_f32_16x16x32_bf16 v[60:63],a[196:199],a[0:3],v[16:19]
v_lshlrev_b32_sdwa v28,1,v28 dst_sel:DWORD dst_unused:UNUSED_PAD src0_sel:DWORD src1_sel:WORD_1
v_fma_mixlo_f16 v8,v45,s99,0
v_mfma_f32_16x16x32_bf16 v[60:63],a[192:195],a[16:19],v[60:63]
ds_read_u16 v28,v28
v_lshlrev_b32_sdwa v30,1,v30 dst_sel:DWORD dst_unused:UNUSED_PAD src0_sel:DWORD src1_sel:WORD_1
v_mfma_f32_16x16x32_bf16 v[60:63],a[200:203],a[32:35],v[60:63]
ds_read_u16_d16_hi v30,v30
v_lshl_add_u64 v[40:41],v[40:41],0,v[42:43]
v_mfma_f32_16x16x32_bf16 v[32:35],a[204:207],a[32:35],v[20:23]
s_waitcnt lgkmcnt(8)
v_lshl_add_u64 v[42:43],v[36:37],0,v[38:39]
v_fma_mixhi_f16 v8,v47,s99,0
v_mfma_f32_16x16x32_bf16 v[32:35],a[208:211],a[16:19],v[32:35]
s_waitcnt lgkmcnt(4)
v_lshl_add_u64 v[36:37],v[56:57],0,v[58:59]
v_lshlrev_b32_sdwa v29,1,v29 dst_sel:DWORD dst_unused:UNUSED_PAD src0_sel:DWORD src1_sel:WORD_1
v_pk_fma_f16 v9,v8,v8,s54
v_mfma_f32_16x16x32_bf16 v[32:35],a[212:215],a[0:3],v[32:35]
ds_read_u16 v29,v29
v_lshlrev_b32_sdwa v31,1,v31 dst_sel:DWORD dst_unused:UNUSED_PAD src0_sel:DWORD src1_sel:WORD_1
v_mfma_f32_16x16x32_bf16 v[56:59],a[212:215],a[4:7],v[20:23]
s_waitcnt lgkmcnt(3)
ds_read_u16_d16_hi v31,v31
v_or_b32 v38,v52,v54
v_pk_mul_f16 v9,v8,v9
v_mfma_f32_16x16x32_bf16 v[56:59],a[208:211],a[20:23],v[56:59]
v_cvt_pk_bf16_f32 v39,v53,v55
v_lshlrev_b32_sdwa v24,1,v24 dst_sel:DWORD dst_unused:UNUSED_PAD src0_sel:DWORD src1_sel:WORD_1
v_mfma_f32_16x16x32_bf16 v[56:59],a[204:207],a[36:39],v[56:59]
s_barrier
v_exp_f16 v8,v9
v_mfma_f32_16x16x32_bf16 v[52:55],a[200:203],a[36:39],v[16:19]
ds_read_u16 v24,v24
v_lshlrev_b32_sdwa v26,1,v26 dst_sel:DWORD dst_unused:UNUSED_PAD src0_sel:DWORD src1_sel:WORD_1
v_mfma_f32_16x16x32_bf16 v[52:55],a[192:195],a[20:23],v[52:55]
s_add_u32 m0,s58,6160
ds_read_u16_d16_hi v26,v26
v_lshlrev_b32_sdwa v25,1,v25 dst_sel:DWORD dst_unused:UNUSED_PAD src0_sel:DWORD src1_sel:WORD_1
v_mfma_f32_16x16x32_bf16 v[52:55],a[196:199],a[4:7],v[52:55]
s_add_u32 s69,s69,s59
v_exp_f16_sdwa v8,v9 dst_sel:WORD_1 dst_unused:UNUSED_PRESERVE src0_sel:WORD_1
v_mfma_f32_16x16x32_bf16 v[232:235],a[232:235],v[40:43],v[232:235]
buffer_load_dwordx4 v5,s[44:47],s69 offen offset:0 lds
v_lshlrev_b32_sdwa v27,1,v27 dst_sel:DWORD dst_unused:UNUSED_PAD src0_sel:DWORD src1_sel:WORD_1
v_mfma_f32_16x16x32_bf16 v[236:239],a[232:235],v[36:39],v[236:239]
ds_read_u16 v25,v25
v_lshlrev_b32_sdwa v48,1,v48 dst_sel:DWORD dst_unused:UNUSED_PAD src0_sel:DWORD src1_sel:WORD_1
v_mfma_f32_16x16x32_bf16 v[164:167],a[244:247],v[36:39],v[164:167]
ds_read_u16_d16_hi v27,v27
v_pk_fma_f16 v8,v8,s55,s55
v_mfma_f32_16x16x32_bf16 v[160:163],a[244:247],v[40:43],v[160:163]
v_rcp_f16 v9,v8
v_lshlrev_b32_sdwa v50,1,v50 dst_sel:DWORD dst_unused:UNUSED_PAD src0_sel:DWORD src1_sel:WORD_1
v_mfma_f32_16x16x32_bf16 v[240:243],a[240:243],v[40:43],v[240:243]
s_add_u32 s69,s69,s53
ds_read_u16 v48,v48
v_lshlrev_b32_sdwa v49,1,v49 dst_sel:DWORD dst_unused:UNUSED_PAD src0_sel:DWORD src1_sel:WORD_1
v_mfma_f32_16x16x32_bf16 v[244:247],a[240:243],v[36:39],v[244:247]
buffer_load_dwordx4 v5,s[44:47],s69 offen offset:1024 lds
ds_read_u16_d16_hi v50,v50
v_mfma_f32_16x16x32_bf16 v[180:183],a[228:231],v[36:39],v[180:183]
ds_read_u16 v49,v49
v_lshlrev_b32_sdwa v51,1,v51 dst_sel:DWORD dst_unused:UNUSED_PAD src0_sel:DWORD src1_sel:WORD_1
v_mfma_f32_16x16x32_bf16 v[176:179],a[228:231],v[40:43],v[176:179]
v_rcp_f16_sdwa v9,v8 dst_sel:WORD_1 dst_unused:UNUSED_PRESERVE src0_sel:WORD_1
v_mfma_f32_16x16x32_bf16 v[248:251],a[248:251],v[40:43],v[248:251]
ds_read_u16_d16_hi v51,v51
v_lshlrev_b32_sdwa v44,1,v44 dst_sel:DWORD dst_unused:UNUSED_PAD src0_sel:DWORD src1_sel:WORD_1
v_mfma_f32_16x16x32_bf16 v[252:255],a[248:251],v[36:39],v[252:255]
s_add_u32 s69,s69,s53
ds_read_u16 v44,v44
v_lshlrev_b32_sdwa v46,1,v46 dst_sel:DWORD dst_unused:UNUSED_PAD src0_sel:DWORD src1_sel:WORD_1
v_mfma_f32_16x16x32_bf16 v[196:199],a[236:239],v[36:39],v[196:199]
buffer_load_dwordx4 v5,s[44:47],s69 offen offset:2048 lds
v_fma_mix_f32 v45,v45,v9,0 op_sel_hi:[0,1,0]
v_mfma_f32_16x16x32_bf16 v[192:195],a[236:239],v[40:43],v[192:195]
ds_read_u16_d16_hi v46,v46
v_fma_mix_f32 v47,v47,v9,0 op_sel:[0,1,0] op_sel_hi:[0,1,0]
v_mfma_f32_16x16x32_bf16 v[36:39],a[196:199],a[8:11],v[16:19]
ds_read_b64_tr_b16 a[252:253],v3 offset:12320
ds_read_b64_tr_b16 a[254:255],v3 offset:13856
v_lshlrev_b32_sdwa v60,1,v60 dst_sel:DWORD dst_unused:UNUSED_PAD src0_sel:DWORD src1_sel:WORD_1
v_mfma_f32_16x16x32_bf16 v[36:39],a[192:195],a[24:27],v[36:39]
ds_read_u16 v60,v60
ds_read_b64_tr_b16 a[224:225],v3 offset:12328
v_fma_mixlo_f16 v8,v57,s99,0
v_mfma_f32_16x16x32_bf16 v[36:39],a[200:203],a[40:43],v[36:39]
s_waitcnt lgkmcnt(8)
ds_read_b64_tr_b16 a[226:227],v3 offset:13864
v_lshl_add_u64 v[28:29],v[28:29],0,v[30:31]
v_lshlrev_b32_sdwa v62,1,v62 dst_sel:DWORD dst_unused:UNUSED_PAD src0_sel:DWORD src1_sel:WORD_1
v_mfma_f32_16x16x32_bf16 v[40:43],a[204:207],a[40:43],v[20:23]
ds_read_u16_d16_hi v62,v62
v_lshl_add_u64 v[30:31],v[24:25],0,v[26:27]
v_fma_mixhi_f16 v8,v59,s99,0
v_mfma_f32_16x16x32_bf16 v[40:43],a[208:211],a[24:27],v[40:43]
s_waitcnt lgkmcnt(8)
ds_read_b64_tr_b16 a[220:221],v1 offset:12320
v_lshl_add_u64 v[24:25],v[48:49],0,v[50:51]
v_lshlrev_b32_sdwa v61,1,v61 dst_sel:DWORD dst_unused:UNUSED_PAD src0_sel:DWORD src1_sel:WORD_1
v_pk_fma_f16 v9,v8,v8,s54
v_mfma_f32_16x16x32_bf16 v[40:43],a[212:215],a[8:11],v[40:43]
ds_read_u16 v61,v61
v_lshlrev_b32_sdwa v63,1,v63 dst_sel:DWORD dst_unused:UNUSED_PAD src0_sel:DWORD src1_sel:WORD_1
v_mfma_f32_16x16x32_bf16 v[48:51],a[212:215],a[12:15],v[20:23]
s_waitcnt lgkmcnt(8)
ds_read_u16_d16_hi v63,v63
v_or_b32 v26,v44,v46
v_pk_mul_f16 v9,v8,v9
v_mfma_f32_16x16x32_bf16 v[48:51],a[208:211],a[28:31],v[48:51]
ds_read_b64_tr_b16 a[222:223],v1 offset:13344
v_cvt_pk_bf16_f32 v27,v45,v47
v_lshlrev_b32_sdwa v32,1,v32 dst_sel:DWORD dst_unused:UNUSED_PAD src0_sel:DWORD src1_sel:WORD_1
v_mfma_f32_16x16x32_bf16 v[48:51],a[204:207],a[44:47],v[48:51]
v_exp_f16 v8,v9
v_mfma_f32_16x16x32_bf16 v[44:47],a[200:203],a[44:47],v[16:19]
ds_read_u16 v32,v32
v_lshlrev_b32_sdwa v34,1,v34 dst_sel:DWORD dst_unused:UNUSED_PAD src0_sel:DWORD src1_sel:WORD_1
v_mfma_f32_16x16x32_bf16 v[44:47],a[192:195],a[28:31],v[44:47]
ds_read_u16_d16_hi v34,v34
v_lshlrev_b32_sdwa v33,1,v33 dst_sel:DWORD dst_unused:UNUSED_PAD src0_sel:DWORD src1_sel:WORD_1
v_mfma_f32_16x16x32_bf16 v[44:47],a[196:199],a[12:15],v[44:47]
v_exp_f16_sdwa v8,v9 dst_sel:WORD_1 dst_unused:UNUSED_PRESERVE src0_sel:WORD_1
v_mfma_f32_16x16x32_bf16 v[208:211],a[232:235],v[28:31],v[208:211]
ds_read_u16 v33,v33
v_lshlrev_b32_sdwa v35,1,v35 dst_sel:DWORD dst_unused:UNUSED_PAD src0_sel:DWORD src1_sel:WORD_1
v_mfma_f32_16x16x32_bf16 v[212:215],a[232:235],v[24:27],v[212:215]
ds_read_u16_d16_hi v35,v35
v_lshlrev_b32_sdwa v52,1,v52 dst_sel:DWORD dst_unused:UNUSED_PAD src0_sel:DWORD src1_sel:WORD_1
v_mfma_f32_16x16x32_bf16 v[172:175],a[244:247],v[24:27],v[172:175]
ds_read_u16 v52,v52
v_pk_fma_f16 v8,v8,s55,s55
v_mfma_f32_16x16x32_bf16 v[168:171],a[244:247],v[28:31],v[168:171]
ds_read_b64_tr_b16 a[232:233],v3 offset:12384
v_rcp_f16 v9,v8
v_lshlrev_b32_sdwa v54,1,v54 dst_sel:DWORD dst_unused:UNUSED_PAD src0_sel:DWORD src1_sel:WORD_1
v_mfma_f32_16x16x32_bf16 v[216:219],a[240:243],v[28:31],v[216:219]
ds_read_u16_d16_hi v54,v54
v_lshlrev_b32_sdwa v53,1,v53 dst_sel:DWORD dst_unused:UNUSED_PAD src0_sel:DWORD src1_sel:WORD_1
v_mfma_f32_16x16x32_bf16 v[220:223],a[240:243],v[24:27],v[220:223]
ds_read_u16 v53,v53
ds_read_b64_tr_b16 a[234:235],v3 offset:13920
v_mfma_f32_16x16x32_bf16 v[188:191],a[228:231],v[24:27],v[188:191]
ds_read_b64_tr_b16 a[244:245],v3 offset:12392
v_lshlrev_b32_sdwa v55,1,v55 dst_sel:DWORD dst_unused:UNUSED_PAD src0_sel:DWORD src1_sel:WORD_1
v_mfma_f32_16x16x32_bf16 v[184:187],a[228:231],v[28:31],v[184:187]
ds_read_u16_d16_hi v55,v55
v_rcp_f16_sdwa v9,v8 dst_sel:WORD_1 dst_unused:UNUSED_PRESERVE src0_sel:WORD_1
v_mfma_f32_16x16x32_bf16 v[224:227],a[248:251],v[28:31],v[224:227]
ds_read_b64_tr_b16 a[246:247],v3 offset:13928
ds_read_b64_tr_b16 a[240:241],v3 offset:12448
v_lshlrev_b32_sdwa v56,1,v56 dst_sel:DWORD dst_unused:UNUSED_PAD src0_sel:DWORD src1_sel:WORD_1
v_mfma_f32_16x16x32_bf16 v[228:231],a[248:251],v[24:27],v[228:231]
ds_read_u16 v56,v56
v_lshlrev_b32_sdwa v58,1,v58 dst_sel:DWORD dst_unused:UNUSED_PAD src0_sel:DWORD src1_sel:WORD_1
v_mfma_f32_16x16x32_bf16 v[204:207],a[236:239],v[24:27],v[204:207]
ds_read_u16_d16_hi v58,v58
v_fma_mix_f32 v57,v57,v9,0 op_sel_hi:[0,1,0]
v_mfma_f32_16x16x32_bf16 v[200:203],a[236:239],v[28:31],v[200:203]
ds_read_b64_tr_b16 a[242:243],v3 offset:13984
v_fma_mix_f32 v59,v59,v9,0 op_sel:[0,1,0] op_sel_hi:[0,1,0]
v_mfma_f32_16x16x32_bf16 v[24:27],a[196:199],a[48:51],v[16:19]
ds_read_b64_tr_b16 a[228:229],v3 offset:12456
v_lshlrev_b32_sdwa v36,1,v36 dst_sel:DWORD dst_unused:UNUSED_PAD src0_sel:DWORD src1_sel:WORD_1
v_mfma_f32_16x16x32_bf16 v[24:27],a[192:195],a[64:67],v[24:27]
ds_read_b64_tr_b16 a[230:231],v3 offset:13992
v_fma_mixlo_f16 v8,v49,s99,0
v_mfma_f32_16x16x32_bf16 v[24:27],a[200:203],a[80:83],v[24:27]
s_waitcnt lgkmcnt(9)
ds_read_u16 v36,v36
v_lshl_add_u64 v[60:61],v[60:61],0,v[62:63]
v_lshlrev_b32_sdwa v38,1,v38 dst_sel:DWORD dst_unused:UNUSED_PAD src0_sel:DWORD src1_sel:WORD_1
v_mfma_f32_16x16x32_bf16 v[28:31],a[204:207],a[80:83],v[20:23]
ds_read_u16_d16_hi v38,v38
v_lshl_add_u64 v[62:63],v[32:33],0,v[34:35]
v_fma_mixhi_f16 v8,v51,s99,0
v_mfma_f32_16x16x32_bf16 v[28:31],a[208:211],a[64:67],v[28:31]
s_waitcnt lgkmcnt(9)
ds_read_b64_tr_b16 a[216:217],v1 offset:14368
v_lshl_add_u64 v[32:33],v[52:53],0,v[54:55]
v_lshlrev_b32_sdwa v37,1,v37 dst_sel:DWORD dst_unused:UNUSED_PAD src0_sel:DWORD src1_sel:WORD_1
v_pk_fma_f16 v9,v8,v8,s54
v_mfma_f32_16x16x32_bf16 v[28:31],a[212:215],a[48:51],v[28:31]
ds_read_u16 v37,v37
v_lshlrev_b32_sdwa v39,1,v39 dst_sel:DWORD dst_unused:UNUSED_PAD src0_sel:DWORD src1_sel:WORD_1
v_mfma_f32_16x16x32_bf16 v[52:55],a[212:215],a[52:55],v[20:23]
s_waitcnt lgkmcnt(7)
ds_read_u16_d16_hi v39,v39
v_or_b32 v34,v56,v58
v_pk_mul_f16 v9,v8,v9
v_mfma_f32_16x16x32_bf16 v[52:55],a[208:211],a[68:71],v[52:55]
ds_read_b64_tr_b16 a[218:219],v1 offset:15392
v_cvt_pk_bf16_f32 v35,v57,v59
v_lshlrev_b32_sdwa v40,1,v40 dst_sel:DWORD dst_unused:UNUSED_PAD src0_sel:DWORD src1_sel:WORD_1
v_mfma_f32_16x16x32_bf16 v[52:55],a[204:207],a[84:87],v[52:55]
v_exp_f16 v8,v9
v_mfma_f32_16x16x32_bf16 v[56:59],a[200:203],a[84:87],v[16:19]
ds_read_u16 v40,v40
v_lshlrev_b32_sdwa v42,1,v42 dst_sel:DWORD dst_unused:UNUSED_PAD src0_sel:DWORD src1_sel:WORD_1
v_mfma_f32_16x16x32_bf16 v[56:59],a[192:195],a[68:71],v[56:59]
ds_read_u16_d16_hi v42,v42
v_lshlrev_b32_sdwa v41,1,v41 dst_sel:DWORD dst_unused:UNUSED_PAD src0_sel:DWORD src1_sel:WORD_1
v_mfma_f32_16x16x32_bf16 v[56:59],a[196:199],a[52:55],v[56:59]
v_exp_f16_sdwa v8,v9 dst_sel:WORD_1 dst_unused:UNUSED_PRESERVE src0_sel:WORD_1
v_mfma_f32_16x16x32_bf16 v[136:139],a[252:255],v[60:63],v[136:139]
ds_read_u16 v41,v41
v_lshlrev_b32_sdwa v43,1,v43 dst_sel:DWORD dst_unused:UNUSED_PAD src0_sel:DWORD src1_sel:WORD_1
v_mfma_f32_16x16x32_bf16 v[140:143],a[252:255],v[32:35],v[140:143]
ds_read_u16_d16_hi v43,v43
v_lshlrev_b32_sdwa v44,1,v44 dst_sel:DWORD dst_unused:UNUSED_PAD src0_sel:DWORD src1_sel:WORD_1
v_mfma_f32_16x16x32_bf16 v[68:71],a[224:227],v[32:35],v[68:71]
ds_read_u16 v44,v44
v_pk_fma_f16 v8,v8,s55,s55
v_mfma_f32_16x16x32_bf16 v[64:67],a[224:227],v[60:63],v[64:67]
v_rcp_f16 v9,v8
v_lshlrev_b32_sdwa v46,1,v46 dst_sel:DWORD dst_unused:UNUSED_PAD src0_sel:DWORD src1_sel:WORD_1
v_mfma_f32_16x16x32_bf16 v[144:147],a[232:235],v[60:63],v[144:147]
ds_read_u16_d16_hi v46,v46
v_lshlrev_b32_sdwa v45,1,v45 dst_sel:DWORD dst_unused:UNUSED_PAD src0_sel:DWORD src1_sel:WORD_1
v_mfma_f32_16x16x32_bf16 v[148:151],a[232:235],v[32:35],v[148:151]
ds_read_u16 v45,v45
v_lshlrev_b32_sdwa v47,1,v47 dst_sel:DWORD dst_unused:UNUSED_PAD src0_sel:DWORD src1_sel:WORD_1
v_mfma_f32_16x16x32_bf16 v[84:87],a[244:247],v[32:35],v[84:87]
ds_read_u16_d16_hi v47,v47
v_lshlrev_b32_sdwa v48,1,v48 dst_sel:DWORD dst_unused:UNUSED_PAD src0_sel:DWORD src1_sel:WORD_1
v_mfma_f32_16x16x32_bf16 v[80:83],a[244:247],v[60:63],v[80:83]
s_waitcnt lgkmcnt(6)
v_rcp_f16_sdwa v9,v8 dst_sel:WORD_1 dst_unused:UNUSED_PRESERVE src0_sel:WORD_1
v_mfma_f32_16x16x32_bf16 v[152:155],a[240:243],v[60:63],v[152:155]
ds_read_u16 v48,v48
v_lshlrev_b32_sdwa v50,1,v50 dst_sel:DWORD dst_unused:UNUSED_PAD src0_sel:DWORD src1_sel:WORD_1
v_mfma_f32_16x16x32_bf16 v[156:159],a[240:243],v[32:35],v[156:159]
ds_read_u16_d16_hi v50,v50
v_fma_mix_f32 v49,v49,v9,0 op_sel_hi:[0,1,0]
v_mfma_f32_16x16x32_bf16 v[100:103],a[228:231],v[32:35],v[100:103]
v_fma_mix_f32 v51,v51,v9,0 op_sel:[0,1,0] op_sel_hi:[0,1,0]
v_mfma_f32_16x16x32_bf16 v[96:99],a[228:231],v[60:63],v[96:99]
v_mfma_f32_16x16x32_bf16 v[32:35],a[196:199],a[56:59],v[16:19]
v_lshlrev_b32_sdwa v24,1,v24 dst_sel:DWORD dst_unused:UNUSED_PAD src0_sel:DWORD src1_sel:WORD_1
v_fma_mixlo_f16 v8,v53,s99,0
v_mfma_f32_16x16x32_bf16 v[32:35],a[192:195],a[72:75],v[32:35]
ds_read_u16 v24,v24
v_lshlrev_b32_sdwa v26,1,v26 dst_sel:DWORD dst_unused:UNUSED_PAD src0_sel:DWORD src1_sel:WORD_1
v_mfma_f32_16x16x32_bf16 v[32:35],a[200:203],a[88:91],v[32:35]
ds_read_u16_d16_hi v26,v26
v_lshl_add_u64 v[36:37],v[36:37],0,v[38:39]
v_mfma_f32_16x16x32_bf16 v[60:63],a[204:207],a[88:91],v[20:23]
s_waitcnt lgkmcnt(4)
v_lshl_add_u64 v[38:39],v[40:41],0,v[42:43]
v_fma_mixhi_f16 v8,v55,s99,0
v_mfma_f32_16x16x32_bf16 v[60:63],a[208:211],a[72:75],v[60:63]
v_lshl_add_u64 v[40:41],v[44:45],0,v[46:47]
v_lshlrev_b32_sdwa v25,1,v25 dst_sel:DWORD dst_unused:UNUSED_PAD src0_sel:DWORD src1_sel:WORD_1
v_pk_fma_f16 v9,v8,v8,s54
v_mfma_f32_16x16x32_bf16 v[60:63],a[212:215],a[56:59],v[60:63]
ds_read_u16 v25,v25
v_lshlrev_b32_sdwa v27,1,v27 dst_sel:DWORD dst_unused:UNUSED_PAD src0_sel:DWORD src1_sel:WORD_1
v_mfma_f32_16x16x32_bf16 v[44:47],a[212:215],a[60:63],v[20:23]
s_waitcnt lgkmcnt(3)
ds_read_b128 v[20:23],v4 offset:928
v_or_b32 v42,v48,v50
v_pk_mul_f16 v9,v8,v9
v_mfma_f32_16x16x32_bf16 v[44:47],a[208:211],a[76:79],v[44:47]
ds_read_u16_d16_hi v27,v27
v_cvt_pk_bf16_f32 v43,v49,v51
v_lshlrev_b32_sdwa v28,1,v28 dst_sel:DWORD dst_unused:UNUSED_PAD src0_sel:DWORD src1_sel:WORD_1
v_mfma_f32_16x16x32_bf16 v[44:47],a[204:207],a[92:95],v[44:47]
ds_read_b64_tr_b16 a[212:213],v1 offset:16416
v_exp_f16 v8,v9
v_mfma_f32_16x16x32_bf16 v[48:51],a[200:203],a[92:95],v[16:19]
ds_read_b128 v[16:19],v4 offset:896
ds_read_b64_tr_b16 a[214:215],v1 offset:17440
v_lshlrev_b32_sdwa v30,1,v30 dst_sel:DWORD dst_unused:UNUSED_PAD src0_sel:DWORD src1_sel:WORD_1
v_mfma_f32_16x16x32_bf16 v[48:51],a[192:195],a[76:79],v[48:51]
ds_read_b64_tr_b16 a[208:209],v2 offset:16416
ds_read_b64_tr_b16 a[210:211],v2 offset:17440
v_lshlrev_b32_sdwa v29,1,v29 dst_sel:DWORD dst_unused:UNUSED_PAD src0_sel:DWORD src1_sel:WORD_1
v_mfma_f32_16x16x32_bf16 v[48:51],a[196:199],a[60:63],v[48:51]
ds_read_u16 v28,v28
v_exp_f16_sdwa v8,v9 dst_sel:WORD_1 dst_unused:UNUSED_PRESERVE src0_sel:WORD_1
v_mfma_f32_16x16x32_bf16 v[112:115],a[252:255],v[36:39],v[112:115]
ds_read_u16_d16_hi v30,v30
v_lshlrev_b32_sdwa v31,1,v31 dst_sel:DWORD dst_unused:UNUSED_PAD src0_sel:DWORD src1_sel:WORD_1
v_mfma_f32_16x16x32_bf16 v[116:119],a[252:255],v[40:43],v[116:119]
ds_read_u16 v29,v29
v_lshlrev_b32_sdwa v56,1,v56 dst_sel:DWORD dst_unused:UNUSED_PAD src0_sel:DWORD src1_sel:WORD_1
v_mfma_f32_16x16x32_bf16 v[76:79],a[224:227],v[40:43],v[76:79]
ds_read_u16_d16_hi v31,v31
v_pk_fma_f16 v8,v8,s55,s55
v_mfma_f32_16x16x32_bf16 v[72:75],a[224:227],v[36:39],v[72:75]
v_rcp_f16 v9,v8
v_lshlrev_b32_sdwa v58,1,v58 dst_sel:DWORD dst_unused:UNUSED_PAD src0_sel:DWORD src1_sel:WORD_1
v_mfma_f32_16x16x32_bf16 v[120:123],a[232:235],v[36:39],v[120:123]
ds_read_b64_tr_b16 a[204:205],v2 offset:14368
v_lshlrev_b32_sdwa v57,1,v57 dst_sel:DWORD dst_unused:UNUSED_PAD src0_sel:DWORD src1_sel:WORD_1
v_mfma_f32_16x16x32_bf16 v[124:127],a[232:235],v[40:43],v[124:127]
ds_read_b64_tr_b16 a[206:207],v2 offset:15392
v_lshlrev_b32_sdwa v59,1,v59 dst_sel:DWORD dst_unused:UNUSED_PAD src0_sel:DWORD src1_sel:WORD_1
v_mfma_f32_16x16x32_bf16 v[92:95],a[244:247],v[40:43],v[92:95]
ds_read_u16 v56,v56
v_lshlrev_b32_sdwa v52,1,v52 dst_sel:DWORD dst_unused:UNUSED_PAD src0_sel:DWORD src1_sel:WORD_1
v_mfma_f32_16x16x32_bf16 v[88:91],a[244:247],v[36:39],v[88:91]
ds_read_u16_d16_hi v58,v58
v_rcp_f16_sdwa v9,v8 dst_sel:WORD_1 dst_unused:UNUSED_PRESERVE src0_sel:WORD_1
v_mfma_f32_16x16x32_bf16 v[128:131],a[240:243],v[36:39],v[128:131]
TE12_%=:
s_cbranch_execz TP10846_%=
ds_read_u16 v57,v57
ds_read_u16_d16_hi v59,v59
v_mfma_f32_16x16x32_bf16 v[132:135],a[240:243],v[40:43],v[132:135]
ds_read_b64_tr_b16 a[200:201],v2 offset:12320
ds_read_b64_tr_b16 a[202:203],v2 offset:13344
v_lshlrev_b32_sdwa v54,1,v54 dst_sel:DWORD dst_unused:UNUSED_PAD src0_sel:DWORD src1_sel:WORD_1
v_mfma_f32_16x16x32_bf16 v[108:111],a[228:231],v[40:43],v[108:111]
ds_read_u16 v52,v52
v_fma_mix_f32 v53,v53,v9,0 op_sel_hi:[0,1,0]
v_mfma_f32_16x16x32_bf16 v[104:107],a[228:231],v[36:39],v[104:107]
s_waitcnt lgkmcnt(9)
ds_read_u16_d16_hi v54,v54
v_fma_mix_f32 v55,v55,v9,0 op_sel:[0,1,0] op_sel_hi:[0,1,0]
v_mfma_f32_16x16x32_bf16 v[40:43],a[220:223],a[0:3],v[16:19]
v_lshlrev_b32_sdwa v32,1,v32 dst_sel:DWORD dst_unused:UNUSED_PAD src0_sel:DWORD src1_sel:WORD_1
v_fma_mixlo_f16 v8,v45,s99,0
v_mfma_f32_16x16x32_bf16 v[40:43],a[216:219],a[16:19],v[40:43]
ds_read_u16 v32,v32
v_lshlrev_b32_sdwa v34,1,v34 dst_sel:DWORD dst_unused:UNUSED_PAD src0_sel:DWORD src1_sel:WORD_1
v_mfma_f32_16x16x32_bf16 v[40:43],a[212:215],a[32:35],v[40:43]
ds_read_u16_d16_hi v34,v34
v_lshl_add_u64 v[24:25],v[24:25],0,v[26:27]
v_mfma_f32_16x16x32_bf16 v[36:39],a[208:211],a[32:35],v[20:23]
s_waitcnt lgkmcnt(9)
v_lshl_add_u64 v[26:27],v[28:29],0,v[30:31]
v_fma_mixhi_f16 v8,v47,s99,0
v_mfma_f32_16x16x32_bf16 v[36:39],a[204:207],a[16:19],v[36:39]
s_waitcnt lgkmcnt(4)
v_lshl_add_u64 v[28:29],v[56:57],0,v[58:59]
v_lshlrev_b32_sdwa v33,1,v33 dst_sel:DWORD dst_unused:UNUSED_PAD src0_sel:DWORD src1_sel:WORD_1
v_pk_fma_f16 v9,v8,v8,s54
v_mfma_f32_16x16x32_bf16 v[36:39],a[200:203],a[0:3],v[36:39]
ds_read_u16 v33,v33
v_lshlrev_b32_sdwa v35,1,v35 dst_sel:DWORD dst_unused:UNUSED_PAD src0_sel:DWORD src1_sel:WORD_1
v_mfma_f32_16x16x32_bf16 v[56:59],a[200:203],a[4:7],v[20:23]
s_waitcnt lgkmcnt(3)
ds_read_u16_d16_hi v35,v35
v_or_b32 v30,v52,v54
v_pk_mul_f16 v9,v8,v9
v_mfma_f32_16x16x32_bf16 v[56:59],a[204:207],a[20:23],v[56:59]
s_waitcnt vmcnt(18)
v_cvt_pk_bf16_f32 v31,v53,v55
v_lshlrev_b32_sdwa v60,1,v60 dst_sel:DWORD dst_unused:UNUSED_PAD src0_sel:DWORD src1_sel:WORD_1
v_mfma_f32_16x16x32_bf16 v[56:59],a[208:211],a[36:39],v[56:59]
s_barrier
v_exp_f16 v8,v9
v_mfma_f32_16x16x32_bf16 v[52:55],a[212:215],a[36:39],v[16:19]
ds_read_u16 v60,v60
v_lshlrev_b32_sdwa v62,1,v62 dst_sel:DWORD dst_unused:UNUSED_PAD src0_sel:DWORD src1_sel:WORD_1
v_mfma_f32_16x16x32_bf16 v[52:55],a[216:219],a[20:23],v[52:55]
s_add_u32 m0,s58,12320
ds_read_u16_d16_hi v62,v62
v_lshlrev_b32_sdwa v61,1,v61 dst_sel:DWORD dst_unused:UNUSED_PAD src0_sel:DWORD src1_sel:WORD_1
v_mfma_f32_16x16x32_bf16 v[52:55],a[220:223],a[4:7],v[52:55]
s_add_u32 s69,s69,s59
v_exp_f16_sdwa v8,v9 dst_sel:WORD_1 dst_unused:UNUSED_PRESERVE src0_sel:WORD_1
v_mfma_f32_16x16x32_bf16 v[232:235],a[252:255],v[24:27],v[232:235]
buffer_load_dwordx4 v5,s[44:47],s69 offen offset:0 lds
v_lshlrev_b32_sdwa v63,1,v63 dst_sel:DWORD dst_unused:UNUSED_PAD src0_sel:DWORD src1_sel:WORD_1
v_mfma_f32_16x16x32_bf16 v[236:239],a[252:255],v[28:31],v[236:239]
ds_read_u16 v61,v61
v_lshlrev_b32_sdwa v48,1,v48 dst_sel:DWORD dst_unused:UNUSED_PAD src0_sel:DWORD src1_sel:WORD_1
v_mfma_f32_16x16x32_bf16 v[164:167],a[224:227],v[28:31],v[164:167]
ds_read_u16_d16_hi v63,v63
v_pk_fma_f16 v8,v8,s55,s55
v_mfma_f32_16x16x32_bf16 v[160:163],a[224:227],v[24:27],v[160:163]
v_rcp_f16 v9,v8
v_lshlrev_b32_sdwa v50,1,v50 dst_sel:DWORD dst_unused:UNUSED_PAD src0_sel:DWORD src1_sel:WORD_1
v_mfma_f32_16x16x32_bf16 v[240:243],a[232:235],v[24:27],v[240:243]
s_add_u32 s69,s69,s53
ds_read_u16 v48,v48
v_lshlrev_b32_sdwa v49,1,v49 dst_sel:DWORD dst_unused:UNUSED_PAD src0_sel:DWORD src1_sel:WORD_1
v_mfma_f32_16x16x32_bf16 v[244:247],a[232:235],v[28:31],v[244:247]
buffer_load_dwordx4 v5,s[44:47],s69 offen offset:1024 lds
ds_read_u16_d16_hi v50,v50
v_mfma_f32_16x16x32_bf16 v[180:183],a[244:247],v[28:31],v[180:183]
ds_read_u16 v49,v49
v_lshlrev_b32_sdwa v51,1,v51 dst_sel:DWORD dst_unused:UNUSED_PAD src0_sel:DWORD src1_sel:WORD_1
v_mfma_f32_16x16x32_bf16 v[176:179],a[244:247],v[24:27],v[176:179]
v_rcp_f16_sdwa v9,v8 dst_sel:WORD_1 dst_unused:UNUSED_PRESERVE src0_sel:WORD_1
v_mfma_f32_16x16x32_bf16 v[248:251],a[240:243],v[24:27],v[248:251]
ds_read_u16_d16_hi v51,v51
v_lshlrev_b32_sdwa v44,1,v44 dst_sel:DWORD dst_unused:UNUSED_PAD src0_sel:DWORD src1_sel:WORD_1
v_mfma_f32_16x16x32_bf16 v[252:255],a[240:243],v[28:31],v[252:255]
s_add_u32 s69,s69,s53
ds_read_u16 v44,v44
v_lshlrev_b32_sdwa v46,1,v46 dst_sel:DWORD dst_unused:UNUSED_PAD src0_sel:DWORD src1_sel:WORD_1
v_mfma_f32_16x16x32_bf16 v[196:199],a[228:231],v[28:31],v[196:199]
buffer_load_dwordx4 v5,s[44:47],s69 offen offset:2048 lds
v_fma_mix_f32 v45,v45,v9,0 op_sel_hi:[0,1,0]
v_mfma_f32_16x16x32_bf16 v[192:195],a[228:231],v[24:27],v[192:195]
ds_read_u16_d16_hi v46,v46
v_fma_mix_f32 v47,v47,v9,0 op_sel:[0,1,0] op_sel_hi:[0,1,0]
v_mfma_f32_16x16x32_bf16 v[28:31],a[220:223],a[8:11],v[16:19]
ds_read_b64_tr_b16 a[248:249],v3 offset:18480
ds_read_b64_tr_b16 a[250:251],v3 offset:20016
v_lshlrev_b32_sdwa v40,1,v40 dst_sel:DWORD dst_unused:UNUSED_PAD src0_sel:DWORD src1_sel:WORD_1
v_mfma_f32_16x16x32_bf16 v[28:31],a[216:219],a[24:27],v[28:31]
ds_read_u16 v40,v40
ds_read_b64_tr_b16 a[236:237],v3 offset:18488
v_fma_mixlo_f16 v8,v57,s99,0
v_mfma_f32_16x16x32_bf16 v[28:31],a[212:215],a[40:43],v[28:31]
s_waitcnt lgkmcnt(8)
ds_read_b64_tr_b16 a[238:239],v3 offset:20024
v_lshl_add_u64 v[32:33],v[32:33],0,v[34:35]
v_lshlrev_b32_sdwa v42,1,v42 dst_sel:DWORD dst_unused:UNUSED_PAD src0_sel:DWORD src1_sel:WORD_1
v_mfma_f32_16x16x32_bf16 v[24:27],a[208:211],a[40:43],v[20:23]
ds_read_u16_d16_hi v42,v42
v_lshl_add_u64 v[34:35],v[60:61],0,v[62:63]
v_fma_mixhi_f16 v8,v59,s99,0
v_mfma_f32_16x16x32_bf16 v[24:27],a[204:207],a[24:27],v[24:27]
s_waitcnt lgkmcnt(8)
ds_read_b64_tr_b16 a[192:193],v1 offset:18480
v_lshl_add_u64 v[60:61],v[48:49],0,v[50:51]
v_lshlrev_b32_sdwa v41,1,v41 dst_sel:DWORD dst_unused:UNUSED_PAD src0_sel:DWORD src1_sel:WORD_1
v_pk_fma_f16 v9,v8,v8,s54
v_mfma_f32_16x16x32_bf16 v[24:27],a[200:203],a[8:11],v[24:27]
ds_read_u16 v41,v41
v_lshlrev_b32_sdwa v43,1,v43 dst_sel:DWORD dst_unused:UNUSED_PAD src0_sel:DWORD src1_sel:WORD_1
v_mfma_f32_16x16x32_bf16 v[48:51],a[200:203],a[12:15],v[20:23]
s_waitcnt lgkmcnt(8)
ds_read_u16_d16_hi v43,v43
v_or_b32 v62,v44,v46
v_pk_mul_f16 v9,v8,v9
v_mfma_f32_16x16x32_bf16 v[48:51],a[204:207],a[28:31],v[48:51]
ds_read_b64_tr_b16 a[194:195],v1 offset:19504
v_cvt_pk_bf16_f32 v63,v45,v47
v_lshlrev_b32_sdwa v36,1,v36 dst_sel:DWORD dst_unused:UNUSED_PAD src0_sel:DWORD src1_sel:WORD_1
v_mfma_f32_16x16x32_bf16 v[48:51],a[208:211],a[44:47],v[48:51]
v_exp_f16 v8,v9
v_mfma_f32_16x16x32_bf16 v[44:47],a[212:215],a[44:47],v[16:19]
ds_read_u16 v36,v36
v_lshlrev_b32_sdwa v38,1,v38 dst_sel:DWORD dst_unused:UNUSED_PAD src0_sel:DWORD src1_sel:WORD_1
v_mfma_f32_16x16x32_bf16 v[44:47],a[216:219],a[28:31],v[44:47]
ds_read_u16_d16_hi v38,v38
v_lshlrev_b32_sdwa v37,1,v37 dst_sel:DWORD dst_unused:UNUSED_PAD src0_sel:DWORD src1_sel:WORD_1
v_mfma_f32_16x16x32_bf16 v[44:47],a[220:223],a[12:15],v[44:47]
v_exp_f16_sdwa v8,v9 dst_sel:WORD_1 dst_unused:UNUSED_PRESERVE src0_sel:WORD_1
v_mfma_f32_16x16x32_bf16 v[208:211],a[252:255],v[32:35],v[208:211]
ds_read_u16 v37,v37
v_lshlrev_b32_sdwa v39,1,v39 dst_sel:DWORD dst_unused:UNUSED_PAD src0_sel:DWORD src1_sel:WORD_1
v_mfma_f32_16x16x32_bf16 v[212:215],a[252:255],v[60:63],v[212:215]
ds_read_u16_d16_hi v39,v39
v_lshlrev_b32_sdwa v52,1,v52 dst_sel:DWORD dst_unused:UNUSED_PAD src0_sel:DWORD src1_sel:WORD_1
v_mfma_f32_16x16x32_bf16 v[172:175],a[224:227],v[60:63],v[172:175]
ds_read_u16 v52,v52
v_pk_fma_f16 v8,v8,s55,s55
v_mfma_f32_16x16x32_bf16 v[168:171],a[224:227],v[32:35],v[168:171]
ds_read_b64_tr_b16 a[252:253],v3 offset:18544
v_rcp_f16 v9,v8
v_lshlrev_b32_sdwa v54,1,v54 dst_sel:DWORD dst_unused:UNUSED_PAD src0_sel:DWORD src1_sel:WORD_1
v_mfma_f32_16x16x32_bf16 v[216:219],a[232:235],v[32:35],v[216:219]
ds_read_u16_d16_hi v54,v54
v_lshlrev_b32_sdwa v53,1,v53 dst_sel:DWORD dst_unused:UNUSED_PAD src0_sel:DWORD src1_sel:WORD_1
v_mfma_f32_16x16x32_bf16 v[220:223],a[232:235],v[60:63],v[220:223]
ds_read_u16 v53,v53
ds_read_b64_tr_b16 a[254:255],v3 offset:20080
v_mfma_f32_16x16x32_bf16 v[188:191],a[244:247],v[60:63],v[188:191]
ds_read_b64_tr_b16 a[224:225],v3 offset:18552
v_lshlrev_b32_sdwa v55,1,v55 dst_sel:DWORD dst_unused:UNUSED_PAD src0_sel:DWORD src1_sel:WORD_1
v_mfma_f32_16x16x32_bf16 v[184:187],a[244:247],v[32:35],v[184:187]
ds_read_u16_d16_hi v55,v55
v_rcp_f16_sdwa v9,v8 dst_sel:WORD_1 dst_unused:UNUSED_PRESERVE src0_sel:WORD_1
v_mfma_f32_16x16x32_bf16 v[224:227],a[240:243],v[32:35],v[224:227]
ds_read_b64_tr_b16 a[226:227],v3 offset:20088
ds_read_b64_tr_b16 a[232:233],v3 offset:18608
v_lshlrev_b32_sdwa v56,1,v56 dst_sel:DWORD dst_unused:UNUSED_PAD src0_sel:DWORD src1_sel:WORD_1
v_mfma_f32_16x16x32_bf16 v[228:231],a[240:243],v[60:63],v[228:231]
ds_read_u16 v56,v56
v_lshlrev_b32_sdwa v58,1,v58 dst_sel:DWORD dst_unused:UNUSED_PAD src0_sel:DWORD src1_sel:WORD_1
v_mfma_f32_16x16x32_bf16 v[204:207],a[228:231],v[60:63],v[204:207]
ds_read_u16_d16_hi v58,v58
v_fma_mix_f32 v57,v57,v9,0 op_sel_hi:[0,1,0]
v_mfma_f32_16x16x32_bf16 v[200:203],a[228:231],v[32:35],v[200:203]
ds_read_b64_tr_b16 a[234:235],v3 offset:20144
v_fma_mix_f32 v59,v59,v9,0 op_sel:[0,1,0] op_sel_hi:[0,1,0]
v_mfma_f32_16x16x32_bf16 v[60:63],a[220:223],a[48:51],v[16:19]
ds_read_b64_tr_b16 a[244:245],v3 offset:18616
v_lshlrev_b32_sdwa v28,1,v28 dst_sel:DWORD dst_unused:UNUSED_PAD src0_sel:DWORD src1_sel:WORD_1
v_mfma_f32_16x16x32_bf16 v[60:63],a[216:219],a[64:67],v[60:63]
ds_read_b64_tr_b16 a[246:247],v3 offset:20152
v_fma_mixlo_f16 v8,v49,s99,0
v_mfma_f32_16x16x32_bf16 v[60:63],a[212:215],a[80:83],v[60:63]
s_waitcnt lgkmcnt(9)
ds_read_u16 v28,v28
v_lshl_add_u64 v[40:41],v[40:41],0,v[42:43]
v_lshlrev_b32_sdwa v30,1,v30 dst_sel:DWORD dst_unused:UNUSED_PAD src0_sel:DWORD src1_sel:WORD_1
v_mfma_f32_16x16x32_bf16 v[32:35],a[208:211],a[80:83],v[20:23]
ds_read_u16_d16_hi v30,v30
v_lshl_add_u64 v[42:43],v[36:37],0,v[38:39]
v_fma_mixhi_f16 v8,v51,s99,0
v_mfma_f32_16x16x32_bf16 v[32:35],a[204:207],a[64:67],v[32:35]
s_waitcnt lgkmcnt(9)
ds_read_b64_tr_b16 a[196:197],v1 offset:20528
v_lshl_add_u64 v[36:37],v[52:53],0,v[54:55]
v_lshlrev_b32_sdwa v29,1,v29 dst_sel:DWORD dst_unused:UNUSED_PAD src0_sel:DWORD src1_sel:WORD_1
v_pk_fma_f16 v9,v8,v8,s54
v_mfma_f32_16x16x32_bf16 v[32:35],a[200:203],a[48:51],v[32:35]
ds_read_u16 v29,v29
v_lshlrev_b32_sdwa v31,1,v31 dst_sel:DWORD dst_unused:UNUSED_PAD src0_sel:DWORD src1_sel:WORD_1
v_mfma_f32_16x16x32_bf16 v[52:55],a[200:203],a[52:55],v[20:23]
s_waitcnt lgkmcnt(7)
ds_read_u16_d16_hi v31,v31
v_or_b32 v38,v56,v58
v_pk_mul_f16 v9,v8,v9
v_mfma_f32_16x16x32_bf16 v[52:55],a[204:207],a[68:71],v[52:55]
ds_read_b64_tr_b16 a[198:199],v1 offset:21552
v_cvt_pk_bf16_f32 v39,v57,v59
v_lshlrev_b32_sdwa v24,1,v24 dst_sel:DWORD dst_unused:UNUSED_PAD src0_sel:DWORD src1_sel:WORD_1
v_mfma_f32_16x16x32_bf16 v[52:55],a[208:211],a[84:87],v[52:55]
v_exp_f16 v8,v9
v_mfma_f32_16x16x32_bf16 v[56:59],a[212:215],a[84:87],v[16:19]
ds_read_u16 v24,v24
v_lshlrev_b32_sdwa v26,1,v26 dst_sel:DWORD dst_unused:UNUSED_PAD src0_sel:DWORD src1_sel:WORD_1
v_mfma_f32_16x16x32_bf16 v[56:59],a[216:219],a[68:71],v[56:59]
ds_read_u16_d16_hi v26,v26
v_lshlrev_b32_sdwa v25,1,v25 dst_sel:DWORD dst_unused:UNUSED_PAD src0_sel:DWORD src1_sel:WORD_1
v_mfma_f32_16x16x32_bf16 v[56:59],a[220:223],a[52:55],v[56:59]
v_exp_f16_sdwa v8,v9 dst_sel:WORD_1 dst_unused:UNUSED_PRESERVE src0_sel:WORD_1
v_mfma_f32_16x16x32_bf16 v[136:139],a[248:251],v[40:43],v[136:139]
ds_read_u16 v25,v25
v_lshlrev_b32_sdwa v27,1,v27 dst_sel:DWORD dst_unused:UNUSED_PAD src0_sel:DWORD src1_sel:WORD_1
v_mfma_f32_16x16x32_bf16 v[140:143],a[248:251],v[36:39],v[140:143]
ds_read_u16_d16_hi v27,v27
v_lshlrev_b32_sdwa v44,1,v44 dst_sel:DWORD dst_unused:UNUSED_PAD src0_sel:DWORD src1_sel:WORD_1
v_mfma_f32_16x16x32_bf16 v[68:71],a[236:239],v[36:39],v[68:71]
ds_read_u16 v44,v44
v_pk_fma_f16 v8,v8,s55,s55
v_mfma_f32_16x16x32_bf16 v[64:67],a[236:239],v[40:43],v[64:67]
v_rcp_f16 v9,v8
v_lshlrev_b32_sdwa v46,1,v46 dst_sel:DWORD dst_unused:UNUSED_PAD src0_sel:DWORD src1_sel:WORD_1
v_mfma_f32_16x16x32_bf16 v[144:147],a[252:255],v[40:43],v[144:147]
ds_read_u16_d16_hi v46,v46
v_lshlrev_b32_sdwa v45,1,v45 dst_sel:DWORD dst_unused:UNUSED_PAD src0_sel:DWORD src1_sel:WORD_1
v_mfma_f32_16x16x32_bf16 v[148:151],a[252:255],v[36:39],v[148:151]
ds_read_u16 v45,v45
v_lshlrev_b32_sdwa v47,1,v47 dst_sel:DWORD dst_unused:UNUSED_PAD src0_sel:DWORD src1_sel:WORD_1
v_mfma_f32_16x16x32_bf16 v[84:87],a[224:227],v[36:39],v[84:87]
ds_read_u16_d16_hi v47,v47
v_lshlrev_b32_sdwa v48,1,v48 dst_sel:DWORD dst_unused:UNUSED_PAD src0_sel:DWORD src1_sel:WORD_1
v_mfma_f32_16x16x32_bf16 v[80:83],a[224:227],v[40:43],v[80:83]
s_waitcnt lgkmcnt(6)
v_rcp_f16_sdwa v9,v8 dst_sel:WORD_1 dst_unused:UNUSED_PRESERVE src0_sel:WORD_1
v_mfma_f32_16x16x32_bf16 v[152:155],a[232:235],v[40:43],v[152:155]
ds_read_u16 v48,v48
v_lshlrev_b32_sdwa v50,1,v50 dst_sel:DWORD dst_unused:UNUSED_PAD src0_sel:DWORD src1_sel:WORD_1
v_mfma_f32_16x16x32_bf16 v[156:159],a[232:235],v[36:39],v[156:159]
ds_read_u16_d16_hi v50,v50
v_fma_mix_f32 v49,v49,v9,0 op_sel_hi:[0,1,0]
v_mfma_f32_16x16x32_bf16 v[100:103],a[244:247],v[36:39],v[100:103]
v_fma_mix_f32 v51,v51,v9,0 op_sel:[0,1,0] op_sel_hi:[0,1,0]
v_mfma_f32_16x16x32_bf16 v[96:99],a[244:247],v[40:43],v[96:99]
v_mfma_f32_16x16x32_bf16 v[36:39],a[220:223],a[56:59],v[16:19]
v_lshlrev_b32_sdwa v60,1,v60 dst_sel:DWORD dst_unused:UNUSED_PAD src0_sel:DWORD src1_sel:WORD_1
v_fma_mixlo_f16 v8,v53,s99,0
v_mfma_f32_16x16x32_bf16 v[36:39],a[216:219],a[72:75],v[36:39]
ds_read_u16 v60,v60
v_lshlrev_b32_sdwa v62,1,v62 dst_sel:DWORD dst_unused:UNUSED_PAD src0_sel:DWORD src1_sel:WORD_1
v_mfma_f32_16x16x32_bf16 v[36:39],a[212:215],a[88:91],v[36:39]
ds_read_u16_d16_hi v62,v62
v_lshl_add_u64 v[28:29],v[28:29],0,v[30:31]
v_mfma_f32_16x16x32_bf16 v[40:43],a[208:211],a[88:91],v[20:23]
s_waitcnt lgkmcnt(4)
v_lshl_add_u64 v[30:31],v[24:25],0,v[26:27]
v_fma_mixhi_f16 v8,v55,s99,0
v_mfma_f32_16x16x32_bf16 v[40:43],a[204:207],a[72:75],v[40:43]
v_lshl_add_u64 v[24:25],v[44:45],0,v[46:47]
v_lshlrev_b32_sdwa v61,1,v61 dst_sel:DWORD dst_unused:UNUSED_PAD src0_sel:DWORD src1_sel:WORD_1
v_pk_fma_f16 v9,v8,v8,s54
v_mfma_f32_16x16x32_bf16 v[40:43],a[200:203],a[56:59],v[40:43]
ds_read_u16 v61,v61
v_lshlrev_b32_sdwa v63,1,v63 dst_sel:DWORD dst_unused:UNUSED_PAD src0_sel:DWORD src1_sel:WORD_1
v_mfma_f32_16x16x32_bf16 v[44:47],a[200:203],a[60:63],v[20:23]
s_waitcnt lgkmcnt(3)
ds_read_b128 v[20:23],v4 offset:1056
v_or_b32 v26,v48,v50
v_pk_mul_f16 v9,v8,v9
v_mfma_f32_16x16x32_bf16 v[44:47],a[204:207],a[76:79],v[44:47]
ds_read_u16_d16_hi v63,v63
v_cvt_pk_bf16_f32 v27,v49,v51
v_lshlrev_b32_sdwa v32,1,v32 dst_sel:DWORD dst_unused:UNUSED_PAD src0_sel:DWORD src1_sel:WORD_1
v_mfma_f32_16x16x32_bf16 v[44:47],a[208:211],a[92:95],v[44:47]
ds_read_b64_tr_b16 a[200:201],v1 offset:22576
v_exp_f16 v8,v9
v_mfma_f32_16x16x32_bf16 v[48:51],a[212:215],a[92:95],v[16:19]
ds_read_b128 v[16:19],v4 offset:1024
ds_read_b64_tr_b16 a[202:203],v1 offset:23600
v_lshlrev_b32_sdwa v34,1,v34 dst_sel:DWORD dst_unused:UNUSED_PAD src0_sel:DWORD src1_sel:WORD_1
v_mfma_f32_16x16x32_bf16 v[48:51],a[216:219],a[76:79],v[48:51]
ds_read_b64_tr_b16 a[204:205],v2 offset:22576
ds_read_b64_tr_b16 a[206:207],v2 offset:23600
v_lshlrev_b32_sdwa v33,1,v33 dst_sel:DWORD dst_unused:UNUSED_PAD src0_sel:DWORD src1_sel:WORD_1
v_mfma_f32_16x16x32_bf16 v[48:51],a[220:223],a[60:63],v[48:51]
ds_read_u16 v32,v32
v_exp_f16_sdwa v8,v9 dst_sel:WORD_1 dst_unused:UNUSED_PRESERVE src0_sel:WORD_1
v_mfma_f32_16x16x32_bf16 v[112:115],a[248:251],v[28:31],v[112:115]
ds_read_u16_d16_hi v34,v34
v_lshlrev_b32_sdwa v35,1,v35 dst_sel:DWORD dst_unused:UNUSED_PAD src0_sel:DWORD src1_sel:WORD_1
v_mfma_f32_16x16x32_bf16 v[116:119],a[248:251],v[24:27],v[116:119]
ds_read_u16 v33,v33
v_lshlrev_b32_sdwa v56,1,v56 dst_sel:DWORD dst_unused:UNUSED_PAD src0_sel:DWORD src1_sel:WORD_1
v_mfma_f32_16x16x32_bf16 v[76:79],a[236:239],v[24:27],v[76:79]
ds_read_u16_d16_hi v35,v35
v_pk_fma_f16 v8,v8,s55,s55
v_mfma_f32_16x16x32_bf16 v[72:75],a[236:239],v[28:31],v[72:75]
v_rcp_f16 v9,v8
v_lshlrev_b32_sdwa v58,1,v58 dst_sel:DWORD dst_unused:UNUSED_PAD src0_sel:DWORD src1_sel:WORD_1
v_mfma_f32_16x16x32_bf16 v[120:123],a[252:255],v[28:31],v[120:123]
ds_read_b64_tr_b16 a[208:209],v2 offset:20528
v_lshlrev_b32_sdwa v57,1,v57 dst_sel:DWORD dst_unused:UNUSED_PAD src0_sel:DWORD src1_sel:WORD_1
v_mfma_f32_16x16x32_bf16 v[124:127],a[252:255],v[24:27],v[124:127]
ds_read_b64_tr_b16 a[210:211],v2 offset:21552
v_lshlrev_b32_sdwa v59,1,v59 dst_sel:DWORD dst_unused:UNUSED_PAD src0_sel:DWORD src1_sel:WORD_1
v_mfma_f32_16x16x32_bf16 v[92:95],a[224:227],v[24:27],v[92:95]
ds_read_u16 v56,v56
v_lshlrev_b32_sdwa v52,1,v52 dst_sel:DWORD dst_unused:UNUSED_PAD src0_sel:DWORD src1_sel:WORD_1
v_mfma_f32_16x16x32_bf16 v[88:91],a[224:227],v[28:31],v[88:91]
ds_read_u16_d16_hi v58,v58
v_rcp_f16_sdwa v9,v8 dst_sel:WORD_1 dst_unused:UNUSED_PRESERVE src0_sel:WORD_1
v_mfma_f32_16x16x32_bf16 v[128:131],a[232:235],v[28:31],v[128:131]
ds_read_u16 v57,v57
ds_read_u16_d16_hi v59,v59
v_mfma_f32_16x16x32_bf16 v[132:135],a[232:235],v[24:27],v[132:135]
ds_read_b64_tr_b16 a[212:213],v2 offset:18480
ds_read_b64_tr_b16 a[214:215],v2 offset:19504
v_lshlrev_b32_sdwa v54,1,v54 dst_sel:DWORD dst_unused:UNUSED_PAD src0_sel:DWORD src1_sel:WORD_1
v_mfma_f32_16x16x32_bf16 v[108:111],a[244:247],v[24:27],v[108:111]
ds_read_u16 v52,v52
v_fma_mix_f32 v53,v53,v9,0 op_sel_hi:[0,1,0]
v_mfma_f32_16x16x32_bf16 v[104:107],a[244:247],v[28:31],v[104:107]
s_waitcnt lgkmcnt(9)
ds_read_u16_d16_hi v54,v54
v_fma_mix_f32 v55,v55,v9,0 op_sel:[0,1,0] op_sel_hi:[0,1,0]
v_mfma_f32_16x16x32_bf16 v[24:27],a[192:195],a[0:3],v[16:19]
v_lshlrev_b32_sdwa v36,1,v36 dst_sel:DWORD dst_unused:UNUSED_PAD src0_sel:DWORD src1_sel:WORD_1
v_fma_mixlo_f16 v8,v45,s99,0
v_mfma_f32_16x16x32_bf16 v[24:27],a[196:199],a[16:19],v[24:27]
ds_read_u16 v36,v36
v_lshlrev_b32_sdwa v38,1,v38 dst_sel:DWORD dst_unused:UNUSED_PAD src0_sel:DWORD src1_sel:WORD_1
v_mfma_f32_16x16x32_bf16 v[24:27],a[200:203],a[32:35],v[24:27]
ds_read_u16_d16_hi v38,v38
v_lshl_add_u64 v[60:61],v[60:61],0,v[62:63]
v_mfma_f32_16x16x32_bf16 v[28:31],a[204:207],a[32:35],v[20:23]
s_waitcnt lgkmcnt(9)
v_lshl_add_u64 v[62:63],v[32:33],0,v[34:35]
v_fma_mixhi_f16 v8,v47,s99,0
v_mfma_f32_16x16x32_bf16 v[28:31],a[208:211],a[16:19],v[28:31]
s_waitcnt lgkmcnt(4)
v_lshl_add_u64 v[32:33],v[56:57],0,v[58:59]
v_lshlrev_b32_sdwa v37,1,v37 dst_sel:DWORD dst_unused:UNUSED_PAD src0_sel:DWORD src1_sel:WORD_1
v_pk_fma_f16 v9,v8,v8,s54
v_mfma_f32_16x16x32_bf16 v[28:31],a[212:215],a[0:3],v[28:31]
ds_read_u16 v37,v37
v_lshlrev_b32_sdwa v39,1,v39 dst_sel:DWORD dst_unused:UNUSED_PAD src0_sel:DWORD src1_sel:WORD_1
v_mfma_f32_16x16x32_bf16 v[56:59],a[212:215],a[4:7],v[20:23]
s_waitcnt lgkmcnt(3)
ds_read_u16_d16_hi v39,v39
v_or_b32 v34,v52,v54
v_pk_mul_f16 v9,v8,v9
v_mfma_f32_16x16x32_bf16 v[56:59],a[208:211],a[20:23],v[56:59]
s_waitcnt vmcnt(14)
v_cvt_pk_bf16_f32 v35,v53,v55
v_lshlrev_b32_sdwa v40,1,v40 dst_sel:DWORD dst_unused:UNUSED_PAD src0_sel:DWORD src1_sel:WORD_1
v_mfma_f32_16x16x32_bf16 v[56:59],a[204:207],a[36:39],v[56:59]
s_barrier
v_exp_f16 v8,v9
v_mfma_f32_16x16x32_bf16 v[52:55],a[200:203],a[36:39],v[16:19]
ds_read_u16 v40,v40
v_lshlrev_b32_sdwa v42,1,v42 dst_sel:DWORD dst_unused:UNUSED_PAD src0_sel:DWORD src1_sel:WORD_1
v_mfma_f32_16x16x32_bf16 v[52:55],a[196:199],a[20:23],v[52:55]
s_add_u32 m0,s58,18480
ds_read_u16_d16_hi v42,v42
v_lshlrev_b32_sdwa v41,1,v41 dst_sel:DWORD dst_unused:UNUSED_PAD src0_sel:DWORD src1_sel:WORD_1
v_mfma_f32_16x16x32_bf16 v[52:55],a[192:195],a[4:7],v[52:55]
s_mov_b32 s69,s88
v_exp_f16_sdwa v8,v9 dst_sel:WORD_1 dst_unused:UNUSED_PRESERVE src0_sel:WORD_1
v_mfma_f32_16x16x32_bf16 v[232:235],a[248:251],v[60:63],v[232:235]
buffer_load_dwordx4 v5,s[44:47],s69 offen offset:0 lds
v_lshlrev_b32_sdwa v43,1,v43 dst_sel:DWORD dst_unused:UNUSED_PAD src0_sel:DWORD src1_sel:WORD_1
v_mfma_f32_16x16x32_bf16 v[236:239],a[248:251],v[32:35],v[236:239]
ds_read_u16 v41,v41
v_lshlrev_b32_sdwa v48,1,v48 dst_sel:DWORD dst_unused:UNUSED_PAD src0_sel:DWORD src1_sel:WORD_1
v_mfma_f32_16x16x32_bf16 v[164:167],a[236:239],v[32:35],v[164:167]
ds_read_u16_d16_hi v43,v43
v_pk_fma_f16 v8,v8,s55,s55
v_mfma_f32_16x16x32_bf16 v[160:163],a[236:239],v[60:63],v[160:163]
v_rcp_f16 v9,v8
v_lshlrev_b32_sdwa v50,1,v50 dst_sel:DWORD dst_unused:UNUSED_PAD src0_sel:DWORD src1_sel:WORD_1
v_mfma_f32_16x16x32_bf16 v[240:243],a[252:255],v[60:63],v[240:243]
s_add_u32 s69,s69,s53
ds_read_u16 v48,v48
v_lshlrev_b32_sdwa v49,1,v49 dst_sel:DWORD dst_unused:UNUSED_PAD src0_sel:DWORD src1_sel:WORD_1
v_mfma_f32_16x16x32_bf16 v[244:247],a[252:255],v[32:35],v[244:247]
buffer_load_dwordx4 v5,s[44:47],s69 offen offset:1024 lds
ds_read_u16_d16_hi v50,v50
v_mfma_f32_16x16x32_bf16 v[180:183],a[224:227],v[32:35],v[180:183]
ds_read_u16 v49,v49
v_lshlrev_b32_sdwa v51,1,v51 dst_sel:DWORD dst_unused:UNUSED_PAD src0_sel:DWORD src1_sel:WORD_1
v_mfma_f32_16x16x32_bf16 v[176:179],a[224:227],v[60:63],v[176:179]
v_rcp_f16_sdwa v9,v8 dst_sel:WORD_1 dst_unused:UNUSED_PRESERVE src0_sel:WORD_1
v_mfma_f32_16x16x32_bf16 v[248:251],a[232:235],v[60:63],v[248:251]
ds_read_u16_d16_hi v51,v51
v_lshlrev_b32_sdwa v44,1,v44 dst_sel:DWORD dst_unused:UNUSED_PAD src0_sel:DWORD src1_sel:WORD_1
v_mfma_f32_16x16x32_bf16 v[252:255],a[232:235],v[32:35],v[252:255]
s_add_u32 s69,s69,s53
ds_read_u16 v44,v44
v_lshlrev_b32_sdwa v46,1,v46 dst_sel:DWORD dst_unused:UNUSED_PAD src0_sel:DWORD src1_sel:WORD_1
v_mfma_f32_16x16x32_bf16 v[196:199],a[244:247],v[32:35],v[196:199]
buffer_load_dwordx4 v5,s[44:47],s69 offen offset:2048 lds
v_fma_mix_f32 v45,v45,v9,0 op_sel_hi:[0,1,0]
v_mfma_f32_16x16x32_bf16 v[192:195],a[244:247],v[60:63],v[192:195]
ds_read_u16_d16_hi v46,v46
v_fma_mix_f32 v47,v47,v9,0 op_sel:[0,1,0] op_sel_hi:[0,1,0]
v_mfma_f32_16x16x32_bf16 v[32:35],a[192:195],a[8:11],v[16:19]
ds_read_b64_tr_b16 a[240:241],v3 offset:0
ds_read_b64_tr_b16 a[242:243],v3 offset:1536
v_lshlrev_b32_sdwa v24,1,v24 dst_sel:DWORD dst_unused:UNUSED_PAD src0_sel:DWORD src1_sel:WORD_1
v_mfma_f32_16x16x32_bf16 v[32:35],a[196:199],a[24:27],v[32:35]
ds_read_u16 v24,v24
ds_read_b64_tr_b16 a[228:229],v3 offset:8
v_fma_mixlo_f16 v8,v57,s99,0
v_mfma_f32_16x16x32_bf16 v[32:35],a[200:203],a[40:43],v[32:35]
s_waitcnt lgkmcnt(8)
ds_read_b64_tr_b16 a[230:231],v3 offset:1544
v_lshl_add_u64 v[36:37],v[36:37],0,v[38:39]
v_lshlrev_b32_sdwa v26,1,v26 dst_sel:DWORD dst_unused:UNUSED_PAD src0_sel:DWORD src1_sel:WORD_1
v_mfma_f32_16x16x32_bf16 v[60:63],a[204:207],a[40:43],v[20:23]
ds_read_u16_d16_hi v26,v26
v_lshl_add_u64 v[38:39],v[40:41],0,v[42:43]
v_fma_mixhi_f16 v8,v59,s99,0
v_mfma_f32_16x16x32_bf16 v[60:63],a[208:211],a[24:27],v[60:63]
s_waitcnt lgkmcnt(8)
ds_read_b64_tr_b16 a[216:217],v1 offset:0
v_lshl_add_u64 v[40:41],v[48:49],0,v[50:51]
v_lshlrev_b32_sdwa v25,1,v25 dst_sel:DWORD dst_unused:UNUSED_PAD src0_sel:DWORD src1_sel:WORD_1
v_pk_fma_f16 v9,v8,v8,s54
v_mfma_f32_16x16x32_bf16 v[60:63],a[212:215],a[8:11],v[60:63]
ds_read_u16 v25,v25
v_lshlrev_b32_sdwa v27,1,v27 dst_sel:DWORD dst_unused:UNUSED_PAD src0_sel:DWORD src1_sel:WORD_1
v_mfma_f32_16x16x32_bf16 v[48:51],a[212:215],a[12:15],v[20:23]
s_waitcnt lgkmcnt(8)
ds_read_u16_d16_hi v27,v27
v_or_b32 v42,v44,v46
v_pk_mul_f16 v9,v8,v9
v_mfma_f32_16x16x32_bf16 v[48:51],a[208:211],a[28:31],v[48:51]
ds_read_b64_tr_b16 a[218:219],v1 offset:1024
v_cvt_pk_bf16_f32 v43,v45,v47
v_lshlrev_b32_sdwa v28,1,v28 dst_sel:DWORD dst_unused:UNUSED_PAD src0_sel:DWORD src1_sel:WORD_1
v_mfma_f32_16x16x32_bf16 v[48:51],a[204:207],a[44:47],v[48:51]
v_exp_f16 v8,v9
v_mfma_f32_16x16x32_bf16 v[44:47],a[200:203],a[44:47],v[16:19]
ds_read_u16 v28,v28
v_lshlrev_b32_sdwa v30,1,v30 dst_sel:DWORD dst_unused:UNUSED_PAD src0_sel:DWORD src1_sel:WORD_1
v_mfma_f32_16x16x32_bf16 v[44:47],a[196:199],a[28:31],v[44:47]
ds_read_u16_d16_hi v30,v30
v_lshlrev_b32_sdwa v29,1,v29 dst_sel:DWORD dst_unused:UNUSED_PAD src0_sel:DWORD src1_sel:WORD_1
v_mfma_f32_16x16x32_bf16 v[44:47],a[192:195],a[12:15],v[44:47]
v_exp_f16_sdwa v8,v9 dst_sel:WORD_1 dst_unused:UNUSED_PRESERVE src0_sel:WORD_1
v_mfma_f32_16x16x32_bf16 v[208:211],a[248:251],v[36:39],v[208:211]
ds_read_u16 v29,v29
v_lshlrev_b32_sdwa v31,1,v31 dst_sel:DWORD dst_unused:UNUSED_PAD src0_sel:DWORD src1_sel:WORD_1
v_mfma_f32_16x16x32_bf16 v[212:215],a[248:251],v[40:43],v[212:215]
ds_read_u16_d16_hi v31,v31
v_lshlrev_b32_sdwa v52,1,v52 dst_sel:DWORD dst_unused:UNUSED_PAD src0_sel:DWORD src1_sel:WORD_1
v_mfma_f32_16x16x32_bf16 v[172:175],a[236:239],v[40:43],v[172:175]
ds_read_u16 v52,v52
v_pk_fma_f16 v8,v8,s55,s55
v_mfma_f32_16x16x32_bf16 v[168:171],a[236:239],v[36:39],v[168:171]
ds_read_b64_tr_b16 a[248:249],v3 offset:64
v_rcp_f16 v9,v8
v_lshlrev_b32_sdwa v54,1,v54 dst_sel:DWORD dst_unused:UNUSED_PAD src0_sel:DWORD src1_sel:WORD_1
v_mfma_f32_16x16x32_bf16 v[216:219],a[252:255],v[36:39],v[216:219]
ds_read_u16_d16_hi v54,v54
v_lshlrev_b32_sdwa v53,1,v53 dst_sel:DWORD dst_unused:UNUSED_PAD src0_sel:DWORD src1_sel:WORD_1
v_mfma_f32_16x16x32_bf16 v[220:223],a[252:255],v[40:43],v[220:223]
ds_read_u16 v53,v53
ds_read_b64_tr_b16 a[250:251],v3 offset:1600
v_mfma_f32_16x16x32_bf16 v[188:191],a[224:227],v[40:43],v[188:191]
ds_read_b64_tr_b16 a[236:237],v3 offset:72
v_lshlrev_b32_sdwa v55,1,v55 dst_sel:DWORD dst_unused:UNUSED_PAD src0_sel:DWORD src1_sel:WORD_1
v_mfma_f32_16x16x32_bf16 v[184:187],a[224:227],v[36:39],v[184:187]
ds_read_u16_d16_hi v55,v55
v_rcp_f16_sdwa v9,v8 dst_sel:WORD_1 dst_unused:UNUSED_PRESERVE src0_sel:WORD_1
v_mfma_f32_16x16x32_bf16 v[224:227],a[232:235],v[36:39],v[224:227]
ds_read_b64_tr_b16 a[238:239],v3 offset:1608
ds_read_b64_tr_b16 a[252:253],v3 offset:128
v_lshlrev_b32_sdwa v56,1,v56 dst_sel:DWORD dst_unused:UNUSED_PAD src0_sel:DWORD src1_sel:WORD_1
v_mfma_f32_16x16x32_bf16 v[228:231],a[232:235],v[40:43],v[228:231]
ds_read_u16 v56,v56
v_lshlrev_b32_sdwa v58,1,v58 dst_sel:DWORD dst_unused:UNUSED_PAD src0_sel:DWORD src1_sel:WORD_1
v_mfma_f32_16x16x32_bf16 v[204:207],a[244:247],v[40:43],v[204:207]
ds_read_u16_d16_hi v58,v58
v_fma_mix_f32 v57,v57,v9,0 op_sel_hi:[0,1,0]
v_mfma_f32_16x16x32_bf16 v[200:203],a[244:247],v[36:39],v[200:203]
ds_read_b64_tr_b16 a[254:255],v3 offset:1664
v_fma_mix_f32 v59,v59,v9,0 op_sel:[0,1,0] op_sel_hi:[0,1,0]
v_mfma_f32_16x16x32_bf16 v[40:43],a[192:195],a[48:51],v[16:19]
ds_read_b64_tr_b16 a[224:225],v3 offset:136
v_lshlrev_b32_sdwa v32,1,v32 dst_sel:DWORD dst_unused:UNUSED_PAD src0_sel:DWORD src1_sel:WORD_1
v_mfma_f32_16x16x32_bf16 v[40:43],a[196:199],a[64:67],v[40:43]
ds_read_b64_tr_b16 a[226:227],v3 offset:1672
v_fma_mixlo_f16 v8,v49,s99,0
v_mfma_f32_16x16x32_bf16 v[40:43],a[200:203],a[80:83],v[40:43]
s_waitcnt lgkmcnt(9)
ds_read_u16 v32,v32
v_lshl_add_u64 v[24:25],v[24:25],0,v[26:27]
v_lshlrev_b32_sdwa v34,1,v34 dst_sel:DWORD dst_unused:UNUSED_PAD src0_sel:DWORD src1_sel:WORD_1
v_mfma_f32_16x16x32_bf16 v[36:39],a[204:207],a[80:83],v[20:23]
ds_read_u16_d16_hi v34,v34
v_lshl_add_u64 v[26:27],v[28:29],0,v[30:31]
v_fma_mixhi_f16 v8,v51,s99,0
v_mfma_f32_16x16x32_bf16 v[36:39],a[208:211],a[64:67],v[36:39]
s_waitcnt lgkmcnt(9)
ds_read_b64_tr_b16 a[220:221],v1 offset:2048
v_lshl_add_u64 v[28:29],v[52:53],0,v[54:55]
v_lshlrev_b32_sdwa v33,1,v33 dst_sel:DWORD dst_unused:UNUSED_PAD src0_sel:DWORD src1_sel:WORD_1
v_pk_fma_f16 v9,v8,v8,s54
v_mfma_f32_16x16x32_bf16 v[36:39],a[212:215],a[48:51],v[36:39]
ds_read_u16 v33,v33
v_lshlrev_b32_sdwa v35,1,v35 dst_sel:DWORD dst_unused:UNUSED_PAD src0_sel:DWORD src1_sel:WORD_1
v_mfma_f32_16x16x32_bf16 v[52:55],a[212:215],a[52:55],v[20:23]
s_waitcnt lgkmcnt(7)
ds_read_u16_d16_hi v35,v35
v_or_b32 v30,v56,v58
v_pk_mul_f16 v9,v8,v9
v_mfma_f32_16x16x32_bf16 v[52:55],a[208:211],a[68:71],v[52:55]
ds_read_b64_tr_b16 a[222:223],v1 offset:3072
v_cvt_pk_bf16_f32 v31,v57,v59
v_lshlrev_b32_sdwa v60,1,v60 dst_sel:DWORD dst_unused:UNUSED_PAD src0_sel:DWORD src1_sel:WORD_1
v_mfma_f32_16x16x32_bf16 v[52:55],a[204:207],a[84:87],v[52:55]
v_exp_f16 v8,v9
v_mfma_f32_16x16x32_bf16 v[56:59],a[200:203],a[84:87],v[16:19]
ds_read_u16 v60,v60
v_lshlrev_b32_sdwa v62,1,v62 dst_sel:DWORD dst_unused:UNUSED_PAD src0_sel:DWORD src1_sel:WORD_1
v_mfma_f32_16x16x32_bf16 v[56:59],a[196:199],a[68:71],v[56:59]
ds_read_u16_d16_hi v62,v62
v_lshlrev_b32_sdwa v61,1,v61 dst_sel:DWORD dst_unused:UNUSED_PAD src0_sel:DWORD src1_sel:WORD_1
v_mfma_f32_16x16x32_bf16 v[56:59],a[192:195],a[52:55],v[56:59]
TE13_%=:
s_cbranch_execz TP11388_%=
v_exp_f16_sdwa v8,v9 dst_sel:WORD_1 dst_unused:UNUSED_PRESERVE src0_sel:WORD_1
v_mfma_f32_16x16x32_bf16 v[136:139],a[240:243],v[24:27],v[136:139]
ds_read_u16 v61,v61
v_lshlrev_b32_sdwa v63,1,v63 dst_sel:DWORD dst_unused:UNUSED_PAD src0_sel:DWORD src1_sel:WORD_1
v_mfma_f32_16x16x32_bf16 v[140:143],a[240:243],v[28:31],v[140:143]
ds_read_u16_d16_hi v63,v63
v_lshlrev_b32_sdwa v44,1,v44 dst_sel:DWORD dst_unused:UNUSED_PAD src0_sel:DWORD src1_sel:WORD_1
v_mfma_f32_16x16x32_bf16 v[68:71],a[228:231],v[28:31],v[68:71]
ds_read_u16 v44,v44
v_pk_fma_f16 v8,v8,s55,s55
v_mfma_f32_16x16x32_bf16 v[64:67],a[228:231],v[24:27],v[64:67]
v_rcp_f16 v9,v8
v_lshlrev_b32_sdwa v46,1,v46 dst_sel:DWORD dst_unused:UNUSED_PAD src0_sel:DWORD src1_sel:WORD_1
v_mfma_f32_16x16x32_bf16 v[144:147],a[248:251],v[24:27],v[144:147]
ds_read_u16_d16_hi v46,v46
v_lshlrev_b32_sdwa v45,1,v45 dst_sel:DWORD dst_unused:UNUSED_PAD src0_sel:DWORD src1_sel:WORD_1
v_mfma_f32_16x16x32_bf16 v[148:151],a[248:251],v[28:31],v[148:151]
ds_read_u16 v45,v45
v_lshlrev_b32_sdwa v47,1,v47 dst_sel:DWORD dst_unused:UNUSED_PAD src0_sel:DWORD src1_sel:WORD_1
v_mfma_f32_16x16x32_bf16 v[84:87],a[236:239],v[28:31],v[84:87]
ds_read_u16_d16_hi v47,v47
v_lshlrev_b32_sdwa v48,1,v48 dst_sel:DWORD dst_unused:UNUSED_PAD src0_sel:DWORD src1_sel:WORD_1
v_mfma_f32_16x16x32_bf16 v[80:83],a[236:239],v[24:27],v[80:83]
s_waitcnt lgkmcnt(6)
v_rcp_f16_sdwa v9,v8 dst_sel:WORD_1 dst_unused:UNUSED_PRESERVE src0_sel:WORD_1
v_mfma_f32_16x16x32_bf16 v[152:155],a[252:255],v[24:27],v[152:155]
ds_read_u16 v48,v48
v_lshlrev_b32_sdwa v50,1,v50 dst_sel:DWORD dst_unused:UNUSED_PAD src0_sel:DWORD src1_sel:WORD_1
v_mfma_f32_16x16x32_bf16 v[156:159],a[252:255],v[28:31],v[156:159]
ds_read_u16_d16_hi v50,v50
v_fma_mix_f32 v49,v49,v9,0 op_sel_hi:[0,1,0]
v_mfma_f32_16x16x32_bf16 v[100:103],a[224:227],v[28:31],v[100:103]
v_fma_mix_f32 v51,v51,v9,0 op_sel:[0,1,0] op_sel_hi:[0,1,0]
v_mfma_f32_16x16x32_bf16 v[96:99],a[224:227],v[24:27],v[96:99]
v_mfma_f32_16x16x32_bf16 v[28:31],a[192:195],a[56:59],v[16:19]
v_lshlrev_b32_sdwa v40,1,v40 dst_sel:DWORD dst_unused:UNUSED_PAD src0_sel:DWORD src1_sel:WORD_1
v_fma_mixlo_f16 v8,v53,s99,0
v_mfma_f32_16x16x32_bf16 v[28:31],a[196:199],a[72:75],v[28:31]
ds_read_u16 v40,v40
v_lshlrev_b32_sdwa v42,1,v42 dst_sel:DWORD dst_unused:UNUSED_PAD src0_sel:DWORD src1_sel:WORD_1
v_mfma_f32_16x16x32_bf16 v[28:31],a[200:203],a[88:91],v[28:31]
ds_read_u16_d16_hi v42,v42
v_lshl_add_u64 v[32:33],v[32:33],0,v[34:35]
v_mfma_f32_16x16x32_bf16 v[24:27],a[204:207],a[88:91],v[20:23]
s_waitcnt lgkmcnt(4)
v_lshl_add_u64 v[34:35],v[60:61],0,v[62:63]
v_fma_mixhi_f16 v8,v55,s99,0
v_mfma_f32_16x16x32_bf16 v[24:27],a[208:211],a[72:75],v[24:27]
v_lshl_add_u64 v[60:61],v[44:45],0,v[46:47]
v_lshlrev_b32_sdwa v41,1,v41 dst_sel:DWORD dst_unused:UNUSED_PAD src0_sel:DWORD src1_sel:WORD_1
v_pk_fma_f16 v9,v8,v8,s54
v_mfma_f32_16x16x32_bf16 v[24:27],a[212:215],a[56:59],v[24:27]
ds_read_u16 v41,v41
v_lshlrev_b32_sdwa v43,1,v43 dst_sel:DWORD dst_unused:UNUSED_PAD src0_sel:DWORD src1_sel:WORD_1
v_mfma_f32_16x16x32_bf16 v[44:47],a[212:215],a[60:63],v[20:23]
s_waitcnt lgkmcnt(3)
ds_read_b128 v[20:23],v4 offset:1184
v_or_b32 v62,v48,v50
v_pk_mul_f16 v9,v8,v9
v_mfma_f32_16x16x32_bf16 v[44:47],a[208:211],a[76:79],v[44:47]
ds_read_u16_d16_hi v43,v43
v_cvt_pk_bf16_f32 v63,v49,v51
v_lshlrev_b32_sdwa v36,1,v36 dst_sel:DWORD dst_unused:UNUSED_PAD src0_sel:DWORD src1_sel:WORD_1
v_mfma_f32_16x16x32_bf16 v[44:47],a[204:207],a[92:95],v[44:47]
ds_read_b64_tr_b16 a[212:213],v1 offset:4096
v_exp_f16 v8,v9
v_mfma_f32_16x16x32_bf16 v[48:51],a[200:203],a[92:95],v[16:19]
ds_read_b128 v[16:19],v4 offset:1152
ds_read_b64_tr_b16 a[214:215],v1 offset:5120
v_lshlrev_b32_sdwa v38,1,v38 dst_sel:DWORD dst_unused:UNUSED_PAD src0_sel:DWORD src1_sel:WORD_1
v_mfma_f32_16x16x32_bf16 v[48:51],a[196:199],a[76:79],v[48:51]
ds_read_b64_tr_b16 a[208:209],v2 offset:4096
ds_read_b64_tr_b16 a[210:211],v2 offset:5120
v_lshlrev_b32_sdwa v37,1,v37 dst_sel:DWORD dst_unused:UNUSED_PAD src0_sel:DWORD src1_sel:WORD_1
v_mfma_f32_16x16x32_bf16 v[48:51],a[192:195],a[60:63],v[48:51]
ds_read_u16 v36,v36
v_exp_f16_sdwa v8,v9 dst_sel:WORD_1 dst_unused:UNUSED_PRESERVE src0_sel:WORD_1
v_mfma_f32_16x16x32_bf16 v[112:115],a[240:243],v[32:35],v[112:115]
ds_read_u16_d16_hi v38,v38
v_lshlrev_b32_sdwa v39,1,v39 dst_sel:DWORD dst_unused:UNUSED_PAD src0_sel:DWORD src1_sel:WORD_1
v_mfma_f32_16x16x32_bf16 v[116:119],a[240:243],v[60:63],v[116:119]
ds_read_u16 v37,v37
v_lshlrev_b32_sdwa v56,1,v56 dst_sel:DWORD dst_unused:UNUSED_PAD src0_sel:DWORD src1_sel:WORD_1
v_mfma_f32_16x16x32_bf16 v[76:79],a[228:231],v[60:63],v[76:79]
ds_read_u16_d16_hi v39,v39
v_pk_fma_f16 v8,v8,s55,s55
v_mfma_f32_16x16x32_bf16 v[72:75],a[228:231],v[32:35],v[72:75]
v_rcp_f16 v9,v8
v_lshlrev_b32_sdwa v58,1,v58 dst_sel:DWORD dst_unused:UNUSED_PAD src0_sel:DWORD src1_sel:WORD_1
v_mfma_f32_16x16x32_bf16 v[120:123],a[248:251],v[32:35],v[120:123]
ds_read_b64_tr_b16 a[204:205],v2 offset:2048
v_lshlrev_b32_sdwa v57,1,v57 dst_sel:DWORD dst_unused:UNUSED_PAD src0_sel:DWORD src1_sel:WORD_1
v_mfma_f32_16x16x32_bf16 v[124:127],a[248:251],v[60:63],v[124:127]
ds_read_b64_tr_b16 a[206:207],v2 offset:3072
v_lshlrev_b32_sdwa v59,1,v59 dst_sel:DWORD dst_unused:UNUSED_PAD src0_sel:DWORD src1_sel:WORD_1
v_mfma_f32_16x16x32_bf16 v[92:95],a[236:239],v[60:63],v[92:95]
ds_read_u16 v56,v56
v_lshlrev_b32_sdwa v52,1,v52 dst_sel:DWORD dst_unused:UNUSED_PAD src0_sel:DWORD src1_sel:WORD_1
v_mfma_f32_16x16x32_bf16 v[88:91],a[236:239],v[32:35],v[88:91]
ds_read_u16_d16_hi v58,v58
v_rcp_f16_sdwa v9,v8 dst_sel:WORD_1 dst_unused:UNUSED_PRESERVE src0_sel:WORD_1
v_mfma_f32_16x16x32_bf16 v[128:131],a[252:255],v[32:35],v[128:131]
ds_read_u16 v57,v57
ds_read_u16_d16_hi v59,v59
v_mfma_f32_16x16x32_bf16 v[132:135],a[252:255],v[60:63],v[132:135]
ds_read_b64_tr_b16 a[200:201],v2 offset:0
ds_read_b64_tr_b16 a[202:203],v2 offset:1024
v_lshlrev_b32_sdwa v54,1,v54 dst_sel:DWORD dst_unused:UNUSED_PAD src0_sel:DWORD src1_sel:WORD_1
v_mfma_f32_16x16x32_bf16 v[108:111],a[224:227],v[60:63],v[108:111]
ds_read_u16 v52,v52
v_fma_mix_f32 v53,v53,v9,0 op_sel_hi:[0,1,0]
v_mfma_f32_16x16x32_bf16 v[104:107],a[224:227],v[32:35],v[104:107]
s_waitcnt lgkmcnt(9)
ds_read_u16_d16_hi v54,v54
v_fma_mix_f32 v55,v55,v9,0 op_sel:[0,1,0] op_sel_hi:[0,1,0]
v_mfma_f32_16x16x32_bf16 v[60:63],a[216:219],a[0:3],v[16:19]
v_lshlrev_b32_sdwa v28,1,v28 dst_sel:DWORD dst_unused:UNUSED_PAD src0_sel:DWORD src1_sel:WORD_1
v_fma_mixlo_f16 v8,v45,s99,0
v_mfma_f32_16x16x32_bf16 v[60:63],a[220:223],a[16:19],v[60:63]
ds_read_u16 v28,v28
v_lshlrev_b32_sdwa v30,1,v30 dst_sel:DWORD dst_unused:UNUSED_PAD src0_sel:DWORD src1_sel:WORD_1
v_mfma_f32_16x16x32_bf16 v[60:63],a[212:215],a[32:35],v[60:63]
ds_read_u16_d16_hi v30,v30
v_lshl_add_u64 v[40:41],v[40:41],0,v[42:43]
v_mfma_f32_16x16x32_bf16 v[32:35],a[208:211],a[32:35],v[20:23]
s_waitcnt lgkmcnt(9)
v_lshl_add_u64 v[42:43],v[36:37],0,v[38:39]
v_fma_mixhi_f16 v8,v47,s99,0
v_mfma_f32_16x16x32_bf16 v[32:35],a[204:207],a[16:19],v[32:35]
s_waitcnt lgkmcnt(4)
v_lshl_add_u64 v[36:37],v[56:57],0,v[58:59]
v_lshlrev_b32_sdwa v29,1,v29 dst_sel:DWORD dst_unused:UNUSED_PAD src0_sel:DWORD src1_sel:WORD_1
v_pk_fma_f16 v9,v8,v8,s54
v_mfma_f32_16x16x32_bf16 v[32:35],a[200:203],a[0:3],v[32:35]
ds_read_u16 v29,v29
v_lshlrev_b32_sdwa v31,1,v31 dst_sel:DWORD dst_unused:UNUSED_PAD src0_sel:DWORD src1_sel:WORD_1
v_mfma_f32_16x16x32_bf16 v[56:59],a[200:203],a[4:7],v[20:23]
s_waitcnt lgkmcnt(3)
ds_read_u16_d16_hi v31,v31
v_or_b32 v38,v52,v54
v_pk_mul_f16 v9,v8,v9
v_mfma_f32_16x16x32_bf16 v[56:59],a[204:207],a[20:23],v[56:59]
s_waitcnt vmcnt(6)
v_cvt_pk_bf16_f32 v39,v53,v55
v_lshlrev_b32_sdwa v24,1,v24 dst_sel:DWORD dst_unused:UNUSED_PAD src0_sel:DWORD src1_sel:WORD_1
v_mfma_f32_16x16x32_bf16 v[56:59],a[208:211],a[36:39],v[56:59]
s_barrier
v_exp_f16 v8,v9
v_mfma_f32_16x16x32_bf16 v[52:55],a[212:215],a[36:39],v[16:19]
ds_read_u16 v24,v24
v_lshlrev_b32_sdwa v26,1,v26 dst_sel:DWORD dst_unused:UNUSED_PAD src0_sel:DWORD src1_sel:WORD_1
v_mfma_f32_16x16x32_bf16 v[52:55],a[220:223],a[20:23],v[52:55]
s_add_u32 m0,s58,0
ds_read_u16_d16_hi v26,v26
v_lshlrev_b32_sdwa v25,1,v25 dst_sel:DWORD dst_unused:UNUSED_PAD src0_sel:DWORD src1_sel:WORD_1
v_mfma_f32_16x16x32_bf16 v[52:55],a[216:219],a[4:7],v[52:55]
s_mov_b32 s69,s87
v_exp_f16_sdwa v8,v9 dst_sel:WORD_1 dst_unused:UNUSED_PRESERVE src0_sel:WORD_1
v_mfma_f32_16x16x32_bf16 v[232:235],a[240:243],v[40:43],v[232:235]
buffer_load_dwordx4 v5,s[44:47],s69 offen offset:0 lds
v_lshlrev_b32_sdwa v27,1,v27 dst_sel:DWORD dst_unused:UNUSED_PAD src0_sel:DWORD src1_sel:WORD_1
v_mfma_f32_16x16x32_bf16 v[236:239],a[240:243],v[36:39],v[236:239]
ds_read_u16 v25,v25
v_lshlrev_b32_sdwa v48,1,v48 dst_sel:DWORD dst_unused:UNUSED_PAD src0_sel:DWORD src1_sel:WORD_1
v_mfma_f32_16x16x32_bf16 v[164:167],a[228:231],v[36:39],v[164:167]
ds_read_u16_d16_hi v27,v27
v_pk_fma_f16 v8,v8,s55,s55
v_mfma_f32_16x16x32_bf16 v[160:163],a[228:231],v[40:43],v[160:163]
v_rcp_f16 v9,v8
v_lshlrev_b32_sdwa v50,1,v50 dst_sel:DWORD dst_unused:UNUSED_PAD src0_sel:DWORD src1_sel:WORD_1
v_mfma_f32_16x16x32_bf16 v[240:243],a[248:251],v[40:43],v[240:243]
s_add_u32 s69,s69,s53
ds_read_u16 v48,v48
v_lshlrev_b32_sdwa v49,1,v49 dst_sel:DWORD dst_unused:UNUSED_PAD src0_sel:DWORD src1_sel:WORD_1
v_mfma_f32_16x16x32_bf16 v[244:247],a[248:251],v[36:39],v[244:247]
buffer_load_dwordx4 v5,s[44:47],s69 offen offset:1024 lds
ds_read_u16_d16_hi v50,v50
v_mfma_f32_16x16x32_bf16 v[180:183],a[236:239],v[36:39],v[180:183]
ds_read_u16 v49,v49
v_lshlrev_b32_sdwa v51,1,v51 dst_sel:DWORD dst_unused:UNUSED_PAD src0_sel:DWORD src1_sel:WORD_1
v_mfma_f32_16x16x32_bf16 v[176:179],a[236:239],v[40:43],v[176:179]
v_rcp_f16_sdwa v9,v8 dst_sel:WORD_1 dst_unused:UNUSED_PRESERVE src0_sel:WORD_1
v_mfma_f32_16x16x32_bf16 v[248:251],a[252:255],v[40:43],v[248:251]
ds_read_u16_d16_hi v51,v51
v_lshlrev_b32_sdwa v44,1,v44 dst_sel:DWORD dst_unused:UNUSED_PAD src0_sel:DWORD src1_sel:WORD_1
v_mfma_f32_16x16x32_bf16 v[252:255],a[252:255],v[36:39],v[252:255]
s_add_u32 s69,s69,s53
ds_read_u16 v44,v44
v_lshlrev_b32_sdwa v46,1,v46 dst_sel:DWORD dst_unused:UNUSED_PAD src0_sel:DWORD src1_sel:WORD_1
v_mfma_f32_16x16x32_bf16 v[196:199],a[224:227],v[36:39],v[196:199]
buffer_load_dwordx4 v5,s[44:47],s69 offen offset:2048 lds
v_fma_mix_f32 v45,v45,v9,0 op_sel_hi:[0,1,0]
v_mfma_f32_16x16x32_bf16 v[192:195],a[224:227],v[40:43],v[192:195]
ds_read_u16_d16_hi v46,v46
v_fma_mix_f32 v47,v47,v9,0 op_sel:[0,1,0] op_sel_hi:[0,1,0]
v_mfma_f32_16x16x32_bf16 v[36:39],a[216:219],a[8:11],v[16:19]
ds_read_b64_tr_b16 a[232:233],v3 offset:6160
ds_read_b64_tr_b16 a[234:235],v3 offset:7696
v_lshlrev_b32_sdwa v60,1,v60 dst_sel:DWORD dst_unused:UNUSED_PAD src0_sel:DWORD src1_sel:WORD_1
v_mfma_f32_16x16x32_bf16 v[36:39],a[220:223],a[24:27],v[36:39]
ds_read_u16 v60,v60
ds_read_b64_tr_b16 a[244:245],v3 offset:6168
v_fma_mixlo_f16 v8,v57,s99,0
v_mfma_f32_16x16x32_bf16 v[36:39],a[212:215],a[40:43],v[36:39]
s_waitcnt lgkmcnt(8)
ds_read_b64_tr_b16 a[246:247],v3 offset:7704
v_lshl_add_u64 v[28:29],v[28:29],0,v[30:31]
v_lshlrev_b32_sdwa v62,1,v62 dst_sel:DWORD dst_unused:UNUSED_PAD src0_sel:DWORD src1_sel:WORD_1
v_mfma_f32_16x16x32_bf16 v[40:43],a[208:211],a[40:43],v[20:23]
ds_read_u16_d16_hi v62,v62
v_lshl_add_u64 v[30:31],v[24:25],0,v[26:27]
v_fma_mixhi_f16 v8,v59,s99,0
v_mfma_f32_16x16x32_bf16 v[40:43],a[204:207],a[24:27],v[40:43]
s_waitcnt lgkmcnt(8)
ds_read_b64_tr_b16 a[196:197],v1 offset:6160
v_lshl_add_u64 v[24:25],v[48:49],0,v[50:51]
v_lshlrev_b32_sdwa v61,1,v61 dst_sel:DWORD dst_unused:UNUSED_PAD src0_sel:DWORD src1_sel:WORD_1
v_pk_fma_f16 v9,v8,v8,s54
v_mfma_f32_16x16x32_bf16 v[40:43],a[200:203],a[8:11],v[40:43]
ds_read_u16 v61,v61
v_lshlrev_b32_sdwa v63,1,v63 dst_sel:DWORD dst_unused:UNUSED_PAD src0_sel:DWORD src1_sel:WORD_1
v_mfma_f32_16x16x32_bf16 v[48:51],a[200:203],a[12:15],v[20:23]
s_waitcnt lgkmcnt(8)
ds_read_u16_d16_hi v63,v63
v_or_b32 v26,v44,v46
v_pk_mul_f16 v9,v8,v9
v_mfma_f32_16x16x32_bf16 v[48:51],a[204:207],a[28:31],v[48:51]
ds_read_b64_tr_b16 a[198:199],v1 offset:7184
v_cvt_pk_bf16_f32 v27,v45,v47
v_lshlrev_b32_sdwa v32,1,v32 dst_sel:DWORD dst_unused:UNUSED_PAD src0_sel:DWORD src1_sel:WORD_1
v_mfma_f32_16x16x32_bf16 v[48:51],a[208:211],a[44:47],v[48:51]
v_exp_f16 v8,v9
v_mfma_f32_16x16x32_bf16 v[44:47],a[212:215],a[44:47],v[16:19]
ds_read_u16 v32,v32
v_lshlrev_b32_sdwa v34,1,v34 dst_sel:DWORD dst_unused:UNUSED_PAD src0_sel:DWORD src1_sel:WORD_1
v_mfma_f32_16x16x32_bf16 v[44:47],a[220:223],a[28:31],v[44:47]
ds_read_u16_d16_hi v34,v34
v_lshlrev_b32_sdwa v33,1,v33 dst_sel:DWORD dst_unused:UNUSED_PAD src0_sel:DWORD src1_sel:WORD_1
v_mfma_f32_16x16x32_bf16 v[44:47],a[216:219],a[12:15],v[44:47]
v_exp_f16_sdwa v8,v9 dst_sel:WORD_1 dst_unused:UNUSED_PRESERVE src0_sel:WORD_1
v_mfma_f32_16x16x32_bf16 v[208:211],a[240:243],v[28:31],v[208:211]
ds_read_u16 v33,v33
v_lshlrev_b32_sdwa v35,1,v35 dst_sel:DWORD dst_unused:UNUSED_PAD src0_sel:DWORD src1_sel:WORD_1
v_mfma_f32_16x16x32_bf16 v[212:215],a[240:243],v[24:27],v[212:215]
ds_read_u16_d16_hi v35,v35
v_lshlrev_b32_sdwa v52,1,v52 dst_sel:DWORD dst_unused:UNUSED_PAD src0_sel:DWORD src1_sel:WORD_1
v_mfma_f32_16x16x32_bf16 v[172:175],a[228:231],v[24:27],v[172:175]
ds_read_u16 v52,v52
v_pk_fma_f16 v8,v8,s55,s55
v_mfma_f32_16x16x32_bf16 v[168:171],a[228:231],v[28:31],v[168:171]
ds_read_b64_tr_b16 a[240:241],v3 offset:6224
v_rcp_f16 v9,v8
v_lshlrev_b32_sdwa v54,1,v54 dst_sel:DWORD dst_unused:UNUSED_PAD src0_sel:DWORD src1_sel:WORD_1
v_mfma_f32_16x16x32_bf16 v[216:219],a[248:251],v[28:31],v[216:219]
ds_read_u16_d16_hi v54,v54
v_lshlrev_b32_sdwa v53,1,v53 dst_sel:DWORD dst_unused:UNUSED_PAD src0_sel:DWORD src1_sel:WORD_1
v_mfma_f32_16x16x32_bf16 v[220:223],a[248:251],v[24:27],v[220:223]
ds_read_u16 v53,v53
ds_read_b64_tr_b16 a[242:243],v3 offset:7760
v_mfma_f32_16x16x32_bf16 v[188:191],a[236:239],v[24:27],v[188:191]
ds_read_b64_tr_b16 a[228:229],v3 offset:6232
v_lshlrev_b32_sdwa v55,1,v55 dst_sel:DWORD dst_unused:UNUSED_PAD src0_sel:DWORD src1_sel:WORD_1
v_mfma_f32_16x16x32_bf16 v[184:187],a[236:239],v[28:31],v[184:187]
ds_read_u16_d16_hi v55,v55
v_rcp_f16_sdwa v9,v8 dst_sel:WORD_1 dst_unused:UNUSED_PRESERVE src0_sel:WORD_1
v_mfma_f32_16x16x32_bf16 v[224:227],a[252:255],v[28:31],v[224:227]
ds_read_b64_tr_b16 a[230:231],v3 offset:7768
ds_read_b64_tr_b16 a[248:249],v3 offset:6288
v_lshlrev_b32_sdwa v56,1,v56 dst_sel:DWORD dst_unused:UNUSED_PAD src0_sel:DWORD src1_sel:WORD_1
v_mfma_f32_16x16x32_bf16 v[228:231],a[252:255],v[24:27],v[228:231]
ds_read_u16 v56,v56
v_lshlrev_b32_sdwa v58,1,v58 dst_sel:DWORD dst_unused:UNUSED_PAD src0_sel:DWORD src1_sel:WORD_1
v_mfma_f32_16x16x32_bf16 v[204:207],a[224:227],v[24:27],v[204:207]
ds_read_u16_d16_hi v58,v58
v_fma_mix_f32 v57,v57,v9,0 op_sel_hi:[0,1,0]
v_mfma_f32_16x16x32_bf16 v[200:203],a[224:227],v[28:31],v[200:203]
ds_read_b64_tr_b16 a[250:251],v3 offset:7824
v_fma_mix_f32 v59,v59,v9,0 op_sel:[0,1,0] op_sel_hi:[0,1,0]
v_mfma_f32_16x16x32_bf16 v[24:27],a[216:219],a[48:51],v[16:19]
ds_read_b64_tr_b16 a[236:237],v3 offset:6296
v_lshlrev_b32_sdwa v36,1,v36 dst_sel:DWORD dst_unused:UNUSED_PAD src0_sel:DWORD src1_sel:WORD_1
v_mfma_f32_16x16x32_bf16 v[24:27],a[220:223],a[64:67],v[24:27]
ds_read_b64_tr_b16 a[238:239],v3 offset:7832
v_fma_mixlo_f16 v8,v49,s99,0
v_mfma_f32_16x16x32_bf16 v[24:27],a[212:215],a[80:83],v[24:27]
s_waitcnt lgkmcnt(9)
ds_read_u16 v36,v36
v_lshl_add_u64 v[60:61],v[60:61],0,v[62:63]
v_lshlrev_b32_sdwa v38,1,v38 dst_sel:DWORD dst_unused:UNUSED_PAD src0_sel:DWORD src1_sel:WORD_1
v_mfma_f32_16x16x32_bf16 v[28:31],a[208:211],a[80:83],v[20:23]
ds_read_u16_d16_hi v38,v38
v_lshl_add_u64 v[62:63],v[32:33],0,v[34:35]
v_fma_mixhi_f16 v8,v51,s99,0
v_mfma_f32_16x16x32_bf16 v[28:31],a[204:207],a[64:67],v[28:31]
s_waitcnt lgkmcnt(9)
ds_read_b64_tr_b16 a[192:193],v1 offset:8208
v_lshl_add_u64 v[32:33],v[52:53],0,v[54:55]
v_lshlrev_b32_sdwa v37,1,v37 dst_sel:DWORD dst_unused:UNUSED_PAD src0_sel:DWORD src1_sel:WORD_1
v_pk_fma_f16 v9,v8,v8,s54
v_mfma_f32_16x16x32_bf16 v[28:31],a[200:203],a[48:51],v[28:31]
ds_read_u16 v37,v37
v_lshlrev_b32_sdwa v39,1,v39 dst_sel:DWORD dst_unused:UNUSED_PAD src0_sel:DWORD src1_sel:WORD_1
v_mfma_f32_16x16x32_bf16 v[52:55],a[200:203],a[52:55],v[20:23]
s_waitcnt lgkmcnt(7)
ds_read_u16_d16_hi v39,v39
v_or_b32 v34,v56,v58
v_pk_mul_f16 v9,v8,v9
v_mfma_f32_16x16x32_bf16 v[52:55],a[204:207],a[68:71],v[52:55]
ds_read_b64_tr_b16 a[194:195],v1 offset:9232
v_cvt_pk_bf16_f32 v35,v57,v59
v_lshlrev_b32_sdwa v40,1,v40 dst_sel:DWORD dst_unused:UNUSED_PAD src0_sel:DWORD src1_sel:WORD_1
v_mfma_f32_16x16x32_bf16 v[52:55],a[208:211],a[84:87],v[52:55]
v_exp_f16 v8,v9
v_mfma_f32_16x16x32_bf16 v[56:59],a[212:215],a[84:87],v[16:19]
ds_read_u16 v40,v40
v_lshlrev_b32_sdwa v42,1,v42 dst_sel:DWORD dst_unused:UNUSED_PAD src0_sel:DWORD src1_sel:WORD_1
v_mfma_f32_16x16x32_bf16 v[56:59],a[220:223],a[68:71],v[56:59]
ds_read_u16_d16_hi v42,v42
v_lshlrev_b32_sdwa v41,1,v41 dst_sel:DWORD dst_unused:UNUSED_PAD src0_sel:DWORD src1_sel:WORD_1
v_mfma_f32_16x16x32_bf16 v[56:59],a[216:219],a[52:55],v[56:59]
v_exp_f16_sdwa v8,v9 dst_sel:WORD_1 dst_unused:UNUSED_PRESERVE src0_sel:WORD_1
v_mfma_f32_16x16x32_bf16 v[136:139],a[232:235],v[60:63],v[136:139]
ds_read_u16 v41,v41
v_lshlrev_b32_sdwa v43,1,v43 dst_sel:DWORD dst_unused:UNUSED_PAD src0_sel:DWORD src1_sel:WORD_1
v_mfma_f32_16x16x32_bf16 v[140:143],a[232:235],v[32:35],v[140:143]
ds_read_u16_d16_hi v43,v43
v_lshlrev_b32_sdwa v44,1,v44 dst_sel:DWORD dst_unused:UNUSED_PAD src0_sel:DWORD src1_sel:WORD_1
v_mfma_f32_16x16x32_bf16 v[68:71],a[244:247],v[32:35],v[68:71]
ds_read_u16 v44,v44
v_pk_fma_f16 v8,v8,s55,s55
v_mfma_f32_16x16x32_bf16 v[64:67],a[244:247],v[60:63],v[64:67]
v_rcp_f16 v9,v8
v_lshlrev_b32_sdwa v46,1,v46 dst_sel:DWORD dst_unused:UNUSED_PAD src0_sel:DWORD src1_sel:WORD_1
v_mfma_f32_16x16x32_bf16 v[144:147],a[240:243],v[60:63],v[144:147]
ds_read_u16_d16_hi v46,v46
v_lshlrev_b32_sdwa v45,1,v45 dst_sel:DWORD dst_unused:UNUSED_PAD src0_sel:DWORD src1_sel:WORD_1
v_mfma_f32_16x16x32_bf16 v[148:151],a[240:243],v[32:35],v[148:151]
ds_read_u16 v45,v45
v_lshlrev_b32_sdwa v47,1,v47 dst_sel:DWORD dst_unused:UNUSED_PAD src0_sel:DWORD src1_sel:WORD_1
v_mfma_f32_16x16x32_bf16 v[84:87],a[228:231],v[32:35],v[84:87]
ds_read_u16_d16_hi v47,v47
v_lshlrev_b32_sdwa v48,1,v48 dst_sel:DWORD dst_unused:UNUSED_PAD src0_sel:DWORD src1_sel:WORD_1
v_mfma_f32_16x16x32_bf16 v[80:83],a[228:231],v[60:63],v[80:83]
s_waitcnt lgkmcnt(6)
v_rcp_f16_sdwa v9,v8 dst_sel:WORD_1 dst_unused:UNUSED_PRESERVE src0_sel:WORD_1
v_mfma_f32_16x16x32_bf16 v[152:155],a[248:251],v[60:63],v[152:155]
ds_read_u16 v48,v48
v_lshlrev_b32_sdwa v50,1,v50 dst_sel:DWORD dst_unused:UNUSED_PAD src0_sel:DWORD src1_sel:WORD_1
v_mfma_f32_16x16x32_bf16 v[156:159],a[248:251],v[32:35],v[156:159]
ds_read_u16_d16_hi v50,v50
v_fma_mix_f32 v49,v49,v9,0 op_sel_hi:[0,1,0]
v_mfma_f32_16x16x32_bf16 v[100:103],a[236:239],v[32:35],v[100:103]
v_fma_mix_f32 v51,v51,v9,0 op_sel:[0,1,0] op_sel_hi:[0,1,0]
v_mfma_f32_16x16x32_bf16 v[96:99],a[236:239],v[60:63],v[96:99]
v_mfma_f32_16x16x32_bf16 v[32:35],a[216:219],a[56:59],v[16:19]
v_lshlrev_b32_sdwa v24,1,v24 dst_sel:DWORD dst_unused:UNUSED_PAD src0_sel:DWORD src1_sel:WORD_1
v_fma_mixlo_f16 v8,v53,s99,0
v_mfma_f32_16x16x32_bf16 v[32:35],a[220:223],a[72:75],v[32:35]
ds_read_u16 v24,v24
v_lshlrev_b32_sdwa v26,1,v26 dst_sel:DWORD dst_unused:UNUSED_PAD src0_sel:DWORD src1_sel:WORD_1
v_mfma_f32_16x16x32_bf16 v[32:35],a[212:215],a[88:91],v[32:35]
ds_read_u16_d16_hi v26,v26
v_lshl_add_u64 v[36:37],v[36:37],0,v[38:39]
v_mfma_f32_16x16x32_bf16 v[60:63],a[208:211],a[88:91],v[20:23]
s_waitcnt lgkmcnt(4)
v_lshl_add_u64 v[38:39],v[40:41],0,v[42:43]
v_fma_mixhi_f16 v8,v55,s99,0
v_mfma_f32_16x16x32_bf16 v[60:63],a[204:207],a[72:75],v[60:63]
v_lshl_add_u64 v[40:41],v[44:45],0,v[46:47]
v_lshlrev_b32_sdwa v25,1,v25 dst_sel:DWORD dst_unused:UNUSED_PAD src0_sel:DWORD src1_sel:WORD_1
v_pk_fma_f16 v9,v8,v8,s54
v_mfma_f32_16x16x32_bf16 v[60:63],a[200:203],a[56:59],v[60:63]
ds_read_u16 v25,v25
v_lshlrev_b32_sdwa v27,1,v27 dst_sel:DWORD dst_unused:UNUSED_PAD src0_sel:DWORD src1_sel:WORD_1
v_mfma_f32_16x16x32_bf16 v[44:47],a[200:203],a[60:63],v[20:23]
s_waitcnt lgkmcnt(3)
ds_read_b128 v[20:23],v4 offset:1312
v_or_b32 v42,v48,v50
v_pk_mul_f16 v9,v8,v9
v_mfma_f32_16x16x32_bf16 v[44:47],a[204:207],a[76:79],v[44:47]
ds_read_u16_d16_hi v27,v27
v_cvt_pk_bf16_f32 v43,v49,v51
v_lshlrev_b32_sdwa v28,1,v28 dst_sel:DWORD dst_unused:UNUSED_PAD src0_sel:DWORD src1_sel:WORD_1
v_mfma_f32_16x16x32_bf16 v[44:47],a[208:211],a[92:95],v[44:47]
ds_read_b64_tr_b16 a[200:201],v1 offset:10256
v_exp_f16 v8,v9
v_mfma_f32_16x16x32_bf16 v[48:51],a[212:215],a[92:95],v[16:19]
ds_read_b128 v[16:19],v4 offset:1280
ds_read_b64_tr_b16 a[202:203],v1 offset:11280
v_lshlrev_b32_sdwa v30,1,v30 dst_sel:DWORD dst_unused:UNUSED_PAD src0_sel:DWORD src1_sel:WORD_1
v_mfma_f32_16x16x32_bf16 v[48:51],a[220:223],a[76:79],v[48:51]
ds_read_b64_tr_b16 a[204:205],v2 offset:10256
ds_read_b64_tr_b16 a[206:207],v2 offset:11280
v_lshlrev_b32_sdwa v29,1,v29 dst_sel:DWORD dst_unused:UNUSED_PAD src0_sel:DWORD src1_sel:WORD_1
v_mfma_f32_16x16x32_bf16 v[48:51],a[216:219],a[60:63],v[48:51]
ds_read_u16 v28,v28
v_exp_f16_sdwa v8,v9 dst_sel:WORD_1 dst_unused:UNUSED_PRESERVE src0_sel:WORD_1
v_mfma_f32_16x16x32_bf16 v[112:115],a[232:235],v[36:39],v[112:115]
ds_read_u16_d16_hi v30,v30
v_lshlrev_b32_sdwa v31,1,v31 dst_sel:DWORD dst_unused:UNUSED_PAD src0_sel:DWORD src1_sel:WORD_1
v_mfma_f32_16x16x32_bf16 v[116:119],a[232:235],v[40:43],v[116:119]
ds_read_u16 v29,v29
v_lshlrev_b32_sdwa v56,1,v56 dst_sel:DWORD dst_unused:UNUSED_PAD src0_sel:DWORD src1_sel:WORD_1
v_mfma_f32_16x16x32_bf16 v[76:79],a[244:247],v[40:43],v[76:79]
ds_read_u16_d16_hi v31,v31
v_pk_fma_f16 v8,v8,s55,s55
v_mfma_f32_16x16x32_bf16 v[72:75],a[244:247],v[36:39],v[72:75]
v_rcp_f16 v9,v8
v_lshlrev_b32_sdwa v58,1,v58 dst_sel:DWORD dst_unused:UNUSED_PAD src0_sel:DWORD src1_sel:WORD_1
v_mfma_f32_16x16x32_bf16 v[120:123],a[240:243],v[36:39],v[120:123]
ds_read_b64_tr_b16 a[208:209],v2 offset:8208
v_lshlrev_b32_sdwa v57,1,v57 dst_sel:DWORD dst_unused:UNUSED_PAD src0_sel:DWORD src1_sel:WORD_1
v_mfma_f32_16x16x32_bf16 v[124:127],a[240:243],v[40:43],v[124:127]
ds_read_b64_tr_b16 a[210:211],v2 offset:9232
v_lshlrev_b32_sdwa v59,1,v59 dst_sel:DWORD dst_unused:UNUSED_PAD src0_sel:DWORD src1_sel:WORD_1
v_mfma_f32_16x16x32_bf16 v[92:95],a[228:231],v[40:43],v[92:95]
ds_read_u16 v56,v56
v_lshlrev_b32_sdwa v52,1,v52 dst_sel:DWORD dst_unused:UNUSED_PAD src0_sel:DWORD src1_sel:WORD_1
v_mfma_f32_16x16x32_bf16 v[88:91],a[228:231],v[36:39],v[88:91]
ds_read_u16_d16_hi v58,v58
v_rcp_f16_sdwa v9,v8 dst_sel:WORD_1 dst_unused:UNUSED_PRESERVE src0_sel:WORD_1
v_mfma_f32_16x16x32_bf16 v[128:131],a[248:251],v[36:39],v[128:131]
ds_read_u16 v57,v57
ds_read_u16_d16_hi v59,v59
v_mfma_f32_16x16x32_bf16 v[132:135],a[248:251],v[40:43],v[132:135]
ds_read_b64_tr_b16 a[212:213],v2 offset:6160
ds_read_b64_tr_b16 a[214:215],v2 offset:7184
v_lshlrev_b32_sdwa v54,1,v54 dst_sel:DWORD dst_unused:UNUSED_PAD src0_sel:DWORD src1_sel:WORD_1
v_mfma_f32_16x16x32_bf16 v[108:111],a[236:239],v[40:43],v[108:111]
ds_read_u16 v52,v52
v_fma_mix_f32 v53,v53,v9,0 op_sel_hi:[0,1,0]
v_mfma_f32_16x16x32_bf16 v[104:107],a[236:239],v[36:39],v[104:107]
s_waitcnt lgkmcnt(9)
ds_read_u16_d16_hi v54,v54
v_fma_mix_f32 v55,v55,v9,0 op_sel:[0,1,0] op_sel_hi:[0,1,0]
v_mfma_f32_16x16x32_bf16 v[40:43],a[196:199],a[0:3],v[16:19]
v_lshlrev_b32_sdwa v32,1,v32 dst_sel:DWORD dst_unused:UNUSED_PAD src0_sel:DWORD src1_sel:WORD_1
v_fma_mixlo_f16 v8,v45,s99,0
v_mfma_f32_16x16x32_bf16 v[40:43],a[192:195],a[16:19],v[40:43]
ds_read_u16 v32,v32
v_lshlrev_b32_sdwa v34,1,v34 dst_sel:DWORD dst_unused:UNUSED_PAD src0_sel:DWORD src1_sel:WORD_1
v_mfma_f32_16x16x32_bf16 v[40:43],a[200:203],a[32:35],v[40:43]
ds_read_u16_d16_hi v34,v34
v_lshl_add_u64 v[24:25],v[24:25],0,v[26:27]
v_mfma_f32_16x16x32_bf16 v[36:39],a[204:207],a[32:35],v[20:23]
s_waitcnt lgkmcnt(9)
v_lshl_add_u64 v[26:27],v[28:29],0,v[30:31]
v_fma_mixhi_f16 v8,v47,s99,0
v_mfma_f32_16x16x32_bf16 v[36:39],a[208:211],a[16:19],v[36:39]
s_waitcnt lgkmcnt(4)
v_lshl_add_u64 v[28:29],v[56:57],0,v[58:59]
v_lshlrev_b32_sdwa v33,1,v33 dst_sel:DWORD dst_unused:UNUSED_PAD src0_sel:DWORD src1_sel:WORD_1
v_pk_fma_f16 v9,v8,v8,s54
v_mfma_f32_16x16x32_bf16 v[36:39],a[212:215],a[0:3],v[36:39]
ds_read_u16 v33,v33
v_lshlrev_b32_sdwa v35,1,v35 dst_sel:DWORD dst_unused:UNUSED_PAD src0_sel:DWORD src1_sel:WORD_1
v_mfma_f32_16x16x32_bf16 v[56:59],a[212:215],a[4:7],v[20:23]
s_waitcnt lgkmcnt(3)
ds_read_u16_d16_hi v35,v35
v_or_b32 v30,v52,v54
v_pk_mul_f16 v9,v8,v9
v_mfma_f32_16x16x32_bf16 v[56:59],a[208:211],a[20:23],v[56:59]
s_waitcnt vmcnt(6)
v_cvt_pk_bf16_f32 v31,v53,v55
v_lshlrev_b32_sdwa v60,1,v60 dst_sel:DWORD dst_unused:UNUSED_PAD src0_sel:DWORD src1_sel:WORD_1
v_mfma_f32_16x16x32_bf16 v[56:59],a[204:207],a[36:39],v[56:59]
s_barrier
v_exp_f16 v8,v9
v_mfma_f32_16x16x32_bf16 v[52:55],a[200:203],a[36:39],v[16:19]
ds_read_u16 v60,v60
v_lshlrev_b32_sdwa v62,1,v62 dst_sel:DWORD dst_unused:UNUSED_PAD src0_sel:DWORD src1_sel:WORD_1
v_mfma_f32_16x16x32_bf16 v[52:55],a[192:195],a[20:23],v[52:55]
s_add_u32 m0,s58,6160
ds_read_u16_d16_hi v62,v62
v_lshlrev_b32_sdwa v61,1,v61 dst_sel:DWORD dst_unused:UNUSED_PAD src0_sel:DWORD src1_sel:WORD_1
v_mfma_f32_16x16x32_bf16 v[52:55],a[196:199],a[4:7],v[52:55]
s_add_u32 s69,s69,s59
v_exp_f16_sdwa v8,v9 dst_sel:WORD_1 dst_unused:UNUSED_PRESERVE src0_sel:WORD_1
v_mfma_f32_16x16x32_bf16 v[232:235],a[232:235],v[24:27],v[232:235]
buffer_load_dwordx4 v5,s[44:47],s69 offen offset:0 lds
v_lshlrev_b32_sdwa v63,1,v63 dst_sel:DWORD dst_unused:UNUSED_PAD src0_sel:DWORD src1_sel:WORD_1
v_mfma_f32_16x16x32_bf16 v[236:239],a[232:235],v[28:31],v[236:239]
ds_read_u16 v61,v61
v_lshlrev_b32_sdwa v48,1,v48 dst_sel:DWORD dst_unused:UNUSED_PAD src0_sel:DWORD src1_sel:WORD_1
v_mfma_f32_16x16x32_bf16 v[164:167],a[244:247],v[28:31],v[164:167]
ds_read_u16_d16_hi v63,v63
v_pk_fma_f16 v8,v8,s55,s55
v_mfma_f32_16x16x32_bf16 v[160:163],a[244:247],v[24:27],v[160:163]
v_rcp_f16 v9,v8
v_lshlrev_b32_sdwa v50,1,v50 dst_sel:DWORD dst_unused:UNUSED_PAD src0_sel:DWORD src1_sel:WORD_1
v_mfma_f32_16x16x32_bf16 v[240:243],a[240:243],v[24:27],v[240:243]
s_add_u32 s69,s69,s53
ds_read_u16 v48,v48
v_lshlrev_b32_sdwa v49,1,v49 dst_sel:DWORD dst_unused:UNUSED_PAD src0_sel:DWORD src1_sel:WORD_1
v_mfma_f32_16x16x32_bf16 v[244:247],a[240:243],v[28:31],v[244:247]
buffer_load_dwordx4 v5,s[44:47],s69 offen offset:1024 lds
ds_read_u16_d16_hi v50,v50
v_mfma_f32_16x16x32_bf16 v[180:183],a[228:231],v[28:31],v[180:183]
ds_read_u16 v49,v49
v_lshlrev_b32_sdwa v51,1,v51 dst_sel:DWORD dst_unused:UNUSED_PAD src0_sel:DWORD src1_sel:WORD_1
v_mfma_f32_16x16x32_bf16 v[176:179],a[228:231],v[24:27],v[176:179]
v_rcp_f16_sdwa v9,v8 dst_sel:WORD_1 dst_unused:UNUSED_PRESERVE src0_sel:WORD_1
v_mfma_f32_16x16x32_bf16 v[248:251],a[248:251],v[24:27],v[248:251]
ds_read_u16_d16_hi v51,v51
v_lshlrev_b32_sdwa v44,1,v44 dst_sel:DWORD dst_unused:UNUSED_PAD src0_sel:DWORD src1_sel:WORD_1
v_mfma_f32_16x16x32_bf16 v[252:255],a[248:251],v[28:31],v[252:255]
s_add_u32 s69,s69,s53
ds_read_u16 v44,v44
v_lshlrev_b32_sdwa v46,1,v46 dst_sel:DWORD dst_unused:UNUSED_PAD src0_sel:DWORD src1_sel:WORD_1
v_mfma_f32_16x16x32_bf16 v[196:199],a[236:239],v[28:31],v[196:199]
buffer_load_dwordx4 v5,s[44:47],s69 offen offset:2048 lds
v_fma_mix_f32 v45,v45,v9,0 op_sel_hi:[0,1,0]
v_mfma_f32_16x16x32_bf16 v[192:195],a[236:239],v[24:27],v[192:195]
ds_read_u16_d16_hi v46,v46
v_fma_mix_f32 v47,v47,v9,0 op_sel:[0,1,0] op_sel_hi:[0,1,0]
v_mfma_f32_16x16x32_bf16 v[28:31],a[196:199],a[8:11],v[16:19]
ds_read_b64_tr_b16 a[252:253],v3 offset:12320
ds_read_b64_tr_b16 a[254:255],v3 offset:13856
v_lshlrev_b32_sdwa v40,1,v40 dst_sel:DWORD dst_unused:UNUSED_PAD src0_sel:DWORD src1_sel:WORD_1
v_mfma_f32_16x16x32_bf16 v[28:31],a[192:195],a[24:27],v[28:31]
ds_read_u16 v40,v40
ds_read_b64_tr_b16 a[224:225],v3 offset:12328
v_fma_mixlo_f16 v8,v57,s99,0
v_mfma_f32_16x16x32_bf16 v[28:31],a[200:203],a[40:43],v[28:31]
s_waitcnt lgkmcnt(8)
ds_read_b64_tr_b16 a[226:227],v3 offset:13864
v_lshl_add_u64 v[32:33],v[32:33],0,v[34:35]
v_lshlrev_b32_sdwa v42,1,v42 dst_sel:DWORD dst_unused:UNUSED_PAD src0_sel:DWORD src1_sel:WORD_1
v_mfma_f32_16x16x32_bf16 v[24:27],a[204:207],a[40:43],v[20:23]
ds_read_u16_d16_hi v42,v42
v_lshl_add_u64 v[34:35],v[60:61],0,v[62:63]
v_fma_mixhi_f16 v8,v59,s99,0
v_mfma_f32_16x16x32_bf16 v[24:27],a[208:211],a[24:27],v[24:27]
s_waitcnt lgkmcnt(8)
ds_read_b64_tr_b16 a[220:221],v1 offset:12320
v_lshl_add_u64 v[60:61],v[48:49],0,v[50:51]
v_lshlrev_b32_sdwa v41,1,v41 dst_sel:DWORD dst_unused:UNUSED_PAD src0_sel:DWORD src1_sel:WORD_1
v_pk_fma_f16 v9,v8,v8,s54
v_mfma_f32_16x16x32_bf16 v[24:27],a[212:215],a[8:11],v[24:27]
TP9784_%=:
s_cbranch_execz TR1_%=
ds_read_u16 v41,v41
v_lshlrev_b32_sdwa v43,1,v43 dst_sel:DWORD dst_unused:UNUSED_PAD src0_sel:DWORD src1_sel:WORD_1
v_mfma_f32_16x16x32_bf16 v[48:51],a[212:215],a[12:15],v[20:23]
s_waitcnt lgkmcnt(8)
ds_read_u16_d16_hi v43,v43
v_or_b32 v62,v44,v46
v_pk_mul_f16 v9,v8,v9
v_mfma_f32_16x16x32_bf16 v[48:51],a[208:211],a[28:31],v[48:51]
ds_read_b64_tr_b16 a[222:223],v1 offset:13344
v_cvt_pk_bf16_f32 v63,v45,v47
v_lshlrev_b32_sdwa v36,1,v36 dst_sel:DWORD dst_unused:UNUSED_PAD src0_sel:DWORD src1_sel:WORD_1
v_mfma_f32_16x16x32_bf16 v[48:51],a[204:207],a[44:47],v[48:51]
v_exp_f16 v8,v9
v_mfma_f32_16x16x32_bf16 v[44:47],a[200:203],a[44:47],v[16:19]
ds_read_u16 v36,v36
v_lshlrev_b32_sdwa v38,1,v38 dst_sel:DWORD dst_unused:UNUSED_PAD src0_sel:DWORD src1_sel:WORD_1
v_mfma_f32_16x16x32_bf16 v[44:47],a[192:195],a[28:31],v[44:47]
ds_read_u16_d16_hi v38,v38
v_lshlrev_b32_sdwa v37,1,v37 dst_sel:DWORD dst_unused:UNUSED_PAD src0_sel:DWORD src1_sel:WORD_1
v_mfma_f32_16x16x32_bf16 v[44:47],a[196:199],a[12:15],v[44:47]
v_exp_f16_sdwa v8,v9 dst_sel:WORD_1 dst_unused:UNUSED_PRESERVE src0_sel:WORD_1
v_mfma_f32_16x16x32_bf16 v[208:211],a[232:235],v[32:35],v[208:211]
ds_read_u16 v37,v37
v_lshlrev_b32_sdwa v39,1,v39 dst_sel:DWORD dst_unused:UNUSED_PAD src0_sel:DWORD src1_sel:WORD_1
v_mfma_f32_16x16x32_bf16 v[212:215],a[232:235],v[60:63],v[212:215]
ds_read_u16_d16_hi v39,v39
v_lshlrev_b32_sdwa v52,1,v52 dst_sel:DWORD dst_unused:UNUSED_PAD src0_sel:DWORD src1_sel:WORD_1
v_mfma_f32_16x16x32_bf16 v[172:175],a[244:247],v[60:63],v[172:175]
ds_read_u16 v52,v52
v_pk_fma_f16 v8,v8,s55,s55
v_mfma_f32_16x16x32_bf16 v[168:171],a[244:247],v[32:35],v[168:171]
ds_read_b64_tr_b16 a[232:233],v3 offset:12384
v_rcp_f16 v9,v8
v_lshlrev_b32_sdwa v54,1,v54 dst_sel:DWORD dst_unused:UNUSED_PAD src0_sel:DWORD src1_sel:WORD_1
v_mfma_f32_16x16x32_bf16 v[216:219],a[240:243],v[32:35],v[216:219]
ds_read_u16_d16_hi v54,v54
v_lshlrev_b32_sdwa v53,1,v53 dst_sel:DWORD dst_unused:UNUSED_PAD src0_sel:DWORD src1_sel:WORD_1
v_mfma_f32_16x16x32_bf16 v[220:223],a[240:243],v[60:63],v[220:223]
ds_read_u16 v53,v53
ds_read_b64_tr_b16 a[234:235],v3 offset:13920
v_mfma_f32_16x16x32_bf16 v[188:191],a[228:231],v[60:63],v[188:191]
ds_read_b64_tr_b16 a[244:245],v3 offset:12392
v_lshlrev_b32_sdwa v55,1,v55 dst_sel:DWORD dst_unused:UNUSED_PAD src0_sel:DWORD src1_sel:WORD_1
v_mfma_f32_16x16x32_bf16 v[184:187],a[228:231],v[32:35],v[184:187]
ds_read_u16_d16_hi v55,v55
v_rcp_f16_sdwa v9,v8 dst_sel:WORD_1 dst_unused:UNUSED_PRESERVE src0_sel:WORD_1
v_mfma_f32_16x16x32_bf16 v[224:227],a[248:251],v[32:35],v[224:227]
ds_read_b64_tr_b16 a[246:247],v3 offset:13928
ds_read_b64_tr_b16 a[240:241],v3 offset:12448
v_lshlrev_b32_sdwa v56,1,v56 dst_sel:DWORD dst_unused:UNUSED_PAD src0_sel:DWORD src1_sel:WORD_1
v_mfma_f32_16x16x32_bf16 v[228:231],a[248:251],v[60:63],v[228:231]
ds_read_u16 v56,v56
v_lshlrev_b32_sdwa v58,1,v58 dst_sel:DWORD dst_unused:UNUSED_PAD src0_sel:DWORD src1_sel:WORD_1
v_mfma_f32_16x16x32_bf16 v[204:207],a[236:239],v[60:63],v[204:207]
ds_read_u16_d16_hi v58,v58
v_fma_mix_f32 v57,v57,v9,0 op_sel_hi:[0,1,0]
v_mfma_f32_16x16x32_bf16 v[200:203],a[236:239],v[32:35],v[200:203]
ds_read_b64_tr_b16 a[242:243],v3 offset:13984
v_fma_mix_f32 v59,v59,v9,0 op_sel:[0,1,0] op_sel_hi:[0,1,0]
v_mfma_f32_16x16x32_bf16 v[60:63],a[196:199],a[48:51],v[16:19]
ds_read_b64_tr_b16 a[228:229],v3 offset:12456
v_lshlrev_b32_sdwa v28,1,v28 dst_sel:DWORD dst_unused:UNUSED_PAD src0_sel:DWORD src1_sel:WORD_1
v_mfma_f32_16x16x32_bf16 v[60:63],a[192:195],a[64:67],v[60:63]
ds_read_b64_tr_b16 a[230:231],v3 offset:13992
v_fma_mixlo_f16 v8,v49,s99,0
v_mfma_f32_16x16x32_bf16 v[60:63],a[200:203],a[80:83],v[60:63]
s_waitcnt lgkmcnt(9)
ds_read_u16 v28,v28
v_lshl_add_u64 v[40:41],v[40:41],0,v[42:43]
v_lshlrev_b32_sdwa v30,1,v30 dst_sel:DWORD dst_unused:UNUSED_PAD src0_sel:DWORD src1_sel:WORD_1
v_mfma_f32_16x16x32_bf16 v[32:35],a[204:207],a[80:83],v[20:23]
ds_read_u16_d16_hi v30,v30
v_lshl_add_u64 v[42:43],v[36:37],0,v[38:39]
v_fma_mixhi_f16 v8,v51,s99,0
v_mfma_f32_16x16x32_bf16 v[32:35],a[208:211],a[64:67],v[32:35]
s_waitcnt lgkmcnt(9)
ds_read_b64_tr_b16 a[216:217],v1 offset:14368
v_lshl_add_u64 v[36:37],v[52:53],0,v[54:55]
v_lshlrev_b32_sdwa v29,1,v29 dst_sel:DWORD dst_unused:UNUSED_PAD src0_sel:DWORD src1_sel:WORD_1
v_pk_fma_f16 v9,v8,v8,s54
v_mfma_f32_16x16x32_bf16 v[32:35],a[212:215],a[48:51],v[32:35]
ds_read_u16 v29,v29
v_lshlrev_b32_sdwa v31,1,v31 dst_sel:DWORD dst_unused:UNUSED_PAD src0_sel:DWORD src1_sel:WORD_1
v_mfma_f32_16x16x32_bf16 v[52:55],a[212:215],a[52:55],v[20:23]
s_waitcnt lgkmcnt(7)
ds_read_u16_d16_hi v31,v31
v_or_b32 v38,v56,v58
v_pk_mul_f16 v9,v8,v9
v_mfma_f32_16x16x32_bf16 v[52:55],a[208:211],a[68:71],v[52:55]
ds_read_b64_tr_b16 a[218:219],v1 offset:15392
v_cvt_pk_bf16_f32 v39,v57,v59
v_lshlrev_b32_sdwa v24,1,v24 dst_sel:DWORD dst_unused:UNUSED_PAD src0_sel:DWORD src1_sel:WORD_1
v_mfma_f32_16x16x32_bf16 v[52:55],a[204:207],a[84:87],v[52:55]
v_exp_f16 v8,v9
v_mfma_f32_16x16x32_bf16 v[56:59],a[200:203],a[84:87],v[16:19]
ds_read_u16 v24,v24
v_lshlrev_b32_sdwa v26,1,v26 dst_sel:DWORD dst_unused:UNUSED_PAD src0_sel:DWORD src1_sel:WORD_1
v_mfma_f32_16x16x32_bf16 v[56:59],a[192:195],a[68:71],v[56:59]
ds_read_u16_d16_hi v26,v26
v_lshlrev_b32_sdwa v25,1,v25 dst_sel:DWORD dst_unused:UNUSED_PAD src0_sel:DWORD src1_sel:WORD_1
v_mfma_f32_16x16x32_bf16 v[56:59],a[196:199],a[52:55],v[56:59]
v_exp_f16_sdwa v8,v9 dst_sel:WORD_1 dst_unused:UNUSED_PRESERVE src0_sel:WORD_1
v_mfma_f32_16x16x32_bf16 v[136:139],a[252:255],v[40:43],v[136:139]
ds_read_u16 v25,v25
v_lshlrev_b32_sdwa v27,1,v27 dst_sel:DWORD dst_unused:UNUSED_PAD src0_sel:DWORD src1_sel:WORD_1
v_mfma_f32_16x16x32_bf16 v[140:143],a[252:255],v[36:39],v[140:143]
ds_read_u16_d16_hi v27,v27
v_lshlrev_b32_sdwa v44,1,v44 dst_sel:DWORD dst_unused:UNUSED_PAD src0_sel:DWORD src1_sel:WORD_1
v_mfma_f32_16x16x32_bf16 v[68:71],a[224:227],v[36:39],v[68:71]
ds_read_u16 v44,v44
v_pk_fma_f16 v8,v8,s55,s55
v_mfma_f32_16x16x32_bf16 v[64:67],a[224:227],v[40:43],v[64:67]
v_rcp_f16 v9,v8
v_lshlrev_b32_sdwa v46,1,v46 dst_sel:DWORD dst_unused:UNUSED_PAD src0_sel:DWORD src1_sel:WORD_1
v_mfma_f32_16x16x32_bf16 v[144:147],a[232:235],v[40:43],v[144:147]
ds_read_u16_d16_hi v46,v46
v_lshlrev_b32_sdwa v45,1,v45 dst_sel:DWORD dst_unused:UNUSED_PAD src0_sel:DWORD src1_sel:WORD_1
v_mfma_f32_16x16x32_bf16 v[148:151],a[232:235],v[36:39],v[148:151]
ds_read_u16 v45,v45
v_lshlrev_b32_sdwa v47,1,v47 dst_sel:DWORD dst_unused:UNUSED_PAD src0_sel:DWORD src1_sel:WORD_1
v_mfma_f32_16x16x32_bf16 v[84:87],a[244:247],v[36:39],v[84:87]
ds_read_u16_d16_hi v47,v47
v_lshlrev_b32_sdwa v48,1,v48 dst_sel:DWORD dst_unused:UNUSED_PAD src0_sel:DWORD src1_sel:WORD_1
v_mfma_f32_16x16x32_bf16 v[80:83],a[244:247],v[40:43],v[80:83]
s_waitcnt lgkmcnt(6)
v_rcp_f16_sdwa v9,v8 dst_sel:WORD_1 dst_unused:UNUSED_PRESERVE src0_sel:WORD_1
v_mfma_f32_16x16x32_bf16 v[152:155],a[240:243],v[40:43],v[152:155]
ds_read_u16 v48,v48
v_lshlrev_b32_sdwa v50,1,v50 dst_sel:DWORD dst_unused:UNUSED_PAD src0_sel:DWORD src1_sel:WORD_1
v_mfma_f32_16x16x32_bf16 v[156:159],a[240:243],v[36:39],v[156:159]
ds_read_u16_d16_hi v50,v50
v_fma_mix_f32 v49,v49,v9,0 op_sel_hi:[0,1,0]
v_mfma_f32_16x16x32_bf16 v[100:103],a[228:231],v[36:39],v[100:103]
v_fma_mix_f32 v51,v51,v9,0 op_sel:[0,1,0] op_sel_hi:[0,1,0]
v_mfma_f32_16x16x32_bf16 v[96:99],a[228:231],v[40:43],v[96:99]
v_mfma_f32_16x16x32_bf16 v[36:39],a[196:199],a[56:59],v[16:19]
v_lshlrev_b32_sdwa v60,1,v60 dst_sel:DWORD dst_unused:UNUSED_PAD src0_sel:DWORD src1_sel:WORD_1
v_fma_mixlo_f16 v8,v53,s99,0
v_mfma_f32_16x16x32_bf16 v[36:39],a[192:195],a[72:75],v[36:39]
ds_read_u16 v60,v60
v_lshlrev_b32_sdwa v62,1,v62 dst_sel:DWORD dst_unused:UNUSED_PAD src0_sel:DWORD src1_sel:WORD_1
v_mfma_f32_16x16x32_bf16 v[36:39],a[200:203],a[88:91],v[36:39]
ds_read_u16_d16_hi v62,v62
v_lshl_add_u64 v[28:29],v[28:29],0,v[30:31]
v_mfma_f32_16x16x32_bf16 v[40:43],a[204:207],a[88:91],v[20:23]
s_waitcnt lgkmcnt(4)
v_lshl_add_u64 v[30:31],v[24:25],0,v[26:27]
v_fma_mixhi_f16 v8,v55,s99,0
v_mfma_f32_16x16x32_bf16 v[40:43],a[208:211],a[72:75],v[40:43]
v_lshl_add_u64 v[24:25],v[44:45],0,v[46:47]
v_lshlrev_b32_sdwa v61,1,v61 dst_sel:DWORD dst_unused:UNUSED_PAD src0_sel:DWORD src1_sel:WORD_1
v_pk_fma_f16 v9,v8,v8,s54
v_mfma_f32_16x16x32_bf16 v[40:43],a[212:215],a[56:59],v[40:43]
ds_read_u16 v61,v61
v_lshlrev_b32_sdwa v63,1,v63 dst_sel:DWORD dst_unused:UNUSED_PAD src0_sel:DWORD src1_sel:WORD_1
v_mfma_f32_16x16x32_bf16 v[44:47],a[212:215],a[60:63],v[20:23]
s_waitcnt lgkmcnt(3)
ds_read_b128 v[20:23],v4 offset:1440
v_or_b32 v26,v48,v50
v_pk_mul_f16 v9,v8,v9
v_mfma_f32_16x16x32_bf16 v[44:47],a[208:211],a[76:79],v[44:47]
ds_read_u16_d16_hi v63,v63
v_cvt_pk_bf16_f32 v27,v49,v51
v_lshlrev_b32_sdwa v32,1,v32 dst_sel:DWORD dst_unused:UNUSED_PAD src0_sel:DWORD src1_sel:WORD_1
v_mfma_f32_16x16x32_bf16 v[44:47],a[204:207],a[92:95],v[44:47]
ds_read_b64_tr_b16 a[212:213],v1 offset:16416
v_exp_f16 v8,v9
v_mfma_f32_16x16x32_bf16 v[48:51],a[200:203],a[92:95],v[16:19]
ds_read_b128 v[16:19],v4 offset:1408
ds_read_b64_tr_b16 a[214:215],v1 offset:17440
v_lshlrev_b32_sdwa v34,1,v34 dst_sel:DWORD dst_unused:UNUSED_PAD src0_sel:DWORD src1_sel:WORD_1
v_mfma_f32_16x16x32_bf16 v[48:51],a[192:195],a[76:79],v[48:51]
ds_read_b64_tr_b16 a[208:209],v2 offset:16416
ds_read_b64_tr_b16 a[210:211],v2 offset:17440
v_lshlrev_b32_sdwa v33,1,v33 dst_sel:DWORD dst_unused:UNUSED_PAD src0_sel:DWORD src1_sel:WORD_1
v_mfma_f32_16x16x32_bf16 v[48:51],a[196:199],a[60:63],v[48:51]
ds_read_u16 v32,v32
v_exp_f16_sdwa v8,v9 dst_sel:WORD_1 dst_unused:UNUSED_PRESERVE src0_sel:WORD_1
v_mfma_f32_16x16x32_bf16 v[112:115],a[252:255],v[28:31],v[112:115]
ds_read_u16_d16_hi v34,v34
v_lshlrev_b32_sdwa v35,1,v35 dst_sel:DWORD dst_unused:UNUSED_PAD src0_sel:DWORD src1_sel:WORD_1
v_mfma_f32_16x16x32_bf16 v[116:119],a[252:255],v[24:27],v[116:119]
ds_read_u16 v33,v33
v_lshlrev_b32_sdwa v56,1,v56 dst_sel:DWORD dst_unused:UNUSED_PAD src0_sel:DWORD src1_sel:WORD_1
v_mfma_f32_16x16x32_bf16 v[76:79],a[224:227],v[24:27],v[76:79]
ds_read_u16_d16_hi v35,v35
v_pk_fma_f16 v8,v8,s55,s55
v_mfma_f32_16x16x32_bf16 v[72:75],a[224:227],v[28:31],v[72:75]
v_rcp_f16 v9,v8
v_lshlrev_b32_sdwa v58,1,v58 dst_sel:DWORD dst_unused:UNUSED_PAD src0_sel:DWORD src1_sel:WORD_1
v_mfma_f32_16x16x32_bf16 v[120:123],a[232:235],v[28:31],v[120:123]
ds_read_b64_tr_b16 a[204:205],v2 offset:14368
v_lshlrev_b32_sdwa v57,1,v57 dst_sel:DWORD dst_unused:UNUSED_PAD src0_sel:DWORD src1_sel:WORD_1
v_mfma_f32_16x16x32_bf16 v[124:127],a[232:235],v[24:27],v[124:127]
ds_read_b64_tr_b16 a[206:207],v2 offset:15392
v_lshlrev_b32_sdwa v59,1,v59 dst_sel:DWORD dst_unused:UNUSED_PAD src0_sel:DWORD src1_sel:WORD_1
v_mfma_f32_16x16x32_bf16 v[92:95],a[244:247],v[24:27],v[92:95]
ds_read_u16 v56,v56
v_lshlrev_b32_sdwa v52,1,v52 dst_sel:DWORD dst_unused:UNUSED_PAD src0_sel:DWORD src1_sel:WORD_1
v_mfma_f32_16x16x32_bf16 v[88:91],a[244:247],v[28:31],v[88:91]
ds_read_u16_d16_hi v58,v58
v_rcp_f16_sdwa v9,v8 dst_sel:WORD_1 dst_unused:UNUSED_PRESERVE src0_sel:WORD_1
v_mfma_f32_16x16x32_bf16 v[128:131],a[240:243],v[28:31],v[128:131]
ds_read_u16 v57,v57
ds_read_u16_d16_hi v59,v59
v_mfma_f32_16x16x32_bf16 v[132:135],a[240:243],v[24:27],v[132:135]
ds_read_b64_tr_b16 a[200:201],v2 offset:12320
ds_read_b64_tr_b16 a[202:203],v2 offset:13344
v_lshlrev_b32_sdwa v54,1,v54 dst_sel:DWORD dst_unused:UNUSED_PAD src0_sel:DWORD src1_sel:WORD_1
v_mfma_f32_16x16x32_bf16 v[108:111],a[228:231],v[24:27],v[108:111]
ds_read_u16 v52,v52
v_fma_mix_f32 v53,v53,v9,0 op_sel_hi:[0,1,0]
v_mfma_f32_16x16x32_bf16 v[104:107],a[228:231],v[28:31],v[104:107]
s_waitcnt lgkmcnt(9)
ds_read_u16_d16_hi v54,v54
v_fma_mix_f32 v55,v55,v9,0 op_sel:[0,1,0] op_sel_hi:[0,1,0]
v_mfma_f32_16x16x32_bf16 v[24:27],a[220:223],a[0:3],v[16:19]
v_lshlrev_b32_sdwa v36,1,v36 dst_sel:DWORD dst_unused:UNUSED_PAD src0_sel:DWORD src1_sel:WORD_1
v_fma_mixlo_f16 v8,v45,s99,0
v_mfma_f32_16x16x32_bf16 v[24:27],a[216:219],a[16:19],v[24:27]
ds_read_u16 v36,v36
v_lshlrev_b32_sdwa v38,1,v38 dst_sel:DWORD dst_unused:UNUSED_PAD src0_sel:DWORD src1_sel:WORD_1
v_mfma_f32_16x16x32_bf16 v[24:27],a[212:215],a[32:35],v[24:27]
ds_read_u16_d16_hi v38,v38
v_lshl_add_u64 v[60:61],v[60:61],0,v[62:63]
v_mfma_f32_16x16x32_bf16 v[28:31],a[208:211],a[32:35],v[20:23]
s_waitcnt lgkmcnt(9)
v_lshl_add_u64 v[62:63],v[32:33],0,v[34:35]
v_fma_mixhi_f16 v8,v47,s99,0
v_mfma_f32_16x16x32_bf16 v[28:31],a[204:207],a[16:19],v[28:31]
s_waitcnt lgkmcnt(4)
v_lshl_add_u64 v[32:33],v[56:57],0,v[58:59]
v_lshlrev_b32_sdwa v37,1,v37 dst_sel:DWORD dst_unused:UNUSED_PAD src0_sel:DWORD src1_sel:WORD_1
v_pk_fma_f16 v9,v8,v8,s54
v_mfma_f32_16x16x32_bf16 v[28:31],a[200:203],a[0:3],v[28:31]
ds_read_u16 v37,v37
v_lshlrev_b32_sdwa v39,1,v39 dst_sel:DWORD dst_unused:UNUSED_PAD src0_sel:DWORD src1_sel:WORD_1
v_mfma_f32_16x16x32_bf16 v[56:59],a[200:203],a[4:7],v[20:23]
s_waitcnt lgkmcnt(3)
ds_read_u16_d16_hi v39,v39
v_or_b32 v34,v52,v54
v_pk_mul_f16 v9,v8,v9
v_mfma_f32_16x16x32_bf16 v[56:59],a[204:207],a[20:23],v[56:59]
s_waitcnt vmcnt(6)
v_cvt_pk_bf16_f32 v35,v53,v55
v_lshlrev_b32_sdwa v40,1,v40 dst_sel:DWORD dst_unused:UNUSED_PAD src0_sel:DWORD src1_sel:WORD_1
v_mfma_f32_16x16x32_bf16 v[56:59],a[208:211],a[36:39],v[56:59]
s_barrier
v_exp_f16 v8,v9
v_mfma_f32_16x16x32_bf16 v[52:55],a[212:215],a[36:39],v[16:19]
ds_read_u16 v40,v40
v_lshlrev_b32_sdwa v42,1,v42 dst_sel:DWORD dst_unused:UNUSED_PAD src0_sel:DWORD src1_sel:WORD_1
v_mfma_f32_16x16x32_bf16 v[52:55],a[216:219],a[20:23],v[52:55]
s_add_u32 m0,s58,12320
ds_read_u16_d16_hi v42,v42
v_lshlrev_b32_sdwa v41,1,v41 dst_sel:DWORD dst_unused:UNUSED_PAD src0_sel:DWORD src1_sel:WORD_1
v_mfma_f32_16x16x32_bf16 v[52:55],a[220:223],a[4:7],v[52:55]
s_add_u32 s69,s69,s59
v_exp_f16_sdwa v8,v9 dst_sel:WORD_1 dst_unused:UNUSED_PRESERVE src0_sel:WORD_1
v_mfma_f32_16x16x32_bf16 v[232:235],a[252:255],v[60:63],v[232:235]
buffer_load_dwordx4 v5,s[44:47],s69 offen offset:0 lds
v_lshlrev_b32_sdwa v43,1,v43 dst_sel:DWORD dst_unused:UNUSED_PAD src0_sel:DWORD src1_sel:WORD_1
v_mfma_f32_16x16x32_bf16 v[236:239],a[252:255],v[32:35],v[236:239]
ds_read_u16 v41,v41
v_lshlrev_b32_sdwa v48,1,v48 dst_sel:DWORD dst_unused:UNUSED_PAD src0_sel:DWORD src1_sel:WORD_1
v_mfma_f32_16x16x32_bf16 v[164:167],a[224:227],v[32:35],v[164:167]
ds_read_u16_d16_hi v43,v43
v_pk_fma_f16 v8,v8,s55,s55
v_mfma_f32_16x16x32_bf16 v[160:163],a[224:227],v[60:63],v[160:163]
v_rcp_f16 v9,v8
v_lshlrev_b32_sdwa v50,1,v50 dst_sel:DWORD dst_unused:UNUSED_PAD src0_sel:DWORD src1_sel:WORD_1
v_mfma_f32_16x16x32_bf16 v[240:243],a[232:235],v[60:63],v[240:243]
s_add_u32 s69,s69,s53
ds_read_u16 v48,v48
v_lshlrev_b32_sdwa v49,1,v49 dst_sel:DWORD dst_unused:UNUSED_PAD src0_sel:DWORD src1_sel:WORD_1
v_mfma_f32_16x16x32_bf16 v[244:247],a[232:235],v[32:35],v[244:247]
buffer_load_dwordx4 v5,s[44:47],s69 offen offset:1024 lds
ds_read_u16_d16_hi v50,v50
v_mfma_f32_16x16x32_bf16 v[180:183],a[244:247],v[32:35],v[180:183]
ds_read_u16 v49,v49
v_lshlrev_b32_sdwa v51,1,v51 dst_sel:DWORD dst_unused:UNUSED_PAD src0_sel:DWORD src1_sel:WORD_1
v_mfma_f32_16x16x32_bf16 v[176:179],a[244:247],v[60:63],v[176:179]
v_rcp_f16_sdwa v9,v8 dst_sel:WORD_1 dst_unused:UNUSED_PRESERVE src0_sel:WORD_1
v_mfma_f32_16x16x32_bf16 v[248:251],a[240:243],v[60:63],v[248:251]
ds_read_u16_d16_hi v51,v51
v_lshlrev_b32_sdwa v44,1,v44 dst_sel:DWORD dst_unused:UNUSED_PAD src0_sel:DWORD src1_sel:WORD_1
v_mfma_f32_16x16x32_bf16 v[252:255],a[240:243],v[32:35],v[252:255]
s_add_u32 s69,s69,s53
ds_read_u16 v44,v44
v_lshlrev_b32_sdwa v46,1,v46 dst_sel:DWORD dst_unused:UNUSED_PAD src0_sel:DWORD src1_sel:WORD_1
v_mfma_f32_16x16x32_bf16 v[196:199],a[228:231],v[32:35],v[196:199]
buffer_load_dwordx4 v5,s[44:47],s69 offen offset:2048 lds
v_fma_mix_f32 v45,v45,v9,0 op_sel_hi:[0,1,0]
v_mfma_f32_16x16x32_bf16 v[192:195],a[228:231],v[60:63],v[192:195]
ds_read_u16_d16_hi v46,v46
v_fma_mix_f32 v47,v47,v9,0 op_sel:[0,1,0] op_sel_hi:[0,1,0]
v_mfma_f32_16x16x32_bf16 v[32:35],a[220:223],a[8:11],v[16:19]
ds_read_b64_tr_b16 a[248:249],v3 offset:18480
ds_read_b64_tr_b16 a[250:251],v3 offset:20016
v_lshlrev_b32_sdwa v24,1,v24 dst_sel:DWORD dst_unused:UNUSED_PAD src0_sel:DWORD src1_sel:WORD_1
v_mfma_f32_16x16x32_bf16 v[32:35],a[216:219],a[24:27],v[32:35]
ds_read_u16 v24,v24
ds_read_b64_tr_b16 a[236:237],v3 offset:18488
v_fma_mixlo_f16 v8,v57,s99,0
v_mfma_f32_16x16x32_bf16 v[32:35],a[212:215],a[40:43],v[32:35]
s_waitcnt lgkmcnt(8)
ds_read_b64_tr_b16 a[238:239],v3 offset:20024
v_lshl_add_u64 v[36:37],v[36:37],0,v[38:39]
v_lshlrev_b32_sdwa v26,1,v26 dst_sel:DWORD dst_unused:UNUSED_PAD src0_sel:DWORD src1_sel:WORD_1
v_mfma_f32_16x16x32_bf16 v[60:63],a[208:211],a[40:43],v[20:23]
ds_read_u16_d16_hi v26,v26
v_lshl_add_u64 v[38:39],v[40:41],0,v[42:43]
v_fma_mixhi_f16 v8,v59,s99,0
v_mfma_f32_16x16x32_bf16 v[60:63],a[204:207],a[24:27],v[60:63]
s_waitcnt lgkmcnt(8)
ds_read_b64_tr_b16 a[192:193],v1 offset:18480
v_lshl_add_u64 v[40:41],v[48:49],0,v[50:51]
v_lshlrev_b32_sdwa v25,1,v25 dst_sel:DWORD dst_unused:UNUSED_PAD src0_sel:DWORD src1_sel:WORD_1
v_pk_fma_f16 v9,v8,v8,s54
v_mfma_f32_16x16x32_bf16 v[60:63],a[200:203],a[8:11],v[60:63]
ds_read_u16 v25,v25
v_lshlrev_b32_sdwa v27,1,v27 dst_sel:DWORD dst_unused:UNUSED_PAD src0_sel:DWORD src1_sel:WORD_1
v_mfma_f32_16x16x32_bf16 v[48:51],a[200:203],a[12:15],v[20:23]
s_waitcnt lgkmcnt(8)
ds_read_u16_d16_hi v27,v27
v_or_b32 v42,v44,v46
v_pk_mul_f16 v9,v8,v9
v_mfma_f32_16x16x32_bf16 v[48:51],a[204:207],a[28:31],v[48:51]
ds_read_b64_tr_b16 a[194:195],v1 offset:19504
v_cvt_pk_bf16_f32 v43,v45,v47
v_lshlrev_b32_sdwa v28,1,v28 dst_sel:DWORD dst_unused:UNUSED_PAD src0_sel:DWORD src1_sel:WORD_1
v_mfma_f32_16x16x32_bf16 v[48:51],a[208:211],a[44:47],v[48:51]
v_exp_f16 v8,v9
v_mfma_f32_16x16x32_bf16 v[44:47],a[212:215],a[44:47],v[16:19]
ds_read_u16 v28,v28
v_lshlrev_b32_sdwa v30,1,v30 dst_sel:DWORD dst_unused:UNUSED_PAD src0_sel:DWORD src1_sel:WORD_1
v_mfma_f32_16x16x32_bf16 v[44:47],a[216:219],a[28:31],v[44:47]
ds_read_u16_d16_hi v30,v30
v_lshlrev_b32_sdwa v29,1,v29 dst_sel:DWORD dst_unused:UNUSED_PAD src0_sel:DWORD src1_sel:WORD_1
v_mfma_f32_16x16x32_bf16 v[44:47],a[220:223],a[12:15],v[44:47]
v_exp_f16_sdwa v8,v9 dst_sel:WORD_1 dst_unused:UNUSED_PRESERVE src0_sel:WORD_1
v_mfma_f32_16x16x32_bf16 v[208:211],a[252:255],v[36:39],v[208:211]
ds_read_u16 v29,v29
v_lshlrev_b32_sdwa v31,1,v31 dst_sel:DWORD dst_unused:UNUSED_PAD src0_sel:DWORD src1_sel:WORD_1
v_mfma_f32_16x16x32_bf16 v[212:215],a[252:255],v[40:43],v[212:215]
ds_read_u16_d16_hi v31,v31
v_lshlrev_b32_sdwa v52,1,v52 dst_sel:DWORD dst_unused:UNUSED_PAD src0_sel:DWORD src1_sel:WORD_1
v_mfma_f32_16x16x32_bf16 v[172:175],a[224:227],v[40:43],v[172:175]
ds_read_u16 v52,v52
v_pk_fma_f16 v8,v8,s55,s55
v_mfma_f32_16x16x32_bf16 v[168:171],a[224:227],v[36:39],v[168:171]
ds_read_b64_tr_b16 a[252:253],v3 offset:18544
v_rcp_f16 v9,v8
v_lshlrev_b32_sdwa v54,1,v54 dst_sel:DWORD dst_unused:UNUSED_PAD src0_sel:DWORD src1_sel:WORD_1
v_mfma_f32_16x16x32_bf16 v[216:219],a[232:235],v[36:39],v[216:219]
ds_read_u16_d16_hi v54,v54
v_lshlrev_b32_sdwa v53,1,v53 dst_sel:DWORD dst_unused:UNUSED_PAD src0_sel:DWORD src1_sel:WORD_1
v_mfma_f32_16x16x32_bf16 v[220:223],a[232:235],v[40:43],v[220:223]
ds_read_u16 v53,v53
ds_read_b64_tr_b16 a[254:255],v3 offset:20080
v_mfma_f32_16x16x32_bf16 v[188:191],a[244:247],v[40:43],v[188:191]
ds_read_b64_tr_b16 a[224:225],v3 offset:18552
v_lshlrev_b32_sdwa v55,1,v55 dst_sel:DWORD dst_unused:UNUSED_PAD src0_sel:DWORD src1_sel:WORD_1
v_mfma_f32_16x16x32_bf16 v[184:187],a[244:247],v[36:39],v[184:187]
ds_read_u16_d16_hi v55,v55
v_rcp_f16_sdwa v9,v8 dst_sel:WORD_1 dst_unused:UNUSED_PRESERVE src0_sel:WORD_1
v_mfma_f32_16x16x32_bf16 v[224:227],a[240:243],v[36:39],v[224:227]
ds_read_b64_tr_b16 a[226:227],v3 offset:20088
ds_read_b64_tr_b16 a[232:233],v3 offset:18608
v_lshlrev_b32_sdwa v56,1,v56 dst_sel:DWORD dst_unused:UNUSED_PAD src0_sel:DWORD src1_sel:WORD_1
v_mfma_f32_16x16x32_bf16 v[228:231],a[240:243],v[40:43],v[228:231]
ds_read_u16 v56,v56
v_lshlrev_b32_sdwa v58,1,v58 dst_sel:DWORD dst_unused:UNUSED_PAD src0_sel:DWORD src1_sel:WORD_1
v_mfma_f32_16x16x32_bf16 v[204:207],a[228:231],v[40:43],v[204:207]
ds_read_u16_d16_hi v58,v58
v_fma_mix_f32 v57,v57,v9,0 op_sel_hi:[0,1,0]
v_mfma_f32_16x16x32_bf16 v[200:203],a[228:231],v[36:39],v[200:203]
ds_read_b64_tr_b16 a[234:235],v3 offset:20144
v_fma_mix_f32 v59,v59,v9,0 op_sel:[0,1,0] op_sel_hi:[0,1,0]
v_mfma_f32_16x16x32_bf16 v[40:43],a[220:223],a[48:51],v[16:19]
ds_read_b64_tr_b16 a[244:245],v3 offset:18616
v_lshlrev_b32_sdwa v32,1,v32 dst_sel:DWORD dst_unused:UNUSED_PAD src0_sel:DWORD src1_sel:WORD_1
v_mfma_f32_16x16x32_bf16 v[40:43],a[216:219],a[64:67],v[40:43]
ds_read_b64_tr_b16 a[246:247],v3 offset:20152
v_fma_mixlo_f16 v8,v49,s99,0
v_mfma_f32_16x16x32_bf16 v[40:43],a[212:215],a[80:83],v[40:43]
s_waitcnt lgkmcnt(9)
ds_read_u16 v32,v32
v_lshl_add_u64 v[24:25],v[24:25],0,v[26:27]
v_lshlrev_b32_sdwa v34,1,v34 dst_sel:DWORD dst_unused:UNUSED_PAD src0_sel:DWORD src1_sel:WORD_1
v_mfma_f32_16x16x32_bf16 v[36:39],a[208:211],a[80:83],v[20:23]
ds_read_u16_d16_hi v34,v34
v_lshl_add_u64 v[26:27],v[28:29],0,v[30:31]
v_fma_mixhi_f16 v8,v51,s99,0
v_mfma_f32_16x16x32_bf16 v[36:39],a[204:207],a[64:67],v[36:39]
s_waitcnt lgkmcnt(9)
ds_read_b64_tr_b16 a[196:197],v1 offset:20528
v_lshl_add_u64 v[28:29],v[52:53],0,v[54:55]
v_lshlrev_b32_sdwa v33,1,v33 dst_sel:DWORD dst_unused:UNUSED_PAD src0_sel:DWORD src1_sel:WORD_1
v_pk_fma_f16 v9,v8,v8,s54
v_mfma_f32_16x16x32_bf16 v[36:39],a[200:203],a[48:51],v[36:39]
ds_read_u16 v33,v33
v_lshlrev_b32_sdwa v35,1,v35 dst_sel:DWORD dst_unused:UNUSED_PAD src0_sel:DWORD src1_sel:WORD_1
v_mfma_f32_16x16x32_bf16 v[52:55],a[200:203],a[52:55],v[20:23]
s_waitcnt lgkmcnt(7)
ds_read_u16_d16_hi v35,v35
v_or_b32 v30,v56,v58
v_pk_mul_f16 v9,v8,v9
v_mfma_f32_16x16x32_bf16 v[52:55],a[204:207],a[68:71],v[52:55]
ds_read_b64_tr_b16 a[198:199],v1 offset:21552
v_cvt_pk_bf16_f32 v31,v57,v59
v_lshlrev_b32_sdwa v60,1,v60 dst_sel:DWORD dst_unused:UNUSED_PAD src0_sel:DWORD src1_sel:WORD_1
v_mfma_f32_16x16x32_bf16 v[52:55],a[208:211],a[84:87],v[52:55]
v_exp_f16 v8,v9
v_mfma_f32_16x16x32_bf16 v[56:59],a[212:215],a[84:87],v[16:19]
ds_read_u16 v60,v60
v_lshlrev_b32_sdwa v62,1,v62 dst_sel:DWORD dst_unused:UNUSED_PAD src0_sel:DWORD src1_sel:WORD_1
v_mfma_f32_16x16x32_bf16 v[56:59],a[216:219],a[68:71],v[56:59]
ds_read_u16_d16_hi v62,v62
v_lshlrev_b32_sdwa v61,1,v61 dst_sel:DWORD dst_unused:UNUSED_PAD src0_sel:DWORD src1_sel:WORD_1
v_mfma_f32_16x16x32_bf16 v[56:59],a[220:223],a[52:55],v[56:59]
v_exp_f16_sdwa v8,v9 dst_sel:WORD_1 dst_unused:UNUSED_PRESERVE src0_sel:WORD_1
v_mfma_f32_16x16x32_bf16 v[136:139],a[248:251],v[24:27],v[136:139]
ds_read_u16 v61,v61
v_lshlrev_b32_sdwa v63,1,v63 dst_sel:DWORD dst_unused:UNUSED_PAD src0_sel:DWORD src1_sel:WORD_1
v_mfma_f32_16x16x32_bf16 v[140:143],a[248:251],v[28:31],v[140:143]
ds_read_u16_d16_hi v63,v63
v_lshlrev_b32_sdwa v44,1,v44 dst_sel:DWORD dst_unused:UNUSED_PAD src0_sel:DWORD src1_sel:WORD_1
v_mfma_f32_16x16x32_bf16 v[68:71],a[236:239],v[28:31],v[68:71]
ds_read_u16 v44,v44
v_pk_fma_f16 v8,v8,s55,s55
v_mfma_f32_16x16x32_bf16 v[64:67],a[236:239],v[24:27],v[64:67]
v_rcp_f16 v9,v8
v_lshlrev_b32_sdwa v46,1,v46 dst_sel:DWORD dst_unused:UNUSED_PAD src0_sel:DWORD src1_sel:WORD_1
v_mfma_f32_16x16x32_bf16 v[144:147],a[252:255],v[24:27],v[144:147]
ds_read_u16_d16_hi v46,v46
v_lshlrev_b32_sdwa v45,1,v45 dst_sel:DWORD dst_unused:UNUSED_PAD src0_sel:DWORD src1_sel:WORD_1
v_mfma_f32_16x16x32_bf16 v[148:151],a[252:255],v[28:31],v[148:151]
ds_read_u16 v45,v45
v_lshlrev_b32_sdwa v47,1,v47 dst_sel:DWORD dst_unused:UNUSED_PAD src0_sel:DWORD src1_sel:WORD_1
v_mfma_f32_16x16x32_bf16 v[84:87],a[224:227],v[28:31],v[84:87]
ds_read_u16_d16_hi v47,v47
v_lshlrev_b32_sdwa v48,1,v48 dst_sel:DWORD dst_unused:UNUSED_PAD src0_sel:DWORD src1_sel:WORD_1
v_mfma_f32_16x16x32_bf16 v[80:83],a[224:227],v[24:27],v[80:83]
s_waitcnt lgkmcnt(6)
v_rcp_f16_sdwa v9,v8 dst_sel:WORD_1 dst_unused:UNUSED_PRESERVE src0_sel:WORD_1
v_mfma_f32_16x16x32_bf16 v[152:155],a[232:235],v[24:27],v[152:155]
ds_read_u16 v48,v48
v_lshlrev_b32_sdwa v50,1,v50 dst_sel:DWORD dst_unused:UNUSED_PAD src0_sel:DWORD src1_sel:WORD_1
v_mfma_f32_16x16x32_bf16 v[156:159],a[232:235],v[28:31],v[156:159]
ds_read_u16_d16_hi v50,v50
v_fma_mix_f32 v49,v49,v9,0 op_sel_hi:[0,1,0]
v_mfma_f32_16x16x32_bf16 v[100:103],a[244:247],v[28:31],v[100:103]
v_fma_mix_f32 v51,v51,v9,0 op_sel:[0,1,0] op_sel_hi:[0,1,0]
v_mfma_f32_16x16x32_bf16 v[96:99],a[244:247],v[24:27],v[96:99]
v_mfma_f32_16x16x32_bf16 v[28:31],a[220:223],a[56:59],v[16:19]
v_mul_f32_dpp v136,v14,v136 row_newbcast:0 row_mask:0xf bank_mask:0xf
s_add_u32 s62,s61,3145728
v_lshlrev_b32_sdwa v40,1,v40 dst_sel:DWORD dst_unused:UNUSED_PAD src0_sel:DWORD src1_sel:WORD_1
v_mfma_f32_16x16x32_bf16 v[28:31],a[216:219],a[72:75],v[28:31]
ds_read_u16 v40,v40
v_mul_f32_dpp v137,v14,v137 row_newbcast:1 row_mask:0xf bank_mask:0xf
v_fma_mixlo_f16 v8,v53,s99,0
v_mul_f32_dpp v138,v14,v138 row_newbcast:2 row_mask:0xf bank_mask:0xf
v_mfma_f32_16x16x32_bf16 v[28:31],a[212:215],a[88:91],v[28:31]
v_lshl_add_u64 v[32:33],v[32:33],0,v[34:35]
v_mul_f32_dpp v139,v14,v139 row_newbcast:3 row_mask:0xf bank_mask:0xf
v_lshlrev_b32_sdwa v42,1,v42 dst_sel:DWORD dst_unused:UNUSED_PAD src0_sel:DWORD src1_sel:WORD_1
v_mfma_f32_16x16x32_bf16 v[24:27],a[208:211],a[88:91],v[20:23]
s_waitcnt lgkmcnt(3)
ds_read_u16_d16_hi v42,v42
v_lshl_add_u64 v[34:35],v[60:61],0,v[62:63]
v_mul_f32_dpp v64,v14,v64 row_newbcast:4 row_mask:0xf bank_mask:0xf
v_fma_mixhi_f16 v8,v55,s99,0
v_mul_f32_dpp v65,v14,v65 row_newbcast:5 row_mask:0xf bank_mask:0xf
v_mfma_f32_16x16x32_bf16 v[24:27],a[204:207],a[72:75],v[24:27]
v_lshl_add_u64 v[60:61],v[44:45],0,v[46:47]
v_mul_f32_dpp v66,v14,v66 row_newbcast:6 row_mask:0xf bank_mask:0xf
v_lshlrev_b32_sdwa v41,1,v41 dst_sel:DWORD dst_unused:UNUSED_PAD src0_sel:DWORD src1_sel:WORD_1
v_pk_fma_f16 v9,v8,v8,s54
v_mfma_f32_16x16x32_bf16 v[24:27],a[200:203],a[56:59],v[24:27]
ds_read_u16 v41,v41
v_mul_f32_dpp v67,v14,v67 row_newbcast:7 row_mask:0xf bank_mask:0xf
v_lshlrev_b32_sdwa v43,1,v43 dst_sel:DWORD dst_unused:UNUSED_PAD src0_sel:DWORD src1_sel:WORD_1
v_cvt_pk_bf16_f32 v136,v136,v137
v_mfma_f32_16x16x32_bf16 v[44:47],a[200:203],a[60:63],v[20:23]
s_waitcnt lgkmcnt(3)
ds_read_b128 v[20:23],v4 offset:32
v_or_b32 v62,v48,v50
v_cvt_pk_bf16_f32 v137,v138,v139
v_pk_mul_f16 v9,v8,v9
v_mfma_f32_16x16x32_bf16 v[44:47],a[204:207],a[76:79],v[44:47]
ds_read_u16_d16_hi v43,v43
v_cvt_pk_bf16_f32 v63,v49,v51
v_cvt_pk_bf16_f32 v138,v64,v65
v_lshlrev_b32_sdwa v36,1,v36 dst_sel:DWORD dst_unused:UNUSED_PAD src0_sel:DWORD src1_sel:WORD_1
v_cvt_pk_bf16_f32 v139,v66,v67
v_mfma_f32_16x16x32_bf16 v[44:47],a[208:211],a[92:95],v[44:47]
ds_read_b64_tr_b16 a[200:201],v1 offset:22576
v_exp_f16 v8,v9
v_mul_f32_dpp v144,v14,v144 row_newbcast:8 row_mask:0xf bank_mask:0xf
v_mfma_f32_16x16x32_bf16 v[48:51],a[212:215],a[92:95],v[16:19]
buffer_store_dwordx4 v[136:139],v7,s[40:43],s62 offen offset:0 sc1
ds_read_b128 v[16:19],v4 offset:0
v_mul_f32_dpp v145,v14,v145 row_newbcast:9 row_mask:0xf bank_mask:0xf
v_lshlrev_b32_sdwa v38,1,v38 dst_sel:DWORD dst_unused:UNUSED_PAD src0_sel:DWORD src1_sel:WORD_1
v_mul_f32_dpp v146,v14,v146 row_newbcast:10 row_mask:0xf bank_mask:0xf
v_mfma_f32_16x16x32_bf16 v[48:51],a[216:219],a[76:79],v[48:51]
ds_read_b64_tr_b16 a[202:203],v1 offset:23600
ds_read_b64_tr_b16 a[204:205],v2 offset:22576
v_mul_f32_dpp v147,v14,v147 row_newbcast:11 row_mask:0xf bank_mask:0xf
v_lshlrev_b32_sdwa v37,1,v37 dst_sel:DWORD dst_unused:UNUSED_PAD src0_sel:DWORD src1_sel:WORD_1
v_mfma_f32_16x16x32_bf16 v[48:51],a[220:223],a[60:63],v[48:51]
ds_read_b64_tr_b16 a[206:207],v2 offset:23600
v_exp_f16_sdwa v8,v9 dst_sel:WORD_1 dst_unused:UNUSED_PRESERVE src0_sel:WORD_1
v_mul_f32_dpp v80,v14,v80 row_newbcast:12 row_mask:0xf bank_mask:0xf
v_mul_f32_dpp v81,v14,v81 row_newbcast:13 row_mask:0xf bank_mask:0xf
v_mfma_f32_16x16x32_bf16 v[112:115],a[248:251],v[32:35],v[112:115]
ds_read_u16 v36,v36
ds_read_u16_d16_hi v38,v38
v_mul_f32_dpp v82,v14,v82 row_newbcast:14 row_mask:0xf bank_mask:0xf
v_lshlrev_b32_sdwa v39,1,v39 dst_sel:DWORD dst_unused:UNUSED_PAD src0_sel:DWORD src1_sel:WORD_1
v_mfma_f32_16x16x32_bf16 v[116:119],a[248:251],v[60:63],v[116:119]
ds_read_u16 v37,v37
v_mul_f32_dpp v83,v14,v83 row_newbcast:15 row_mask:0xf bank_mask:0xf
v_lshlrev_b32_sdwa v56,1,v56 dst_sel:DWORD dst_unused:UNUSED_PAD src0_sel:DWORD src1_sel:WORD_1
v_cvt_pk_bf16_f32 v144,v144,v145
v_mfma_f32_16x16x32_bf16 v[76:79],a[236:239],v[60:63],v[76:79]
ds_read_u16_d16_hi v39,v39
v_cvt_pk_bf16_f32 v145,v146,v147
v_pk_fma_f16 v8,v8,s55,s55
v_mfma_f32_16x16x32_bf16 v[72:75],a[236:239],v[32:35],v[72:75]
TP10316_%=:
s_cbranch_execz TR1_%=
v_rcp_f16 v9,v8
v_cvt_pk_bf16_f32 v146,v80,v81
v_lshlrev_b32_sdwa v58,1,v58 dst_sel:DWORD dst_unused:UNUSED_PAD src0_sel:DWORD src1_sel:WORD_1
v_cvt_pk_bf16_f32 v147,v82,v83
v_mfma_f32_16x16x32_bf16 v[120:123],a[252:255],v[32:35],v[120:123]
ds_read_b64_tr_b16 a[208:209],v2 offset:20528
v_mul_f32_dpp v152,v15,v152 row_newbcast:0 row_mask:0xf bank_mask:0xf
v_mfma_f32_16x16x32_bf16 v[124:127],a[252:255],v[60:63],v[124:127]
ds_read_b64_tr_b16 a[210:211],v2 offset:21552
v_mul_f32_dpp v153,v15,v153 row_newbcast:1 row_mask:0xf bank_mask:0xf
v_lshlrev_b32_sdwa v57,1,v57 dst_sel:DWORD dst_unused:UNUSED_PAD src0_sel:DWORD src1_sel:WORD_1
v_mul_f32_dpp v154,v15,v154 row_newbcast:2 row_mask:0xf bank_mask:0xf
v_mfma_f32_16x16x32_bf16 v[92:95],a[224:227],v[60:63],v[92:95]
ds_read_u16 v56,v56
v_mul_f32_dpp v155,v15,v155 row_newbcast:3 row_mask:0xf bank_mask:0xf
v_lshlrev_b32_sdwa v59,1,v59 dst_sel:DWORD dst_unused:UNUSED_PAD src0_sel:DWORD src1_sel:WORD_1
v_mfma_f32_16x16x32_bf16 v[88:91],a[224:227],v[32:35],v[88:91]
v_rcp_f16_sdwa v9,v8 dst_sel:WORD_1 dst_unused:UNUSED_PRESERVE src0_sel:WORD_1
v_mul_f32_dpp v96,v15,v96 row_newbcast:4 row_mask:0xf bank_mask:0xf
v_mul_f32_dpp v97,v15,v97 row_newbcast:5 row_mask:0xf bank_mask:0xf
v_mfma_f32_16x16x32_bf16 v[128:131],a[232:235],v[32:35],v[128:131]
ds_read_u16_d16_hi v58,v58
ds_read_u16 v57,v57
v_mul_f32_dpp v98,v15,v98 row_newbcast:6 row_mask:0xf bank_mask:0xf
v_lshlrev_b32_sdwa v52,1,v52 dst_sel:DWORD dst_unused:UNUSED_PAD src0_sel:DWORD src1_sel:WORD_1
v_mfma_f32_16x16x32_bf16 v[132:135],a[232:235],v[60:63],v[132:135]
ds_read_u16_d16_hi v59,v59
ds_read_b64_tr_b16 a[212:213],v2 offset:18480
v_mul_f32_dpp v99,v15,v99 row_newbcast:7 row_mask:0xf bank_mask:0xf
v_cvt_pk_bf16_f32 v152,v152,v153
v_lshlrev_b32_sdwa v54,1,v54 dst_sel:DWORD dst_unused:UNUSED_PAD src0_sel:DWORD src1_sel:WORD_1
v_mfma_f32_16x16x32_bf16 v[108:111],a[244:247],v[60:63],v[108:111]
buffer_store_dwordx4 v[144:147],v7,s[40:43],s62 offen offset:64 sc1
ds_read_b64_tr_b16 a[214:215],v2 offset:19504
v_cvt_pk_bf16_f32 v153,v154,v155
v_fma_mix_f32 v53,v53,v9,0 op_sel_hi:[0,1,0]
v_mfma_f32_16x16x32_bf16 v[104:107],a[244:247],v[32:35],v[104:107]
s_waitcnt lgkmcnt(8)
ds_read_u16 v52,v52
ds_read_u16_d16_hi v54,v54
v_cvt_pk_bf16_f32 v154,v96,v97
v_cvt_pk_bf16_f32 v155,v98,v99
v_fma_mix_f32 v55,v55,v9,0 op_sel:[0,1,0] op_sel_hi:[0,1,0]
v_mfma_f32_16x16x32_bf16 v[60:63],a[192:195],a[48:51],v[16:19]
v_mul_f32_dpp v140,v14,v140 row_newbcast:0 row_mask:0xf bank_mask:0xf
v_lshlrev_b32_sdwa v28,1,v28 dst_sel:DWORD dst_unused:UNUSED_PAD src0_sel:DWORD src1_sel:WORD_1
v_mfma_f32_16x16x32_bf16 v[60:63],a[196:199],a[64:67],v[60:63]
ds_read_u16 v28,v28
v_mul_f32_dpp v141,v14,v141 row_newbcast:1 row_mask:0xf bank_mask:0xf
v_fma_mixlo_f16 v8,v45,s99,0
v_mul_f32_dpp v142,v14,v142 row_newbcast:2 row_mask:0xf bank_mask:0xf
v_mfma_f32_16x16x32_bf16 v[60:63],a[200:203],a[80:83],v[60:63]
v_lshl_add_u64 v[40:41],v[40:41],0,v[42:43]
v_mul_f32_dpp v143,v14,v143 row_newbcast:3 row_mask:0xf bank_mask:0xf
v_lshlrev_b32_sdwa v30,1,v30 dst_sel:DWORD dst_unused:UNUSED_PAD src0_sel:DWORD src1_sel:WORD_1
v_mfma_f32_16x16x32_bf16 v[32:35],a[204:207],a[80:83],v[20:23]
s_waitcnt lgkmcnt(8)
ds_read_u16_d16_hi v30,v30
v_lshl_add_u64 v[42:43],v[36:37],0,v[38:39]
v_mul_f32_dpp v68,v14,v68 row_newbcast:4 row_mask:0xf bank_mask:0xf
v_fma_mixhi_f16 v8,v47,s99,0
v_mul_f32_dpp v69,v14,v69 row_newbcast:5 row_mask:0xf bank_mask:0xf
v_mfma_f32_16x16x32_bf16 v[32:35],a[208:211],a[64:67],v[32:35]
s_waitcnt lgkmcnt(4)
v_lshl_add_u64 v[36:37],v[56:57],0,v[58:59]
v_mul_f32_dpp v70,v14,v70 row_newbcast:6 row_mask:0xf bank_mask:0xf
v_lshlrev_b32_sdwa v29,1,v29 dst_sel:DWORD dst_unused:UNUSED_PAD src0_sel:DWORD src1_sel:WORD_1
v_pk_fma_f16 v9,v8,v8,s54
v_mfma_f32_16x16x32_bf16 v[32:35],a[212:215],a[48:51],v[32:35]
ds_read_u16 v29,v29
v_mul_f32_dpp v71,v14,v71 row_newbcast:7 row_mask:0xf bank_mask:0xf
v_lshlrev_b32_sdwa v31,1,v31 dst_sel:DWORD dst_unused:UNUSED_PAD src0_sel:DWORD src1_sel:WORD_1
v_cvt_pk_bf16_f32 v140,v140,v141
v_mfma_f32_16x16x32_bf16 v[56:59],a[212:215],a[52:55],v[20:23]
s_waitcnt lgkmcnt(3)
ds_read_u16_d16_hi v31,v31
v_or_b32 v38,v52,v54
v_cvt_pk_bf16_f32 v141,v142,v143
v_pk_mul_f16 v9,v8,v9
v_mfma_f32_16x16x32_bf16 v[56:59],a[208:211],a[68:71],v[56:59]
s_waitcnt vmcnt(8)
v_cvt_pk_bf16_f32 v39,v53,v55
v_cvt_pk_bf16_f32 v142,v68,v69
v_lshlrev_b32_sdwa v24,1,v24 dst_sel:DWORD dst_unused:UNUSED_PAD src0_sel:DWORD src1_sel:WORD_1
v_cvt_pk_bf16_f32 v143,v70,v71
v_mfma_f32_16x16x32_bf16 v[56:59],a[204:207],a[84:87],v[56:59]
s_barrier
v_exp_f16 v8,v9
v_mul_f32_dpp v148,v14,v148 row_newbcast:8 row_mask:0xf bank_mask:0xf
v_mfma_f32_16x16x32_bf16 v[52:55],a[200:203],a[84:87],v[16:19]
ds_read_u16 v24,v24
v_mul_f32_dpp v149,v14,v149 row_newbcast:9 row_mask:0xf bank_mask:0xf
v_lshlrev_b32_sdwa v26,1,v26 dst_sel:DWORD dst_unused:UNUSED_PAD src0_sel:DWORD src1_sel:WORD_1
v_mul_f32_dpp v150,v14,v150 row_newbcast:10 row_mask:0xf bank_mask:0xf
v_mfma_f32_16x16x32_bf16 v[52:55],a[196:199],a[68:71],v[52:55]
s_add_u32 m0,s58,18480
ds_read_u16_d16_hi v26,v26
v_mul_f32_dpp v151,v14,v151 row_newbcast:11 row_mask:0xf bank_mask:0xf
v_lshlrev_b32_sdwa v25,1,v25 dst_sel:DWORD dst_unused:UNUSED_PAD src0_sel:DWORD src1_sel:WORD_1
v_mfma_f32_16x16x32_bf16 v[52:55],a[192:195],a[52:55],v[52:55]
s_add_u32 s69,s69,s59
v_exp_f16_sdwa v8,v9 dst_sel:WORD_1 dst_unused:UNUSED_PRESERVE src0_sel:WORD_1
v_mul_f32_dpp v84,v14,v84 row_newbcast:12 row_mask:0xf bank_mask:0xf
v_mul_f32_dpp v85,v14,v85 row_newbcast:13 row_mask:0xf bank_mask:0xf
v_mfma_f32_16x16x32_bf16 v[232:235],a[248:251],v[40:43],v[232:235]
buffer_load_dwordx4 v5,s[44:47],s69 offen offset:0 lds
v_mul_f32_dpp v86,v14,v86 row_newbcast:14 row_mask:0xf bank_mask:0xf
v_lshlrev_b32_sdwa v27,1,v27 dst_sel:DWORD dst_unused:UNUSED_PAD src0_sel:DWORD src1_sel:WORD_1
v_mfma_f32_16x16x32_bf16 v[236:239],a[248:251],v[36:39],v[236:239]
buffer_store_dwordx4 v[152:155],v7,s[40:43],s62 offen offset:128 sc1
ds_read_u16 v25,v25
v_mul_f32_dpp v87,v14,v87 row_newbcast:15 row_mask:0xf bank_mask:0xf
v_lshlrev_b32_sdwa v48,1,v48 dst_sel:DWORD dst_unused:UNUSED_PAD src0_sel:DWORD src1_sel:WORD_1
v_cvt_pk_bf16_f32 v148,v148,v149
v_mfma_f32_16x16x32_bf16 v[164:167],a[236:239],v[36:39],v[164:167]
s_add_u32 s69,s69,s53
ds_read_u16_d16_hi v27,v27
v_cvt_pk_bf16_f32 v149,v150,v151
v_pk_fma_f16 v8,v8,s55,s55
v_mfma_f32_16x16x32_bf16 v[160:163],a[236:239],v[40:43],v[160:163]
v_rcp_f16 v9,v8
v_cvt_pk_bf16_f32 v150,v84,v85
v_lshlrev_b32_sdwa v50,1,v50 dst_sel:DWORD dst_unused:UNUSED_PAD src0_sel:DWORD src1_sel:WORD_1
v_cvt_pk_bf16_f32 v151,v86,v87
v_mfma_f32_16x16x32_bf16 v[240:243],a[252:255],v[40:43],v[240:243]
buffer_load_dwordx4 v5,s[44:47],s69 offen offset:1024 lds
ds_read_u16 v48,v48
v_mul_f32_dpp v156,v15,v156 row_newbcast:0 row_mask:0xf bank_mask:0xf
v_mfma_f32_16x16x32_bf16 v[244:247],a[252:255],v[36:39],v[244:247]
s_add_u32 s69,s69,s53
ds_read_u16_d16_hi v50,v50
v_mul_f32_dpp v157,v15,v157 row_newbcast:1 row_mask:0xf bank_mask:0xf
v_lshlrev_b32_sdwa v49,1,v49 dst_sel:DWORD dst_unused:UNUSED_PAD src0_sel:DWORD src1_sel:WORD_1
v_mul_f32_dpp v158,v15,v158 row_newbcast:2 row_mask:0xf bank_mask:0xf
v_mfma_f32_16x16x32_bf16 v[180:183],a[224:227],v[36:39],v[180:183]
buffer_load_dwordx4 v5,s[44:47],s69 offen offset:2048 lds
v_mul_f32_dpp v159,v15,v159 row_newbcast:3 row_mask:0xf bank_mask:0xf
v_lshlrev_b32_sdwa v51,1,v51 dst_sel:DWORD dst_unused:UNUSED_PAD src0_sel:DWORD src1_sel:WORD_1
v_mfma_f32_16x16x32_bf16 v[176:179],a[224:227],v[40:43],v[176:179]
ds_read_u16 v49,v49
v_rcp_f16_sdwa v9,v8 dst_sel:WORD_1 dst_unused:UNUSED_PRESERVE src0_sel:WORD_1
v_mul_f32_dpp v100,v15,v100 row_newbcast:4 row_mask:0xf bank_mask:0xf
v_mul_f32_dpp v101,v15,v101 row_newbcast:5 row_mask:0xf bank_mask:0xf
v_mfma_f32_16x16x32_bf16 v[248:251],a[232:235],v[40:43],v[248:251]
ds_read_u16_d16_hi v51,v51
v_mul_f32_dpp v102,v15,v102 row_newbcast:6 row_mask:0xf bank_mask:0xf
v_lshlrev_b32_sdwa v44,1,v44 dst_sel:DWORD dst_unused:UNUSED_PAD src0_sel:DWORD src1_sel:WORD_1
v_mfma_f32_16x16x32_bf16 v[252:255],a[232:235],v[36:39],v[252:255]
ds_read_u16 v44,v44
v_mul_f32_dpp v103,v15,v103 row_newbcast:7 row_mask:0xf bank_mask:0xf
v_cvt_pk_bf16_f32 v156,v156,v157
v_lshlrev_b32_sdwa v46,1,v46 dst_sel:DWORD dst_unused:UNUSED_PAD src0_sel:DWORD src1_sel:WORD_1
v_mfma_f32_16x16x32_bf16 v[196:199],a[244:247],v[36:39],v[196:199]
ds_read_u16_d16_hi v46,v46
v_cvt_pk_bf16_f32 v157,v158,v159
v_fma_mix_f32 v45,v45,v9,0 op_sel_hi:[0,1,0]
v_mfma_f32_16x16x32_bf16 v[192:195],a[244:247],v[40:43],v[192:195]
ds_read_b64_tr_b16 a[240:241],v3 offset:0
v_cvt_pk_bf16_f32 v158,v100,v101
v_cvt_pk_bf16_f32 v159,v102,v103
v_fma_mix_f32 v47,v47,v9,0 op_sel:[0,1,0] op_sel_hi:[0,1,0]
v_mfma_f32_16x16x32_bf16 v[36:39],a[192:195],a[56:59],v[16:19]
ds_read_b64_tr_b16 a[216:217],v1 offset:0
ds_read_b64_tr_b16 a[218:219],v1 offset:1024
v_mul_f32_dpp v112,v14,v112 row_newbcast:0 row_mask:0xf bank_mask:0xf
s_add_u32 s63,s61,3244032
v_lshlrev_b32_sdwa v60,1,v60 dst_sel:DWORD dst_unused:UNUSED_PAD src0_sel:DWORD src1_sel:WORD_1
v_mfma_f32_16x16x32_bf16 v[36:39],a[196:199],a[72:75],v[36:39]
ds_read_b64_tr_b16 a[220:221],v1 offset:2048
v_mul_f32_dpp v113,v14,v113 row_newbcast:1 row_mask:0xf bank_mask:0xf
v_fma_mixlo_f16 v8,v57,s99,0
v_mul_f32_dpp v114,v14,v114 row_newbcast:2 row_mask:0xf bank_mask:0xf
v_mfma_f32_16x16x32_bf16 v[36:39],a[200:203],a[88:91],v[36:39]
s_waitcnt lgkmcnt(8)
buffer_store_dwordx4 v[140:143],v7,s[40:43],s62 offen offset:192 sc1
v_lshl_add_u64 v[28:29],v[28:29],0,v[30:31]
v_mul_f32_dpp v115,v14,v115 row_newbcast:3 row_mask:0xf bank_mask:0xf
v_lshlrev_b32_sdwa v62,1,v62 dst_sel:DWORD dst_unused:UNUSED_PAD src0_sel:DWORD src1_sel:WORD_1
v_mfma_f32_16x16x32_bf16 v[40:43],a[204:207],a[88:91],v[20:23]
ds_read_b64_tr_b16 a[222:223],v1 offset:3072
v_lshl_add_u64 v[30:31],v[24:25],0,v[26:27]
v_mul_f32_dpp v72,v14,v72 row_newbcast:4 row_mask:0xf bank_mask:0xf
v_fma_mixhi_f16 v8,v59,s99,0
v_mul_f32_dpp v73,v14,v73 row_newbcast:5 row_mask:0xf bank_mask:0xf
v_mfma_f32_16x16x32_bf16 v[40:43],a[208:211],a[72:75],v[40:43]
s_waitcnt lgkmcnt(7)
ds_read_u16 v60,v60
v_lshl_add_u64 v[24:25],v[48:49],0,v[50:51]
v_mul_f32_dpp v74,v14,v74 row_newbcast:6 row_mask:0xf bank_mask:0xf
v_lshlrev_b32_sdwa v61,1,v61 dst_sel:DWORD dst_unused:UNUSED_PAD src0_sel:DWORD src1_sel:WORD_1
v_pk_fma_f16 v9,v8,v8,s54
v_mfma_f32_16x16x32_bf16 v[40:43],a[212:215],a[56:59],v[40:43]
ds_read_u16_d16_hi v62,v62
v_mul_f32_dpp v75,v14,v75 row_newbcast:7 row_mask:0xf bank_mask:0xf
v_lshlrev_b32_sdwa v63,1,v63 dst_sel:DWORD dst_unused:UNUSED_PAD src0_sel:DWORD src1_sel:WORD_1
v_cvt_pk_bf16_f32 v112,v112,v113
v_mfma_f32_16x16x32_bf16 v[48:51],a[212:215],a[60:63],v[20:23]
s_waitcnt lgkmcnt(7)
ds_read_b128 v[20:23],v4 offset:160
ds_read_u16 v61,v61
v_or_b32 v26,v44,v46
v_cvt_pk_bf16_f32 v113,v114,v115
v_pk_mul_f16 v9,v8,v9
v_mfma_f32_16x16x32_bf16 v[48:51],a[208:211],a[76:79],v[48:51]
ds_read_u16_d16_hi v63,v63
ds_read_b64_tr_b16 a[212:213],v1 offset:4096
v_cvt_pk_bf16_f32 v27,v45,v47
v_cvt_pk_bf16_f32 v114,v72,v73
v_lshlrev_b32_sdwa v32,1,v32 dst_sel:DWORD dst_unused:UNUSED_PAD src0_sel:DWORD src1_sel:WORD_1
v_cvt_pk_bf16_f32 v115,v74,v75
v_mfma_f32_16x16x32_bf16 v[48:51],a[204:207],a[92:95],v[48:51]
ds_read_b64_tr_b16 a[214:215],v1 offset:5120
v_exp_f16 v8,v9
v_mul_f32_dpp v120,v14,v120 row_newbcast:8 row_mask:0xf bank_mask:0xf
v_mfma_f32_16x16x32_bf16 v[44:47],a[200:203],a[92:95],v[16:19]
ds_read_b128 v[16:19],v4 offset:128
ds_read_b64_tr_b16 a[208:209],v2 offset:4096
v_mul_f32_dpp v121,v14,v121 row_newbcast:9 row_mask:0xf bank_mask:0xf
v_lshlrev_b32_sdwa v34,1,v34 dst_sel:DWORD dst_unused:UNUSED_PAD src0_sel:DWORD src1_sel:WORD_1
v_mul_f32_dpp v122,v14,v122 row_newbcast:10 row_mask:0xf bank_mask:0xf
v_mfma_f32_16x16x32_bf16 v[44:47],a[196:199],a[76:79],v[44:47]
ds_read_b64_tr_b16 a[210:211],v2 offset:5120
ds_read_u16 v32,v32
v_mul_f32_dpp v123,v14,v123 row_newbcast:11 row_mask:0xf bank_mask:0xf
v_lshlrev_b32_sdwa v33,1,v33 dst_sel:DWORD dst_unused:UNUSED_PAD src0_sel:DWORD src1_sel:WORD_1
v_mfma_f32_16x16x32_bf16 v[44:47],a[192:195],a[60:63],v[44:47]
ds_read_u16_d16_hi v34,v34
v_exp_f16_sdwa v8,v9 dst_sel:WORD_1 dst_unused:UNUSED_PRESERVE src0_sel:WORD_1
v_mul_f32_dpp v88,v14,v88 row_newbcast:12 row_mask:0xf bank_mask:0xf
v_mul_f32_dpp v89,v14,v89 row_newbcast:13 row_mask:0xf bank_mask:0xf
v_mfma_f32_16x16x32_bf16 v[208:211],a[248:251],v[28:31],v[208:211]
ds_read_u16 v33,v33
ds_read_b64_tr_b16 a[204:205],v2 offset:2048
v_mul_f32_dpp v90,v14,v90 row_newbcast:14 row_mask:0xf bank_mask:0xf
v_lshlrev_b32_sdwa v35,1,v35 dst_sel:DWORD dst_unused:UNUSED_PAD src0_sel:DWORD src1_sel:WORD_1
v_mfma_f32_16x16x32_bf16 v[212:215],a[248:251],v[24:27],v[212:215]
ds_read_u16_d16_hi v35,v35
ds_read_b64_tr_b16 a[206:207],v2 offset:3072
v_mul_f32_dpp v91,v14,v91 row_newbcast:15 row_mask:0xf bank_mask:0xf
v_lshlrev_b32_sdwa v52,1,v52 dst_sel:DWORD dst_unused:UNUSED_PAD src0_sel:DWORD src1_sel:WORD_1
v_cvt_pk_bf16_f32 v120,v120,v121
v_mfma_f32_16x16x32_bf16 v[172:175],a[236:239],v[24:27],v[172:175]
buffer_store_dwordx4 v[148:151],v7,s[40:43],s62 offen offset:256 sc1
ds_read_u16 v52,v52
v_cvt_pk_bf16_f32 v121,v122,v123
v_pk_fma_f16 v8,v8,s55,s55
v_mfma_f32_16x16x32_bf16 v[168:171],a[236:239],v[28:31],v[168:171]
ds_read_b64_tr_b16 a[200:201],v2 offset:0
v_rcp_f16 v9,v8
v_cvt_pk_bf16_f32 v122,v88,v89
v_lshlrev_b32_sdwa v54,1,v54 dst_sel:DWORD dst_unused:UNUSED_PAD src0_sel:DWORD src1_sel:WORD_1
v_cvt_pk_bf16_f32 v123,v90,v91
v_mfma_f32_16x16x32_bf16 v[216:219],a[252:255],v[28:31],v[216:219]
ds_read_u16_d16_hi v54,v54
ds_read_b64_tr_b16 a[202:203],v2 offset:1024
v_mul_f32_dpp v128,v15,v128 row_newbcast:0 row_mask:0xf bank_mask:0xf
v_mfma_f32_16x16x32_bf16 v[220:223],a[252:255],v[24:27],v[220:223]
ds_read_b64_tr_b16 a[242:243],v3 offset:1536
ds_read_b64_tr_b16 a[228:229],v3 offset:8
v_mul_f32_dpp v129,v15,v129 row_newbcast:1 row_mask:0xf bank_mask:0xf
v_lshlrev_b32_sdwa v53,1,v53 dst_sel:DWORD dst_unused:UNUSED_PAD src0_sel:DWORD src1_sel:WORD_1
v_mul_f32_dpp v130,v15,v130 row_newbcast:2 row_mask:0xf bank_mask:0xf
v_mfma_f32_16x16x32_bf16 v[188:191],a[224:227],v[24:27],v[188:191]
ds_read_u16 v53,v53
ds_read_b64_tr_b16 a[230:231],v3 offset:1544
v_mul_f32_dpp v131,v15,v131 row_newbcast:3 row_mask:0xf bank_mask:0xf
v_lshlrev_b32_sdwa v55,1,v55 dst_sel:DWORD dst_unused:UNUSED_PAD src0_sel:DWORD src1_sel:WORD_1
v_mfma_f32_16x16x32_bf16 v[184:187],a[224:227],v[28:31],v[184:187]
ds_read_u16_d16_hi v55,v55
v_rcp_f16_sdwa v9,v8 dst_sel:WORD_1 dst_unused:UNUSED_PRESERVE src0_sel:WORD_1
v_mul_f32_dpp v104,v15,v104 row_newbcast:4 row_mask:0xf bank_mask:0xf
v_mul_f32_dpp v105,v15,v105 row_newbcast:5 row_mask:0xf bank_mask:0xf
v_mfma_f32_16x16x32_bf16 v[224:227],a[232:235],v[28:31],v[224:227]
ds_read_b64_tr_b16 a[248:249],v3 offset:64
ds_read_b64_tr_b16 a[250:251],v3 offset:1600
v_mul_f32_dpp v106,v15,v106 row_newbcast:6 row_mask:0xf bank_mask:0xf
v_lshlrev_b32_sdwa v56,1,v56 dst_sel:DWORD dst_unused:UNUSED_PAD src0_sel:DWORD src1_sel:WORD_1
v_mfma_f32_16x16x32_bf16 v[228:231],a[232:235],v[24:27],v[228:231]
ds_read_u16 v56,v56
ds_read_b64_tr_b16 a[236:237],v3 offset:72
v_mul_f32_dpp v107,v15,v107 row_newbcast:7 row_mask:0xf bank_mask:0xf
v_cvt_pk_bf16_f32 v128,v128,v129
v_lshlrev_b32_sdwa v58,1,v58 dst_sel:DWORD dst_unused:UNUSED_PAD src0_sel:DWORD src1_sel:WORD_1
v_mfma_f32_16x16x32_bf16 v[204:207],a[244:247],v[24:27],v[204:207]
ds_read_u16_d16_hi v58,v58
v_cvt_pk_bf16_f32 v129,v130,v131
v_fma_mix_f32 v57,v57,v9,0 op_sel_hi:[0,1,0]
v_mfma_f32_16x16x32_bf16 v[200:203],a[244:247],v[28:31],v[200:203]
s_waitcnt lgkmcnt(13)
ds_read_b64_tr_b16 a[238:239],v3 offset:1608
v_cvt_pk_bf16_f32 v130,v104,v105
v_cvt_pk_bf16_f32 v131,v106,v107
v_fma_mix_f32 v59,v59,v9,0 op_sel:[0,1,0] op_sel_hi:[0,1,0]
v_mfma_f32_16x16x32_bf16 v[24:27],a[216:219],a[48:51],v[16:19]
ds_read_b64_tr_b16 a[252:253],v3 offset:128
v_mul_f32_dpp v116,v14,v116 row_newbcast:0 row_mask:0xf bank_mask:0xf
v_lshlrev_b32_sdwa v36,1,v36 dst_sel:DWORD dst_unused:UNUSED_PAD src0_sel:DWORD src1_sel:WORD_1
v_mfma_f32_16x16x32_bf16 v[24:27],a[220:223],a[64:67],v[24:27]
ds_read_b64_tr_b16 a[254:255],v3 offset:1664
v_mul_f32_dpp v117,v14,v117 row_newbcast:1 row_mask:0xf bank_mask:0xf
v_fma_mixlo_f16 v8,v49,s99,0
v_mul_f32_dpp v118,v14,v118 row_newbcast:2 row_mask:0xf bank_mask:0xf
v_mfma_f32_16x16x32_bf16 v[24:27],a[212:215],a[80:83],v[24:27]
ds_read_b64_tr_b16 a[224:225],v3 offset:136
v_lshl_add_u64 v[60:61],v[60:61],0,v[62:63]
v_mul_f32_dpp v119,v14,v119 row_newbcast:3 row_mask:0xf bank_mask:0xf
v_lshlrev_b32_sdwa v38,1,v38 dst_sel:DWORD dst_unused:UNUSED_PAD src0_sel:DWORD src1_sel:WORD_1
v_mfma_f32_16x16x32_bf16 v[28:31],a[208:211],a[80:83],v[20:23]
buffer_store_dwordx4 v[156:159],v7,s[40:43],s62 offen offset:320 sc1
v_lshl_add_u64 v[62:63],v[32:33],0,v[34:35]
v_mul_f32_dpp v76,v14,v76 row_newbcast:4 row_mask:0xf bank_mask:0xf
v_fma_mixhi_f16 v8,v51,s99,0
v_mul_f32_dpp v77,v14,v77 row_newbcast:5 row_mask:0xf bank_mask:0xf
v_mfma_f32_16x16x32_bf16 v[28:31],a[204:207],a[64:67],v[28:31]
s_waitcnt lgkmcnt(9)
ds_read_b64_tr_b16 a[226:227],v3 offset:1672
v_lshl_add_u64 v[32:33],v[52:53],0,v[54:55]
v_mul_f32_dpp v78,v14,v78 row_newbcast:6 row_mask:0xf bank_mask:0xf
v_lshlrev_b32_sdwa v37,1,v37 dst_sel:DWORD dst_unused:UNUSED_PAD src0_sel:DWORD src1_sel:WORD_1
v_pk_fma_f16 v9,v8,v8,s54
v_mfma_f32_16x16x32_bf16 v[28:31],a[200:203],a[48:51],v[28:31]
ds_read_u16 v36,v36
v_mul_f32_dpp v79,v14,v79 row_newbcast:7 row_mask:0xf bank_mask:0xf
v_lshlrev_b32_sdwa v39,1,v39 dst_sel:DWORD dst_unused:UNUSED_PAD src0_sel:DWORD src1_sel:WORD_1
v_cvt_pk_bf16_f32 v116,v116,v117
v_mfma_f32_16x16x32_bf16 v[52:55],a[200:203],a[52:55],v[20:23]
s_waitcnt lgkmcnt(6)
ds_read_u16_d16_hi v38,v38
v_or_b32 v34,v56,v58
v_cvt_pk_bf16_f32 v117,v118,v119
v_pk_mul_f16 v9,v8,v9
v_mfma_f32_16x16x32_bf16 v[52:55],a[204:207],a[68:71],v[52:55]
s_waitcnt vmcnt(12)
ds_read_u16 v37,v37
v_cvt_pk_bf16_f32 v35,v57,v59
v_cvt_pk_bf16_f32 v118,v76,v77
v_lshlrev_b32_sdwa v40,1,v40 dst_sel:DWORD dst_unused:UNUSED_PAD src0_sel:DWORD src1_sel:WORD_1
v_cvt_pk_bf16_f32 v119,v78,v79
v_mfma_f32_16x16x32_bf16 v[52:55],a[208:211],a[84:87],v[52:55]
s_barrier
v_exp_f16 v8,v9
v_mul_f32_dpp v124,v14,v124 row_newbcast:8 row_mask:0xf bank_mask:0xf
v_mfma_f32_16x16x32_bf16 v[56:59],a[212:215],a[84:87],v[16:19]
ds_read_u16_d16_hi v39,v39
v_mul_f32_dpp v125,v14,v125 row_newbcast:9 row_mask:0xf bank_mask:0xf
v_lshlrev_b32_sdwa v42,1,v42 dst_sel:DWORD dst_unused:UNUSED_PAD src0_sel:DWORD src1_sel:WORD_1
v_mul_f32_dpp v126,v14,v126 row_newbcast:10 row_mask:0xf bank_mask:0xf
v_mfma_f32_16x16x32_bf16 v[56:59],a[220:223],a[68:71],v[56:59]
ds_read_u16 v40,v40
v_mul_f32_dpp v127,v14,v127 row_newbcast:11 row_mask:0xf bank_mask:0xf
v_lshlrev_b32_sdwa v41,1,v41 dst_sel:DWORD dst_unused:UNUSED_PAD src0_sel:DWORD src1_sel:WORD_1
v_mfma_f32_16x16x32_bf16 v[56:59],a[216:219],a[52:55],v[56:59]
ds_read_u16_d16_hi v42,v42
v_exp_f16_sdwa v8,v9 dst_sel:WORD_1 dst_unused:UNUSED_PRESERVE src0_sel:WORD_1
v_mul_f32_dpp v92,v14,v92 row_newbcast:12 row_mask:0xf bank_mask:0xf
v_mul_f32_dpp v93,v14,v93 row_newbcast:13 row_mask:0xf bank_mask:0xf
v_mfma_f32_16x16x32_bf16 v[232:235],a[240:243],v[60:63],v[232:235]
ds_read_u16 v41,v41
v_mul_f32_dpp v94,v14,v94 row_newbcast:14 row_mask:0xf bank_mask:0xf
v_lshlrev_b32_sdwa v43,1,v43 dst_sel:DWORD dst_unused:UNUSED_PAD src0_sel:DWORD src1_sel:WORD_1
v_mfma_f32_16x16x32_bf16 v[236:239],a[240:243],v[32:35],v[236:239]
ds_read_u16_d16_hi v43,v43
v_mul_f32_dpp v95,v14,v95 row_newbcast:15 row_mask:0xf bank_mask:0xf
v_lshlrev_b32_sdwa v44,1,v44 dst_sel:DWORD dst_unused:UNUSED_PAD src0_sel:DWORD src1_sel:WORD_1
v_cvt_pk_bf16_f32 v124,v124,v125
v_mfma_f32_16x16x32_bf16 v[164:167],a[228:231],v[32:35],v[164:167]
ds_read_u16 v44,v44
v_cvt_pk_bf16_f32 v125,v126,v127
v_pk_fma_f16 v8,v8,s55,s55
v_mfma_f32_16x16x32_bf16 v[160:163],a[228:231],v[60:63],v[160:163]
v_rcp_f16 v9,v8
v_cvt_pk_bf16_f32 v126,v92,v93
v_lshlrev_b32_sdwa v46,1,v46 dst_sel:DWORD dst_unused:UNUSED_PAD src0_sel:DWORD src1_sel:WORD_1
v_cvt_pk_bf16_f32 v127,v94,v95
v_mfma_f32_16x16x32_bf16 v[240:243],a[248:251],v[60:63],v[240:243]
ds_read_u16_d16_hi v46,v46
v_mul_f32_dpp v132,v15,v132 row_newbcast:0 row_mask:0xf bank_mask:0xf
v_mfma_f32_16x16x32_bf16 v[244:247],a[248:251],v[32:35],v[244:247]
s_waitcnt lgkmcnt(7)
buffer_store_dwordx4 v[112:115],v7,s[40:43],s63 offen offset:0 sc1
v_mul_f32_dpp v133,v15,v133 row_newbcast:1 row_mask:0xf bank_mask:0xf
v_lshlrev_b32_sdwa v45,1,v45 dst_sel:DWORD dst_unused:UNUSED_PAD src0_sel:DWORD src1_sel:WORD_1
v_mul_f32_dpp v134,v15,v134 row_newbcast:2 row_mask:0xf bank_mask:0xf
v_mfma_f32_16x16x32_bf16 v[180:183],a[236:239],v[32:35],v[180:183]
ds_read_u16 v45,v45
v_mul_f32_dpp v135,v15,v135 row_newbcast:3 row_mask:0xf bank_mask:0xf
v_lshlrev_b32_sdwa v47,1,v47 dst_sel:DWORD dst_unused:UNUSED_PAD src0_sel:DWORD src1_sel:WORD_1
v_mfma_f32_16x16x32_bf16 v[176:179],a[236:239],v[60:63],v[176:179]
v_rcp_f16_sdwa v9,v8 dst_sel:WORD_1 dst_unused:UNUSED_PRESERVE src0_sel:WORD_1
v_mul_f32_dpp v108,v15,v108 row_newbcast:4 row_mask:0xf bank_mask:0xf
v_mul_f32_dpp v109,v15,v109 row_newbcast:5 row_mask:0xf bank_mask:0xf
v_mfma_f32_16x16x32_bf16 v[248:251],a[252:255],v[60:63],v[248:251]
ds_read_u16_d16_hi v47,v47
v_mul_f32_dpp v110,v15,v110 row_newbcast:6 row_mask:0xf bank_mask:0xf
v_lshlrev_b32_sdwa v48,1,v48 dst_sel:DWORD dst_unused:UNUSED_PAD src0_sel:DWORD src1_sel:WORD_1
v_mfma_f32_16x16x32_bf16 v[252:255],a[252:255],v[32:35],v[252:255]
ds_read_u16 v48,v48
v_mul_f32_dpp v111,v15,v111 row_newbcast:7 row_mask:0xf bank_mask:0xf
v_cvt_pk_bf16_f32 v132,v132,v133
v_lshlrev_b32_sdwa v50,1,v50 dst_sel:DWORD dst_unused:UNUSED_PAD src0_sel:DWORD src1_sel:WORD_1
v_mfma_f32_16x16x32_bf16 v[196:199],a[224:227],v[32:35],v[196:199]
ds_read_u16_d16_hi v50,v50
v_cvt_pk_bf16_f32 v133,v134,v135
v_fma_mix_f32 v49,v49,v9,0 op_sel_hi:[0,1,0]
v_mfma_f32_16x16x32_bf16 v[192:195],a[224:227],v[60:63],v[192:195]
ds_read_b64_tr_b16 a[232:233],v3 offset:6160
v_cvt_pk_bf16_f32 v134,v108,v109
v_cvt_pk_bf16_f32 v135,v110,v111
v_fma_mix_f32 v51,v51,v9,0 op_sel:[0,1,0] op_sel_hi:[0,1,0]
v_mfma_f32_16x16x32_bf16 v[32:35],a[216:219],a[56:59],v[16:19]
ds_read_b64_tr_b16 a[196:197],v1 offset:6160
ds_read_b64_tr_b16 a[198:199],v1 offset:7184
v_lshlrev_b32_sdwa v24,1,v24 dst_sel:DWORD dst_unused:UNUSED_PAD src0_sel:DWORD src1_sel:WORD_1
v_mfma_f32_16x16x32_bf16 v[32:35],a[220:223],a[72:75],v[32:35]
ds_read_b64_tr_b16 a[192:193],v1 offset:8208
v_fma_mixlo_f16 v8,v53,s99,0
v_mfma_f32_16x16x32_bf16 v[32:35],a[212:215],a[88:91],v[32:35]
s_waitcnt lgkmcnt(8)
ds_read_b64_tr_b16 a[194:195],v1 offset:9232
v_lshl_add_u64 v[36:37],v[36:37],0,v[38:39]
v_lshlrev_b32_sdwa v26,1,v26 dst_sel:DWORD dst_unused:UNUSED_PAD src0_sel:DWORD src1_sel:WORD_1
v_mfma_f32_16x16x32_bf16 v[60:63],a[208:211],a[88:91],v[20:23]
ds_read_u16 v24,v24
v_lshl_add_u64 v[38:39],v[40:41],0,v[42:43]
v_fma_mixhi_f16 v8,v55,s99,0
v_mfma_f32_16x16x32_bf16 v[60:63],a[204:207],a[72:75],v[60:63]
s_waitcnt lgkmcnt(8)
ds_read_u16_d16_hi v26,v26
v_lshl_add_u64 v[40:41],v[44:45],0,v[46:47]
v_lshlrev_b32_sdwa v25,1,v25 dst_sel:DWORD dst_unused:UNUSED_PAD src0_sel:DWORD src1_sel:WORD_1
v_pk_fma_f16 v9,v8,v8,s54
v_mfma_f32_16x16x32_bf16 v[60:63],a[200:203],a[56:59],v[60:63]
ds_read_u16 v25,v25
v_lshlrev_b32_sdwa v27,1,v27 dst_sel:DWORD dst_unused:UNUSED_PAD src0_sel:DWORD src1_sel:WORD_1
v_mfma_f32_16x16x32_bf16 v[44:47],a[200:203],a[60:63],v[20:23]
s_waitcnt lgkmcnt(8)
ds_read_b128 v[20:23],v4 offset:288
ds_read_u16_d16_hi v27,v27
v_or_b32 v42,v48,v50
v_pk_mul_f16 v9,v8,v9
v_mfma_f32_16x16x32_bf16 v[44:47],a[204:207],a[76:79],v[44:47]
ds_read_b64_tr_b16 a[200:201],v1 offset:10256
ds_read_b64_tr_b16 a[202:203],v1 offset:11280
v_cvt_pk_bf16_f32 v43,v49,v51
v_lshlrev_b32_sdwa v28,1,v28 dst_sel:DWORD dst_unused:UNUSED_PAD src0_sel:DWORD src1_sel:WORD_1
v_mfma_f32_16x16x32_bf16 v[44:47],a[208:211],a[92:95],v[44:47]
ds_read_b64_tr_b16 a[204:205],v2 offset:10256
v_exp_f16 v8,v9
v_mfma_f32_16x16x32_bf16 v[48:51],a[212:215],a[92:95],v[16:19]
buffer_store_dwordx4 v[120:123],v7,s[40:43],s63 offen offset:64 sc1
ds_read_b128 v[16:19],v4 offset:256
v_lshlrev_b32_sdwa v30,1,v30 dst_sel:DWORD dst_unused:UNUSED_PAD src0_sel:DWORD src1_sel:WORD_1
v_mfma_f32_16x16x32_bf16 v[48:51],a[220:223],a[76:79],v[48:51]
ds_read_b64_tr_b16 a[206:207],v2 offset:11280
ds_read_u16 v28,v28
v_lshlrev_b32_sdwa v29,1,v29 dst_sel:DWORD dst_unused:UNUSED_PAD src0_sel:DWORD src1_sel:WORD_1
v_mfma_f32_16x16x32_bf16 v[48:51],a[216:219],a[60:63],v[48:51]
ds_read_u16_d16_hi v30,v30
v_exp_f16_sdwa v8,v9 dst_sel:WORD_1 dst_unused:UNUSED_PRESERVE src0_sel:WORD_1
v_mfma_f32_16x16x32_bf16 v[208:211],a[240:243],v[36:39],v[208:211]
ds_read_u16 v29,v29
ds_read_b64_tr_b16 a[208:209],v2 offset:8208
v_lshlrev_b32_sdwa v31,1,v31 dst_sel:DWORD dst_unused:UNUSED_PAD src0_sel:DWORD src1_sel:WORD_1
v_mfma_f32_16x16x32_bf16 v[212:215],a[240:243],v[40:43],v[212:215]
ds_read_u16_d16_hi v31,v31
ds_read_b64_tr_b16 a[210:211],v2 offset:9232
v_lshlrev_b32_sdwa v56,1,v56 dst_sel:DWORD dst_unused:UNUSED_PAD src0_sel:DWORD src1_sel:WORD_1
v_mfma_f32_16x16x32_bf16 v[172:175],a[228:231],v[40:43],v[172:175]
ds_read_u16 v56,v56
ds_read_b64_tr_b16 a[212:213],v2 offset:6160
v_pk_fma_f16 v8,v8,s55,s55
v_mfma_f32_16x16x32_bf16 v[168:171],a[228:231],v[36:39],v[168:171]
ds_read_b64_tr_b16 a[214:215],v2 offset:7184
v_rcp_f16 v9,v8
v_lshlrev_b32_sdwa v58,1,v58 dst_sel:DWORD dst_unused:UNUSED_PAD src0_sel:DWORD src1_sel:WORD_1
v_mfma_f32_16x16x32_bf16 v[216:219],a[248:251],v[36:39],v[216:219]
ds_read_u16_d16_hi v58,v58
ds_read_b64_tr_b16 a[234:235],v3 offset:7696
v_mfma_f32_16x16x32_bf16 v[220:223],a[248:251],v[40:43],v[220:223]
ds_read_b64_tr_b16 a[244:245],v3 offset:6168
ds_read_b64_tr_b16 a[246:247],v3 offset:7704
v_lshlrev_b32_sdwa v57,1,v57 dst_sel:DWORD dst_unused:UNUSED_PAD src0_sel:DWORD src1_sel:WORD_1
v_mfma_f32_16x16x32_bf16 v[188:191],a[236:239],v[40:43],v[188:191]
ds_read_u16 v57,v57
ds_read_b64_tr_b16 a[240:241],v3 offset:6224
v_lshlrev_b32_sdwa v59,1,v59 dst_sel:DWORD dst_unused:UNUSED_PAD src0_sel:DWORD src1_sel:WORD_1
v_mfma_f32_16x16x32_bf16 v[184:187],a[236:239],v[36:39],v[184:187]
ds_read_u16_d16_hi v59,v59
v_rcp_f16_sdwa v9,v8 dst_sel:WORD_1 dst_unused:UNUSED_PRESERVE src0_sel:WORD_1
v_mfma_f32_16x16x32_bf16 v[224:227],a[252:255],v[36:39],v[224:227]
ds_read_b64_tr_b16 a[242:243],v3 offset:7760
ds_read_b64_tr_b16 a[228:229],v3 offset:6232
v_lshlrev_b32_sdwa v52,1,v52 dst_sel:DWORD dst_unused:UNUSED_PAD src0_sel:DWORD src1_sel:WORD_1
v_mfma_f32_16x16x32_bf16 v[228:231],a[252:255],v[40:43],v[228:231]
ds_read_u16 v52,v52
v_lshlrev_b32_sdwa v54,1,v54 dst_sel:DWORD dst_unused:UNUSED_PAD src0_sel:DWORD src1_sel:WORD_1
v_mfma_f32_16x16x32_bf16 v[204:207],a[224:227],v[40:43],v[204:207]
ds_read_u16_d16_hi v54,v54
v_fma_mix_f32 v53,v53,v9,0 op_sel_hi:[0,1,0]
v_mfma_f32_16x16x32_bf16 v[200:203],a[224:227],v[36:39],v[200:203]
s_waitcnt lgkmcnt(12)
ds_read_b64_tr_b16 a[230:231],v3 offset:7768
v_fma_mix_f32 v55,v55,v9,0 op_sel:[0,1,0] op_sel_hi:[0,1,0]
v_mfma_f32_16x16x32_bf16 v[40:43],a[196:199],a[48:51],v[16:19]
ds_read_b64_tr_b16 a[248:249],v3 offset:6288
v_lshlrev_b32_sdwa v32,1,v32 dst_sel:DWORD dst_unused:UNUSED_PAD src0_sel:DWORD src1_sel:WORD_1
v_mfma_f32_16x16x32_bf16 v[40:43],a[192:195],a[64:67],v[40:43]
buffer_store_dwordx4 v[128:131],v7,s[40:43],s63 offen offset:128 sc1
v_fma_mixlo_f16 v8,v45,s99,0
v_mfma_f32_16x16x32_bf16 v[40:43],a[200:203],a[80:83],v[40:43]
ds_read_b64_tr_b16 a[250:251],v3 offset:7824
v_lshl_add_u64 v[24:25],v[24:25],0,v[26:27]
v_lshlrev_b32_sdwa v34,1,v34 dst_sel:DWORD dst_unused:UNUSED_PAD src0_sel:DWORD src1_sel:WORD_1
v_mfma_f32_16x16x32_bf16 v[36:39],a[204:207],a[80:83],v[20:23]
ds_read_b64_tr_b16 a[236:237],v3 offset:6296
v_lshl_add_u64 v[26:27],v[28:29],0,v[30:31]
v_fma_mixhi_f16 v8,v47,s99,0
v_mfma_f32_16x16x32_bf16 v[36:39],a[208:211],a[64:67],v[36:39]
s_waitcnt lgkmcnt(8)
ds_read_b64_tr_b16 a[238:239],v3 offset:7832
v_lshl_add_u64 v[28:29],v[56:57],0,v[58:59]
v_lshlrev_b32_sdwa v33,1,v33 dst_sel:DWORD dst_unused:UNUSED_PAD src0_sel:DWORD src1_sel:WORD_1
v_pk_fma_f16 v9,v8,v8,s54
v_mfma_f32_16x16x32_bf16 v[36:39],a[212:215],a[48:51],v[36:39]
ds_read_u16 v32,v32
v_lshlrev_b32_sdwa v35,1,v35 dst_sel:DWORD dst_unused:UNUSED_PAD src0_sel:DWORD src1_sel:WORD_1
v_mfma_f32_16x16x32_bf16 v[56:59],a[212:215],a[52:55],v[20:23]
s_waitcnt lgkmcnt(6)
ds_read_u16_d16_hi v34,v34
v_or_b32 v30,v52,v54
TP10846_%=:
s_cbranch_execz TR1_%=
v_pk_mul_f16 v9,v8,v9
v_mfma_f32_16x16x32_bf16 v[56:59],a[208:211],a[68:71],v[56:59]
s_waitcnt vmcnt(12)
ds_read_u16 v33,v33
v_cvt_pk_bf16_f32 v31,v53,v55
v_lshlrev_b32_sdwa v60,1,v60 dst_sel:DWORD dst_unused:UNUSED_PAD src0_sel:DWORD src1_sel:WORD_1
v_mfma_f32_16x16x32_bf16 v[56:59],a[204:207],a[84:87],v[56:59]
s_barrier
v_exp_f16 v8,v9
v_mfma_f32_16x16x32_bf16 v[52:55],a[200:203],a[84:87],v[16:19]
ds_read_u16_d16_hi v35,v35
v_lshlrev_b32_sdwa v62,1,v62 dst_sel:DWORD dst_unused:UNUSED_PAD src0_sel:DWORD src1_sel:WORD_1
v_mfma_f32_16x16x32_bf16 v[52:55],a[192:195],a[68:71],v[52:55]
s_add_u32 m0,s58,0
ds_read_u16 v60,v60
v_lshlrev_b32_sdwa v61,1,v61 dst_sel:DWORD dst_unused:UNUSED_PAD src0_sel:DWORD src1_sel:WORD_1
v_mfma_f32_16x16x32_bf16 v[52:55],a[196:199],a[52:55],v[52:55]
s_add_u32 s69,s69,s59
ds_read_u16_d16_hi v62,v62
v_exp_f16_sdwa v8,v9 dst_sel:WORD_1 dst_unused:UNUSED_PRESERVE src0_sel:WORD_1
v_mfma_f32_16x16x32_bf16 v[232:235],a[232:235],v[24:27],v[232:235]
buffer_load_dwordx4 v5,s[44:47],s69 offen offset:0 lds
v_lshlrev_b32_sdwa v63,1,v63 dst_sel:DWORD dst_unused:UNUSED_PAD src0_sel:DWORD src1_sel:WORD_1
v_mfma_f32_16x16x32_bf16 v[236:239],a[232:235],v[28:31],v[236:239]
ds_read_u16 v61,v61
v_lshlrev_b32_sdwa v48,1,v48 dst_sel:DWORD dst_unused:UNUSED_PAD src0_sel:DWORD src1_sel:WORD_1
v_mfma_f32_16x16x32_bf16 v[164:167],a[244:247],v[28:31],v[164:167]
ds_read_u16_d16_hi v63,v63
v_pk_fma_f16 v8,v8,s55,s55
v_mfma_f32_16x16x32_bf16 v[160:163],a[244:247],v[24:27],v[160:163]
s_add_u32 s69,s69,s53
v_rcp_f16 v9,v8
v_lshlrev_b32_sdwa v50,1,v50 dst_sel:DWORD dst_unused:UNUSED_PAD src0_sel:DWORD src1_sel:WORD_1
v_mfma_f32_16x16x32_bf16 v[240:243],a[240:243],v[24:27],v[240:243]
buffer_load_dwordx4 v5,s[44:47],s69 offen offset:1024 lds
ds_read_u16 v48,v48
v_mfma_f32_16x16x32_bf16 v[244:247],a[240:243],v[28:31],v[244:247]
s_waitcnt lgkmcnt(6)
ds_read_u16_d16_hi v50,v50
v_lshlrev_b32_sdwa v49,1,v49 dst_sel:DWORD dst_unused:UNUSED_PAD src0_sel:DWORD src1_sel:WORD_1
v_mfma_f32_16x16x32_bf16 v[180:183],a[228:231],v[28:31],v[180:183]
ds_read_u16 v49,v49
v_lshlrev_b32_sdwa v51,1,v51 dst_sel:DWORD dst_unused:UNUSED_PAD src0_sel:DWORD src1_sel:WORD_1
v_mfma_f32_16x16x32_bf16 v[176:179],a[228:231],v[24:27],v[176:179]
v_rcp_f16_sdwa v9,v8 dst_sel:WORD_1 dst_unused:UNUSED_PRESERVE src0_sel:WORD_1
v_mfma_f32_16x16x32_bf16 v[248:251],a[248:251],v[24:27],v[248:251]
buffer_store_dwordx4 v[116:119],v7,s[40:43],s63 offen offset:192 sc1
ds_read_u16_d16_hi v51,v51
v_lshlrev_b32_sdwa v44,1,v44 dst_sel:DWORD dst_unused:UNUSED_PAD src0_sel:DWORD src1_sel:WORD_1
v_mfma_f32_16x16x32_bf16 v[252:255],a[248:251],v[28:31],v[252:255]
s_add_u32 s69,s69,s53
ds_read_u16 v44,v44
v_lshlrev_b32_sdwa v46,1,v46 dst_sel:DWORD dst_unused:UNUSED_PAD src0_sel:DWORD src1_sel:WORD_1
v_mfma_f32_16x16x32_bf16 v[196:199],a[236:239],v[28:31],v[196:199]
buffer_load_dwordx4 v5,s[44:47],s69 offen offset:2048 lds
v_fma_mix_f32 v45,v45,v9,0 op_sel_hi:[0,1,0]
v_mfma_f32_16x16x32_bf16 v[192:195],a[236:239],v[24:27],v[192:195]
ds_read_u16_d16_hi v46,v46
v_fma_mix_f32 v47,v47,v9,0 op_sel:[0,1,0] op_sel_hi:[0,1,0]
v_mfma_f32_16x16x32_bf16 v[28:31],a[196:199],a[56:59],v[16:19]
ds_read_b64_tr_b16 a[220:221],v1 offset:12320
ds_read_b64_tr_b16 a[222:223],v1 offset:13344
v_lshlrev_b32_sdwa v40,1,v40 dst_sel:DWORD dst_unused:UNUSED_PAD src0_sel:DWORD src1_sel:WORD_1
v_mfma_f32_16x16x32_bf16 v[28:31],a[192:195],a[72:75],v[28:31]
ds_read_b64_tr_b16 a[216:217],v1 offset:14368
ds_read_b64_tr_b16 a[218:219],v1 offset:15392
v_fma_mixlo_f16 v8,v57,s99,0
v_mfma_f32_16x16x32_bf16 v[28:31],a[200:203],a[88:91],v[28:31]
s_waitcnt lgkmcnt(8)
ds_read_u16 v40,v40
v_lshl_add_u64 v[32:33],v[32:33],0,v[34:35]
v_lshlrev_b32_sdwa v42,1,v42 dst_sel:DWORD dst_unused:UNUSED_PAD src0_sel:DWORD src1_sel:WORD_1
v_mfma_f32_16x16x32_bf16 v[24:27],a[204:207],a[88:91],v[20:23]
ds_read_u16_d16_hi v42,v42
v_lshl_add_u64 v[34:35],v[60:61],0,v[62:63]
v_fma_mixhi_f16 v8,v59,s99,0
v_mfma_f32_16x16x32_bf16 v[24:27],a[208:211],a[72:75],v[24:27]
s_waitcnt lgkmcnt(8)
ds_read_b64_tr_b16 a[252:253],v3 offset:12320
v_lshl_add_u64 v[60:61],v[48:49],0,v[50:51]
v_lshlrev_b32_sdwa v41,1,v41 dst_sel:DWORD dst_unused:UNUSED_PAD src0_sel:DWORD src1_sel:WORD_1
v_pk_fma_f16 v9,v8,v8,s54
v_mfma_f32_16x16x32_bf16 v[24:27],a[212:215],a[56:59],v[24:27]
ds_read_u16 v41,v41
v_lshlrev_b32_sdwa v43,1,v43 dst_sel:DWORD dst_unused:UNUSED_PAD src0_sel:DWORD src1_sel:WORD_1
v_mfma_f32_16x16x32_bf16 v[48:51],a[212:215],a[60:63],v[20:23]
s_waitcnt lgkmcnt(8)
ds_read_b128 v[20:23],v4 offset:416
ds_read_u16_d16_hi v43,v43
v_or_b32 v62,v44,v46
v_pk_mul_f16 v9,v8,v9
v_mfma_f32_16x16x32_bf16 v[48:51],a[208:211],a[76:79],v[48:51]
ds_read_b64_tr_b16 a[212:213],v1 offset:16416
ds_read_b64_tr_b16 a[214:215],v1 offset:17440
v_cvt_pk_bf16_f32 v63,v45,v47
v_lshlrev_b32_sdwa v36,1,v36 dst_sel:DWORD dst_unused:UNUSED_PAD src0_sel:DWORD src1_sel:WORD_1
v_mfma_f32_16x16x32_bf16 v[48:51],a[204:207],a[92:95],v[48:51]
ds_read_b64_tr_b16 a[208:209],v2 offset:16416
v_exp_f16 v8,v9
v_mfma_f32_16x16x32_bf16 v[44:47],a[200:203],a[92:95],v[16:19]
ds_read_b128 v[16:19],v4 offset:384
ds_read_b64_tr_b16 a[210:211],v2 offset:17440
v_lshlrev_b32_sdwa v38,1,v38 dst_sel:DWORD dst_unused:UNUSED_PAD src0_sel:DWORD src1_sel:WORD_1
v_mfma_f32_16x16x32_bf16 v[44:47],a[192:195],a[76:79],v[44:47]
ds_read_u16 v36,v36
ds_read_u16_d16_hi v38,v38
v_lshlrev_b32_sdwa v37,1,v37 dst_sel:DWORD dst_unused:UNUSED_PAD src0_sel:DWORD src1_sel:WORD_1
v_mfma_f32_16x16x32_bf16 v[44:47],a[196:199],a[60:63],v[44:47]
ds_read_u16 v37,v37
v_exp_f16_sdwa v8,v9 dst_sel:WORD_1 dst_unused:UNUSED_PRESERVE src0_sel:WORD_1
v_mfma_f32_16x16x32_bf16 v[208:211],a[232:235],v[32:35],v[208:211]
ds_read_b64_tr_b16 a[204:205],v2 offset:14368
ds_read_b64_tr_b16 a[206:207],v2 offset:15392
v_lshlrev_b32_sdwa v39,1,v39 dst_sel:DWORD dst_unused:UNUSED_PAD src0_sel:DWORD src1_sel:WORD_1
v_mfma_f32_16x16x32_bf16 v[212:215],a[232:235],v[60:63],v[212:215]
buffer_store_dwordx4 v[124:127],v7,s[40:43],s63 offen offset:256 sc1
ds_read_u16_d16_hi v39,v39
v_lshlrev_b32_sdwa v52,1,v52 dst_sel:DWORD dst_unused:UNUSED_PAD src0_sel:DWORD src1_sel:WORD_1
v_mfma_f32_16x16x32_bf16 v[172:175],a[244:247],v[60:63],v[172:175]
ds_read_u16 v52,v52
ds_read_b64_tr_b16 a[200:201],v2 offset:12320
v_pk_fma_f16 v8,v8,s55,s55
v_mfma_f32_16x16x32_bf16 v[168:171],a[244:247],v[32:35],v[168:171]
ds_read_b64_tr_b16 a[202:203],v2 offset:13344
v_rcp_f16 v9,v8
v_lshlrev_b32_sdwa v54,1,v54 dst_sel:DWORD dst_unused:UNUSED_PAD src0_sel:DWORD src1_sel:WORD_1
v_mfma_f32_16x16x32_bf16 v[216:219],a[240:243],v[32:35],v[216:219]
ds_read_u16_d16_hi v54,v54
ds_read_b64_tr_b16 a[254:255],v3 offset:13856
v_mfma_f32_16x16x32_bf16 v[220:223],a[240:243],v[60:63],v[220:223]
ds_read_b64_tr_b16 a[224:225],v3 offset:12328
ds_read_b64_tr_b16 a[226:227],v3 offset:13864
v_lshlrev_b32_sdwa v53,1,v53 dst_sel:DWORD dst_unused:UNUSED_PAD src0_sel:DWORD src1_sel:WORD_1
v_mfma_f32_16x16x32_bf16 v[188:191],a[228:231],v[60:63],v[188:191]
ds_read_u16 v53,v53
ds_read_b64_tr_b16 a[232:233],v3 offset:12384
v_lshlrev_b32_sdwa v55,1,v55 dst_sel:DWORD dst_unused:UNUSED_PAD src0_sel:DWORD src1_sel:WORD_1
v_mfma_f32_16x16x32_bf16 v[184:187],a[228:231],v[32:35],v[184:187]
ds_read_u16_d16_hi v55,v55
v_rcp_f16_sdwa v9,v8 dst_sel:WORD_1 dst_unused:UNUSED_PRESERVE src0_sel:WORD_1
v_mfma_f32_16x16x32_bf16 v[224:227],a[248:251],v[32:35],v[224:227]
ds_read_b64_tr_b16 a[234:235],v3 offset:13920
ds_read_b64_tr_b16 a[244:245],v3 offset:12392
v_lshlrev_b32_sdwa v56,1,v56 dst_sel:DWORD dst_unused:UNUSED_PAD src0_sel:DWORD src1_sel:WORD_1
v_mfma_f32_16x16x32_bf16 v[228:231],a[248:251],v[60:63],v[228:231]
ds_read_u16 v56,v56
v_lshlrev_b32_sdwa v58,1,v58 dst_sel:DWORD dst_unused:UNUSED_PAD src0_sel:DWORD src1_sel:WORD_1
v_mfma_f32_16x16x32_bf16 v[204:207],a[236:239],v[60:63],v[204:207]
ds_read_u16_d16_hi v58,v58
v_fma_mix_f32 v57,v57,v9,0 op_sel_hi:[0,1,0]
v_mfma_f32_16x16x32_bf16 v[200:203],a[236:239],v[32:35],v[200:203]
s_waitcnt lgkmcnt(12)
ds_read_b64_tr_b16 a[246:247],v3 offset:13928
v_fma_mix_f32 v59,v59,v9,0 op_sel:[0,1,0] op_sel_hi:[0,1,0]
v_mfma_f32_16x16x32_bf16 v[60:63],a[220:223],a[48:51],v[16:19]
ds_read_b64_tr_b16 a[240:241],v3 offset:12448
v_lshlrev_b32_sdwa v28,1,v28 dst_sel:DWORD dst_unused:UNUSED_PAD src0_sel:DWORD src1_sel:WORD_1
v_mfma_f32_16x16x32_bf16 v[60:63],a[216:219],a[64:67],v[60:63]
ds_read_b64_tr_b16 a[242:243],v3 offset:13984
v_fma_mixlo_f16 v8,v49,s99,0
v_mfma_f32_16x16x32_bf16 v[60:63],a[212:215],a[80:83],v[60:63]
ds_read_b64_tr_b16 a[228:229],v3 offset:12456
v_lshl_add_u64 v[40:41],v[40:41],0,v[42:43]
v_lshlrev_b32_sdwa v30,1,v30 dst_sel:DWORD dst_unused:UNUSED_PAD src0_sel:DWORD src1_sel:WORD_1
v_mfma_f32_16x16x32_bf16 v[32:35],a[208:211],a[80:83],v[20:23]
ds_read_b64_tr_b16 a[230:231],v3 offset:13992
v_lshl_add_u64 v[42:43],v[36:37],0,v[38:39]
v_fma_mixhi_f16 v8,v51,s99,0
v_mfma_f32_16x16x32_bf16 v[32:35],a[204:207],a[64:67],v[32:35]
s_waitcnt lgkmcnt(9)
ds_read_u16 v28,v28
v_lshl_add_u64 v[36:37],v[52:53],0,v[54:55]
v_lshlrev_b32_sdwa v29,1,v29 dst_sel:DWORD dst_unused:UNUSED_PAD src0_sel:DWORD src1_sel:WORD_1
v_pk_fma_f16 v9,v8,v8,s54
v_mfma_f32_16x16x32_bf16 v[32:35],a[200:203],a[48:51],v[32:35]
ds_read_u16_d16_hi v30,v30
v_lshlrev_b32_sdwa v31,1,v31 dst_sel:DWORD dst_unused:UNUSED_PAD src0_sel:DWORD src1_sel:WORD_1
v_mfma_f32_16x16x32_bf16 v[52:55],a[200:203],a[52:55],v[20:23]
s_waitcnt lgkmcnt(7)
buffer_store_dwordx4 v[132:135],v7,s[40:43],s63 offen offset:320 sc1
v_or_b32 v38,v56,v58
v_pk_mul_f16 v9,v8,v9
v_mfma_f32_16x16x32_bf16 v[52:55],a[204:207],a[68:71],v[52:55]
s_waitcnt vmcnt(12)
ds_read_u16 v29,v29
v_cvt_pk_bf16_f32 v39,v57,v59
v_lshlrev_b32_sdwa v24,1,v24 dst_sel:DWORD dst_unused:UNUSED_PAD src0_sel:DWORD src1_sel:WORD_1
v_mfma_f32_16x16x32_bf16 v[52:55],a[208:211],a[84:87],v[52:55]
s_barrier
v_exp_f16 v8,v9
v_mfma_f32_16x16x32_bf16 v[56:59],a[212:215],a[84:87],v[16:19]
ds_read_u16_d16_hi v31,v31
v_lshlrev_b32_sdwa v26,1,v26 dst_sel:DWORD dst_unused:UNUSED_PAD src0_sel:DWORD src1_sel:WORD_1
v_mfma_f32_16x16x32_bf16 v[56:59],a[216:219],a[68:71],v[56:59]
ds_read_u16 v24,v24
v_lshlrev_b32_sdwa v25,1,v25 dst_sel:DWORD dst_unused:UNUSED_PAD src0_sel:DWORD src1_sel:WORD_1
v_mfma_f32_16x16x32_bf16 v[56:59],a[220:223],a[52:55],v[56:59]
ds_read_u16_d16_hi v26,v26
v_exp_f16_sdwa v8,v9 dst_sel:WORD_1 dst_unused:UNUSED_PRESERVE src0_sel:WORD_1
v_mfma_f32_16x16x32_bf16 v[232:235],a[252:255],v[40:43],v[232:235]
ds_read_u16 v25,v25
v_lshlrev_b32_sdwa v27,1,v27 dst_sel:DWORD dst_unused:UNUSED_PAD src0_sel:DWORD src1_sel:WORD_1
v_mfma_f32_16x16x32_bf16 v[236:239],a[252:255],v[36:39],v[236:239]
ds_read_u16_d16_hi v27,v27
v_lshlrev_b32_sdwa v44,1,v44 dst_sel:DWORD dst_unused:UNUSED_PAD src0_sel:DWORD src1_sel:WORD_1
v_mfma_f32_16x16x32_bf16 v[164:167],a[224:227],v[36:39],v[164:167]
ds_read_u16 v44,v44
v_pk_fma_f16 v8,v8,s55,s55
v_mfma_f32_16x16x32_bf16 v[160:163],a[224:227],v[40:43],v[160:163]
v_rcp_f16 v9,v8
v_lshlrev_b32_sdwa v46,1,v46 dst_sel:DWORD dst_unused:UNUSED_PAD src0_sel:DWORD src1_sel:WORD_1
v_mfma_f32_16x16x32_bf16 v[240:243],a[232:235],v[40:43],v[240:243]
ds_read_u16_d16_hi v46,v46
v_lshlrev_b32_sdwa v45,1,v45 dst_sel:DWORD dst_unused:UNUSED_PAD src0_sel:DWORD src1_sel:WORD_1
v_mfma_f32_16x16x32_bf16 v[244:247],a[232:235],v[36:39],v[244:247]
s_waitcnt lgkmcnt(7)
ds_read_u16 v45,v45
v_lshlrev_b32_sdwa v47,1,v47 dst_sel:DWORD dst_unused:UNUSED_PAD src0_sel:DWORD src1_sel:WORD_1
v_mfma_f32_16x16x32_bf16 v[180:183],a[244:247],v[36:39],v[180:183]
ds_read_u16_d16_hi v47,v47
v_lshlrev_b32_sdwa v48,1,v48 dst_sel:DWORD dst_unused:UNUSED_PAD src0_sel:DWORD src1_sel:WORD_1
v_mfma_f32_16x16x32_bf16 v[176:179],a[244:247],v[40:43],v[176:179]
v_rcp_f16_sdwa v9,v8 dst_sel:WORD_1 dst_unused:UNUSED_PRESERVE src0_sel:WORD_1
v_mfma_f32_16x16x32_bf16 v[248:251],a[240:243],v[40:43],v[248:251]
ds_read_u16 v48,v48
v_lshlrev_b32_sdwa v50,1,v50 dst_sel:DWORD dst_unused:UNUSED_PAD src0_sel:DWORD src1_sel:WORD_1
v_mfma_f32_16x16x32_bf16 v[252:255],a[240:243],v[36:39],v[252:255]
ds_read_u16_d16_hi v50,v50
v_fma_mix_f32 v49,v49,v9,0 op_sel_hi:[0,1,0]
v_mfma_f32_16x16x32_bf16 v[196:199],a[228:231],v[36:39],v[196:199]
ds_read_b64_tr_b16 a[248:249],v3 offset:18480
v_fma_mix_f32 v51,v51,v9,0 op_sel:[0,1,0] op_sel_hi:[0,1,0]
v_mfma_f32_16x16x32_bf16 v[192:195],a[228:231],v[40:43],v[192:195]
ds_read_b64_tr_b16 a[250:251],v3 offset:20016
v_mfma_f32_16x16x32_bf16 v[36:39],a[220:223],a[56:59],v[16:19]
ds_read_b64_tr_b16 a[192:193],v1 offset:18480
v_lshlrev_b32_sdwa v60,1,v60 dst_sel:DWORD dst_unused:UNUSED_PAD src0_sel:DWORD src1_sel:WORD_1
v_mfma_f32_16x16x32_bf16 v[36:39],a[216:219],a[72:75],v[36:39]
ds_read_b64_tr_b16 a[194:195],v1 offset:19504
v_fma_mixlo_f16 v8,v53,s99,0
v_mfma_f32_16x16x32_bf16 v[36:39],a[212:215],a[88:91],v[36:39]
s_waitcnt lgkmcnt(7)
ds_read_b64_tr_b16 a[196:197],v1 offset:20528
v_lshl_add_u64 v[28:29],v[28:29],0,v[30:31]
v_lshlrev_b32_sdwa v62,1,v62 dst_sel:DWORD dst_unused:UNUSED_PAD src0_sel:DWORD src1_sel:WORD_1
v_mfma_f32_16x16x32_bf16 v[40:43],a[208:211],a[88:91],v[20:23]
ds_read_b64_tr_b16 a[198:199],v1 offset:21552
v_lshl_add_u64 v[30:31],v[24:25],0,v[26:27]
v_fma_mixhi_f16 v8,v55,s99,0
v_mfma_f32_16x16x32_bf16 v[40:43],a[204:207],a[72:75],v[40:43]
s_waitcnt lgkmcnt(8)
ds_read_u16 v60,v60
v_lshl_add_u64 v[24:25],v[44:45],0,v[46:47]
v_lshlrev_b32_sdwa v61,1,v61 dst_sel:DWORD dst_unused:UNUSED_PAD src0_sel:DWORD src1_sel:WORD_1
v_pk_fma_f16 v9,v8,v8,s54
v_mfma_f32_16x16x32_bf16 v[40:43],a[200:203],a[56:59],v[40:43]
ds_read_u16_d16_hi v62,v62
v_lshlrev_b32_sdwa v63,1,v63 dst_sel:DWORD dst_unused:UNUSED_PAD src0_sel:DWORD src1_sel:WORD_1
v_mfma_f32_16x16x32_bf16 v[44:47],a[200:203],a[60:63],v[20:23]
s_waitcnt lgkmcnt(8)
ds_read_b128 v[20:23],v4 offset:544
ds_read_u16 v61,v61
v_or_b32 v26,v48,v50
v_pk_mul_f16 v9,v8,v9
v_mfma_f32_16x16x32_bf16 v[44:47],a[204:207],a[76:79],v[44:47]
ds_read_u16_d16_hi v63,v63
ds_read_b64_tr_b16 a[200:201],v1 offset:22576
v_cvt_pk_bf16_f32 v27,v49,v51
v_lshlrev_b32_sdwa v32,1,v32 dst_sel:DWORD dst_unused:UNUSED_PAD src0_sel:DWORD src1_sel:WORD_1
v_mfma_f32_16x16x32_bf16 v[44:47],a[208:211],a[92:95],v[44:47]
ds_read_b64_tr_b16 a[202:203],v1 offset:23600
v_exp_f16 v8,v9
v_mfma_f32_16x16x32_bf16 v[48:51],a[212:215],a[92:95],v[16:19]
ds_read_b128 v[16:19],v4 offset:512
ds_read_b64_tr_b16 a[204:205],v2 offset:22576
v_lshlrev_b32_sdwa v34,1,v34 dst_sel:DWORD dst_unused:UNUSED_PAD src0_sel:DWORD src1_sel:WORD_1
v_mfma_f32_16x16x32_bf16 v[48:51],a[216:219],a[76:79],v[48:51]
ds_read_b64_tr_b16 a[206:207],v2 offset:23600
ds_read_u16 v32,v32
v_lshlrev_b32_sdwa v33,1,v33 dst_sel:DWORD dst_unused:UNUSED_PAD src0_sel:DWORD src1_sel:WORD_1
v_mfma_f32_16x16x32_bf16 v[48:51],a[220:223],a[60:63],v[48:51]
ds_read_u16_d16_hi v34,v34
v_exp_f16_sdwa v8,v9 dst_sel:WORD_1 dst_unused:UNUSED_PRESERVE src0_sel:WORD_1
v_mfma_f32_16x16x32_bf16 v[208:211],a[252:255],v[28:31],v[208:211]
ds_read_u16 v33,v33
ds_read_b64_tr_b16 a[208:209],v2 offset:20528
v_lshlrev_b32_sdwa v35,1,v35 dst_sel:DWORD dst_unused:UNUSED_PAD src0_sel:DWORD src1_sel:WORD_1
v_mfma_f32_16x16x32_bf16 v[212:215],a[252:255],v[24:27],v[212:215]
ds_read_u16_d16_hi v35,v35
ds_read_b64_tr_b16 a[210:211],v2 offset:21552
v_lshlrev_b32_sdwa v56,1,v56 dst_sel:DWORD dst_unused:UNUSED_PAD src0_sel:DWORD src1_sel:WORD_1
v_mfma_f32_16x16x32_bf16 v[172:175],a[224:227],v[24:27],v[172:175]
ds_read_u16 v56,v56
ds_read_b64_tr_b16 a[212:213],v2 offset:18480
v_pk_fma_f16 v8,v8,s55,s55
v_mfma_f32_16x16x32_bf16 v[168:171],a[224:227],v[28:31],v[168:171]
ds_read_b64_tr_b16 a[214:215],v2 offset:19504
v_rcp_f16 v9,v8
v_lshlrev_b32_sdwa v58,1,v58 dst_sel:DWORD dst_unused:UNUSED_PAD src0_sel:DWORD src1_sel:WORD_1
v_mfma_f32_16x16x32_bf16 v[216:219],a[232:235],v[28:31],v[216:219]
ds_read_u16_d16_hi v58,v58
ds_read_b64_tr_b16 a[236:237],v3 offset:18488
v_mfma_f32_16x16x32_bf16 v[220:223],a[232:235],v[24:27],v[220:223]
ds_read_b64_tr_b16 a[238:239],v3 offset:20024
ds_read_b64_tr_b16 a[252:253],v3 offset:18544
v_lshlrev_b32_sdwa v57,1,v57 dst_sel:DWORD dst_unused:UNUSED_PAD src0_sel:DWORD src1_sel:WORD_1
v_mfma_f32_16x16x32_bf16 v[188:191],a[244:247],v[24:27],v[188:191]
ds_read_u16 v57,v57
v_lshlrev_b32_sdwa v59,1,v59 dst_sel:DWORD dst_unused:UNUSED_PAD src0_sel:DWORD src1_sel:WORD_1
v_mfma_f32_16x16x32_bf16 v[184:187],a[244:247],v[28:31],v[184:187]
ds_read_u16_d16_hi v59,v59
v_rcp_f16_sdwa v9,v8 dst_sel:WORD_1 dst_unused:UNUSED_PRESERVE src0_sel:WORD_1
v_mfma_f32_16x16x32_bf16 v[224:227],a[240:243],v[28:31],v[224:227]
ds_read_b64_tr_b16 a[254:255],v3 offset:20080
ds_read_b64_tr_b16 a[224:225],v3 offset:18552
v_lshlrev_b32_sdwa v52,1,v52 dst_sel:DWORD dst_unused:UNUSED_PAD src0_sel:DWORD src1_sel:WORD_1
v_mfma_f32_16x16x32_bf16 v[228:231],a[240:243],v[24:27],v[228:231]
ds_read_u16 v52,v52
v_lshlrev_b32_sdwa v54,1,v54 dst_sel:DWORD dst_unused:UNUSED_PAD src0_sel:DWORD src1_sel:WORD_1
v_mfma_f32_16x16x32_bf16 v[204:207],a[228:231],v[24:27],v[204:207]
ds_read_u16_d16_hi v54,v54
v_fma_mix_f32 v53,v53,v9,0 op_sel_hi:[0,1,0]
v_mfma_f32_16x16x32_bf16 v[200:203],a[228:231],v[28:31],v[200:203]
s_waitcnt lgkmcnt(11)
ds_read_b64_tr_b16 a[226:227],v3 offset:20088
v_fma_mix_f32 v55,v55,v9,0 op_sel:[0,1,0] op_sel_hi:[0,1,0]
v_mfma_f32_16x16x32_bf16 v[24:27],a[192:195],a[48:51],v[16:19]
ds_read_b64_tr_b16 a[232:233],v3 offset:18608
v_lshlrev_b32_sdwa v36,1,v36 dst_sel:DWORD dst_unused:UNUSED_PAD src0_sel:DWORD src1_sel:WORD_1
v_mfma_f32_16x16x32_bf16 v[24:27],a[196:199],a[64:67],v[24:27]
ds_read_b64_tr_b16 a[234:235],v3 offset:20144
v_fma_mixlo_f16 v8,v45,s99,0
v_mfma_f32_16x16x32_bf16 v[24:27],a[200:203],a[80:83],v[24:27]
ds_read_b64_tr_b16 a[244:245],v3 offset:18616
v_lshl_add_u64 v[60:61],v[60:61],0,v[62:63]
v_lshlrev_b32_sdwa v38,1,v38 dst_sel:DWORD dst_unused:UNUSED_PAD src0_sel:DWORD src1_sel:WORD_1
v_mfma_f32_16x16x32_bf16 v[28:31],a[204:207],a[80:83],v[20:23]
ds_read_b64_tr_b16 a[246:247],v3 offset:20152
v_lshl_add_u64 v[62:63],v[32:33],0,v[34:35]
v_fma_mixhi_f16 v8,v47,s99,0
v_mfma_f32_16x16x32_bf16 v[28:31],a[208:211],a[64:67],v[28:31]
s_waitcnt lgkmcnt(9)
ds_read_u16 v36,v36
v_lshl_add_u64 v[32:33],v[56:57],0,v[58:59]
v_lshlrev_b32_sdwa v37,1,v37 dst_sel:DWORD dst_unused:UNUSED_PAD src0_sel:DWORD src1_sel:WORD_1
v_pk_fma_f16 v9,v8,v8,s54
v_mfma_f32_16x16x32_bf16 v[28:31],a[212:215],a[48:51],v[28:31]
ds_read_u16_d16_hi v38,v38
v_lshlrev_b32_sdwa v39,1,v39 dst_sel:DWORD dst_unused:UNUSED_PAD src0_sel:DWORD src1_sel:WORD_1
v_mfma_f32_16x16x32_bf16 v[56:59],a[212:215],a[52:55],v[20:23]
s_waitcnt lgkmcnt(7)
ds_read_u16 v37,v37
v_or_b32 v34,v52,v54
v_pk_mul_f16 v9,v8,v9
v_mfma_f32_16x16x32_bf16 v[56:59],a[208:211],a[68:71],v[56:59]
s_waitcnt vmcnt(2)
ds_read_u16_d16_hi v39,v39
v_cvt_pk_bf16_f32 v35,v53,v55
v_lshlrev_b32_sdwa v40,1,v40 dst_sel:DWORD dst_unused:UNUSED_PAD src0_sel:DWORD src1_sel:WORD_1
v_mfma_f32_16x16x32_bf16 v[56:59],a[204:207],a[84:87],v[56:59]
s_barrier
v_exp_f16 v8,v9
v_mfma_f32_16x16x32_bf16 v[52:55],a[200:203],a[84:87],v[16:19]
ds_read_u16 v40,v40
v_lshlrev_b32_sdwa v42,1,v42 dst_sel:DWORD dst_unused:UNUSED_PAD src0_sel:DWORD src1_sel:WORD_1
v_mfma_f32_16x16x32_bf16 v[52:55],a[196:199],a[68:71],v[52:55]
ds_read_u16_d16_hi v42,v42
v_lshlrev_b32_sdwa v41,1,v41 dst_sel:DWORD dst_unused:UNUSED_PAD src0_sel:DWORD src1_sel:WORD_1
v_mfma_f32_16x16x32_bf16 v[52:55],a[192:195],a[52:55],v[52:55]
v_exp_f16_sdwa v8,v9 dst_sel:WORD_1 dst_unused:UNUSED_PRESERVE src0_sel:WORD_1
v_mfma_f32_16x16x32_bf16 v[232:235],a[248:251],v[60:63],v[232:235]
ds_read_u16 v41,v41
v_lshlrev_b32_sdwa v43,1,v43 dst_sel:DWORD dst_unused:UNUSED_PAD src0_sel:DWORD src1_sel:WORD_1
v_mfma_f32_16x16x32_bf16 v[236:239],a[248:251],v[32:35],v[236:239]
ds_read_u16_d16_hi v43,v43
v_lshlrev_b32_sdwa v48,1,v48 dst_sel:DWORD dst_unused:UNUSED_PAD src0_sel:DWORD src1_sel:WORD_1
v_mfma_f32_16x16x32_bf16 v[164:167],a[236:239],v[32:35],v[164:167]
ds_read_u16 v48,v48
v_pk_fma_f16 v8,v8,s55,s55
v_mfma_f32_16x16x32_bf16 v[160:163],a[236:239],v[60:63],v[160:163]
v_rcp_f16 v9,v8
v_lshlrev_b32_sdwa v50,1,v50 dst_sel:DWORD dst_unused:UNUSED_PAD src0_sel:DWORD src1_sel:WORD_1
v_mfma_f32_16x16x32_bf16 v[240:243],a[252:255],v[60:63],v[240:243]
ds_read_u16_d16_hi v50,v50
v_lshlrev_b32_sdwa v49,1,v49 dst_sel:DWORD dst_unused:UNUSED_PAD src0_sel:DWORD src1_sel:WORD_1
v_mfma_f32_16x16x32_bf16 v[244:247],a[252:255],v[32:35],v[244:247]
s_waitcnt lgkmcnt(6)
ds_read_u16 v49,v49
v_lshlrev_b32_sdwa v51,1,v51 dst_sel:DWORD dst_unused:UNUSED_PAD src0_sel:DWORD src1_sel:WORD_1
v_mfma_f32_16x16x32_bf16 v[180:183],a[224:227],v[32:35],v[180:183]
ds_read_u16_d16_hi v51,v51
v_lshlrev_b32_sdwa v44,1,v44 dst_sel:DWORD dst_unused:UNUSED_PAD src0_sel:DWORD src1_sel:WORD_1
v_mfma_f32_16x16x32_bf16 v[176:179],a[224:227],v[60:63],v[176:179]
v_rcp_f16_sdwa v9,v8 dst_sel:WORD_1 dst_unused:UNUSED_PRESERVE src0_sel:WORD_1
v_mfma_f32_16x16x32_bf16 v[248:251],a[232:235],v[60:63],v[248:251]
ds_read_u16 v44,v44
v_lshlrev_b32_sdwa v46,1,v46 dst_sel:DWORD dst_unused:UNUSED_PAD src0_sel:DWORD src1_sel:WORD_1
v_mfma_f32_16x16x32_bf16 v[252:255],a[232:235],v[32:35],v[252:255]
ds_read_u16_d16_hi v46,v46
v_fma_mix_f32 v45,v45,v9,0 op_sel_hi:[0,1,0]
v_mfma_f32_16x16x32_bf16 v[196:199],a[244:247],v[32:35],v[196:199]
ds_read_b64_tr_b16 a[240:241],v3 offset:0
v_fma_mix_f32 v47,v47,v9,0 op_sel:[0,1,0] op_sel_hi:[0,1,0]
v_mfma_f32_16x16x32_bf16 v[192:195],a[244:247],v[60:63],v[192:195]
ds_read_b64_tr_b16 a[242:243],v3 offset:1536
v_mov_b32 v11,0
v_mfma_f32_16x16x32_bf16 v[32:35],a[192:195],a[56:59],v[16:19]
ds_read_b128 v[64:67],v11 offset:11648
v_lshlrev_b32_sdwa v24,1,v24 dst_sel:DWORD dst_unused:UNUSED_PAD src0_sel:DWORD src1_sel:WORD_1
v_mfma_f32_16x16x32_bf16 v[32:35],a[196:199],a[72:75],v[32:35]
ds_read_u16 v24,v24
v_fma_mixlo_f16 v8,v57,s99,0
v_mfma_f32_16x16x32_bf16 v[32:35],a[200:203],a[88:91],v[32:35]
ds_read_b64_tr_b16 a[228:229],v3 offset:8
v_lshl_add_u64 v[36:37],v[36:37],0,v[38:39]
v_lshlrev_b32_sdwa v26,1,v26 dst_sel:DWORD dst_unused:UNUSED_PAD src0_sel:DWORD src1_sel:WORD_1
v_mfma_f32_16x16x32_bf16 v[60:63],a[204:207],a[88:91],v[20:23]
s_waitcnt lgkmcnt(7)
ds_read_u16_d16_hi v26,v26
v_lshl_add_u64 v[38:39],v[40:41],0,v[42:43]
v_fma_mixhi_f16 v8,v59,s99,0
v_mfma_f32_16x16x32_bf16 v[60:63],a[208:211],a[72:75],v[60:63]
ds_read_b64_tr_b16 a[230:231],v3 offset:1544
v_lshl_add_u64 v[40:41],v[48:49],0,v[50:51]
v_lshlrev_b32_sdwa v25,1,v25 dst_sel:DWORD dst_unused:UNUSED_PAD src0_sel:DWORD src1_sel:WORD_1
v_pk_fma_f16 v9,v8,v8,s54
v_mfma_f32_16x16x32_bf16 v[60:63],a[212:215],a[56:59],v[60:63]
ds_read_u16 v25,v25
v_lshlrev_b32_sdwa v27,1,v27 dst_sel:DWORD dst_unused:UNUSED_PAD src0_sel:DWORD src1_sel:WORD_1
v_mfma_f32_16x16x32_bf16 v[48:51],a[212:215],a[60:63],v[20:23]
s_waitcnt lgkmcnt(8)
ds_read_u16_d16_hi v27,v27
v_or_b32 v42,v44,v46
v_pk_mul_f16 v9,v8,v9
v_mfma_f32_16x16x32_bf16 v[48:51],a[208:211],a[76:79],v[48:51]
v_cvt_pk_bf16_f32 v43,v45,v47
v_lshlrev_b32_sdwa v28,1,v28 dst_sel:DWORD dst_unused:UNUSED_PAD src0_sel:DWORD src1_sel:WORD_1
v_mfma_f32_16x16x32_bf16 v[48:51],a[204:207],a[92:95],v[48:51]
v_exp_f16 v8,v9
v_mfma_f32_16x16x32_bf16 v[44:47],a[200:203],a[92:95],v[16:19]
ds_read_u16 v28,v28
v_lshlrev_b32_sdwa v30,1,v30 dst_sel:DWORD dst_unused:UNUSED_PAD src0_sel:DWORD src1_sel:WORD_1
v_mfma_f32_16x16x32_bf16 v[44:47],a[196:199],a[76:79],v[44:47]
ds_read_u16_d16_hi v30,v30
v_lshlrev_b32_sdwa v29,1,v29 dst_sel:DWORD dst_unused:UNUSED_PAD src0_sel:DWORD src1_sel:WORD_1
v_mfma_f32_16x16x32_bf16 v[44:47],a[192:195],a[60:63],v[44:47]
v_exp_f16_sdwa v8,v9 dst_sel:WORD_1 dst_unused:UNUSED_PRESERVE src0_sel:WORD_1
v_mfma_f32_16x16x32_bf16 v[208:211],a[248:251],v[36:39],v[208:211]
ds_read_u16 v29,v29
v_lshlrev_b32_sdwa v31,1,v31 dst_sel:DWORD dst_unused:UNUSED_PAD src0_sel:DWORD src1_sel:WORD_1
v_mfma_f32_16x16x32_bf16 v[212:215],a[248:251],v[40:43],v[212:215]
ds_read_u16_d16_hi v31,v31
v_lshlrev_b32_sdwa v52,1,v52 dst_sel:DWORD dst_unused:UNUSED_PAD src0_sel:DWORD src1_sel:WORD_1
v_mfma_f32_16x16x32_bf16 v[172:175],a[236:239],v[40:43],v[172:175]
ds_read_u16 v52,v52
v_pk_fma_f16 v8,v8,s55,s55
v_mfma_f32_16x16x32_bf16 v[168:171],a[236:239],v[36:39],v[168:171]
v_rcp_f16 v9,v8
v_lshlrev_b32_sdwa v54,1,v54 dst_sel:DWORD dst_unused:UNUSED_PAD src0_sel:DWORD src1_sel:WORD_1
v_mfma_f32_16x16x32_bf16 v[216:219],a[252:255],v[36:39],v[216:219]
ds_read_u16_d16_hi v54,v54
v_lshlrev_b32_sdwa v53,1,v53 dst_sel:DWORD dst_unused:UNUSED_PAD src0_sel:DWORD src1_sel:WORD_1
v_mfma_f32_16x16x32_bf16 v[220:223],a[252:255],v[40:43],v[220:223]
ds_read_u16 v53,v53
ds_read_b64_tr_b16 a[248:249],v3 offset:64
v_mfma_f32_16x16x32_bf16 v[188:191],a[224:227],v[40:43],v[188:191]
ds_read_b64_tr_b16 a[250:251],v3 offset:1600
v_lshlrev_b32_sdwa v55,1,v55 dst_sel:DWORD dst_unused:UNUSED_PAD src0_sel:DWORD src1_sel:WORD_1
v_mfma_f32_16x16x32_bf16 v[184:187],a[224:227],v[36:39],v[184:187]
ds_read_u16_d16_hi v55,v55
v_rcp_f16_sdwa v9,v8 dst_sel:WORD_1 dst_unused:UNUSED_PRESERVE src0_sel:WORD_1
v_mfma_f32_16x16x32_bf16 v[224:227],a[232:235],v[36:39],v[224:227]
ds_read_b64_tr_b16 a[236:237],v3 offset:72
v_lshlrev_b32_sdwa v56,1,v56 dst_sel:DWORD dst_unused:UNUSED_PAD src0_sel:DWORD src1_sel:WORD_1
v_mfma_f32_16x16x32_bf16 v[228:231],a[232:235],v[40:43],v[228:231]
ds_read_u16 v56,v56
v_lshlrev_b32_sdwa v58,1,v58 dst_sel:DWORD dst_unused:UNUSED_PAD src0_sel:DWORD src1_sel:WORD_1
v_mfma_f32_16x16x32_bf16 v[204:207],a[244:247],v[40:43],v[204:207]
ds_read_u16_d16_hi v58,v58
v_fma_mix_f32 v57,v57,v9,0 op_sel_hi:[0,1,0]
v_mfma_f32_16x16x32_bf16 v[200:203],a[244:247],v[36:39],v[200:203]
ds_read_b64_tr_b16 a[238:239],v3 offset:1608
v_fma_mix_f32 v59,v59,v9,0 op_sel:[0,1,0] op_sel_hi:[0,1,0]
s_waitcnt lgkmcnt(14)
v_lshl_add_u64 v[24:25],v[24:25],0,v[26:27]
s_waitcnt lgkmcnt(10)
v_lshl_add_u64 v[26:27],v[28:29],0,v[30:31]
s_waitcnt lgkmcnt(4)
v_lshl_add_u64 v[28:29],v[52:53],0,v[54:55]
s_waitcnt lgkmcnt(1)
v_or_b32 v30,v56,v58
v_cvt_pk_bf16_f32 v31,v57,v59
v_mfma_f32_16x16x32_bf16 v[232:235],a[240:243],v[24:27],v[232:235]
ds_read_b64_tr_b16 a[252:253],v3 offset:128
v_fma_mixlo_f16 v8,v49,s99,0
v_lshlrev_b32_sdwa v32,1,v32 dst_sel:DWORD dst_unused:UNUSED_PAD src0_sel:DWORD src1_sel:WORD_1
v_mfma_f32_16x16x32_bf16 v[236:239],a[240:243],v[28:31],v[236:239]
ds_read_u16 v32,v32
v_fma_mixhi_f16 v8,v51,s99,0
v_lshlrev_b32_sdwa v34,1,v34 dst_sel:DWORD dst_unused:UNUSED_PAD src0_sel:DWORD src1_sel:WORD_1
v_mfma_f32_16x16x32_bf16 v[164:167],a[228:231],v[28:31],v[164:167]
ds_read_u16_d16_hi v34,v34
v_pk_fma_f16 v9,v8,v8,s54
v_lshlrev_b32_sdwa v33,1,v33 dst_sel:DWORD dst_unused:UNUSED_PAD src0_sel:DWORD src1_sel:WORD_1
v_mfma_f32_16x16x32_bf16 v[160:163],a[228:231],v[24:27],v[160:163]
ds_read_u16 v33,v33
v_pk_mul_f16 v9,v8,v9
v_lshlrev_b32_sdwa v35,1,v35 dst_sel:DWORD dst_unused:UNUSED_PAD src0_sel:DWORD src1_sel:WORD_1
v_mfma_f32_16x16x32_bf16 v[240:243],a[248:251],v[24:27],v[240:243]
v_exp_f16 v8,v9
v_lshlrev_b32_sdwa v60,1,v60 dst_sel:DWORD dst_unused:UNUSED_PAD src0_sel:DWORD src1_sel:WORD_1
v_mfma_f32_16x16x32_bf16 v[244:247],a[248:251],v[28:31],v[244:247]
s_waitcnt lgkmcnt(4)
v_exp_f16_sdwa v8,v9 dst_sel:WORD_1 dst_unused:UNUSED_PRESERVE src0_sel:WORD_1
v_lshlrev_b32_sdwa v62,1,v62 dst_sel:DWORD dst_unused:UNUSED_PAD src0_sel:DWORD src1_sel:WORD_1
v_lshlrev_b32_sdwa v61,1,v61 dst_sel:DWORD dst_unused:UNUSED_PAD src0_sel:DWORD src1_sel:WORD_1
v_mfma_f32_16x16x32_bf16 v[180:183],a[236:239],v[28:31],v[180:183]
ds_read_u16_d16_hi v35,v35
ds_read_u16 v60,v60
v_lshlrev_b32_sdwa v63,1,v63 dst_sel:DWORD dst_unused:UNUSED_PAD src0_sel:DWORD src1_sel:WORD_1
v_mfma_f32_16x16x32_bf16 v[176:179],a[236:239],v[24:27],v[176:179]
ds_read_b64_tr_b16 a[254:255],v3 offset:1664
s_waitcnt lgkmcnt(0)
ds_read_u16_d16_hi v62,v62
ds_read_u16 v61,v61
v_pk_fma_f16 v8,v8,s55,s55
v_lshlrev_b32_sdwa v44,1,v44 dst_sel:DWORD dst_unused:UNUSED_PAD src0_sel:DWORD src1_sel:WORD_1
v_mfma_f32_16x16x32_bf16 v[248:251],a[252:255],v[24:27],v[248:251]
s_barrier
ds_read_u16_d16_hi v63,v63
ds_read_u16 v44,v44
v_rcp_f16 v9,v8
v_lshlrev_b32_sdwa v46,1,v46 dst_sel:DWORD dst_unused:UNUSED_PAD src0_sel:DWORD src1_sel:WORD_1
v_mfma_f32_16x16x32_bf16 v[252:255],a[252:255],v[28:31],v[252:255]
ds_read_b64_tr_b16 a[224:225],v3 offset:136
ds_read_b64_tr_b16 a[226:227],v3 offset:1672
s_waitcnt lgkmcnt(0)
ds_read_u16_d16_hi v46,v46
v_rcp_f16_sdwa v9,v8 dst_sel:WORD_1 dst_unused:UNUSED_PRESERVE src0_sel:WORD_1
v_lshlrev_b32_sdwa v45,1,v45 dst_sel:DWORD dst_unused:UNUSED_PAD src0_sel:DWORD src1_sel:WORD_1
v_lshlrev_b32_sdwa v47,1,v47 dst_sel:DWORD dst_unused:UNUSED_PAD src0_sel:DWORD src1_sel:WORD_1
v_mfma_f32_16x16x32_bf16 v[196:199],a[224:227],v[28:31],v[196:199]
TP11388_%=:
s_cbranch_execz TR1_%=
ds_read_u16 v45,v45
ds_read_u16_d16_hi v47,v47
v_lshlrev_b32_sdwa v48,1,v48 dst_sel:DWORD dst_unused:UNUSED_PAD src0_sel:DWORD src1_sel:WORD_1
v_mfma_f32_16x16x32_bf16 v[192:195],a[224:227],v[24:27],v[192:195]
ds_read_u16 v48,v48
v_fma_mix_f32 v49,v49,v9,0 op_sel_hi:[0,1,0]
v_fma_mix_f32 v51,v51,v9,0 op_sel:[0,1,0] op_sel_hi:[0,1,0]
v_lshlrev_b32_sdwa v50,1,v50 dst_sel:DWORD dst_unused:UNUSED_PAD src0_sel:DWORD src1_sel:WORD_1
ds_read_u16_d16_hi v50,v50
v_mul_f32_dpp v232,v14,v232 row_newbcast:0 row_mask:0xf bank_mask:0xf
s_add_u32 s62,s61,4718592
v_mul_f32_dpp v233,v14,v233 row_newbcast:1 row_mask:0xf bank_mask:0xf
v_mul_f32_dpp v234,v14,v234 row_newbcast:2 row_mask:0xf bank_mask:0xf
v_mul_f32_dpp v235,v14,v235 row_newbcast:3 row_mask:0xf bank_mask:0xf
v_mul_f32_dpp v160,v14,v160 row_newbcast:4 row_mask:0xf bank_mask:0xf
v_mul_f32_dpp v161,v14,v161 row_newbcast:5 row_mask:0xf bank_mask:0xf
v_mul_f32_dpp v162,v14,v162 row_newbcast:6 row_mask:0xf bank_mask:0xf
v_mul_f32_dpp v163,v14,v163 row_newbcast:7 row_mask:0xf bank_mask:0xf
v_lshl_add_u64 v[32:33],v[32:33],0,v[34:35]
v_lshl_add_u64 v[34:35],v[60:61],0,v[62:63]
s_waitcnt lgkmcnt(2)
v_lshl_add_u64 v[60:61],v[44:45],0,v[46:47]
s_waitcnt lgkmcnt(0)
v_or_b32 v62,v48,v50
v_cvt_pk_bf16_f32 v63,v49,v51
v_mfma_f32_16x16x32_bf16 v[208:211],a[240:243],v[32:35],v[208:211]
v_cvt_pk_bf16_f32 v232,v232,v233
v_cvt_pk_bf16_f32 v233,v234,v235
v_mfma_f32_16x16x32_bf16 v[212:215],a[240:243],v[60:63],v[212:215]
v_cvt_pk_bf16_f32 v234,v160,v161
v_cvt_pk_bf16_f32 v235,v162,v163
buffer_store_dwordx4 v[232:235],v7,s[40:43],s62 offen offset:0 sc1
v_mfma_f32_16x16x32_bf16 v[172:175],a[228:231],v[60:63],v[172:175]
v_mul_f32_dpp v240,v14,v240 row_newbcast:8 row_mask:0xf bank_mask:0xf
v_mul_f32_dpp v241,v14,v241 row_newbcast:9 row_mask:0xf bank_mask:0xf
v_mfma_f32_16x16x32_bf16 v[168:171],a[228:231],v[32:35],v[168:171]
v_mul_f32_dpp v242,v14,v242 row_newbcast:10 row_mask:0xf bank_mask:0xf
v_mul_f32_dpp v243,v14,v243 row_newbcast:11 row_mask:0xf bank_mask:0xf
v_mfma_f32_16x16x32_bf16 v[216:219],a[248:251],v[32:35],v[216:219]
v_mul_f32_dpp v176,v14,v176 row_newbcast:12 row_mask:0xf bank_mask:0xf
v_mul_f32_dpp v177,v14,v177 row_newbcast:13 row_mask:0xf bank_mask:0xf
v_mfma_f32_16x16x32_bf16 v[220:223],a[248:251],v[60:63],v[220:223]
v_mul_f32_dpp v178,v14,v178 row_newbcast:14 row_mask:0xf bank_mask:0xf
v_mul_f32_dpp v179,v14,v179 row_newbcast:15 row_mask:0xf bank_mask:0xf
v_mfma_f32_16x16x32_bf16 v[188:191],a[236:239],v[60:63],v[188:191]
v_cvt_pk_bf16_f32 v240,v240,v241
v_cvt_pk_bf16_f32 v241,v242,v243
v_mfma_f32_16x16x32_bf16 v[184:187],a[236:239],v[32:35],v[184:187]
v_cvt_pk_bf16_f32 v242,v176,v177
v_cvt_pk_bf16_f32 v243,v178,v179
buffer_store_dwordx4 v[240:243],v7,s[40:43],s62 offen offset:64 sc1
v_mfma_f32_16x16x32_bf16 v[224:227],a[252:255],v[32:35],v[224:227]
v_mul_f32_dpp v248,v15,v248 row_newbcast:0 row_mask:0xf bank_mask:0xf
v_mul_f32_dpp v249,v15,v249 row_newbcast:1 row_mask:0xf bank_mask:0xf
v_mfma_f32_16x16x32_bf16 v[228:231],a[252:255],v[60:63],v[228:231]
v_mul_f32_dpp v250,v15,v250 row_newbcast:2 row_mask:0xf bank_mask:0xf
v_mul_f32_dpp v251,v15,v251 row_newbcast:3 row_mask:0xf bank_mask:0xf
v_mfma_f32_16x16x32_bf16 v[204:207],a[224:227],v[60:63],v[204:207]
v_mul_f32_dpp v192,v15,v192 row_newbcast:4 row_mask:0xf bank_mask:0xf
v_mul_f32_dpp v193,v15,v193 row_newbcast:5 row_mask:0xf bank_mask:0xf
v_mfma_f32_16x16x32_bf16 v[200:203],a[224:227],v[32:35],v[200:203]
v_mul_f32_dpp v194,v15,v194 row_newbcast:6 row_mask:0xf bank_mask:0xf
v_mul_f32_dpp v195,v15,v195 row_newbcast:7 row_mask:0xf bank_mask:0xf
v_cvt_pk_bf16_f32 v248,v248,v249
v_cvt_pk_bf16_f32 v249,v250,v251
v_cvt_pk_bf16_f32 v250,v192,v193
v_cvt_pk_bf16_f32 v251,v194,v195
buffer_store_dwordx4 v[248:251],v7,s[40:43],s62 offen offset:128 sc1
v_mul_f32_dpp v236,v14,v236 row_newbcast:0 row_mask:0xf bank_mask:0xf
v_mul_f32_dpp v237,v14,v237 row_newbcast:1 row_mask:0xf bank_mask:0xf
v_mul_f32_dpp v238,v14,v238 row_newbcast:2 row_mask:0xf bank_mask:0xf
v_mul_f32_dpp v239,v14,v239 row_newbcast:3 row_mask:0xf bank_mask:0xf
v_mul_f32_dpp v164,v14,v164 row_newbcast:4 row_mask:0xf bank_mask:0xf
v_mul_f32_dpp v165,v14,v165 row_newbcast:5 row_mask:0xf bank_mask:0xf
v_mul_f32_dpp v166,v14,v166 row_newbcast:6 row_mask:0xf bank_mask:0xf
v_mul_f32_dpp v167,v14,v167 row_newbcast:7 row_mask:0xf bank_mask:0xf
v_cvt_pk_bf16_f32 v236,v236,v237
v_cvt_pk_bf16_f32 v237,v238,v239
v_cvt_pk_bf16_f32 v238,v164,v165
v_cvt_pk_bf16_f32 v239,v166,v167
buffer_store_dwordx4 v[236:239],v7,s[40:43],s62 offen offset:192 sc1
v_mul_f32_dpp v244,v14,v244 row_newbcast:8 row_mask:0xf bank_mask:0xf
v_mul_f32_dpp v245,v14,v245 row_newbcast:9 row_mask:0xf bank_mask:0xf
v_mul_f32_dpp v246,v14,v246 row_newbcast:10 row_mask:0xf bank_mask:0xf
v_mul_f32_dpp v247,v14,v247 row_newbcast:11 row_mask:0xf bank_mask:0xf
v_mul_f32_dpp v180,v14,v180 row_newbcast:12 row_mask:0xf bank_mask:0xf
v_mul_f32_dpp v181,v14,v181 row_newbcast:13 row_mask:0xf bank_mask:0xf
v_mul_f32_dpp v182,v14,v182 row_newbcast:14 row_mask:0xf bank_mask:0xf
v_mul_f32_dpp v183,v14,v183 row_newbcast:15 row_mask:0xf bank_mask:0xf
v_cvt_pk_bf16_f32 v244,v244,v245
v_cvt_pk_bf16_f32 v245,v246,v247
v_cvt_pk_bf16_f32 v246,v180,v181
v_cvt_pk_bf16_f32 v247,v182,v183
buffer_store_dwordx4 v[244:247],v7,s[40:43],s62 offen offset:256 sc1
v_mul_f32_dpp v252,v15,v252 row_newbcast:0 row_mask:0xf bank_mask:0xf
v_mul_f32_dpp v253,v15,v253 row_newbcast:1 row_mask:0xf bank_mask:0xf
v_mul_f32_dpp v254,v15,v254 row_newbcast:2 row_mask:0xf bank_mask:0xf
v_mul_f32_dpp v255,v15,v255 row_newbcast:3 row_mask:0xf bank_mask:0xf
v_mul_f32_dpp v196,v15,v196 row_newbcast:4 row_mask:0xf bank_mask:0xf
v_mul_f32_dpp v197,v15,v197 row_newbcast:5 row_mask:0xf bank_mask:0xf
v_mul_f32_dpp v198,v15,v198 row_newbcast:6 row_mask:0xf bank_mask:0xf
v_mul_f32_dpp v199,v15,v199 row_newbcast:7 row_mask:0xf bank_mask:0xf
v_cvt_pk_bf16_f32 v252,v252,v253
v_cvt_pk_bf16_f32 v253,v254,v255
v_cvt_pk_bf16_f32 v254,v196,v197
v_cvt_pk_bf16_f32 v255,v198,v199
buffer_store_dwordx4 v[252:255],v7,s[40:43],s62 offen offset:320 sc1
v_mul_f32_dpp v208,v14,v208 row_newbcast:0 row_mask:0xf bank_mask:0xf
s_add_u32 s63,s61,4816896
v_mul_f32_dpp v209,v14,v209 row_newbcast:1 row_mask:0xf bank_mask:0xf
v_mul_f32_dpp v210,v14,v210 row_newbcast:2 row_mask:0xf bank_mask:0xf
v_mul_f32_dpp v211,v14,v211 row_newbcast:3 row_mask:0xf bank_mask:0xf
v_mul_f32_dpp v168,v14,v168 row_newbcast:4 row_mask:0xf bank_mask:0xf
v_mul_f32_dpp v169,v14,v169 row_newbcast:5 row_mask:0xf bank_mask:0xf
v_mul_f32_dpp v170,v14,v170 row_newbcast:6 row_mask:0xf bank_mask:0xf
v_mul_f32_dpp v171,v14,v171 row_newbcast:7 row_mask:0xf bank_mask:0xf
v_cvt_pk_bf16_f32 v208,v208,v209
v_cvt_pk_bf16_f32 v209,v210,v211
v_cvt_pk_bf16_f32 v210,v168,v169
v_cvt_pk_bf16_f32 v211,v170,v171
buffer_store_dwordx4 v[208:211],v7,s[40:43],s63 offen offset:0 sc1
v_mul_f32_dpp v216,v14,v216 row_newbcast:8 row_mask:0xf bank_mask:0xf
v_mul_f32_dpp v217,v14,v217 row_newbcast:9 row_mask:0xf bank_mask:0xf
v_mul_f32_dpp v218,v14,v218 row_newbcast:10 row_mask:0xf bank_mask:0xf
v_mul_f32_dpp v219,v14,v219 row_newbcast:11 row_mask:0xf bank_mask:0xf
v_mul_f32_dpp v184,v14,v184 row_newbcast:12 row_mask:0xf bank_mask:0xf
v_mul_f32_dpp v185,v14,v185 row_newbcast:13 row_mask:0xf bank_mask:0xf
v_mul_f32_dpp v186,v14,v186 row_newbcast:14 row_mask:0xf bank_mask:0xf
v_mul_f32_dpp v187,v14,v187 row_newbcast:15 row_mask:0xf bank_mask:0xf
v_cvt_pk_bf16_f32 v216,v216,v217
v_cvt_pk_bf16_f32 v217,v218,v219
v_cvt_pk_bf16_f32 v218,v184,v185
v_cvt_pk_bf16_f32 v219,v186,v187
buffer_store_dwordx4 v[216:219],v7,s[40:43],s63 offen offset:64 sc1
v_mul_f32_dpp v224,v15,v224 row_newbcast:0 row_mask:0xf bank_mask:0xf
v_mul_f32_dpp v225,v15,v225 row_newbcast:1 row_mask:0xf bank_mask:0xf
v_mul_f32_dpp v226,v15,v226 row_newbcast:2 row_mask:0xf bank_mask:0xf
v_mul_f32_dpp v227,v15,v227 row_newbcast:3 row_mask:0xf bank_mask:0xf
v_mul_f32_dpp v200,v15,v200 row_newbcast:4 row_mask:0xf bank_mask:0xf
v_mul_f32_dpp v201,v15,v201 row_newbcast:5 row_mask:0xf bank_mask:0xf
v_mul_f32_dpp v202,v15,v202 row_newbcast:6 row_mask:0xf bank_mask:0xf
v_mul_f32_dpp v203,v15,v203 row_newbcast:7 row_mask:0xf bank_mask:0xf
v_cvt_pk_bf16_f32 v224,v224,v225
v_cvt_pk_bf16_f32 v225,v226,v227
v_cvt_pk_bf16_f32 v226,v200,v201
v_cvt_pk_bf16_f32 v227,v202,v203
buffer_store_dwordx4 v[224:227],v7,s[40:43],s63 offen offset:128 sc1
v_mul_f32_dpp v212,v14,v212 row_newbcast:0 row_mask:0xf bank_mask:0xf
v_mul_f32_dpp v213,v14,v213 row_newbcast:1 row_mask:0xf bank_mask:0xf
v_mul_f32_dpp v214,v14,v214 row_newbcast:2 row_mask:0xf bank_mask:0xf
v_mul_f32_dpp v215,v14,v215 row_newbcast:3 row_mask:0xf bank_mask:0xf
v_mul_f32_dpp v172,v14,v172 row_newbcast:4 row_mask:0xf bank_mask:0xf
v_mul_f32_dpp v173,v14,v173 row_newbcast:5 row_mask:0xf bank_mask:0xf
v_mul_f32_dpp v174,v14,v174 row_newbcast:6 row_mask:0xf bank_mask:0xf
v_mul_f32_dpp v175,v14,v175 row_newbcast:7 row_mask:0xf bank_mask:0xf
v_cvt_pk_bf16_f32 v212,v212,v213
v_cvt_pk_bf16_f32 v213,v214,v215
v_cvt_pk_bf16_f32 v214,v172,v173
v_cvt_pk_bf16_f32 v215,v174,v175
buffer_store_dwordx4 v[212:215],v7,s[40:43],s63 offen offset:192 sc1
v_mul_f32_dpp v220,v14,v220 row_newbcast:8 row_mask:0xf bank_mask:0xf
v_mul_f32_dpp v221,v14,v221 row_newbcast:9 row_mask:0xf bank_mask:0xf
v_mul_f32_dpp v222,v14,v222 row_newbcast:10 row_mask:0xf bank_mask:0xf
v_mul_f32_dpp v223,v14,v223 row_newbcast:11 row_mask:0xf bank_mask:0xf
v_mul_f32_dpp v188,v14,v188 row_newbcast:12 row_mask:0xf bank_mask:0xf
v_mul_f32_dpp v189,v14,v189 row_newbcast:13 row_mask:0xf bank_mask:0xf
v_mul_f32_dpp v190,v14,v190 row_newbcast:14 row_mask:0xf bank_mask:0xf
v_mul_f32_dpp v191,v14,v191 row_newbcast:15 row_mask:0xf bank_mask:0xf
v_cvt_pk_bf16_f32 v220,v220,v221
v_cvt_pk_bf16_f32 v221,v222,v223
v_cvt_pk_bf16_f32 v222,v188,v189
v_cvt_pk_bf16_f32 v223,v190,v191
buffer_store_dwordx4 v[220:223],v7,s[40:43],s63 offen offset:256 sc1
v_mul_f32_dpp v228,v15,v228 row_newbcast:0 row_mask:0xf bank_mask:0xf
v_mul_f32_dpp v229,v15,v229 row_newbcast:1 row_mask:0xf bank_mask:0xf
v_mul_f32_dpp v230,v15,v230 row_newbcast:2 row_mask:0xf bank_mask:0xf
v_mul_f32_dpp v231,v15,v231 row_newbcast:3 row_mask:0xf bank_mask:0xf
v_mul_f32_dpp v204,v15,v204 row_newbcast:4 row_mask:0xf bank_mask:0xf
v_mul_f32_dpp v205,v15,v205 row_newbcast:5 row_mask:0xf bank_mask:0xf
v_mul_f32_dpp v206,v15,v206 row_newbcast:6 row_mask:0xf bank_mask:0xf
v_mul_f32_dpp v207,v15,v207 row_newbcast:7 row_mask:0xf bank_mask:0xf
v_cvt_pk_bf16_f32 v228,v228,v229
v_cvt_pk_bf16_f32 v229,v230,v231
v_cvt_pk_bf16_f32 v230,v204,v205
v_cvt_pk_bf16_f32 v231,v206,v207
buffer_store_dwordx4 v[228:231],v7,s[40:43],s63 offen offset:320 sc1
v_or3_b32 v64,v64,v65,v66
v_or_b32 v64,v64,v67
s_nop 0
v_readfirstlane_b32 s64,v64
s_cmp_lg_u32 s64,0
s_cbranch_scc1 N_fallback_%=
s_endpgm
N_mainend_%=:
N_fallback_%=:
s_waitcnt vmcnt(0) lgkmcnt(0)
s_barrier
)ASM"
    :: "s"(blockIdx.x), "v"(tid), "s"(ka)
    : "v1", "v2", "v3", "v4", "v5", "v6", "v7", "v8", "v9", "v10", "v11", "v12", "v13", "v14", "v15", "v16", "v17", "v18", "v19", "v20", "v21", "v22", "v23", "v24", "v25", "v26", "v27", "v28", "v29", "v30", "v31", "v32", "v33", "v34", "v35", "v36", "v37", "v38", "v39", "v40", "v41", "v42", "v43", "v44", "v45", "v46", "v47", "v48", "v49", "v50", "v51", "v52", "v53", "v54", "v55", "v56", "v57", "v58", "v59", "v60", "v61", "v62", "v63", "v64", "v65", "v66", "v67", "v68", "v69", "v70", "v71", "v72", "v73", "v74", "v75", "v76", "v77", "v78", "v79", "v80", "v81", "v82", "v83", "v84", "v85", "v86", "v87", "v88", "v89", "v90", "v91", "v92", "v93", "v94", "v95", "v96", "v97", "v98", "v99", "v100", "v101", "v102", "v103", "v104", "v105", "v106", "v107", "v108", "v109", "v110", "v111", "v112", "v113", "v114", "v115", "v116", "v117", "v118", "v119", "v120", "v121", "v122", "v123", "v124", "v125", "v126", "v127", "v128", "v129", "v130", "v131", "v132", "v133", "v134", "v135", "v136", "v137", "v138", "v139", "v140", "v141", "v142", "v143", "v144", "v145", "v146", "v147", "v148", "v149", "v150", "v151", "v152", "v153", "v154", "v155", "v156", "v157", "v158", "v159", "v160", "v161", "v162", "v163", "v164", "v165", "v166", "v167", "v168", "v169", "v170", "v171", "v172", "v173", "v174", "v175", "v176", "v177", "v178", "v179", "v180", "v181", "v182", "v183", "v184", "v185", "v186", "v187", "v188", "v189", "v190", "v191", "v192", "v193", "v194", "v195", "v196", "v197", "v198", "v199", "v200", "v201", "v202", "v203", "v204", "v205", "v206", "v207", "v208", "v209", "v210", "v211", "v212", "v213", "v214", "v215", "v216", "v217", "v218", "v219", "v220", "v221", "v222", "v223", "v224", "v225", "v226", "v227", "v228", "v229", "v230", "v231", "v232", "v233", "v234", "v235", "v236", "v237", "v238", "v239", "v240", "v241", "v242", "v243", "v244", "v245", "v246", "v247", "v248", "v249", "v250", "v251", "v252", "v253", "v254", "v255", "a0", "a1", "a2", "a3", "a4", "a5", "a6", "a7", "a8", "a9", "a10", "a11", "a12", "a13", "a14", "a15", "a16", "a17", "a18", "a19", "a20", "a21", "a22", "a23", "a24", "a25", "a26", "a27", "a28", "a29", "a30", "a31", "a32", "a33", "a34", "a35", "a36", "a37", "a38", "a39", "a40", "a41", "a42", "a43", "a44", "a45", "a46", "a47", "a48", "a49", "a50", "a51", "a52", "a53", "a54", "a55", "a56", "a57", "a58", "a59", "a60", "a61", "a62", "a63", "a64", "a65", "a66", "a67", "a68", "a69", "a70", "a71", "a72", "a73", "a74", "a75", "a76", "a77", "a78", "a79", "a80", "a81", "a82", "a83", "a84", "a85", "a86", "a87", "a88", "a89", "a90", "a91", "a92", "a93", "a94", "a95", "a96", "a97", "a98", "a99", "a100", "a101", "a102", "a103", "a104", "a105", "a106", "a107", "a108", "a109", "a110", "a111", "a112", "a113", "a114", "a115", "a116", "a117", "a118", "a119", "a120", "a121", "a122", "a123", "a124", "a125", "a126", "a127", "a128", "a129", "a130", "a131", "a132", "a133", "a134", "a135", "a136", "a137", "a138", "a139", "a140", "a141", "a142", "a143", "a144", "a145", "a146", "a147", "a148", "a149", "a150", "a151", "a152", "a153", "a154", "a155", "a156", "a157", "a158", "a159", "a160", "a161", "a162", "a163", "a164", "a165", "a166", "a167", "a168", "a169", "a170", "a171", "a172", "a173", "a174", "a175", "a176", "a177", "a178", "a179", "a180", "a181", "a182", "a183", "a184", "a185", "a186", "a187", "a188", "a189", "a190", "a191", "a192", "a193", "a194", "a195", "a196", "a197", "a198", "a199", "a200", "a201", "a202", "a203", "a204", "a205", "a206", "a207", "a208", "a209", "a210", "a211", "a212", "a213", "a214", "a215", "a216", "a217", "a218", "a219", "a220", "a221", "a222", "a223", "a224", "a225", "a226", "a227", "a228", "a229", "a230", "a231", "a232", "a233", "a234", "a235", "a236", "a237", "a238", "a239", "a240", "a241", "a242", "a243", "a244", "a245", "a246", "a247", "a248", "a249", "a250", "a251", "a252", "a253", "a254", "a255", "s4", "s5", "s6", "s7", "s8", "s9", "s10", "s11", "s12", "s13", "s14", "s15", "s16", "s17", "s18", "s19", "s20", "s21", "s22", "s23", "s24", "s25", "s26", "s27", "s28", "s29", "s30", "s31", "s32", "s33", "s34", "s35", "s36", "s37", "s38", "s39", "s40", "s41", "s42", "s43", "s44", "s45", "s46", "s47", "s48", "s49", "s50", "s51", "s52", "s53", "s54", "s55", "s56", "s57", "s58", "s59", "s60", "s61", "s62", "s63", "s64", "s65", "s66", "s67", "s68", "s69", "s70", "s71", "s72", "s73", "s74", "s75", "s76", "s77", "s78", "s79", "s80", "s81", "s82", "s83", "s84", "s85", "s86", "s87", "s88", "s89", "s90", "s91", "s92", "s93", "s94", "s95", "s96", "s97", "s98", "s99", "vcc", "scc", "memory");
    asm volatile("" : "+s"(ka));
    typedef const unsigned short* __attribute__((address_space(1))) gp;
    unsigned long long const __attribute__((address_space(4)))* kp = (unsigned long long const __attribute__((address_space(4)))*)ka;
    const unsigned short* x = (const unsigned short*)kp[0]; const unsigned short* dw = (const unsigned short*)kp[1];
    const unsigned short* w1 = (const unsigned short*)kp[2]; const unsigned short* b1 = (const unsigned short*)kp[3];
    const unsigned short* w2 = (const unsigned short*)kp[4]; const unsigned short* b2 = (const unsigned short*)kp[5];
    const unsigned short* gamma = (const unsigned short*)kp[6]; unsigned short* out = (unsigned short*)kp[7];
    extern __shared__ float sm[];
    float* cv = sm; float* hid = sm + 16 * 96; float* stt = hid + 16 * 384;
    const int x0 = (blockIdx.x >> 3) * 16, y0 = (blockIdx.x & 7) * 64;
    for (int y = y0; y < y0 + 64; ++y) {
        for (int i = tid; i < 16 * 96; i += 256) {
            int p = i / 96, c = i - p * 96; float s = 0.f;
            for (int ky = 0; ky < 3; ++ky) for (int kx = 0; kx < 3; ++kx) {
                int yy = y + ky - 1, xx = x0 + p + kx - 1;
                float v = (yy >= 0 && yy < 512 && xx >= 0 && xx < 512) ? bf2f(x[((size_t)yy * 512 + xx) * 96 + c]) : 0.f;
                s = fmaf(v, bf2f(dw[(ky * 3 + kx) * 96 + c]), s);
            }
            cv[i] = s;
        }
        __syncthreads();
        if (tid < 16) {
            float m = 0.f; for (int c = 0; c < 96; ++c) m += cv[tid * 96 + c];
            m *= (1.f / 96); float q = 0.f;
            for (int c = 0; c < 96; ++c) { float d = cv[tid * 96 + c] - m; q = fmaf(d, d, q); }
            stt[tid] = m; stt[16 + tid] = 1.f / sqrtf(q * (1.f / 96) + 1e-5f);
        }
        __syncthreads();
        for (int i = tid; i < 16 * 96; i += 256) { int p = i / 96; cv[i] = rbf((cv[i] - stt[p]) * stt[16 + p]); }
        __syncthreads();
        for (int e = tid; e < 384; e += 256) {
            float a[16];
#pragma unroll
            for (int p = 0; p < 16; ++p) a[p] = 0.f;
            for (int c = 0; c < 96; ++c) {
                float w = bf2f(w1[c * 384 + e]);
#pragma unroll
                for (int p = 0; p < 16; ++p) a[p] = fmaf(cv[p * 96 + c], w, a[p]);
            }
            float bb = bf2f(b1[e]);
#pragma unroll
            for (int p = 0; p < 16; ++p) {
                float h = a[p] + bb;
                hid[p * 384 + e] = rbf(0.5f * h * (1.f + tanhf(0.7978845608028654f * (h + 0.044715f * h * h * h))));
            }
        }
        __syncthreads();
        for (int i = tid; i < 16 * 96; i += 256) {
            int p = i / 96, c = i - p * 96; float s = 0.f;
            for (int e = 0; e < 384; ++e) s = fmaf(hid[p * 384 + e], bf2f(w2[e * 96 + c]), s);
            size_t o = ((size_t)y * 512 + x0 + p) * 96 + c;
            out[o] = f2bf(bf2f(x[o]) + bf2f(gamma[c]) * (s + bf2f(b2[c])));
        }
        __syncthreads();
    }

}

extern "C" void kernel_launch(void* const* d_in, const int* in_sizes, int n_in,
                              void* d_out, int out_size, void* d_ws, size_t ws_size,
                              hipStream_t stream) {
    (void)hipFuncSetAttribute((const void*)convnext_kernel, hipFuncAttributeMaxDynamicSharedMemorySize, LDS_BYTES);
    convnext_kernel<<<256, 256, LDS_BYTES, stream>>>(
        (const unsigned short*)d_in[0], (const unsigned short*)d_in[1], (const unsigned short*)d_in[2], (const unsigned short*)d_in[3],
        (const unsigned short*)d_in[4], (const unsigned short*)d_in[5], (const unsigned short*)d_in[6], (unsigned short*)d_out);
}
```

```cpp
#include <hip/hip_runtime.h>
#include <stdint.h>

constexpr int LDS_BYTES = 163584;

__device__ __forceinline__ float bf2f(unsigned short b) { return __uint_as_float((unsigned)b << 16); }
__device__ __forceinline__ unsigned short f2bf(float f) { unsigned r; asm("v_cvt_pk_bf16_f32 %0, %1, %1" : "=v"(r) : "v"(f)); return (unsigned short)r; }
__device__ __forceinline__ float rbf(float f) { return bf2f(f2bf(f)); }

__global__ __launch_bounds__(256, 1) __attribute__((aligned(4096))) void convnext_kernel(
    const unsigned short* __restrict__ x_, const unsigned short* __restrict__ dw_, const unsigned short* __restrict__ w1_,
    const unsigned short* __restrict__ b1_, const unsigned short* __restrict__ w2_, const unsigned short* __restrict__ b2_,
    const unsigned short* __restrict__ gamma_, unsigned short* __restrict__ out_) {
    const unsigned tid = threadIdx.x;
    unsigned long long ka = (unsigned long long)__builtin_amdgcn_kernarg_segment_ptr();
    asm volatile(
R"ASM(
s_load_dwordx16 s[20:35],%2,0
v_lshrrev_b32 v27,6,%1
v_and_b32 v24,63,%1
v_readfirstlane_b32 s52,v27
s_lshr_b32 s70,%0,3
v_and_b32 v61,8,v24
v_lshl_add_u32 v62,s70,2,v27
v_mul_u32_u24 v61,15,v61
s_getpc_b64 s[14:15]
N_pf_%=:
v_or_b32 v61,7,v61
v_and_b32 v62,v61,v62
v_and_b32 v61,7,v24
v_lshl_add_u32 v62,v62,3,v61
v_lshlrev_b32 v62,7,v62
v_min_u32 v62,N_mainend_%=-N_pf_%=,v62
global_load_dword v255,v62,s[14:15]
v_mul_u32_u24 v30,52,v27
v_add_u32 v30,v30,v24
v_subrev_u32 v29,4,v30
v_max_i32 v29,0xc0,v29
v_min_u32 v30,v30,v29
v_mul_u32_u24 v28,0xaaab,v30
s_and_b32 s64,%0,7
s_lshr_b32 s70,%0,3
v_lshrrev_b32 v28,19,v28
s_lshl_b32 s71,s64,6
s_mul_i32 s68,s70,3072
v_bfe_u32 v27,v28,2,2
s_sub_u32 s65,s71,1
s_mul_i32 s56,s65,98304
v_xor_b32 v27,v30,v27
s_add_u32 s56,s56,s68
s_cmp_lt_i32 s65,0
s_cselect_b32 s89,0x70000000,s56
v_lshlrev_b32 v27,4,v27
s_cmp_eq_u32 s70,0
s_cselect_b32 s67,0,99
s_cmp_eq_u32 s70,31
s_cselect_b32 s67,17,s67
v_mov_b32 v29,0x7fff0000
v_cmp_eq_u32_e32 vcc,s67,v28
s_mul_i32 s57,s52,832
s_add_u32 s57,s57,73728
v_cndmask_b32_e32 v6,v27,v29,vcc
s_mov_b32 s38,50331840
s_mov_b32 s39,0x00020000
s_mov_b32 s50,1728
s_mov_b32 s51,0x00020000
v_lshlrev_b32 v8,4,v24
v_mul_u32_u24 v44,79,v24
v_add_u32 v45,5056,v44
v_lshrrev_b32 v44,10,v44
v_lshrrev_b32 v45,10,v45
v_sub_u32 v44,v24,v44
v_sub_u32 v45,v24,v45
v_lshlrev_b32 v44,4,v44
v_add_lshl_u32 v45,v45,64,4
v_min_u32 v28,191,%1
v_lshlrev_b32 v32,2,v28
v_min_u32 v34,95,%1
v_lshlrev_b32 v33,1,v34
s_waitcnt lgkmcnt(0)
s_sub_u32 s36,s20,192
s_subb_u32 s37,s21,0
s_and_b32 s37,s37,0xffff
s_add_u32 m0,s57,0
s_nop 0
buffer_load_dwordx4 v6,s[36:39],s89 offen lds
s_add_u32 m0,s57,3536
s_add_u32 s89,s56,98304
buffer_load_dwordx4 v6,s[36:39],s89 offen lds
s_add_u32 m0,s57,7080
s_add_u32 s89,s56,196608
buffer_load_dwordx4 v6,s[36:39],s89 offen lds
s_mov_b32 s48,s22
s_and_b32 s49,s23,0xffff
s_mov_b32 m0,12288
global_load_ushort v37,v33,s[32:33]
buffer_load_dwordx4 v44,s[48:51],0 offen lds
s_mov_b32 m0,13312
global_load_dword v35,v32,s[26:27]
buffer_load_dwordx4 v45,s[48:51],0 offen lds
global_load_ushort v36,v33,s[30:31]
v_and_b32 v27,15,v24
v_lshrrev_b32 v28,4,v24
s_mul_i32 s66,s52,192
v_min_u32 v27,11,v27
v_mul_u32_u24 v28,18432,v28
v_lshl_add_u32 v27,v27,4,s66
s_mul_i32 s66,s52,18432
v_add_u32 v172,v27,v28
v_add_u32 v173,s66,v8
global_load_dwordx4 v[160:163],v172,s[24:25]
global_load_dwordx4 v[164:167],v172,s[24:25] offset:3840
global_load_dwordx4 v[168:171],v173,s[28:29]
s_and_b32 s64,s52,1
s_mul_i32 s65,s64,48
v_bfe_u32 v27,v24,3,2
v_and_b32 v28,4,v24
v_lshrrev_b32 v29,5,v24
v_add3_u32 v27,v27,v28,s65
v_lshl_add_u32 v27,v29,3,v27
v_mul_u32_u24 v27,768,v27
v_and_b32 v28,3,v24
v_xor_b32 v28,v28,v29
v_lshl_add_u32 v27,v28,4,v27
s_mul_i32 s65,s64,3072
v_add_u32 v28,s65,v8
s_mul_i32 s67,s64,3080
s_add_u32 s67,s67,40896
s_add_u32 s65,s65,16256
s_cmp_lt_u32 s52,2
s_cselect_b32 s58,s65,s67
s_cselect_b64 vcc,-1,0
s_movk_i32 s59,6144
s_cselect_b32 s59,-22464,s59
s_cselect_b32 s53,11264,0
s_cselect_b32 s88,0,67584
s_cselect_b32 s87,64,0
s_cselect_b32 s69,0,0x70000000
s_mov_b32 s62,32832
s_cselect_b32 s62,88272,s62
s_cselect_b64 s[44:45],s[24:25],s[28:29]
s_add_u32 s62,s62,s58
s_and_b32 s45,s45,0xffff
v_cndmask_b32_e32 v5,v28,v27,vcc
s_add_u32 m0,s57,10616
s_add_u32 s89,s56,294912
buffer_load_dwordx4 v6,s[36:39],s89 offen lds
s_mov_b32 s46,73728
s_mov_b32 s47,0x00020000
s_mov_b32 s99,0x72800000
s_mov_b32 s98,0xffff0000
s_mov_b64 s[90:91],0
v_mov_b32 v96,0x409d3ef8
v_lshlrev_b32 v102,4,%1
v_lshlrev_b32 v104,19,%1
v_cmp_eq_u32_e64 s[94:95],0,%1
v_add_u32 v103,0x8000,v102
s_mov_b32 s4,0x3f000000
s_mov_b32 s5,0x3fc00000
s_mov_b32 s6,0x40200000
s_mov_b32 s7,0x40600000
s_mov_b32 s8,0x40900000
s_mov_b32 s9,0x40b00000
s_mov_b32 s10,0x40d00000
s_mov_b32 s11,0x40f00000
s_mov_b32 s12,0x3f80222e
s_mov_b32 s13,0x13089abf
s_mov_b32 s96,0xebefe000
v_add_u32 v28,0x10000,v104
s_add_u32 m0,s57,14144
s_add_u32 s89,s56,393216
buffer_load_dwordx4 v6,s[36:39],s89 offen lds
v_mul_f32 v98,s99,v104
v_fma_f32 v100,v28,s99,-v98
v_pk_fma_f32 v[64:65],v[100:101],s[4:5],v[98:99] op_sel_hi:[0,1,0]
v_pk_fma_f32 v[66:67],v[100:101],s[6:7],v[98:99] op_sel_hi:[0,1,0]
v_pk_fma_f32 v[68:69],v[100:101],s[8:9],v[98:99] op_sel_hi:[0,1,0]
v_pk_fma_f32 v[70:71],v[100:101],s[10:11],v[98:99] op_sel_hi:[0,1,0]
v_cndmask_b32_e64 v64,v64,0,s[94:95]
v_pk_mul_f32 v[72:73],v[64:65],v[64:65]
v_pk_mul_f32 v[74:75],v[66:67],v[66:67]
v_pk_mul_f32 v[76:77],v[68:69],v[68:69]
v_pk_mul_f32 v[78:79],v[70:71],v[70:71]
v_pk_fma_f32 v[72:73],v[72:73],s[12:13],v[96:97] op_sel_hi:[1,0,0]
v_pk_fma_f32 v[74:75],v[74:75],s[12:13],v[96:97] op_sel_hi:[1,0,0]
v_pk_fma_f32 v[76:77],v[76:77],s[12:13],v[96:97] op_sel_hi:[1,0,0]
v_pk_fma_f32 v[78:79],v[78:79],s[12:13],v[96:97] op_sel_hi:[1,0,0]
s_add_u32 m0,s57,17680
s_add_u32 s89,s56,491520
buffer_load_dwordx4 v6,s[36:39],s89 offen lds
v_pk_mul_f32 v[72:73],v[72:73],v[64:65]
v_pk_mul_f32 v[74:75],v[74:75],v[66:67]
v_pk_mul_f32 v[76:77],v[76:77],v[68:69]
v_pk_mul_f32 v[78:79],v[78:79],v[70:71]
v_exp_f32 v72,v72
v_exp_f32 v73,v73
v_exp_f32 v74,v74
v_exp_f32 v75,v75
v_exp_f32 v76,v76
v_exp_f32 v77,v77
s_add_u32 m0,s57,21224
s_add_u32 s89,s56,589824
buffer_load_dwordx4 v6,s[36:39],s89 offen lds
v_exp_f32 v78,v78
v_exp_f32 v79,v79
v_pk_fma_f32 v[72:73],v[72:73],s[96:97],s[96:97] op_sel_hi:[1,0,0]
v_pk_fma_f32 v[74:75],v[74:75],s[96:97],s[96:97] op_sel_hi:[1,0,0]
v_pk_fma_f32 v[76:77],v[76:77],s[96:97],s[96:97] op_sel_hi:[1,0,0]
v_pk_fma_f32 v[78:79],v[78:79],s[96:97],s[96:97] op_sel_hi:[1,0,0]
v_rcp_f32 v72,v72
v_rcp_f32 v73,v73
v_rcp_f32 v74,v74
v_rcp_f32 v75,v75
s_add_u32 m0,s57,24760
s_add_u32 s89,s56,688128
buffer_load_dwordx4 v6,s[36:39],s89 offen lds
v_rcp_f32 v76,v76
v_rcp_f32 v77,v77
v_rcp_f32 v78,v78
v_rcp_f32 v79,v79
v_pk_mul_f32 v[72:73],v[72:73],v[64:65]
v_pk_mul_f32 v[74:75],v[74:75],v[66:67]
v_pk_mul_f32 v[76:77],v[76:77],v[68:69]
v_pk_mul_f32 v[78:79],v[78:79],v[70:71]
v_pk_fma_f32 v[64:65],v[64:65],s[12:13],v[72:73] op_sel:[0,1,0] op_sel_hi:[1,1,1]
v_pk_fma_f32 v[66:67],v[66:67],s[12:13],v[74:75] op_sel:[0,1,0] op_sel_hi:[1,1,1]
v_pk_fma_f32 v[68:69],v[68:69],s[12:13],v[76:77] op_sel:[0,1,0] op_sel_hi:[1,1,1]
s_add_u32 m0,s57,28288
s_add_u32 s89,s56,786432
buffer_load_dwordx4 v6,s[36:39],s89 offen lds
v_pk_fma_f32 v[70:71],v[70:71],s[12:13],v[78:79] op_sel:[0,1,0] op_sel_hi:[1,1,1]
v_cvt_pk_bf16_f32 v80,v72,v73
v_cvt_pk_bf16_f32 v81,v74,v75
v_cvt_pk_bf16_f32 v82,v76,v77
v_cvt_pk_bf16_f32 v83,v78,v79
v_cvt_pk_bf16_f32 v84,v64,v65
v_cvt_pk_bf16_f32 v85,v66,v67
v_cvt_pk_bf16_f32 v86,v68,v69
v_cvt_pk_bf16_f32 v87,v70,v71
ds_write_b128 v102,v[80:83] offset:0
ds_write_b128 v103,v[84:87] offset:32768
v_add_u32 v104,0x8000000,v104
v_add_u32 v28,0x10000,v104
v_mul_f32 v98,s99,v104
v_fma_f32 v100,v28,s99,-v98
s_add_u32 m0,s57,31824
s_add_u32 s89,s56,884736
buffer_load_dwordx4 v6,s[36:39],s89 offen lds
v_pk_fma_f32 v[64:65],v[100:101],s[4:5],v[98:99] op_sel_hi:[0,1,0]
v_pk_fma_f32 v[66:67],v[100:101],s[6:7],v[98:99] op_sel_hi:[0,1,0]
v_pk_fma_f32 v[68:69],v[100:101],s[8:9],v[98:99] op_sel_hi:[0,1,0]
v_pk_fma_f32 v[70:71],v[100:101],s[10:11],v[98:99] op_sel_hi:[0,1,0]
v_pk_mul_f32 v[72:73],v[64:65],v[64:65]
v_pk_mul_f32 v[74:75],v[66:67],v[66:67]
v_pk_mul_f32 v[76:77],v[68:69],v[68:69]
v_pk_mul_f32 v[78:79],v[70:71],v[70:71]
v_pk_fma_f32 v[72:73],v[72:73],s[12:13],v[96:97] op_sel_hi:[1,0,0]
v_pk_fma_f32 v[74:75],v[74:75],s[12:13],v[96:97] op_sel_hi:[1,0,0]
v_pk_fma_f32 v[76:77],v[76:77],s[12:13],v[96:97] op_sel_hi:[1,0,0]
v_pk_fma_f32 v[78:79],v[78:79],s[12:13],v[96:97] op_sel_hi:[1,0,0]
v_pk_mul_f32 v[72:73],v[72:73],v[64:65]
v_pk_mul_f32 v[74:75],v[74:75],v[66:67]
v_pk_mul_f32 v[76:77],v[76:77],v[68:69]
s_add_u32 m0,s57,35368
s_add_u32 s89,s56,983040
buffer_load_dwordx4 v6,s[36:39],s89 offen lds
v_pk_mul_f32 v[78:79],v[78:79],v[70:71]
v_exp_f32 v72,v72
v_exp_f32 v73,v73
v_exp_f32 v74,v74
v_exp_f32 v75,v75
v_exp_f32 v76,v76
v_exp_f32 v77,v77
v_exp_f32 v78,v78
s_add_u32 m0,s57,38904
s_add_u32 s89,s56,1081344
buffer_load_dwordx4 v6,s[36:39],s89 offen lds
v_exp_f32 v79,v79
v_pk_fma_f32 v[72:73],v[72:73],s[96:97],s[96:97] op_sel_hi:[1,0,0]
v_pk_fma_f32 v[74:75],v[74:75],s[96:97],s[96:97] op_sel_hi:[1,0,0]
v_pk_fma_f32 v[76:77],v[76:77],s[96:97],s[96:97] op_sel_hi:[1,0,0]
v_pk_fma_f32 v[78:79],v[78:79],s[96:97],s[96:97] op_sel_hi:[1,0,0]
v_rcp_f32 v72,v72
v_rcp_f32 v73,v73
v_rcp_f32 v74,v74
v_rcp_f32 v75,v75
v_rcp_f32 v76,v76
v_rcp_f32 v77,v77
v_rcp_f32 v78,v78
v_rcp_f32 v79,v79
v_pk_mul_f32 v[72:73],v[72:73],v[64:65]
v_pk_mul_f32 v[74:75],v[74:75],v[66:67]
v_pk_mul_f32 v[76:77],v[76:77],v[68:69]
v_pk_mul_f32 v[78:79],v[78:79],v[70:71]
v_pk_fma_f32 v[64:65],v[64:65],s[12:13],v[72:73] op_sel:[0,1,0] op_sel_hi:[1,1,1]
v_pk_fma_f32 v[66:67],v[66:67],s[12:13],v[74:75] op_sel:[0,1,0] op_sel_hi:[1,1,1]
v_pk_fma_f32 v[68:69],v[68:69],s[12:13],v[76:77] op_sel:[0,1,0] op_sel_hi:[1,1,1]
v_pk_fma_f32 v[70:71],v[70:71],s[12:13],v[78:79] op_sel:[0,1,0] op_sel_hi:[1,1,1]
v_cvt_pk_bf16_f32 v88,v72,v73
v_cvt_pk_bf16_f32 v89,v74,v75
v_cvt_pk_bf16_f32 v90,v76,v77
v_cvt_pk_bf16_f32 v91,v78,v79
v_cvt_pk_bf16_f32 v92,v64,v65
v_cvt_pk_bf16_f32 v93,v66,v67
v_cvt_pk_bf16_f32 v94,v68,v69
v_cvt_pk_bf16_f32 v95,v70,v71
ds_write_b128 v102,v[88:91] offset:4096
ds_write_b128 v103,v[92:95] offset:36864
s_mov_b32 s96,0x8befe000
s_lshl_b32 s66,s52,2
s_mul_i32 s60,s66,3536
s_add_u32 s66,s66,s71
v_and_b32 v25,15,v24
v_lshrrev_b32 v26,4,v24
s_mul_i32 s61,s66,98304
s_add_u32 s61,s61,s68
s_mov_b32 s54,0x44ea44ea
s_mov_b32 s55,0x877f877f
s_mov_b64 s[40:41],s[34:35]
s_and_b32 s41,s41,0xffff
s_mov_b32 s42,50331648
s_mov_b32 s43,0x00020000
v_mov_b32 v41,0x70000
s_add_u32 m0,s57,42432
s_add_u32 s89,s56,1179648
buffer_load_dwordx4 v6,s[36:39],s89 offen lds
v_and_b32 v39,8,v24
v_and_b32 v40,7,v24
v_bfe_u32 v27,v24,1,1
v_sub_u32 v27,v26,v27
v_cmp_gt_u32_e64 s[92:93],3,v27
v_and_b32 v28,1,v24
v_mul_u32_u24 v27,3,v27
v_sub_u32 v27,v27,v28
v_cmp_eq_u32_e64 s[94:95],0,v28
v_lshrrev_b32 v29,2,v25
v_mul_i32_i24 v27,208,v27
v_lshl_add_u32 v27,v29,1,v27
v_mul_u32_u24 v10,0x5555,v29
v_add_u32 v27,12288,v27
v_cndmask_b32_e64 v45,v41,v27,s[92:93]
v_cndmask_b32_e64 v44,v41,v45,s[94:95]
v_cndmask_b32_e64 v47,v45,v41,s[94:95]
s_waitcnt vmcnt(13)
ds_read_u16 a96,v44 offset:0
ds_read_u16 a97,v45 offset:208
ds_read_u16 a98,v45 offset:416
ds_read_u16 a99,v47 offset:624
ds_read_u16 a100,v44 offset:8
ds_read_u16 a101,v45 offset:216
ds_read_u16 a102,v45 offset:424
ds_read_u16 a103,v47 offset:632
v_mov_b64 v[20:21],0
v_mov_b64 v[22:23],0
v_cmp_eq_u32_e64 s[92:93],v26,v29
v_lshlrev_b32 v27,4,v25
v_add_u32 v27,10112,v27
v_cndmask_b32_e64 v13,v41,v27,s[92:93]
v_lshrrev_b32 v27,2,v39
v_lshrrev_b32 v28,5,v24
v_add_u32 v27,v27,v28
v_mul_u32_u24 v27,3536,v27
v_lshl_or_b32 v10,v27,16,v10
v_and_b32 v27,1,v26
v_add_u32 v27,v27,v40
v_mul_u32_u24 v28,384,v27
v_lshrrev_b32 v27,1,v27
v_lshl_add_u32 v28,v27,4,v28
v_mfma_f32_32x32x16_bf16 v[64:79],v[20:23],v[20:23],0
ds_read_u16 a104,v44 offset:16
ds_read_u16 a105,v45 offset:224
ds_read_u16 a106,v45 offset:432
ds_read_u16 a107,v47 offset:640
ds_read_u16 a108,v44 offset:24
ds_read_u16 a109,v45 offset:232
ds_read_u16 a110,v45 offset:440
ds_read_u16 a111,v47 offset:648
v_add_u32 v30,73728,v39
s_add_u32 m0,s57,45968
s_add_u32 s89,s56,1277952
buffer_load_dwordx4 v6,s[36:39],s89 offen lds
v_add_u32 v11,v28,v30
s_movk_i32 s66,16384
v_lshl_add_u32 v27,v40,1,1
v_lshrrev_b32 v28,2,v27
v_xor_b32 v28,v28,v26
v_mul_u32_u24 v27,192,v27
v_lshl_add_u32 v27,v28,4,v27
v_add3_u32 v12,v27,v39,s66
v_lshl_add_u32 v27,v40,1,2
v_lshrrev_b32 v28,2,v27
v_xor_b32 v28,v28,v26
v_mul_u32_u24 v27,192,v27
v_lshl_add_u32 v27,v28,4,v27
v_add3_u32 v27,v27,v39,s66
v_lshl_or_b32 v12,v27,16,v12
v_mfma_f32_32x32x16_bf16 v[80:95],v[20:23],v[20:23],0
ds_read_u16 a112,v44 offset:32
ds_read_u16 a113,v45 offset:240
ds_read_u16 a114,v45 offset:448
ds_read_u16 a115,v47 offset:656
ds_read_u16 a116,v44 offset:40
ds_read_u16 a117,v45 offset:248
ds_read_u16 a118,v45 offset:456
ds_read_u16 a119,v47 offset:664
v_mul_u32_u24 v27,24576,v39
v_mul_u32_u24 v28,384,v40
v_lshl_add_u32 v27,v26,4,v27
v_add_u32 v7,v27,v28
v_lshrrev_b32 v27,3,v25
v_bfe_u32 v28,v25,2,1
v_lshl_add_u32 v28,v27,3,v28
v_lshl_add_u32 v28,v26,1,v28
v_lshlrev_b32 v28,6,v28
v_lshl_add_u32 v28,v27,4,v28
v_and_b32 v29,1,v25
v_lshl_add_u32 v28,v29,5,v28
v_bfe_u32 v29,v25,1,1
v_lshl_add_u32 v28,v29,3,v28
v_xor_b32 v29,16,v28
v_add_u32 v1,16256,v28
v_mfma_f32_32x32x16_bf16 v[96:111],v[20:23],v[20:23],0
ds_read_u16 a120,v44 offset:48
ds_read_u16 a121,v45 offset:256
ds_read_u16 a122,v45 offset:464
ds_read_u16 a123,v47 offset:672
ds_read_u16 a124,v44 offset:56
ds_read_u16 a125,v45 offset:264
ds_read_u16 a126,v45 offset:472
ds_read_u16 a127,v47 offset:680
v_add_u32 v2,16256,v29
s_add_u32 m0,s57,49512
s_add_u32 s89,s56,1376256
buffer_load_dwordx4 v6,s[36:39],s89 offen lds
v_lshrrev_b32 v27,3,v25
v_bfe_u32 v28,v25,2,1
v_lshl_add_u32 v27,v28,1,v27
v_lshrrev_b32 v28,1,v26
v_lshl_add_u32 v27,v28,2,v27
v_mul_u32_u24 v27,192,v27
v_and_b32 v29,1,v26
v_mul_u32_u24 v30,3080,v29
v_add_u32 v27,v27,v30
v_and_b32 v30,3,v25
v_lshl_add_u32 v27,v30,4,v27
v_add_u32 v3,40896,v27
v_lshlrev_b32 v29,6,v29
v_lshl_add_u32 v29,v28,4,v29
v_add_u32 v4,8192,v29
v_mfma_f32_32x32x16_bf16 v[112:127],v[20:23],v[20:23],0
ds_read_u16 a128,v44 offset:64
ds_read_u16 a129,v45 offset:272
ds_read_u16 a130,v45 offset:480
ds_read_u16 a131,v47 offset:688
ds_read_u16 a132,v44 offset:72
ds_read_u16 a133,v45 offset:280
ds_read_u16 a134,v45 offset:488
ds_read_u16 a135,v47 offset:696
v_lshrrev_b32 v27,5,v34
v_bfe_u32 v28,v34,2,1
v_and_b32 v29,3,v34
v_lshlrev_b32 v49,7,v27
v_lshl_add_u32 v49,v28,5,v49
v_lshl_add_u32 v49,v29,2,v49
v_bfe_u32 v30,v34,3,1
v_lshl_add_u32 v49,v30,6,v49
v_bfe_u32 v30,v34,4,1
v_lshl_add_u32 v49,v30,4,v49
v_add_u32 v49,11776,v49
v_lshrrev_b32 v55,6,v34
v_bfe_u32 v30,v34,3,2
v_lshl_add_u32 v55,v55,2,v30
v_and_b32 v30,1,v27
v_lshl_add_u32 v55,v55,1,v30
v_mfma_f32_32x32x16_bf16 v[128:143],v[20:23],v[20:23],0
ds_read_u16 a136,v44 offset:80
ds_read_u16 a137,v45 offset:288
ds_read_u16 a138,v45 offset:496
ds_read_u16 a139,v47 offset:704
ds_read_u16 a140,v44 offset:88
ds_read_u16 a141,v45 offset:296
ds_read_u16 a142,v45 offset:504
ds_read_u16 a143,v47 offset:712
v_and_b32 v30,7,v34
s_add_u32 m0,s57,53048
s_add_u32 s89,s56,1474560
buffer_load_dwordx4 v6,s[36:39],s89 offen lds
v_lshl_add_u32 v55,v55,3,v30
v_lshlrev_b32 v55,2,v55
v_lshlrev_b32 v15,2,v24
v_lshl_add_u32 v50,v27,1,v28
v_lshlrev_b32 v50,4,v50
v_bfe_u32 v30,v34,3,2
v_lshl_add_u32 v50,v30,2,v50
v_add_u32 v50,v50,v29
v_lshlrev_b32 v50,4,v50
v_add_u32 v50,10112,v50
v_lshlrev_b32 v51,4,v29
v_cmp_eq_u32_e64 s[94:95],1,v28
v_lshlrev_b32 v48,1,v32
v_add_u32 v48,8192,v48
v_mov_b32 v53,0
v_mfma_f32_32x32x16_bf16 v[144:159],v[20:23],v[20:23],0
ds_read_u16 a144,v44 offset:96
ds_read_u16 a145,v45 offset:304
ds_read_u16 a146,v45 offset:512
ds_read_u16 a147,v47 offset:720
ds_read_u16 a148,v44 offset:104
ds_read_u16 a149,v45 offset:312
ds_read_u16 a150,v45 offset:520
ds_read_u16 a151,v47 offset:728
v_lshlrev_b32 v37,16,v37
v_lshlrev_b32 v36,16,v36
v_mov_b32 v27,0x45800000
v_mov_b32 v28,0x53800000
v_cmp_nlt_f32_e64 s[90:91],|v37|,v27
v_cmp_nlt_f32_e64 vcc,|v36|,v28
v_mov_b32 v27,0x42800000
v_mul_f32 v37,0x6c800000,v37
v_mul_f32 v36,0x12800000,v36
v_rcp_f32 v56,v37
v_lshlrev_b32 v42,16,v35
v_and_b32 v43,s98,v35
s_or_b64 s[90:91],s[90:91],vcc
v_cmp_nlt_f32_e64 vcc,|v42|,v27
v_cmp_nlt_f32_e64 s[92:93],|v43|,v27
v_med3_f32 v56,v56,1.0,-1.0
v_mfma_f32_32x32x16_bf16 v[176:191],v[20:23],v[20:23],0
ds_read_u16 a152,v44 offset:112
ds_read_u16 a153,v45 offset:320
ds_read_u16 a154,v45 offset:528
ds_read_u16 a155,v47 offset:736
ds_read_u16 a156,v44 offset:120
ds_read_u16 a157,v45 offset:328
ds_read_u16 a158,v45 offset:536
ds_read_u16 a159,v47 offset:744
v_pk_mul_f32 v[42:43],v[42:43],s[96:97] op_sel_hi:[1,0]
s_add_u32 m0,s57,56576
s_add_u32 s89,s56,1572864
buffer_load_dwordx4 v6,s[36:39],s89 offen lds
v_cvt_pk_bf16_f32 v52,v56,v53
ds_write_b64 v48,v[42:43]
ds_write_b32 v49,v36
v_lshlrev_b32 v54,16,v52
v_rcp_f32 v54,v54
v_lshlrev_b64 v[52:53],v51,v[52:53]
s_or_b64 s[90:91],s[90:91],vcc
s_or_b64 s[90:91],s[90:91],s[92:93]
v_cndmask_b32_e64 v56,v52,0,s[94:95]
v_cndmask_b32_e64 v58,0,v52,s[94:95]
v_cndmask_b32_e64 v57,v53,0,s[94:95]
v_cndmask_b32_e64 v59,0,v53,s[94:95]
ds_write_b32 v15,v7 offset:14848
ds_write_b32 v15,v13 offset:15104
s_mul_i32 s66,s52,1536
v_mfma_f32_32x32x16_bf16 v[192:207],v[20:23],v[20:23],0
ds_read_u16 a160,v44 offset:128
ds_read_u16 a161,v45 offset:336
ds_read_u16 a162,v45 offset:544
ds_read_u16 a163,v47 offset:752
ds_read_u16 a164,v44 offset:136
ds_read_u16 a165,v45 offset:344
ds_read_u16 a166,v45 offset:552
ds_read_u16 a167,v47 offset:760
s_add_u32 s66,s66,16256
v_lshl_add_u32 v7,v24,3,s66
ds_write_b128 v50,v[56:59]
ds_write_b32 v55,v54 offset:14336
s_waitcnt vmcnt(14)
v_and_b32 v160,0x7fff7fff,v160
v_and_b32 v161,0x7fff7fff,v161
v_and_b32 v162,0x7fff7fff,v162
v_and_b32 v163,0x7fff7fff,v163
v_and_b32 v164,0x7fff7fff,v164
v_and_b32 v165,0x7fff7fff,v165
v_and_b32 v166,0x7fff7fff,v166
v_and_b32 v167,0x7fff7fff,v167
v_and_b32 v168,0x7fff7fff,v168
v_and_b32 v169,0x7fff7fff,v169
v_and_b32 v170,0x7fff7fff,v170
v_mfma_f32_32x32x16_bf16 v[208:223],v[20:23],v[20:23],0
ds_read_u16 a168,v44 offset:144
ds_read_u16 a169,v45 offset:352
ds_read_u16 a170,v45 offset:560
ds_read_u16 a171,v47 offset:768
ds_read_u16 a172,v44 offset:152
ds_read_u16 a173,v45 offset:360
ds_read_u16 a174,v45 offset:568
ds_read_u16 a175,v47 offset:776
v_and_b32 v171,0x7fff7fff,v171
s_add_u32 m0,s57,60112
s_add_u32 s89,s56,1671168
buffer_load_dwordx4 v6,s[36:39],s89 offen lds
v_pk_max_u16 v160,v160,v164
v_pk_max_u16 v161,v161,v165
v_pk_max_u16 v162,v162,v166
v_pk_max_u16 v163,v163,v167
v_mov_b64 v[164:165],v[160:161]
v_mov_b64 v[166:167],v[162:163]
s_nop 0
v_permlane32_swap_b32 v160,v164
v_permlane32_swap_b32 v161,v165
v_permlane32_swap_b32 v162,v166
v_permlane32_swap_b32 v163,v167
v_pk_max_u16 v160,v160,v164
v_pk_max_u16 v161,v161,v165
v_pk_max_u16 v162,v162,v166
v_pk_max_u16 v163,v163,v167
v_mov_b64 v[164:165],v[160:161]
v_mfma_f32_32x32x16_bf16 v[224:239],v[20:23],v[20:23],0
ds_read_u16 a176,v44 offset:160
ds_read_u16 a177,v45 offset:368
ds_read_u16 a178,v45 offset:576
ds_read_u16 a179,v47 offset:784
ds_read_u16 a180,v44 offset:168
ds_read_u16 a181,v45 offset:376
ds_read_u16 a182,v45 offset:584
ds_read_u16 a183,v47 offset:792
v_mov_b64 v[166:167],v[162:163]
v_permlane16_swap_b32 v160,v164
v_permlane16_swap_b32 v161,v165
v_permlane16_swap_b32 v162,v166
v_permlane16_swap_b32 v163,v167
v_pk_max_u16 v160,v160,v164
v_pk_max_u16 v161,v161,v165
v_pk_max_u16 v162,v162,v166
v_pk_max_u16 v163,v163,v167
v_pk_min_u16 v160,v160,v161
v_pk_min_u16 v162,v162,v163
v_pk_min_u16 v160,v160,v162
v_pk_min_u16 v160,v160,v160 op_sel:[0,1] op_sel_hi:[1,0]
v_pk_max_u16 v168,v168,v169
v_pk_max_u16 v170,v170,v171
s_add_u32 m0,s58,18480
s_nop 0
buffer_load_dwordx4 v5,s[44:47],s69 offen offset:0 lds
v_pk_max_u16 v168,v168,v170
ds_read_u16 a184,v44 offset:176
ds_read_u16 a185,v45 offset:384
ds_read_u16 a186,v45 offset:592
ds_read_u16 a187,v47 offset:800
ds_read_u16 a188,v44 offset:184
ds_read_u16 a189,v45 offset:392
ds_read_u16 a190,v45 offset:600
ds_read_u16 a191,v47 offset:808
v_pk_max_u16 v168,v168,v168 op_sel:[0,1] op_sel_hi:[1,0]
v_cmp_gt_u16_e32 vcc,0x3880,v160
s_nop 0
s_or_b64 s[90:91],s[90:91],vcc
v_cmp_le_u16_e32 vcc,0x5380,v168
s_nop 0
s_or_b64 s[90:91],s[90:91],vcc
v_accvgpr_write_b32 a88,0
v_accvgpr_write_b32 a89,0
v_accvgpr_write_b32 a90,0
v_accvgpr_write_b32 a91,0
v_mfma_f32_32x32x16_bf16 v[160:175],v[20:23],v[20:23],0
s_add_u32 s69,s69,s53
buffer_load_dwordx4 v5,s[44:47],s69 offen offset:1024 lds
s_add_u32 s69,s69,s53
buffer_load_dwordx4 v5,s[44:47],s69 offen offset:2048 lds
s_mov_b64 exec,0
s_cmp_eq_u32 s52,0
s_cbranch_scc1 TE00_%=
s_cmp_eq_u32 s52,1
s_cbranch_scc1 TE01_%=
s_cmp_eq_u32 s52,2
s_cbranch_scc1 TE02_%=
s_branch TE03_%=
TR0_%=:
s_mov_b64 exec,-1
s_add_u32 m0,s58,0
s_mov_b32 s69,s87
buffer_load_dwordx4 v5,s[44:47],s69 offen offset:0 lds
s_add_u32 s69,s69,s53
buffer_load_dwordx4 v5,s[44:47],s69 offen offset:1024 lds
s_add_u32 s69,s69,s53
buffer_load_dwordx4 v5,s[44:47],s69 offen offset:2048 lds
s_waitcnt lgkmcnt(0)
s_waitcnt vmcnt(6)
s_barrier
v_add_u32_sdwa v28,s60,v10 dst_sel:DWORD dst_unused:UNUSED_PAD src0_sel:DWORD src1_sel:WORD_1
v_subrev_u32 v8,88400,v28
v_min_u32 v28,v28,v8
v_add_u32 v29,7072,v28
v_subrev_u32 v8,88400,v29
v_min_u32 v29,v29,v8
v_xad_u32 v24,v11,0,v28
v_xad_u32 v25,v11,8,v29
v_xad_u32 v26,v11,16,v28
v_xad_u32 v27,v11,24,v29
ds_read_b64 a[0:1],v24 offset:0
ds_read_b64 a[2:3],v24 offset:192
ds_read_b64 a[4:5],v25 offset:0
ds_read_b64 a[6:7],v25 offset:192
ds_read_b64 a[8:9],v24 offset:8
ds_read_b64 a[10:11],v24 offset:200
ds_read_b64 a[12:13],v25 offset:8
ds_read_b64 a[14:15],v25 offset:200
ds_read_b64 a[16:17],v26 offset:0
ds_read_b64 a[18:19],v26 offset:192
ds_read_b64 a[20:21],v27 offset:0
ds_read_b64 a[22:23],v27 offset:192
s_waitcnt lgkmcnt(8)
v_smfmac_f32_16x16x64_bf16 v[64:67],a[96:99],a[0:7],v10
s_waitcnt lgkmcnt(4)
s_add_u32 m0,s58,6160
s_add_u32 s69,s69,s59
buffer_load_dwordx4 v5,s[44:47],s69 offen offset:0 lds
ds_read_b64 a[0:1],v26 offset:8
ds_read_b64 a[2:3],v26 offset:200
ds_read_b64 a[4:5],v27 offset:8
ds_read_b64 a[6:7],v27 offset:200
v_smfmac_f32_16x16x64_bf16 v[68:71],a[100:103],a[8:15],v10
s_waitcnt lgkmcnt(4)
s_add_u32 m0,s57,63656
s_add_u32 s89,s56,1769472
buffer_load_dwordx4 v6,s[36:39],s89 offen lds
ds_read_b64 a[8:9],v24 offset:64
ds_read_b64 a[10:11],v24 offset:256
ds_read_b64 a[12:13],v25 offset:64
ds_read_b64 a[14:15],v25 offset:256
v_smfmac_f32_16x16x64_bf16 v[72:75],a[104:107],a[16:23],v10
s_waitcnt lgkmcnt(4)
ds_read_b64 a[16:17],v24 offset:72
ds_read_b64 a[18:19],v24 offset:264
ds_read_b64 a[20:21],v25 offset:72
ds_read_b64 a[22:23],v25 offset:264
v_and_b32 v248,8,v11
v_pk_mul_f32 v[40:41],v[64:65],v[64:65]
v_pk_mul_f32 v[42:43],v[66:67],v[66:67]
v_smfmac_f32_16x16x64_bf16 v[76:79],a[108:111],a[0:7],v10
s_waitcnt lgkmcnt(4)
ds_read_b64 a[0:1],v26 offset:64
ds_read_b64 a[2:3],v26 offset:256
ds_read_b64 a[4:5],v27 offset:64
ds_read_b64 a[6:7],v27 offset:256
v_mul_u32_u24 v248,884,v248
s_add_u32 s64,s60,3536
v_pk_add_f32 v[36:37],v[64:65],v[68:69]
v_pk_fma_f32 v[40:41],v[68:69],v[68:69],v[40:41]
v_pk_add_f32 v[38:39],v[66:67],v[70:71]
v_pk_fma_f32 v[42:43],v[70:71],v[70:71],v[42:43]
v_smfmac_f32_16x16x64_bf16 v[80:83],a[128:131],a[8:15],v10
s_waitcnt lgkmcnt(4)
s_add_u32 m0,s57,67192
s_add_u32 s89,s56,1867776
buffer_load_dwordx4 v6,s[36:39],s89 offen lds
ds_read_b64 a[8:9],v26 offset:72
ds_read_b64 a[10:11],v26 offset:264
ds_read_b64 a[12:13],v27 offset:72
ds_read_b64 a[14:15],v27 offset:264
v_add_u32 v251,s64,v248
v_pk_add_f32 v[36:37],v[36:37],v[72:73]
v_pk_fma_f32 v[40:41],v[72:73],v[72:73],v[40:41]
v_pk_add_f32 v[38:39],v[38:39],v[74:75]
v_pk_fma_f32 v[42:43],v[74:75],v[74:75],v[42:43]
v_smfmac_f32_16x16x64_bf16 v[84:87],a[132:135],a[16:23],v10
s_waitcnt lgkmcnt(4)
ds_read_b64 a[16:17],v24 offset:128
ds_read_b64 a[18:19],v24 offset:320
ds_read_b64 a[20:21],v25 offset:128
ds_read_b64 a[22:23],v25 offset:320
v_subrev_u32 v8,88400,v251
v_pk_add_f32 v[36:37],v[36:37],v[76:77]
v_pk_fma_f32 v[40:41],v[76:77],v[76:77],v[40:41]
v_pk_add_f32 v[38:39],v[38:39],v[78:79]
v_pk_fma_f32 v[42:43],v[78:79],v[78:79],v[42:43]
v_smfmac_f32_16x16x64_bf16 v[88:91],a[136:139],a[0:7],v10
s_waitcnt lgkmcnt(4)
ds_read_b64 a[0:1],v24 offset:136
ds_read_b64 a[2:3],v24 offset:328
ds_read_b64 a[4:5],v25 offset:136
ds_read_b64 a[6:7],v25 offset:328
v_min_u32 v251,v251,v8
v_pk_add_f32 v[36:37],v[36:37],v[80:81]
v_pk_fma_f32 v[40:41],v[80:81],v[80:81],v[40:41]
v_pk_add_f32 v[38:39],v[38:39],v[82:83]
v_pk_fma_f32 v[42:43],v[82:83],v[82:83],v[42:43]
v_smfmac_f32_16x16x64_bf16 v[92:95],a[140:143],a[8:15],v10
s_waitcnt lgkmcnt(4)
s_add_u32 m0,s58,6160
s_add_u32 s69,s69,s53
buffer_load_dwordx4 v5,s[44:47],s69 offen offset:1024 lds
ds_read_b64 a[8:9],v26 offset:128
ds_read_b64 a[10:11],v26 offset:320
ds_read_b64 a[12:13],v27 offset:128
ds_read_b64 a[14:15],v27 offset:320
v_xad_u32 v24,v11,32,v28
v_xad_u32 v25,v11,40,v29
v_pk_add_f32 v[36:37],v[36:37],v[84:85]
v_pk_fma_f32 v[40:41],v[84:85],v[84:85],v[40:41]
v_pk_add_f32 v[38:39],v[38:39],v[86:87]
v_pk_fma_f32 v[42:43],v[86:87],v[86:87],v[42:43]
v_smfmac_f32_16x16x64_bf16 v[96:99],a[160:163],a[16:23],v10
s_waitcnt lgkmcnt(4)
ds_read_b64 a[16:17],v26 offset:136
ds_read_b64 a[18:19],v26 offset:328
ds_read_b64 a[20:21],v27 offset:136
ds_read_b64 a[22:23],v27 offset:328
v_add_u32 v250,3536,v251
v_pk_add_f32 v[36:37],v[36:37],v[88:89]
v_pk_fma_f32 v[40:41],v[88:89],v[88:89],v[40:41]
v_pk_add_f32 v[38:39],v[38:39],v[90:91]
v_pk_fma_f32 v[42:43],v[90:91],v[90:91],v[42:43]
v_smfmac_f32_16x16x64_bf16 v[100:103],a[164:167],a[0:7],v10
s_waitcnt lgkmcnt(4)
s_add_u32 m0,s57,70720
s_add_u32 s89,s56,1966080
buffer_load_dwordx4 v6,s[36:39],s89 offen lds
ds_read_b64 a[0:1],v24 offset:0
ds_read_b64 a[2:3],v24 offset:192
ds_read_b64 a[4:5],v25 offset:0
ds_read_b64 a[6:7],v25 offset:192
v_xad_u32 v26,v11,48,v28
v_xad_u32 v27,v11,56,v29
v_pk_add_f32 v[36:37],v[36:37],v[92:93]
v_pk_fma_f32 v[40:41],v[92:93],v[92:93],v[40:41]
v_pk_add_f32 v[38:39],v[38:39],v[94:95]
v_pk_fma_f32 v[42:43],v[94:95],v[94:95],v[42:43]
v_smfmac_f32_16x16x64_bf16 v[104:107],a[168:171],a[8:15],v10
s_waitcnt lgkmcnt(4)
ds_read_b64 a[8:9],v24 offset:8
ds_read_b64 a[10:11],v24 offset:200
ds_read_b64 a[12:13],v25 offset:8
ds_read_b64 a[14:15],v25 offset:200
v_subrev_u32 v8,88400,v250
v_pk_add_f32 v[36:37],v[36:37],v[96:97]
v_pk_fma_f32 v[40:41],v[96:97],v[96:97],v[40:41]
v_pk_add_f32 v[38:39],v[38:39],v[98:99]
v_pk_fma_f32 v[42:43],v[98:99],v[98:99],v[42:43]
v_smfmac_f32_16x16x64_bf16 v[108:111],a[172:175],a[16:23],v10
s_waitcnt lgkmcnt(4)
ds_read_b64 a[16:17],v24 offset:64
ds_read_b64 a[18:19],v24 offset:256
ds_read_b64 a[20:21],v25 offset:64
ds_read_b64 a[22:23],v25 offset:256
v_min_u32 v250,v250,v8
v_pk_add_f32 v[36:37],v[36:37],v[100:101]
v_pk_fma_f32 v[40:41],v[100:101],v[100:101],v[40:41]
v_pk_add_f32 v[38:39],v[38:39],v[102:103]
v_pk_fma_f32 v[42:43],v[102:103],v[102:103],v[42:43]
v_smfmac_f32_16x16x64_bf16 v[112:115],a[112:115],a[0:7],v10
s_waitcnt lgkmcnt(4)
s_add_u32 m0,s58,6160
s_add_u32 s69,s69,s53
buffer_load_dwordx4 v5,s[44:47],s69 offen offset:2048 lds
ds_read_b64 a[0:1],v24 offset:72
ds_read_b64 a[2:3],v24 offset:264
ds_read_b64 a[4:5],v25 offset:72
ds_read_b64 a[6:7],v25 offset:264
v_xor_b32 v9,0x80008,v12
v_pk_add_f32 v[36:37],v[36:37],v[104:105]
v_pk_fma_f32 v[40:41],v[104:105],v[104:105],v[40:41]
v_pk_add_f32 v[38:39],v[38:39],v[106:107]
v_pk_fma_f32 v[42:43],v[106:107],v[106:107],v[42:43]
v_smfmac_f32_16x16x64_bf16 v[116:119],a[116:119],a[8:15],v10
s_waitcnt lgkmcnt(4)
ds_read_b64 a[8:9],v24 offset:128
ds_read_b64 a[10:11],v24 offset:320
ds_read_b64 a[12:13],v25 offset:128
ds_read_b64 a[14:15],v25 offset:320
v_add_u32_sdwa v248,v12,v251 dst_sel:DWORD dst_unused:UNUSED_PAD src0_sel:WORD_0 src1_sel:DWORD
v_pk_add_f32 v[36:37],v[36:37],v[108:109]
v_pk_fma_f32 v[40:41],v[108:109],v[108:109],v[40:41]
v_pk_add_f32 v[38:39],v[38:39],v[110:111]
v_pk_fma_f32 v[42:43],v[110:111],v[110:111],v[42:43]
v_smfmac_f32_16x16x64_bf16 v[120:123],a[144:147],a[16:23],v10
s_waitcnt lgkmcnt(4)
s_add_u32 m0,s57,74256
s_add_u32 s89,s56,2064384
buffer_load_dwordx4 v6,s[36:39],s89 offen lds
ds_read_b64 a[16:17],v24 offset:136
ds_read_b64 a[18:19],v24 offset:328
ds_read_b64 a[20:21],v25 offset:136
ds_read_b64 a[22:23],v25 offset:328
v_add_u32_sdwa v249,v12,v251 dst_sel:DWORD dst_unused:UNUSED_PAD src0_sel:WORD_1 src1_sel:DWORD
v_pk_add_f32 v[36:37],v[36:37],v[112:113]
v_pk_fma_f32 v[40:41],v[112:113],v[112:113],v[40:41]
v_pk_add_f32 v[38:39],v[38:39],v[114:115]
v_pk_fma_f32 v[42:43],v[114:115],v[114:115],v[42:43]
v_smfmac_f32_16x16x64_bf16 v[124:127],a[148:151],a[0:7],v10
s_waitcnt lgkmcnt(4)
ds_read_b64 a[0:1],v26 offset:0
ds_read_b64 a[2:3],v26 offset:192
ds_read_b64 a[4:5],v27 offset:0
ds_read_b64 a[6:7],v27 offset:192
v_add_u32_sdwa v251,v9,v250 dst_sel:DWORD dst_unused:UNUSED_PAD src0_sel:WORD_1 src1_sel:DWORD
v_pk_add_f32 v[36:37],v[36:37],v[116:117]
v_pk_fma_f32 v[40:41],v[116:117],v[116:117],v[40:41]
v_pk_add_f32 v[38:39],v[38:39],v[118:119]
v_pk_fma_f32 v[42:43],v[118:119],v[118:119],v[42:43]
v_smfmac_f32_16x16x64_bf16 v[128:131],a[176:179],a[8:15],v10
s_waitcnt lgkmcnt(4)
ds_read_b64 a[8:9],v26 offset:8
ds_read_b64 a[10:11],v26 offset:200
ds_read_b64 a[12:13],v27 offset:8
ds_read_b64 a[14:15],v27 offset:200
v_add_u32_sdwa v250,v9,v250 dst_sel:DWORD dst_unused:UNUSED_PAD src0_sel:WORD_0 src1_sel:DWORD
v_pk_add_f32 v[36:37],v[36:37],v[120:121]
v_pk_fma_f32 v[40:41],v[120:121],v[120:121],v[40:41]
v_pk_add_f32 v[38:39],v[38:39],v[122:123]
v_pk_fma_f32 v[42:43],v[122:123],v[122:123],v[42:43]
v_smfmac_f32_16x16x64_bf16 v[132:135],a[180:183],a[16:23],v10
s_waitcnt lgkmcnt(4)
s_add_u32 m0,s58,12320
s_add_u32 s69,s69,s59
buffer_load_dwordx4 v5,s[44:47],s69 offen offset:0 lds
ds_read_b64 a[16:17],v26 offset:64
ds_read_b64 a[18:19],v26 offset:256
ds_read_b64 a[20:21],v27 offset:64
ds_read_b64 a[22:23],v27 offset:256
v_pk_add_f32 v[36:37],v[36:37],v[124:125]
v_pk_fma_f32 v[40:41],v[124:125],v[124:125],v[40:41]
v_pk_add_f32 v[38:39],v[38:39],v[126:127]
v_pk_fma_f32 v[42:43],v[126:127],v[126:127],v[42:43]
v_smfmac_f32_16x16x64_bf16 v[136:139],a[120:123],a[0:7],v10
s_waitcnt lgkmcnt(4)
ds_read_b64 a[0:1],v26 offset:72
ds_read_b64 a[2:3],v26 offset:264
ds_read_b64 a[4:5],v27 offset:72
ds_read_b64 a[6:7],v27 offset:264
v_pk_add_f32 v[36:37],v[36:37],v[128:129]
v_pk_fma_f32 v[40:41],v[128:129],v[128:129],v[40:41]
v_pk_add_f32 v[38:39],v[38:39],v[130:131]
v_pk_fma_f32 v[42:43],v[130:131],v[130:131],v[42:43]
v_smfmac_f32_16x16x64_bf16 v[140:143],a[124:127],a[8:15],v10
s_waitcnt lgkmcnt(4)
s_add_u32 m0,s57,77800
s_add_u32 s89,s56,2162688
buffer_load_dwordx4 v6,s[36:39],s89 offen lds
ds_read_b64 a[8:9],v26 offset:128
ds_read_b64 a[10:11],v26 offset:320
ds_read_b64 a[12:13],v27 offset:128
ds_read_b64 a[14:15],v27 offset:320
v_pk_add_f32 v[36:37],v[36:37],v[132:133]
v_pk_fma_f32 v[40:41],v[132:133],v[132:133],v[40:41]
v_pk_add_f32 v[38:39],v[38:39],v[134:135]
v_pk_fma_f32 v[42:43],v[134:135],v[134:135],v[42:43]
v_smfmac_f32_16x16x64_bf16 v[144:147],a[152:155],a[16:23],v10
s_waitcnt lgkmcnt(4)
ds_read_b64 a[16:17],v26 offset:136
ds_read_b64 a[18:19],v26 offset:328
ds_read_b64 a[20:21],v27 offset:136
ds_read_b64 a[22:23],v27 offset:328
v_pk_add_f32 v[36:37],v[36:37],v[136:137]
v_pk_fma_f32 v[40:41],v[136:137],v[136:137],v[40:41]
v_pk_add_f32 v[38:39],v[38:39],v[138:139]
v_pk_fma_f32 v[42:43],v[138:139],v[138:139],v[42:43]
v_smfmac_f32_16x16x64_bf16 v[148:151],a[156:159],a[0:7],v10
s_waitcnt lgkmcnt(4)
ds_read_b64 a[224:225],v248 offset:57344
ds_read_b64 a[226:227],v248 offset:57352
ds_read_b64 a[228:229],v249 offset:57344
ds_read_b64 a[230:231],v249 offset:57352
ds_read_b128 a[240:243],v13 offset:0
v_pk_add_f32 v[36:37],v[36:37],v[140:141]
v_pk_fma_f32 v[40:41],v[140:141],v[140:141],v[40:41]
v_pk_add_f32 v[38:39],v[38:39],v[142:143]
v_pk_fma_f32 v[42:43],v[142:143],v[142:143],v[42:43]
v_smfmac_f32_16x16x64_bf16 v[152:155],a[184:187],a[8:15],v10
s_waitcnt lgkmcnt(5)
s_add_u32 m0,s58,12320
s_add_u32 s69,s69,s53
buffer_load_dwordx4 v5,s[44:47],s69 offen offset:1024 lds
ds_read_b64 a[232:233],v250 offset:57344
ds_read_b64 a[234:235],v250 offset:57352
ds_read_b64 a[236:237],v251 offset:57344
ds_read_b64 a[238:239],v251 offset:57352
ds_read_b128 a[244:247],v13 offset:256
v_pk_add_f32 v[36:37],v[36:37],v[144:145]
v_pk_fma_f32 v[40:41],v[144:145],v[144:145],v[40:41]
v_pk_add_f32 v[38:39],v[38:39],v[146:147]
v_pk_fma_f32 v[42:43],v[146:147],v[146:147],v[42:43]
v_smfmac_f32_16x16x64_bf16 v[156:159],a[188:191],a[16:23],v10
ds_read_b128 v[240:243],v4 offset:3584
ds_read_b128 v[244:247],v4 offset:3616
v_pk_add_f32 v[36:37],v[36:37],v[148:149]
v_pk_fma_f32 v[40:41],v[148:149],v[148:149],v[40:41]
v_pk_add_f32 v[38:39],v[38:39],v[150:151]
v_pk_fma_f32 v[42:43],v[150:151],v[150:151],v[42:43]
v_pk_add_f32 v[36:37],v[36:37],v[152:153]
v_pk_fma_f32 v[40:41],v[152:153],v[152:153],v[40:41]
v_pk_add_f32 v[38:39],v[38:39],v[154:155]
v_pk_fma_f32 v[42:43],v[154:155],v[154:155],v[42:43]
v_pk_add_f32 v[36:37],v[36:37],v[156:157]
v_pk_fma_f32 v[40:41],v[156:157],v[156:157],v[40:41]
v_pk_add_f32 v[38:39],v[38:39],v[158:159]
v_pk_fma_f32 v[42:43],v[158:159],v[158:159],v[42:43]
s_nop 0
v_permlane32_swap_b32 v36,v38
v_permlane32_swap_b32 v37,v39
v_permlane32_swap_b32 v40,v42
v_permlane32_swap_b32 v41,v43
v_pk_add_f32 v[36:37],v[36:37],v[38:39]
v_pk_add_f32 v[40:41],v[40:41],v[42:43]
s_add_u32 s60,s60,56576
s_sub_u32 s64,s60,88400
s_min_u32 s60,s60,s64
v_permlane16_swap_b32 v36,v37
v_permlane16_swap_b32 v40,v41
v_add_f32 v8,v36,v37
v_add_f32 v9,v40,v41
v_mul_f32 v8,0x3c2aaaab,v8
v_mul_f32 v9,0x3c2aaaab,v9
v_cmp_ngt_f32 vcc,0x5d800000,v9
v_fma_f32 v9,-v8,v8,v9
v_max_f32 v9,0,v9
s_or_b64 s[90:91],s[90:91],vcc
v_add_f32 v9,0x3727c5ac,v9
v_rsq_f32 v9,v9
s_nop 0
v_mul_f32 v36,s96,v9
v_mul_f32 v37,s96,v9
v_mul_f32 v40,-v8,v36
v_mul_f32 v41,-v8,v36
v_permlane16_swap_b32 v36,v37
s_nop 0
v_permlane16_swap_b32 v40,v41
v_mov_b64 v[38:39],v[36:37]
v_mov_b64 v[42:43],v[40:41]
s_nop 0
v_permlane32_swap_b32 v36,v38
v_permlane32_swap_b32 v37,v39
v_permlane32_swap_b32 v40,v42
v_permlane32_swap_b32 v41,v43
s_waitcnt vmcnt(10)
s_barrier
ds_read_b64_tr_b16 a[192:193],v1 offset:18480
ds_read_b64_tr_b16 a[194:195],v1 offset:19504
ds_read_b64_tr_b16 a[196:197],v1 offset:20528
ds_read_b64_tr_b16 a[198:199],v1 offset:21552
ds_read_b64_tr_b16 a[200:201],v1 offset:22576
ds_read_b64_tr_b16 a[202:203],v1 offset:23600
ds_read_b64_tr_b16 a[204:205],v2 offset:22576
ds_read_b64_tr_b16 a[206:207],v2 offset:23600
ds_read_b64_tr_b16 a[208:209],v2 offset:20528
ds_read_b64_tr_b16 a[210:211],v2 offset:21552
ds_read_b64_tr_b16 a[212:213],v2 offset:18480
ds_read_b64_tr_b16 a[214:215],v2 offset:19504
ds_read_b128 v[16:19],v4 offset:0
ds_read_b128 v[20:23],v4 offset:32
ds_read_b64 v[14:15],v7 offset:18480
v_pk_fma_f32 v[64:65],v[64:65],v[36:37],v[40:41]
v_pk_fma_f32 v[66:67],v[66:67],v[38:39],v[42:43]
v_pk_fma_f32 v[68:69],v[68:69],v[36:37],v[40:41]
v_pk_fma_f32 v[70:71],v[70:71],v[38:39],v[42:43]
v_pk_fma_f32 v[72:73],v[72:73],v[36:37],v[40:41]
v_pk_fma_f32 v[74:75],v[74:75],v[38:39],v[42:43]
v_pk_fma_f32 v[76:77],v[76:77],v[36:37],v[40:41]
v_pk_fma_f32 v[78:79],v[78:79],v[38:39],v[42:43]
v_pk_fma_f32 v[112:113],v[112:113],v[36:37],v[40:41]
v_pk_fma_f32 v[114:115],v[114:115],v[38:39],v[42:43]
v_pk_fma_f32 v[116:117],v[116:117],v[36:37],v[40:41]
v_pk_fma_f32 v[118:119],v[118:119],v[38:39],v[42:43]
s_add_u32 m0,s57,81336
s_add_u32 s89,s56,2260992
buffer_load_dwordx4 v6,s[36:39],s89 offen lds
v_pk_fma_f32 v[136:137],v[136:137],v[36:37],v[40:41]
v_pk_fma_f32 v[138:139],v[138:139],v[38:39],v[42:43]
v_pk_fma_f32 v[140:141],v[140:141],v[36:37],v[40:41]
v_pk_fma_f32 v[142:143],v[142:143],v[38:39],v[42:43]
v_pk_fma_f32 v[80:81],v[80:81],v[36:37],v[40:41]
v_pk_fma_f32 v[82:83],v[82:83],v[38:39],v[42:43]
v_pk_fma_f32 v[84:85],v[84:85],v[36:37],v[40:41]
v_pk_fma_f32 v[86:87],v[86:87],v[38:39],v[42:43]
v_pk_fma_f32 v[88:89],v[88:89],v[36:37],v[40:41]
v_pk_fma_f32 v[90:91],v[90:91],v[38:39],v[42:43]
v_pk_fma_f32 v[92:93],v[92:93],v[36:37],v[40:41]
v_pk_fma_f32 v[94:95],v[94:95],v[38:39],v[42:43]
s_add_u32 m0,s58,12320
s_add_u32 s69,s69,s53
buffer_load_dwordx4 v5,s[44:47],s69 offen offset:2048 lds
v_pk_fma_f32 v[120:121],v[120:121],v[36:37],v[40:41]
v_pk_fma_f32 v[122:123],v[122:123],v[38:39],v[42:43]
v_pk_fma_f32 v[124:125],v[124:125],v[36:37],v[40:41]
v_pk_fma_f32 v[126:127],v[126:127],v[38:39],v[42:43]
v_pk_fma_f32 v[144:145],v[144:145],v[36:37],v[40:41]
v_pk_fma_f32 v[146:147],v[146:147],v[38:39],v[42:43]
v_pk_fma_f32 v[148:149],v[148:149],v[36:37],v[40:41]
v_pk_fma_f32 v[150:151],v[150:151],v[38:39],v[42:43]
v_pk_fma_f32 v[96:97],v[96:97],v[36:37],v[40:41]
v_pk_fma_f32 v[98:99],v[98:99],v[38:39],v[42:43]
v_pk_fma_f32 v[100:101],v[100:101],v[36:37],v[40:41]
v_pk_fma_f32 v[102:103],v[102:103],v[38:39],v[42:43]
s_add_u32 m0,s57,84864
s_add_u32 s89,s56,2359296
buffer_load_dwordx4 v6,s[36:39],s89 offen lds
v_pk_fma_f32 v[104:105],v[104:105],v[36:37],v[40:41]
v_pk_fma_f32 v[106:107],v[106:107],v[38:39],v[42:43]
v_pk_fma_f32 v[108:109],v[108:109],v[36:37],v[40:41]
v_pk_fma_f32 v[110:111],v[110:111],v[38:39],v[42:43]
v_pk_fma_f32 v[128:129],v[128:129],v[36:37],v[40:41]
v_pk_fma_f32 v[130:131],v[130:131],v[38:39],v[42:43]
v_pk_fma_f32 v[132:133],v[132:133],v[36:37],v[40:41]
v_pk_fma_f32 v[134:135],v[134:135],v[38:39],v[42:43]
v_pk_fma_f32 v[152:153],v[152:153],v[36:37],v[40:41]
v_pk_fma_f32 v[154:155],v[154:155],v[38:39],v[42:43]
v_pk_fma_f32 v[156:157],v[156:157],v[36:37],v[40:41]
v_pk_fma_f32 v[158:159],v[158:159],v[38:39],v[42:43]
v_cvt_pk_bf16_f32 v64,v64,v68
s_waitcnt lgkmcnt(0)
v_or3_b32 v252,v14,v15,0
ds_read_b64 v[14:15],v7 offset:18992
v_cvt_pk_bf16_f32 v72,v72,v76
v_cvt_pk_bf16_f32 v112,v112,v116
v_accvgpr_write_b32 a0,v64
v_accvgpr_write_b32 a1,v72
v_cvt_pk_bf16_f32 v136,v136,v140
v_accvgpr_write_b32 a2,v112
v_cvt_pk_bf16_f32 v65,v65,v69
v_accvgpr_write_b32 a3,v136
v_cvt_pk_bf16_f32 v73,v73,v77
v_accvgpr_write_b32 a4,v65
v_cvt_pk_bf16_f32 v113,v113,v117
v_accvgpr_write_b32 a5,v73
v_cvt_pk_bf16_f32 v137,v137,v141
v_accvgpr_write_b32 a6,v113
v_cvt_pk_bf16_f32 v80,v80,v84
v_accvgpr_write_b32 a7,v137
v_cvt_pk_bf16_f32 v88,v88,v92
v_accvgpr_write_b32 a16,v80
v_cvt_pk_bf16_f32 v120,v120,v124
s_waitcnt lgkmcnt(0)
v_or3_b32 v252,v14,v15,v252
ds_read_b64 v[14:15],v7 offset:19504
v_accvgpr_write_b32 a17,v88
v_cvt_pk_bf16_f32 v144,v144,v148
v_accvgpr_write_b32 a18,v120
v_cvt_pk_bf16_f32 v81,v81,v85
v_accvgpr_write_b32 a19,v144
v_cvt_pk_bf16_f32 v89,v89,v93
v_accvgpr_write_b32 a20,v81
v_accvgpr_write_b32 a21,v89
v_cvt_pk_bf16_f32 v121,v121,v125
v_accvgpr_write_b32 a22,v121
v_cvt_pk_bf16_f32 v145,v145,v149
v_accvgpr_write_b32 a23,v145
v_cvt_pk_bf16_f32 v96,v96,v100
v_accvgpr_write_b32 a32,v96
v_cvt_pk_bf16_f32 v104,v104,v108
v_accvgpr_write_b32 a33,v104
v_cvt_pk_bf16_f32 v128,v128,v132
v_accvgpr_write_b32 a34,v128
v_cvt_pk_bf16_f32 v152,v152,v156
s_waitcnt lgkmcnt(0)
v_or3_b32 v252,v14,v15,v252
v_accvgpr_write_b32 a35,v152
v_cvt_pk_bf16_f32 v97,v97,v101
v_accvgpr_write_b32 a36,v97
v_cvt_pk_bf16_f32 v105,v105,v109
v_accvgpr_write_b32 a37,v105
v_cvt_pk_bf16_f32 v129,v129,v133
v_accvgpr_write_b32 a38,v129
v_cvt_pk_bf16_f32 v153,v153,v157
v_accvgpr_write_b32 a39,v153
v_mfma_f32_16x16x32_bf16 v[44:47],a[192:195],a[0:3],v[16:19]
v_cvt_pk_bf16_f32 v66,v66,v70
v_cvt_pk_bf16_f32 v74,v74,v78
v_cvt_pk_bf16_f32 v114,v114,v118
v_accvgpr_write_b32 a8,v66
v_mfma_f32_16x16x32_bf16 v[44:47],a[196:199],a[16:19],v[44:47]
v_accvgpr_write_b32 a9,v74
v_cvt_pk_bf16_f32 v138,v138,v142
v_accvgpr_write_b32 a10,v114
v_cvt_pk_bf16_f32 v67,v67,v71
v_mfma_f32_16x16x32_bf16 v[44:47],a[200:203],a[32:35],v[44:47]
v_accvgpr_write_b32 a11,v138
v_cvt_pk_bf16_f32 v75,v75,v79
v_accvgpr_write_b32 a12,v67
v_cvt_pk_bf16_f32 v115,v115,v119
v_mfma_f32_16x16x32_bf16 v[48:51],a[204:207],a[32:35],v[20:23]
v_accvgpr_write_b32 a13,v75
v_cvt_pk_bf16_f32 v139,v139,v143
v_accvgpr_write_b32 a14,v115
v_cvt_pk_bf16_f32 v82,v82,v86
v_mfma_f32_16x16x32_bf16 v[48:51],a[208:211],a[16:19],v[48:51]
TE00_%=:
s_cbranch_execz TP3327_%=
v_accvgpr_write_b32 a15,v139
v_cvt_pk_bf16_f32 v90,v90,v94
v_accvgpr_write_b32 a24,v82
v_cvt_pk_bf16_f32 v122,v122,v126
v_mfma_f32_16x16x32_bf16 v[48:51],a[212:215],a[0:3],v[48:51]
v_accvgpr_write_b32 a25,v90
v_cvt_pk_bf16_f32 v146,v146,v150
v_accvgpr_write_b32 a26,v122
v_cvt_pk_bf16_f32 v83,v83,v87
v_mfma_f32_16x16x32_bf16 v[52:55],a[212:215],a[4:7],v[20:23]
v_accvgpr_write_b32 a27,v146
v_cvt_pk_bf16_f32 v91,v91,v95
v_accvgpr_write_b32 a28,v83
v_accvgpr_write_b32 a29,v91
v_mfma_f32_16x16x32_bf16 v[52:55],a[208:211],a[20:23],v[52:55]
v_cvt_pk_bf16_f32 v123,v123,v127
v_accvgpr_write_b32 a30,v123
v_cvt_pk_bf16_f32 v147,v147,v151
v_accvgpr_write_b32 a31,v147
v_mfma_f32_16x16x32_bf16 v[52:55],a[204:207],a[36:39],v[52:55]
v_cvt_pk_bf16_f32 v98,v98,v102
v_accvgpr_write_b32 a40,v98
v_cvt_pk_bf16_f32 v106,v106,v110
v_accvgpr_write_b32 a41,v106
v_mfma_f32_16x16x32_bf16 v[56:59],a[200:203],a[36:39],v[16:19]
v_cvt_pk_bf16_f32 v130,v130,v134
v_accvgpr_write_b32 a42,v130
v_cvt_pk_bf16_f32 v154,v154,v158
v_accvgpr_write_b32 a43,v154
v_mfma_f32_16x16x32_bf16 v[56:59],a[196:199],a[20:23],v[56:59]
v_cvt_pk_bf16_f32 v99,v99,v103
v_accvgpr_write_b32 a44,v99
v_cvt_pk_bf16_f32 v107,v107,v111
v_accvgpr_write_b32 a45,v107
v_mfma_f32_16x16x32_bf16 v[56:59],a[192:195],a[4:7],v[56:59]
v_cvt_pk_bf16_f32 v131,v131,v135
v_accvgpr_write_b32 a46,v131
v_cvt_pk_bf16_f32 v155,v155,v159
v_accvgpr_write_b32 a47,v155
v_mfma_f32_16x16x32_bf16 v[60:63],a[192:195],a[8:11],v[16:19]
ds_read_b64 v[14:15],v7 offset:0
v_lshlrev_b32_sdwa v44,1,v44 dst_sel:DWORD dst_unused:UNUSED_PAD src0_sel:DWORD src1_sel:WORD_1
v_mfma_f32_16x16x32_bf16 v[60:63],a[196:199],a[24:27],v[60:63]
ds_read_b64_tr_b16 a[216:217],v1 offset:0
v_fma_mixlo_f16 v8,v53,s99,0
v_mfma_f32_16x16x32_bf16 v[60:63],a[200:203],a[40:43],v[60:63]
ds_read_b64_tr_b16 a[218:219],v1 offset:1024
v_lshlrev_b32_sdwa v46,1,v46 dst_sel:DWORD dst_unused:UNUSED_PAD src0_sel:DWORD src1_sel:WORD_1
v_mfma_f32_16x16x32_bf16 v[32:35],a[204:207],a[40:43],v[20:23]
ds_read_b64_tr_b16 a[220:221],v1 offset:2048
v_fma_mixhi_f16 v8,v55,s99,0
v_mfma_f32_16x16x32_bf16 v[32:35],a[208:211],a[24:27],v[32:35]
ds_read_b64_tr_b16 a[222:223],v1 offset:3072
v_lshlrev_b32_sdwa v45,1,v45 dst_sel:DWORD dst_unused:UNUSED_PAD src0_sel:DWORD src1_sel:WORD_1
v_mfma_f32_16x16x32_bf16 v[32:35],a[212:215],a[8:11],v[32:35]
ds_read_u16 v44,v44
v_pk_fma_f16 v9,v8,v8,s54
v_mfma_f32_16x16x32_bf16 v[36:39],a[212:215],a[12:15],v[20:23]
ds_read_b128 v[20:23],v4 offset:128
v_lshlrev_b32_sdwa v47,1,v47 dst_sel:DWORD dst_unused:UNUSED_PAD src0_sel:DWORD src1_sel:WORD_1
v_mfma_f32_16x16x32_bf16 v[36:39],a[208:211],a[28:31],v[36:39]
s_waitcnt lgkmcnt(6)
ds_read_b64_tr_b16 a[212:213],v1 offset:4096
ds_read_b64_tr_b16 a[214:215],v1 offset:5120
v_or3_b32 v252,v14,v15,v252
v_mfma_f32_16x16x32_bf16 v[36:39],a[204:207],a[44:47],v[36:39]
ds_read_b64 v[14:15],v7 offset:512
ds_read_b64_tr_b16 a[208:209],v2 offset:4096
v_mfma_f32_16x16x32_bf16 v[40:43],a[200:203],a[44:47],v[16:19]
ds_read_b128 v[16:19],v4 offset:160
ds_read_b64_tr_b16 a[210:211],v2 offset:5120
v_pk_mul_f16 v9,v8,v9
v_mfma_f32_16x16x32_bf16 v[40:43],a[196:199],a[28:31],v[40:43]
ds_read_b64_tr_b16 a[204:205],v2 offset:2048
ds_read_b64_tr_b16 a[206:207],v2 offset:3072
v_lshlrev_b32_sdwa v48,1,v48 dst_sel:DWORD dst_unused:UNUSED_PAD src0_sel:DWORD src1_sel:WORD_1
v_mfma_f32_16x16x32_bf16 v[40:43],a[192:195],a[12:15],v[40:43]
ds_read_b64_tr_b16 a[200:201],v2 offset:0
v_exp_f16 v8,v9
v_mfma_f32_16x16x32_bf16 v[136:139],a[240:243],a[224:227],v[240:243]
ds_read_b64_tr_b16 a[202:203],v2 offset:1024
v_lshlrev_b32_sdwa v50,1,v50 dst_sel:DWORD dst_unused:UNUSED_PAD src0_sel:DWORD src1_sel:WORD_1
v_mfma_f32_16x16x32_bf16 v[140:143],a[240:243],a[228:231],v[240:243]
ds_read_u16_d16_hi v46,v46
v_lshlrev_b32_sdwa v49,1,v49 dst_sel:DWORD dst_unused:UNUSED_PAD src0_sel:DWORD src1_sel:WORD_1
v_mfma_f32_16x16x32_bf16 v[112:115],a[240:243],a[232:235],v[240:243]
ds_read_u16 v45,v45
v_lshlrev_b32_sdwa v51,1,v51 dst_sel:DWORD dst_unused:UNUSED_PAD src0_sel:DWORD src1_sel:WORD_1
v_mfma_f32_16x16x32_bf16 v[116:119],a[240:243],a[236:239],v[240:243]
ds_read_b128 a[240:243],v13 offset:512
ds_read_b128 v[240:243],v4 offset:3712
v_mfma_f32_16x16x32_bf16 v[64:67],a[244:247],a[224:227],v[244:247]
s_waitcnt lgkmcnt(11)
ds_read_b64 a[224:225],v248 offset:57408
ds_read_b64 a[226:227],v248 offset:57416
v_or3_b32 v252,v14,v15,v252
v_mfma_f32_16x16x32_bf16 v[68:71],a[244:247],a[228:231],v[244:247]
ds_read_b64 v[14:15],v7 offset:1024
ds_read_b64 a[228:229],v249 offset:57408
v_exp_f16_sdwa v8,v9 dst_sel:WORD_1 dst_unused:UNUSED_PRESERVE src0_sel:WORD_1
v_mfma_f32_16x16x32_bf16 v[72:75],a[244:247],a[232:235],v[244:247]
ds_read_b64 a[230:231],v249 offset:57416
ds_read_b64 a[232:233],v250 offset:57408
v_mfma_f32_16x16x32_bf16 v[76:79],a[244:247],a[236:239],v[244:247]
s_waitcnt lgkmcnt(4)
ds_read_b64 a[234:235],v250 offset:57416
ds_read_b64 a[236:237],v251 offset:57408
v_mfma_f32_16x16x32_bf16 v[144:147],a[240:243],a[224:227],v[240:243]
s_waitcnt lgkmcnt(3)
ds_read_b64 a[238:239],v251 offset:57416
ds_read_b128 a[244:247],v13 offset:768
v_lshlrev_b32_sdwa v56,1,v56 dst_sel:DWORD dst_unused:UNUSED_PAD src0_sel:DWORD src1_sel:WORD_1
v_mfma_f32_16x16x32_bf16 v[148:151],a[240:243],a[228:231],v[240:243]
s_waitcnt lgkmcnt(3)
ds_read_b128 v[244:247],v4 offset:3744
ds_read_u16_d16_hi v47,v47
v_pk_fma_f16 v8,v8,s55,s55
v_mfma_f32_16x16x32_bf16 v[120:123],a[240:243],a[232:235],v[240:243]
s_waitcnt lgkmcnt(3)
ds_read_u16 v48,v48
v_lshlrev_b32_sdwa v58,1,v58 dst_sel:DWORD dst_unused:UNUSED_PAD src0_sel:DWORD src1_sel:WORD_1
v_mfma_f32_16x16x32_bf16 v[124:127],a[240:243],a[236:239],v[240:243]
s_waitcnt lgkmcnt(2)
ds_read_b128 a[240:243],v13 offset:1024
ds_read_b128 v[240:243],v4 offset:3840
v_rcp_f16 v9,v8
v_mfma_f32_16x16x32_bf16 v[80:83],a[244:247],a[224:227],v[244:247]
ds_read_b64 a[224:225],v248 offset:57472
ds_read_b64 a[226:227],v248 offset:57480
v_mfma_f32_16x16x32_bf16 v[84:87],a[244:247],a[228:231],v[244:247]
ds_read_b64 a[228:229],v249 offset:57472
ds_read_b64 a[230:231],v249 offset:57480
v_or3_b32 v252,v14,v15,v252
v_lshlrev_b32_sdwa v57,1,v57 dst_sel:DWORD dst_unused:UNUSED_PAD src0_sel:DWORD src1_sel:WORD_1
v_mfma_f32_16x16x32_bf16 v[88:91],a[244:247],a[232:235],v[244:247]
ds_read_b64 a[232:233],v250 offset:57472
ds_read_b64 a[234:235],v250 offset:57480
v_mfma_f32_16x16x32_bf16 v[92:95],a[244:247],a[236:239],v[244:247]
s_waitcnt lgkmcnt(4)
ds_read_b64 a[236:237],v251 offset:57472
ds_read_b64 a[238:239],v251 offset:57480
v_lshlrev_b32_sdwa v59,1,v59 dst_sel:DWORD dst_unused:UNUSED_PAD src0_sel:DWORD src1_sel:WORD_1
v_mfma_f32_16x16x32_bf16 v[152:155],a[240:243],a[224:227],v[240:243]
s_waitcnt lgkmcnt(4)
ds_read_b128 a[244:247],v13 offset:1280
ds_read_b128 v[244:247],v4 offset:3872
v_rcp_f16_sdwa v9,v8 dst_sel:WORD_1 dst_unused:UNUSED_PRESERVE src0_sel:WORD_1
v_mfma_f32_16x16x32_bf16 v[156:159],a[240:243],a[228:231],v[240:243]
s_waitcnt lgkmcnt(4)
ds_read_u16_d16_hi v50,v50
ds_read_u16 v49,v49
v_mfma_f32_16x16x32_bf16 v[128:131],a[240:243],a[232:235],v[240:243]
s_waitcnt lgkmcnt(4)
ds_read_u16_d16_hi v51,v51
ds_read_u16 v56,v56
v_lshlrev_b32_sdwa v52,1,v52 dst_sel:DWORD dst_unused:UNUSED_PAD src0_sel:DWORD src1_sel:WORD_1
v_mfma_f32_16x16x32_bf16 v[132:135],a[240:243],a[236:239],v[240:243]
s_waitcnt lgkmcnt(4)
ds_read_u16_d16_hi v58,v58
ds_read_u16 v57,v57
v_mfma_f32_16x16x32_bf16 v[96:99],a[244:247],a[224:227],v[244:247]
ds_read_u16_d16_hi v59,v59
ds_read_u16 v52,v52
v_lshlrev_b32_sdwa v54,1,v54 dst_sel:DWORD dst_unused:UNUSED_PAD src0_sel:DWORD src1_sel:WORD_1
v_mfma_f32_16x16x32_bf16 v[100:103],a[244:247],a[228:231],v[244:247]
ds_read_u16_d16_hi v54,v54
ds_read_b64_tr_b16 a[248:249],v3 offset:0
v_fma_mix_f32 v53,v53,v9,0 op_sel_hi:[0,1,0]
v_mfma_f32_16x16x32_bf16 v[104:107],a[244:247],a[232:235],v[244:247]
ds_read_b64_tr_b16 a[250:251],v3 offset:1536
ds_read_b64_tr_b16 a[252:253],v3 offset:8
v_mfma_f32_16x16x32_bf16 v[108:111],a[244:247],a[236:239],v[244:247]
s_barrier
ds_read_b64_tr_b16 a[254:255],v3 offset:1544
ds_read_b64_tr_b16 a[240:241],v3 offset:64
v_fma_mix_f32 v55,v55,v9,0 op_sel:[0,1,0] op_sel_hi:[0,1,0]
v_mfma_f32_16x16x32_bf16 v[24:27],a[216:219],a[0:3],v[20:23]
s_add_u32 m0,s57,0
ds_read_b64_tr_b16 a[242:243],v3 offset:1600
ds_read_b64_tr_b16 a[224:225],v3 offset:72
v_lshlrev_b32_sdwa v60,1,v60 dst_sel:DWORD dst_unused:UNUSED_PAD src0_sel:DWORD src1_sel:WORD_1
v_mfma_f32_16x16x32_bf16 v[24:27],a[220:223],a[16:19],v[24:27]
s_add_u32 s89,s56,2457600
ds_read_b64_tr_b16 a[226:227],v3 offset:1608
v_fma_mixlo_f16 v8,v37,s99,0
v_mfma_f32_16x16x32_bf16 v[24:27],a[212:215],a[32:35],v[24:27]
buffer_load_dwordx4 v6,s[36:39],s89 offen lds
v_lshl_add_u64 v[44:45],v[44:45],0,v[46:47]
v_lshlrev_b32_sdwa v62,1,v62 dst_sel:DWORD dst_unused:UNUSED_PAD src0_sel:DWORD src1_sel:WORD_1
v_mfma_f32_16x16x32_bf16 v[28:31],a[208:211],a[32:35],v[16:19]
s_waitcnt lgkmcnt(13)
ds_read_b64_tr_b16 a[228:229],v3 offset:128
v_lshl_add_u64 v[46:47],v[48:49],0,v[50:51]
v_fma_mixhi_f16 v8,v39,s99,0
v_mfma_f32_16x16x32_bf16 v[28:31],a[204:207],a[16:19],v[28:31]
s_waitcnt lgkmcnt(11)
ds_read_b64_tr_b16 a[230:231],v3 offset:1664
v_lshl_add_u64 v[48:49],v[56:57],0,v[58:59]
v_lshlrev_b32_sdwa v61,1,v61 dst_sel:DWORD dst_unused:UNUSED_PAD src0_sel:DWORD src1_sel:WORD_1
v_pk_fma_f16 v9,v8,v8,s54
v_mfma_f32_16x16x32_bf16 v[28:31],a[200:203],a[0:3],v[28:31]
s_add_u32 m0,s58,18480
ds_read_b64_tr_b16 a[232:233],v3 offset:136
v_lshlrev_b32_sdwa v63,1,v63 dst_sel:DWORD dst_unused:UNUSED_PAD src0_sel:DWORD src1_sel:WORD_1
v_mfma_f32_16x16x32_bf16 v[56:59],a[200:203],a[4:7],v[16:19]
s_waitcnt lgkmcnt(10)
ds_read_b64_tr_b16 a[234:235],v3 offset:1672
v_or_b32 v50,v52,v54
v_pk_mul_f16 v9,v8,v9
v_mfma_f32_16x16x32_bf16 v[56:59],a[204:207],a[20:23],v[56:59]
s_waitcnt vmcnt(8)
ds_read_u16 v60,v60
v_cvt_pk_bf16_f32 v51,v53,v55
v_lshlrev_b32_sdwa v32,1,v32 dst_sel:DWORD dst_unused:UNUSED_PAD src0_sel:DWORD src1_sel:WORD_1
v_mfma_f32_16x16x32_bf16 v[56:59],a[208:211],a[36:39],v[56:59]
s_barrier
v_exp_f16 v8,v9
v_mfma_f32_16x16x32_bf16 v[52:55],a[212:215],a[36:39],v[20:23]
s_add_u32 s69,s69,s59
ds_read_u16_d16_hi v62,v62
v_lshlrev_b32_sdwa v34,1,v34 dst_sel:DWORD dst_unused:UNUSED_PAD src0_sel:DWORD src1_sel:WORD_1
v_mfma_f32_16x16x32_bf16 v[52:55],a[220:223],a[20:23],v[52:55]
buffer_load_dwordx4 v5,s[44:47],s69 offen offset:0 lds
v_lshlrev_b32_sdwa v33,1,v33 dst_sel:DWORD dst_unused:UNUSED_PAD src0_sel:DWORD src1_sel:WORD_1
v_mfma_f32_16x16x32_bf16 v[52:55],a[216:219],a[4:7],v[52:55]
s_waitcnt lgkmcnt(6)
v_exp_f16_sdwa v8,v9 dst_sel:WORD_1 dst_unused:UNUSED_PRESERVE src0_sel:WORD_1
v_mfma_f32_16x16x32_bf16 v[136:139],a[248:251],v[44:47],v[136:139]
s_add_u32 s69,s69,s53
ds_read_u16 v61,v61
v_lshlrev_b32_sdwa v35,1,v35 dst_sel:DWORD dst_unused:UNUSED_PAD src0_sel:DWORD src1_sel:WORD_1
v_mfma_f32_16x16x32_bf16 v[140:143],a[248:251],v[48:51],v[140:143]
buffer_load_dwordx4 v5,s[44:47],s69 offen offset:1024 lds
v_lshlrev_b32_sdwa v40,1,v40 dst_sel:DWORD dst_unused:UNUSED_PAD src0_sel:DWORD src1_sel:WORD_1
v_mfma_f32_16x16x32_bf16 v[68:71],a[252:255],v[48:51],v[68:71]
s_add_u32 s69,s69,s53
ds_read_u16_d16_hi v63,v63
v_pk_fma_f16 v8,v8,s55,s55
v_mfma_f32_16x16x32_bf16 v[64:67],a[252:255],v[44:47],v[64:67]
v_rcp_f16 v9,v8
v_lshlrev_b32_sdwa v42,1,v42 dst_sel:DWORD dst_unused:UNUSED_PAD src0_sel:DWORD src1_sel:WORD_1
v_mfma_f32_16x16x32_bf16 v[144:147],a[240:243],v[44:47],v[144:147]
buffer_load_dwordx4 v5,s[44:47],s69 offen offset:2048 lds
ds_read_u16 v32,v32
v_mfma_f32_16x16x32_bf16 v[148:151],a[240:243],v[48:51],v[148:151]
ds_read_u16_d16_hi v34,v34
v_lshlrev_b32_sdwa v41,1,v41 dst_sel:DWORD dst_unused:UNUSED_PAD src0_sel:DWORD src1_sel:WORD_1
v_mfma_f32_16x16x32_bf16 v[84:87],a[224:227],v[48:51],v[84:87]
ds_read_u16 v33,v33
v_lshlrev_b32_sdwa v43,1,v43 dst_sel:DWORD dst_unused:UNUSED_PAD src0_sel:DWORD src1_sel:WORD_1
v_mfma_f32_16x16x32_bf16 v[80:83],a[224:227],v[44:47],v[80:83]
s_waitcnt lgkmcnt(5)
ds_read_u16_d16_hi v35,v35
v_rcp_f16_sdwa v9,v8 dst_sel:WORD_1 dst_unused:UNUSED_PRESERVE src0_sel:WORD_1
v_mfma_f32_16x16x32_bf16 v[152:155],a[228:231],v[44:47],v[152:155]
ds_read_u16 v40,v40
ds_read_u16_d16_hi v42,v42
v_lshlrev_b32_sdwa v36,1,v36 dst_sel:DWORD dst_unused:UNUSED_PAD src0_sel:DWORD src1_sel:WORD_1
v_mfma_f32_16x16x32_bf16 v[156:159],a[228:231],v[48:51],v[156:159]
s_add_u32 m0,s57,3544
ds_read_u16 v41,v41
ds_read_u16_d16_hi v43,v43
v_lshlrev_b32_sdwa v38,1,v38 dst_sel:DWORD dst_unused:UNUSED_PAD src0_sel:DWORD src1_sel:WORD_1
v_mfma_f32_16x16x32_bf16 v[100:103],a[232:235],v[48:51],v[100:103]
s_add_u32 s89,s56,2555904
ds_read_u16 v36,v36
v_fma_mix_f32 v37,v37,v9,0 op_sel_hi:[0,1,0]
v_mfma_f32_16x16x32_bf16 v[96:99],a[232:235],v[44:47],v[96:99]
buffer_load_dwordx4 v6,s[36:39],s89 offen lds
ds_read_u16_d16_hi v38,v38
v_fma_mix_f32 v39,v39,v9,0 op_sel:[0,1,0] op_sel_hi:[0,1,0]
v_mfma_f32_16x16x32_bf16 v[48:51],a[216:219],a[8:11],v[20:23]
ds_read_b64 v[14:15],v7 offset:6160
ds_read_b64_tr_b16 a[196:197],v1 offset:6160
v_lshlrev_b32_sdwa v24,1,v24 dst_sel:DWORD dst_unused:UNUSED_PAD src0_sel:DWORD src1_sel:WORD_1
v_mfma_f32_16x16x32_bf16 v[48:51],a[220:223],a[24:27],v[48:51]
ds_read_b64_tr_b16 a[198:199],v1 offset:7184
ds_read_b64_tr_b16 a[192:193],v1 offset:8208
v_fma_mixlo_f16 v8,v57,s99,0
v_mfma_f32_16x16x32_bf16 v[48:51],a[212:215],a[40:43],v[48:51]
s_waitcnt lgkmcnt(12)
ds_read_b64_tr_b16 a[194:195],v1 offset:9232
v_lshl_add_u64 v[60:61],v[60:61],0,v[62:63]
v_lshlrev_b32_sdwa v26,1,v26 dst_sel:DWORD dst_unused:UNUSED_PAD src0_sel:DWORD src1_sel:WORD_1
v_mfma_f32_16x16x32_bf16 v[44:47],a[208:211],a[40:43],v[16:19]
s_waitcnt lgkmcnt(11)
ds_read_u16 v24,v24
v_lshl_add_u64 v[62:63],v[32:33],0,v[34:35]
v_fma_mixhi_f16 v8,v59,s99,0
v_mfma_f32_16x16x32_bf16 v[44:47],a[204:207],a[24:27],v[44:47]
s_waitcnt lgkmcnt(8)
ds_read_u16_d16_hi v26,v26
v_lshl_add_u64 v[32:33],v[40:41],0,v[42:43]
v_lshlrev_b32_sdwa v25,1,v25 dst_sel:DWORD dst_unused:UNUSED_PAD src0_sel:DWORD src1_sel:WORD_1
v_pk_fma_f16 v9,v8,v8,s54
v_mfma_f32_16x16x32_bf16 v[44:47],a[200:203],a[8:11],v[44:47]
s_waitcnt lgkmcnt(6)
ds_read_u16 v25,v25
v_or3_b32 v252,v14,v15,v252
v_lshlrev_b32_sdwa v27,1,v27 dst_sel:DWORD dst_unused:UNUSED_PAD src0_sel:DWORD src1_sel:WORD_1
v_mfma_f32_16x16x32_bf16 v[40:43],a[200:203],a[12:15],v[16:19]
ds_read_b64 v[14:15],v7 offset:6672
ds_read_b128 v[16:19],v4 offset:256
v_or_b32 v34,v36,v38
v_pk_mul_f16 v9,v8,v9
v_mfma_f32_16x16x32_bf16 v[40:43],a[204:207],a[28:31],v[40:43]
ds_read_u16_d16_hi v27,v27
ds_read_b64_tr_b16 a[200:201],v1 offset:10256
v_cvt_pk_bf16_f32 v35,v37,v39
v_lshlrev_b32_sdwa v28,1,v28 dst_sel:DWORD dst_unused:UNUSED_PAD src0_sel:DWORD src1_sel:WORD_1
v_mfma_f32_16x16x32_bf16 v[40:43],a[208:211],a[44:47],v[40:43]
ds_read_b64_tr_b16 a[202:203],v1 offset:11280
v_exp_f16 v8,v9
v_mfma_f32_16x16x32_bf16 v[36:39],a[212:215],a[44:47],v[20:23]
ds_read_b128 v[20:23],v4 offset:288
ds_read_b64_tr_b16 a[204:205],v2 offset:10256
v_lshlrev_b32_sdwa v30,1,v30 dst_sel:DWORD dst_unused:UNUSED_PAD src0_sel:DWORD src1_sel:WORD_1
v_mfma_f32_16x16x32_bf16 v[36:39],a[220:223],a[28:31],v[36:39]
ds_read_b64_tr_b16 a[206:207],v2 offset:11280
ds_read_u16 v28,v28
v_lshlrev_b32_sdwa v29,1,v29 dst_sel:DWORD dst_unused:UNUSED_PAD src0_sel:DWORD src1_sel:WORD_1
v_mfma_f32_16x16x32_bf16 v[36:39],a[216:219],a[12:15],v[36:39]
s_waitcnt lgkmcnt(8)
ds_read_u16_d16_hi v30,v30
ds_read_u16 v29,v29
v_or3_b32 v252,v14,v15,v252
v_mfma_f32_16x16x32_bf16 v[112:115],a[248:251],v[60:63],v[112:115]
ds_read_b64 v[14:15],v7 offset:7184
ds_read_b64_tr_b16 a[208:209],v2 offset:8208
v_exp_f16_sdwa v8,v9 dst_sel:WORD_1 dst_unused:UNUSED_PRESERVE src0_sel:WORD_1
v_lshlrev_b32_sdwa v31,1,v31 dst_sel:DWORD dst_unused:UNUSED_PAD src0_sel:DWORD src1_sel:WORD_1
v_mfma_f32_16x16x32_bf16 v[116:119],a[248:251],v[32:35],v[116:119]
ds_read_u16_d16_hi v31,v31
ds_read_b64_tr_b16 a[210:211],v2 offset:9232
v_lshlrev_b32_sdwa v52,1,v52 dst_sel:DWORD dst_unused:UNUSED_PAD src0_sel:DWORD src1_sel:WORD_1
v_mfma_f32_16x16x32_bf16 v[76:79],a[252:255],v[32:35],v[76:79]
s_add_u32 m0,s57,7080
ds_read_u16 v52,v52
ds_read_b64_tr_b16 a[212:213],v2 offset:6160
v_pk_fma_f16 v8,v8,s55,s55
v_mfma_f32_16x16x32_bf16 v[72:75],a[252:255],v[60:63],v[72:75]
s_add_u32 s89,s56,2654208
ds_read_b64_tr_b16 a[214:215],v2 offset:7184
v_rcp_f16 v9,v8
v_lshlrev_b32_sdwa v54,1,v54 dst_sel:DWORD dst_unused:UNUSED_PAD src0_sel:DWORD src1_sel:WORD_1
v_mfma_f32_16x16x32_bf16 v[120:123],a[240:243],v[60:63],v[120:123]
buffer_load_dwordx4 v6,s[36:39],s89 offen lds
ds_read_u16_d16_hi v54,v54
v_mfma_f32_16x16x32_bf16 v[124:127],a[240:243],v[32:35],v[124:127]
s_waitcnt lgkmcnt(7)
ds_read_b64_tr_b16 a[236:237],v3 offset:6160
ds_read_b64_tr_b16 a[238:239],v3 offset:7696
v_or3_b32 v252,v14,v15,v252
v_lshlrev_b32_sdwa v53,1,v53 dst_sel:DWORD dst_unused:UNUSED_PAD src0_sel:DWORD src1_sel:WORD_1
v_mfma_f32_16x16x32_bf16 v[92:95],a[224:227],v[32:35],v[92:95]
ds_read_u16 v53,v53
ds_read_b64_tr_b16 a[244:245],v3 offset:6168
v_lshlrev_b32_sdwa v55,1,v55 dst_sel:DWORD dst_unused:UNUSED_PAD src0_sel:DWORD src1_sel:WORD_1
v_mfma_f32_16x16x32_bf16 v[88:91],a[224:227],v[60:63],v[88:91]
ds_read_u16_d16_hi v55,v55
v_rcp_f16_sdwa v9,v8 dst_sel:WORD_1 dst_unused:UNUSED_PRESERVE src0_sel:WORD_1
v_mfma_f32_16x16x32_bf16 v[128:131],a[228:231],v[60:63],v[128:131]
ds_read_b64_tr_b16 a[246:247],v3 offset:7704
ds_read_b64_tr_b16 a[248:249],v3 offset:6224
v_lshlrev_b32_sdwa v56,1,v56 dst_sel:DWORD dst_unused:UNUSED_PAD src0_sel:DWORD src1_sel:WORD_1
v_mfma_f32_16x16x32_bf16 v[132:135],a[228:231],v[32:35],v[132:135]
ds_read_u16 v56,v56
ds_read_b64_tr_b16 a[250:251],v3 offset:7760
v_lshlrev_b32_sdwa v58,1,v58 dst_sel:DWORD dst_unused:UNUSED_PAD src0_sel:DWORD src1_sel:WORD_1
v_mfma_f32_16x16x32_bf16 v[108:111],a[232:235],v[32:35],v[108:111]
ds_read_u16_d16_hi v58,v58
ds_read_b64_tr_b16 a[252:253],v3 offset:6232
v_fma_mix_f32 v57,v57,v9,0 op_sel_hi:[0,1,0]
v_mfma_f32_16x16x32_bf16 v[104:107],a[232:235],v[60:63],v[104:107]
ds_read_b64_tr_b16 a[254:255],v3 offset:7768
v_fma_mix_f32 v59,v59,v9,0 op_sel:[0,1,0] op_sel_hi:[0,1,0]
v_mfma_f32_16x16x32_bf16 v[32:35],a[196:199],a[0:3],v[16:19]
ds_read_b64_tr_b16 a[240:241],v3 offset:6288
v_lshlrev_b32_sdwa v48,1,v48 dst_sel:DWORD dst_unused:UNUSED_PAD src0_sel:DWORD src1_sel:WORD_1
v_mfma_f32_16x16x32_bf16 v[32:35],a[192:195],a[16:19],v[32:35]
ds_read_b64_tr_b16 a[242:243],v3 offset:7824
v_fma_mixlo_f16 v8,v41,s99,0
v_mfma_f32_16x16x32_bf16 v[32:35],a[200:203],a[32:35],v[32:35]
ds_read_b64_tr_b16 a[224:225],v3 offset:6296
v_lshl_add_u64 v[24:25],v[24:25],0,v[26:27]
v_lshlrev_b32_sdwa v50,1,v50 dst_sel:DWORD dst_unused:UNUSED_PAD src0_sel:DWORD src1_sel:WORD_1
v_mfma_f32_16x16x32_bf16 v[60:63],a[204:207],a[32:35],v[20:23]
s_waitcnt lgkmcnt(11)
ds_read_b64_tr_b16 a[226:227],v3 offset:7832
v_lshl_add_u64 v[26:27],v[28:29],0,v[30:31]
v_fma_mixhi_f16 v8,v43,s99,0
v_mfma_f32_16x16x32_bf16 v[60:63],a[208:211],a[16:19],v[60:63]
s_waitcnt lgkmcnt(11)
ds_read_u16 v48,v48
v_lshl_add_u64 v[28:29],v[52:53],0,v[54:55]
v_lshlrev_b32_sdwa v49,1,v49 dst_sel:DWORD dst_unused:UNUSED_PAD src0_sel:DWORD src1_sel:WORD_1
v_pk_fma_f16 v9,v8,v8,s54
v_mfma_f32_16x16x32_bf16 v[60:63],a[212:215],a[0:3],v[60:63]
ds_read_u16_d16_hi v50,v50
v_lshlrev_b32_sdwa v51,1,v51 dst_sel:DWORD dst_unused:UNUSED_PAD src0_sel:DWORD src1_sel:WORD_1
v_mfma_f32_16x16x32_bf16 v[52:55],a[212:215],a[4:7],v[20:23]
s_waitcnt lgkmcnt(8)
ds_read_u16 v49,v49
v_or_b32 v30,v56,v58
v_pk_mul_f16 v9,v8,v9
v_mfma_f32_16x16x32_bf16 v[52:55],a[208:211],a[20:23],v[52:55]
s_waitcnt vmcnt(7)
ds_read_u16_d16_hi v51,v51
v_cvt_pk_bf16_f32 v31,v57,v59
v_lshlrev_b32_sdwa v44,1,v44 dst_sel:DWORD dst_unused:UNUSED_PAD src0_sel:DWORD src1_sel:WORD_1
v_mfma_f32_16x16x32_bf16 v[52:55],a[204:207],a[36:39],v[52:55]
s_barrier
v_exp_f16 v8,v9
v_mfma_f32_16x16x32_bf16 v[56:59],a[200:203],a[36:39],v[16:19]
s_add_u32 m0,s57,10608
ds_read_u16 v44,v44
v_lshlrev_b32_sdwa v46,1,v46 dst_sel:DWORD dst_unused:UNUSED_PAD src0_sel:DWORD src1_sel:WORD_1
v_mfma_f32_16x16x32_bf16 v[56:59],a[192:195],a[20:23],v[56:59]
s_add_u32 s89,s56,2752512
ds_read_u16_d16_hi v46,v46
v_lshlrev_b32_sdwa v45,1,v45 dst_sel:DWORD dst_unused:UNUSED_PAD src0_sel:DWORD src1_sel:WORD_1
v_mfma_f32_16x16x32_bf16 v[56:59],a[196:199],a[4:7],v[56:59]
v_exp_f16_sdwa v8,v9 dst_sel:WORD_1 dst_unused:UNUSED_PRESERVE src0_sel:WORD_1
v_mfma_f32_16x16x32_bf16 v[136:139],a[236:239],v[24:27],v[136:139]
buffer_load_dwordx4 v6,s[36:39],s89 offen lds
v_lshlrev_b32_sdwa v47,1,v47 dst_sel:DWORD dst_unused:UNUSED_PAD src0_sel:DWORD src1_sel:WORD_1
v_mfma_f32_16x16x32_bf16 v[140:143],a[236:239],v[28:31],v[140:143]
s_add_u32 m0,s58,0
ds_read_u16 v45,v45
v_lshlrev_b32_sdwa v36,1,v36 dst_sel:DWORD dst_unused:UNUSED_PAD src0_sel:DWORD src1_sel:WORD_1
v_mfma_f32_16x16x32_bf16 v[68:71],a[244:247],v[28:31],v[68:71]
s_add_u32 s69,s69,s59
ds_read_u16_d16_hi v47,v47
v_pk_fma_f16 v8,v8,s55,s55
v_mfma_f32_16x16x32_bf16 v[64:67],a[244:247],v[24:27],v[64:67]
v_rcp_f16 v9,v8
v_lshlrev_b32_sdwa v38,1,v38 dst_sel:DWORD dst_unused:UNUSED_PAD src0_sel:DWORD src1_sel:WORD_1
v_mfma_f32_16x16x32_bf16 v[144:147],a[248:251],v[24:27],v[144:147]
buffer_load_dwordx4 v5,s[44:47],s69 offen offset:0 lds
v_lshlrev_b32_sdwa v37,1,v37 dst_sel:DWORD dst_unused:UNUSED_PAD src0_sel:DWORD src1_sel:WORD_1
v_mfma_f32_16x16x32_bf16 v[148:151],a[248:251],v[28:31],v[148:151]
s_waitcnt lgkmcnt(4)
ds_read_u16 v36,v36
v_lshlrev_b32_sdwa v39,1,v39 dst_sel:DWORD dst_unused:UNUSED_PAD src0_sel:DWORD src1_sel:WORD_1
v_mfma_f32_16x16x32_bf16 v[84:87],a[252:255],v[28:31],v[84:87]
s_add_u32 s69,s69,s53
ds_read_u16_d16_hi v38,v38
v_lshlrev_b32_sdwa v40,1,v40 dst_sel:DWORD dst_unused:UNUSED_PAD src0_sel:DWORD src1_sel:WORD_1
v_mfma_f32_16x16x32_bf16 v[80:83],a[252:255],v[24:27],v[80:83]
v_rcp_f16_sdwa v9,v8 dst_sel:WORD_1 dst_unused:UNUSED_PRESERVE src0_sel:WORD_1
v_mfma_f32_16x16x32_bf16 v[152:155],a[240:243],v[24:27],v[152:155]
buffer_load_dwordx4 v5,s[44:47],s69 offen offset:1024 lds
ds_read_u16 v37,v37
v_mfma_f32_16x16x32_bf16 v[156:159],a[240:243],v[28:31],v[156:159]
s_add_u32 s69,s69,s53
ds_read_u16_d16_hi v39,v39
v_lshlrev_b32_sdwa v42,1,v42 dst_sel:DWORD dst_unused:UNUSED_PAD src0_sel:DWORD src1_sel:WORD_1
v_mfma_f32_16x16x32_bf16 v[100:103],a[224:227],v[28:31],v[100:103]
buffer_load_dwordx4 v5,s[44:47],s69 offen offset:2048 lds
v_fma_mix_f32 v41,v41,v9,0 op_sel_hi:[0,1,0]
v_mfma_f32_16x16x32_bf16 v[96:99],a[224:227],v[24:27],v[96:99]
ds_read_u16 v40,v40
ds_read_u16_d16_hi v42,v42
v_fma_mix_f32 v43,v43,v9,0 op_sel:[0,1,0] op_sel_hi:[0,1,0]
v_mfma_f32_16x16x32_bf16 v[28:31],a[196:199],a[8:11],v[16:19]
ds_read_b64 v[14:15],v7 offset:12320
ds_read_b64_tr_b16 a[220:221],v1 offset:12320
v_lshlrev_b32_sdwa v32,1,v32 dst_sel:DWORD dst_unused:UNUSED_PAD src0_sel:DWORD src1_sel:WORD_1
v_mfma_f32_16x16x32_bf16 v[28:31],a[192:195],a[24:27],v[28:31]
ds_read_b64_tr_b16 a[222:223],v1 offset:13344
ds_read_b64_tr_b16 a[216:217],v1 offset:14368
v_fma_mixlo_f16 v8,v53,s99,0
v_mfma_f32_16x16x32_bf16 v[28:31],a[200:203],a[40:43],v[28:31]
ds_read_b64_tr_b16 a[218:219],v1 offset:15392
v_lshl_add_u64 v[48:49],v[48:49],0,v[50:51]
v_lshlrev_b32_sdwa v34,1,v34 dst_sel:DWORD dst_unused:UNUSED_PAD src0_sel:DWORD src1_sel:WORD_1
v_mfma_f32_16x16x32_bf16 v[24:27],a[204:207],a[40:43],v[20:23]
s_waitcnt lgkmcnt(9)
ds_read_u16 v32,v32
v_lshl_add_u64 v[50:51],v[44:45],0,v[46:47]
v_fma_mixhi_f16 v8,v55,s99,0
v_mfma_f32_16x16x32_bf16 v[24:27],a[208:211],a[24:27],v[24:27]
s_waitcnt lgkmcnt(8)
ds_read_u16_d16_hi v34,v34
v_lshl_add_u64 v[44:45],v[36:37],0,v[38:39]
v_lshlrev_b32_sdwa v33,1,v33 dst_sel:DWORD dst_unused:UNUSED_PAD src0_sel:DWORD src1_sel:WORD_1
v_pk_fma_f16 v9,v8,v8,s54
v_mfma_f32_16x16x32_bf16 v[24:27],a[212:215],a[8:11],v[24:27]
s_waitcnt lgkmcnt(6)
ds_read_u16 v33,v33
v_or3_b32 v252,v14,v15,v252
v_lshlrev_b32_sdwa v35,1,v35 dst_sel:DWORD dst_unused:UNUSED_PAD src0_sel:DWORD src1_sel:WORD_1
v_mfma_f32_16x16x32_bf16 v[36:39],a[212:215],a[12:15],v[20:23]
s_add_u32 m0,s57,14144
ds_read_b64 v[14:15],v7 offset:12832
ds_read_b128 v[20:23],v4 offset:384
v_or_b32 v46,v40,v42
v_pk_mul_f16 v9,v8,v9
v_mfma_f32_16x16x32_bf16 v[36:39],a[208:211],a[28:31],v[36:39]
s_add_u32 s89,s56,2850816
ds_read_u16_d16_hi v35,v35
ds_read_b64_tr_b16 a[212:213],v1 offset:16416
v_cvt_pk_bf16_f32 v47,v41,v43
v_lshlrev_b32_sdwa v60,1,v60 dst_sel:DWORD dst_unused:UNUSED_PAD src0_sel:DWORD src1_sel:WORD_1
v_mfma_f32_16x16x32_bf16 v[36:39],a[204:207],a[44:47],v[36:39]
ds_read_b64_tr_b16 a[214:215],v1 offset:17440
v_exp_f16 v8,v9
v_mfma_f32_16x16x32_bf16 v[40:43],a[200:203],a[44:47],v[16:19]
buffer_load_dwordx4 v6,s[36:39],s89 offen lds
ds_read_b128 v[16:19],v4 offset:416
v_lshlrev_b32_sdwa v62,1,v62 dst_sel:DWORD dst_unused:UNUSED_PAD src0_sel:DWORD src1_sel:WORD_1
v_mfma_f32_16x16x32_bf16 v[40:43],a[192:195],a[28:31],v[40:43]
ds_read_b64_tr_b16 a[208:209],v2 offset:16416
ds_read_b64_tr_b16 a[210:211],v2 offset:17440
v_lshlrev_b32_sdwa v61,1,v61 dst_sel:DWORD dst_unused:UNUSED_PAD src0_sel:DWORD src1_sel:WORD_1
v_mfma_f32_16x16x32_bf16 v[40:43],a[196:199],a[12:15],v[40:43]
s_waitcnt lgkmcnt(7)
ds_read_u16 v60,v60
ds_read_u16_d16_hi v62,v62
v_or3_b32 v252,v14,v15,v252
v_mfma_f32_16x16x32_bf16 v[112:115],a[236:239],v[48:51],v[112:115]
ds_read_b64 v[14:15],v7 offset:13344
ds_read_u16 v61,v61
v_exp_f16_sdwa v8,v9 dst_sel:WORD_1 dst_unused:UNUSED_PRESERVE src0_sel:WORD_1
v_lshlrev_b32_sdwa v63,1,v63 dst_sel:DWORD dst_unused:UNUSED_PAD src0_sel:DWORD src1_sel:WORD_1
v_mfma_f32_16x16x32_bf16 v[116:119],a[236:239],v[44:47],v[116:119]
ds_read_u16_d16_hi v63,v63
ds_read_b64_tr_b16 a[204:205],v2 offset:14368
v_lshlrev_b32_sdwa v56,1,v56 dst_sel:DWORD dst_unused:UNUSED_PAD src0_sel:DWORD src1_sel:WORD_1
v_mfma_f32_16x16x32_bf16 v[76:79],a[244:247],v[44:47],v[76:79]
ds_read_b64_tr_b16 a[206:207],v2 offset:15392
ds_read_u16 v56,v56
v_pk_fma_f16 v8,v8,s55,s55
v_mfma_f32_16x16x32_bf16 v[72:75],a[244:247],v[48:51],v[72:75]
ds_read_b64_tr_b16 a[200:201],v2 offset:12320
v_rcp_f16 v9,v8
v_lshlrev_b32_sdwa v58,1,v58 dst_sel:DWORD dst_unused:UNUSED_PAD src0_sel:DWORD src1_sel:WORD_1
v_mfma_f32_16x16x32_bf16 v[120:123],a[248:251],v[48:51],v[120:123]
ds_read_u16_d16_hi v58,v58
ds_read_b64_tr_b16 a[202:203],v2 offset:13344
v_mfma_f32_16x16x32_bf16 v[124:127],a[248:251],v[44:47],v[124:127]
s_waitcnt lgkmcnt(8)
ds_read_b64_tr_b16 a[228:229],v3 offset:12320
ds_read_b64_tr_b16 a[230:231],v3 offset:13856
v_or3_b32 v252,v14,v15,v252
v_lshlrev_b32_sdwa v57,1,v57 dst_sel:DWORD dst_unused:UNUSED_PAD src0_sel:DWORD src1_sel:WORD_1
v_mfma_f32_16x16x32_bf16 v[92:95],a[252:255],v[44:47],v[92:95]
TE01_%=:
s_cbranch_execz TP3865_%=
ds_read_u16 v57,v57
ds_read_b64_tr_b16 a[232:233],v3 offset:12328
v_lshlrev_b32_sdwa v59,1,v59 dst_sel:DWORD dst_unused:UNUSED_PAD src0_sel:DWORD src1_sel:WORD_1
v_mfma_f32_16x16x32_bf16 v[88:91],a[252:255],v[48:51],v[88:91]
ds_read_u16_d16_hi v59,v59
v_rcp_f16_sdwa v9,v8 dst_sel:WORD_1 dst_unused:UNUSED_PRESERVE src0_sel:WORD_1
v_mfma_f32_16x16x32_bf16 v[128:131],a[240:243],v[48:51],v[128:131]
ds_read_b64_tr_b16 a[234:235],v3 offset:13864
ds_read_b64_tr_b16 a[236:237],v3 offset:12384
v_lshlrev_b32_sdwa v52,1,v52 dst_sel:DWORD dst_unused:UNUSED_PAD src0_sel:DWORD src1_sel:WORD_1
v_mfma_f32_16x16x32_bf16 v[132:135],a[240:243],v[44:47],v[132:135]
ds_read_u16 v52,v52
ds_read_b64_tr_b16 a[238:239],v3 offset:13920
v_lshlrev_b32_sdwa v54,1,v54 dst_sel:DWORD dst_unused:UNUSED_PAD src0_sel:DWORD src1_sel:WORD_1
v_mfma_f32_16x16x32_bf16 v[108:111],a[224:227],v[44:47],v[108:111]
s_add_u32 m0,s57,17688
ds_read_u16_d16_hi v54,v54
ds_read_b64_tr_b16 a[244:245],v3 offset:12392
v_fma_mix_f32 v53,v53,v9,0 op_sel_hi:[0,1,0]
v_mfma_f32_16x16x32_bf16 v[104:107],a[224:227],v[48:51],v[104:107]
s_add_u32 s89,s56,2949120
ds_read_b64_tr_b16 a[246:247],v3 offset:13928
v_fma_mix_f32 v55,v55,v9,0 op_sel:[0,1,0] op_sel_hi:[0,1,0]
v_mfma_f32_16x16x32_bf16 v[44:47],a[220:223],a[0:3],v[20:23]
buffer_load_dwordx4 v6,s[36:39],s89 offen lds
v_lshlrev_b32_sdwa v28,1,v28 dst_sel:DWORD dst_unused:UNUSED_PAD src0_sel:DWORD src1_sel:WORD_1
v_mfma_f32_16x16x32_bf16 v[44:47],a[216:219],a[16:19],v[44:47]
ds_read_b64_tr_b16 a[248:249],v3 offset:12448
v_fma_mixlo_f16 v8,v37,s99,0
v_mfma_f32_16x16x32_bf16 v[44:47],a[212:215],a[32:35],v[44:47]
ds_read_b64_tr_b16 a[250:251],v3 offset:13984
v_lshl_add_u64 v[32:33],v[32:33],0,v[34:35]
v_lshlrev_b32_sdwa v30,1,v30 dst_sel:DWORD dst_unused:UNUSED_PAD src0_sel:DWORD src1_sel:WORD_1
v_mfma_f32_16x16x32_bf16 v[48:51],a[208:211],a[32:35],v[16:19]
s_waitcnt lgkmcnt(10)
ds_read_b64_tr_b16 a[252:253],v3 offset:12456
v_lshl_add_u64 v[34:35],v[60:61],0,v[62:63]
v_fma_mixhi_f16 v8,v39,s99,0
v_mfma_f32_16x16x32_bf16 v[48:51],a[204:207],a[16:19],v[48:51]
s_waitcnt lgkmcnt(10)
ds_read_b64_tr_b16 a[254:255],v3 offset:13992
v_lshl_add_u64 v[60:61],v[56:57],0,v[58:59]
v_lshlrev_b32_sdwa v29,1,v29 dst_sel:DWORD dst_unused:UNUSED_PAD src0_sel:DWORD src1_sel:WORD_1
v_pk_fma_f16 v9,v8,v8,s54
v_mfma_f32_16x16x32_bf16 v[48:51],a[200:203],a[0:3],v[48:51]
ds_read_u16 v28,v28
v_lshlrev_b32_sdwa v31,1,v31 dst_sel:DWORD dst_unused:UNUSED_PAD src0_sel:DWORD src1_sel:WORD_1
v_mfma_f32_16x16x32_bf16 v[56:59],a[200:203],a[4:7],v[16:19]
s_waitcnt lgkmcnt(7)
ds_read_u16_d16_hi v30,v30
v_or_b32 v62,v52,v54
v_pk_mul_f16 v9,v8,v9
v_mfma_f32_16x16x32_bf16 v[56:59],a[204:207],a[20:23],v[56:59]
s_waitcnt vmcnt(8)
ds_read_u16 v29,v29
v_cvt_pk_bf16_f32 v63,v53,v55
v_lshlrev_b32_sdwa v24,1,v24 dst_sel:DWORD dst_unused:UNUSED_PAD src0_sel:DWORD src1_sel:WORD_1
v_mfma_f32_16x16x32_bf16 v[56:59],a[208:211],a[36:39],v[56:59]
s_barrier
v_exp_f16 v8,v9
v_mfma_f32_16x16x32_bf16 v[52:55],a[212:215],a[36:39],v[20:23]
ds_read_u16_d16_hi v31,v31
v_lshlrev_b32_sdwa v26,1,v26 dst_sel:DWORD dst_unused:UNUSED_PAD src0_sel:DWORD src1_sel:WORD_1
v_mfma_f32_16x16x32_bf16 v[52:55],a[216:219],a[20:23],v[52:55]
ds_read_u16 v24,v24
v_lshlrev_b32_sdwa v25,1,v25 dst_sel:DWORD dst_unused:UNUSED_PAD src0_sel:DWORD src1_sel:WORD_1
v_mfma_f32_16x16x32_bf16 v[52:55],a[220:223],a[4:7],v[52:55]
s_add_u32 m0,s58,6160
v_exp_f16_sdwa v8,v9 dst_sel:WORD_1 dst_unused:UNUSED_PRESERVE src0_sel:WORD_1
v_mfma_f32_16x16x32_bf16 v[136:139],a[228:231],v[32:35],v[136:139]
s_add_u32 s69,s69,s59
ds_read_u16_d16_hi v26,v26
v_lshlrev_b32_sdwa v27,1,v27 dst_sel:DWORD dst_unused:UNUSED_PAD src0_sel:DWORD src1_sel:WORD_1
v_mfma_f32_16x16x32_bf16 v[140:143],a[228:231],v[60:63],v[140:143]
buffer_load_dwordx4 v5,s[44:47],s69 offen offset:0 lds
v_lshlrev_b32_sdwa v40,1,v40 dst_sel:DWORD dst_unused:UNUSED_PAD src0_sel:DWORD src1_sel:WORD_1
v_mfma_f32_16x16x32_bf16 v[68:71],a[232:235],v[60:63],v[68:71]
ds_read_u16 v25,v25
v_pk_fma_f16 v8,v8,s55,s55
v_mfma_f32_16x16x32_bf16 v[64:67],a[232:235],v[32:35],v[64:67]
v_rcp_f16 v9,v8
v_lshlrev_b32_sdwa v42,1,v42 dst_sel:DWORD dst_unused:UNUSED_PAD src0_sel:DWORD src1_sel:WORD_1
v_mfma_f32_16x16x32_bf16 v[144:147],a[236:239],v[32:35],v[144:147]
s_add_u32 s69,s69,s53
ds_read_u16_d16_hi v27,v27
v_lshlrev_b32_sdwa v41,1,v41 dst_sel:DWORD dst_unused:UNUSED_PAD src0_sel:DWORD src1_sel:WORD_1
v_mfma_f32_16x16x32_bf16 v[148:151],a[236:239],v[60:63],v[148:151]
s_waitcnt lgkmcnt(5)
buffer_load_dwordx4 v5,s[44:47],s69 offen offset:1024 lds
v_lshlrev_b32_sdwa v43,1,v43 dst_sel:DWORD dst_unused:UNUSED_PAD src0_sel:DWORD src1_sel:WORD_1
v_mfma_f32_16x16x32_bf16 v[84:87],a[244:247],v[60:63],v[84:87]
s_add_u32 m0,s57,21224
ds_read_u16 v40,v40
v_lshlrev_b32_sdwa v36,1,v36 dst_sel:DWORD dst_unused:UNUSED_PAD src0_sel:DWORD src1_sel:WORD_1
v_mfma_f32_16x16x32_bf16 v[80:83],a[244:247],v[32:35],v[80:83]
s_add_u32 s89,s56,3047424
v_rcp_f16_sdwa v9,v8 dst_sel:WORD_1 dst_unused:UNUSED_PRESERVE src0_sel:WORD_1
v_mfma_f32_16x16x32_bf16 v[152:155],a[248:251],v[32:35],v[152:155]
buffer_load_dwordx4 v6,s[36:39],s89 offen lds
ds_read_u16_d16_hi v42,v42
v_mfma_f32_16x16x32_bf16 v[156:159],a[248:251],v[60:63],v[156:159]
s_add_u32 m0,s58,6160
ds_read_u16 v41,v41
ds_read_u16_d16_hi v43,v43
v_lshlrev_b32_sdwa v38,1,v38 dst_sel:DWORD dst_unused:UNUSED_PAD src0_sel:DWORD src1_sel:WORD_1
v_mfma_f32_16x16x32_bf16 v[100:103],a[252:255],v[60:63],v[100:103]
s_add_u32 s69,s69,s53
ds_read_u16 v36,v36
v_fma_mix_f32 v37,v37,v9,0 op_sel_hi:[0,1,0]
v_mfma_f32_16x16x32_bf16 v[96:99],a[252:255],v[32:35],v[96:99]
buffer_load_dwordx4 v5,s[44:47],s69 offen offset:2048 lds
ds_read_u16_d16_hi v38,v38
v_fma_mix_f32 v39,v39,v9,0 op_sel:[0,1,0] op_sel_hi:[0,1,0]
v_mfma_f32_16x16x32_bf16 v[60:63],a[220:223],a[8:11],v[20:23]
ds_read_b64 v[14:15],v7 offset:18480
ds_read_b64_tr_b16 a[192:193],v1 offset:18480
v_lshlrev_b32_sdwa v44,1,v44 dst_sel:DWORD dst_unused:UNUSED_PAD src0_sel:DWORD src1_sel:WORD_1
v_mfma_f32_16x16x32_bf16 v[60:63],a[216:219],a[24:27],v[60:63]
ds_read_b64_tr_b16 a[194:195],v1 offset:19504
ds_read_b64_tr_b16 a[196:197],v1 offset:20528
v_fma_mixlo_f16 v8,v57,s99,0
v_mfma_f32_16x16x32_bf16 v[60:63],a[212:215],a[40:43],v[60:63]
s_waitcnt lgkmcnt(10)
ds_read_b64_tr_b16 a[198:199],v1 offset:21552
v_lshl_add_u64 v[28:29],v[28:29],0,v[30:31]
v_lshlrev_b32_sdwa v46,1,v46 dst_sel:DWORD dst_unused:UNUSED_PAD src0_sel:DWORD src1_sel:WORD_1
v_mfma_f32_16x16x32_bf16 v[32:35],a[208:211],a[40:43],v[16:19]
ds_read_u16 v44,v44
v_lshl_add_u64 v[30:31],v[24:25],0,v[26:27]
v_fma_mixhi_f16 v8,v59,s99,0
v_mfma_f32_16x16x32_bf16 v[32:35],a[204:207],a[24:27],v[32:35]
s_waitcnt lgkmcnt(8)
ds_read_u16_d16_hi v46,v46
v_lshl_add_u64 v[24:25],v[40:41],0,v[42:43]
v_lshlrev_b32_sdwa v45,1,v45 dst_sel:DWORD dst_unused:UNUSED_PAD src0_sel:DWORD src1_sel:WORD_1
v_pk_fma_f16 v9,v8,v8,s54
v_mfma_f32_16x16x32_bf16 v[32:35],a[200:203],a[8:11],v[32:35]
s_waitcnt lgkmcnt(6)
ds_read_u16 v45,v45
v_or3_b32 v252,v14,v15,v252
v_lshlrev_b32_sdwa v47,1,v47 dst_sel:DWORD dst_unused:UNUSED_PAD src0_sel:DWORD src1_sel:WORD_1
v_mfma_f32_16x16x32_bf16 v[40:43],a[200:203],a[12:15],v[16:19]
ds_read_b64 v[14:15],v7 offset:18992
ds_read_b128 v[16:19],v4 offset:512
v_or_b32 v26,v36,v38
v_pk_mul_f16 v9,v8,v9
v_mfma_f32_16x16x32_bf16 v[40:43],a[204:207],a[28:31],v[40:43]
ds_read_u16_d16_hi v47,v47
ds_read_b64_tr_b16 a[200:201],v1 offset:22576
v_cvt_pk_bf16_f32 v27,v37,v39
v_lshlrev_b32_sdwa v48,1,v48 dst_sel:DWORD dst_unused:UNUSED_PAD src0_sel:DWORD src1_sel:WORD_1
v_mfma_f32_16x16x32_bf16 v[40:43],a[208:211],a[44:47],v[40:43]
ds_read_b64_tr_b16 a[202:203],v1 offset:23600
v_exp_f16 v8,v9
v_mfma_f32_16x16x32_bf16 v[36:39],a[212:215],a[44:47],v[20:23]
ds_read_b128 v[20:23],v4 offset:544
ds_read_b64_tr_b16 a[204:205],v2 offset:22576
v_lshlrev_b32_sdwa v50,1,v50 dst_sel:DWORD dst_unused:UNUSED_PAD src0_sel:DWORD src1_sel:WORD_1
v_mfma_f32_16x16x32_bf16 v[36:39],a[216:219],a[28:31],v[36:39]
ds_read_b64_tr_b16 a[206:207],v2 offset:23600
ds_read_u16 v48,v48
v_lshlrev_b32_sdwa v49,1,v49 dst_sel:DWORD dst_unused:UNUSED_PAD src0_sel:DWORD src1_sel:WORD_1
v_mfma_f32_16x16x32_bf16 v[36:39],a[220:223],a[12:15],v[36:39]
s_waitcnt lgkmcnt(8)
ds_read_u16_d16_hi v50,v50
ds_read_u16 v49,v49
v_or3_b32 v252,v14,v15,v252
v_mfma_f32_16x16x32_bf16 v[112:115],a[228:231],v[28:31],v[112:115]
ds_read_b64 v[14:15],v7 offset:19504
ds_read_b64_tr_b16 a[208:209],v2 offset:20528
v_exp_f16_sdwa v8,v9 dst_sel:WORD_1 dst_unused:UNUSED_PRESERVE src0_sel:WORD_1
v_lshlrev_b32_sdwa v51,1,v51 dst_sel:DWORD dst_unused:UNUSED_PAD src0_sel:DWORD src1_sel:WORD_1
v_mfma_f32_16x16x32_bf16 v[116:119],a[228:231],v[24:27],v[116:119]
s_add_u32 m0,s57,24752
ds_read_u16_d16_hi v51,v51
ds_read_b64_tr_b16 a[210:211],v2 offset:21552
v_lshlrev_b32_sdwa v52,1,v52 dst_sel:DWORD dst_unused:UNUSED_PAD src0_sel:DWORD src1_sel:WORD_1
v_mfma_f32_16x16x32_bf16 v[76:79],a[232:235],v[24:27],v[76:79]
s_add_u32 s89,s56,3145728
ds_read_u16 v52,v52
ds_read_b64_tr_b16 a[212:213],v2 offset:18480
v_pk_fma_f16 v8,v8,s55,s55
v_mfma_f32_16x16x32_bf16 v[72:75],a[232:235],v[28:31],v[72:75]
ds_read_b64_tr_b16 a[214:215],v2 offset:19504
v_rcp_f16 v9,v8
v_lshlrev_b32_sdwa v54,1,v54 dst_sel:DWORD dst_unused:UNUSED_PAD src0_sel:DWORD src1_sel:WORD_1
v_mfma_f32_16x16x32_bf16 v[120:123],a[236:239],v[28:31],v[120:123]
buffer_load_dwordx4 v6,s[36:39],s89 offen lds
ds_read_u16_d16_hi v54,v54
v_mfma_f32_16x16x32_bf16 v[124:127],a[236:239],v[24:27],v[124:127]
s_waitcnt lgkmcnt(7)
ds_read_b64_tr_b16 a[240:241],v3 offset:18480
ds_read_b64_tr_b16 a[242:243],v3 offset:20016
v_or3_b32 v252,v14,v15,v252
v_lshlrev_b32_sdwa v53,1,v53 dst_sel:DWORD dst_unused:UNUSED_PAD src0_sel:DWORD src1_sel:WORD_1
v_mfma_f32_16x16x32_bf16 v[92:95],a[244:247],v[24:27],v[92:95]
ds_read_u16 v53,v53
ds_read_b64_tr_b16 a[224:225],v3 offset:18488
v_lshlrev_b32_sdwa v55,1,v55 dst_sel:DWORD dst_unused:UNUSED_PAD src0_sel:DWORD src1_sel:WORD_1
v_mfma_f32_16x16x32_bf16 v[88:91],a[244:247],v[28:31],v[88:91]
ds_read_u16_d16_hi v55,v55
v_rcp_f16_sdwa v9,v8 dst_sel:WORD_1 dst_unused:UNUSED_PRESERVE src0_sel:WORD_1
v_mfma_f32_16x16x32_bf16 v[128:131],a[248:251],v[28:31],v[128:131]
ds_read_b64_tr_b16 a[226:227],v3 offset:20024
ds_read_b64_tr_b16 a[228:229],v3 offset:18544
v_lshlrev_b32_sdwa v56,1,v56 dst_sel:DWORD dst_unused:UNUSED_PAD src0_sel:DWORD src1_sel:WORD_1
v_mfma_f32_16x16x32_bf16 v[132:135],a[248:251],v[24:27],v[132:135]
ds_read_u16 v56,v56
ds_read_b64_tr_b16 a[230:231],v3 offset:20080
v_lshlrev_b32_sdwa v58,1,v58 dst_sel:DWORD dst_unused:UNUSED_PAD src0_sel:DWORD src1_sel:WORD_1
v_mfma_f32_16x16x32_bf16 v[108:111],a[252:255],v[24:27],v[108:111]
ds_read_u16_d16_hi v58,v58
ds_read_b64_tr_b16 a[232:233],v3 offset:18552
v_fma_mix_f32 v57,v57,v9,0 op_sel_hi:[0,1,0]
v_mfma_f32_16x16x32_bf16 v[104:107],a[252:255],v[28:31],v[104:107]
ds_read_b64_tr_b16 a[234:235],v3 offset:20088
v_fma_mix_f32 v59,v59,v9,0 op_sel:[0,1,0] op_sel_hi:[0,1,0]
v_mfma_f32_16x16x32_bf16 v[24:27],a[192:195],a[0:3],v[16:19]
ds_read_b64_tr_b16 a[236:237],v3 offset:18608
v_lshlrev_b32_sdwa v60,1,v60 dst_sel:DWORD dst_unused:UNUSED_PAD src0_sel:DWORD src1_sel:WORD_1
v_mfma_f32_16x16x32_bf16 v[24:27],a[196:199],a[16:19],v[24:27]
ds_read_b64_tr_b16 a[238:239],v3 offset:20144
v_fma_mixlo_f16 v8,v41,s99,0
v_mfma_f32_16x16x32_bf16 v[24:27],a[200:203],a[32:35],v[24:27]
ds_read_b64_tr_b16 a[244:245],v3 offset:18616
v_lshl_add_u64 v[44:45],v[44:45],0,v[46:47]
v_lshlrev_b32_sdwa v62,1,v62 dst_sel:DWORD dst_unused:UNUSED_PAD src0_sel:DWORD src1_sel:WORD_1
v_mfma_f32_16x16x32_bf16 v[28:31],a[204:207],a[32:35],v[20:23]
s_waitcnt lgkmcnt(11)
ds_read_b64_tr_b16 a[246:247],v3 offset:20152
v_lshl_add_u64 v[46:47],v[48:49],0,v[50:51]
v_fma_mixhi_f16 v8,v43,s99,0
v_mfma_f32_16x16x32_bf16 v[28:31],a[208:211],a[16:19],v[28:31]
s_waitcnt lgkmcnt(11)
ds_read_u16 v60,v60
v_lshl_add_u64 v[48:49],v[52:53],0,v[54:55]
v_lshlrev_b32_sdwa v61,1,v61 dst_sel:DWORD dst_unused:UNUSED_PAD src0_sel:DWORD src1_sel:WORD_1
v_pk_fma_f16 v9,v8,v8,s54
v_mfma_f32_16x16x32_bf16 v[28:31],a[212:215],a[0:3],v[28:31]
ds_read_u16_d16_hi v62,v62
v_lshlrev_b32_sdwa v63,1,v63 dst_sel:DWORD dst_unused:UNUSED_PAD src0_sel:DWORD src1_sel:WORD_1
v_mfma_f32_16x16x32_bf16 v[52:55],a[212:215],a[4:7],v[20:23]
s_waitcnt lgkmcnt(8)
ds_read_u16 v61,v61
v_or_b32 v50,v56,v58
v_pk_mul_f16 v9,v8,v9
v_mfma_f32_16x16x32_bf16 v[52:55],a[208:211],a[20:23],v[52:55]
s_waitcnt vmcnt(7)
ds_read_u16_d16_hi v63,v63
v_cvt_pk_bf16_f32 v51,v57,v59
v_lshlrev_b32_sdwa v32,1,v32 dst_sel:DWORD dst_unused:UNUSED_PAD src0_sel:DWORD src1_sel:WORD_1
v_mfma_f32_16x16x32_bf16 v[52:55],a[204:207],a[36:39],v[52:55]
s_barrier
v_exp_f16 v8,v9
v_mfma_f32_16x16x32_bf16 v[56:59],a[200:203],a[36:39],v[16:19]
s_add_u32 m0,s57,28288
ds_read_u16 v32,v32
v_lshlrev_b32_sdwa v34,1,v34 dst_sel:DWORD dst_unused:UNUSED_PAD src0_sel:DWORD src1_sel:WORD_1
v_mfma_f32_16x16x32_bf16 v[56:59],a[196:199],a[20:23],v[56:59]
s_add_u32 s89,s56,3244032
ds_read_u16_d16_hi v34,v34
v_lshlrev_b32_sdwa v33,1,v33 dst_sel:DWORD dst_unused:UNUSED_PAD src0_sel:DWORD src1_sel:WORD_1
v_mfma_f32_16x16x32_bf16 v[56:59],a[192:195],a[4:7],v[56:59]
v_exp_f16_sdwa v8,v9 dst_sel:WORD_1 dst_unused:UNUSED_PRESERVE src0_sel:WORD_1
v_mfma_f32_16x16x32_bf16 v[136:139],a[240:243],v[44:47],v[136:139]
buffer_load_dwordx4 v6,s[36:39],s89 offen lds
v_lshlrev_b32_sdwa v35,1,v35 dst_sel:DWORD dst_unused:UNUSED_PAD src0_sel:DWORD src1_sel:WORD_1
v_mfma_f32_16x16x32_bf16 v[140:143],a[240:243],v[48:51],v[140:143]
s_add_u32 m0,s58,12320
ds_read_u16 v33,v33
v_lshlrev_b32_sdwa v36,1,v36 dst_sel:DWORD dst_unused:UNUSED_PAD src0_sel:DWORD src1_sel:WORD_1
v_mfma_f32_16x16x32_bf16 v[68:71],a[224:227],v[48:51],v[68:71]
s_add_u32 s69,s69,s59
ds_read_u16_d16_hi v35,v35
v_pk_fma_f16 v8,v8,s55,s55
v_mfma_f32_16x16x32_bf16 v[64:67],a[224:227],v[44:47],v[64:67]
v_rcp_f16 v9,v8
v_lshlrev_b32_sdwa v38,1,v38 dst_sel:DWORD dst_unused:UNUSED_PAD src0_sel:DWORD src1_sel:WORD_1
v_mfma_f32_16x16x32_bf16 v[144:147],a[228:231],v[44:47],v[144:147]
buffer_load_dwordx4 v5,s[44:47],s69 offen offset:0 lds
v_lshlrev_b32_sdwa v37,1,v37 dst_sel:DWORD dst_unused:UNUSED_PAD src0_sel:DWORD src1_sel:WORD_1
v_mfma_f32_16x16x32_bf16 v[148:151],a[228:231],v[48:51],v[148:151]
s_waitcnt lgkmcnt(4)
ds_read_u16 v36,v36
v_lshlrev_b32_sdwa v39,1,v39 dst_sel:DWORD dst_unused:UNUSED_PAD src0_sel:DWORD src1_sel:WORD_1
v_mfma_f32_16x16x32_bf16 v[84:87],a[232:235],v[48:51],v[84:87]
s_add_u32 s69,s69,s53
ds_read_u16_d16_hi v38,v38
v_lshlrev_b32_sdwa v40,1,v40 dst_sel:DWORD dst_unused:UNUSED_PAD src0_sel:DWORD src1_sel:WORD_1
v_mfma_f32_16x16x32_bf16 v[80:83],a[232:235],v[44:47],v[80:83]
v_rcp_f16_sdwa v9,v8 dst_sel:WORD_1 dst_unused:UNUSED_PRESERVE src0_sel:WORD_1
v_mfma_f32_16x16x32_bf16 v[152:155],a[236:239],v[44:47],v[152:155]
buffer_load_dwordx4 v5,s[44:47],s69 offen offset:1024 lds
ds_read_u16 v37,v37
v_mfma_f32_16x16x32_bf16 v[156:159],a[236:239],v[48:51],v[156:159]
s_add_u32 s69,s69,s53
ds_read_u16_d16_hi v39,v39
v_lshlrev_b32_sdwa v42,1,v42 dst_sel:DWORD dst_unused:UNUSED_PAD src0_sel:DWORD src1_sel:WORD_1
v_mfma_f32_16x16x32_bf16 v[100:103],a[244:247],v[48:51],v[100:103]
buffer_load_dwordx4 v5,s[44:47],s69 offen offset:2048 lds
v_fma_mix_f32 v41,v41,v9,0 op_sel_hi:[0,1,0]
v_mfma_f32_16x16x32_bf16 v[96:99],a[244:247],v[44:47],v[96:99]
ds_read_u16 v40,v40
ds_read_u16_d16_hi v42,v42
v_fma_mix_f32 v43,v43,v9,0 op_sel:[0,1,0] op_sel_hi:[0,1,0]
v_mfma_f32_16x16x32_bf16 v[48:51],a[192:195],a[8:11],v[16:19]
ds_read_b64 v[14:15],v7 offset:0
ds_read_b64_tr_b16 a[216:217],v1 offset:0
v_lshlrev_b32_sdwa v24,1,v24 dst_sel:DWORD dst_unused:UNUSED_PAD src0_sel:DWORD src1_sel:WORD_1
v_mfma_f32_16x16x32_bf16 v[48:51],a[196:199],a[24:27],v[48:51]
ds_read_b64_tr_b16 a[218:219],v1 offset:1024
ds_read_b64_tr_b16 a[220:221],v1 offset:2048
v_fma_mixlo_f16 v8,v53,s99,0
v_mfma_f32_16x16x32_bf16 v[48:51],a[200:203],a[40:43],v[48:51]
ds_read_b64_tr_b16 a[222:223],v1 offset:3072
v_lshl_add_u64 v[60:61],v[60:61],0,v[62:63]
v_lshlrev_b32_sdwa v26,1,v26 dst_sel:DWORD dst_unused:UNUSED_PAD src0_sel:DWORD src1_sel:WORD_1
v_mfma_f32_16x16x32_bf16 v[44:47],a[204:207],a[40:43],v[20:23]
s_waitcnt lgkmcnt(9)
ds_read_u16 v24,v24
v_lshl_add_u64 v[62:63],v[32:33],0,v[34:35]
v_fma_mixhi_f16 v8,v55,s99,0
v_mfma_f32_16x16x32_bf16 v[44:47],a[208:211],a[24:27],v[44:47]
s_waitcnt lgkmcnt(8)
ds_read_u16_d16_hi v26,v26
v_lshl_add_u64 v[32:33],v[36:37],0,v[38:39]
v_lshlrev_b32_sdwa v25,1,v25 dst_sel:DWORD dst_unused:UNUSED_PAD src0_sel:DWORD src1_sel:WORD_1
v_pk_fma_f16 v9,v8,v8,s54
v_mfma_f32_16x16x32_bf16 v[44:47],a[212:215],a[8:11],v[44:47]
s_waitcnt lgkmcnt(6)
ds_read_u16 v25,v25
v_or3_b32 v252,v14,v15,v252
v_lshlrev_b32_sdwa v27,1,v27 dst_sel:DWORD dst_unused:UNUSED_PAD src0_sel:DWORD src1_sel:WORD_1
v_mfma_f32_16x16x32_bf16 v[36:39],a[212:215],a[12:15],v[20:23]
s_add_u32 m0,s57,31832
ds_read_b64 v[14:15],v7 offset:512
ds_read_b128 v[20:23],v4 offset:640
v_or_b32 v34,v40,v42
v_pk_mul_f16 v9,v8,v9
v_mfma_f32_16x16x32_bf16 v[36:39],a[208:211],a[28:31],v[36:39]
s_add_u32 s89,s56,3342336
ds_read_u16_d16_hi v27,v27
ds_read_b64_tr_b16 a[212:213],v1 offset:4096
v_cvt_pk_bf16_f32 v35,v41,v43
v_lshlrev_b32_sdwa v28,1,v28 dst_sel:DWORD dst_unused:UNUSED_PAD src0_sel:DWORD src1_sel:WORD_1
v_mfma_f32_16x16x32_bf16 v[36:39],a[204:207],a[44:47],v[36:39]
ds_read_b64_tr_b16 a[214:215],v1 offset:5120
v_exp_f16 v8,v9
v_mfma_f32_16x16x32_bf16 v[40:43],a[200:203],a[44:47],v[16:19]
buffer_load_dwordx4 v6,s[36:39],s89 offen lds
ds_read_b128 v[16:19],v4 offset:672
v_lshlrev_b32_sdwa v30,1,v30 dst_sel:DWORD dst_unused:UNUSED_PAD src0_sel:DWORD src1_sel:WORD_1
v_mfma_f32_16x16x32_bf16 v[40:43],a[196:199],a[28:31],v[40:43]
ds_read_b64_tr_b16 a[208:209],v2 offset:4096
ds_read_b64_tr_b16 a[210:211],v2 offset:5120
v_lshlrev_b32_sdwa v29,1,v29 dst_sel:DWORD dst_unused:UNUSED_PAD src0_sel:DWORD src1_sel:WORD_1
v_mfma_f32_16x16x32_bf16 v[40:43],a[192:195],a[12:15],v[40:43]
s_waitcnt lgkmcnt(7)
ds_read_u16 v28,v28
ds_read_u16_d16_hi v30,v30
v_or3_b32 v252,v14,v15,v252
v_mfma_f32_16x16x32_bf16 v[112:115],a[240:243],v[60:63],v[112:115]
ds_read_b64 v[14:15],v7 offset:1024
ds_read_u16 v29,v29
v_exp_f16_sdwa v8,v9 dst_sel:WORD_1 dst_unused:UNUSED_PRESERVE src0_sel:WORD_1
v_lshlrev_b32_sdwa v31,1,v31 dst_sel:DWORD dst_unused:UNUSED_PAD src0_sel:DWORD src1_sel:WORD_1
v_mfma_f32_16x16x32_bf16 v[116:119],a[240:243],v[32:35],v[116:119]
ds_read_u16_d16_hi v31,v31
ds_read_b64_tr_b16 a[204:205],v2 offset:2048
v_lshlrev_b32_sdwa v56,1,v56 dst_sel:DWORD dst_unused:UNUSED_PAD src0_sel:DWORD src1_sel:WORD_1
v_mfma_f32_16x16x32_bf16 v[76:79],a[224:227],v[32:35],v[76:79]
ds_read_b64_tr_b16 a[206:207],v2 offset:3072
ds_read_u16 v56,v56
v_pk_fma_f16 v8,v8,s55,s55
v_mfma_f32_16x16x32_bf16 v[72:75],a[224:227],v[60:63],v[72:75]
ds_read_b64_tr_b16 a[200:201],v2 offset:0
v_rcp_f16 v9,v8
v_lshlrev_b32_sdwa v58,1,v58 dst_sel:DWORD dst_unused:UNUSED_PAD src0_sel:DWORD src1_sel:WORD_1
v_mfma_f32_16x16x32_bf16 v[120:123],a[228:231],v[60:63],v[120:123]
ds_read_u16_d16_hi v58,v58
ds_read_b64_tr_b16 a[202:203],v2 offset:1024
v_mfma_f32_16x16x32_bf16 v[124:127],a[228:231],v[32:35],v[124:127]
s_waitcnt lgkmcnt(8)
ds_read_b64_tr_b16 a[248:249],v3 offset:0
ds_read_b64_tr_b16 a[250:251],v3 offset:1536
v_or3_b32 v252,v14,v15,v252
v_lshlrev_b32_sdwa v57,1,v57 dst_sel:DWORD dst_unused:UNUSED_PAD src0_sel:DWORD src1_sel:WORD_1
v_mfma_f32_16x16x32_bf16 v[92:95],a[232:235],v[32:35],v[92:95]
ds_read_u16 v57,v57
ds_read_b64_tr_b16 a[252:253],v3 offset:8
v_and_b32 v252,0x40004000,v252
v_lshlrev_b32_sdwa v59,1,v59 dst_sel:DWORD dst_unused:UNUSED_PAD src0_sel:DWORD src1_sel:WORD_1
v_mfma_f32_16x16x32_bf16 v[88:91],a[232:235],v[60:63],v[88:91]
ds_read_u16_d16_hi v59,v59
ds_read_b64_tr_b16 a[254:255],v3 offset:1544
v_cmp_ne_u32_e32 vcc,0,v252
s_nop 0
s_or_b64 s[90:91],s[90:91],vcc
v_mfma_f32_16x16x32_bf16 v[128:131],a[236:239],v[60:63],v[128:131]
s_add_u32 m0,s57,35368
ds_read_b64_tr_b16 a[240:241],v3 offset:64
v_rcp_f16_sdwa v9,v8 dst_sel:WORD_1 dst_unused:UNUSED_PRESERVE src0_sel:WORD_1
v_lshlrev_b32_sdwa v52,1,v52 dst_sel:DWORD dst_unused:UNUSED_PAD src0_sel:DWORD src1_sel:WORD_1
v_mfma_f32_16x16x32_bf16 v[132:135],a[236:239],v[32:35],v[132:135]
s_add_u32 s89,s56,3440640
ds_read_u16 v52,v52
ds_read_b64_tr_b16 a[242:243],v3 offset:1600
v_lshlrev_b32_sdwa v54,1,v54 dst_sel:DWORD dst_unused:UNUSED_PAD src0_sel:DWORD src1_sel:WORD_1
v_mfma_f32_16x16x32_bf16 v[108:111],a[244:247],v[32:35],v[108:111]
buffer_load_dwordx4 v6,s[36:39],s89 offen lds
ds_read_u16_d16_hi v54,v54
v_fma_mix_f32 v53,v53,v9,0 op_sel_hi:[0,1,0]
v_mfma_f32_16x16x32_bf16 v[104:107],a[244:247],v[60:63],v[104:107]
ds_read_b64_tr_b16 a[224:225],v3 offset:72
v_fma_mix_f32 v55,v55,v9,0 op_sel:[0,1,0] op_sel_hi:[0,1,0]
v_mfma_f32_16x16x32_bf16 v[32:35],a[216:219],a[0:3],v[20:23]
ds_read_b64_tr_b16 a[226:227],v3 offset:1608
v_lshlrev_b32_sdwa v48,1,v48 dst_sel:DWORD dst_unused:UNUSED_PAD src0_sel:DWORD src1_sel:WORD_1
v_mfma_f32_16x16x32_bf16 v[32:35],a[220:223],a[16:19],v[32:35]
ds_read_b64_tr_b16 a[228:229],v3 offset:128
v_fma_mixlo_f16 v8,v37,s99,0
v_mfma_f32_16x16x32_bf16 v[32:35],a[212:215],a[32:35],v[32:35]
ds_read_b64_tr_b16 a[230:231],v3 offset:1664
v_lshl_add_u64 v[24:25],v[24:25],0,v[26:27]
v_lshlrev_b32_sdwa v50,1,v50 dst_sel:DWORD dst_unused:UNUSED_PAD src0_sel:DWORD src1_sel:WORD_1
v_mfma_f32_16x16x32_bf16 v[60:63],a[208:211],a[32:35],v[16:19]
s_waitcnt lgkmcnt(10)
ds_read_b64_tr_b16 a[232:233],v3 offset:136
v_lshl_add_u64 v[26:27],v[28:29],0,v[30:31]
v_fma_mixhi_f16 v8,v39,s99,0
v_mfma_f32_16x16x32_bf16 v[60:63],a[204:207],a[16:19],v[60:63]
s_waitcnt lgkmcnt(9)
ds_read_b64_tr_b16 a[234:235],v3 offset:1672
v_lshl_add_u64 v[28:29],v[56:57],0,v[58:59]
v_lshlrev_b32_sdwa v49,1,v49 dst_sel:DWORD dst_unused:UNUSED_PAD src0_sel:DWORD src1_sel:WORD_1
v_pk_fma_f16 v9,v8,v8,s54
v_mfma_f32_16x16x32_bf16 v[60:63],a[200:203],a[0:3],v[60:63]
ds_read_u16 v48,v48
v_lshlrev_b32_sdwa v51,1,v51 dst_sel:DWORD dst_unused:UNUSED_PAD src0_sel:DWORD src1_sel:WORD_1
v_mfma_f32_16x16x32_bf16 v[56:59],a[200:203],a[4:7],v[16:19]
s_waitcnt lgkmcnt(7)
ds_read_u16_d16_hi v50,v50
v_or_b32 v30,v52,v54
v_pk_mul_f16 v9,v8,v9
v_mfma_f32_16x16x32_bf16 v[56:59],a[204:207],a[20:23],v[56:59]
s_waitcnt vmcnt(7)
ds_read_u16 v49,v49
v_cvt_pk_bf16_f32 v31,v53,v55
v_lshlrev_b32_sdwa v44,1,v44 dst_sel:DWORD dst_unused:UNUSED_PAD src0_sel:DWORD src1_sel:WORD_1
v_mfma_f32_16x16x32_bf16 v[56:59],a[208:211],a[36:39],v[56:59]
s_barrier
v_exp_f16 v8,v9
v_mfma_f32_16x16x32_bf16 v[52:55],a[212:215],a[36:39],v[20:23]
ds_read_u16_d16_hi v51,v51
v_lshlrev_b32_sdwa v46,1,v46 dst_sel:DWORD dst_unused:UNUSED_PAD src0_sel:DWORD src1_sel:WORD_1
v_mfma_f32_16x16x32_bf16 v[52:55],a[220:223],a[20:23],v[52:55]
ds_read_u16 v44,v44
v_lshlrev_b32_sdwa v45,1,v45 dst_sel:DWORD dst_unused:UNUSED_PAD src0_sel:DWORD src1_sel:WORD_1
v_mfma_f32_16x16x32_bf16 v[52:55],a[216:219],a[4:7],v[52:55]
s_add_u32 m0,s58,18480
v_exp_f16_sdwa v8,v9 dst_sel:WORD_1 dst_unused:UNUSED_PRESERVE src0_sel:WORD_1
v_mfma_f32_16x16x32_bf16 v[136:139],a[248:251],v[24:27],v[136:139]
s_add_u32 s69,s69,s59
ds_read_u16_d16_hi v46,v46
v_lshlrev_b32_sdwa v47,1,v47 dst_sel:DWORD dst_unused:UNUSED_PAD src0_sel:DWORD src1_sel:WORD_1
v_mfma_f32_16x16x32_bf16 v[140:143],a[248:251],v[28:31],v[140:143]
buffer_load_dwordx4 v5,s[44:47],s69 offen offset:0 lds
v_lshlrev_b32_sdwa v40,1,v40 dst_sel:DWORD dst_unused:UNUSED_PAD src0_sel:DWORD src1_sel:WORD_1
v_mfma_f32_16x16x32_bf16 v[68:71],a[252:255],v[28:31],v[68:71]
ds_read_u16 v45,v45
v_pk_fma_f16 v8,v8,s55,s55
v_mfma_f32_16x16x32_bf16 v[64:67],a[252:255],v[24:27],v[64:67]
v_rcp_f16 v9,v8
v_lshlrev_b32_sdwa v42,1,v42 dst_sel:DWORD dst_unused:UNUSED_PAD src0_sel:DWORD src1_sel:WORD_1
v_mfma_f32_16x16x32_bf16 v[144:147],a[240:243],v[24:27],v[144:147]
s_add_u32 s69,s69,s53
ds_read_u16_d16_hi v47,v47
v_lshlrev_b32_sdwa v41,1,v41 dst_sel:DWORD dst_unused:UNUSED_PAD src0_sel:DWORD src1_sel:WORD_1
v_mfma_f32_16x16x32_bf16 v[148:151],a[240:243],v[28:31],v[148:151]
s_waitcnt lgkmcnt(5)
buffer_load_dwordx4 v5,s[44:47],s69 offen offset:1024 lds
v_mfma_f32_32x32x16_bf16 v[240:255],a[88:91],a[88:91],0
v_lshlrev_b32_sdwa v43,1,v43 dst_sel:DWORD dst_unused:UNUSED_PAD src0_sel:DWORD src1_sel:WORD_1
v_lshlrev_b32_sdwa v36,1,v36 dst_sel:DWORD dst_unused:UNUSED_PAD src0_sel:DWORD src1_sel:WORD_1
v_lshlrev_b32_sdwa v38,1,v38 dst_sel:DWORD dst_unused:UNUSED_PAD src0_sel:DWORD src1_sel:WORD_1
v_mfma_f32_16x16x32_bf16 v[84:87],a[224:227],v[28:31],v[84:87]
s_add_u32 m0,s57,38896
ds_read_u16 v40,v40
v_rcp_f16_sdwa v9,v8 dst_sel:WORD_1 dst_unused:UNUSED_PRESERVE src0_sel:WORD_1
v_mfma_f32_16x16x32_bf16 v[80:83],a[224:227],v[24:27],v[80:83]
s_add_u32 s89,s56,3538944
ds_read_u16_d16_hi v42,v42
v_fma_mix_f32 v37,v37,v9,0 op_sel_hi:[0,1,0]
v_mfma_f32_16x16x32_bf16 v[152:155],a[228:231],v[24:27],v[152:155]
buffer_load_dwordx4 v6,s[36:39],s89 offen lds
ds_read_u16 v41,v41
v_mfma_f32_16x16x32_bf16 v[156:159],a[228:231],v[28:31],v[156:159]
s_add_u32 m0,s58,18480
ds_read_u16_d16_hi v43,v43
v_fma_mix_f32 v39,v39,v9,0 op_sel:[0,1,0] op_sel_hi:[0,1,0]
v_mfma_f32_16x16x32_bf16 v[100:103],a[232:235],v[28:31],v[100:103]
s_add_u32 s69,s69,s53
ds_read_u16 v36,v36
v_mfma_f32_16x16x32_bf16 v[96:99],a[232:235],v[24:27],v[96:99]
buffer_load_dwordx4 v5,s[44:47],s69 offen offset:2048 lds
ds_read_u16_d16_hi v38,v38
ds_read_b64_tr_b16 a[236:237],v3 offset:6160
ds_read_b64_tr_b16 a[238:239],v3 offset:7696
ds_read_b64_tr_b16 a[244:245],v3 offset:6168
ds_read_b64_tr_b16 a[246:247],v3 offset:7704
s_waitcnt vmcnt(9)
s_barrier
v_add_u32_sdwa v24,s60,v10 dst_sel:DWORD dst_unused:UNUSED_PAD src0_sel:DWORD src1_sel:WORD_1
v_subrev_u32 v8,88400,v24
v_min_u32 v24,v24,v8
v_add_u32 v25,7072,v24
v_subrev_u32 v8,88400,v25
v_min_u32 v25,v25,v8
s_waitcnt lgkmcnt(14)
v_lshl_add_u64 v[48:49],v[48:49],0,v[50:51]
s_waitcnt lgkmcnt(10)
v_lshl_add_u64 v[50:51],v[44:45],0,v[46:47]
s_waitcnt lgkmcnt(6)
v_lshl_add_u64 v[44:45],v[40:41],0,v[42:43]
s_waitcnt lgkmcnt(4)
v_or_b32 v46,v36,v38
v_cvt_pk_bf16_f32 v47,v37,v39
v_mfma_f32_16x16x32_bf16 v[112:115],a[248:251],v[48:51],v[112:115]
s_add_u32 m0,s58,0
s_add_u32 s69,s69,s59
buffer_load_dwordx4 v5,s[44:47],s69 offen offset:0 lds
v_xad_u32 v28,v11,0,v24
v_xad_u32 v29,v11,8,v25
v_mfma_f32_16x16x32_bf16 v[116:119],a[248:251],v[44:47],v[116:119]
v_xad_u32 v30,v11,16,v24
v_xad_u32 v31,v11,24,v25
v_mfma_f32_16x16x32_bf16 v[76:79],a[252:255],v[44:47],v[76:79]
ds_read_b64 a[48:49],v28 offset:0
ds_read_b64 a[50:51],v28 offset:192
v_mfma_f32_16x16x32_bf16 v[72:75],a[252:255],v[48:51],v[72:75]
TE02_%=:
s_cbranch_execz TP4404_%=
ds_read_b64 a[52:53],v29 offset:0
ds_read_b64 a[54:55],v29 offset:192
v_mfma_f32_16x16x32_bf16 v[120:123],a[240:243],v[48:51],v[120:123]
s_add_u32 s69,s69,s53
buffer_load_dwordx4 v5,s[44:47],s69 offen offset:1024 lds
ds_read_b64 a[56:57],v28 offset:8
ds_read_b64 a[58:59],v28 offset:200
v_mfma_f32_16x16x32_bf16 v[124:127],a[240:243],v[44:47],v[124:127]
ds_read_b64 a[60:61],v29 offset:8
ds_read_b64 a[62:63],v29 offset:200
v_mfma_f32_16x16x32_bf16 v[92:95],a[224:227],v[44:47],v[92:95]
v_mfma_f32_16x16x32_bf16 v[88:91],a[224:227],v[48:51],v[88:91]
v_mfma_f32_16x16x32_bf16 v[128:131],a[228:231],v[48:51],v[128:131]
v_mfma_f32_16x16x32_bf16 v[132:135],a[228:231],v[44:47],v[132:135]
v_mfma_f32_16x16x32_bf16 v[108:111],a[232:235],v[44:47],v[108:111]
v_mfma_f32_16x16x32_bf16 v[104:107],a[232:235],v[48:51],v[104:107]
ds_read_b64 a[64:65],v30 offset:0
ds_read_b64 a[66:67],v30 offset:192
ds_read_b64 a[68:69],v31 offset:0
ds_read_b64 a[70:71],v31 offset:192
s_waitcnt lgkmcnt(8)
v_smfmac_f32_16x16x64_bf16 v[160:163],a[96:99],a[48:55],v10
s_waitcnt lgkmcnt(4)
s_add_u32 s69,s69,s53
buffer_load_dwordx4 v5,s[44:47],s69 offen offset:2048 lds
ds_read_b64 a[48:49],v30 offset:8
ds_read_b64 a[50:51],v30 offset:200
ds_read_b64 a[52:53],v31 offset:8
ds_read_b64 a[54:55],v31 offset:200
v_smfmac_f32_16x16x64_bf16 v[164:167],a[100:103],a[56:63],v10
s_waitcnt lgkmcnt(4)
s_add_u32 m0,s57,42432
s_add_u32 s89,s56,3637248
buffer_load_dwordx4 v6,s[36:39],s89 offen lds
ds_read_b64 a[56:57],v28 offset:64
ds_read_b64 a[58:59],v28 offset:256
ds_read_b64 a[60:61],v29 offset:64
ds_read_b64 a[62:63],v29 offset:256
v_smfmac_f32_16x16x64_bf16 v[168:171],a[104:107],a[64:71],v10
s_waitcnt lgkmcnt(4)
ds_read_b64 a[64:65],v28 offset:72
ds_read_b64 a[66:67],v28 offset:264
ds_read_b64 a[68:69],v29 offset:72
ds_read_b64 a[70:71],v29 offset:264
v_and_b32 v48,8,v11
v_pk_mul_f32 v[44:45],v[160:161],v[160:161]
v_pk_mul_f32 v[46:47],v[162:163],v[162:163]
v_smfmac_f32_16x16x64_bf16 v[172:175],a[108:111],a[48:55],v10
s_waitcnt lgkmcnt(4)
ds_read_b64 a[48:49],v30 offset:64
ds_read_b64 a[50:51],v30 offset:256
ds_read_b64 a[52:53],v31 offset:64
ds_read_b64 a[54:55],v31 offset:256
v_mul_u32_u24 v48,884,v48
s_add_u32 s64,s60,3536
v_pk_add_f32 v[36:37],v[160:161],v[164:165]
v_pk_fma_f32 v[44:45],v[164:165],v[164:165],v[44:45]
v_pk_add_f32 v[38:39],v[162:163],v[166:167]
v_pk_fma_f32 v[46:47],v[166:167],v[166:167],v[46:47]
v_smfmac_f32_16x16x64_bf16 v[176:179],a[128:131],a[56:63],v10
s_waitcnt lgkmcnt(4)
ds_read_b64 a[56:57],v30 offset:72
ds_read_b64 a[58:59],v30 offset:264
ds_read_b64 a[60:61],v31 offset:72
ds_read_b64 a[62:63],v31 offset:264
v_add_u32 v51,s64,v48
v_pk_add_f32 v[36:37],v[36:37],v[168:169]
v_pk_fma_f32 v[44:45],v[168:169],v[168:169],v[44:45]
v_pk_add_f32 v[38:39],v[38:39],v[170:171]
v_pk_fma_f32 v[46:47],v[170:171],v[170:171],v[46:47]
v_smfmac_f32_16x16x64_bf16 v[180:183],a[132:135],a[64:71],v10
s_waitcnt lgkmcnt(4)
ds_read_b64 a[64:65],v28 offset:128
ds_read_b64 a[66:67],v28 offset:320
ds_read_b64 a[68:69],v29 offset:128
ds_read_b64 a[70:71],v29 offset:320
v_subrev_u32 v8,88400,v51
v_pk_add_f32 v[36:37],v[36:37],v[172:173]
v_pk_fma_f32 v[44:45],v[172:173],v[172:173],v[44:45]
v_pk_add_f32 v[38:39],v[38:39],v[174:175]
v_pk_fma_f32 v[46:47],v[174:175],v[174:175],v[46:47]
v_smfmac_f32_16x16x64_bf16 v[184:187],a[136:139],a[48:55],v10
s_waitcnt lgkmcnt(4)
ds_read_b64 a[48:49],v28 offset:136
ds_read_b64 a[50:51],v28 offset:328
ds_read_b64 a[52:53],v29 offset:136
ds_read_b64 a[54:55],v29 offset:328
v_min_u32 v51,v51,v8
v_pk_add_f32 v[36:37],v[36:37],v[176:177]
v_pk_fma_f32 v[44:45],v[176:177],v[176:177],v[44:45]
v_pk_add_f32 v[38:39],v[38:39],v[178:179]
v_pk_fma_f32 v[46:47],v[178:179],v[178:179],v[46:47]
v_smfmac_f32_16x16x64_bf16 v[188:191],a[140:143],a[56:63],v10
s_waitcnt lgkmcnt(4)
ds_read_b64 a[56:57],v30 offset:128
ds_read_b64 a[58:59],v30 offset:320
ds_read_b64 a[60:61],v31 offset:128
ds_read_b64 a[62:63],v31 offset:320
v_xad_u32 v28,v11,32,v24
v_xad_u32 v29,v11,40,v25
v_pk_add_f32 v[36:37],v[36:37],v[180:181]
v_pk_fma_f32 v[44:45],v[180:181],v[180:181],v[44:45]
v_pk_add_f32 v[38:39],v[38:39],v[182:183]
v_pk_fma_f32 v[46:47],v[182:183],v[182:183],v[46:47]
v_smfmac_f32_16x16x64_bf16 v[192:195],a[160:163],a[64:71],v10
s_waitcnt lgkmcnt(4)
s_add_u32 m0,s57,45976
s_add_u32 s89,s56,3735552
buffer_load_dwordx4 v6,s[36:39],s89 offen lds
ds_read_b64 a[64:65],v30 offset:136
ds_read_b64 a[66:67],v30 offset:328
ds_read_b64 a[68:69],v31 offset:136
ds_read_b64 a[70:71],v31 offset:328
v_add_u32 v50,3536,v51
v_pk_add_f32 v[36:37],v[36:37],v[184:185]
v_pk_fma_f32 v[44:45],v[184:185],v[184:185],v[44:45]
v_pk_add_f32 v[38:39],v[38:39],v[186:187]
v_pk_fma_f32 v[46:47],v[186:187],v[186:187],v[46:47]
v_smfmac_f32_16x16x64_bf16 v[196:199],a[164:167],a[48:55],v10
s_waitcnt lgkmcnt(4)
ds_read_b64 a[48:49],v28 offset:0
ds_read_b64 a[50:51],v28 offset:192
ds_read_b64 a[52:53],v29 offset:0
ds_read_b64 a[54:55],v29 offset:192
v_xad_u32 v30,v11,48,v24
v_xad_u32 v31,v11,56,v25
v_pk_add_f32 v[36:37],v[36:37],v[188:189]
v_pk_fma_f32 v[44:45],v[188:189],v[188:189],v[44:45]
v_pk_add_f32 v[38:39],v[38:39],v[190:191]
v_pk_fma_f32 v[46:47],v[190:191],v[190:191],v[46:47]
v_smfmac_f32_16x16x64_bf16 v[200:203],a[168:171],a[56:63],v10
s_waitcnt lgkmcnt(4)
ds_read_b64 a[56:57],v28 offset:8
ds_read_b64 a[58:59],v28 offset:200
ds_read_b64 a[60:61],v29 offset:8
ds_read_b64 a[62:63],v29 offset:200
v_subrev_u32 v8,88400,v50
v_pk_add_f32 v[36:37],v[36:37],v[192:193]
v_pk_fma_f32 v[44:45],v[192:193],v[192:193],v[44:45]
v_pk_add_f32 v[38:39],v[38:39],v[194:195]
v_pk_fma_f32 v[46:47],v[194:195],v[194:195],v[46:47]
v_smfmac_f32_16x16x64_bf16 v[204:207],a[172:175],a[64:71],v10
s_waitcnt lgkmcnt(4)
ds_read_b64 a[64:65],v28 offset:64
ds_read_b64 a[66:67],v28 offset:256
ds_read_b64 a[68:69],v29 offset:64
ds_read_b64 a[70:71],v29 offset:256
v_min_u32 v50,v50,v8
v_pk_add_f32 v[36:37],v[36:37],v[196:197]
v_pk_fma_f32 v[44:45],v[196:197],v[196:197],v[44:45]
v_pk_add_f32 v[38:39],v[38:39],v[198:199]
v_pk_fma_f32 v[46:47],v[198:199],v[198:199],v[46:47]
v_smfmac_f32_16x16x64_bf16 v[208:211],a[112:115],a[48:55],v10
s_waitcnt lgkmcnt(4)
ds_read_b64 a[48:49],v28 offset:72
ds_read_b64 a[50:51],v28 offset:264
ds_read_b64 a[52:53],v29 offset:72
ds_read_b64 a[54:55],v29 offset:264
v_xor_b32 v9,0x80008,v12
v_pk_add_f32 v[36:37],v[36:37],v[200:201]
v_pk_fma_f32 v[44:45],v[200:201],v[200:201],v[44:45]
v_pk_add_f32 v[38:39],v[38:39],v[202:203]
v_pk_fma_f32 v[46:47],v[202:203],v[202:203],v[46:47]
v_smfmac_f32_16x16x64_bf16 v[212:215],a[116:119],a[56:63],v10
s_waitcnt lgkmcnt(4)
ds_read_b64 a[56:57],v28 offset:128
ds_read_b64 a[58:59],v28 offset:320
ds_read_b64 a[60:61],v29 offset:128
ds_read_b64 a[62:63],v29 offset:320
v_add_u32_sdwa v48,v12,v51 dst_sel:DWORD dst_unused:UNUSED_PAD src0_sel:WORD_0 src1_sel:DWORD
v_pk_add_f32 v[36:37],v[36:37],v[204:205]
v_pk_fma_f32 v[44:45],v[204:205],v[204:205],v[44:45]
v_pk_add_f32 v[38:39],v[38:39],v[206:207]
v_pk_fma_f32 v[46:47],v[206:207],v[206:207],v[46:47]
v_smfmac_f32_16x16x64_bf16 v[216:219],a[144:147],a[64:71],v10
s_waitcnt lgkmcnt(4)
ds_read_b64 a[64:65],v28 offset:136
ds_read_b64 a[66:67],v28 offset:328
ds_read_b64 a[68:69],v29 offset:136
ds_read_b64 a[70:71],v29 offset:328
v_add_u32_sdwa v49,v12,v51 dst_sel:DWORD dst_unused:UNUSED_PAD src0_sel:WORD_1 src1_sel:DWORD
v_pk_add_f32 v[36:37],v[36:37],v[208:209]
v_pk_fma_f32 v[44:45],v[208:209],v[208:209],v[44:45]
v_pk_add_f32 v[38:39],v[38:39],v[210:211]
v_pk_fma_f32 v[46:47],v[210:211],v[210:211],v[46:47]
v_smfmac_f32_16x16x64_bf16 v[220:223],a[148:151],a[48:55],v10
s_waitcnt lgkmcnt(4)
ds_read_b64 a[48:49],v30 offset:0
ds_read_b64 a[50:51],v30 offset:192
ds_read_b64 a[52:53],v31 offset:0
ds_read_b64 a[54:55],v31 offset:192
v_add_u32_sdwa v51,v9,v50 dst_sel:DWORD dst_unused:UNUSED_PAD src0_sel:WORD_1 src1_sel:DWORD
v_pk_add_f32 v[36:37],v[36:37],v[212:213]
v_pk_fma_f32 v[44:45],v[212:213],v[212:213],v[44:45]
v_pk_add_f32 v[38:39],v[38:39],v[214:215]
v_pk_fma_f32 v[46:47],v[214:215],v[214:215],v[46:47]
v_smfmac_f32_16x16x64_bf16 v[224:227],a[176:179],a[56:63],v10
s_waitcnt lgkmcnt(4)
s_add_u32 m0,s57,49512
s_add_u32 s89,s56,3833856
buffer_load_dwordx4 v6,s[36:39],s89 offen lds
ds_read_b64 a[56:57],v30 offset:8
ds_read_b64 a[58:59],v30 offset:200
ds_read_b64 a[60:61],v31 offset:8
ds_read_b64 a[62:63],v31 offset:200
v_add_u32_sdwa v50,v9,v50 dst_sel:DWORD dst_unused:UNUSED_PAD src0_sel:WORD_0 src1_sel:DWORD
v_pk_add_f32 v[36:37],v[36:37],v[216:217]
v_pk_fma_f32 v[44:45],v[216:217],v[216:217],v[44:45]
v_pk_add_f32 v[38:39],v[38:39],v[218:219]
v_pk_fma_f32 v[46:47],v[218:219],v[218:219],v[46:47]
v_smfmac_f32_16x16x64_bf16 v[228:231],a[180:183],a[64:71],v10
s_waitcnt lgkmcnt(4)
ds_read_b64 a[64:65],v30 offset:64
ds_read_b64 a[66:67],v30 offset:256
ds_read_b64 a[68:69],v31 offset:64
ds_read_b64 a[70:71],v31 offset:256
v_pk_add_f32 v[36:37],v[36:37],v[220:221]
v_pk_fma_f32 v[44:45],v[220:221],v[220:221],v[44:45]
v_pk_add_f32 v[38:39],v[38:39],v[222:223]
v_pk_fma_f32 v[46:47],v[222:223],v[222:223],v[46:47]
v_smfmac_f32_16x16x64_bf16 v[232:235],a[120:123],a[48:55],v10
s_waitcnt lgkmcnt(4)
ds_read_b64 a[48:49],v30 offset:72
ds_read_b64 a[50:51],v30 offset:264
ds_read_b64 a[52:53],v31 offset:72
ds_read_b64 a[54:55],v31 offset:264
v_pk_add_f32 v[36:37],v[36:37],v[224:225]
v_pk_fma_f32 v[44:45],v[224:225],v[224:225],v[44:45]
v_pk_add_f32 v[38:39],v[38:39],v[226:227]
v_pk_fma_f32 v[46:47],v[226:227],v[226:227],v[46:47]
v_smfmac_f32_16x16x64_bf16 v[236:239],a[124:127],a[56:63],v10
s_waitcnt lgkmcnt(4)
ds_read_b64 a[56:57],v30 offset:128
ds_read_b64 a[58:59],v30 offset:320
ds_read_b64 a[60:61],v31 offset:128
ds_read_b64 a[62:63],v31 offset:320
v_pk_add_f32 v[36:37],v[36:37],v[228:229]
v_pk_fma_f32 v[44:45],v[228:229],v[228:229],v[44:45]
v_pk_add_f32 v[38:39],v[38:39],v[230:231]
v_pk_fma_f32 v[46:47],v[230:231],v[230:231],v[46:47]
v_smfmac_f32_16x16x64_bf16 v[240:243],a[152:155],a[64:71],v10
s_waitcnt lgkmcnt(4)
ds_read_b64 a[64:65],v30 offset:136
ds_read_b64 a[66:67],v30 offset:328
ds_read_b64 a[68:69],v31 offset:136
ds_read_b64 a[70:71],v31 offset:328
v_pk_add_f32 v[36:37],v[36:37],v[232:233]
v_pk_fma_f32 v[44:45],v[232:233],v[232:233],v[44:45]
v_pk_add_f32 v[38:39],v[38:39],v[234:235]
v_pk_fma_f32 v[46:47],v[234:235],v[234:235],v[46:47]
v_smfmac_f32_16x16x64_bf16 v[244:247],a[156:159],a[48:55],v10
s_waitcnt lgkmcnt(4)
ds_read_b64 a[248:249],v48 offset:57344
ds_read_b64 a[250:251],v48 offset:57352
ds_read_b64 a[252:253],v49 offset:57344
ds_read_b64 a[254:255],v49 offset:57352
ds_read_b128 a[228:231],v13 offset:0
v_pk_add_f32 v[36:37],v[36:37],v[236:237]
v_pk_fma_f32 v[44:45],v[236:237],v[236:237],v[44:45]
v_pk_add_f32 v[38:39],v[38:39],v[238:239]
v_pk_fma_f32 v[46:47],v[238:239],v[238:239],v[46:47]
v_smfmac_f32_16x16x64_bf16 v[248:251],a[184:187],a[56:63],v10
s_waitcnt lgkmcnt(5)
ds_read_b64 a[240:241],v50 offset:57344
ds_read_b64 a[242:243],v50 offset:57352
ds_read_b64 a[224:225],v51 offset:57344
ds_read_b64 a[226:227],v51 offset:57352
ds_read_b128 a[232:235],v13 offset:256
v_pk_add_f32 v[36:37],v[36:37],v[240:241]
v_pk_fma_f32 v[44:45],v[240:241],v[240:241],v[44:45]
v_pk_add_f32 v[38:39],v[38:39],v[242:243]
v_pk_fma_f32 v[46:47],v[242:243],v[242:243],v[46:47]
v_smfmac_f32_16x16x64_bf16 v[252:255],a[188:191],a[64:71],v10
s_add_u32 m0,s57,53040
s_add_u32 s89,s56,3932160
buffer_load_dwordx4 v6,s[36:39],s89 offen lds
ds_read_b128 v[28:31],v4 offset:3584
ds_read_b128 v[24:27],v4 offset:3616
v_pk_add_f32 v[36:37],v[36:37],v[244:245]
v_pk_fma_f32 v[44:45],v[244:245],v[244:245],v[44:45]
v_pk_add_f32 v[38:39],v[38:39],v[246:247]
v_pk_fma_f32 v[46:47],v[246:247],v[246:247],v[46:47]
v_pk_add_f32 v[36:37],v[36:37],v[248:249]
v_pk_fma_f32 v[44:45],v[248:249],v[248:249],v[44:45]
v_pk_add_f32 v[38:39],v[38:39],v[250:251]
v_pk_fma_f32 v[46:47],v[250:251],v[250:251],v[46:47]
v_pk_add_f32 v[36:37],v[36:37],v[252:253]
v_pk_fma_f32 v[44:45],v[252:253],v[252:253],v[44:45]
v_pk_add_f32 v[38:39],v[38:39],v[254:255]
v_pk_fma_f32 v[46:47],v[254:255],v[254:255],v[46:47]
s_nop 0
v_permlane32_swap_b32 v36,v38
v_permlane32_swap_b32 v37,v39
v_permlane32_swap_b32 v44,v46
v_permlane32_swap_b32 v45,v47
v_pk_add_f32 v[36:37],v[36:37],v[38:39]
v_pk_add_f32 v[44:45],v[44:45],v[46:47]
s_add_u32 s60,s60,56576
s_sub_u32 s64,s60,88400
s_min_u32 s60,s60,s64
v_permlane16_swap_b32 v36,v37
v_permlane16_swap_b32 v44,v45
v_add_f32 v8,v36,v37
v_add_f32 v9,v44,v45
v_mul_f32 v8,0x3c2aaaab,v8
v_mul_f32 v9,0x3c2aaaab,v9
v_cmp_ngt_f32 vcc,0x5d800000,v9
v_fma_f32 v9,-v8,v8,v9
v_max_f32 v9,0,v9
s_or_b64 s[90:91],s[90:91],vcc
v_add_f32 v9,0x3727c5ac,v9
v_rsq_f32 v9,v9
s_nop 0
v_mul_f32 v36,s96,v9
v_mul_f32 v37,s96,v9
v_mul_f32 v44,-v8,v36
v_mul_f32 v45,-v8,v36
v_permlane16_swap_b32 v36,v37
s_nop 0
v_permlane16_swap_b32 v44,v45
v_mov_b64 v[38:39],v[36:37]
v_mov_b64 v[46:47],v[44:45]
s_nop 0
v_permlane32_swap_b32 v36,v38
v_permlane32_swap_b32 v37,v39
v_permlane32_swap_b32 v44,v46
v_permlane32_swap_b32 v45,v47
v_pk_fma_f32 v[160:161],v[160:161],v[36:37],v[44:45]
v_pk_fma_f32 v[162:163],v[162:163],v[38:39],v[46:47]
v_pk_fma_f32 v[164:165],v[164:165],v[36:37],v[44:45]
v_pk_fma_f32 v[166:167],v[166:167],v[38:39],v[46:47]
v_pk_fma_f32 v[168:169],v[168:169],v[36:37],v[44:45]
v_pk_fma_f32 v[170:171],v[170:171],v[38:39],v[46:47]
v_pk_fma_f32 v[172:173],v[172:173],v[36:37],v[44:45]
v_pk_fma_f32 v[174:175],v[174:175],v[38:39],v[46:47]
v_pk_fma_f32 v[208:209],v[208:209],v[36:37],v[44:45]
v_pk_fma_f32 v[210:211],v[210:211],v[38:39],v[46:47]
v_pk_fma_f32 v[212:213],v[212:213],v[36:37],v[44:45]
v_pk_fma_f32 v[214:215],v[214:215],v[38:39],v[46:47]
v_pk_fma_f32 v[232:233],v[232:233],v[36:37],v[44:45]
v_pk_fma_f32 v[234:235],v[234:235],v[38:39],v[46:47]
v_pk_fma_f32 v[236:237],v[236:237],v[36:37],v[44:45]
v_pk_fma_f32 v[238:239],v[238:239],v[38:39],v[46:47]
v_pk_fma_f32 v[176:177],v[176:177],v[36:37],v[44:45]
v_pk_fma_f32 v[178:179],v[178:179],v[38:39],v[46:47]
v_pk_fma_f32 v[180:181],v[180:181],v[36:37],v[44:45]
v_pk_fma_f32 v[182:183],v[182:183],v[38:39],v[46:47]
v_pk_fma_f32 v[184:185],v[184:185],v[36:37],v[44:45]
v_pk_fma_f32 v[186:187],v[186:187],v[38:39],v[46:47]
v_pk_fma_f32 v[188:189],v[188:189],v[36:37],v[44:45]
v_pk_fma_f32 v[190:191],v[190:191],v[38:39],v[46:47]
v_pk_fma_f32 v[216:217],v[216:217],v[36:37],v[44:45]
v_pk_fma_f32 v[218:219],v[218:219],v[38:39],v[46:47]
v_pk_fma_f32 v[220:221],v[220:221],v[36:37],v[44:45]
v_pk_fma_f32 v[222:223],v[222:223],v[38:39],v[46:47]
v_pk_fma_f32 v[240:241],v[240:241],v[36:37],v[44:45]
v_pk_fma_f32 v[242:243],v[242:243],v[38:39],v[46:47]
v_pk_fma_f32 v[244:245],v[244:245],v[36:37],v[44:45]
v_pk_fma_f32 v[246:247],v[246:247],v[38:39],v[46:47]
v_pk_fma_f32 v[192:193],v[192:193],v[36:37],v[44:45]
v_pk_fma_f32 v[194:195],v[194:195],v[38:39],v[46:47]
v_pk_fma_f32 v[196:197],v[196:197],v[36:37],v[44:45]
v_pk_fma_f32 v[198:199],v[198:199],v[38:39],v[46:47]
v_pk_fma_f32 v[200:201],v[200:201],v[36:37],v[44:45]
v_pk_fma_f32 v[202:203],v[202:203],v[38:39],v[46:47]
v_pk_fma_f32 v[204:205],v[204:205],v[36:37],v[44:45]
v_pk_fma_f32 v[206:207],v[206:207],v[38:39],v[46:47]
v_pk_fma_f32 v[224:225],v[224:225],v[36:37],v[44:45]
v_pk_fma_f32 v[226:227],v[226:227],v[38:39],v[46:47]
v_pk_fma_f32 v[228:229],v[228:229],v[36:37],v[44:45]
v_pk_fma_f32 v[230:231],v[230:231],v[38:39],v[46:47]
v_pk_fma_f32 v[248:249],v[248:249],v[36:37],v[44:45]
v_pk_fma_f32 v[250:251],v[250:251],v[38:39],v[46:47]
v_pk_fma_f32 v[252:253],v[252:253],v[36:37],v[44:45]
v_pk_fma_f32 v[254:255],v[254:255],v[38:39],v[46:47]
v_mfma_f32_16x16x32_bf16 v[40:43],a[216:219],a[8:11],v[20:23]
ds_read_b64_tr_b16 a[196:197],v1 offset:6160
v_cvt_pk_bf16_f32 v160,v160,v164
v_cvt_pk_bf16_f32 v168,v168,v172
v_cvt_pk_bf16_f32 v208,v208,v212
v_lshlrev_b32_sdwa v32,1,v32 dst_sel:DWORD dst_unused:UNUSED_PAD src0_sel:DWORD src1_sel:WORD_1
v_mfma_f32_16x16x32_bf16 v[40:43],a[220:223],a[24:27],v[40:43]
ds_read_u16 v32,v32
v_accvgpr_write_b32 a48,v160
v_accvgpr_write_b32 a49,v168
v_cvt_pk_bf16_f32 v232,v232,v236
v_accvgpr_write_b32 a50,v208
v_mfma_f32_16x16x32_bf16 v[40:43],a[212:215],a[40:43],v[40:43]
ds_read_b64_tr_b16 a[198:199],v1 offset:7184
v_cvt_pk_bf16_f32 v161,v161,v165
v_accvgpr_write_b32 a51,v232
v_cvt_pk_bf16_f32 v169,v169,v173
v_accvgpr_write_b32 a52,v161
v_fma_mixlo_f16 v8,v57,s99,0
v_mfma_f32_16x16x32_bf16 v[36:39],a[208:211],a[40:43],v[16:19]
ds_read_b64_tr_b16 a[192:193],v1 offset:8208
v_cvt_pk_bf16_f32 v209,v209,v213
v_accvgpr_write_b32 a53,v169
v_cvt_pk_bf16_f32 v233,v233,v237
v_accvgpr_write_b32 a54,v209
v_lshlrev_b32_sdwa v34,1,v34 dst_sel:DWORD dst_unused:UNUSED_PAD src0_sel:DWORD src1_sel:WORD_1
v_mfma_f32_16x16x32_bf16 v[36:39],a[204:207],a[24:27],v[36:39]
ds_read_u16_d16_hi v34,v34
v_cvt_pk_bf16_f32 v162,v162,v166
v_accvgpr_write_b32 a55,v233
v_cvt_pk_bf16_f32 v170,v170,v174
v_accvgpr_write_b32 a56,v162
v_fma_mixhi_f16 v8,v59,s99,0
v_mfma_f32_16x16x32_bf16 v[36:39],a[200:203],a[8:11],v[36:39]
ds_read_b64_tr_b16 a[194:195],v1 offset:9232
v_accvgpr_write_b32 a57,v170
v_cvt_pk_bf16_f32 v210,v210,v214
v_accvgpr_write_b32 a58,v210
v_lshlrev_b32_sdwa v33,1,v33 dst_sel:DWORD dst_unused:UNUSED_PAD src0_sel:DWORD src1_sel:WORD_1
v_cvt_pk_bf16_f32 v234,v234,v238
v_mfma_f32_16x16x32_bf16 v[44:47],a[200:203],a[12:15],v[16:19]
ds_read_u16 v33,v33
v_accvgpr_write_b32 a59,v234
v_cvt_pk_bf16_f32 v163,v163,v167
v_accvgpr_write_b32 a60,v163
v_mfma_f32_16x16x32_bf16 v[44:47],a[204:207],a[28:31],v[44:47]
v_cvt_pk_bf16_f32 v171,v171,v175
v_accvgpr_write_b32 a61,v171
v_cvt_pk_bf16_f32 v211,v211,v215
v_accvgpr_write_b32 a62,v211
v_pk_fma_f16 v9,v8,v8,s54
v_mfma_f32_16x16x32_bf16 v[44:47],a[208:211],a[44:47],v[44:47]
s_waitcnt lgkmcnt(7)
v_cvt_pk_bf16_f32 v235,v235,v239
v_accvgpr_write_b32 a63,v235
v_cvt_pk_bf16_f32 v176,v176,v180
v_cvt_pk_bf16_f32 v184,v184,v188
v_lshlrev_b32_sdwa v35,1,v35 dst_sel:DWORD dst_unused:UNUSED_PAD src0_sel:DWORD src1_sel:WORD_1
v_mfma_f32_16x16x32_bf16 v[232:235],a[228:231],a[248:251],v[28:31]
ds_read_u16_d16_hi v35,v35
v_cvt_pk_bf16_f32 v216,v216,v220
v_accvgpr_write_b32 a64,v176
v_accvgpr_write_b32 a65,v184
v_cvt_pk_bf16_f32 v240,v240,v244
v_pk_mul_f16 v9,v8,v9
v_mfma_f32_16x16x32_bf16 v[236:239],a[228:231],a[252:255],v[28:31]
v_accvgpr_write_b32 a66,v216
v_cvt_pk_bf16_f32 v177,v177,v181
v_accvgpr_write_b32 a67,v240
v_cvt_pk_bf16_f32 v185,v185,v189
v_lshlrev_b32_sdwa v60,1,v60 dst_sel:DWORD dst_unused:UNUSED_PAD src0_sel:DWORD src1_sel:WORD_1
v_mfma_f32_16x16x32_bf16 v[208:211],a[228:231],a[240:243],v[28:31]
v_exp_f16 v8,v9
v_accvgpr_write_b32 a68,v177
v_cvt_pk_bf16_f32 v217,v217,v221
v_accvgpr_write_b32 a69,v185
v_cvt_pk_bf16_f32 v241,v241,v245
v_mfma_f32_16x16x32_bf16 v[212:215],a[228:231],a[224:227],v[28:31]
ds_read_b128 a[228:231],v13 offset:512
ds_read_b128 v[28:31],v4 offset:3712
v_accvgpr_write_b32 a70,v217
v_cvt_pk_bf16_f32 v178,v178,v182
v_accvgpr_write_b32 a71,v241
v_mfma_f32_16x16x32_bf16 v[160:163],a[232:235],a[248:251],v[24:27]
ds_read_b64 a[248:249],v48 offset:57408
ds_read_b64 a[250:251],v48 offset:57416
v_cvt_pk_bf16_f32 v186,v186,v190
v_accvgpr_write_b32 a72,v178
v_accvgpr_write_b32 a73,v186
v_cvt_pk_bf16_f32 v218,v218,v222
v_lshlrev_b32_sdwa v62,1,v62 dst_sel:DWORD dst_unused:UNUSED_PAD src0_sel:DWORD src1_sel:WORD_1
v_mfma_f32_16x16x32_bf16 v[164:167],a[232:235],a[252:255],v[24:27]
ds_read_b64 a[252:253],v49 offset:57408
ds_read_b64 a[254:255],v49 offset:57416
v_accvgpr_write_b32 a74,v218
v_cvt_pk_bf16_f32 v242,v242,v246
v_accvgpr_write_b32 a75,v242
v_cvt_pk_bf16_f32 v179,v179,v183
v_mfma_f32_16x16x32_bf16 v[168:171],a[232:235],a[240:243],v[24:27]
ds_read_b64 a[240:241],v50 offset:57408
ds_read_b64 a[242:243],v50 offset:57416
v_accvgpr_write_b32 a76,v179
v_cvt_pk_bf16_f32 v187,v187,v191
v_accvgpr_write_b32 a77,v187
v_cvt_pk_bf16_f32 v219,v219,v223
v_lshlrev_b32_sdwa v61,1,v61 dst_sel:DWORD dst_unused:UNUSED_PAD src0_sel:DWORD src1_sel:WORD_1
v_mfma_f32_16x16x32_bf16 v[172:175],a[232:235],a[224:227],v[24:27]
s_waitcnt lgkmcnt(4)
ds_read_b64 a[224:225],v51 offset:57408
ds_read_b64 a[226:227],v51 offset:57416
v_exp_f16_sdwa v8,v9 dst_sel:WORD_1 dst_unused:UNUSED_PRESERVE src0_sel:WORD_1
v_accvgpr_write_b32 a78,v219
v_cvt_pk_bf16_f32 v243,v243,v247
v_accvgpr_write_b32 a79,v243
v_cvt_pk_bf16_f32 v192,v192,v196
v_mfma_f32_16x16x32_bf16 v[240:243],a[228:231],a[248:251],v[28:31]
s_waitcnt lgkmcnt(4)
ds_read_b128 a[232:235],v13 offset:768
ds_read_b128 v[24:27],v4 offset:3744
v_cvt_pk_bf16_f32 v200,v200,v204
v_cvt_pk_bf16_f32 v224,v224,v228
v_accvgpr_write_b32 a80,v192
v_accvgpr_write_b32 a81,v200
v_lshlrev_b32_sdwa v63,1,v63 dst_sel:DWORD dst_unused:UNUSED_PAD src0_sel:DWORD src1_sel:WORD_1
v_mfma_f32_16x16x32_bf16 v[244:247],a[228:231],a[252:255],v[28:31]
s_waitcnt lgkmcnt(4)
ds_read_u16 v60,v60
v_cvt_pk_bf16_f32 v248,v248,v252
v_accvgpr_write_b32 a82,v224
v_cvt_pk_bf16_f32 v193,v193,v197
v_mfma_f32_16x16x32_bf16 v[216:219],a[228:231],a[240:243],v[28:31]
s_waitcnt lgkmcnt(3)
ds_read_u16_d16_hi v62,v62
v_accvgpr_write_b32 a83,v248
v_cvt_pk_bf16_f32 v201,v201,v205
v_accvgpr_write_b32 a84,v193
v_cvt_pk_bf16_f32 v225,v225,v229
v_mfma_f32_16x16x32_bf16 v[220:223],a[228:231],a[224:227],v[28:31]
s_waitcnt lgkmcnt(2)
ds_read_b128 a[228:231],v13 offset:1024
ds_read_b128 v[28:31],v4 offset:3840
v_accvgpr_write_b32 a85,v201
v_cvt_pk_bf16_f32 v249,v249,v253
v_accvgpr_write_b32 a86,v225
v_cvt_pk_bf16_f32 v194,v194,v198
v_lshlrev_b32_sdwa v52,1,v52 dst_sel:DWORD dst_unused:UNUSED_PAD src0_sel:DWORD src1_sel:WORD_1
v_mfma_f32_16x16x32_bf16 v[176:179],a[232:235],a[248:251],v[24:27]
ds_read_b64 a[248:249],v48 offset:57472
ds_read_b64 a[250:251],v48 offset:57480
v_accvgpr_write_b32 a87,v249
v_cvt_pk_bf16_f32 v202,v202,v206
v_accvgpr_write_b32 a88,v194
v_accvgpr_write_b32 a89,v202
v_pk_fma_f16 v8,v8,s55,s55
v_mfma_f32_16x16x32_bf16 v[180:183],a[232:235],a[252:255],v[24:27]
ds_read_b64 a[252:253],v49 offset:57472
ds_read_b64 a[254:255],v49 offset:57480
v_cvt_pk_bf16_f32 v226,v226,v230
v_accvgpr_write_b32 a90,v226
v_cvt_pk_bf16_f32 v250,v250,v254
v_accvgpr_write_b32 a91,v250
v_lshlrev_b32_sdwa v54,1,v54 dst_sel:DWORD dst_unused:UNUSED_PAD src0_sel:DWORD src1_sel:WORD_1
v_mfma_f32_16x16x32_bf16 v[184:187],a[232:235],a[240:243],v[24:27]
ds_read_b64 a[240:241],v50 offset:57472
ds_read_b64 a[242:243],v50 offset:57480
v_rcp_f16 v9,v8
v_cvt_pk_bf16_f32 v195,v195,v199
TE03_%=:
s_cbranch_execz TP4947_%=
v_accvgpr_write_b32 a92,v195
v_cvt_pk_bf16_f32 v203,v203,v207
v_accvgpr_write_b32 a93,v203
v_mfma_f32_16x16x32_bf16 v[188:191],a[232:235],a[224:227],v[24:27]
s_waitcnt lgkmcnt(4)
ds_read_b64 a[224:225],v51 offset:57472
ds_read_b64 a[226:227],v51 offset:57480
v_cvt_pk_bf16_f32 v227,v227,v231
v_accvgpr_write_b32 a94,v227
v_cvt_pk_bf16_f32 v251,v251,v255
v_accvgpr_write_b32 a95,v251
v_mfma_f32_16x16x32_bf16 v[248:251],a[228:231],a[248:251],v[28:31]
s_waitcnt lgkmcnt(4)
ds_read_b128 a[232:235],v13 offset:1280
ds_read_b128 v[24:27],v4 offset:3872
v_lshlrev_b32_sdwa v53,1,v53 dst_sel:DWORD dst_unused:UNUSED_PAD src0_sel:DWORD src1_sel:WORD_1
v_mfma_f32_16x16x32_bf16 v[252:255],a[228:231],a[252:255],v[28:31]
s_waitcnt lgkmcnt(4)
ds_read_u16 v61,v61
v_lshlrev_b32_sdwa v55,1,v55 dst_sel:DWORD dst_unused:UNUSED_PAD src0_sel:DWORD src1_sel:WORD_1
v_mfma_f32_16x16x32_bf16 v[224:227],a[228:231],a[240:243],v[28:31]
s_waitcnt lgkmcnt(3)
ds_read_u16_d16_hi v63,v63
v_lshlrev_b32_sdwa v56,1,v56 dst_sel:DWORD dst_unused:UNUSED_PAD src0_sel:DWORD src1_sel:WORD_1
v_mfma_f32_16x16x32_bf16 v[228:231],a[228:231],a[224:227],v[28:31]
s_waitcnt lgkmcnt(2)
ds_read_b64_tr_b16 a[228:229],v3 offset:6224
ds_read_b64_tr_b16 a[230:231],v3 offset:7760
v_rcp_f16_sdwa v9,v8 dst_sel:WORD_1 dst_unused:UNUSED_PRESERVE src0_sel:WORD_1
v_mfma_f32_16x16x32_bf16 v[192:195],a[232:235],a[248:251],v[24:27]
ds_read_b64_tr_b16 a[248:249],v3 offset:6232
ds_read_b64_tr_b16 a[250:251],v3 offset:7768
v_mfma_f32_16x16x32_bf16 v[196:199],a[232:235],a[252:255],v[24:27]
ds_read_b64_tr_b16 a[252:253],v3 offset:6288
ds_read_b64_tr_b16 a[254:255],v3 offset:7824
v_mfma_f32_16x16x32_bf16 v[200:203],a[232:235],a[240:243],v[24:27]
ds_read_b64_tr_b16 a[240:241],v3 offset:6296
ds_read_b64_tr_b16 a[242:243],v3 offset:7832
v_mfma_f32_16x16x32_bf16 v[204:207],a[232:235],a[224:227],v[24:27]
ds_read_u16 v52,v52
ds_read_u16_d16_hi v54,v54
v_lshlrev_b32_sdwa v58,1,v58 dst_sel:DWORD dst_unused:UNUSED_PAD src0_sel:DWORD src1_sel:WORD_1
v_mfma_f32_16x16x32_bf16 v[48:51],a[212:215],a[44:47],v[20:23]
ds_read_u16 v53,v53
ds_read_u16_d16_hi v55,v55
v_fma_mix_f32 v57,v57,v9,0 op_sel_hi:[0,1,0]
v_mfma_f32_16x16x32_bf16 v[48:51],a[220:223],a[28:31],v[48:51]
ds_read_u16 v56,v56
v_fma_mix_f32 v59,v59,v9,0 op_sel:[0,1,0] op_sel_hi:[0,1,0]
v_mfma_f32_16x16x32_bf16 v[48:51],a[216:219],a[12:15],v[48:51]
s_barrier
ds_read_u16_d16_hi v58,v58
v_mfma_f32_16x16x32_bf16 v[28:31],a[216:219],a[48:51],v[20:23]
s_add_u32 m0,s57,56576
ds_read_b64 v[14:15],v7 offset:6160
v_lshlrev_b32_sdwa v40,1,v40 dst_sel:DWORD dst_unused:UNUSED_PAD src0_sel:DWORD src1_sel:WORD_1
v_mfma_f32_16x16x32_bf16 v[28:31],a[220:223],a[64:67],v[28:31]
s_add_u32 s89,s56,4030464
ds_read_u16 v40,v40
v_fma_mixlo_f16 v8,v45,s99,0
v_mfma_f32_16x16x32_bf16 v[28:31],a[212:215],a[80:83],v[28:31]
buffer_load_dwordx4 v6,s[36:39],s89 offen lds
v_lshl_add_u64 v[32:33],v[32:33],0,v[34:35]
v_lshlrev_b32_sdwa v42,1,v42 dst_sel:DWORD dst_unused:UNUSED_PAD src0_sel:DWORD src1_sel:WORD_1
v_mfma_f32_16x16x32_bf16 v[24:27],a[208:211],a[80:83],v[16:19]
s_waitcnt lgkmcnt(10)
ds_read_u16_d16_hi v42,v42
v_lshl_add_u64 v[34:35],v[60:61],0,v[62:63]
v_fma_mixhi_f16 v8,v47,s99,0
v_mfma_f32_16x16x32_bf16 v[24:27],a[204:207],a[64:67],v[24:27]
s_waitcnt lgkmcnt(5)
v_lshl_add_u64 v[60:61],v[52:53],0,v[54:55]
v_lshlrev_b32_sdwa v41,1,v41 dst_sel:DWORD dst_unused:UNUSED_PAD src0_sel:DWORD src1_sel:WORD_1
v_pk_fma_f16 v9,v8,v8,s54
v_mfma_f32_16x16x32_bf16 v[24:27],a[200:203],a[48:51],v[24:27]
s_waitcnt lgkmcnt(2)
ds_read_u16 v41,v41
v_or3_b32 v13,v14,v15,0
v_lshlrev_b32_sdwa v43,1,v43 dst_sel:DWORD dst_unused:UNUSED_PAD src0_sel:DWORD src1_sel:WORD_1
v_mfma_f32_16x16x32_bf16 v[52:55],a[200:203],a[52:55],v[16:19]
ds_read_b64 v[14:15],v7 offset:6672
v_or_b32 v62,v56,v58
v_pk_mul_f16 v9,v8,v9
v_mfma_f32_16x16x32_bf16 v[52:55],a[204:207],a[68:71],v[52:55]
ds_read_u16_d16_hi v43,v43
v_cvt_pk_bf16_f32 v63,v57,v59
v_lshlrev_b32_sdwa v36,1,v36 dst_sel:DWORD dst_unused:UNUSED_PAD src0_sel:DWORD src1_sel:WORD_1
v_mfma_f32_16x16x32_bf16 v[52:55],a[208:211],a[84:87],v[52:55]
v_exp_f16 v8,v9
v_mfma_f32_16x16x32_bf16 v[56:59],a[212:215],a[84:87],v[20:23]
ds_read_u16 v36,v36
v_lshlrev_b32_sdwa v38,1,v38 dst_sel:DWORD dst_unused:UNUSED_PAD src0_sel:DWORD src1_sel:WORD_1
v_mfma_f32_16x16x32_bf16 v[56:59],a[220:223],a[68:71],v[56:59]
ds_read_u16_d16_hi v38,v38
v_lshlrev_b32_sdwa v37,1,v37 dst_sel:DWORD dst_unused:UNUSED_PAD src0_sel:DWORD src1_sel:WORD_1
v_mfma_f32_16x16x32_bf16 v[56:59],a[216:219],a[52:55],v[56:59]
s_waitcnt lgkmcnt(3)
ds_read_u16 v37,v37
v_or3_b32 v13,v14,v15,v13
v_mfma_f32_16x16x32_bf16 v[136:139],a[236:239],v[32:35],v[136:139]
ds_read_b64 v[14:15],v7 offset:7184
v_exp_f16_sdwa v8,v9 dst_sel:WORD_1 dst_unused:UNUSED_PRESERVE src0_sel:WORD_1
v_lshlrev_b32_sdwa v39,1,v39 dst_sel:DWORD dst_unused:UNUSED_PAD src0_sel:DWORD src1_sel:WORD_1
v_mfma_f32_16x16x32_bf16 v[140:143],a[236:239],v[60:63],v[140:143]
ds_read_u16_d16_hi v39,v39
v_lshlrev_b32_sdwa v48,1,v48 dst_sel:DWORD dst_unused:UNUSED_PAD src0_sel:DWORD src1_sel:WORD_1
v_mfma_f32_16x16x32_bf16 v[68:71],a[244:247],v[60:63],v[68:71]
ds_read_u16 v48,v48
v_pk_fma_f16 v8,v8,s55,s55
v_mfma_f32_16x16x32_bf16 v[64:67],a[244:247],v[32:35],v[64:67]
v_rcp_f16 v9,v8
v_lshlrev_b32_sdwa v50,1,v50 dst_sel:DWORD dst_unused:UNUSED_PAD src0_sel:DWORD src1_sel:WORD_1
v_mfma_f32_16x16x32_bf16 v[144:147],a[228:231],v[32:35],v[144:147]
ds_read_u16_d16_hi v50,v50
v_lshlrev_b32_sdwa v49,1,v49 dst_sel:DWORD dst_unused:UNUSED_PAD src0_sel:DWORD src1_sel:WORD_1
v_mfma_f32_16x16x32_bf16 v[148:151],a[228:231],v[60:63],v[148:151]
s_waitcnt lgkmcnt(3)
ds_read_u16 v49,v49
v_or3_b32 v13,v14,v15,v13
v_mfma_f32_16x16x32_bf16 v[84:87],a[248:251],v[60:63],v[84:87]
v_lshlrev_b32_sdwa v51,1,v51 dst_sel:DWORD dst_unused:UNUSED_PAD src0_sel:DWORD src1_sel:WORD_1
v_lshlrev_b32_sdwa v44,1,v44 dst_sel:DWORD dst_unused:UNUSED_PAD src0_sel:DWORD src1_sel:WORD_1
v_mfma_f32_16x16x32_bf16 v[80:83],a[248:251],v[32:35],v[80:83]
v_rcp_f16_sdwa v9,v8 dst_sel:WORD_1 dst_unused:UNUSED_PRESERVE src0_sel:WORD_1
v_mfma_f32_16x16x32_bf16 v[152:155],a[252:255],v[32:35],v[152:155]
ds_read_u16_d16_hi v51,v51
v_lshlrev_b32_sdwa v46,1,v46 dst_sel:DWORD dst_unused:UNUSED_PAD src0_sel:DWORD src1_sel:WORD_1
v_mfma_f32_16x16x32_bf16 v[156:159],a[252:255],v[60:63],v[156:159]
ds_read_u16 v44,v44
v_fma_mix_f32 v45,v45,v9,0 op_sel_hi:[0,1,0]
v_mfma_f32_16x16x32_bf16 v[100:103],a[240:243],v[60:63],v[100:103]
ds_read_u16_d16_hi v46,v46
v_fma_mix_f32 v47,v47,v9,0 op_sel:[0,1,0] op_sel_hi:[0,1,0]
v_mfma_f32_16x16x32_bf16 v[96:99],a[240:243],v[32:35],v[96:99]
v_mfma_f32_16x16x32_bf16 v[60:63],a[216:219],a[56:59],v[20:23]
v_lshlrev_b32_sdwa v28,1,v28 dst_sel:DWORD dst_unused:UNUSED_PAD src0_sel:DWORD src1_sel:WORD_1
v_fma_mixlo_f16 v8,v53,s99,0
v_mfma_f32_16x16x32_bf16 v[60:63],a[220:223],a[72:75],v[60:63]
ds_read_u16 v28,v28
v_lshlrev_b32_sdwa v30,1,v30 dst_sel:DWORD dst_unused:UNUSED_PAD src0_sel:DWORD src1_sel:WORD_1
v_mfma_f32_16x16x32_bf16 v[60:63],a[212:215],a[88:91],v[60:63]
ds_read_u16_d16_hi v30,v30
v_lshl_add_u64 v[40:41],v[40:41],0,v[42:43]
v_mfma_f32_16x16x32_bf16 v[32:35],a[208:211],a[88:91],v[16:19]
s_waitcnt lgkmcnt(5)
v_lshl_add_u64 v[42:43],v[36:37],0,v[38:39]
v_fma_mixhi_f16 v8,v55,s99,0
v_mfma_f32_16x16x32_bf16 v[32:35],a[204:207],a[72:75],v[32:35]
s_waitcnt lgkmcnt(4)
v_lshl_add_u64 v[36:37],v[48:49],0,v[50:51]
v_lshlrev_b32_sdwa v29,1,v29 dst_sel:DWORD dst_unused:UNUSED_PAD src0_sel:DWORD src1_sel:WORD_1
v_pk_fma_f16 v9,v8,v8,s54
v_mfma_f32_16x16x32_bf16 v[32:35],a[200:203],a[56:59],v[32:35]
ds_read_u16 v29,v29
v_lshlrev_b32_sdwa v31,1,v31 dst_sel:DWORD dst_unused:UNUSED_PAD src0_sel:DWORD src1_sel:WORD_1
v_mfma_f32_16x16x32_bf16 v[48:51],a[200:203],a[60:63],v[16:19]
s_waitcnt lgkmcnt(3)
ds_read_b128 v[16:19],v4 offset:768
ds_read_b64_tr_b16 a[200:201],v1 offset:10256
v_or_b32 v38,v44,v46
v_pk_mul_f16 v9,v8,v9
v_mfma_f32_16x16x32_bf16 v[48:51],a[204:207],a[76:79],v[48:51]
ds_read_b64_tr_b16 a[202:203],v1 offset:11280
ds_read_b64_tr_b16 a[204:205],v2 offset:10256
v_cvt_pk_bf16_f32 v39,v45,v47
v_lshlrev_b32_sdwa v24,1,v24 dst_sel:DWORD dst_unused:UNUSED_PAD src0_sel:DWORD src1_sel:WORD_1
v_mfma_f32_16x16x32_bf16 v[48:51],a[208:211],a[92:95],v[48:51]
ds_read_b64_tr_b16 a[206:207],v2 offset:11280
ds_read_b64_tr_b16 a[208:209],v2 offset:8208
v_exp_f16 v8,v9
v_mfma_f32_16x16x32_bf16 v[44:47],a[212:215],a[92:95],v[20:23]
ds_read_b128 v[20:23],v4 offset:800
ds_read_b64_tr_b16 a[210:211],v2 offset:9232
v_lshlrev_b32_sdwa v26,1,v26 dst_sel:DWORD dst_unused:UNUSED_PAD src0_sel:DWORD src1_sel:WORD_1
v_mfma_f32_16x16x32_bf16 v[44:47],a[220:223],a[76:79],v[44:47]
ds_read_b64_tr_b16 a[212:213],v2 offset:6160
ds_read_b64_tr_b16 a[214:215],v2 offset:7184
v_lshlrev_b32_sdwa v25,1,v25 dst_sel:DWORD dst_unused:UNUSED_PAD src0_sel:DWORD src1_sel:WORD_1
v_mfma_f32_16x16x32_bf16 v[44:47],a[216:219],a[60:63],v[44:47]
v_exp_f16_sdwa v8,v9 dst_sel:WORD_1 dst_unused:UNUSED_PRESERVE src0_sel:WORD_1
v_mfma_f32_16x16x32_bf16 v[112:115],a[236:239],v[40:43],v[112:115]
ds_read_u16_d16_hi v31,v31
v_lshlrev_b32_sdwa v27,1,v27 dst_sel:DWORD dst_unused:UNUSED_PAD src0_sel:DWORD src1_sel:WORD_1
v_mfma_f32_16x16x32_bf16 v[116:119],a[236:239],v[36:39],v[116:119]
ds_read_u16 v24,v24
v_lshlrev_b32_sdwa v56,1,v56 dst_sel:DWORD dst_unused:UNUSED_PAD src0_sel:DWORD src1_sel:WORD_1
v_mfma_f32_16x16x32_bf16 v[76:79],a[244:247],v[36:39],v[76:79]
ds_read_u16_d16_hi v26,v26
v_pk_fma_f16 v8,v8,s55,s55
v_mfma_f32_16x16x32_bf16 v[72:75],a[244:247],v[40:43],v[72:75]
v_rcp_f16 v9,v8
v_lshlrev_b32_sdwa v58,1,v58 dst_sel:DWORD dst_unused:UNUSED_PAD src0_sel:DWORD src1_sel:WORD_1
v_mfma_f32_16x16x32_bf16 v[120:123],a[228:231],v[40:43],v[120:123]
ds_read_u16 v25,v25
v_lshlrev_b32_sdwa v57,1,v57 dst_sel:DWORD dst_unused:UNUSED_PAD src0_sel:DWORD src1_sel:WORD_1
v_mfma_f32_16x16x32_bf16 v[124:127],a[228:231],v[36:39],v[124:127]
ds_read_u16_d16_hi v27,v27
v_lshlrev_b32_sdwa v59,1,v59 dst_sel:DWORD dst_unused:UNUSED_PAD src0_sel:DWORD src1_sel:WORD_1
v_mfma_f32_16x16x32_bf16 v[92:95],a[248:251],v[36:39],v[92:95]
ds_read_u16 v56,v56
v_lshlrev_b32_sdwa v52,1,v52 dst_sel:DWORD dst_unused:UNUSED_PAD src0_sel:DWORD src1_sel:WORD_1
v_mfma_f32_16x16x32_bf16 v[88:91],a[248:251],v[40:43],v[88:91]
v_rcp_f16_sdwa v9,v8 dst_sel:WORD_1 dst_unused:UNUSED_PRESERVE src0_sel:WORD_1
v_mfma_f32_16x16x32_bf16 v[128:131],a[252:255],v[40:43],v[128:131]
ds_read_u16_d16_hi v58,v58
ds_read_u16 v57,v57
v_mfma_f32_16x16x32_bf16 v[132:135],a[252:255],v[36:39],v[132:135]
ds_read_u16_d16_hi v59,v59
v_lshlrev_b32_sdwa v54,1,v54 dst_sel:DWORD dst_unused:UNUSED_PAD src0_sel:DWORD src1_sel:WORD_1
v_mfma_f32_16x16x32_bf16 v[108:111],a[240:243],v[36:39],v[108:111]
ds_read_u16 v52,v52
v_fma_mix_f32 v53,v53,v9,0 op_sel_hi:[0,1,0]
v_mfma_f32_16x16x32_bf16 v[104:107],a[240:243],v[40:43],v[104:107]
s_waitcnt lgkmcnt(7)
ds_read_u16_d16_hi v54,v54
v_fma_mix_f32 v55,v55,v9,0 op_sel:[0,1,0] op_sel_hi:[0,1,0]
v_mfma_f32_16x16x32_bf16 v[36:39],a[196:199],a[0:3],v[16:19]
s_add_u32 m0,s57,60120
v_lshlrev_b32_sdwa v60,1,v60 dst_sel:DWORD dst_unused:UNUSED_PAD src0_sel:DWORD src1_sel:WORD_1
v_fma_mixlo_f16 v8,v49,s99,0
v_mfma_f32_16x16x32_bf16 v[36:39],a[192:195],a[16:19],v[36:39]
s_add_u32 s89,s56,4128768
ds_read_u16 v60,v60
v_lshlrev_b32_sdwa v62,1,v62 dst_sel:DWORD dst_unused:UNUSED_PAD src0_sel:DWORD src1_sel:WORD_1
v_mfma_f32_16x16x32_bf16 v[36:39],a[200:203],a[32:35],v[36:39]
buffer_load_dwordx4 v6,s[36:39],s89 offen lds
v_lshl_add_u64 v[28:29],v[28:29],0,v[30:31]
v_mfma_f32_16x16x32_bf16 v[40:43],a[204:207],a[32:35],v[20:23]
s_waitcnt lgkmcnt(6)
ds_read_u16_d16_hi v62,v62
v_lshl_add_u64 v[30:31],v[24:25],0,v[26:27]
v_fma_mixhi_f16 v8,v51,s99,0
v_mfma_f32_16x16x32_bf16 v[40:43],a[208:211],a[16:19],v[40:43]
s_waitcnt lgkmcnt(4)
v_lshl_add_u64 v[24:25],v[56:57],0,v[58:59]
v_lshlrev_b32_sdwa v61,1,v61 dst_sel:DWORD dst_unused:UNUSED_PAD src0_sel:DWORD src1_sel:WORD_1
v_pk_fma_f16 v9,v8,v8,s54
v_mfma_f32_16x16x32_bf16 v[40:43],a[212:215],a[0:3],v[40:43]
ds_read_u16 v61,v61
v_lshlrev_b32_sdwa v63,1,v63 dst_sel:DWORD dst_unused:UNUSED_PAD src0_sel:DWORD src1_sel:WORD_1
v_mfma_f32_16x16x32_bf16 v[56:59],a[212:215],a[4:7],v[20:23]
s_waitcnt lgkmcnt(3)
ds_read_u16_d16_hi v63,v63
v_or_b32 v26,v52,v54
v_pk_mul_f16 v9,v8,v9
v_mfma_f32_16x16x32_bf16 v[56:59],a[208:211],a[20:23],v[56:59]
s_waitcnt vmcnt(15)
v_cvt_pk_bf16_f32 v27,v53,v55
v_lshlrev_b32_sdwa v32,1,v32 dst_sel:DWORD dst_unused:UNUSED_PAD src0_sel:DWORD src1_sel:WORD_1
v_mfma_f32_16x16x32_bf16 v[56:59],a[204:207],a[36:39],v[56:59]
s_barrier
v_exp_f16 v8,v9
v_mfma_f32_16x16x32_bf16 v[52:55],a[200:203],a[36:39],v[16:19]
ds_read_u16 v32,v32
v_lshlrev_b32_sdwa v34,1,v34 dst_sel:DWORD dst_unused:UNUSED_PAD src0_sel:DWORD src1_sel:WORD_1
v_mfma_f32_16x16x32_bf16 v[52:55],a[192:195],a[20:23],v[52:55]
s_add_u32 m0,s58,6160
ds_read_u16_d16_hi v34,v34
v_lshlrev_b32_sdwa v33,1,v33 dst_sel:DWORD dst_unused:UNUSED_PAD src0_sel:DWORD src1_sel:WORD_1
v_mfma_f32_16x16x32_bf16 v[52:55],a[196:199],a[4:7],v[52:55]
s_add_u32 s69,s69,s59
v_exp_f16_sdwa v8,v9 dst_sel:WORD_1 dst_unused:UNUSED_PRESERVE src0_sel:WORD_1
v_mfma_f32_16x16x32_bf16 v[232:235],a[236:239],v[28:31],v[232:235]
buffer_load_dwordx4 v5,s[44:47],s69 offen offset:0 lds
v_lshlrev_b32_sdwa v35,1,v35 dst_sel:DWORD dst_unused:UNUSED_PAD src0_sel:DWORD src1_sel:WORD_1
v_mfma_f32_16x16x32_bf16 v[236:239],a[236:239],v[24:27],v[236:239]
ds_read_u16 v33,v33
v_lshlrev_b32_sdwa v44,1,v44 dst_sel:DWORD dst_unused:UNUSED_PAD src0_sel:DWORD src1_sel:WORD_1
v_mfma_f32_16x16x32_bf16 v[164:167],a[244:247],v[24:27],v[164:167]
s_add_u32 m0,s57,63656
ds_read_u16_d16_hi v35,v35
v_pk_fma_f16 v8,v8,s55,s55
v_mfma_f32_16x16x32_bf16 v[160:163],a[244:247],v[28:31],v[160:163]
s_add_u32 s89,s56,4227072
v_rcp_f16 v9,v8
v_lshlrev_b32_sdwa v46,1,v46 dst_sel:DWORD dst_unused:UNUSED_PAD src0_sel:DWORD src1_sel:WORD_1
v_mfma_f32_16x16x32_bf16 v[240:243],a[228:231],v[28:31],v[240:243]
buffer_load_dwordx4 v6,s[36:39],s89 offen lds
v_lshlrev_b32_sdwa v45,1,v45 dst_sel:DWORD dst_unused:UNUSED_PAD src0_sel:DWORD src1_sel:WORD_1
v_mfma_f32_16x16x32_bf16 v[244:247],a[228:231],v[24:27],v[244:247]
s_add_u32 m0,s58,6160
ds_read_u16 v44,v44
v_lshlrev_b32_sdwa v47,1,v47 dst_sel:DWORD dst_unused:UNUSED_PAD src0_sel:DWORD src1_sel:WORD_1
v_mfma_f32_16x16x32_bf16 v[180:183],a[248:251],v[24:27],v[180:183]
s_add_u32 s69,s69,s53
ds_read_u16_d16_hi v46,v46
v_lshlrev_b32_sdwa v48,1,v48 dst_sel:DWORD dst_unused:UNUSED_PAD src0_sel:DWORD src1_sel:WORD_1
v_mfma_f32_16x16x32_bf16 v[176:179],a[248:251],v[28:31],v[176:179]
v_rcp_f16_sdwa v9,v8 dst_sel:WORD_1 dst_unused:UNUSED_PRESERVE src0_sel:WORD_1
v_mfma_f32_16x16x32_bf16 v[248:251],a[252:255],v[28:31],v[248:251]
buffer_load_dwordx4 v5,s[44:47],s69 offen offset:1024 lds
ds_read_u16 v45,v45
v_mfma_f32_16x16x32_bf16 v[252:255],a[252:255],v[24:27],v[252:255]
s_add_u32 s69,s69,s53
ds_read_u16_d16_hi v47,v47
v_lshlrev_b32_sdwa v50,1,v50 dst_sel:DWORD dst_unused:UNUSED_PAD src0_sel:DWORD src1_sel:WORD_1
v_mfma_f32_16x16x32_bf16 v[196:199],a[240:243],v[24:27],v[196:199]
buffer_load_dwordx4 v5,s[44:47],s69 offen offset:2048 lds
v_fma_mix_f32 v49,v49,v9,0 op_sel_hi:[0,1,0]
v_mfma_f32_16x16x32_bf16 v[192:195],a[240:243],v[28:31],v[192:195]
ds_read_u16 v48,v48
ds_read_u16_d16_hi v50,v50
v_fma_mix_f32 v51,v51,v9,0 op_sel:[0,1,0] op_sel_hi:[0,1,0]
v_mfma_f32_16x16x32_bf16 v[24:27],a[196:199],a[8:11],v[16:19]
ds_read_b64_tr_b16 a[224:225],v3 offset:12320
v_lshlrev_b32_sdwa v36,1,v36 dst_sel:DWORD dst_unused:UNUSED_PAD src0_sel:DWORD src1_sel:WORD_1
v_mfma_f32_16x16x32_bf16 v[24:27],a[192:195],a[24:27],v[24:27]
ds_read_u16 v36,v36
v_fma_mixlo_f16 v8,v57,s99,0
v_mfma_f32_16x16x32_bf16 v[24:27],a[200:203],a[40:43],v[24:27]
s_waitcnt lgkmcnt(7)
ds_read_b64_tr_b16 a[226:227],v3 offset:13856
v_lshl_add_u64 v[60:61],v[60:61],0,v[62:63]
v_lshlrev_b32_sdwa v38,1,v38 dst_sel:DWORD dst_unused:UNUSED_PAD src0_sel:DWORD src1_sel:WORD_1
v_mfma_f32_16x16x32_bf16 v[28:31],a[204:207],a[40:43],v[20:23]
ds_read_u16_d16_hi v38,v38
v_lshl_add_u64 v[62:63],v[32:33],0,v[34:35]
v_fma_mixhi_f16 v8,v59,s99,0
v_mfma_f32_16x16x32_bf16 v[28:31],a[208:211],a[24:27],v[28:31]
s_waitcnt lgkmcnt(6)
ds_read_b64_tr_b16 a[232:233],v3 offset:12328
v_lshl_add_u64 v[32:33],v[44:45],0,v[46:47]
v_lshlrev_b32_sdwa v37,1,v37 dst_sel:DWORD dst_unused:UNUSED_PAD src0_sel:DWORD src1_sel:WORD_1
v_pk_fma_f16 v9,v8,v8,s54
v_mfma_f32_16x16x32_bf16 v[28:31],a[212:215],a[8:11],v[28:31]
ds_read_u16 v37,v37
v_lshlrev_b32_sdwa v39,1,v39 dst_sel:DWORD dst_unused:UNUSED_PAD src0_sel:DWORD src1_sel:WORD_1
v_mfma_f32_16x16x32_bf16 v[44:47],a[212:215],a[12:15],v[20:23]
s_waitcnt lgkmcnt(6)
ds_read_u16_d16_hi v39,v39
v_or_b32 v34,v48,v50
v_pk_mul_f16 v9,v8,v9
v_mfma_f32_16x16x32_bf16 v[44:47],a[208:211],a[28:31],v[44:47]
ds_read_b64_tr_b16 a[234:235],v3 offset:13864
v_cvt_pk_bf16_f32 v35,v49,v51
v_lshlrev_b32_sdwa v40,1,v40 dst_sel:DWORD dst_unused:UNUSED_PAD src0_sel:DWORD src1_sel:WORD_1
v_mfma_f32_16x16x32_bf16 v[44:47],a[204:207],a[44:47],v[44:47]
v_exp_f16 v8,v9
v_mfma_f32_16x16x32_bf16 v[48:51],a[200:203],a[44:47],v[16:19]
ds_read_u16 v40,v40
v_lshlrev_b32_sdwa v42,1,v42 dst_sel:DWORD dst_unused:UNUSED_PAD src0_sel:DWORD src1_sel:WORD_1
v_mfma_f32_16x16x32_bf16 v[48:51],a[192:195],a[28:31],v[48:51]
ds_read_u16_d16_hi v42,v42
v_lshlrev_b32_sdwa v41,1,v41 dst_sel:DWORD dst_unused:UNUSED_PAD src0_sel:DWORD src1_sel:WORD_1
v_mfma_f32_16x16x32_bf16 v[48:51],a[196:199],a[12:15],v[48:51]
v_exp_f16_sdwa v8,v9 dst_sel:WORD_1 dst_unused:UNUSED_PRESERVE src0_sel:WORD_1
v_mfma_f32_16x16x32_bf16 v[208:211],a[236:239],v[60:63],v[208:211]
ds_read_u16 v41,v41
v_lshlrev_b32_sdwa v43,1,v43 dst_sel:DWORD dst_unused:UNUSED_PAD src0_sel:DWORD src1_sel:WORD_1
v_mfma_f32_16x16x32_bf16 v[212:215],a[236:239],v[32:35],v[212:215]
ds_read_u16_d16_hi v43,v43
v_lshlrev_b32_sdwa v52,1,v52 dst_sel:DWORD dst_unused:UNUSED_PAD src0_sel:DWORD src1_sel:WORD_1
v_mfma_f32_16x16x32_bf16 v[172:175],a[244:247],v[32:35],v[172:175]
ds_read_u16 v52,v52
v_pk_fma_f16 v8,v8,s55,s55
v_mfma_f32_16x16x32_bf16 v[168:171],a[244:247],v[60:63],v[168:171]
v_rcp_f16 v9,v8
v_lshlrev_b32_sdwa v54,1,v54 dst_sel:DWORD dst_unused:UNUSED_PAD src0_sel:DWORD src1_sel:WORD_1
v_mfma_f32_16x16x32_bf16 v[216:219],a[228:231],v[60:63],v[216:219]
ds_read_u16_d16_hi v54,v54
v_lshlrev_b32_sdwa v53,1,v53 dst_sel:DWORD dst_unused:UNUSED_PAD src0_sel:DWORD src1_sel:WORD_1
v_mfma_f32_16x16x32_bf16 v[220:223],a[228:231],v[32:35],v[220:223]
ds_read_u16 v53,v53
ds_read_b64_tr_b16 a[236:237],v3 offset:12384
v_mfma_f32_16x16x32_bf16 v[188:191],a[248:251],v[32:35],v[188:191]
ds_read_b64_tr_b16 a[238:239],v3 offset:13920
v_lshlrev_b32_sdwa v55,1,v55 dst_sel:DWORD dst_unused:UNUSED_PAD src0_sel:DWORD src1_sel:WORD_1
v_mfma_f32_16x16x32_bf16 v[184:187],a[248:251],v[60:63],v[184:187]
ds_read_u16_d16_hi v55,v55
v_rcp_f16_sdwa v9,v8 dst_sel:WORD_1 dst_unused:UNUSED_PRESERVE src0_sel:WORD_1
v_mfma_f32_16x16x32_bf16 v[224:227],a[252:255],v[60:63],v[224:227]
ds_read_b64_tr_b16 a[244:245],v3 offset:12392
v_lshlrev_b32_sdwa v56,1,v56 dst_sel:DWORD dst_unused:UNUSED_PAD src0_sel:DWORD src1_sel:WORD_1
v_mfma_f32_16x16x32_bf16 v[228:231],a[252:255],v[32:35],v[228:231]
ds_read_u16 v56,v56
v_lshlrev_b32_sdwa v58,1,v58 dst_sel:DWORD dst_unused:UNUSED_PAD src0_sel:DWORD src1_sel:WORD_1
v_mfma_f32_16x16x32_bf16 v[204:207],a[240:243],v[32:35],v[204:207]
ds_read_u16_d16_hi v58,v58
v_fma_mix_f32 v57,v57,v9,0 op_sel_hi:[0,1,0]
v_mfma_f32_16x16x32_bf16 v[200:203],a[240:243],v[60:63],v[200:203]
ds_read_b64_tr_b16 a[246:247],v3 offset:13928
v_fma_mix_f32 v59,v59,v9,0 op_sel:[0,1,0] op_sel_hi:[0,1,0]
v_mfma_f32_16x16x32_bf16 v[32:35],a[196:199],a[48:51],v[16:19]
s_add_u32 m0,s57,67184
ds_read_b64 v[14:15],v7 offset:12320
v_lshlrev_b32_sdwa v24,1,v24 dst_sel:DWORD dst_unused:UNUSED_PAD src0_sel:DWORD src1_sel:WORD_1
v_mfma_f32_16x16x32_bf16 v[32:35],a[192:195],a[64:67],v[32:35]
s_add_u32 s89,s56,4325376
ds_read_b64_tr_b16 a[228:229],v3 offset:12448
v_fma_mixlo_f16 v8,v45,s99,0
v_mfma_f32_16x16x32_bf16 v[32:35],a[200:203],a[80:83],v[32:35]
s_waitcnt lgkmcnt(8)
buffer_load_dwordx4 v6,s[36:39],s89 offen lds
v_lshl_add_u64 v[36:37],v[36:37],0,v[38:39]
v_lshlrev_b32_sdwa v26,1,v26 dst_sel:DWORD dst_unused:UNUSED_PAD src0_sel:DWORD src1_sel:WORD_1
v_mfma_f32_16x16x32_bf16 v[60:63],a[204:207],a[80:83],v[20:23]
ds_read_b64_tr_b16 a[230:231],v3 offset:13984
v_lshl_add_u64 v[38:39],v[40:41],0,v[42:43]
v_fma_mixhi_f16 v8,v47,s99,0
v_mfma_f32_16x16x32_bf16 v[60:63],a[208:211],a[64:67],v[60:63]
s_waitcnt lgkmcnt(7)
ds_read_b64_tr_b16 a[248:249],v3 offset:12456
v_lshl_add_u64 v[40:41],v[52:53],0,v[54:55]
v_lshlrev_b32_sdwa v25,1,v25 dst_sel:DWORD dst_unused:UNUSED_PAD src0_sel:DWORD src1_sel:WORD_1
v_pk_fma_f16 v9,v8,v8,s54
v_mfma_f32_16x16x32_bf16 v[60:63],a[212:215],a[48:51],v[60:63]
s_waitcnt lgkmcnt(3)
ds_read_b64_tr_b16 a[250:251],v3 offset:13992
v_or3_b32 v13,v14,v15,v13
v_lshlrev_b32_sdwa v27,1,v27 dst_sel:DWORD dst_unused:UNUSED_PAD src0_sel:DWORD src1_sel:WORD_1
v_mfma_f32_16x16x32_bf16 v[52:55],a[212:215],a[52:55],v[20:23]
ds_read_b64 v[14:15],v7 offset:12832
v_or_b32 v42,v56,v58
v_pk_mul_f16 v9,v8,v9
v_mfma_f32_16x16x32_bf16 v[52:55],a[208:211],a[68:71],v[52:55]
ds_read_u16 v24,v24
v_cvt_pk_bf16_f32 v43,v57,v59
v_lshlrev_b32_sdwa v28,1,v28 dst_sel:DWORD dst_unused:UNUSED_PAD src0_sel:DWORD src1_sel:WORD_1
v_mfma_f32_16x16x32_bf16 v[52:55],a[204:207],a[84:87],v[52:55]
v_exp_f16 v8,v9
v_mfma_f32_16x16x32_bf16 v[56:59],a[200:203],a[84:87],v[16:19]
ds_read_u16_d16_hi v26,v26
v_lshlrev_b32_sdwa v30,1,v30 dst_sel:DWORD dst_unused:UNUSED_PAD src0_sel:DWORD src1_sel:WORD_1
v_mfma_f32_16x16x32_bf16 v[56:59],a[192:195],a[68:71],v[56:59]
ds_read_u16 v25,v25
ds_read_u16_d16_hi v27,v27
v_lshlrev_b32_sdwa v29,1,v29 dst_sel:DWORD dst_unused:UNUSED_PAD src0_sel:DWORD src1_sel:WORD_1
v_mfma_f32_16x16x32_bf16 v[56:59],a[196:199],a[52:55],v[56:59]
s_waitcnt lgkmcnt(4)
ds_read_u16 v28,v28
ds_read_u16_d16_hi v30,v30
v_or3_b32 v13,v14,v15,v13
v_mfma_f32_16x16x32_bf16 v[136:139],a[224:227],v[36:39],v[136:139]
ds_read_b64 v[14:15],v7 offset:13344
v_exp_f16_sdwa v8,v9 dst_sel:WORD_1 dst_unused:UNUSED_PRESERVE src0_sel:WORD_1
v_lshlrev_b32_sdwa v31,1,v31 dst_sel:DWORD dst_unused:UNUSED_PAD src0_sel:DWORD src1_sel:WORD_1
v_mfma_f32_16x16x32_bf16 v[140:143],a[224:227],v[40:43],v[140:143]
ds_read_u16 v29,v29
v_lshlrev_b32_sdwa v48,1,v48 dst_sel:DWORD dst_unused:UNUSED_PAD src0_sel:DWORD src1_sel:WORD_1
v_mfma_f32_16x16x32_bf16 v[68:71],a[232:235],v[40:43],v[68:71]
ds_read_u16_d16_hi v31,v31
v_pk_fma_f16 v8,v8,s55,s55
v_mfma_f32_16x16x32_bf16 v[64:67],a[232:235],v[36:39],v[64:67]
v_rcp_f16 v9,v8
v_lshlrev_b32_sdwa v50,1,v50 dst_sel:DWORD dst_unused:UNUSED_PAD src0_sel:DWORD src1_sel:WORD_1
v_mfma_f32_16x16x32_bf16 v[144:147],a[236:239],v[36:39],v[144:147]
ds_read_u16 v48,v48
v_lshlrev_b32_sdwa v49,1,v49 dst_sel:DWORD dst_unused:UNUSED_PAD src0_sel:DWORD src1_sel:WORD_1
v_mfma_f32_16x16x32_bf16 v[148:151],a[236:239],v[40:43],v[148:151]
s_waitcnt lgkmcnt(3)
ds_read_u16_d16_hi v50,v50
v_or3_b32 v13,v14,v15,v13
v_mfma_f32_16x16x32_bf16 v[84:87],a[244:247],v[40:43],v[84:87]
ds_read_u16 v49,v49
v_lshlrev_b32_sdwa v51,1,v51 dst_sel:DWORD dst_unused:UNUSED_PAD src0_sel:DWORD src1_sel:WORD_1
v_mfma_f32_16x16x32_bf16 v[80:83],a[244:247],v[36:39],v[80:83]
v_rcp_f16_sdwa v9,v8 dst_sel:WORD_1 dst_unused:UNUSED_PRESERVE src0_sel:WORD_1
v_mfma_f32_16x16x32_bf16 v[152:155],a[228:231],v[36:39],v[152:155]
ds_read_u16_d16_hi v51,v51
v_lshlrev_b32_sdwa v44,1,v44 dst_sel:DWORD dst_unused:UNUSED_PAD src0_sel:DWORD src1_sel:WORD_1
v_mfma_f32_16x16x32_bf16 v[156:159],a[228:231],v[40:43],v[156:159]
ds_read_u16 v44,v44
v_lshlrev_b32_sdwa v46,1,v46 dst_sel:DWORD dst_unused:UNUSED_PAD src0_sel:DWORD src1_sel:WORD_1
v_mfma_f32_16x16x32_bf16 v[100:103],a[248:251],v[40:43],v[100:103]
ds_read_u16_d16_hi v46,v46
v_fma_mix_f32 v45,v45,v9,0 op_sel_hi:[0,1,0]
v_mfma_f32_16x16x32_bf16 v[96:99],a[248:251],v[36:39],v[96:99]
ds_read_b64_tr_b16 a[220:221],v1 offset:12320
v_fma_mix_f32 v47,v47,v9,0 op_sel:[0,1,0] op_sel_hi:[0,1,0]
v_mfma_f32_16x16x32_bf16 v[40:43],a[196:199],a[56:59],v[16:19]
ds_read_b64_tr_b16 a[222:223],v1 offset:13344
v_lshlrev_b32_sdwa v32,1,v32 dst_sel:DWORD dst_unused:UNUSED_PAD src0_sel:DWORD src1_sel:WORD_1
v_mfma_f32_16x16x32_bf16 v[40:43],a[192:195],a[72:75],v[40:43]
ds_read_b64_tr_b16 a[216:217],v1 offset:14368
v_fma_mixlo_f16 v8,v53,s99,0
v_mfma_f32_16x16x32_bf16 v[40:43],a[200:203],a[88:91],v[40:43]
ds_read_b64_tr_b16 a[218:219],v1 offset:15392
v_lshl_add_u64 v[24:25],v[24:25],0,v[26:27]
v_lshlrev_b32_sdwa v34,1,v34 dst_sel:DWORD dst_unused:UNUSED_PAD src0_sel:DWORD src1_sel:WORD_1
v_mfma_f32_16x16x32_bf16 v[36:39],a[204:207],a[88:91],v[20:23]
s_waitcnt lgkmcnt(7)
ds_read_u16 v32,v32
v_lshl_add_u64 v[26:27],v[28:29],0,v[30:31]
v_fma_mixhi_f16 v8,v55,s99,0
v_mfma_f32_16x16x32_bf16 v[36:39],a[208:211],a[72:75],v[36:39]
s_waitcnt lgkmcnt(7)
ds_read_u16_d16_hi v34,v34
v_lshl_add_u64 v[28:29],v[48:49],0,v[50:51]
v_lshlrev_b32_sdwa v33,1,v33 dst_sel:DWORD dst_unused:UNUSED_PAD src0_sel:DWORD src1_sel:WORD_1
v_pk_fma_f16 v9,v8,v8,s54
v_mfma_f32_16x16x32_bf16 v[36:39],a[212:215],a[56:59],v[36:39]
ds_read_u16 v33,v33
v_lshlrev_b32_sdwa v35,1,v35 dst_sel:DWORD dst_unused:UNUSED_PAD src0_sel:DWORD src1_sel:WORD_1
v_mfma_f32_16x16x32_bf16 v[48:51],a[212:215],a[60:63],v[20:23]
s_waitcnt lgkmcnt(7)
ds_read_b128 v[20:23],v4 offset:896
v_or_b32 v30,v44,v46
v_pk_mul_f16 v9,v8,v9
v_mfma_f32_16x16x32_bf16 v[48:51],a[208:211],a[76:79],v[48:51]
ds_read_u16_d16_hi v35,v35
v_cvt_pk_bf16_f32 v31,v45,v47
v_lshlrev_b32_sdwa v60,1,v60 dst_sel:DWORD dst_unused:UNUSED_PAD src0_sel:DWORD src1_sel:WORD_1
v_mfma_f32_16x16x32_bf16 v[48:51],a[204:207],a[92:95],v[48:51]
ds_read_b64_tr_b16 a[212:213],v1 offset:16416
v_exp_f16 v8,v9
v_mfma_f32_16x16x32_bf16 v[44:47],a[200:203],a[92:95],v[16:19]
ds_read_b128 v[16:19],v4 offset:928
ds_read_b64_tr_b16 a[214:215],v1 offset:17440
v_lshlrev_b32_sdwa v62,1,v62 dst_sel:DWORD dst_unused:UNUSED_PAD src0_sel:DWORD src1_sel:WORD_1
v_mfma_f32_16x16x32_bf16 v[44:47],a[192:195],a[76:79],v[44:47]
ds_read_b64_tr_b16 a[208:209],v2 offset:16416
ds_read_b64_tr_b16 a[210:211],v2 offset:17440
v_lshlrev_b32_sdwa v61,1,v61 dst_sel:DWORD dst_unused:UNUSED_PAD src0_sel:DWORD src1_sel:WORD_1
v_mfma_f32_16x16x32_bf16 v[44:47],a[196:199],a[60:63],v[44:47]
ds_read_u16 v60,v60
v_exp_f16_sdwa v8,v9 dst_sel:WORD_1 dst_unused:UNUSED_PRESERVE src0_sel:WORD_1
v_mfma_f32_16x16x32_bf16 v[112:115],a[224:227],v[24:27],v[112:115]
ds_read_u16_d16_hi v62,v62
v_lshlrev_b32_sdwa v63,1,v63 dst_sel:DWORD dst_unused:UNUSED_PAD src0_sel:DWORD src1_sel:WORD_1
v_mfma_f32_16x16x32_bf16 v[116:119],a[224:227],v[28:31],v[116:119]
ds_read_u16 v61,v61
v_lshlrev_b32_sdwa v56,1,v56 dst_sel:DWORD dst_unused:UNUSED_PAD src0_sel:DWORD src1_sel:WORD_1
v_mfma_f32_16x16x32_bf16 v[76:79],a[232:235],v[28:31],v[76:79]
ds_read_u16_d16_hi v63,v63
v_pk_fma_f16 v8,v8,s55,s55
v_mfma_f32_16x16x32_bf16 v[72:75],a[232:235],v[24:27],v[72:75]
v_rcp_f16 v9,v8
v_lshlrev_b32_sdwa v58,1,v58 dst_sel:DWORD dst_unused:UNUSED_PAD src0_sel:DWORD src1_sel:WORD_1
v_mfma_f32_16x16x32_bf16 v[120:123],a[236:239],v[24:27],v[120:123]
ds_read_b64_tr_b16 a[204:205],v2 offset:14368
v_lshlrev_b32_sdwa v57,1,v57 dst_sel:DWORD dst_unused:UNUSED_PAD src0_sel:DWORD src1_sel:WORD_1
v_mfma_f32_16x16x32_bf16 v[124:127],a[236:239],v[28:31],v[124:127]
ds_read_b64_tr_b16 a[206:207],v2 offset:15392
v_lshlrev_b32_sdwa v59,1,v59 dst_sel:DWORD dst_unused:UNUSED_PAD src0_sel:DWORD src1_sel:WORD_1
v_mfma_f32_16x16x32_bf16 v[92:95],a[244:247],v[28:31],v[92:95]
ds_read_u16 v56,v56
v_lshlrev_b32_sdwa v52,1,v52 dst_sel:DWORD dst_unused:UNUSED_PAD src0_sel:DWORD src1_sel:WORD_1
v_mfma_f32_16x16x32_bf16 v[88:91],a[244:247],v[24:27],v[88:91]
TP3327_%=:
s_cbranch_execz TP5482_%=
ds_read_u16_d16_hi v58,v58
v_rcp_f16_sdwa v9,v8 dst_sel:WORD_1 dst_unused:UNUSED_PRESERVE src0_sel:WORD_1
v_mfma_f32_16x16x32_bf16 v[128:131],a[228:231],v[24:27],v[128:131]
ds_read_u16 v57,v57
ds_read_u16_d16_hi v59,v59
v_mfma_f32_16x16x32_bf16 v[132:135],a[228:231],v[28:31],v[132:135]
ds_read_b64_tr_b16 a[200:201],v2 offset:12320
ds_read_b64_tr_b16 a[202:203],v2 offset:13344
v_lshlrev_b32_sdwa v54,1,v54 dst_sel:DWORD dst_unused:UNUSED_PAD src0_sel:DWORD src1_sel:WORD_1
v_mfma_f32_16x16x32_bf16 v[108:111],a[248:251],v[28:31],v[108:111]
ds_read_u16 v52,v52
v_fma_mix_f32 v53,v53,v9,0 op_sel_hi:[0,1,0]
v_mfma_f32_16x16x32_bf16 v[104:107],a[248:251],v[24:27],v[104:107]
s_waitcnt lgkmcnt(9)
ds_read_u16_d16_hi v54,v54
v_fma_mix_f32 v55,v55,v9,0 op_sel:[0,1,0] op_sel_hi:[0,1,0]
v_mfma_f32_16x16x32_bf16 v[28:31],a[220:223],a[0:3],v[20:23]
s_add_u32 m0,s57,70720
v_lshlrev_b32_sdwa v40,1,v40 dst_sel:DWORD dst_unused:UNUSED_PAD src0_sel:DWORD src1_sel:WORD_1
v_fma_mixlo_f16 v8,v49,s99,0
v_mfma_f32_16x16x32_bf16 v[28:31],a[216:219],a[16:19],v[28:31]
s_add_u32 s89,s56,4423680
ds_read_u16 v40,v40
v_lshlrev_b32_sdwa v42,1,v42 dst_sel:DWORD dst_unused:UNUSED_PAD src0_sel:DWORD src1_sel:WORD_1
v_mfma_f32_16x16x32_bf16 v[28:31],a[212:215],a[32:35],v[28:31]
buffer_load_dwordx4 v6,s[36:39],s89 offen lds
v_lshl_add_u64 v[32:33],v[32:33],0,v[34:35]
v_mfma_f32_16x16x32_bf16 v[24:27],a[208:211],a[32:35],v[16:19]
s_waitcnt lgkmcnt(8)
ds_read_u16_d16_hi v42,v42
v_lshl_add_u64 v[34:35],v[60:61],0,v[62:63]
v_fma_mixhi_f16 v8,v51,s99,0
v_mfma_f32_16x16x32_bf16 v[24:27],a[204:207],a[16:19],v[24:27]
s_waitcnt lgkmcnt(4)
v_lshl_add_u64 v[60:61],v[56:57],0,v[58:59]
v_lshlrev_b32_sdwa v41,1,v41 dst_sel:DWORD dst_unused:UNUSED_PAD src0_sel:DWORD src1_sel:WORD_1
v_pk_fma_f16 v9,v8,v8,s54
v_mfma_f32_16x16x32_bf16 v[24:27],a[200:203],a[0:3],v[24:27]
ds_read_u16 v41,v41
v_lshlrev_b32_sdwa v43,1,v43 dst_sel:DWORD dst_unused:UNUSED_PAD src0_sel:DWORD src1_sel:WORD_1
v_mfma_f32_16x16x32_bf16 v[56:59],a[200:203],a[4:7],v[16:19]
s_waitcnt lgkmcnt(3)
ds_read_u16_d16_hi v43,v43
v_or_b32 v62,v52,v54
v_pk_mul_f16 v9,v8,v9
v_mfma_f32_16x16x32_bf16 v[56:59],a[204:207],a[20:23],v[56:59]
s_waitcnt vmcnt(15)
v_cvt_pk_bf16_f32 v63,v53,v55
v_lshlrev_b32_sdwa v36,1,v36 dst_sel:DWORD dst_unused:UNUSED_PAD src0_sel:DWORD src1_sel:WORD_1
v_mfma_f32_16x16x32_bf16 v[56:59],a[208:211],a[36:39],v[56:59]
s_barrier
v_exp_f16 v8,v9
v_mfma_f32_16x16x32_bf16 v[52:55],a[212:215],a[36:39],v[20:23]
s_add_u32 m0,s57,74264
ds_read_u16 v36,v36
v_lshlrev_b32_sdwa v38,1,v38 dst_sel:DWORD dst_unused:UNUSED_PAD src0_sel:DWORD src1_sel:WORD_1
v_mfma_f32_16x16x32_bf16 v[52:55],a[216:219],a[20:23],v[52:55]
s_add_u32 s89,s56,4521984
ds_read_u16_d16_hi v38,v38
v_lshlrev_b32_sdwa v37,1,v37 dst_sel:DWORD dst_unused:UNUSED_PAD src0_sel:DWORD src1_sel:WORD_1
v_mfma_f32_16x16x32_bf16 v[52:55],a[220:223],a[4:7],v[52:55]
v_exp_f16_sdwa v8,v9 dst_sel:WORD_1 dst_unused:UNUSED_PRESERVE src0_sel:WORD_1
v_mfma_f32_16x16x32_bf16 v[232:235],a[224:227],v[32:35],v[232:235]
buffer_load_dwordx4 v6,s[36:39],s89 offen lds
v_lshlrev_b32_sdwa v39,1,v39 dst_sel:DWORD dst_unused:UNUSED_PAD src0_sel:DWORD src1_sel:WORD_1
v_mfma_f32_16x16x32_bf16 v[236:239],a[224:227],v[60:63],v[236:239]
s_add_u32 m0,s58,12320
ds_read_u16 v37,v37
v_lshlrev_b32_sdwa v44,1,v44 dst_sel:DWORD dst_unused:UNUSED_PAD src0_sel:DWORD src1_sel:WORD_1
v_mfma_f32_16x16x32_bf16 v[164:167],a[232:235],v[60:63],v[164:167]
s_add_u32 s69,s69,s59
ds_read_u16_d16_hi v39,v39
v_pk_fma_f16 v8,v8,s55,s55
v_mfma_f32_16x16x32_bf16 v[160:163],a[232:235],v[32:35],v[160:163]
v_rcp_f16 v9,v8
v_lshlrev_b32_sdwa v46,1,v46 dst_sel:DWORD dst_unused:UNUSED_PAD src0_sel:DWORD src1_sel:WORD_1
v_mfma_f32_16x16x32_bf16 v[240:243],a[236:239],v[32:35],v[240:243]
buffer_load_dwordx4 v5,s[44:47],s69 offen offset:0 lds
v_lshlrev_b32_sdwa v45,1,v45 dst_sel:DWORD dst_unused:UNUSED_PAD src0_sel:DWORD src1_sel:WORD_1
v_mfma_f32_16x16x32_bf16 v[244:247],a[236:239],v[60:63],v[244:247]
s_add_u32 s69,s69,s53
ds_read_u16 v44,v44
v_lshlrev_b32_sdwa v47,1,v47 dst_sel:DWORD dst_unused:UNUSED_PAD src0_sel:DWORD src1_sel:WORD_1
v_mfma_f32_16x16x32_bf16 v[180:183],a[244:247],v[60:63],v[180:183]
buffer_load_dwordx4 v5,s[44:47],s69 offen offset:1024 lds
v_lshlrev_b32_sdwa v48,1,v48 dst_sel:DWORD dst_unused:UNUSED_PAD src0_sel:DWORD src1_sel:WORD_1
v_mfma_f32_16x16x32_bf16 v[176:179],a[244:247],v[32:35],v[176:179]
v_rcp_f16_sdwa v9,v8 dst_sel:WORD_1 dst_unused:UNUSED_PRESERVE src0_sel:WORD_1
v_mfma_f32_16x16x32_bf16 v[248:251],a[228:231],v[32:35],v[248:251]
ds_read_u16_d16_hi v46,v46
ds_read_u16 v45,v45
v_mfma_f32_16x16x32_bf16 v[252:255],a[228:231],v[60:63],v[252:255]
s_add_u32 s69,s69,s53
ds_read_u16_d16_hi v47,v47
v_lshlrev_b32_sdwa v50,1,v50 dst_sel:DWORD dst_unused:UNUSED_PAD src0_sel:DWORD src1_sel:WORD_1
v_mfma_f32_16x16x32_bf16 v[196:199],a[248:251],v[60:63],v[196:199]
buffer_load_dwordx4 v5,s[44:47],s69 offen offset:2048 lds
v_fma_mix_f32 v49,v49,v9,0 op_sel_hi:[0,1,0]
v_mfma_f32_16x16x32_bf16 v[192:195],a[248:251],v[32:35],v[192:195]
ds_read_u16 v48,v48
ds_read_u16_d16_hi v50,v50
v_fma_mix_f32 v51,v51,v9,0 op_sel:[0,1,0] op_sel_hi:[0,1,0]
v_mfma_f32_16x16x32_bf16 v[60:63],a[220:223],a[8:11],v[20:23]
ds_read_b64_tr_b16 a[252:253],v3 offset:18480
v_lshlrev_b32_sdwa v28,1,v28 dst_sel:DWORD dst_unused:UNUSED_PAD src0_sel:DWORD src1_sel:WORD_1
v_mfma_f32_16x16x32_bf16 v[60:63],a[216:219],a[24:27],v[60:63]
ds_read_u16 v28,v28
v_fma_mixlo_f16 v8,v57,s99,0
v_mfma_f32_16x16x32_bf16 v[60:63],a[212:215],a[40:43],v[60:63]
s_waitcnt lgkmcnt(7)
ds_read_b64_tr_b16 a[254:255],v3 offset:20016
v_lshl_add_u64 v[40:41],v[40:41],0,v[42:43]
v_lshlrev_b32_sdwa v30,1,v30 dst_sel:DWORD dst_unused:UNUSED_PAD src0_sel:DWORD src1_sel:WORD_1
v_mfma_f32_16x16x32_bf16 v[32:35],a[208:211],a[40:43],v[16:19]
ds_read_u16_d16_hi v30,v30
v_lshl_add_u64 v[42:43],v[36:37],0,v[38:39]
v_fma_mixhi_f16 v8,v59,s99,0
v_mfma_f32_16x16x32_bf16 v[32:35],a[204:207],a[24:27],v[32:35]
s_waitcnt lgkmcnt(6)
ds_read_b64_tr_b16 a[240:241],v3 offset:18488
v_lshl_add_u64 v[36:37],v[44:45],0,v[46:47]
v_lshlrev_b32_sdwa v29,1,v29 dst_sel:DWORD dst_unused:UNUSED_PAD src0_sel:DWORD src1_sel:WORD_1
v_pk_fma_f16 v9,v8,v8,s54
v_mfma_f32_16x16x32_bf16 v[32:35],a[200:203],a[8:11],v[32:35]
ds_read_u16 v29,v29
v_lshlrev_b32_sdwa v31,1,v31 dst_sel:DWORD dst_unused:UNUSED_PAD src0_sel:DWORD src1_sel:WORD_1
v_mfma_f32_16x16x32_bf16 v[44:47],a[200:203],a[12:15],v[16:19]
s_waitcnt lgkmcnt(6)
ds_read_u16_d16_hi v31,v31
v_or_b32 v38,v48,v50
v_pk_mul_f16 v9,v8,v9
v_mfma_f32_16x16x32_bf16 v[44:47],a[204:207],a[28:31],v[44:47]
ds_read_b64_tr_b16 a[242:243],v3 offset:20024
v_cvt_pk_bf16_f32 v39,v49,v51
v_lshlrev_b32_sdwa v24,1,v24 dst_sel:DWORD dst_unused:UNUSED_PAD src0_sel:DWORD src1_sel:WORD_1
v_mfma_f32_16x16x32_bf16 v[44:47],a[208:211],a[44:47],v[44:47]
v_exp_f16 v8,v9
v_mfma_f32_16x16x32_bf16 v[48:51],a[212:215],a[44:47],v[20:23]
ds_read_u16 v24,v24
v_lshlrev_b32_sdwa v26,1,v26 dst_sel:DWORD dst_unused:UNUSED_PAD src0_sel:DWORD src1_sel:WORD_1
v_mfma_f32_16x16x32_bf16 v[48:51],a[216:219],a[28:31],v[48:51]
ds_read_u16_d16_hi v26,v26
v_lshlrev_b32_sdwa v25,1,v25 dst_sel:DWORD dst_unused:UNUSED_PAD src0_sel:DWORD src1_sel:WORD_1
v_mfma_f32_16x16x32_bf16 v[48:51],a[220:223],a[12:15],v[48:51]
v_exp_f16_sdwa v8,v9 dst_sel:WORD_1 dst_unused:UNUSED_PRESERVE src0_sel:WORD_1
v_mfma_f32_16x16x32_bf16 v[208:211],a[224:227],v[40:43],v[208:211]
ds_read_u16 v25,v25
v_lshlrev_b32_sdwa v27,1,v27 dst_sel:DWORD dst_unused:UNUSED_PAD src0_sel:DWORD src1_sel:WORD_1
v_mfma_f32_16x16x32_bf16 v[212:215],a[224:227],v[36:39],v[212:215]
ds_read_u16_d16_hi v27,v27
v_lshlrev_b32_sdwa v52,1,v52 dst_sel:DWORD dst_unused:UNUSED_PAD src0_sel:DWORD src1_sel:WORD_1
v_mfma_f32_16x16x32_bf16 v[172:175],a[232:235],v[36:39],v[172:175]
ds_read_u16 v52,v52
v_pk_fma_f16 v8,v8,s55,s55
v_mfma_f32_16x16x32_bf16 v[168:171],a[232:235],v[40:43],v[168:171]
v_rcp_f16 v9,v8
v_lshlrev_b32_sdwa v54,1,v54 dst_sel:DWORD dst_unused:UNUSED_PAD src0_sel:DWORD src1_sel:WORD_1
v_mfma_f32_16x16x32_bf16 v[216:219],a[236:239],v[40:43],v[216:219]
ds_read_u16_d16_hi v54,v54
v_lshlrev_b32_sdwa v53,1,v53 dst_sel:DWORD dst_unused:UNUSED_PAD src0_sel:DWORD src1_sel:WORD_1
v_mfma_f32_16x16x32_bf16 v[220:223],a[236:239],v[36:39],v[220:223]
ds_read_u16 v53,v53
ds_read_b64_tr_b16 a[224:225],v3 offset:18544
v_mfma_f32_16x16x32_bf16 v[188:191],a[244:247],v[36:39],v[188:191]
ds_read_b64_tr_b16 a[226:227],v3 offset:20080
v_lshlrev_b32_sdwa v55,1,v55 dst_sel:DWORD dst_unused:UNUSED_PAD src0_sel:DWORD src1_sel:WORD_1
v_mfma_f32_16x16x32_bf16 v[184:187],a[244:247],v[40:43],v[184:187]
ds_read_u16_d16_hi v55,v55
v_rcp_f16_sdwa v9,v8 dst_sel:WORD_1 dst_unused:UNUSED_PRESERVE src0_sel:WORD_1
v_mfma_f32_16x16x32_bf16 v[224:227],a[228:231],v[40:43],v[224:227]
ds_read_b64_tr_b16 a[232:233],v3 offset:18552
v_lshlrev_b32_sdwa v56,1,v56 dst_sel:DWORD dst_unused:UNUSED_PAD src0_sel:DWORD src1_sel:WORD_1
v_mfma_f32_16x16x32_bf16 v[228:231],a[228:231],v[36:39],v[228:231]
ds_read_u16 v56,v56
v_lshlrev_b32_sdwa v58,1,v58 dst_sel:DWORD dst_unused:UNUSED_PAD src0_sel:DWORD src1_sel:WORD_1
v_mfma_f32_16x16x32_bf16 v[204:207],a[248:251],v[36:39],v[204:207]
ds_read_u16_d16_hi v58,v58
v_fma_mix_f32 v57,v57,v9,0 op_sel_hi:[0,1,0]
v_mfma_f32_16x16x32_bf16 v[200:203],a[248:251],v[40:43],v[200:203]
ds_read_b64_tr_b16 a[234:235],v3 offset:20088
v_fma_mix_f32 v59,v59,v9,0 op_sel:[0,1,0] op_sel_hi:[0,1,0]
v_mfma_f32_16x16x32_bf16 v[36:39],a[220:223],a[48:51],v[20:23]
s_add_u32 m0,s57,77800
ds_read_b64 v[14:15],v7 offset:18480
v_lshlrev_b32_sdwa v60,1,v60 dst_sel:DWORD dst_unused:UNUSED_PAD src0_sel:DWORD src1_sel:WORD_1
v_mfma_f32_16x16x32_bf16 v[36:39],a[216:219],a[64:67],v[36:39]
s_add_u32 s89,s56,4620288
ds_read_b64_tr_b16 a[236:237],v3 offset:18608
v_fma_mixlo_f16 v8,v45,s99,0
v_mfma_f32_16x16x32_bf16 v[36:39],a[212:215],a[80:83],v[36:39]
s_waitcnt lgkmcnt(8)
buffer_load_dwordx4 v6,s[36:39],s89 offen lds
v_lshl_add_u64 v[28:29],v[28:29],0,v[30:31]
v_lshlrev_b32_sdwa v62,1,v62 dst_sel:DWORD dst_unused:UNUSED_PAD src0_sel:DWORD src1_sel:WORD_1
v_mfma_f32_16x16x32_bf16 v[40:43],a[208:211],a[80:83],v[16:19]
ds_read_b64_tr_b16 a[238:239],v3 offset:20144
v_lshl_add_u64 v[30:31],v[24:25],0,v[26:27]
v_fma_mixhi_f16 v8,v47,s99,0
v_mfma_f32_16x16x32_bf16 v[40:43],a[204:207],a[64:67],v[40:43]
s_waitcnt lgkmcnt(7)
ds_read_b64_tr_b16 a[244:245],v3 offset:18616
v_lshl_add_u64 v[24:25],v[52:53],0,v[54:55]
v_lshlrev_b32_sdwa v61,1,v61 dst_sel:DWORD dst_unused:UNUSED_PAD src0_sel:DWORD src1_sel:WORD_1
v_pk_fma_f16 v9,v8,v8,s54
v_mfma_f32_16x16x32_bf16 v[40:43],a[200:203],a[48:51],v[40:43]
s_waitcnt lgkmcnt(3)
ds_read_b64_tr_b16 a[246:247],v3 offset:20152
v_or3_b32 v13,v14,v15,v13
v_lshlrev_b32_sdwa v63,1,v63 dst_sel:DWORD dst_unused:UNUSED_PAD src0_sel:DWORD src1_sel:WORD_1
v_mfma_f32_16x16x32_bf16 v[52:55],a[200:203],a[52:55],v[16:19]
ds_read_b64 v[14:15],v7 offset:18992
v_or_b32 v26,v56,v58
v_pk_mul_f16 v9,v8,v9
v_mfma_f32_16x16x32_bf16 v[52:55],a[204:207],a[68:71],v[52:55]
ds_read_u16 v60,v60
v_cvt_pk_bf16_f32 v27,v57,v59
v_lshlrev_b32_sdwa v32,1,v32 dst_sel:DWORD dst_unused:UNUSED_PAD src0_sel:DWORD src1_sel:WORD_1
v_mfma_f32_16x16x32_bf16 v[52:55],a[208:211],a[84:87],v[52:55]
v_exp_f16 v8,v9
v_mfma_f32_16x16x32_bf16 v[56:59],a[212:215],a[84:87],v[20:23]
ds_read_u16_d16_hi v62,v62
v_lshlrev_b32_sdwa v34,1,v34 dst_sel:DWORD dst_unused:UNUSED_PAD src0_sel:DWORD src1_sel:WORD_1
v_mfma_f32_16x16x32_bf16 v[56:59],a[216:219],a[68:71],v[56:59]
ds_read_u16 v61,v61
ds_read_u16_d16_hi v63,v63
v_lshlrev_b32_sdwa v33,1,v33 dst_sel:DWORD dst_unused:UNUSED_PAD src0_sel:DWORD src1_sel:WORD_1
v_mfma_f32_16x16x32_bf16 v[56:59],a[220:223],a[52:55],v[56:59]
s_waitcnt lgkmcnt(4)
ds_read_u16 v32,v32
ds_read_u16_d16_hi v34,v34
v_or3_b32 v13,v14,v15,v13
v_mfma_f32_16x16x32_bf16 v[136:139],a[252:255],v[28:31],v[136:139]
ds_read_b64 v[14:15],v7 offset:19504
v_exp_f16_sdwa v8,v9 dst_sel:WORD_1 dst_unused:UNUSED_PRESERVE src0_sel:WORD_1
v_lshlrev_b32_sdwa v35,1,v35 dst_sel:DWORD dst_unused:UNUSED_PAD src0_sel:DWORD src1_sel:WORD_1
v_mfma_f32_16x16x32_bf16 v[140:143],a[252:255],v[24:27],v[140:143]
ds_read_u16 v33,v33
v_lshlrev_b32_sdwa v48,1,v48 dst_sel:DWORD dst_unused:UNUSED_PAD src0_sel:DWORD src1_sel:WORD_1
v_mfma_f32_16x16x32_bf16 v[68:71],a[240:243],v[24:27],v[68:71]
ds_read_u16_d16_hi v35,v35
v_pk_fma_f16 v8,v8,s55,s55
v_mfma_f32_16x16x32_bf16 v[64:67],a[240:243],v[28:31],v[64:67]
v_rcp_f16 v9,v8
v_lshlrev_b32_sdwa v50,1,v50 dst_sel:DWORD dst_unused:UNUSED_PAD src0_sel:DWORD src1_sel:WORD_1
v_mfma_f32_16x16x32_bf16 v[144:147],a[224:227],v[28:31],v[144:147]
ds_read_u16 v48,v48
v_lshlrev_b32_sdwa v49,1,v49 dst_sel:DWORD dst_unused:UNUSED_PAD src0_sel:DWORD src1_sel:WORD_1
v_mfma_f32_16x16x32_bf16 v[148:151],a[224:227],v[24:27],v[148:151]
s_waitcnt lgkmcnt(3)
ds_read_u16_d16_hi v50,v50
v_or3_b32 v13,v14,v15,v13
v_mfma_f32_16x16x32_bf16 v[84:87],a[232:235],v[24:27],v[84:87]
ds_read_u16 v49,v49
v_lshlrev_b32_sdwa v51,1,v51 dst_sel:DWORD dst_unused:UNUSED_PAD src0_sel:DWORD src1_sel:WORD_1
v_mfma_f32_16x16x32_bf16 v[80:83],a[232:235],v[28:31],v[80:83]
v_rcp_f16_sdwa v9,v8 dst_sel:WORD_1 dst_unused:UNUSED_PRESERVE src0_sel:WORD_1
v_mfma_f32_16x16x32_bf16 v[152:155],a[236:239],v[28:31],v[152:155]
ds_read_u16_d16_hi v51,v51
v_lshlrev_b32_sdwa v44,1,v44 dst_sel:DWORD dst_unused:UNUSED_PAD src0_sel:DWORD src1_sel:WORD_1
v_mfma_f32_16x16x32_bf16 v[156:159],a[236:239],v[24:27],v[156:159]
ds_read_u16 v44,v44
v_lshlrev_b32_sdwa v46,1,v46 dst_sel:DWORD dst_unused:UNUSED_PAD src0_sel:DWORD src1_sel:WORD_1
v_mfma_f32_16x16x32_bf16 v[100:103],a[244:247],v[24:27],v[100:103]
ds_read_u16_d16_hi v46,v46
v_fma_mix_f32 v45,v45,v9,0 op_sel_hi:[0,1,0]
v_mfma_f32_16x16x32_bf16 v[96:99],a[244:247],v[28:31],v[96:99]
s_add_u32 m0,s57,81328
ds_read_b64_tr_b16 a[192:193],v1 offset:18480
v_fma_mix_f32 v47,v47,v9,0 op_sel:[0,1,0] op_sel_hi:[0,1,0]
v_mfma_f32_16x16x32_bf16 v[24:27],a[220:223],a[56:59],v[20:23]
s_add_u32 s89,s56,4718592
ds_read_b64_tr_b16 a[194:195],v1 offset:19504
v_lshlrev_b32_sdwa v36,1,v36 dst_sel:DWORD dst_unused:UNUSED_PAD src0_sel:DWORD src1_sel:WORD_1
v_mfma_f32_16x16x32_bf16 v[24:27],a[216:219],a[72:75],v[24:27]
buffer_load_dwordx4 v6,s[36:39],s89 offen lds
v_fma_mixlo_f16 v8,v53,s99,0
v_mfma_f32_16x16x32_bf16 v[24:27],a[212:215],a[88:91],v[24:27]
ds_read_b64_tr_b16 a[196:197],v1 offset:20528
v_lshl_add_u64 v[60:61],v[60:61],0,v[62:63]
v_lshlrev_b32_sdwa v38,1,v38 dst_sel:DWORD dst_unused:UNUSED_PAD src0_sel:DWORD src1_sel:WORD_1
v_mfma_f32_16x16x32_bf16 v[28:31],a[208:211],a[88:91],v[16:19]
s_waitcnt lgkmcnt(6)
ds_read_b64_tr_b16 a[198:199],v1 offset:21552
v_lshl_add_u64 v[62:63],v[32:33],0,v[34:35]
v_fma_mixhi_f16 v8,v55,s99,0
v_mfma_f32_16x16x32_bf16 v[28:31],a[204:207],a[72:75],v[28:31]
s_waitcnt lgkmcnt(6)
ds_read_u16 v36,v36
v_lshl_add_u64 v[32:33],v[48:49],0,v[50:51]
v_lshlrev_b32_sdwa v37,1,v37 dst_sel:DWORD dst_unused:UNUSED_PAD src0_sel:DWORD src1_sel:WORD_1
v_pk_fma_f16 v9,v8,v8,s54
v_mfma_f32_16x16x32_bf16 v[28:31],a[200:203],a[56:59],v[28:31]
ds_read_u16_d16_hi v38,v38
v_lshlrev_b32_sdwa v39,1,v39 dst_sel:DWORD dst_unused:UNUSED_PAD src0_sel:DWORD src1_sel:WORD_1
v_mfma_f32_16x16x32_bf16 v[48:51],a[200:203],a[60:63],v[16:19]
s_waitcnt lgkmcnt(6)
ds_read_b128 v[16:19],v4 offset:1024
v_or_b32 v34,v44,v46
v_pk_mul_f16 v9,v8,v9
v_mfma_f32_16x16x32_bf16 v[48:51],a[204:207],a[76:79],v[48:51]
ds_read_u16 v37,v37
v_cvt_pk_bf16_f32 v35,v45,v47
v_lshlrev_b32_sdwa v40,1,v40 dst_sel:DWORD dst_unused:UNUSED_PAD src0_sel:DWORD src1_sel:WORD_1
v_mfma_f32_16x16x32_bf16 v[48:51],a[208:211],a[92:95],v[48:51]
ds_read_u16_d16_hi v39,v39
v_exp_f16 v8,v9
v_mfma_f32_16x16x32_bf16 v[44:47],a[212:215],a[92:95],v[20:23]
ds_read_b128 v[20:23],v4 offset:1056
ds_read_b64_tr_b16 a[200:201],v1 offset:22576
v_lshlrev_b32_sdwa v42,1,v42 dst_sel:DWORD dst_unused:UNUSED_PAD src0_sel:DWORD src1_sel:WORD_1
v_mfma_f32_16x16x32_bf16 v[44:47],a[216:219],a[76:79],v[44:47]
ds_read_b64_tr_b16 a[202:203],v1 offset:23600
ds_read_b64_tr_b16 a[204:205],v2 offset:22576
v_lshlrev_b32_sdwa v41,1,v41 dst_sel:DWORD dst_unused:UNUSED_PAD src0_sel:DWORD src1_sel:WORD_1
v_mfma_f32_16x16x32_bf16 v[44:47],a[220:223],a[60:63],v[44:47]
ds_read_b64_tr_b16 a[206:207],v2 offset:23600
v_exp_f16_sdwa v8,v9 dst_sel:WORD_1 dst_unused:UNUSED_PRESERVE src0_sel:WORD_1
v_mfma_f32_16x16x32_bf16 v[112:115],a[252:255],v[60:63],v[112:115]
ds_read_u16 v40,v40
ds_read_u16_d16_hi v42,v42
v_lshlrev_b32_sdwa v43,1,v43 dst_sel:DWORD dst_unused:UNUSED_PAD src0_sel:DWORD src1_sel:WORD_1
v_mfma_f32_16x16x32_bf16 v[116:119],a[252:255],v[32:35],v[116:119]
ds_read_u16 v41,v41
v_lshlrev_b32_sdwa v56,1,v56 dst_sel:DWORD dst_unused:UNUSED_PAD src0_sel:DWORD src1_sel:WORD_1
v_mfma_f32_16x16x32_bf16 v[76:79],a[240:243],v[32:35],v[76:79]
ds_read_u16_d16_hi v43,v43
v_pk_fma_f16 v8,v8,s55,s55
v_mfma_f32_16x16x32_bf16 v[72:75],a[240:243],v[60:63],v[72:75]
v_rcp_f16 v9,v8
v_lshlrev_b32_sdwa v58,1,v58 dst_sel:DWORD dst_unused:UNUSED_PAD src0_sel:DWORD src1_sel:WORD_1
v_mfma_f32_16x16x32_bf16 v[120:123],a[224:227],v[60:63],v[120:123]
ds_read_b64_tr_b16 a[208:209],v2 offset:20528
v_lshlrev_b32_sdwa v57,1,v57 dst_sel:DWORD dst_unused:UNUSED_PAD src0_sel:DWORD src1_sel:WORD_1
v_mfma_f32_16x16x32_bf16 v[124:127],a[224:227],v[32:35],v[124:127]
ds_read_b64_tr_b16 a[210:211],v2 offset:21552
v_lshlrev_b32_sdwa v59,1,v59 dst_sel:DWORD dst_unused:UNUSED_PAD src0_sel:DWORD src1_sel:WORD_1
v_mfma_f32_16x16x32_bf16 v[92:95],a[232:235],v[32:35],v[92:95]
ds_read_u16 v56,v56
v_lshlrev_b32_sdwa v52,1,v52 dst_sel:DWORD dst_unused:UNUSED_PAD src0_sel:DWORD src1_sel:WORD_1
v_mfma_f32_16x16x32_bf16 v[88:91],a[232:235],v[60:63],v[88:91]
ds_read_u16_d16_hi v58,v58
v_rcp_f16_sdwa v9,v8 dst_sel:WORD_1 dst_unused:UNUSED_PRESERVE src0_sel:WORD_1
v_mfma_f32_16x16x32_bf16 v[128:131],a[236:239],v[60:63],v[128:131]
ds_read_u16 v57,v57
ds_read_u16_d16_hi v59,v59
v_mfma_f32_16x16x32_bf16 v[132:135],a[236:239],v[32:35],v[132:135]
ds_read_b64_tr_b16 a[212:213],v2 offset:18480
ds_read_b64_tr_b16 a[214:215],v2 offset:19504
v_lshlrev_b32_sdwa v54,1,v54 dst_sel:DWORD dst_unused:UNUSED_PAD src0_sel:DWORD src1_sel:WORD_1
v_mfma_f32_16x16x32_bf16 v[108:111],a[244:247],v[32:35],v[108:111]
ds_read_u16 v52,v52
v_fma_mix_f32 v53,v53,v9,0 op_sel_hi:[0,1,0]
v_mfma_f32_16x16x32_bf16 v[104:107],a[244:247],v[60:63],v[104:107]
s_waitcnt lgkmcnt(9)
ds_read_u16_d16_hi v54,v54
v_fma_mix_f32 v55,v55,v9,0 op_sel:[0,1,0] op_sel_hi:[0,1,0]
v_mfma_f32_16x16x32_bf16 v[32:35],a[192:195],a[0:3],v[16:19]
v_lshlrev_b32_sdwa v24,1,v24 dst_sel:DWORD dst_unused:UNUSED_PAD src0_sel:DWORD src1_sel:WORD_1
v_fma_mixlo_f16 v8,v49,s99,0
v_mfma_f32_16x16x32_bf16 v[32:35],a[196:199],a[16:19],v[32:35]
ds_read_u16 v24,v24
v_lshlrev_b32_sdwa v26,1,v26 dst_sel:DWORD dst_unused:UNUSED_PAD src0_sel:DWORD src1_sel:WORD_1
v_mfma_f32_16x16x32_bf16 v[32:35],a[200:203],a[32:35],v[32:35]
ds_read_u16_d16_hi v26,v26
v_lshl_add_u64 v[36:37],v[36:37],0,v[38:39]
v_mfma_f32_16x16x32_bf16 v[60:63],a[204:207],a[32:35],v[20:23]
s_waitcnt lgkmcnt(9)
v_lshl_add_u64 v[38:39],v[40:41],0,v[42:43]
v_fma_mixhi_f16 v8,v51,s99,0
v_mfma_f32_16x16x32_bf16 v[60:63],a[208:211],a[16:19],v[60:63]
s_waitcnt lgkmcnt(4)
v_lshl_add_u64 v[40:41],v[56:57],0,v[58:59]
v_lshlrev_b32_sdwa v25,1,v25 dst_sel:DWORD dst_unused:UNUSED_PAD src0_sel:DWORD src1_sel:WORD_1
v_pk_fma_f16 v9,v8,v8,s54
v_mfma_f32_16x16x32_bf16 v[60:63],a[212:215],a[0:3],v[60:63]
s_add_u32 m0,s57,84864
ds_read_u16 v25,v25
v_lshlrev_b32_sdwa v27,1,v27 dst_sel:DWORD dst_unused:UNUSED_PAD src0_sel:DWORD src1_sel:WORD_1
v_mfma_f32_16x16x32_bf16 v[56:59],a[212:215],a[4:7],v[20:23]
s_waitcnt lgkmcnt(3)
ds_read_u16_d16_hi v27,v27
v_or_b32 v42,v52,v54
v_pk_mul_f16 v9,v8,v9
v_mfma_f32_16x16x32_bf16 v[56:59],a[208:211],a[20:23],v[56:59]
s_waitcnt vmcnt(18)
v_cvt_pk_bf16_f32 v43,v53,v55
v_lshlrev_b32_sdwa v28,1,v28 dst_sel:DWORD dst_unused:UNUSED_PAD src0_sel:DWORD src1_sel:WORD_1
v_mfma_f32_16x16x32_bf16 v[56:59],a[204:207],a[36:39],v[56:59]
s_barrier
v_exp_f16 v8,v9
v_mfma_f32_16x16x32_bf16 v[52:55],a[200:203],a[36:39],v[16:19]
s_add_u32 s89,s56,4816896
ds_read_u16 v28,v28
v_lshlrev_b32_sdwa v30,1,v30 dst_sel:DWORD dst_unused:UNUSED_PAD src0_sel:DWORD src1_sel:WORD_1
v_mfma_f32_16x16x32_bf16 v[52:55],a[196:199],a[20:23],v[52:55]
buffer_load_dwordx4 v6,s[36:39],s89 offen lds
v_lshlrev_b32_sdwa v29,1,v29 dst_sel:DWORD dst_unused:UNUSED_PAD src0_sel:DWORD src1_sel:WORD_1
v_mfma_f32_16x16x32_bf16 v[52:55],a[192:195],a[4:7],v[52:55]
s_add_u32 m0,s58,18480
v_exp_f16_sdwa v8,v9 dst_sel:WORD_1 dst_unused:UNUSED_PRESERVE src0_sel:WORD_1
v_mfma_f32_16x16x32_bf16 v[232:235],a[252:255],v[36:39],v[232:235]
s_mov_b32 s69,s88
ds_read_u16_d16_hi v30,v30
v_lshlrev_b32_sdwa v31,1,v31 dst_sel:DWORD dst_unused:UNUSED_PAD src0_sel:DWORD src1_sel:WORD_1
v_mfma_f32_16x16x32_bf16 v[236:239],a[252:255],v[40:43],v[236:239]
buffer_load_dwordx4 v5,s[44:47],s69 offen offset:0 lds
v_lshlrev_b32_sdwa v44,1,v44 dst_sel:DWORD dst_unused:UNUSED_PAD src0_sel:DWORD src1_sel:WORD_1
v_mfma_f32_16x16x32_bf16 v[164:167],a[240:243],v[40:43],v[164:167]
ds_read_u16 v29,v29
v_pk_fma_f16 v8,v8,s55,s55
v_mfma_f32_16x16x32_bf16 v[160:163],a[240:243],v[36:39],v[160:163]
v_rcp_f16 v9,v8
v_lshlrev_b32_sdwa v46,1,v46 dst_sel:DWORD dst_unused:UNUSED_PAD src0_sel:DWORD src1_sel:WORD_1
v_mfma_f32_16x16x32_bf16 v[240:243],a[224:227],v[36:39],v[240:243]
s_add_u32 s69,s69,s53
ds_read_u16_d16_hi v31,v31
v_lshlrev_b32_sdwa v45,1,v45 dst_sel:DWORD dst_unused:UNUSED_PAD src0_sel:DWORD src1_sel:WORD_1
v_mfma_f32_16x16x32_bf16 v[244:247],a[224:227],v[40:43],v[244:247]
buffer_load_dwordx4 v5,s[44:47],s69 offen offset:1024 lds
v_lshlrev_b32_sdwa v47,1,v47 dst_sel:DWORD dst_unused:UNUSED_PAD src0_sel:DWORD src1_sel:WORD_1
v_mfma_f32_16x16x32_bf16 v[180:183],a[232:235],v[40:43],v[180:183]
ds_read_u16 v44,v44
v_lshlrev_b32_sdwa v48,1,v48 dst_sel:DWORD dst_unused:UNUSED_PAD src0_sel:DWORD src1_sel:WORD_1
v_mfma_f32_16x16x32_bf16 v[176:179],a[232:235],v[36:39],v[176:179]
v_rcp_f16_sdwa v9,v8 dst_sel:WORD_1 dst_unused:UNUSED_PRESERVE src0_sel:WORD_1
v_mfma_f32_16x16x32_bf16 v[248:251],a[236:239],v[36:39],v[248:251]
ds_read_u16_d16_hi v46,v46
ds_read_u16 v45,v45
v_mfma_f32_16x16x32_bf16 v[252:255],a[236:239],v[40:43],v[252:255]
s_add_u32 s69,s69,s53
ds_read_u16_d16_hi v47,v47
v_lshlrev_b32_sdwa v50,1,v50 dst_sel:DWORD dst_unused:UNUSED_PAD src0_sel:DWORD src1_sel:WORD_1
v_mfma_f32_16x16x32_bf16 v[196:199],a[244:247],v[40:43],v[196:199]
buffer_load_dwordx4 v5,s[44:47],s69 offen offset:2048 lds
v_fma_mix_f32 v49,v49,v9,0 op_sel_hi:[0,1,0]
v_mfma_f32_16x16x32_bf16 v[192:195],a[244:247],v[36:39],v[192:195]
ds_read_u16 v48,v48
ds_read_u16_d16_hi v50,v50
v_fma_mix_f32 v51,v51,v9,0 op_sel:[0,1,0] op_sel_hi:[0,1,0]
v_mfma_f32_16x16x32_bf16 v[40:43],a[192:195],a[8:11],v[16:19]
ds_read_b64_tr_b16 a[228:229],v3 offset:0
v_lshlrev_b32_sdwa v32,1,v32 dst_sel:DWORD dst_unused:UNUSED_PAD src0_sel:DWORD src1_sel:WORD_1
v_mfma_f32_16x16x32_bf16 v[40:43],a[196:199],a[24:27],v[40:43]
ds_read_u16 v32,v32
v_fma_mixlo_f16 v8,v57,s99,0
v_mfma_f32_16x16x32_bf16 v[40:43],a[200:203],a[40:43],v[40:43]
s_waitcnt lgkmcnt(8)
ds_read_b64_tr_b16 a[230:231],v3 offset:1536
v_lshl_add_u64 v[24:25],v[24:25],0,v[26:27]
v_lshlrev_b32_sdwa v34,1,v34 dst_sel:DWORD dst_unused:UNUSED_PAD src0_sel:DWORD src1_sel:WORD_1
v_mfma_f32_16x16x32_bf16 v[36:39],a[204:207],a[40:43],v[20:23]
ds_read_u16_d16_hi v34,v34
v_lshl_add_u64 v[26:27],v[28:29],0,v[30:31]
v_fma_mixhi_f16 v8,v59,s99,0
v_mfma_f32_16x16x32_bf16 v[36:39],a[208:211],a[24:27],v[36:39]
s_waitcnt lgkmcnt(6)
ds_read_b64_tr_b16 a[248:249],v3 offset:8
v_lshl_add_u64 v[28:29],v[44:45],0,v[46:47]
v_lshlrev_b32_sdwa v33,1,v33 dst_sel:DWORD dst_unused:UNUSED_PAD src0_sel:DWORD src1_sel:WORD_1
v_pk_fma_f16 v9,v8,v8,s54
v_mfma_f32_16x16x32_bf16 v[36:39],a[212:215],a[8:11],v[36:39]
ds_read_u16 v33,v33
v_lshlrev_b32_sdwa v35,1,v35 dst_sel:DWORD dst_unused:UNUSED_PAD src0_sel:DWORD src1_sel:WORD_1
v_mfma_f32_16x16x32_bf16 v[44:47],a[212:215],a[12:15],v[20:23]
s_waitcnt lgkmcnt(6)
ds_read_u16_d16_hi v35,v35
v_or_b32 v30,v48,v50
v_pk_mul_f16 v9,v8,v9
v_mfma_f32_16x16x32_bf16 v[44:47],a[208:211],a[28:31],v[44:47]
ds_read_b64_tr_b16 a[250:251],v3 offset:1544
v_cvt_pk_bf16_f32 v31,v49,v51
v_lshlrev_b32_sdwa v60,1,v60 dst_sel:DWORD dst_unused:UNUSED_PAD src0_sel:DWORD src1_sel:WORD_1
v_mfma_f32_16x16x32_bf16 v[44:47],a[204:207],a[44:47],v[44:47]
v_exp_f16 v8,v9
v_mfma_f32_16x16x32_bf16 v[48:51],a[200:203],a[44:47],v[16:19]
ds_read_u16 v60,v60
v_lshlrev_b32_sdwa v62,1,v62 dst_sel:DWORD dst_unused:UNUSED_PAD src0_sel:DWORD src1_sel:WORD_1
v_mfma_f32_16x16x32_bf16 v[48:51],a[196:199],a[28:31],v[48:51]
ds_read_u16_d16_hi v62,v62
v_lshlrev_b32_sdwa v61,1,v61 dst_sel:DWORD dst_unused:UNUSED_PAD src0_sel:DWORD src1_sel:WORD_1
v_mfma_f32_16x16x32_bf16 v[48:51],a[192:195],a[12:15],v[48:51]
v_exp_f16_sdwa v8,v9 dst_sel:WORD_1 dst_unused:UNUSED_PRESERVE src0_sel:WORD_1
v_mfma_f32_16x16x32_bf16 v[208:211],a[252:255],v[24:27],v[208:211]
ds_read_u16 v61,v61
v_lshlrev_b32_sdwa v63,1,v63 dst_sel:DWORD dst_unused:UNUSED_PAD src0_sel:DWORD src1_sel:WORD_1
v_mfma_f32_16x16x32_bf16 v[212:215],a[252:255],v[28:31],v[212:215]
ds_read_u16_d16_hi v63,v63
v_lshlrev_b32_sdwa v52,1,v52 dst_sel:DWORD dst_unused:UNUSED_PAD src0_sel:DWORD src1_sel:WORD_1
v_mfma_f32_16x16x32_bf16 v[172:175],a[240:243],v[28:31],v[172:175]
ds_read_u16 v52,v52
v_pk_fma_f16 v8,v8,s55,s55
v_mfma_f32_16x16x32_bf16 v[168:171],a[240:243],v[24:27],v[168:171]
v_rcp_f16 v9,v8
v_lshlrev_b32_sdwa v54,1,v54 dst_sel:DWORD dst_unused:UNUSED_PAD src0_sel:DWORD src1_sel:WORD_1
v_mfma_f32_16x16x32_bf16 v[216:219],a[224:227],v[24:27],v[216:219]
ds_read_u16_d16_hi v54,v54
v_lshlrev_b32_sdwa v53,1,v53 dst_sel:DWORD dst_unused:UNUSED_PAD src0_sel:DWORD src1_sel:WORD_1
v_mfma_f32_16x16x32_bf16 v[220:223],a[224:227],v[28:31],v[220:223]
ds_read_u16 v53,v53
ds_read_b64_tr_b16 a[252:253],v3 offset:64
v_mfma_f32_16x16x32_bf16 v[188:191],a[232:235],v[28:31],v[188:191]
ds_read_b64_tr_b16 a[254:255],v3 offset:1600
v_lshlrev_b32_sdwa v55,1,v55 dst_sel:DWORD dst_unused:UNUSED_PAD src0_sel:DWORD src1_sel:WORD_1
v_mfma_f32_16x16x32_bf16 v[184:187],a[232:235],v[24:27],v[184:187]
ds_read_u16_d16_hi v55,v55
v_rcp_f16_sdwa v9,v8 dst_sel:WORD_1 dst_unused:UNUSED_PRESERVE src0_sel:WORD_1
v_mfma_f32_16x16x32_bf16 v[224:227],a[236:239],v[24:27],v[224:227]
ds_read_b64_tr_b16 a[240:241],v3 offset:72
v_lshlrev_b32_sdwa v56,1,v56 dst_sel:DWORD dst_unused:UNUSED_PAD src0_sel:DWORD src1_sel:WORD_1
v_mfma_f32_16x16x32_bf16 v[228:231],a[236:239],v[28:31],v[228:231]
ds_read_u16 v56,v56
v_lshlrev_b32_sdwa v58,1,v58 dst_sel:DWORD dst_unused:UNUSED_PAD src0_sel:DWORD src1_sel:WORD_1
v_mfma_f32_16x16x32_bf16 v[204:207],a[244:247],v[28:31],v[204:207]
ds_read_u16_d16_hi v58,v58
v_fma_mix_f32 v57,v57,v9,0 op_sel_hi:[0,1,0]
v_mfma_f32_16x16x32_bf16 v[200:203],a[244:247],v[24:27],v[200:203]
ds_read_b64_tr_b16 a[242:243],v3 offset:1608
v_fma_mix_f32 v59,v59,v9,0 op_sel:[0,1,0] op_sel_hi:[0,1,0]
v_mfma_f32_16x16x32_bf16 v[28:31],a[192:195],a[48:51],v[16:19]
s_add_u32 m0,s57,8
ds_read_b64 v[14:15],v7 offset:0
v_lshlrev_b32_sdwa v40,1,v40 dst_sel:DWORD dst_unused:UNUSED_PAD src0_sel:DWORD src1_sel:WORD_1
v_mfma_f32_16x16x32_bf16 v[28:31],a[196:199],a[64:67],v[28:31]
s_add_u32 s89,s56,4915200
ds_read_b64_tr_b16 a[224:225],v3 offset:128
v_fma_mixlo_f16 v8,v45,s99,0
v_mfma_f32_16x16x32_bf16 v[28:31],a[200:203],a[80:83],v[28:31]
s_waitcnt lgkmcnt(8)
buffer_load_dwordx4 v6,s[36:39],s89 offen lds
v_lshl_add_u64 v[32:33],v[32:33],0,v[34:35]
v_lshlrev_b32_sdwa v42,1,v42 dst_sel:DWORD dst_unused:UNUSED_PAD src0_sel:DWORD src1_sel:WORD_1
v_mfma_f32_16x16x32_bf16 v[24:27],a[204:207],a[80:83],v[20:23]
ds_read_b64_tr_b16 a[226:227],v3 offset:1664
v_lshl_add_u64 v[34:35],v[60:61],0,v[62:63]
v_fma_mixhi_f16 v8,v47,s99,0
v_mfma_f32_16x16x32_bf16 v[24:27],a[208:211],a[64:67],v[24:27]
s_waitcnt lgkmcnt(7)
ds_read_b64_tr_b16 a[232:233],v3 offset:136
TP3865_%=:
s_cbranch_execz TP6024_%=
v_lshl_add_u64 v[60:61],v[52:53],0,v[54:55]
v_lshlrev_b32_sdwa v41,1,v41 dst_sel:DWORD dst_unused:UNUSED_PAD src0_sel:DWORD src1_sel:WORD_1
v_pk_fma_f16 v9,v8,v8,s54
v_mfma_f32_16x16x32_bf16 v[24:27],a[212:215],a[48:51],v[24:27]
s_waitcnt lgkmcnt(3)
ds_read_b64_tr_b16 a[234:235],v3 offset:1672
v_or3_b32 v13,v14,v15,v13
v_lshlrev_b32_sdwa v43,1,v43 dst_sel:DWORD dst_unused:UNUSED_PAD src0_sel:DWORD src1_sel:WORD_1
v_mfma_f32_16x16x32_bf16 v[52:55],a[212:215],a[52:55],v[20:23]
ds_read_b64 v[14:15],v7 offset:512
v_or_b32 v62,v56,v58
v_pk_mul_f16 v9,v8,v9
v_mfma_f32_16x16x32_bf16 v[52:55],a[208:211],a[68:71],v[52:55]
ds_read_u16 v40,v40
v_cvt_pk_bf16_f32 v63,v57,v59
v_lshlrev_b32_sdwa v36,1,v36 dst_sel:DWORD dst_unused:UNUSED_PAD src0_sel:DWORD src1_sel:WORD_1
v_mfma_f32_16x16x32_bf16 v[52:55],a[204:207],a[84:87],v[52:55]
v_exp_f16 v8,v9
v_mfma_f32_16x16x32_bf16 v[56:59],a[200:203],a[84:87],v[16:19]
ds_read_u16_d16_hi v42,v42
v_lshlrev_b32_sdwa v38,1,v38 dst_sel:DWORD dst_unused:UNUSED_PAD src0_sel:DWORD src1_sel:WORD_1
v_mfma_f32_16x16x32_bf16 v[56:59],a[196:199],a[68:71],v[56:59]
ds_read_u16 v41,v41
ds_read_u16_d16_hi v43,v43
v_lshlrev_b32_sdwa v37,1,v37 dst_sel:DWORD dst_unused:UNUSED_PAD src0_sel:DWORD src1_sel:WORD_1
v_mfma_f32_16x16x32_bf16 v[56:59],a[192:195],a[52:55],v[56:59]
s_waitcnt lgkmcnt(4)
ds_read_u16 v36,v36
ds_read_u16_d16_hi v38,v38
v_or3_b32 v13,v14,v15,v13
v_mfma_f32_16x16x32_bf16 v[136:139],a[228:231],v[32:35],v[136:139]
ds_read_b64 v[14:15],v7 offset:1024
v_exp_f16_sdwa v8,v9 dst_sel:WORD_1 dst_unused:UNUSED_PRESERVE src0_sel:WORD_1
v_lshlrev_b32_sdwa v39,1,v39 dst_sel:DWORD dst_unused:UNUSED_PAD src0_sel:DWORD src1_sel:WORD_1
v_mfma_f32_16x16x32_bf16 v[140:143],a[228:231],v[60:63],v[140:143]
ds_read_u16 v37,v37
v_lshlrev_b32_sdwa v48,1,v48 dst_sel:DWORD dst_unused:UNUSED_PAD src0_sel:DWORD src1_sel:WORD_1
v_mfma_f32_16x16x32_bf16 v[68:71],a[248:251],v[60:63],v[68:71]
ds_read_u16_d16_hi v39,v39
v_pk_fma_f16 v8,v8,s55,s55
v_mfma_f32_16x16x32_bf16 v[64:67],a[248:251],v[32:35],v[64:67]
v_rcp_f16 v9,v8
v_lshlrev_b32_sdwa v50,1,v50 dst_sel:DWORD dst_unused:UNUSED_PAD src0_sel:DWORD src1_sel:WORD_1
v_mfma_f32_16x16x32_bf16 v[144:147],a[252:255],v[32:35],v[144:147]
ds_read_u16 v48,v48
v_lshlrev_b32_sdwa v49,1,v49 dst_sel:DWORD dst_unused:UNUSED_PAD src0_sel:DWORD src1_sel:WORD_1
v_mfma_f32_16x16x32_bf16 v[148:151],a[252:255],v[60:63],v[148:151]
s_waitcnt lgkmcnt(3)
ds_read_u16_d16_hi v50,v50
v_or3_b32 v13,v14,v15,v13
v_mfma_f32_16x16x32_bf16 v[84:87],a[240:243],v[60:63],v[84:87]
ds_read_u16 v49,v49
v_lshlrev_b32_sdwa v51,1,v51 dst_sel:DWORD dst_unused:UNUSED_PAD src0_sel:DWORD src1_sel:WORD_1
v_mfma_f32_16x16x32_bf16 v[80:83],a[240:243],v[32:35],v[80:83]
s_add_u32 m0,s57,3544
v_rcp_f16_sdwa v9,v8 dst_sel:WORD_1 dst_unused:UNUSED_PRESERVE src0_sel:WORD_1
v_mfma_f32_16x16x32_bf16 v[152:155],a[224:227],v[32:35],v[152:155]
s_add_u32 s89,s56,5013504
ds_read_u16_d16_hi v51,v51
v_lshlrev_b32_sdwa v44,1,v44 dst_sel:DWORD dst_unused:UNUSED_PAD src0_sel:DWORD src1_sel:WORD_1
v_mfma_f32_16x16x32_bf16 v[156:159],a[224:227],v[60:63],v[156:159]
buffer_load_dwordx4 v6,s[36:39],s89 offen lds
v_lshlrev_b32_sdwa v46,1,v46 dst_sel:DWORD dst_unused:UNUSED_PAD src0_sel:DWORD src1_sel:WORD_1
v_mfma_f32_16x16x32_bf16 v[100:103],a[232:235],v[60:63],v[100:103]
ds_read_u16 v44,v44
v_fma_mix_f32 v45,v45,v9,0 op_sel_hi:[0,1,0]
v_mfma_f32_16x16x32_bf16 v[96:99],a[232:235],v[32:35],v[96:99]
ds_read_u16_d16_hi v46,v46
v_fma_mix_f32 v47,v47,v9,0 op_sel:[0,1,0] op_sel_hi:[0,1,0]
v_mfma_f32_16x16x32_bf16 v[60:63],a[192:195],a[56:59],v[16:19]
ds_read_b64_tr_b16 a[216:217],v1 offset:0
v_lshlrev_b32_sdwa v28,1,v28 dst_sel:DWORD dst_unused:UNUSED_PAD src0_sel:DWORD src1_sel:WORD_1
v_mfma_f32_16x16x32_bf16 v[60:63],a[196:199],a[72:75],v[60:63]
ds_read_b64_tr_b16 a[218:219],v1 offset:1024
v_fma_mixlo_f16 v8,v53,s99,0
v_mfma_f32_16x16x32_bf16 v[60:63],a[200:203],a[88:91],v[60:63]
ds_read_b64_tr_b16 a[220:221],v1 offset:2048
v_lshl_add_u64 v[40:41],v[40:41],0,v[42:43]
v_lshlrev_b32_sdwa v30,1,v30 dst_sel:DWORD dst_unused:UNUSED_PAD src0_sel:DWORD src1_sel:WORD_1
v_mfma_f32_16x16x32_bf16 v[32:35],a[204:207],a[88:91],v[20:23]
s_waitcnt lgkmcnt(6)
ds_read_b64_tr_b16 a[222:223],v1 offset:3072
v_lshl_add_u64 v[42:43],v[36:37],0,v[38:39]
v_fma_mixhi_f16 v8,v55,s99,0
v_mfma_f32_16x16x32_bf16 v[32:35],a[208:211],a[72:75],v[32:35]
s_waitcnt lgkmcnt(6)
ds_read_u16 v28,v28
v_lshl_add_u64 v[36:37],v[48:49],0,v[50:51]
v_lshlrev_b32_sdwa v29,1,v29 dst_sel:DWORD dst_unused:UNUSED_PAD src0_sel:DWORD src1_sel:WORD_1
v_pk_fma_f16 v9,v8,v8,s54
v_mfma_f32_16x16x32_bf16 v[32:35],a[212:215],a[56:59],v[32:35]
ds_read_u16_d16_hi v30,v30
v_lshlrev_b32_sdwa v31,1,v31 dst_sel:DWORD dst_unused:UNUSED_PAD src0_sel:DWORD src1_sel:WORD_1
v_mfma_f32_16x16x32_bf16 v[48:51],a[212:215],a[60:63],v[20:23]
s_waitcnt lgkmcnt(6)
ds_read_b128 v[20:23],v4 offset:1152
v_or_b32 v38,v44,v46
v_pk_mul_f16 v9,v8,v9
v_mfma_f32_16x16x32_bf16 v[48:51],a[208:211],a[76:79],v[48:51]
ds_read_u16 v29,v29
v_cvt_pk_bf16_f32 v39,v45,v47
v_lshlrev_b32_sdwa v24,1,v24 dst_sel:DWORD dst_unused:UNUSED_PAD src0_sel:DWORD src1_sel:WORD_1
v_mfma_f32_16x16x32_bf16 v[48:51],a[204:207],a[92:95],v[48:51]
ds_read_u16_d16_hi v31,v31
v_exp_f16 v8,v9
v_mfma_f32_16x16x32_bf16 v[44:47],a[200:203],a[92:95],v[16:19]
ds_read_b128 v[16:19],v4 offset:1184
ds_read_b64_tr_b16 a[212:213],v1 offset:4096
v_lshlrev_b32_sdwa v26,1,v26 dst_sel:DWORD dst_unused:UNUSED_PAD src0_sel:DWORD src1_sel:WORD_1
v_mfma_f32_16x16x32_bf16 v[44:47],a[196:199],a[76:79],v[44:47]
ds_read_b64_tr_b16 a[214:215],v1 offset:5120
ds_read_b64_tr_b16 a[208:209],v2 offset:4096
v_lshlrev_b32_sdwa v25,1,v25 dst_sel:DWORD dst_unused:UNUSED_PAD src0_sel:DWORD src1_sel:WORD_1
v_mfma_f32_16x16x32_bf16 v[44:47],a[192:195],a[60:63],v[44:47]
ds_read_b64_tr_b16 a[210:211],v2 offset:5120
v_exp_f16_sdwa v8,v9 dst_sel:WORD_1 dst_unused:UNUSED_PRESERVE src0_sel:WORD_1
v_mfma_f32_16x16x32_bf16 v[112:115],a[228:231],v[40:43],v[112:115]
ds_read_u16 v24,v24
ds_read_u16_d16_hi v26,v26
v_lshlrev_b32_sdwa v27,1,v27 dst_sel:DWORD dst_unused:UNUSED_PAD src0_sel:DWORD src1_sel:WORD_1
v_mfma_f32_16x16x32_bf16 v[116:119],a[228:231],v[36:39],v[116:119]
ds_read_u16 v25,v25
v_lshlrev_b32_sdwa v56,1,v56 dst_sel:DWORD dst_unused:UNUSED_PAD src0_sel:DWORD src1_sel:WORD_1
v_mfma_f32_16x16x32_bf16 v[76:79],a[248:251],v[36:39],v[76:79]
ds_read_u16_d16_hi v27,v27
v_pk_fma_f16 v8,v8,s55,s55
v_mfma_f32_16x16x32_bf16 v[72:75],a[248:251],v[40:43],v[72:75]
v_rcp_f16 v9,v8
v_lshlrev_b32_sdwa v58,1,v58 dst_sel:DWORD dst_unused:UNUSED_PAD src0_sel:DWORD src1_sel:WORD_1
v_mfma_f32_16x16x32_bf16 v[120:123],a[252:255],v[40:43],v[120:123]
ds_read_b64_tr_b16 a[204:205],v2 offset:2048
v_lshlrev_b32_sdwa v57,1,v57 dst_sel:DWORD dst_unused:UNUSED_PAD src0_sel:DWORD src1_sel:WORD_1
v_mfma_f32_16x16x32_bf16 v[124:127],a[252:255],v[36:39],v[124:127]
ds_read_b64_tr_b16 a[206:207],v2 offset:3072
v_lshlrev_b32_sdwa v59,1,v59 dst_sel:DWORD dst_unused:UNUSED_PAD src0_sel:DWORD src1_sel:WORD_1
v_mfma_f32_16x16x32_bf16 v[92:95],a[240:243],v[36:39],v[92:95]
ds_read_u16 v56,v56
v_lshlrev_b32_sdwa v52,1,v52 dst_sel:DWORD dst_unused:UNUSED_PAD src0_sel:DWORD src1_sel:WORD_1
v_mfma_f32_16x16x32_bf16 v[88:91],a[240:243],v[40:43],v[88:91]
ds_read_u16_d16_hi v58,v58
v_rcp_f16_sdwa v9,v8 dst_sel:WORD_1 dst_unused:UNUSED_PRESERVE src0_sel:WORD_1
v_mfma_f32_16x16x32_bf16 v[128:131],a[224:227],v[40:43],v[128:131]
ds_read_u16 v57,v57
ds_read_u16_d16_hi v59,v59
v_mfma_f32_16x16x32_bf16 v[132:135],a[224:227],v[36:39],v[132:135]
ds_read_b64_tr_b16 a[200:201],v2 offset:0
ds_read_b64_tr_b16 a[202:203],v2 offset:1024
v_lshlrev_b32_sdwa v54,1,v54 dst_sel:DWORD dst_unused:UNUSED_PAD src0_sel:DWORD src1_sel:WORD_1
v_mfma_f32_16x16x32_bf16 v[108:111],a[232:235],v[36:39],v[108:111]
ds_read_u16 v52,v52
v_fma_mix_f32 v53,v53,v9,0 op_sel_hi:[0,1,0]
v_mfma_f32_16x16x32_bf16 v[104:107],a[232:235],v[40:43],v[104:107]
s_waitcnt lgkmcnt(9)
ds_read_u16_d16_hi v54,v54
v_fma_mix_f32 v55,v55,v9,0 op_sel:[0,1,0] op_sel_hi:[0,1,0]
v_mfma_f32_16x16x32_bf16 v[36:39],a[216:219],a[0:3],v[20:23]
v_lshlrev_b32_sdwa v60,1,v60 dst_sel:DWORD dst_unused:UNUSED_PAD src0_sel:DWORD src1_sel:WORD_1
v_fma_mixlo_f16 v8,v49,s99,0
v_mfma_f32_16x16x32_bf16 v[36:39],a[220:223],a[16:19],v[36:39]
s_add_u32 m0,s57,7072
ds_read_u16 v60,v60
v_lshlrev_b32_sdwa v62,1,v62 dst_sel:DWORD dst_unused:UNUSED_PAD src0_sel:DWORD src1_sel:WORD_1
v_mfma_f32_16x16x32_bf16 v[36:39],a[212:215],a[32:35],v[36:39]
s_add_u32 s89,s56,5111808
ds_read_u16_d16_hi v62,v62
v_lshl_add_u64 v[28:29],v[28:29],0,v[30:31]
v_mfma_f32_16x16x32_bf16 v[40:43],a[208:211],a[32:35],v[16:19]
s_waitcnt lgkmcnt(9)
buffer_load_dwordx4 v6,s[36:39],s89 offen lds
v_lshl_add_u64 v[30:31],v[24:25],0,v[26:27]
v_fma_mixhi_f16 v8,v51,s99,0
v_mfma_f32_16x16x32_bf16 v[40:43],a[204:207],a[16:19],v[40:43]
s_waitcnt lgkmcnt(4)
v_lshl_add_u64 v[24:25],v[56:57],0,v[58:59]
v_lshlrev_b32_sdwa v61,1,v61 dst_sel:DWORD dst_unused:UNUSED_PAD src0_sel:DWORD src1_sel:WORD_1
v_pk_fma_f16 v9,v8,v8,s54
v_mfma_f32_16x16x32_bf16 v[40:43],a[200:203],a[0:3],v[40:43]
ds_read_u16 v61,v61
v_lshlrev_b32_sdwa v63,1,v63 dst_sel:DWORD dst_unused:UNUSED_PAD src0_sel:DWORD src1_sel:WORD_1
v_mfma_f32_16x16x32_bf16 v[56:59],a[200:203],a[4:7],v[16:19]
s_waitcnt lgkmcnt(3)
ds_read_u16_d16_hi v63,v63
v_or_b32 v26,v52,v54
v_pk_mul_f16 v9,v8,v9
v_mfma_f32_16x16x32_bf16 v[56:59],a[204:207],a[20:23],v[56:59]
s_waitcnt vmcnt(15)
v_cvt_pk_bf16_f32 v27,v53,v55
v_lshlrev_b32_sdwa v32,1,v32 dst_sel:DWORD dst_unused:UNUSED_PAD src0_sel:DWORD src1_sel:WORD_1
v_mfma_f32_16x16x32_bf16 v[56:59],a[208:211],a[36:39],v[56:59]
s_barrier
v_exp_f16 v8,v9
v_mfma_f32_16x16x32_bf16 v[52:55],a[212:215],a[36:39],v[20:23]
ds_read_u16 v32,v32
v_lshlrev_b32_sdwa v34,1,v34 dst_sel:DWORD dst_unused:UNUSED_PAD src0_sel:DWORD src1_sel:WORD_1
v_mfma_f32_16x16x32_bf16 v[52:55],a[220:223],a[20:23],v[52:55]
s_add_u32 m0,s58,0
ds_read_u16_d16_hi v34,v34
v_lshlrev_b32_sdwa v33,1,v33 dst_sel:DWORD dst_unused:UNUSED_PAD src0_sel:DWORD src1_sel:WORD_1
v_mfma_f32_16x16x32_bf16 v[52:55],a[216:219],a[4:7],v[52:55]
s_mov_b32 s69,s87
v_exp_f16_sdwa v8,v9 dst_sel:WORD_1 dst_unused:UNUSED_PRESERVE src0_sel:WORD_1
v_mfma_f32_16x16x32_bf16 v[232:235],a[228:231],v[28:31],v[232:235]
buffer_load_dwordx4 v5,s[44:47],s69 offen offset:0 lds
v_lshlrev_b32_sdwa v35,1,v35 dst_sel:DWORD dst_unused:UNUSED_PAD src0_sel:DWORD src1_sel:WORD_1
v_mfma_f32_16x16x32_bf16 v[236:239],a[228:231],v[24:27],v[236:239]
ds_read_u16 v33,v33
v_lshlrev_b32_sdwa v44,1,v44 dst_sel:DWORD dst_unused:UNUSED_PAD src0_sel:DWORD src1_sel:WORD_1
v_mfma_f32_16x16x32_bf16 v[164:167],a[248:251],v[24:27],v[164:167]
ds_read_u16_d16_hi v35,v35
v_pk_fma_f16 v8,v8,s55,s55
v_mfma_f32_16x16x32_bf16 v[160:163],a[248:251],v[28:31],v[160:163]
v_rcp_f16 v9,v8
v_lshlrev_b32_sdwa v46,1,v46 dst_sel:DWORD dst_unused:UNUSED_PAD src0_sel:DWORD src1_sel:WORD_1
v_mfma_f32_16x16x32_bf16 v[240:243],a[252:255],v[28:31],v[240:243]
s_add_u32 s69,s69,s53
ds_read_u16 v44,v44
v_lshlrev_b32_sdwa v45,1,v45 dst_sel:DWORD dst_unused:UNUSED_PAD src0_sel:DWORD src1_sel:WORD_1
v_mfma_f32_16x16x32_bf16 v[244:247],a[252:255],v[24:27],v[244:247]
buffer_load_dwordx4 v5,s[44:47],s69 offen offset:1024 lds
v_lshlrev_b32_sdwa v47,1,v47 dst_sel:DWORD dst_unused:UNUSED_PAD src0_sel:DWORD src1_sel:WORD_1
v_mfma_f32_16x16x32_bf16 v[180:183],a[240:243],v[24:27],v[180:183]
ds_read_u16_d16_hi v46,v46
v_lshlrev_b32_sdwa v48,1,v48 dst_sel:DWORD dst_unused:UNUSED_PAD src0_sel:DWORD src1_sel:WORD_1
v_mfma_f32_16x16x32_bf16 v[176:179],a[240:243],v[28:31],v[176:179]
v_rcp_f16_sdwa v9,v8 dst_sel:WORD_1 dst_unused:UNUSED_PRESERVE src0_sel:WORD_1
v_mfma_f32_16x16x32_bf16 v[248:251],a[224:227],v[28:31],v[248:251]
ds_read_u16 v45,v45
ds_read_u16_d16_hi v47,v47
v_mfma_f32_16x16x32_bf16 v[252:255],a[224:227],v[24:27],v[252:255]
s_add_u32 s69,s69,s53
ds_read_u16 v48,v48
v_lshlrev_b32_sdwa v50,1,v50 dst_sel:DWORD dst_unused:UNUSED_PAD src0_sel:DWORD src1_sel:WORD_1
v_mfma_f32_16x16x32_bf16 v[196:199],a[232:235],v[24:27],v[196:199]
buffer_load_dwordx4 v5,s[44:47],s69 offen offset:2048 lds
v_fma_mix_f32 v49,v49,v9,0 op_sel_hi:[0,1,0]
v_mfma_f32_16x16x32_bf16 v[192:195],a[232:235],v[28:31],v[192:195]
ds_read_u16_d16_hi v50,v50
v_fma_mix_f32 v51,v51,v9,0 op_sel:[0,1,0] op_sel_hi:[0,1,0]
v_mfma_f32_16x16x32_bf16 v[24:27],a[216:219],a[8:11],v[20:23]
ds_read_b64_tr_b16 a[236:237],v3 offset:6160
v_lshlrev_b32_sdwa v36,1,v36 dst_sel:DWORD dst_unused:UNUSED_PAD src0_sel:DWORD src1_sel:WORD_1
v_mfma_f32_16x16x32_bf16 v[24:27],a[220:223],a[24:27],v[24:27]
ds_read_u16 v36,v36
v_fma_mixlo_f16 v8,v57,s99,0
v_mfma_f32_16x16x32_bf16 v[24:27],a[212:215],a[40:43],v[24:27]
s_waitcnt lgkmcnt(7)
ds_read_b64_tr_b16 a[238:239],v3 offset:7696
v_lshl_add_u64 v[60:61],v[60:61],0,v[62:63]
v_lshlrev_b32_sdwa v38,1,v38 dst_sel:DWORD dst_unused:UNUSED_PAD src0_sel:DWORD src1_sel:WORD_1
v_mfma_f32_16x16x32_bf16 v[28:31],a[208:211],a[40:43],v[16:19]
ds_read_u16_d16_hi v38,v38
v_lshl_add_u64 v[62:63],v[32:33],0,v[34:35]
v_fma_mixhi_f16 v8,v59,s99,0
v_mfma_f32_16x16x32_bf16 v[28:31],a[204:207],a[24:27],v[28:31]
s_waitcnt lgkmcnt(6)
ds_read_b64_tr_b16 a[244:245],v3 offset:6168
v_lshl_add_u64 v[32:33],v[44:45],0,v[46:47]
v_lshlrev_b32_sdwa v37,1,v37 dst_sel:DWORD dst_unused:UNUSED_PAD src0_sel:DWORD src1_sel:WORD_1
v_pk_fma_f16 v9,v8,v8,s54
v_mfma_f32_16x16x32_bf16 v[28:31],a[200:203],a[8:11],v[28:31]
ds_read_u16 v37,v37
v_lshlrev_b32_sdwa v39,1,v39 dst_sel:DWORD dst_unused:UNUSED_PAD src0_sel:DWORD src1_sel:WORD_1
v_mfma_f32_16x16x32_bf16 v[44:47],a[200:203],a[12:15],v[16:19]
s_waitcnt lgkmcnt(6)
ds_read_u16_d16_hi v39,v39
v_or_b32 v34,v48,v50
v_pk_mul_f16 v9,v8,v9
v_mfma_f32_16x16x32_bf16 v[44:47],a[204:207],a[28:31],v[44:47]
ds_read_b64_tr_b16 a[246:247],v3 offset:7704
v_cvt_pk_bf16_f32 v35,v49,v51
v_lshlrev_b32_sdwa v40,1,v40 dst_sel:DWORD dst_unused:UNUSED_PAD src0_sel:DWORD src1_sel:WORD_1
v_mfma_f32_16x16x32_bf16 v[44:47],a[208:211],a[44:47],v[44:47]
v_exp_f16 v8,v9
v_mfma_f32_16x16x32_bf16 v[48:51],a[212:215],a[44:47],v[20:23]
ds_read_u16 v40,v40
v_lshlrev_b32_sdwa v42,1,v42 dst_sel:DWORD dst_unused:UNUSED_PAD src0_sel:DWORD src1_sel:WORD_1
v_mfma_f32_16x16x32_bf16 v[48:51],a[220:223],a[28:31],v[48:51]
ds_read_u16_d16_hi v42,v42
v_lshlrev_b32_sdwa v41,1,v41 dst_sel:DWORD dst_unused:UNUSED_PAD src0_sel:DWORD src1_sel:WORD_1
v_mfma_f32_16x16x32_bf16 v[48:51],a[216:219],a[12:15],v[48:51]
v_exp_f16_sdwa v8,v9 dst_sel:WORD_1 dst_unused:UNUSED_PRESERVE src0_sel:WORD_1
v_mfma_f32_16x16x32_bf16 v[208:211],a[228:231],v[60:63],v[208:211]
ds_read_u16 v41,v41
v_lshlrev_b32_sdwa v43,1,v43 dst_sel:DWORD dst_unused:UNUSED_PAD src0_sel:DWORD src1_sel:WORD_1
v_mfma_f32_16x16x32_bf16 v[212:215],a[228:231],v[32:35],v[212:215]
ds_read_u16_d16_hi v43,v43
v_lshlrev_b32_sdwa v52,1,v52 dst_sel:DWORD dst_unused:UNUSED_PAD src0_sel:DWORD src1_sel:WORD_1
v_mfma_f32_16x16x32_bf16 v[172:175],a[248:251],v[32:35],v[172:175]
ds_read_u16 v52,v52
v_pk_fma_f16 v8,v8,s55,s55
v_mfma_f32_16x16x32_bf16 v[168:171],a[248:251],v[60:63],v[168:171]
v_rcp_f16 v9,v8
v_lshlrev_b32_sdwa v54,1,v54 dst_sel:DWORD dst_unused:UNUSED_PAD src0_sel:DWORD src1_sel:WORD_1
v_mfma_f32_16x16x32_bf16 v[216:219],a[252:255],v[60:63],v[216:219]
ds_read_u16_d16_hi v54,v54
v_lshlrev_b32_sdwa v53,1,v53 dst_sel:DWORD dst_unused:UNUSED_PAD src0_sel:DWORD src1_sel:WORD_1
v_mfma_f32_16x16x32_bf16 v[220:223],a[252:255],v[32:35],v[220:223]
ds_read_u16 v53,v53
ds_read_b64_tr_b16 a[228:229],v3 offset:6224
v_mfma_f32_16x16x32_bf16 v[188:191],a[240:243],v[32:35],v[188:191]
ds_read_b64_tr_b16 a[230:231],v3 offset:7760
v_lshlrev_b32_sdwa v55,1,v55 dst_sel:DWORD dst_unused:UNUSED_PAD src0_sel:DWORD src1_sel:WORD_1
v_mfma_f32_16x16x32_bf16 v[184:187],a[240:243],v[60:63],v[184:187]
ds_read_u16_d16_hi v55,v55
v_rcp_f16_sdwa v9,v8 dst_sel:WORD_1 dst_unused:UNUSED_PRESERVE src0_sel:WORD_1
v_mfma_f32_16x16x32_bf16 v[224:227],a[224:227],v[60:63],v[224:227]
ds_read_b64_tr_b16 a[248:249],v3 offset:6232
v_lshlrev_b32_sdwa v56,1,v56 dst_sel:DWORD dst_unused:UNUSED_PAD src0_sel:DWORD src1_sel:WORD_1
v_mfma_f32_16x16x32_bf16 v[228:231],a[224:227],v[32:35],v[228:231]
ds_read_u16 v56,v56
v_lshlrev_b32_sdwa v58,1,v58 dst_sel:DWORD dst_unused:UNUSED_PAD src0_sel:DWORD src1_sel:WORD_1
v_mfma_f32_16x16x32_bf16 v[204:207],a[232:235],v[32:35],v[204:207]
ds_read_u16_d16_hi v58,v58
v_fma_mix_f32 v57,v57,v9,0 op_sel_hi:[0,1,0]
v_mfma_f32_16x16x32_bf16 v[200:203],a[232:235],v[60:63],v[200:203]
ds_read_b64_tr_b16 a[250:251],v3 offset:7768
v_fma_mix_f32 v59,v59,v9,0 op_sel:[0,1,0] op_sel_hi:[0,1,0]
v_mfma_f32_16x16x32_bf16 v[32:35],a[216:219],a[48:51],v[20:23]
s_add_u32 m0,s57,10608
ds_read_b64 v[14:15],v7 offset:6160
v_lshlrev_b32_sdwa v24,1,v24 dst_sel:DWORD dst_unused:UNUSED_PAD src0_sel:DWORD src1_sel:WORD_1
v_mfma_f32_16x16x32_bf16 v[32:35],a[220:223],a[64:67],v[32:35]
s_add_u32 s89,s56,5210112
ds_read_b64_tr_b16 a[252:253],v3 offset:6288
v_fma_mixlo_f16 v8,v45,s99,0
v_mfma_f32_16x16x32_bf16 v[32:35],a[212:215],a[80:83],v[32:35]
s_waitcnt lgkmcnt(8)
buffer_load_dwordx4 v6,s[36:39],s89 offen lds
v_lshl_add_u64 v[36:37],v[36:37],0,v[38:39]
v_lshlrev_b32_sdwa v26,1,v26 dst_sel:DWORD dst_unused:UNUSED_PAD src0_sel:DWORD src1_sel:WORD_1
v_mfma_f32_16x16x32_bf16 v[60:63],a[208:211],a[80:83],v[16:19]
ds_read_b64_tr_b16 a[254:255],v3 offset:7824
v_lshl_add_u64 v[38:39],v[40:41],0,v[42:43]
v_fma_mixhi_f16 v8,v47,s99,0
v_mfma_f32_16x16x32_bf16 v[60:63],a[204:207],a[64:67],v[60:63]
s_waitcnt lgkmcnt(7)
ds_read_b64_tr_b16 a[240:241],v3 offset:6296
v_lshl_add_u64 v[40:41],v[52:53],0,v[54:55]
v_lshlrev_b32_sdwa v25,1,v25 dst_sel:DWORD dst_unused:UNUSED_PAD src0_sel:DWORD src1_sel:WORD_1
v_pk_fma_f16 v9,v8,v8,s54
v_mfma_f32_16x16x32_bf16 v[60:63],a[200:203],a[48:51],v[60:63]
s_waitcnt lgkmcnt(3)
ds_read_b64_tr_b16 a[242:243],v3 offset:7832
v_or3_b32 v13,v14,v15,v13
v_lshlrev_b32_sdwa v27,1,v27 dst_sel:DWORD dst_unused:UNUSED_PAD src0_sel:DWORD src1_sel:WORD_1
v_mfma_f32_16x16x32_bf16 v[52:55],a[200:203],a[52:55],v[16:19]
ds_read_b64 v[14:15],v7 offset:6672
v_or_b32 v42,v56,v58
v_pk_mul_f16 v9,v8,v9
v_mfma_f32_16x16x32_bf16 v[52:55],a[204:207],a[68:71],v[52:55]
ds_read_u16 v24,v24
v_cvt_pk_bf16_f32 v43,v57,v59
v_lshlrev_b32_sdwa v28,1,v28 dst_sel:DWORD dst_unused:UNUSED_PAD src0_sel:DWORD src1_sel:WORD_1
v_mfma_f32_16x16x32_bf16 v[52:55],a[208:211],a[84:87],v[52:55]
v_exp_f16 v8,v9
v_mfma_f32_16x16x32_bf16 v[56:59],a[212:215],a[84:87],v[20:23]
ds_read_u16_d16_hi v26,v26
v_lshlrev_b32_sdwa v30,1,v30 dst_sel:DWORD dst_unused:UNUSED_PAD src0_sel:DWORD src1_sel:WORD_1
v_mfma_f32_16x16x32_bf16 v[56:59],a[220:223],a[68:71],v[56:59]
ds_read_u16 v25,v25
ds_read_u16_d16_hi v27,v27
v_lshlrev_b32_sdwa v29,1,v29 dst_sel:DWORD dst_unused:UNUSED_PAD src0_sel:DWORD src1_sel:WORD_1
v_mfma_f32_16x16x32_bf16 v[56:59],a[216:219],a[52:55],v[56:59]
s_waitcnt lgkmcnt(4)
ds_read_u16 v28,v28
ds_read_u16_d16_hi v30,v30
v_or3_b32 v13,v14,v15,v13
v_mfma_f32_16x16x32_bf16 v[136:139],a[236:239],v[36:39],v[136:139]
ds_read_b64 v[14:15],v7 offset:7184
v_exp_f16_sdwa v8,v9 dst_sel:WORD_1 dst_unused:UNUSED_PRESERVE src0_sel:WORD_1
v_lshlrev_b32_sdwa v31,1,v31 dst_sel:DWORD dst_unused:UNUSED_PAD src0_sel:DWORD src1_sel:WORD_1
v_mfma_f32_16x16x32_bf16 v[140:143],a[236:239],v[40:43],v[140:143]
ds_read_u16 v29,v29
v_lshlrev_b32_sdwa v48,1,v48 dst_sel:DWORD dst_unused:UNUSED_PAD src0_sel:DWORD src1_sel:WORD_1
v_mfma_f32_16x16x32_bf16 v[68:71],a[244:247],v[40:43],v[68:71]
s_add_u32 m0,s57,14152
ds_read_u16_d16_hi v31,v31
v_pk_fma_f16 v8,v8,s55,s55
v_mfma_f32_16x16x32_bf16 v[64:67],a[244:247],v[36:39],v[64:67]
s_add_u32 s89,s56,5308416
v_rcp_f16 v9,v8
v_lshlrev_b32_sdwa v50,1,v50 dst_sel:DWORD dst_unused:UNUSED_PAD src0_sel:DWORD src1_sel:WORD_1
v_mfma_f32_16x16x32_bf16 v[144:147],a[228:231],v[36:39],v[144:147]
buffer_load_dwordx4 v6,s[36:39],s89 offen lds
ds_read_u16 v48,v48
v_mfma_f32_16x16x32_bf16 v[148:151],a[228:231],v[40:43],v[148:151]
s_waitcnt lgkmcnt(3)
ds_read_u16_d16_hi v50,v50
v_or3_b32 v13,v14,v15,v13
v_lshlrev_b32_sdwa v49,1,v49 dst_sel:DWORD dst_unused:UNUSED_PAD src0_sel:DWORD src1_sel:WORD_1
v_mfma_f32_16x16x32_bf16 v[84:87],a[248:251],v[40:43],v[84:87]
ds_read_u16 v49,v49
v_lshlrev_b32_sdwa v51,1,v51 dst_sel:DWORD dst_unused:UNUSED_PAD src0_sel:DWORD src1_sel:WORD_1
v_mfma_f32_16x16x32_bf16 v[80:83],a[248:251],v[36:39],v[80:83]
v_rcp_f16_sdwa v9,v8 dst_sel:WORD_1 dst_unused:UNUSED_PRESERVE src0_sel:WORD_1
v_mfma_f32_16x16x32_bf16 v[152:155],a[252:255],v[36:39],v[152:155]
ds_read_u16_d16_hi v51,v51
v_lshlrev_b32_sdwa v44,1,v44 dst_sel:DWORD dst_unused:UNUSED_PAD src0_sel:DWORD src1_sel:WORD_1
v_mfma_f32_16x16x32_bf16 v[156:159],a[252:255],v[40:43],v[156:159]
ds_read_u16 v44,v44
v_lshlrev_b32_sdwa v46,1,v46 dst_sel:DWORD dst_unused:UNUSED_PAD src0_sel:DWORD src1_sel:WORD_1
v_mfma_f32_16x16x32_bf16 v[100:103],a[240:243],v[40:43],v[100:103]
ds_read_u16_d16_hi v46,v46
v_fma_mix_f32 v45,v45,v9,0 op_sel_hi:[0,1,0]
v_mfma_f32_16x16x32_bf16 v[96:99],a[240:243],v[36:39],v[96:99]
ds_read_b64_tr_b16 a[196:197],v1 offset:6160
v_fma_mix_f32 v47,v47,v9,0 op_sel:[0,1,0] op_sel_hi:[0,1,0]
v_mfma_f32_16x16x32_bf16 v[40:43],a[216:219],a[56:59],v[20:23]
ds_read_b64_tr_b16 a[198:199],v1 offset:7184
v_lshlrev_b32_sdwa v32,1,v32 dst_sel:DWORD dst_unused:UNUSED_PAD src0_sel:DWORD src1_sel:WORD_1
v_mfma_f32_16x16x32_bf16 v[40:43],a[220:223],a[72:75],v[40:43]
ds_read_b64_tr_b16 a[192:193],v1 offset:8208
v_fma_mixlo_f16 v8,v53,s99,0
v_mfma_f32_16x16x32_bf16 v[40:43],a[212:215],a[88:91],v[40:43]
ds_read_b64_tr_b16 a[194:195],v1 offset:9232
v_lshl_add_u64 v[24:25],v[24:25],0,v[26:27]
v_lshlrev_b32_sdwa v34,1,v34 dst_sel:DWORD dst_unused:UNUSED_PAD src0_sel:DWORD src1_sel:WORD_1
v_mfma_f32_16x16x32_bf16 v[36:39],a[208:211],a[88:91],v[16:19]
s_waitcnt lgkmcnt(7)
ds_read_u16 v32,v32
v_lshl_add_u64 v[26:27],v[28:29],0,v[30:31]
v_fma_mixhi_f16 v8,v55,s99,0
v_mfma_f32_16x16x32_bf16 v[36:39],a[204:207],a[72:75],v[36:39]
s_waitcnt lgkmcnt(7)
ds_read_u16_d16_hi v34,v34
v_lshl_add_u64 v[28:29],v[48:49],0,v[50:51]
v_lshlrev_b32_sdwa v33,1,v33 dst_sel:DWORD dst_unused:UNUSED_PAD src0_sel:DWORD src1_sel:WORD_1
v_pk_fma_f16 v9,v8,v8,s54
v_mfma_f32_16x16x32_bf16 v[36:39],a[200:203],a[56:59],v[36:39]
ds_read_u16 v33,v33
v_lshlrev_b32_sdwa v35,1,v35 dst_sel:DWORD dst_unused:UNUSED_PAD src0_sel:DWORD src1_sel:WORD_1
v_mfma_f32_16x16x32_bf16 v[48:51],a[200:203],a[60:63],v[16:19]
s_waitcnt lgkmcnt(7)
ds_read_b128 v[16:19],v4 offset:1280
v_or_b32 v30,v44,v46
v_pk_mul_f16 v9,v8,v9
v_mfma_f32_16x16x32_bf16 v[48:51],a[204:207],a[76:79],v[48:51]
ds_read_u16_d16_hi v35,v35
v_cvt_pk_bf16_f32 v31,v45,v47
v_lshlrev_b32_sdwa v60,1,v60 dst_sel:DWORD dst_unused:UNUSED_PAD src0_sel:DWORD src1_sel:WORD_1
v_mfma_f32_16x16x32_bf16 v[48:51],a[208:211],a[92:95],v[48:51]
ds_read_b64_tr_b16 a[200:201],v1 offset:10256
v_exp_f16 v8,v9
v_mfma_f32_16x16x32_bf16 v[44:47],a[212:215],a[92:95],v[20:23]
ds_read_b128 v[20:23],v4 offset:1312
ds_read_b64_tr_b16 a[202:203],v1 offset:11280
v_lshlrev_b32_sdwa v62,1,v62 dst_sel:DWORD dst_unused:UNUSED_PAD src0_sel:DWORD src1_sel:WORD_1
v_mfma_f32_16x16x32_bf16 v[44:47],a[220:223],a[76:79],v[44:47]
ds_read_b64_tr_b16 a[204:205],v2 offset:10256
ds_read_b64_tr_b16 a[206:207],v2 offset:11280
v_lshlrev_b32_sdwa v61,1,v61 dst_sel:DWORD dst_unused:UNUSED_PAD src0_sel:DWORD src1_sel:WORD_1
v_mfma_f32_16x16x32_bf16 v[44:47],a[216:219],a[60:63],v[44:47]
ds_read_u16 v60,v60
v_exp_f16_sdwa v8,v9 dst_sel:WORD_1 dst_unused:UNUSED_PRESERVE src0_sel:WORD_1
v_mfma_f32_16x16x32_bf16 v[112:115],a[236:239],v[24:27],v[112:115]
ds_read_u16_d16_hi v62,v62
v_lshlrev_b32_sdwa v63,1,v63 dst_sel:DWORD dst_unused:UNUSED_PAD src0_sel:DWORD src1_sel:WORD_1
v_mfma_f32_16x16x32_bf16 v[116:119],a[236:239],v[28:31],v[116:119]
ds_read_u16 v61,v61
v_lshlrev_b32_sdwa v56,1,v56 dst_sel:DWORD dst_unused:UNUSED_PAD src0_sel:DWORD src1_sel:WORD_1
v_mfma_f32_16x16x32_bf16 v[76:79],a[244:247],v[28:31],v[76:79]
ds_read_u16_d16_hi v63,v63
v_pk_fma_f16 v8,v8,s55,s55
v_mfma_f32_16x16x32_bf16 v[72:75],a[244:247],v[24:27],v[72:75]
v_rcp_f16 v9,v8
v_lshlrev_b32_sdwa v58,1,v58 dst_sel:DWORD dst_unused:UNUSED_PAD src0_sel:DWORD src1_sel:WORD_1
v_mfma_f32_16x16x32_bf16 v[120:123],a[228:231],v[24:27],v[120:123]
ds_read_b64_tr_b16 a[208:209],v2 offset:8208
v_lshlrev_b32_sdwa v57,1,v57 dst_sel:DWORD dst_unused:UNUSED_PAD src0_sel:DWORD src1_sel:WORD_1
v_mfma_f32_16x16x32_bf16 v[124:127],a[228:231],v[28:31],v[124:127]
ds_read_b64_tr_b16 a[210:211],v2 offset:9232
v_lshlrev_b32_sdwa v59,1,v59 dst_sel:DWORD dst_unused:UNUSED_PAD src0_sel:DWORD src1_sel:WORD_1
v_mfma_f32_16x16x32_bf16 v[92:95],a[248:251],v[28:31],v[92:95]
ds_read_u16 v56,v56
v_lshlrev_b32_sdwa v52,1,v52 dst_sel:DWORD dst_unused:UNUSED_PAD src0_sel:DWORD src1_sel:WORD_1
v_mfma_f32_16x16x32_bf16 v[88:91],a[248:251],v[24:27],v[88:91]
ds_read_u16_d16_hi v58,v58
v_rcp_f16_sdwa v9,v8 dst_sel:WORD_1 dst_unused:UNUSED_PRESERVE src0_sel:WORD_1
v_mfma_f32_16x16x32_bf16 v[128:131],a[252:255],v[24:27],v[128:131]
ds_read_u16 v57,v57
ds_read_u16_d16_hi v59,v59
v_mfma_f32_16x16x32_bf16 v[132:135],a[252:255],v[28:31],v[132:135]
ds_read_b64_tr_b16 a[212:213],v2 offset:6160
ds_read_b64_tr_b16 a[214:215],v2 offset:7184
v_lshlrev_b32_sdwa v54,1,v54 dst_sel:DWORD dst_unused:UNUSED_PAD src0_sel:DWORD src1_sel:WORD_1
v_mfma_f32_16x16x32_bf16 v[108:111],a[240:243],v[28:31],v[108:111]
ds_read_u16 v52,v52
v_fma_mix_f32 v53,v53,v9,0 op_sel_hi:[0,1,0]
v_mfma_f32_16x16x32_bf16 v[104:107],a[240:243],v[24:27],v[104:107]
s_waitcnt lgkmcnt(9)
ds_read_u16_d16_hi v54,v54
v_fma_mix_f32 v55,v55,v9,0 op_sel:[0,1,0] op_sel_hi:[0,1,0]
v_mfma_f32_16x16x32_bf16 v[28:31],a[196:199],a[0:3],v[16:19]
s_add_u32 m0,s57,17688
v_lshlrev_b32_sdwa v40,1,v40 dst_sel:DWORD dst_unused:UNUSED_PAD src0_sel:DWORD src1_sel:WORD_1
v_fma_mixlo_f16 v8,v49,s99,0
v_mfma_f32_16x16x32_bf16 v[28:31],a[192:195],a[16:19],v[28:31]
s_add_u32 s89,s56,5406720
ds_read_u16 v40,v40
v_lshlrev_b32_sdwa v42,1,v42 dst_sel:DWORD dst_unused:UNUSED_PAD src0_sel:DWORD src1_sel:WORD_1
v_mfma_f32_16x16x32_bf16 v[28:31],a[200:203],a[32:35],v[28:31]
buffer_load_dwordx4 v6,s[36:39],s89 offen lds
v_lshl_add_u64 v[32:33],v[32:33],0,v[34:35]
v_mfma_f32_16x16x32_bf16 v[24:27],a[204:207],a[32:35],v[20:23]
s_waitcnt lgkmcnt(8)
ds_read_u16_d16_hi v42,v42
v_lshl_add_u64 v[34:35],v[60:61],0,v[62:63]
v_fma_mixhi_f16 v8,v51,s99,0
v_mfma_f32_16x16x32_bf16 v[24:27],a[208:211],a[16:19],v[24:27]
s_waitcnt lgkmcnt(4)
v_lshl_add_u64 v[60:61],v[56:57],0,v[58:59]
v_lshlrev_b32_sdwa v41,1,v41 dst_sel:DWORD dst_unused:UNUSED_PAD src0_sel:DWORD src1_sel:WORD_1
v_pk_fma_f16 v9,v8,v8,s54
v_mfma_f32_16x16x32_bf16 v[24:27],a[212:215],a[0:3],v[24:27]
ds_read_u16 v41,v41
v_lshlrev_b32_sdwa v43,1,v43 dst_sel:DWORD dst_unused:UNUSED_PAD src0_sel:DWORD src1_sel:WORD_1
v_mfma_f32_16x16x32_bf16 v[56:59],a[212:215],a[4:7],v[20:23]
s_waitcnt lgkmcnt(3)
ds_read_u16_d16_hi v43,v43
v_or_b32 v62,v52,v54
v_pk_mul_f16 v9,v8,v9
v_mfma_f32_16x16x32_bf16 v[56:59],a[208:211],a[20:23],v[56:59]
s_waitcnt vmcnt(15)
v_cvt_pk_bf16_f32 v63,v53,v55
v_lshlrev_b32_sdwa v36,1,v36 dst_sel:DWORD dst_unused:UNUSED_PAD src0_sel:DWORD src1_sel:WORD_1
v_mfma_f32_16x16x32_bf16 v[56:59],a[204:207],a[36:39],v[56:59]
s_barrier
v_exp_f16 v8,v9
v_mfma_f32_16x16x32_bf16 v[52:55],a[200:203],a[36:39],v[16:19]
ds_read_u16 v36,v36
v_lshlrev_b32_sdwa v38,1,v38 dst_sel:DWORD dst_unused:UNUSED_PAD src0_sel:DWORD src1_sel:WORD_1
v_mfma_f32_16x16x32_bf16 v[52:55],a[192:195],a[20:23],v[52:55]
s_add_u32 m0,s58,6160
ds_read_u16_d16_hi v38,v38
v_lshlrev_b32_sdwa v37,1,v37 dst_sel:DWORD dst_unused:UNUSED_PAD src0_sel:DWORD src1_sel:WORD_1
v_mfma_f32_16x16x32_bf16 v[52:55],a[196:199],a[4:7],v[52:55]
s_add_u32 s69,s69,s59
v_exp_f16_sdwa v8,v9 dst_sel:WORD_1 dst_unused:UNUSED_PRESERVE src0_sel:WORD_1
v_mfma_f32_16x16x32_bf16 v[232:235],a[236:239],v[32:35],v[232:235]
TP4404_%=:
s_cbranch_execz TP6560_%=
buffer_load_dwordx4 v5,s[44:47],s69 offen offset:0 lds
v_lshlrev_b32_sdwa v39,1,v39 dst_sel:DWORD dst_unused:UNUSED_PAD src0_sel:DWORD src1_sel:WORD_1
v_mfma_f32_16x16x32_bf16 v[236:239],a[236:239],v[60:63],v[236:239]
ds_read_u16 v37,v37
v_lshlrev_b32_sdwa v44,1,v44 dst_sel:DWORD dst_unused:UNUSED_PAD src0_sel:DWORD src1_sel:WORD_1
v_mfma_f32_16x16x32_bf16 v[164:167],a[244:247],v[60:63],v[164:167]
ds_read_u16_d16_hi v39,v39
v_pk_fma_f16 v8,v8,s55,s55
v_mfma_f32_16x16x32_bf16 v[160:163],a[244:247],v[32:35],v[160:163]
v_rcp_f16 v9,v8
v_lshlrev_b32_sdwa v46,1,v46 dst_sel:DWORD dst_unused:UNUSED_PAD src0_sel:DWORD src1_sel:WORD_1
v_mfma_f32_16x16x32_bf16 v[240:243],a[228:231],v[32:35],v[240:243]
s_add_u32 s69,s69,s53
ds_read_u16 v44,v44
v_lshlrev_b32_sdwa v45,1,v45 dst_sel:DWORD dst_unused:UNUSED_PAD src0_sel:DWORD src1_sel:WORD_1
v_mfma_f32_16x16x32_bf16 v[244:247],a[228:231],v[60:63],v[244:247]
buffer_load_dwordx4 v5,s[44:47],s69 offen offset:1024 lds
v_lshlrev_b32_sdwa v47,1,v47 dst_sel:DWORD dst_unused:UNUSED_PAD src0_sel:DWORD src1_sel:WORD_1
v_mfma_f32_16x16x32_bf16 v[180:183],a[248:251],v[60:63],v[180:183]
ds_read_u16_d16_hi v46,v46
v_lshlrev_b32_sdwa v48,1,v48 dst_sel:DWORD dst_unused:UNUSED_PAD src0_sel:DWORD src1_sel:WORD_1
v_mfma_f32_16x16x32_bf16 v[176:179],a[248:251],v[32:35],v[176:179]
v_rcp_f16_sdwa v9,v8 dst_sel:WORD_1 dst_unused:UNUSED_PRESERVE src0_sel:WORD_1
v_mfma_f32_16x16x32_bf16 v[248:251],a[252:255],v[32:35],v[248:251]
ds_read_u16 v45,v45
ds_read_u16_d16_hi v47,v47
v_mfma_f32_16x16x32_bf16 v[252:255],a[252:255],v[60:63],v[252:255]
s_add_u32 s69,s69,s53
ds_read_u16 v48,v48
v_lshlrev_b32_sdwa v50,1,v50 dst_sel:DWORD dst_unused:UNUSED_PAD src0_sel:DWORD src1_sel:WORD_1
v_mfma_f32_16x16x32_bf16 v[196:199],a[240:243],v[60:63],v[196:199]
buffer_load_dwordx4 v5,s[44:47],s69 offen offset:2048 lds
v_fma_mix_f32 v49,v49,v9,0 op_sel_hi:[0,1,0]
v_mfma_f32_16x16x32_bf16 v[192:195],a[240:243],v[32:35],v[192:195]
ds_read_u16_d16_hi v50,v50
v_fma_mix_f32 v51,v51,v9,0 op_sel:[0,1,0] op_sel_hi:[0,1,0]
v_mfma_f32_16x16x32_bf16 v[60:63],a[196:199],a[8:11],v[16:19]
ds_read_b64_tr_b16 a[224:225],v3 offset:12320
v_lshlrev_b32_sdwa v28,1,v28 dst_sel:DWORD dst_unused:UNUSED_PAD src0_sel:DWORD src1_sel:WORD_1
v_mfma_f32_16x16x32_bf16 v[60:63],a[192:195],a[24:27],v[60:63]
ds_read_u16 v28,v28
v_fma_mixlo_f16 v8,v57,s99,0
v_mfma_f32_16x16x32_bf16 v[60:63],a[200:203],a[40:43],v[60:63]
s_waitcnt lgkmcnt(7)
ds_read_b64_tr_b16 a[226:227],v3 offset:13856
v_lshl_add_u64 v[40:41],v[40:41],0,v[42:43]
v_lshlrev_b32_sdwa v30,1,v30 dst_sel:DWORD dst_unused:UNUSED_PAD src0_sel:DWORD src1_sel:WORD_1
v_mfma_f32_16x16x32_bf16 v[32:35],a[204:207],a[40:43],v[20:23]
ds_read_u16_d16_hi v30,v30
v_lshl_add_u64 v[42:43],v[36:37],0,v[38:39]
v_fma_mixhi_f16 v8,v59,s99,0
v_mfma_f32_16x16x32_bf16 v[32:35],a[208:211],a[24:27],v[32:35]
s_waitcnt lgkmcnt(6)
ds_read_b64_tr_b16 a[232:233],v3 offset:12328
v_lshl_add_u64 v[36:37],v[44:45],0,v[46:47]
v_lshlrev_b32_sdwa v29,1,v29 dst_sel:DWORD dst_unused:UNUSED_PAD src0_sel:DWORD src1_sel:WORD_1
v_pk_fma_f16 v9,v8,v8,s54
v_mfma_f32_16x16x32_bf16 v[32:35],a[212:215],a[8:11],v[32:35]
ds_read_u16 v29,v29
v_lshlrev_b32_sdwa v31,1,v31 dst_sel:DWORD dst_unused:UNUSED_PAD src0_sel:DWORD src1_sel:WORD_1
v_mfma_f32_16x16x32_bf16 v[44:47],a[212:215],a[12:15],v[20:23]
s_waitcnt lgkmcnt(6)
ds_read_u16_d16_hi v31,v31
v_or_b32 v38,v48,v50
v_pk_mul_f16 v9,v8,v9
v_mfma_f32_16x16x32_bf16 v[44:47],a[208:211],a[28:31],v[44:47]
ds_read_b64_tr_b16 a[234:235],v3 offset:13864
v_cvt_pk_bf16_f32 v39,v49,v51
v_lshlrev_b32_sdwa v24,1,v24 dst_sel:DWORD dst_unused:UNUSED_PAD src0_sel:DWORD src1_sel:WORD_1
v_mfma_f32_16x16x32_bf16 v[44:47],a[204:207],a[44:47],v[44:47]
v_exp_f16 v8,v9
v_mfma_f32_16x16x32_bf16 v[48:51],a[200:203],a[44:47],v[16:19]
ds_read_u16 v24,v24
v_lshlrev_b32_sdwa v26,1,v26 dst_sel:DWORD dst_unused:UNUSED_PAD src0_sel:DWORD src1_sel:WORD_1
v_mfma_f32_16x16x32_bf16 v[48:51],a[192:195],a[28:31],v[48:51]
ds_read_u16_d16_hi v26,v26
v_lshlrev_b32_sdwa v25,1,v25 dst_sel:DWORD dst_unused:UNUSED_PAD src0_sel:DWORD src1_sel:WORD_1
v_mfma_f32_16x16x32_bf16 v[48:51],a[196:199],a[12:15],v[48:51]
v_exp_f16_sdwa v8,v9 dst_sel:WORD_1 dst_unused:UNUSED_PRESERVE src0_sel:WORD_1
v_mfma_f32_16x16x32_bf16 v[208:211],a[236:239],v[40:43],v[208:211]
ds_read_u16 v25,v25
v_lshlrev_b32_sdwa v27,1,v27 dst_sel:DWORD dst_unused:UNUSED_PAD src0_sel:DWORD src1_sel:WORD_1
v_mfma_f32_16x16x32_bf16 v[212:215],a[236:239],v[36:39],v[212:215]
ds_read_u16_d16_hi v27,v27
v_lshlrev_b32_sdwa v52,1,v52 dst_sel:DWORD dst_unused:UNUSED_PAD src0_sel:DWORD src1_sel:WORD_1
v_mfma_f32_16x16x32_bf16 v[172:175],a[244:247],v[36:39],v[172:175]
ds_read_u16 v52,v52
v_pk_fma_f16 v8,v8,s55,s55
v_mfma_f32_16x16x32_bf16 v[168:171],a[244:247],v[40:43],v[168:171]
v_rcp_f16 v9,v8
v_lshlrev_b32_sdwa v54,1,v54 dst_sel:DWORD dst_unused:UNUSED_PAD src0_sel:DWORD src1_sel:WORD_1
v_mfma_f32_16x16x32_bf16 v[216:219],a[228:231],v[40:43],v[216:219]
ds_read_u16_d16_hi v54,v54
v_lshlrev_b32_sdwa v53,1,v53 dst_sel:DWORD dst_unused:UNUSED_PAD src0_sel:DWORD src1_sel:WORD_1
v_mfma_f32_16x16x32_bf16 v[220:223],a[228:231],v[36:39],v[220:223]
ds_read_u16 v53,v53
ds_read_b64_tr_b16 a[236:237],v3 offset:12384
v_mfma_f32_16x16x32_bf16 v[188:191],a[248:251],v[36:39],v[188:191]
ds_read_b64_tr_b16 a[238:239],v3 offset:13920
v_lshlrev_b32_sdwa v55,1,v55 dst_sel:DWORD dst_unused:UNUSED_PAD src0_sel:DWORD src1_sel:WORD_1
v_mfma_f32_16x16x32_bf16 v[184:187],a[248:251],v[40:43],v[184:187]
ds_read_u16_d16_hi v55,v55
v_rcp_f16_sdwa v9,v8 dst_sel:WORD_1 dst_unused:UNUSED_PRESERVE src0_sel:WORD_1
v_mfma_f32_16x16x32_bf16 v[224:227],a[252:255],v[40:43],v[224:227]
ds_read_b64_tr_b16 a[244:245],v3 offset:12392
v_lshlrev_b32_sdwa v56,1,v56 dst_sel:DWORD dst_unused:UNUSED_PAD src0_sel:DWORD src1_sel:WORD_1
v_mfma_f32_16x16x32_bf16 v[228:231],a[252:255],v[36:39],v[228:231]
ds_read_u16 v56,v56
v_lshlrev_b32_sdwa v58,1,v58 dst_sel:DWORD dst_unused:UNUSED_PAD src0_sel:DWORD src1_sel:WORD_1
v_mfma_f32_16x16x32_bf16 v[204:207],a[240:243],v[36:39],v[204:207]
ds_read_u16_d16_hi v58,v58
v_fma_mix_f32 v57,v57,v9,0 op_sel_hi:[0,1,0]
v_mfma_f32_16x16x32_bf16 v[200:203],a[240:243],v[40:43],v[200:203]
ds_read_b64_tr_b16 a[246:247],v3 offset:13928
v_fma_mix_f32 v59,v59,v9,0 op_sel:[0,1,0] op_sel_hi:[0,1,0]
v_mfma_f32_16x16x32_bf16 v[36:39],a[196:199],a[48:51],v[16:19]
s_add_u32 m0,s57,21216
ds_read_b64 v[14:15],v7 offset:12320
v_lshlrev_b32_sdwa v60,1,v60 dst_sel:DWORD dst_unused:UNUSED_PAD src0_sel:DWORD src1_sel:WORD_1
v_mfma_f32_16x16x32_bf16 v[36:39],a[192:195],a[64:67],v[36:39]
s_add_u32 s89,s56,5505024
ds_read_b64_tr_b16 a[228:229],v3 offset:12448
v_fma_mixlo_f16 v8,v45,s99,0
v_mfma_f32_16x16x32_bf16 v[36:39],a[200:203],a[80:83],v[36:39]
s_waitcnt lgkmcnt(8)
buffer_load_dwordx4 v6,s[36:39],s89 offen lds
v_lshl_add_u64 v[28:29],v[28:29],0,v[30:31]
v_lshlrev_b32_sdwa v62,1,v62 dst_sel:DWORD dst_unused:UNUSED_PAD src0_sel:DWORD src1_sel:WORD_1
v_mfma_f32_16x16x32_bf16 v[40:43],a[204:207],a[80:83],v[20:23]
ds_read_b64_tr_b16 a[230:231],v3 offset:13984
v_lshl_add_u64 v[30:31],v[24:25],0,v[26:27]
v_fma_mixhi_f16 v8,v47,s99,0
v_mfma_f32_16x16x32_bf16 v[40:43],a[208:211],a[64:67],v[40:43]
s_waitcnt lgkmcnt(7)
ds_read_b64_tr_b16 a[248:249],v3 offset:12456
v_lshl_add_u64 v[24:25],v[52:53],0,v[54:55]
v_lshlrev_b32_sdwa v61,1,v61 dst_sel:DWORD dst_unused:UNUSED_PAD src0_sel:DWORD src1_sel:WORD_1
v_pk_fma_f16 v9,v8,v8,s54
v_mfma_f32_16x16x32_bf16 v[40:43],a[212:215],a[48:51],v[40:43]
s_waitcnt lgkmcnt(3)
ds_read_b64_tr_b16 a[250:251],v3 offset:13992
v_or3_b32 v13,v14,v15,v13
v_lshlrev_b32_sdwa v63,1,v63 dst_sel:DWORD dst_unused:UNUSED_PAD src0_sel:DWORD src1_sel:WORD_1
v_mfma_f32_16x16x32_bf16 v[52:55],a[212:215],a[52:55],v[20:23]
ds_read_b64 v[14:15],v7 offset:12832
v_or_b32 v26,v56,v58
v_pk_mul_f16 v9,v8,v9
v_mfma_f32_16x16x32_bf16 v[52:55],a[208:211],a[68:71],v[52:55]
ds_read_u16 v60,v60
v_cvt_pk_bf16_f32 v27,v57,v59
v_lshlrev_b32_sdwa v32,1,v32 dst_sel:DWORD dst_unused:UNUSED_PAD src0_sel:DWORD src1_sel:WORD_1
v_mfma_f32_16x16x32_bf16 v[52:55],a[204:207],a[84:87],v[52:55]
v_exp_f16 v8,v9
v_mfma_f32_16x16x32_bf16 v[56:59],a[200:203],a[84:87],v[16:19]
ds_read_u16_d16_hi v62,v62
v_lshlrev_b32_sdwa v34,1,v34 dst_sel:DWORD dst_unused:UNUSED_PAD src0_sel:DWORD src1_sel:WORD_1
v_mfma_f32_16x16x32_bf16 v[56:59],a[192:195],a[68:71],v[56:59]
ds_read_u16 v61,v61
ds_read_u16_d16_hi v63,v63
v_lshlrev_b32_sdwa v33,1,v33 dst_sel:DWORD dst_unused:UNUSED_PAD src0_sel:DWORD src1_sel:WORD_1
v_mfma_f32_16x16x32_bf16 v[56:59],a[196:199],a[52:55],v[56:59]
s_waitcnt lgkmcnt(4)
ds_read_u16 v32,v32
ds_read_u16_d16_hi v34,v34
v_or3_b32 v13,v14,v15,v13
v_mfma_f32_16x16x32_bf16 v[136:139],a[224:227],v[28:31],v[136:139]
ds_read_b64 v[14:15],v7 offset:13344
v_exp_f16_sdwa v8,v9 dst_sel:WORD_1 dst_unused:UNUSED_PRESERVE src0_sel:WORD_1
v_lshlrev_b32_sdwa v35,1,v35 dst_sel:DWORD dst_unused:UNUSED_PAD src0_sel:DWORD src1_sel:WORD_1
v_mfma_f32_16x16x32_bf16 v[140:143],a[224:227],v[24:27],v[140:143]
ds_read_u16 v33,v33
v_lshlrev_b32_sdwa v48,1,v48 dst_sel:DWORD dst_unused:UNUSED_PAD src0_sel:DWORD src1_sel:WORD_1
v_mfma_f32_16x16x32_bf16 v[68:71],a[232:235],v[24:27],v[68:71]
ds_read_u16_d16_hi v35,v35
v_pk_fma_f16 v8,v8,s55,s55
v_mfma_f32_16x16x32_bf16 v[64:67],a[232:235],v[28:31],v[64:67]
v_rcp_f16 v9,v8
v_lshlrev_b32_sdwa v50,1,v50 dst_sel:DWORD dst_unused:UNUSED_PAD src0_sel:DWORD src1_sel:WORD_1
v_mfma_f32_16x16x32_bf16 v[144:147],a[236:239],v[28:31],v[144:147]
ds_read_u16 v48,v48
v_lshlrev_b32_sdwa v49,1,v49 dst_sel:DWORD dst_unused:UNUSED_PAD src0_sel:DWORD src1_sel:WORD_1
v_mfma_f32_16x16x32_bf16 v[148:151],a[236:239],v[24:27],v[148:151]
s_waitcnt lgkmcnt(3)
ds_read_u16_d16_hi v50,v50
v_or3_b32 v13,v14,v15,v13
v_mfma_f32_16x16x32_bf16 v[84:87],a[244:247],v[24:27],v[84:87]
ds_read_u16 v49,v49
v_and_b32 v13,0x40004000,v13
v_lshlrev_b32_sdwa v51,1,v51 dst_sel:DWORD dst_unused:UNUSED_PAD src0_sel:DWORD src1_sel:WORD_1
v_mfma_f32_16x16x32_bf16 v[80:83],a[244:247],v[28:31],v[80:83]
ds_read_u16_d16_hi v51,v51
v_cmp_ne_u32_e32 vcc,0,v13
s_nop 0
s_or_b64 s[90:91],s[90:91],vcc
v_mfma_f32_16x16x32_bf16 v[152:155],a[228:231],v[28:31],v[152:155]
ds_read_b64_tr_b16 a[220:221],v1 offset:12320
v_mbcnt_lo_u32_b32 v15,-1,0
v_lshlrev_b32_sdwa v44,1,v44 dst_sel:DWORD dst_unused:UNUSED_PAD src0_sel:DWORD src1_sel:WORD_1
v_mfma_f32_16x16x32_bf16 v[156:159],a[228:231],v[24:27],v[156:159]
ds_read_u16 v44,v44
v_mbcnt_hi_u32_b32 v15,-1,v15
v_lshlrev_b32_sdwa v46,1,v46 dst_sel:DWORD dst_unused:UNUSED_PAD src0_sel:DWORD src1_sel:WORD_1
v_mfma_f32_16x16x32_bf16 v[100:103],a[248:251],v[24:27],v[100:103]
ds_read_u16_d16_hi v46,v46
v_lshlrev_b32 v15,2,v15
v_rcp_f16_sdwa v9,v8 dst_sel:WORD_1 dst_unused:UNUSED_PRESERVE src0_sel:WORD_1
v_mfma_f32_16x16x32_bf16 v[96:99],a[248:251],v[28:31],v[96:99]
ds_read_b32 v7,v15 offset:14848
ds_read_b32 v13,v15 offset:15104
v_fma_mix_f32 v45,v45,v9,0 op_sel_hi:[0,1,0]
v_fma_mix_f32 v47,v47,v9,0 op_sel:[0,1,0] op_sel_hi:[0,1,0]
v_mfma_f32_16x16x32_bf16 v[24:27],a[196:199],a[56:59],v[16:19]
ds_read_b32 v14,v15 offset:14336
ds_read_b32 v15,v15 offset:14592
v_lshlrev_b32_sdwa v36,1,v36 dst_sel:DWORD dst_unused:UNUSED_PAD src0_sel:DWORD src1_sel:WORD_1
v_mfma_f32_16x16x32_bf16 v[24:27],a[192:195],a[72:75],v[24:27]
ds_read_b64_tr_b16 a[222:223],v1 offset:13344
v_fma_mixlo_f16 v8,v53,s99,0
v_mfma_f32_16x16x32_bf16 v[24:27],a[200:203],a[88:91],v[24:27]
ds_read_b64_tr_b16 a[216:217],v1 offset:14368
v_lshl_add_u64 v[60:61],v[60:61],0,v[62:63]
v_lshlrev_b32_sdwa v38,1,v38 dst_sel:DWORD dst_unused:UNUSED_PAD src0_sel:DWORD src1_sel:WORD_1
v_mfma_f32_16x16x32_bf16 v[28:31],a[204:207],a[88:91],v[20:23]
s_waitcnt lgkmcnt(10)
ds_read_b64_tr_b16 a[218:219],v1 offset:15392
v_lshl_add_u64 v[62:63],v[32:33],0,v[34:35]
v_fma_mixhi_f16 v8,v55,s99,0
v_mfma_f32_16x16x32_bf16 v[28:31],a[208:211],a[72:75],v[28:31]
s_waitcnt lgkmcnt(10)
ds_read_u16 v36,v36
v_lshl_add_u64 v[32:33],v[48:49],0,v[50:51]
v_lshlrev_b32_sdwa v37,1,v37 dst_sel:DWORD dst_unused:UNUSED_PAD src0_sel:DWORD src1_sel:WORD_1
v_pk_fma_f16 v9,v8,v8,s54
v_mfma_f32_16x16x32_bf16 v[28:31],a[212:215],a[56:59],v[28:31]
ds_read_u16_d16_hi v38,v38
v_lshlrev_b32_sdwa v39,1,v39 dst_sel:DWORD dst_unused:UNUSED_PAD src0_sel:DWORD src1_sel:WORD_1
v_mfma_f32_16x16x32_bf16 v[48:51],a[212:215],a[60:63],v[20:23]
s_waitcnt lgkmcnt(9)
ds_read_b128 v[20:23],v4 offset:1408
v_or_b32 v34,v44,v46
v_pk_mul_f16 v9,v8,v9
v_mfma_f32_16x16x32_bf16 v[48:51],a[208:211],a[76:79],v[48:51]
ds_read_u16 v37,v37
v_cvt_pk_bf16_f32 v35,v45,v47
v_lshlrev_b32_sdwa v40,1,v40 dst_sel:DWORD dst_unused:UNUSED_PAD src0_sel:DWORD src1_sel:WORD_1
v_mfma_f32_16x16x32_bf16 v[48:51],a[204:207],a[92:95],v[48:51]
ds_read_u16_d16_hi v39,v39
v_exp_f16 v8,v9
v_mfma_f32_16x16x32_bf16 v[44:47],a[200:203],a[92:95],v[16:19]
ds_read_b128 v[16:19],v4 offset:1440
ds_read_b64_tr_b16 a[212:213],v1 offset:16416
v_lshlrev_b32_sdwa v42,1,v42 dst_sel:DWORD dst_unused:UNUSED_PAD src0_sel:DWORD src1_sel:WORD_1
v_mfma_f32_16x16x32_bf16 v[44:47],a[192:195],a[76:79],v[44:47]
ds_read_b64_tr_b16 a[214:215],v1 offset:17440
ds_read_b64_tr_b16 a[208:209],v2 offset:16416
v_lshlrev_b32_sdwa v41,1,v41 dst_sel:DWORD dst_unused:UNUSED_PAD src0_sel:DWORD src1_sel:WORD_1
v_mfma_f32_16x16x32_bf16 v[44:47],a[196:199],a[60:63],v[44:47]
ds_read_b64_tr_b16 a[210:211],v2 offset:17440
v_exp_f16_sdwa v8,v9 dst_sel:WORD_1 dst_unused:UNUSED_PRESERVE src0_sel:WORD_1
v_mfma_f32_16x16x32_bf16 v[112:115],a[224:227],v[60:63],v[112:115]
ds_read_u16 v40,v40
ds_read_u16_d16_hi v42,v42
v_lshlrev_b32_sdwa v43,1,v43 dst_sel:DWORD dst_unused:UNUSED_PAD src0_sel:DWORD src1_sel:WORD_1
v_mfma_f32_16x16x32_bf16 v[116:119],a[224:227],v[32:35],v[116:119]
ds_read_u16 v41,v41
v_lshlrev_b32_sdwa v56,1,v56 dst_sel:DWORD dst_unused:UNUSED_PAD src0_sel:DWORD src1_sel:WORD_1
v_mfma_f32_16x16x32_bf16 v[76:79],a[232:235],v[32:35],v[76:79]
ds_read_u16_d16_hi v43,v43
v_pk_fma_f16 v8,v8,s55,s55
v_mfma_f32_16x16x32_bf16 v[72:75],a[232:235],v[60:63],v[72:75]
v_rcp_f16 v9,v8
v_lshlrev_b32_sdwa v58,1,v58 dst_sel:DWORD dst_unused:UNUSED_PAD src0_sel:DWORD src1_sel:WORD_1
v_mfma_f32_16x16x32_bf16 v[120:123],a[236:239],v[60:63],v[120:123]
ds_read_b64_tr_b16 a[204:205],v2 offset:14368
v_lshlrev_b32_sdwa v57,1,v57 dst_sel:DWORD dst_unused:UNUSED_PAD src0_sel:DWORD src1_sel:WORD_1
v_mfma_f32_16x16x32_bf16 v[124:127],a[236:239],v[32:35],v[124:127]
ds_read_b64_tr_b16 a[206:207],v2 offset:15392
v_lshlrev_b32_sdwa v59,1,v59 dst_sel:DWORD dst_unused:UNUSED_PAD src0_sel:DWORD src1_sel:WORD_1
v_mfma_f32_16x16x32_bf16 v[92:95],a[244:247],v[32:35],v[92:95]
ds_read_u16 v56,v56
v_lshlrev_b32_sdwa v52,1,v52 dst_sel:DWORD dst_unused:UNUSED_PAD src0_sel:DWORD src1_sel:WORD_1
v_mfma_f32_16x16x32_bf16 v[88:91],a[244:247],v[60:63],v[88:91]
ds_read_u16_d16_hi v58,v58
v_rcp_f16_sdwa v9,v8 dst_sel:WORD_1 dst_unused:UNUSED_PRESERVE src0_sel:WORD_1
v_mfma_f32_16x16x32_bf16 v[128:131],a[228:231],v[60:63],v[128:131]
ds_read_u16 v57,v57
ds_read_u16_d16_hi v59,v59
v_mfma_f32_16x16x32_bf16 v[132:135],a[228:231],v[32:35],v[132:135]
ds_read_b64_tr_b16 a[200:201],v2 offset:12320
ds_read_b64_tr_b16 a[202:203],v2 offset:13344
v_lshlrev_b32_sdwa v54,1,v54 dst_sel:DWORD dst_unused:UNUSED_PAD src0_sel:DWORD src1_sel:WORD_1
v_mfma_f32_16x16x32_bf16 v[108:111],a[248:251],v[32:35],v[108:111]
ds_read_u16 v52,v52
v_fma_mix_f32 v53,v53,v9,0 op_sel_hi:[0,1,0]
v_mfma_f32_16x16x32_bf16 v[104:107],a[248:251],v[60:63],v[104:107]
s_waitcnt lgkmcnt(9)
ds_read_u16_d16_hi v54,v54
v_fma_mix_f32 v55,v55,v9,0 op_sel:[0,1,0] op_sel_hi:[0,1,0]
v_mfma_f32_16x16x32_bf16 v[32:35],a[220:223],a[0:3],v[20:23]
v_lshlrev_b32_sdwa v24,1,v24 dst_sel:DWORD dst_unused:UNUSED_PAD src0_sel:DWORD src1_sel:WORD_1
v_fma_mixlo_f16 v8,v49,s99,0
v_mfma_f32_16x16x32_bf16 v[32:35],a[216:219],a[16:19],v[32:35]
ds_read_u16 v24,v24
v_lshlrev_b32_sdwa v26,1,v26 dst_sel:DWORD dst_unused:UNUSED_PAD src0_sel:DWORD src1_sel:WORD_1
v_mfma_f32_16x16x32_bf16 v[32:35],a[212:215],a[32:35],v[32:35]
ds_read_u16_d16_hi v26,v26
v_lshl_add_u64 v[36:37],v[36:37],0,v[38:39]
v_mfma_f32_16x16x32_bf16 v[60:63],a[208:211],a[32:35],v[16:19]
s_waitcnt lgkmcnt(9)
v_lshl_add_u64 v[38:39],v[40:41],0,v[42:43]
v_fma_mixhi_f16 v8,v51,s99,0
v_mfma_f32_16x16x32_bf16 v[60:63],a[204:207],a[16:19],v[60:63]
s_waitcnt lgkmcnt(4)
v_lshl_add_u64 v[40:41],v[56:57],0,v[58:59]
v_lshlrev_b32_sdwa v25,1,v25 dst_sel:DWORD dst_unused:UNUSED_PAD src0_sel:DWORD src1_sel:WORD_1
v_pk_fma_f16 v9,v8,v8,s54
v_mfma_f32_16x16x32_bf16 v[60:63],a[200:203],a[0:3],v[60:63]
ds_read_u16 v25,v25
v_lshlrev_b32_sdwa v27,1,v27 dst_sel:DWORD dst_unused:UNUSED_PAD src0_sel:DWORD src1_sel:WORD_1
v_mfma_f32_16x16x32_bf16 v[56:59],a[200:203],a[4:7],v[16:19]
s_waitcnt lgkmcnt(3)
ds_read_u16_d16_hi v27,v27
v_or_b32 v42,v52,v54
v_pk_mul_f16 v9,v8,v9
v_mfma_f32_16x16x32_bf16 v[56:59],a[204:207],a[20:23],v[56:59]
s_waitcnt vmcnt(13)
v_cvt_pk_bf16_f32 v43,v53,v55
v_lshlrev_b32_sdwa v28,1,v28 dst_sel:DWORD dst_unused:UNUSED_PAD src0_sel:DWORD src1_sel:WORD_1
v_mfma_f32_16x16x32_bf16 v[56:59],a[208:211],a[36:39],v[56:59]
s_barrier
v_exp_f16 v8,v9
v_mfma_f32_16x16x32_bf16 v[52:55],a[212:215],a[36:39],v[20:23]
ds_read_u16 v28,v28
v_lshlrev_b32_sdwa v30,1,v30 dst_sel:DWORD dst_unused:UNUSED_PAD src0_sel:DWORD src1_sel:WORD_1
v_mfma_f32_16x16x32_bf16 v[52:55],a[216:219],a[20:23],v[52:55]
s_add_u32 m0,s58,12320
ds_read_u16_d16_hi v30,v30
v_lshlrev_b32_sdwa v29,1,v29 dst_sel:DWORD dst_unused:UNUSED_PAD src0_sel:DWORD src1_sel:WORD_1
v_mfma_f32_16x16x32_bf16 v[52:55],a[220:223],a[4:7],v[52:55]
s_add_u32 s69,s69,s59
v_exp_f16_sdwa v8,v9 dst_sel:WORD_1 dst_unused:UNUSED_PRESERVE src0_sel:WORD_1
v_mfma_f32_16x16x32_bf16 v[232:235],a[224:227],v[36:39],v[232:235]
buffer_load_dwordx4 v5,s[44:47],s69 offen offset:0 lds
v_lshlrev_b32_sdwa v31,1,v31 dst_sel:DWORD dst_unused:UNUSED_PAD src0_sel:DWORD src1_sel:WORD_1
v_mfma_f32_16x16x32_bf16 v[236:239],a[224:227],v[40:43],v[236:239]
ds_read_u16 v29,v29
v_lshlrev_b32_sdwa v44,1,v44 dst_sel:DWORD dst_unused:UNUSED_PAD src0_sel:DWORD src1_sel:WORD_1
v_mfma_f32_16x16x32_bf16 v[164:167],a[232:235],v[40:43],v[164:167]
ds_read_u16_d16_hi v31,v31
v_pk_fma_f16 v8,v8,s55,s55
v_mfma_f32_16x16x32_bf16 v[160:163],a[232:235],v[36:39],v[160:163]
v_rcp_f16 v9,v8
v_lshlrev_b32_sdwa v46,1,v46 dst_sel:DWORD dst_unused:UNUSED_PAD src0_sel:DWORD src1_sel:WORD_1
v_mfma_f32_16x16x32_bf16 v[240:243],a[236:239],v[36:39],v[240:243]
s_add_u32 s69,s69,s53
ds_read_u16 v44,v44
v_lshlrev_b32_sdwa v45,1,v45 dst_sel:DWORD dst_unused:UNUSED_PAD src0_sel:DWORD src1_sel:WORD_1
v_mfma_f32_16x16x32_bf16 v[244:247],a[236:239],v[40:43],v[244:247]
buffer_load_dwordx4 v5,s[44:47],s69 offen offset:1024 lds
v_lshlrev_b32_sdwa v47,1,v47 dst_sel:DWORD dst_unused:UNUSED_PAD src0_sel:DWORD src1_sel:WORD_1
v_mfma_f32_16x16x32_bf16 v[180:183],a[244:247],v[40:43],v[180:183]
ds_read_u16_d16_hi v46,v46
v_lshlrev_b32_sdwa v48,1,v48 dst_sel:DWORD dst_unused:UNUSED_PAD src0_sel:DWORD src1_sel:WORD_1
v_mfma_f32_16x16x32_bf16 v[176:179],a[244:247],v[36:39],v[176:179]
v_rcp_f16_sdwa v9,v8 dst_sel:WORD_1 dst_unused:UNUSED_PRESERVE src0_sel:WORD_1
v_mfma_f32_16x16x32_bf16 v[248:251],a[228:231],v[36:39],v[248:251]
ds_read_u16 v45,v45
ds_read_u16_d16_hi v47,v47
v_mfma_f32_16x16x32_bf16 v[252:255],a[228:231],v[40:43],v[252:255]
s_add_u32 s69,s69,s53
ds_read_u16 v48,v48
v_lshlrev_b32_sdwa v50,1,v50 dst_sel:DWORD dst_unused:UNUSED_PAD src0_sel:DWORD src1_sel:WORD_1
v_mfma_f32_16x16x32_bf16 v[196:199],a[248:251],v[40:43],v[196:199]
buffer_load_dwordx4 v5,s[44:47],s69 offen offset:2048 lds
v_fma_mix_f32 v49,v49,v9,0 op_sel_hi:[0,1,0]
v_mfma_f32_16x16x32_bf16 v[192:195],a[248:251],v[36:39],v[192:195]
ds_read_u16_d16_hi v50,v50
v_fma_mix_f32 v51,v51,v9,0 op_sel:[0,1,0] op_sel_hi:[0,1,0]
v_mfma_f32_16x16x32_bf16 v[40:43],a[220:223],a[8:11],v[20:23]
ds_read_b64_tr_b16 a[252:253],v3 offset:18480
v_lshlrev_b32_sdwa v32,1,v32 dst_sel:DWORD dst_unused:UNUSED_PAD src0_sel:DWORD src1_sel:WORD_1
v_mfma_f32_16x16x32_bf16 v[40:43],a[216:219],a[24:27],v[40:43]
ds_read_u16 v32,v32
v_fma_mixlo_f16 v8,v57,s99,0
v_mfma_f32_16x16x32_bf16 v[40:43],a[212:215],a[40:43],v[40:43]
s_waitcnt lgkmcnt(7)
ds_read_b64_tr_b16 a[254:255],v3 offset:20016
v_lshl_add_u64 v[24:25],v[24:25],0,v[26:27]
v_lshlrev_b32_sdwa v34,1,v34 dst_sel:DWORD dst_unused:UNUSED_PAD src0_sel:DWORD src1_sel:WORD_1
v_mfma_f32_16x16x32_bf16 v[36:39],a[208:211],a[40:43],v[16:19]
ds_read_u16_d16_hi v34,v34
v_lshl_add_u64 v[26:27],v[28:29],0,v[30:31]
v_fma_mixhi_f16 v8,v59,s99,0
v_mfma_f32_16x16x32_bf16 v[36:39],a[204:207],a[24:27],v[36:39]
s_waitcnt lgkmcnt(6)
ds_read_b64_tr_b16 a[240:241],v3 offset:18488
v_lshl_add_u64 v[28:29],v[44:45],0,v[46:47]
v_lshlrev_b32_sdwa v33,1,v33 dst_sel:DWORD dst_unused:UNUSED_PAD src0_sel:DWORD src1_sel:WORD_1
v_pk_fma_f16 v9,v8,v8,s54
v_mfma_f32_16x16x32_bf16 v[36:39],a[200:203],a[8:11],v[36:39]
ds_read_u16 v33,v33
v_lshlrev_b32_sdwa v35,1,v35 dst_sel:DWORD dst_unused:UNUSED_PAD src0_sel:DWORD src1_sel:WORD_1
v_mfma_f32_16x16x32_bf16 v[44:47],a[200:203],a[12:15],v[16:19]
s_waitcnt lgkmcnt(6)
ds_read_u16_d16_hi v35,v35
v_or_b32 v30,v48,v50
v_pk_mul_f16 v9,v8,v9
v_mfma_f32_16x16x32_bf16 v[44:47],a[204:207],a[28:31],v[44:47]
ds_read_b64_tr_b16 a[242:243],v3 offset:20024
v_cvt_pk_bf16_f32 v31,v49,v51
v_lshlrev_b32_sdwa v60,1,v60 dst_sel:DWORD dst_unused:UNUSED_PAD src0_sel:DWORD src1_sel:WORD_1
v_mfma_f32_16x16x32_bf16 v[44:47],a[208:211],a[44:47],v[44:47]
v_exp_f16 v8,v9
v_mfma_f32_16x16x32_bf16 v[48:51],a[212:215],a[44:47],v[20:23]
ds_read_u16 v60,v60
v_lshlrev_b32_sdwa v62,1,v62 dst_sel:DWORD dst_unused:UNUSED_PAD src0_sel:DWORD src1_sel:WORD_1
v_mfma_f32_16x16x32_bf16 v[48:51],a[216:219],a[28:31],v[48:51]
ds_read_u16_d16_hi v62,v62
v_lshlrev_b32_sdwa v61,1,v61 dst_sel:DWORD dst_unused:UNUSED_PAD src0_sel:DWORD src1_sel:WORD_1
v_mfma_f32_16x16x32_bf16 v[48:51],a[220:223],a[12:15],v[48:51]
v_exp_f16_sdwa v8,v9 dst_sel:WORD_1 dst_unused:UNUSED_PRESERVE src0_sel:WORD_1
v_mfma_f32_16x16x32_bf16 v[208:211],a[224:227],v[24:27],v[208:211]
ds_read_u16 v61,v61
v_lshlrev_b32_sdwa v63,1,v63 dst_sel:DWORD dst_unused:UNUSED_PAD src0_sel:DWORD src1_sel:WORD_1
v_mfma_f32_16x16x32_bf16 v[212:215],a[224:227],v[28:31],v[212:215]
ds_read_u16_d16_hi v63,v63
v_lshlrev_b32_sdwa v52,1,v52 dst_sel:DWORD dst_unused:UNUSED_PAD src0_sel:DWORD src1_sel:WORD_1
v_mfma_f32_16x16x32_bf16 v[172:175],a[232:235],v[28:31],v[172:175]
ds_read_u16 v52,v52
v_pk_fma_f16 v8,v8,s55,s55
v_mfma_f32_16x16x32_bf16 v[168:171],a[232:235],v[24:27],v[168:171]
v_rcp_f16 v9,v8
v_lshlrev_b32_sdwa v54,1,v54 dst_sel:DWORD dst_unused:UNUSED_PAD src0_sel:DWORD src1_sel:WORD_1
v_mfma_f32_16x16x32_bf16 v[216:219],a[236:239],v[24:27],v[216:219]
ds_read_u16_d16_hi v54,v54
v_lshlrev_b32_sdwa v53,1,v53 dst_sel:DWORD dst_unused:UNUSED_PAD src0_sel:DWORD src1_sel:WORD_1
v_mfma_f32_16x16x32_bf16 v[220:223],a[236:239],v[28:31],v[220:223]
ds_read_u16 v53,v53
ds_read_b64_tr_b16 a[224:225],v3 offset:18544
v_mfma_f32_16x16x32_bf16 v[188:191],a[244:247],v[28:31],v[188:191]
ds_read_b64_tr_b16 a[226:227],v3 offset:20080
v_lshlrev_b32_sdwa v55,1,v55 dst_sel:DWORD dst_unused:UNUSED_PAD src0_sel:DWORD src1_sel:WORD_1
v_mfma_f32_16x16x32_bf16 v[184:187],a[244:247],v[24:27],v[184:187]
ds_read_u16_d16_hi v55,v55
v_rcp_f16_sdwa v9,v8 dst_sel:WORD_1 dst_unused:UNUSED_PRESERVE src0_sel:WORD_1
v_mfma_f32_16x16x32_bf16 v[224:227],a[228:231],v[24:27],v[224:227]
ds_read_b64_tr_b16 a[232:233],v3 offset:18552
v_lshlrev_b32_sdwa v56,1,v56 dst_sel:DWORD dst_unused:UNUSED_PAD src0_sel:DWORD src1_sel:WORD_1
v_mfma_f32_16x16x32_bf16 v[228:231],a[228:231],v[28:31],v[228:231]
ds_read_u16 v56,v56
v_lshlrev_b32_sdwa v58,1,v58 dst_sel:DWORD dst_unused:UNUSED_PAD src0_sel:DWORD src1_sel:WORD_1
v_mfma_f32_16x16x32_bf16 v[204:207],a[248:251],v[28:31],v[204:207]
ds_read_u16_d16_hi v58,v58
v_fma_mix_f32 v57,v57,v9,0 op_sel_hi:[0,1,0]
v_mfma_f32_16x16x32_bf16 v[200:203],a[248:251],v[24:27],v[200:203]
ds_read_b64_tr_b16 a[234:235],v3 offset:20088
v_fma_mix_f32 v59,v59,v9,0 op_sel:[0,1,0] op_sel_hi:[0,1,0]
v_mfma_f32_16x16x32_bf16 v[28:31],a[220:223],a[48:51],v[20:23]
ds_read_b64_tr_b16 a[236:237],v3 offset:18608
v_lshlrev_b32_sdwa v40,1,v40 dst_sel:DWORD dst_unused:UNUSED_PAD src0_sel:DWORD src1_sel:WORD_1
v_mfma_f32_16x16x32_bf16 v[28:31],a[216:219],a[64:67],v[28:31]
ds_read_b64_tr_b16 a[238:239],v3 offset:20144
v_fma_mixlo_f16 v8,v45,s99,0
v_mfma_f32_16x16x32_bf16 v[28:31],a[212:215],a[80:83],v[28:31]
s_waitcnt lgkmcnt(8)
ds_read_b64_tr_b16 a[244:245],v3 offset:18616
v_lshl_add_u64 v[32:33],v[32:33],0,v[34:35]
v_lshlrev_b32_sdwa v42,1,v42 dst_sel:DWORD dst_unused:UNUSED_PAD src0_sel:DWORD src1_sel:WORD_1
v_mfma_f32_16x16x32_bf16 v[24:27],a[208:211],a[80:83],v[16:19]
ds_read_b64_tr_b16 a[246:247],v3 offset:20152
v_lshl_add_u64 v[34:35],v[60:61],0,v[62:63]
v_fma_mixhi_f16 v8,v47,s99,0
v_mfma_f32_16x16x32_bf16 v[24:27],a[204:207],a[64:67],v[24:27]
s_waitcnt lgkmcnt(8)
ds_read_u16 v40,v40
v_lshl_add_u64 v[60:61],v[52:53],0,v[54:55]
v_lshlrev_b32_sdwa v41,1,v41 dst_sel:DWORD dst_unused:UNUSED_PAD src0_sel:DWORD src1_sel:WORD_1
v_pk_fma_f16 v9,v8,v8,s54
v_mfma_f32_16x16x32_bf16 v[24:27],a[200:203],a[48:51],v[24:27]
ds_read_u16_d16_hi v42,v42
v_lshlrev_b32_sdwa v43,1,v43 dst_sel:DWORD dst_unused:UNUSED_PAD src0_sel:DWORD src1_sel:WORD_1
v_mfma_f32_16x16x32_bf16 v[52:55],a[200:203],a[52:55],v[16:19]
s_waitcnt lgkmcnt(7)
ds_read_u16 v41,v41
v_or_b32 v62,v56,v58
v_pk_mul_f16 v9,v8,v9
v_mfma_f32_16x16x32_bf16 v[52:55],a[204:207],a[68:71],v[52:55]
ds_read_u16_d16_hi v43,v43
v_cvt_pk_bf16_f32 v63,v57,v59
v_lshlrev_b32_sdwa v36,1,v36 dst_sel:DWORD dst_unused:UNUSED_PAD src0_sel:DWORD src1_sel:WORD_1
v_mfma_f32_16x16x32_bf16 v[52:55],a[208:211],a[84:87],v[52:55]
v_exp_f16 v8,v9
v_mfma_f32_16x16x32_bf16 v[56:59],a[212:215],a[84:87],v[20:23]
ds_read_u16 v36,v36
v_lshlrev_b32_sdwa v38,1,v38 dst_sel:DWORD dst_unused:UNUSED_PAD src0_sel:DWORD src1_sel:WORD_1
v_mfma_f32_16x16x32_bf16 v[56:59],a[216:219],a[68:71],v[56:59]
ds_read_u16_d16_hi v38,v38
v_lshlrev_b32_sdwa v37,1,v37 dst_sel:DWORD dst_unused:UNUSED_PAD src0_sel:DWORD src1_sel:WORD_1
v_mfma_f32_16x16x32_bf16 v[56:59],a[220:223],a[52:55],v[56:59]
v_exp_f16_sdwa v8,v9 dst_sel:WORD_1 dst_unused:UNUSED_PRESERVE src0_sel:WORD_1
v_mfma_f32_16x16x32_bf16 v[136:139],a[252:255],v[32:35],v[136:139]
ds_read_u16 v37,v37
v_lshlrev_b32_sdwa v39,1,v39 dst_sel:DWORD dst_unused:UNUSED_PAD src0_sel:DWORD src1_sel:WORD_1
v_mfma_f32_16x16x32_bf16 v[140:143],a[252:255],v[60:63],v[140:143]
ds_read_u16_d16_hi v39,v39
v_lshlrev_b32_sdwa v48,1,v48 dst_sel:DWORD dst_unused:UNUSED_PAD src0_sel:DWORD src1_sel:WORD_1
v_mfma_f32_16x16x32_bf16 v[68:71],a[240:243],v[60:63],v[68:71]
ds_read_u16 v48,v48
v_pk_fma_f16 v8,v8,s55,s55
v_mfma_f32_16x16x32_bf16 v[64:67],a[240:243],v[32:35],v[64:67]
v_rcp_f16 v9,v8
v_lshlrev_b32_sdwa v50,1,v50 dst_sel:DWORD dst_unused:UNUSED_PAD src0_sel:DWORD src1_sel:WORD_1
v_mfma_f32_16x16x32_bf16 v[144:147],a[224:227],v[32:35],v[144:147]
ds_read_u16_d16_hi v50,v50
v_lshlrev_b32_sdwa v49,1,v49 dst_sel:DWORD dst_unused:UNUSED_PAD src0_sel:DWORD src1_sel:WORD_1
v_mfma_f32_16x16x32_bf16 v[148:151],a[224:227],v[60:63],v[148:151]
s_waitcnt lgkmcnt(6)
ds_read_u16 v49,v49
v_lshlrev_b32_sdwa v51,1,v51 dst_sel:DWORD dst_unused:UNUSED_PAD src0_sel:DWORD src1_sel:WORD_1
v_mfma_f32_16x16x32_bf16 v[84:87],a[232:235],v[60:63],v[84:87]
ds_read_u16_d16_hi v51,v51
v_lshlrev_b32_sdwa v44,1,v44 dst_sel:DWORD dst_unused:UNUSED_PAD src0_sel:DWORD src1_sel:WORD_1
v_mfma_f32_16x16x32_bf16 v[80:83],a[232:235],v[32:35],v[80:83]
v_rcp_f16_sdwa v9,v8 dst_sel:WORD_1 dst_unused:UNUSED_PRESERVE src0_sel:WORD_1
v_mfma_f32_16x16x32_bf16 v[152:155],a[236:239],v[32:35],v[152:155]
ds_read_u16 v44,v44
v_lshlrev_b32_sdwa v46,1,v46 dst_sel:DWORD dst_unused:UNUSED_PAD src0_sel:DWORD src1_sel:WORD_1
v_mfma_f32_16x16x32_bf16 v[156:159],a[236:239],v[60:63],v[156:159]
ds_read_u16_d16_hi v46,v46
v_fma_mix_f32 v45,v45,v9,0 op_sel_hi:[0,1,0]
v_mfma_f32_16x16x32_bf16 v[100:103],a[244:247],v[60:63],v[100:103]
ds_read_b64_tr_b16 a[192:193],v1 offset:18480
v_fma_mix_f32 v47,v47,v9,0 op_sel:[0,1,0] op_sel_hi:[0,1,0]
v_mfma_f32_16x16x32_bf16 v[96:99],a[244:247],v[32:35],v[96:99]
ds_read_b64_tr_b16 a[194:195],v1 offset:19504
v_mfma_f32_16x16x32_bf16 v[60:63],a[220:223],a[56:59],v[20:23]
ds_read_b64_tr_b16 a[196:197],v1 offset:20528
v_mul_f32_dpp v136,v14,v136 row_newbcast:0 row_mask:0xf bank_mask:0xf
s_add_u32 s8,s61,0
v_mul_f32_dpp v137,v14,v137 row_newbcast:1 row_mask:0xf bank_mask:0xf
v_mul_f32_dpp v138,v14,v138 row_newbcast:2 row_mask:0xf bank_mask:0xf
v_lshlrev_b32_sdwa v28,1,v28 dst_sel:DWORD dst_unused:UNUSED_PAD src0_sel:DWORD src1_sel:WORD_1
v_mfma_f32_16x16x32_bf16 v[60:63],a[216:219],a[72:75],v[60:63]
TP4947_%=:
s_cbranch_execz TP7101_%=
ds_read_b64_tr_b16 a[198:199],v1 offset:21552
v_mul_f32_dpp v139,v14,v139 row_newbcast:3 row_mask:0xf bank_mask:0xf
v_mul_f32_dpp v64,v14,v64 row_newbcast:4 row_mask:0xf bank_mask:0xf
v_fma_mixlo_f16 v8,v53,s99,0
v_mul_f32_dpp v65,v14,v65 row_newbcast:5 row_mask:0xf bank_mask:0xf
v_mfma_f32_16x16x32_bf16 v[60:63],a[212:215],a[88:91],v[60:63]
ds_read_u16 v28,v28
v_lshl_add_u64 v[40:41],v[40:41],0,v[42:43]
v_mul_f32_dpp v66,v14,v66 row_newbcast:6 row_mask:0xf bank_mask:0xf
v_mul_f32_dpp v67,v14,v67 row_newbcast:7 row_mask:0xf bank_mask:0xf
v_lshlrev_b32_sdwa v30,1,v30 dst_sel:DWORD dst_unused:UNUSED_PAD src0_sel:DWORD src1_sel:WORD_1
v_cvt_pk_bf16_f32 v136,v136,v137
v_mfma_f32_16x16x32_bf16 v[32:35],a[208:211],a[88:91],v[16:19]
s_waitcnt lgkmcnt(7)
ds_read_u16_d16_hi v30,v30
v_lshl_add_u64 v[42:43],v[36:37],0,v[38:39]
v_cvt_pk_bf16_f32 v137,v138,v139
v_cvt_pk_bf16_f32 v138,v64,v65
v_fma_mixhi_f16 v8,v55,s99,0
v_cvt_pk_bf16_f32 v139,v66,v67
v_mfma_f32_16x16x32_bf16 v[32:35],a[204:207],a[72:75],v[32:35]
buffer_store_dwordx4 v[136:139],v7,s[40:43],s8 offen offset:0 sc1
v_lshl_add_u64 v[36:37],v[48:49],0,v[50:51]
v_mul_f32_dpp v144,v14,v144 row_newbcast:8 row_mask:0xf bank_mask:0xf
v_mul_f32_dpp v145,v14,v145 row_newbcast:9 row_mask:0xf bank_mask:0xf
v_lshlrev_b32_sdwa v29,1,v29 dst_sel:DWORD dst_unused:UNUSED_PAD src0_sel:DWORD src1_sel:WORD_1
v_pk_fma_f16 v9,v8,v8,s54
v_mul_f32_dpp v146,v14,v146 row_newbcast:10 row_mask:0xf bank_mask:0xf
v_mfma_f32_16x16x32_bf16 v[32:35],a[200:203],a[56:59],v[32:35]
ds_read_u16 v29,v29
v_mul_f32_dpp v147,v14,v147 row_newbcast:11 row_mask:0xf bank_mask:0xf
v_mul_f32_dpp v80,v14,v80 row_newbcast:12 row_mask:0xf bank_mask:0xf
v_lshlrev_b32_sdwa v31,1,v31 dst_sel:DWORD dst_unused:UNUSED_PAD src0_sel:DWORD src1_sel:WORD_1
v_mul_f32_dpp v81,v14,v81 row_newbcast:13 row_mask:0xf bank_mask:0xf
v_mfma_f32_16x16x32_bf16 v[48:51],a[200:203],a[60:63],v[16:19]
s_waitcnt lgkmcnt(7)
ds_read_b128 v[16:19],v4 offset:0
v_or_b32 v38,v44,v46
v_mul_f32_dpp v82,v14,v82 row_newbcast:14 row_mask:0xf bank_mask:0xf
v_mul_f32_dpp v83,v14,v83 row_newbcast:15 row_mask:0xf bank_mask:0xf
v_pk_mul_f16 v9,v8,v9
v_cvt_pk_bf16_f32 v144,v144,v145
v_mfma_f32_16x16x32_bf16 v[48:51],a[204:207],a[76:79],v[48:51]
ds_read_u16_d16_hi v31,v31
v_cvt_pk_bf16_f32 v39,v45,v47
v_cvt_pk_bf16_f32 v145,v146,v147
v_cvt_pk_bf16_f32 v146,v80,v81
v_lshlrev_b32_sdwa v24,1,v24 dst_sel:DWORD dst_unused:UNUSED_PAD src0_sel:DWORD src1_sel:WORD_1
v_cvt_pk_bf16_f32 v147,v82,v83
v_mfma_f32_16x16x32_bf16 v[48:51],a[208:211],a[92:95],v[48:51]
ds_read_b64_tr_b16 a[200:201],v1 offset:22576
v_exp_f16 v8,v9
v_mul_f32_dpp v152,v15,v152 row_newbcast:0 row_mask:0xf bank_mask:0xf
v_mul_f32_dpp v153,v15,v153 row_newbcast:1 row_mask:0xf bank_mask:0xf
v_mul_f32_dpp v154,v15,v154 row_newbcast:2 row_mask:0xf bank_mask:0xf
v_mfma_f32_16x16x32_bf16 v[44:47],a[212:215],a[92:95],v[20:23]
ds_read_b128 v[20:23],v4 offset:32
ds_read_b64_tr_b16 a[202:203],v1 offset:23600
v_mul_f32_dpp v155,v15,v155 row_newbcast:3 row_mask:0xf bank_mask:0xf
v_mul_f32_dpp v96,v15,v96 row_newbcast:4 row_mask:0xf bank_mask:0xf
v_lshlrev_b32_sdwa v26,1,v26 dst_sel:DWORD dst_unused:UNUSED_PAD src0_sel:DWORD src1_sel:WORD_1
v_mul_f32_dpp v97,v15,v97 row_newbcast:5 row_mask:0xf bank_mask:0xf
v_mfma_f32_16x16x32_bf16 v[44:47],a[216:219],a[76:79],v[44:47]
ds_read_b64_tr_b16 a[204:205],v2 offset:22576
ds_read_b64_tr_b16 a[206:207],v2 offset:23600
v_mul_f32_dpp v98,v15,v98 row_newbcast:6 row_mask:0xf bank_mask:0xf
v_mul_f32_dpp v99,v15,v99 row_newbcast:7 row_mask:0xf bank_mask:0xf
v_lshlrev_b32_sdwa v25,1,v25 dst_sel:DWORD dst_unused:UNUSED_PAD src0_sel:DWORD src1_sel:WORD_1
v_cvt_pk_bf16_f32 v152,v152,v153
v_mfma_f32_16x16x32_bf16 v[44:47],a[220:223],a[60:63],v[44:47]
ds_read_u16 v24,v24
v_exp_f16_sdwa v8,v9 dst_sel:WORD_1 dst_unused:UNUSED_PRESERVE src0_sel:WORD_1
v_cvt_pk_bf16_f32 v153,v154,v155
v_cvt_pk_bf16_f32 v154,v96,v97
v_cvt_pk_bf16_f32 v155,v98,v99
v_mfma_f32_16x16x32_bf16 v[112:115],a[252:255],v[40:43],v[112:115]
ds_read_u16_d16_hi v26,v26
v_mul_f32_dpp v140,v14,v140 row_newbcast:0 row_mask:0xf bank_mask:0xf
v_mul_f32_dpp v141,v14,v141 row_newbcast:1 row_mask:0xf bank_mask:0xf
v_lshlrev_b32_sdwa v27,1,v27 dst_sel:DWORD dst_unused:UNUSED_PAD src0_sel:DWORD src1_sel:WORD_1
v_mul_f32_dpp v142,v14,v142 row_newbcast:2 row_mask:0xf bank_mask:0xf
v_mfma_f32_16x16x32_bf16 v[116:119],a[252:255],v[36:39],v[116:119]
ds_read_u16 v25,v25
v_mul_f32_dpp v143,v14,v143 row_newbcast:3 row_mask:0xf bank_mask:0xf
v_mul_f32_dpp v68,v14,v68 row_newbcast:4 row_mask:0xf bank_mask:0xf
v_lshlrev_b32_sdwa v56,1,v56 dst_sel:DWORD dst_unused:UNUSED_PAD src0_sel:DWORD src1_sel:WORD_1
v_mul_f32_dpp v69,v14,v69 row_newbcast:5 row_mask:0xf bank_mask:0xf
v_mfma_f32_16x16x32_bf16 v[76:79],a[240:243],v[36:39],v[76:79]
ds_read_u16_d16_hi v27,v27
v_mul_f32_dpp v70,v14,v70 row_newbcast:6 row_mask:0xf bank_mask:0xf
v_mul_f32_dpp v71,v14,v71 row_newbcast:7 row_mask:0xf bank_mask:0xf
v_pk_fma_f16 v8,v8,s55,s55
v_cvt_pk_bf16_f32 v140,v140,v141
v_mfma_f32_16x16x32_bf16 v[72:75],a[240:243],v[40:43],v[72:75]
v_rcp_f16 v9,v8
v_cvt_pk_bf16_f32 v141,v142,v143
v_cvt_pk_bf16_f32 v142,v68,v69
v_lshlrev_b32_sdwa v58,1,v58 dst_sel:DWORD dst_unused:UNUSED_PAD src0_sel:DWORD src1_sel:WORD_1
v_cvt_pk_bf16_f32 v143,v70,v71
v_mfma_f32_16x16x32_bf16 v[120:123],a[224:227],v[40:43],v[120:123]
buffer_store_dwordx4 v[144:147],v7,s[40:43],s8 offen offset:64 sc1
ds_read_b64_tr_b16 a[208:209],v2 offset:20528
v_mul_f32_dpp v148,v14,v148 row_newbcast:8 row_mask:0xf bank_mask:0xf
v_mul_f32_dpp v149,v14,v149 row_newbcast:9 row_mask:0xf bank_mask:0xf
v_mul_f32_dpp v150,v14,v150 row_newbcast:10 row_mask:0xf bank_mask:0xf
v_mfma_f32_16x16x32_bf16 v[124:127],a[224:227],v[36:39],v[124:127]
ds_read_b64_tr_b16 a[210:211],v2 offset:21552
v_mul_f32_dpp v151,v14,v151 row_newbcast:11 row_mask:0xf bank_mask:0xf
v_mul_f32_dpp v84,v14,v84 row_newbcast:12 row_mask:0xf bank_mask:0xf
v_lshlrev_b32_sdwa v57,1,v57 dst_sel:DWORD dst_unused:UNUSED_PAD src0_sel:DWORD src1_sel:WORD_1
v_mul_f32_dpp v85,v14,v85 row_newbcast:13 row_mask:0xf bank_mask:0xf
v_mfma_f32_16x16x32_bf16 v[92:95],a[232:235],v[36:39],v[92:95]
ds_read_u16 v56,v56
v_mul_f32_dpp v86,v14,v86 row_newbcast:14 row_mask:0xf bank_mask:0xf
v_mul_f32_dpp v87,v14,v87 row_newbcast:15 row_mask:0xf bank_mask:0xf
v_lshlrev_b32_sdwa v59,1,v59 dst_sel:DWORD dst_unused:UNUSED_PAD src0_sel:DWORD src1_sel:WORD_1
v_cvt_pk_bf16_f32 v148,v148,v149
v_mfma_f32_16x16x32_bf16 v[88:91],a[232:235],v[40:43],v[88:91]
v_rcp_f16_sdwa v9,v8 dst_sel:WORD_1 dst_unused:UNUSED_PRESERVE src0_sel:WORD_1
v_cvt_pk_bf16_f32 v149,v150,v151
v_cvt_pk_bf16_f32 v150,v84,v85
v_cvt_pk_bf16_f32 v151,v86,v87
v_mfma_f32_16x16x32_bf16 v[128:131],a[236:239],v[40:43],v[128:131]
ds_read_u16_d16_hi v58,v58
ds_read_u16 v57,v57
v_mul_f32_dpp v156,v15,v156 row_newbcast:0 row_mask:0xf bank_mask:0xf
v_mul_f32_dpp v157,v15,v157 row_newbcast:1 row_mask:0xf bank_mask:0xf
v_lshlrev_b32_sdwa v52,1,v52 dst_sel:DWORD dst_unused:UNUSED_PAD src0_sel:DWORD src1_sel:WORD_1
v_mul_f32_dpp v158,v15,v158 row_newbcast:2 row_mask:0xf bank_mask:0xf
v_mfma_f32_16x16x32_bf16 v[132:135],a[236:239],v[36:39],v[132:135]
ds_read_u16_d16_hi v59,v59
ds_read_b64_tr_b16 a[212:213],v2 offset:18480
v_mul_f32_dpp v159,v15,v159 row_newbcast:3 row_mask:0xf bank_mask:0xf
v_mul_f32_dpp v100,v15,v100 row_newbcast:4 row_mask:0xf bank_mask:0xf
v_mul_f32_dpp v101,v15,v101 row_newbcast:5 row_mask:0xf bank_mask:0xf
v_lshlrev_b32_sdwa v54,1,v54 dst_sel:DWORD dst_unused:UNUSED_PAD src0_sel:DWORD src1_sel:WORD_1
v_mfma_f32_16x16x32_bf16 v[108:111],a[244:247],v[36:39],v[108:111]
ds_read_b64_tr_b16 a[214:215],v2 offset:19504
v_mul_f32_dpp v102,v15,v102 row_newbcast:6 row_mask:0xf bank_mask:0xf
v_mul_f32_dpp v103,v15,v103 row_newbcast:7 row_mask:0xf bank_mask:0xf
v_fma_mix_f32 v53,v53,v9,0 op_sel_hi:[0,1,0]
v_cvt_pk_bf16_f32 v156,v156,v157
v_mfma_f32_16x16x32_bf16 v[104:107],a[244:247],v[40:43],v[104:107]
s_waitcnt lgkmcnt(8)
ds_read_u16 v52,v52
ds_read_u16_d16_hi v54,v54
v_cvt_pk_bf16_f32 v157,v158,v159
v_cvt_pk_bf16_f32 v158,v100,v101
v_cvt_pk_bf16_f32 v159,v102,v103
v_fma_mix_f32 v55,v55,v9,0 op_sel:[0,1,0] op_sel_hi:[0,1,0]
v_mfma_f32_16x16x32_bf16 v[36:39],a[192:195],a[48:51],v[16:19]
v_mul_f32_dpp v112,v14,v112 row_newbcast:0 row_mask:0xf bank_mask:0xf
s_add_u32 s9,s61,98304
v_accvgpr_write_b32 a16,0
v_accvgpr_write_b32 a17,0
v_lshlrev_b32_sdwa v60,1,v60 dst_sel:DWORD dst_unused:UNUSED_PAD src0_sel:DWORD src1_sel:WORD_1
v_mfma_f32_16x16x32_bf16 v[36:39],a[196:199],a[64:67],v[36:39]
ds_read_u16 v60,v60
v_accvgpr_write_b32 a18,0
v_accvgpr_write_b32 a19,0
v_fma_mixlo_f16 v8,v49,s99,0
v_mul_f32_dpp v113,v14,v113 row_newbcast:1 row_mask:0xf bank_mask:0xf
v_mfma_f32_16x16x32_bf16 v[36:39],a[200:203],a[80:83],v[36:39]
buffer_store_dwordx4 v[152:155],v7,s[40:43],s8 offen offset:128 sc1
v_lshl_add_u64 v[28:29],v[28:29],0,v[30:31]
v_mul_f32_dpp v114,v14,v114 row_newbcast:2 row_mask:0xf bank_mask:0xf
v_mul_f32_dpp v115,v14,v115 row_newbcast:3 row_mask:0xf bank_mask:0xf
v_lshlrev_b32_sdwa v62,1,v62 dst_sel:DWORD dst_unused:UNUSED_PAD src0_sel:DWORD src1_sel:WORD_1
v_mul_f32_dpp v72,v14,v72 row_newbcast:4 row_mask:0xf bank_mask:0xf
v_mfma_f32_16x16x32_bf16 v[40:43],a[204:207],a[80:83],v[20:23]
s_waitcnt lgkmcnt(8)
ds_read_u16_d16_hi v62,v62
v_lshl_add_u64 v[30:31],v[24:25],0,v[26:27]
v_mul_f32_dpp v73,v14,v73 row_newbcast:5 row_mask:0xf bank_mask:0xf
v_mul_f32_dpp v74,v14,v74 row_newbcast:6 row_mask:0xf bank_mask:0xf
v_fma_mixhi_f16 v8,v51,s99,0
v_mul_f32_dpp v75,v14,v75 row_newbcast:7 row_mask:0xf bank_mask:0xf
v_mfma_f32_16x16x32_bf16 v[40:43],a[208:211],a[64:67],v[40:43]
s_waitcnt lgkmcnt(4)
v_lshl_add_u64 v[24:25],v[56:57],0,v[58:59]
v_cvt_pk_bf16_f32 v112,v112,v113
v_cvt_pk_bf16_f32 v113,v114,v115
v_lshlrev_b32_sdwa v61,1,v61 dst_sel:DWORD dst_unused:UNUSED_PAD src0_sel:DWORD src1_sel:WORD_1
v_pk_fma_f16 v9,v8,v8,s54
v_cvt_pk_bf16_f32 v114,v72,v73
v_mfma_f32_16x16x32_bf16 v[40:43],a[212:215],a[48:51],v[40:43]
ds_read_u16 v61,v61
v_cvt_pk_bf16_f32 v115,v74,v75
v_mul_f32_dpp v120,v14,v120 row_newbcast:8 row_mask:0xf bank_mask:0xf
v_lshlrev_b32_sdwa v63,1,v63 dst_sel:DWORD dst_unused:UNUSED_PAD src0_sel:DWORD src1_sel:WORD_1
v_accvgpr_write_b32 a24,v112
v_mfma_f32_16x16x32_bf16 v[56:59],a[212:215],a[52:55],v[20:23]
s_waitcnt lgkmcnt(3)
ds_read_u16_d16_hi v63,v63
v_or_b32 v26,v52,v54
v_mul_f32_dpp v121,v14,v121 row_newbcast:9 row_mask:0xf bank_mask:0xf
v_mul_f32_dpp v122,v14,v122 row_newbcast:10 row_mask:0xf bank_mask:0xf
v_pk_mul_f16 v9,v8,v9
v_accvgpr_write_b32 a25,v113
v_mfma_f32_16x16x32_bf16 v[56:59],a[208:211],a[68:71],v[56:59]
s_waitcnt vmcnt(13)
v_cvt_pk_bf16_f32 v27,v53,v55
v_mul_f32_dpp v123,v14,v123 row_newbcast:11 row_mask:0xf bank_mask:0xf
v_mul_f32_dpp v88,v14,v88 row_newbcast:12 row_mask:0xf bank_mask:0xf
v_lshlrev_b32_sdwa v32,1,v32 dst_sel:DWORD dst_unused:UNUSED_PAD src0_sel:DWORD src1_sel:WORD_1
v_accvgpr_write_b32 a26,v114
v_mfma_f32_16x16x32_bf16 v[56:59],a[204:207],a[84:87],v[56:59]
s_barrier
v_exp_f16 v8,v9
v_mul_f32_dpp v89,v14,v89 row_newbcast:13 row_mask:0xf bank_mask:0xf
v_mul_f32_dpp v90,v14,v90 row_newbcast:14 row_mask:0xf bank_mask:0xf
v_accvgpr_write_b32 a27,v115
v_mfma_f32_16x16x32_bf16 v[52:55],a[200:203],a[84:87],v[16:19]
ds_read_u16 v32,v32
v_mul_f32_dpp v91,v14,v91 row_newbcast:15 row_mask:0xf bank_mask:0xf
v_cvt_pk_bf16_f32 v120,v120,v121
v_lshlrev_b32_sdwa v34,1,v34 dst_sel:DWORD dst_unused:UNUSED_PAD src0_sel:DWORD src1_sel:WORD_1
v_cvt_pk_bf16_f32 v121,v122,v123
v_mfma_f32_16x16x32_bf16 v[52:55],a[196:199],a[68:71],v[52:55]
s_add_u32 m0,s58,18480
ds_read_u16_d16_hi v34,v34
v_cvt_pk_bf16_f32 v122,v88,v89
v_cvt_pk_bf16_f32 v123,v90,v91
v_lshlrev_b32_sdwa v33,1,v33 dst_sel:DWORD dst_unused:UNUSED_PAD src0_sel:DWORD src1_sel:WORD_1
v_mul_f32_dpp v128,v15,v128 row_newbcast:0 row_mask:0xf bank_mask:0xf
v_mfma_f32_16x16x32_bf16 v[52:55],a[192:195],a[52:55],v[52:55]
s_add_u32 s69,s69,s59
v_exp_f16_sdwa v8,v9 dst_sel:WORD_1 dst_unused:UNUSED_PRESERVE src0_sel:WORD_1
v_accvgpr_write_b32 a28,v120
v_mul_f32_dpp v129,v15,v129 row_newbcast:1 row_mask:0xf bank_mask:0xf
v_mul_f32_dpp v130,v15,v130 row_newbcast:2 row_mask:0xf bank_mask:0xf
v_mfma_f32_16x16x32_bf16 v[232:235],a[252:255],v[28:31],v[232:235]
buffer_load_dwordx4 v5,s[44:47],s69 offen offset:0 lds
v_accvgpr_write_b32 a29,v121
v_mul_f32_dpp v131,v15,v131 row_newbcast:3 row_mask:0xf bank_mask:0xf
v_lshlrev_b32_sdwa v35,1,v35 dst_sel:DWORD dst_unused:UNUSED_PAD src0_sel:DWORD src1_sel:WORD_1
v_mul_f32_dpp v104,v15,v104 row_newbcast:4 row_mask:0xf bank_mask:0xf
v_mfma_f32_16x16x32_bf16 v[236:239],a[252:255],v[24:27],v[236:239]
s_add_u32 s69,s69,s53
ds_read_u16 v33,v33
v_accvgpr_write_b32 a30,v122
v_mul_f32_dpp v105,v15,v105 row_newbcast:5 row_mask:0xf bank_mask:0xf
v_lshlrev_b32_sdwa v44,1,v44 dst_sel:DWORD dst_unused:UNUSED_PAD src0_sel:DWORD src1_sel:WORD_1
v_mul_f32_dpp v106,v15,v106 row_newbcast:6 row_mask:0xf bank_mask:0xf
v_mfma_f32_16x16x32_bf16 v[164:167],a[240:243],v[24:27],v[164:167]
buffer_load_dwordx4 v5,s[44:47],s69 offen offset:1024 lds
v_accvgpr_write_b32 a31,v123
v_mul_f32_dpp v107,v15,v107 row_newbcast:7 row_mask:0xf bank_mask:0xf
v_pk_fma_f16 v8,v8,s55,s55
v_cvt_pk_bf16_f32 v128,v128,v129
v_mfma_f32_16x16x32_bf16 v[160:163],a[240:243],v[28:31],v[160:163]
v_rcp_f16 v9,v8
v_cvt_pk_bf16_f32 v129,v130,v131
v_cvt_pk_bf16_f32 v130,v104,v105
v_lshlrev_b32_sdwa v46,1,v46 dst_sel:DWORD dst_unused:UNUSED_PAD src0_sel:DWORD src1_sel:WORD_1
v_cvt_pk_bf16_f32 v131,v106,v107
v_mfma_f32_16x16x32_bf16 v[240:243],a[224:227],v[28:31],v[240:243]
buffer_store_dwordx4 v[140:143],v7,s[40:43],s8 offen offset:192 sc1
v_mul_f32_dpp v116,v14,v116 row_newbcast:0 row_mask:0xf bank_mask:0xf
v_accvgpr_write_b32 a32,v128
v_mul_f32_dpp v117,v14,v117 row_newbcast:1 row_mask:0xf bank_mask:0xf
v_mfma_f32_16x16x32_bf16 v[244:247],a[224:227],v[24:27],v[244:247]
s_add_u32 s69,s69,s53
ds_read_u16_d16_hi v35,v35
v_mul_f32_dpp v118,v14,v118 row_newbcast:2 row_mask:0xf bank_mask:0xf
v_accvgpr_write_b32 a33,v129
v_lshlrev_b32_sdwa v45,1,v45 dst_sel:DWORD dst_unused:UNUSED_PAD src0_sel:DWORD src1_sel:WORD_1
v_mul_f32_dpp v119,v14,v119 row_newbcast:3 row_mask:0xf bank_mask:0xf
v_mfma_f32_16x16x32_bf16 v[180:183],a[232:235],v[24:27],v[180:183]
buffer_load_dwordx4 v5,s[44:47],s69 offen offset:2048 lds
v_mul_f32_dpp v76,v14,v76 row_newbcast:4 row_mask:0xf bank_mask:0xf
v_accvgpr_write_b32 a34,v130
v_lshlrev_b32_sdwa v47,1,v47 dst_sel:DWORD dst_unused:UNUSED_PAD src0_sel:DWORD src1_sel:WORD_1
v_mul_f32_dpp v77,v14,v77 row_newbcast:5 row_mask:0xf bank_mask:0xf
v_mfma_f32_16x16x32_bf16 v[176:179],a[232:235],v[28:31],v[176:179]
v_rcp_f16_sdwa v9,v8 dst_sel:WORD_1 dst_unused:UNUSED_PRESERVE src0_sel:WORD_1
v_mul_f32_dpp v78,v14,v78 row_newbcast:6 row_mask:0xf bank_mask:0xf
v_accvgpr_write_b32 a35,v131
v_mul_f32_dpp v79,v14,v79 row_newbcast:7 row_mask:0xf bank_mask:0xf
v_mfma_f32_16x16x32_bf16 v[248:251],a[236:239],v[28:31],v[248:251]
ds_read_u16 v44,v44
ds_read_u16_d16_hi v46,v46
v_cvt_pk_bf16_f32 v116,v116,v117
v_cvt_pk_bf16_f32 v117,v118,v119
v_lshlrev_b32_sdwa v48,1,v48 dst_sel:DWORD dst_unused:UNUSED_PAD src0_sel:DWORD src1_sel:WORD_1
v_cvt_pk_bf16_f32 v118,v76,v77
v_mfma_f32_16x16x32_bf16 v[252:255],a[236:239],v[24:27],v[252:255]
ds_read_u16 v45,v45
ds_read_u16_d16_hi v47,v47
v_cvt_pk_bf16_f32 v119,v78,v79
v_mfma_f32_32x32x16_bf16 v[64:79],a[16:19],a[16:19],0
v_mul_f32_dpp v124,v14,v124 row_newbcast:8 row_mask:0xf bank_mask:0xf
v_lshlrev_b32_sdwa v50,1,v50 dst_sel:DWORD dst_unused:UNUSED_PAD src0_sel:DWORD src1_sel:WORD_1
v_fma_mix_f32 v49,v49,v9,0 op_sel_hi:[0,1,0]
v_accvgpr_write_b32 a36,v116
v_fma_mix_f32 v51,v51,v9,0 op_sel:[0,1,0] op_sel_hi:[0,1,0]
v_mfma_f32_16x16x32_bf16 v[196:199],a[244:247],v[24:27],v[196:199]
ds_read_u16 v48,v48
v_mul_f32_dpp v125,v14,v125 row_newbcast:9 row_mask:0xf bank_mask:0xf
v_mul_f32_dpp v126,v14,v126 row_newbcast:10 row_mask:0xf bank_mask:0xf
v_mfma_f32_16x16x32_bf16 v[192:195],a[244:247],v[28:31],v[192:195]
ds_read_u16_d16_hi v50,v50
v_accvgpr_write_b32 a37,v117
v_mul_f32_dpp v127,v14,v127 row_newbcast:11 row_mask:0xf bank_mask:0xf
v_mul_f32_dpp v92,v14,v92 row_newbcast:12 row_mask:0xf bank_mask:0xf
ds_read_b64_tr_b16 a[228:229],v3 offset:0
ds_read_b64_tr_b16 a[230:231],v3 offset:1536
ds_read_b64_tr_b16 a[248:249],v3 offset:8
ds_read_b64_tr_b16 a[250:251],v3 offset:1544
s_mov_b64 exec,0
s_cmp_eq_u32 s52,0
s_cbranch_scc1 TE10_%=
s_cmp_eq_u32 s52,1
s_cbranch_scc1 TE11_%=
s_cmp_eq_u32 s52,2
s_cbranch_scc1 TE12_%=
s_branch TE13_%=
TR1_%=:
s_mov_b64 exec,-1
v_add_u32_sdwa v28,s60,v10 dst_sel:DWORD dst_unused:UNUSED_PAD src0_sel:DWORD src1_sel:WORD_1
v_subrev_u32 v8,88400,v28
v_min_u32 v28,v28,v8
v_add_u32 v29,7072,v28
v_subrev_u32 v8,88400,v29
v_min_u32 v29,v29,v8
s_waitcnt lgkmcnt(14)
v_lshl_add_u64 v[60:61],v[60:61],0,v[62:63]
s_waitcnt lgkmcnt(10)
v_lshl_add_u64 v[62:63],v[32:33],0,v[34:35]
s_waitcnt lgkmcnt(6)
v_lshl_add_u64 v[32:33],v[44:45],0,v[46:47]
s_waitcnt lgkmcnt(4)
v_or_b32 v34,v48,v50
v_cvt_pk_bf16_f32 v35,v49,v51
v_mfma_f32_16x16x32_bf16 v[208:211],a[252:255],v[60:63],v[208:211]
buffer_store_dwordx4 v[148:151],v7,s[40:43],s8 offen offset:256 sc1
v_xad_u32 v24,v11,0,v28
v_xad_u32 v25,v11,8,v29
v_xad_u32 v26,v11,16,v28
v_xad_u32 v27,v11,24,v29
v_mfma_f32_16x16x32_bf16 v[212:215],a[252:255],v[32:35],v[212:215]
ds_read_b64 a[0:1],v24 offset:0
v_accvgpr_write_b32 a38,v118
v_mul_f32_dpp v93,v14,v93 row_newbcast:13 row_mask:0xf bank_mask:0xf
v_mul_f32_dpp v94,v14,v94 row_newbcast:14 row_mask:0xf bank_mask:0xf
v_accvgpr_write_b32 a39,v119
v_mfma_f32_16x16x32_bf16 v[172:175],a[240:243],v[32:35],v[172:175]
ds_read_b64 a[2:3],v24 offset:192
v_mul_f32_dpp v95,v14,v95 row_newbcast:15 row_mask:0xf bank_mask:0xf
v_cvt_pk_bf16_f32 v124,v124,v125
v_cvt_pk_bf16_f32 v125,v126,v127
v_mfma_f32_16x16x32_bf16 v[168:171],a[240:243],v[60:63],v[168:171]
ds_read_b64 a[4:5],v25 offset:0
v_cvt_pk_bf16_f32 v126,v92,v93
v_cvt_pk_bf16_f32 v127,v94,v95
v_mfma_f32_32x32x16_bf16 v[80:95],a[16:19],a[16:19],0
v_mfma_f32_16x16x32_bf16 v[216:219],a[224:227],v[60:63],v[216:219]
ds_read_b64 a[6:7],v25 offset:192
v_mul_f32_dpp v132,v15,v132 row_newbcast:0 row_mask:0xf bank_mask:0xf
v_accvgpr_write_b32 a40,v124
v_mul_f32_dpp v133,v15,v133 row_newbcast:1 row_mask:0xf bank_mask:0xf
v_mfma_f32_16x16x32_bf16 v[220:223],a[224:227],v[32:35],v[220:223]
ds_read_b64 a[8:9],v24 offset:8
v_mul_f32_dpp v134,v15,v134 row_newbcast:2 row_mask:0xf bank_mask:0xf
v_accvgpr_write_b32 a41,v125
v_mul_f32_dpp v135,v15,v135 row_newbcast:3 row_mask:0xf bank_mask:0xf
v_mfma_f32_16x16x32_bf16 v[188:191],a[232:235],v[32:35],v[188:191]
ds_read_b64 a[10:11],v24 offset:200
v_mul_f32_dpp v108,v15,v108 row_newbcast:4 row_mask:0xf bank_mask:0xf
v_accvgpr_write_b32 a42,v126
v_mul_f32_dpp v109,v15,v109 row_newbcast:5 row_mask:0xf bank_mask:0xf
v_mfma_f32_16x16x32_bf16 v[184:187],a[232:235],v[60:63],v[184:187]
ds_read_b64 a[12:13],v25 offset:8
v_mul_f32_dpp v110,v15,v110 row_newbcast:6 row_mask:0xf bank_mask:0xf
v_accvgpr_write_b32 a43,v127
v_mul_f32_dpp v111,v15,v111 row_newbcast:7 row_mask:0xf bank_mask:0xf
v_mfma_f32_16x16x32_bf16 v[224:227],a[236:239],v[60:63],v[224:227]
ds_read_b64 a[14:15],v25 offset:200
v_cvt_pk_bf16_f32 v132,v132,v133
v_cvt_pk_bf16_f32 v133,v134,v135
v_cvt_pk_bf16_f32 v134,v108,v109
v_mfma_f32_16x16x32_bf16 v[228:231],a[236:239],v[32:35],v[228:231]
v_cvt_pk_bf16_f32 v135,v110,v111
v_mfma_f32_32x32x16_bf16 v[96:111],a[16:19],a[16:19],0
v_accvgpr_write_b32 a44,v132
v_mfma_f32_16x16x32_bf16 v[204:207],a[244:247],v[32:35],v[204:207]
v_accvgpr_write_b32 a45,v133
v_accvgpr_write_b32 a46,v134
v_accvgpr_write_b32 a47,v135
v_mfma_f32_16x16x32_bf16 v[200:203],a[244:247],v[60:63],v[200:203]
buffer_store_dwordx4 v[156:159],v7,s[40:43],s8 offen offset:320 sc1
v_mfma_f32_32x32x16_bf16 v[112:127],a[16:19],a[16:19],0
v_mov_b64 v[44:45],0
v_mov_b64 v[46:47],0
ds_read_b64 a[16:17],v26 offset:0
ds_read_b64 a[18:19],v26 offset:192
ds_read_b64 a[20:21],v27 offset:0
ds_read_b64 a[22:23],v27 offset:192
s_waitcnt lgkmcnt(8)
v_smfmac_f32_16x16x64_bf16 v[64:67],a[96:99],a[0:7],v10
s_waitcnt lgkmcnt(4)
ds_read_b64 a[0:1],v26 offset:8
ds_read_b64 a[2:3],v26 offset:200
ds_read_b64 a[4:5],v27 offset:8
ds_read_b64 a[6:7],v27 offset:200
v_smfmac_f32_16x16x64_bf16 v[68:71],a[100:103],a[8:15],v10
s_waitcnt lgkmcnt(4)
ds_read_b64 a[8:9],v24 offset:64
ds_read_b64 a[10:11],v24 offset:256
ds_read_b64 a[12:13],v25 offset:64
ds_read_b64 a[14:15],v25 offset:256
v_smfmac_f32_16x16x64_bf16 v[72:75],a[104:107],a[16:23],v10
s_waitcnt lgkmcnt(4)
ds_read_b64 a[16:17],v24 offset:72
ds_read_b64 a[18:19],v24 offset:264
ds_read_b64 a[20:21],v25 offset:72
ds_read_b64 a[22:23],v25 offset:264
v_and_b32 v60,8,v11
v_pk_mul_f32 v[32:33],v[64:65],v[64:65]
v_pk_mul_f32 v[34:35],v[66:67],v[66:67]
v_smfmac_f32_16x16x64_bf16 v[76:79],a[108:111],a[0:7],v10
s_waitcnt lgkmcnt(4)
ds_read_b64 a[0:1],v26 offset:64
ds_read_b64 a[2:3],v26 offset:256
ds_read_b64 a[4:5],v27 offset:64
ds_read_b64 a[6:7],v27 offset:256
v_mul_u32_u24 v60,884,v60
s_add_u32 s64,s60,3536
v_pk_add_f32 v[48:49],v[64:65],v[68:69]
v_pk_fma_f32 v[32:33],v[68:69],v[68:69],v[32:33]
v_pk_add_f32 v[50:51],v[66:67],v[70:71]
v_pk_fma_f32 v[34:35],v[70:71],v[70:71],v[34:35]
v_smfmac_f32_16x16x64_bf16 v[80:83],a[128:131],a[8:15],v10
s_waitcnt lgkmcnt(4)
ds_read_b64 a[8:9],v26 offset:72
ds_read_b64 a[10:11],v26 offset:264
ds_read_b64 a[12:13],v27 offset:72
ds_read_b64 a[14:15],v27 offset:264
v_add_u32 v63,s64,v60
v_pk_add_f32 v[48:49],v[48:49],v[72:73]
v_pk_fma_f32 v[32:33],v[72:73],v[72:73],v[32:33]
v_pk_add_f32 v[50:51],v[50:51],v[74:75]
v_pk_fma_f32 v[34:35],v[74:75],v[74:75],v[34:35]
v_smfmac_f32_16x16x64_bf16 v[84:87],a[132:135],a[16:23],v10
s_waitcnt lgkmcnt(4)
ds_read_b64 a[16:17],v24 offset:128
ds_read_b64 a[18:19],v24 offset:320
ds_read_b64 a[20:21],v25 offset:128
ds_read_b64 a[22:23],v25 offset:320
v_subrev_u32 v8,88400,v63
v_pk_add_f32 v[48:49],v[48:49],v[76:77]
v_pk_fma_f32 v[32:33],v[76:77],v[76:77],v[32:33]
v_pk_add_f32 v[50:51],v[50:51],v[78:79]
v_pk_fma_f32 v[34:35],v[78:79],v[78:79],v[34:35]
v_smfmac_f32_16x16x64_bf16 v[88:91],a[136:139],a[0:7],v10
s_waitcnt lgkmcnt(4)
ds_read_b64 a[0:1],v24 offset:136
ds_read_b64 a[2:3],v24 offset:328
ds_read_b64 a[4:5],v25 offset:136
ds_read_b64 a[6:7],v25 offset:328
v_min_u32 v63,v63,v8
v_pk_add_f32 v[48:49],v[48:49],v[80:81]
v_pk_fma_f32 v[32:33],v[80:81],v[80:81],v[32:33]
v_pk_add_f32 v[50:51],v[50:51],v[82:83]
v_pk_fma_f32 v[34:35],v[82:83],v[82:83],v[34:35]
v_smfmac_f32_16x16x64_bf16 v[92:95],a[140:143],a[8:15],v10
s_waitcnt lgkmcnt(4)
buffer_store_dwordx4 a[24:27],v7,s[40:43],s9 offen offset:0 sc1
ds_read_b64 a[8:9],v26 offset:128
ds_read_b64 a[10:11],v26 offset:320
ds_read_b64 a[12:13],v27 offset:128
ds_read_b64 a[14:15],v27 offset:320
v_xad_u32 v24,v11,32,v28
v_xad_u32 v25,v11,40,v29
v_pk_add_f32 v[48:49],v[48:49],v[84:85]
v_pk_fma_f32 v[32:33],v[84:85],v[84:85],v[32:33]
v_pk_add_f32 v[50:51],v[50:51],v[86:87]
v_pk_fma_f32 v[34:35],v[86:87],v[86:87],v[34:35]
v_smfmac_f32_16x16x64_bf16 v[96:99],a[160:163],a[16:23],v10
s_waitcnt lgkmcnt(4)
ds_read_b64 a[16:17],v26 offset:136
ds_read_b64 a[18:19],v26 offset:328
ds_read_b64 a[20:21],v27 offset:136
ds_read_b64 a[22:23],v27 offset:328
v_add_u32 v62,3536,v63
v_pk_add_f32 v[48:49],v[48:49],v[88:89]
v_pk_fma_f32 v[32:33],v[88:89],v[88:89],v[32:33]
v_pk_add_f32 v[50:51],v[50:51],v[90:91]
v_pk_fma_f32 v[34:35],v[90:91],v[90:91],v[34:35]
v_smfmac_f32_16x16x64_bf16 v[100:103],a[164:167],a[0:7],v10
s_waitcnt lgkmcnt(4)
ds_read_b64 a[0:1],v24 offset:0
ds_read_b64 a[2:3],v24 offset:192
ds_read_b64 a[4:5],v25 offset:0
ds_read_b64 a[6:7],v25 offset:192
v_xad_u32 v26,v11,48,v28
v_xad_u32 v27,v11,56,v29
v_pk_add_f32 v[48:49],v[48:49],v[92:93]
v_pk_fma_f32 v[32:33],v[92:93],v[92:93],v[32:33]
v_pk_add_f32 v[50:51],v[50:51],v[94:95]
v_pk_fma_f32 v[34:35],v[94:95],v[94:95],v[34:35]
v_smfmac_f32_16x16x64_bf16 v[104:107],a[168:171],a[8:15],v10
s_waitcnt lgkmcnt(4)
ds_read_b64 a[8:9],v24 offset:8
ds_read_b64 a[10:11],v24 offset:200
ds_read_b64 a[12:13],v25 offset:8
ds_read_b64 a[14:15],v25 offset:200
v_subrev_u32 v8,88400,v62
v_pk_add_f32 v[48:49],v[48:49],v[96:97]
v_pk_fma_f32 v[32:33],v[96:97],v[96:97],v[32:33]
v_pk_add_f32 v[50:51],v[50:51],v[98:99]
v_pk_fma_f32 v[34:35],v[98:99],v[98:99],v[34:35]
v_smfmac_f32_16x16x64_bf16 v[108:111],a[172:175],a[16:23],v10
s_waitcnt lgkmcnt(4)
ds_read_b64 a[16:17],v24 offset:64
ds_read_b64 a[18:19],v24 offset:256
ds_read_b64 a[20:21],v25 offset:64
ds_read_b64 a[22:23],v25 offset:256
v_min_u32 v62,v62,v8
v_pk_add_f32 v[48:49],v[48:49],v[100:101]
v_pk_fma_f32 v[32:33],v[100:101],v[100:101],v[32:33]
v_pk_add_f32 v[50:51],v[50:51],v[102:103]
v_pk_fma_f32 v[34:35],v[102:103],v[102:103],v[34:35]
v_smfmac_f32_16x16x64_bf16 v[112:115],a[112:115],a[0:7],v10
s_waitcnt lgkmcnt(4)
ds_read_b64 a[0:1],v24 offset:72
ds_read_b64 a[2:3],v24 offset:264
ds_read_b64 a[4:5],v25 offset:72
ds_read_b64 a[6:7],v25 offset:264
v_xor_b32 v9,0x80008,v12
v_pk_add_f32 v[48:49],v[48:49],v[104:105]
v_pk_fma_f32 v[32:33],v[104:105],v[104:105],v[32:33]
v_pk_add_f32 v[50:51],v[50:51],v[106:107]
v_pk_fma_f32 v[34:35],v[106:107],v[106:107],v[34:35]
v_mfma_f32_32x32x16_bf16 v[128:143],v[44:47],v[44:47],0
TP5482_%=:
s_cbranch_execz TR0_%=
v_smfmac_f32_16x16x64_bf16 v[116:119],a[116:119],a[8:15],v10
s_waitcnt lgkmcnt(4)
buffer_store_dwordx4 a[28:31],v7,s[40:43],s9 offen offset:64 sc1
ds_read_b64 a[8:9],v24 offset:128
ds_read_b64 a[10:11],v24 offset:320
ds_read_b64 a[12:13],v25 offset:128
ds_read_b64 a[14:15],v25 offset:320
v_add_u32_sdwa v60,v12,v63 dst_sel:DWORD dst_unused:UNUSED_PAD src0_sel:WORD_0 src1_sel:DWORD
v_pk_add_f32 v[48:49],v[48:49],v[108:109]
v_pk_fma_f32 v[32:33],v[108:109],v[108:109],v[32:33]
v_pk_add_f32 v[50:51],v[50:51],v[110:111]
v_pk_fma_f32 v[34:35],v[110:111],v[110:111],v[34:35]
v_smfmac_f32_16x16x64_bf16 v[120:123],a[144:147],a[16:23],v10
s_waitcnt lgkmcnt(4)
ds_read_b64 a[16:17],v24 offset:136
ds_read_b64 a[18:19],v24 offset:328
ds_read_b64 a[20:21],v25 offset:136
ds_read_b64 a[22:23],v25 offset:328
v_add_u32_sdwa v61,v12,v63 dst_sel:DWORD dst_unused:UNUSED_PAD src0_sel:WORD_1 src1_sel:DWORD
v_pk_add_f32 v[48:49],v[48:49],v[112:113]
v_pk_fma_f32 v[32:33],v[112:113],v[112:113],v[32:33]
v_pk_add_f32 v[50:51],v[50:51],v[114:115]
v_pk_fma_f32 v[34:35],v[114:115],v[114:115],v[34:35]
v_smfmac_f32_16x16x64_bf16 v[124:127],a[148:151],a[0:7],v10
s_waitcnt lgkmcnt(4)
ds_read_b64 a[0:1],v26 offset:0
ds_read_b64 a[2:3],v26 offset:192
ds_read_b64 a[4:5],v27 offset:0
ds_read_b64 a[6:7],v27 offset:192
v_add_u32_sdwa v63,v9,v62 dst_sel:DWORD dst_unused:UNUSED_PAD src0_sel:WORD_1 src1_sel:DWORD
v_pk_add_f32 v[48:49],v[48:49],v[116:117]
v_pk_fma_f32 v[32:33],v[116:117],v[116:117],v[32:33]
v_pk_add_f32 v[50:51],v[50:51],v[118:119]
v_pk_fma_f32 v[34:35],v[118:119],v[118:119],v[34:35]
v_smfmac_f32_16x16x64_bf16 v[128:131],a[176:179],a[8:15],v10
s_waitcnt lgkmcnt(4)
ds_read_b64 a[8:9],v26 offset:8
ds_read_b64 a[10:11],v26 offset:200
ds_read_b64 a[12:13],v27 offset:8
ds_read_b64 a[14:15],v27 offset:200
v_add_u32_sdwa v62,v9,v62 dst_sel:DWORD dst_unused:UNUSED_PAD src0_sel:WORD_0 src1_sel:DWORD
v_pk_add_f32 v[48:49],v[48:49],v[120:121]
v_pk_fma_f32 v[32:33],v[120:121],v[120:121],v[32:33]
v_pk_add_f32 v[50:51],v[50:51],v[122:123]
v_pk_fma_f32 v[34:35],v[122:123],v[122:123],v[34:35]
v_mfma_f32_32x32x16_bf16 v[144:159],v[44:47],v[44:47],0
v_smfmac_f32_16x16x64_bf16 v[132:135],a[180:183],a[16:23],v10
s_waitcnt lgkmcnt(4)
ds_read_b64 a[16:17],v26 offset:64
ds_read_b64 a[18:19],v26 offset:256
ds_read_b64 a[20:21],v27 offset:64
ds_read_b64 a[22:23],v27 offset:256
v_pk_add_f32 v[48:49],v[48:49],v[124:125]
v_pk_fma_f32 v[32:33],v[124:125],v[124:125],v[32:33]
v_pk_add_f32 v[50:51],v[50:51],v[126:127]
v_pk_fma_f32 v[34:35],v[126:127],v[126:127],v[34:35]
v_smfmac_f32_16x16x64_bf16 v[136:139],a[120:123],a[0:7],v10
s_waitcnt lgkmcnt(4)
ds_read_b64 a[0:1],v26 offset:72
ds_read_b64 a[2:3],v26 offset:264
ds_read_b64 a[4:5],v27 offset:72
ds_read_b64 a[6:7],v27 offset:264
v_pk_add_f32 v[48:49],v[48:49],v[128:129]
v_pk_fma_f32 v[32:33],v[128:129],v[128:129],v[32:33]
v_pk_add_f32 v[50:51],v[50:51],v[130:131]
v_pk_fma_f32 v[34:35],v[130:131],v[130:131],v[34:35]
v_smfmac_f32_16x16x64_bf16 v[140:143],a[124:127],a[8:15],v10
s_waitcnt lgkmcnt(4)
ds_read_b64 a[8:9],v26 offset:128
ds_read_b64 a[10:11],v26 offset:320
ds_read_b64 a[12:13],v27 offset:128
ds_read_b64 a[14:15],v27 offset:320
v_pk_add_f32 v[48:49],v[48:49],v[132:133]
v_pk_fma_f32 v[32:33],v[132:133],v[132:133],v[32:33]
v_pk_add_f32 v[50:51],v[50:51],v[134:135]
v_pk_fma_f32 v[34:35],v[134:135],v[134:135],v[34:35]
v_smfmac_f32_16x16x64_bf16 v[144:147],a[152:155],a[16:23],v10
s_waitcnt lgkmcnt(4)
buffer_store_dwordx4 a[32:35],v7,s[40:43],s9 offen offset:128 sc1
ds_read_b64 a[16:17],v26 offset:136
ds_read_b64 a[18:19],v26 offset:328
ds_read_b64 a[20:21],v27 offset:136
ds_read_b64 a[22:23],v27 offset:328
v_pk_add_f32 v[48:49],v[48:49],v[136:137]
v_pk_fma_f32 v[32:33],v[136:137],v[136:137],v[32:33]
v_pk_add_f32 v[50:51],v[50:51],v[138:139]
v_pk_fma_f32 v[34:35],v[138:139],v[138:139],v[34:35]
v_smfmac_f32_16x16x64_bf16 v[148:151],a[156:159],a[0:7],v10
s_waitcnt lgkmcnt(4)
ds_read_b64 a[252:253],v60 offset:57344
ds_read_b64 a[254:255],v60 offset:57352
ds_read_b64 a[240:241],v61 offset:57344
ds_read_b64 a[242:243],v61 offset:57352
ds_read_b128 a[236:239],v13 offset:0
v_pk_add_f32 v[48:49],v[48:49],v[140:141]
v_pk_fma_f32 v[32:33],v[140:141],v[140:141],v[32:33]
v_pk_add_f32 v[50:51],v[50:51],v[142:143]
v_pk_fma_f32 v[34:35],v[142:143],v[142:143],v[34:35]
v_smfmac_f32_16x16x64_bf16 v[152:155],a[184:187],a[8:15],v10
s_waitcnt lgkmcnt(5)
ds_read_b64 a[224:225],v62 offset:57344
ds_read_b64 a[226:227],v62 offset:57352
ds_read_b64 a[232:233],v63 offset:57344
ds_read_b64 a[234:235],v63 offset:57352
ds_read_b128 a[244:247],v13 offset:256
v_pk_add_f32 v[48:49],v[48:49],v[144:145]
v_pk_fma_f32 v[32:33],v[144:145],v[144:145],v[32:33]
v_pk_add_f32 v[50:51],v[50:51],v[146:147]
v_pk_fma_f32 v[34:35],v[146:147],v[146:147],v[34:35]
v_smfmac_f32_16x16x64_bf16 v[156:159],a[188:191],a[16:23],v10
ds_read_b128 v[24:27],v4 offset:3584
ds_read_b128 v[28:31],v4 offset:3616
v_pk_add_f32 v[48:49],v[48:49],v[148:149]
v_pk_fma_f32 v[32:33],v[148:149],v[148:149],v[32:33]
v_pk_add_f32 v[50:51],v[50:51],v[150:151]
v_pk_fma_f32 v[34:35],v[150:151],v[150:151],v[34:35]
v_pk_add_f32 v[48:49],v[48:49],v[152:153]
v_pk_fma_f32 v[32:33],v[152:153],v[152:153],v[32:33]
v_pk_add_f32 v[50:51],v[50:51],v[154:155]
v_pk_fma_f32 v[34:35],v[154:155],v[154:155],v[34:35]
v_pk_add_f32 v[48:49],v[48:49],v[156:157]
v_pk_fma_f32 v[32:33],v[156:157],v[156:157],v[32:33]
v_pk_add_f32 v[50:51],v[50:51],v[158:159]
v_pk_fma_f32 v[34:35],v[158:159],v[158:159],v[34:35]
s_nop 0
v_permlane32_swap_b32 v48,v50
v_permlane32_swap_b32 v49,v51
v_permlane32_swap_b32 v32,v34
v_permlane32_swap_b32 v33,v35
v_pk_add_f32 v[48:49],v[48:49],v[50:51]
v_pk_add_f32 v[32:33],v[32:33],v[34:35]
s_add_u32 s60,s60,56576
s_sub_u32 s64,s60,88400
s_min_u32 s60,s60,s64
v_permlane16_swap_b32 v48,v49
v_permlane16_swap_b32 v32,v33
v_add_f32 v8,v48,v49
v_add_f32 v9,v32,v33
v_mul_f32 v8,0x3c2aaaab,v8
v_mul_f32 v9,0x3c2aaaab,v9
v_cmp_ngt_f32 vcc,0x5d800000,v9
v_fma_f32 v9,-v8,v8,v9
v_max_f32 v9,0,v9
s_or_b64 s[90:91],s[90:91],vcc
v_add_f32 v9,0x3727c5ac,v9
v_rsq_f32 v9,v9
s_nop 0
v_mul_f32 v48,s96,v9
v_mul_f32 v49,s96,v9
v_mul_f32 v32,-v8,v48
v_mul_f32 v33,-v8,v48
v_permlane16_swap_b32 v48,v49
s_nop 0
v_permlane16_swap_b32 v32,v33
v_mov_b64 v[50:51],v[48:49]
v_mov_b64 v[34:35],v[32:33]
s_nop 0
v_permlane32_swap_b32 v48,v50
v_permlane32_swap_b32 v49,v51
v_permlane32_swap_b32 v32,v34
v_permlane32_swap_b32 v33,v35
v_pk_fma_f32 v[64:65],v[64:65],v[48:49],v[32:33]
v_pk_fma_f32 v[66:67],v[66:67],v[50:51],v[34:35]
v_pk_fma_f32 v[68:69],v[68:69],v[48:49],v[32:33]
v_pk_fma_f32 v[70:71],v[70:71],v[50:51],v[34:35]
v_pk_fma_f32 v[72:73],v[72:73],v[48:49],v[32:33]
v_pk_fma_f32 v[74:75],v[74:75],v[50:51],v[34:35]
v_pk_fma_f32 v[76:77],v[76:77],v[48:49],v[32:33]
v_pk_fma_f32 v[78:79],v[78:79],v[50:51],v[34:35]
v_pk_fma_f32 v[112:113],v[112:113],v[48:49],v[32:33]
v_pk_fma_f32 v[114:115],v[114:115],v[50:51],v[34:35]
v_pk_fma_f32 v[116:117],v[116:117],v[48:49],v[32:33]
v_pk_fma_f32 v[118:119],v[118:119],v[50:51],v[34:35]
buffer_store_dwordx4 a[36:39],v7,s[40:43],s9 offen offset:192 sc1
v_pk_fma_f32 v[136:137],v[136:137],v[48:49],v[32:33]
v_pk_fma_f32 v[138:139],v[138:139],v[50:51],v[34:35]
v_pk_fma_f32 v[140:141],v[140:141],v[48:49],v[32:33]
v_pk_fma_f32 v[142:143],v[142:143],v[50:51],v[34:35]
v_pk_fma_f32 v[80:81],v[80:81],v[48:49],v[32:33]
v_pk_fma_f32 v[82:83],v[82:83],v[50:51],v[34:35]
v_pk_fma_f32 v[84:85],v[84:85],v[48:49],v[32:33]
v_pk_fma_f32 v[86:87],v[86:87],v[50:51],v[34:35]
v_pk_fma_f32 v[88:89],v[88:89],v[48:49],v[32:33]
v_pk_fma_f32 v[90:91],v[90:91],v[50:51],v[34:35]
v_pk_fma_f32 v[92:93],v[92:93],v[48:49],v[32:33]
v_pk_fma_f32 v[94:95],v[94:95],v[50:51],v[34:35]
v_pk_fma_f32 v[120:121],v[120:121],v[48:49],v[32:33]
v_pk_fma_f32 v[122:123],v[122:123],v[50:51],v[34:35]
v_pk_fma_f32 v[124:125],v[124:125],v[48:49],v[32:33]
v_pk_fma_f32 v[126:127],v[126:127],v[50:51],v[34:35]
v_pk_fma_f32 v[144:145],v[144:145],v[48:49],v[32:33]
v_pk_fma_f32 v[146:147],v[146:147],v[50:51],v[34:35]
v_pk_fma_f32 v[148:149],v[148:149],v[48:49],v[32:33]
v_pk_fma_f32 v[150:151],v[150:151],v[50:51],v[34:35]
v_pk_fma_f32 v[96:97],v[96:97],v[48:49],v[32:33]
v_pk_fma_f32 v[98:99],v[98:99],v[50:51],v[34:35]
v_pk_fma_f32 v[100:101],v[100:101],v[48:49],v[32:33]
v_pk_fma_f32 v[102:103],v[102:103],v[50:51],v[34:35]
v_pk_fma_f32 v[104:105],v[104:105],v[48:49],v[32:33]
v_pk_fma_f32 v[106:107],v[106:107],v[50:51],v[34:35]
v_pk_fma_f32 v[108:109],v[108:109],v[48:49],v[32:33]
v_pk_fma_f32 v[110:111],v[110:111],v[50:51],v[34:35]
v_pk_fma_f32 v[128:129],v[128:129],v[48:49],v[32:33]
v_pk_fma_f32 v[130:131],v[130:131],v[50:51],v[34:35]
v_pk_fma_f32 v[132:133],v[132:133],v[48:49],v[32:33]
v_pk_fma_f32 v[134:135],v[134:135],v[50:51],v[34:35]
v_pk_fma_f32 v[152:153],v[152:153],v[48:49],v[32:33]
v_pk_fma_f32 v[154:155],v[154:155],v[50:51],v[34:35]
v_pk_fma_f32 v[156:157],v[156:157],v[48:49],v[32:33]
v_pk_fma_f32 v[158:159],v[158:159],v[50:51],v[34:35]
v_mfma_f32_16x16x32_bf16 v[44:47],a[192:195],a[56:59],v[16:19]
ds_read_b64_tr_b16 a[216:217],v1 offset:0
v_cvt_pk_bf16_f32 v64,v64,v68
v_cvt_pk_bf16_f32 v72,v72,v76
v_cvt_pk_bf16_f32 v112,v112,v116
v_lshlrev_b32_sdwa v36,1,v36 dst_sel:DWORD dst_unused:UNUSED_PAD src0_sel:DWORD src1_sel:WORD_1
v_mfma_f32_16x16x32_bf16 v[44:47],a[196:199],a[72:75],v[44:47]
ds_read_u16 v36,v36
v_accvgpr_write_b32 a0,v64
v_accvgpr_write_b32 a1,v72
v_cvt_pk_bf16_f32 v136,v136,v140
v_accvgpr_write_b32 a2,v112
v_mfma_f32_16x16x32_bf16 v[44:47],a[200:203],a[88:91],v[44:47]
buffer_store_dwordx4 a[40:43],v7,s[40:43],s9 offen offset:256 sc1
v_cvt_pk_bf16_f32 v65,v65,v69
v_accvgpr_write_b32 a3,v136
v_cvt_pk_bf16_f32 v73,v73,v77
v_accvgpr_write_b32 a4,v65
v_fma_mixlo_f16 v8,v57,s99,0
v_mfma_f32_16x16x32_bf16 v[48:51],a[204:207],a[88:91],v[20:23]
ds_read_b64_tr_b16 a[218:219],v1 offset:1024
v_cvt_pk_bf16_f32 v113,v113,v117
v_accvgpr_write_b32 a5,v73
v_cvt_pk_bf16_f32 v137,v137,v141
v_accvgpr_write_b32 a6,v113
v_lshlrev_b32_sdwa v38,1,v38 dst_sel:DWORD dst_unused:UNUSED_PAD src0_sel:DWORD src1_sel:WORD_1
v_mfma_f32_16x16x32_bf16 v[48:51],a[208:211],a[72:75],v[48:51]
ds_read_u16_d16_hi v38,v38
v_cvt_pk_bf16_f32 v66,v66,v70
v_accvgpr_write_b32 a7,v137
v_cvt_pk_bf16_f32 v74,v74,v78
v_accvgpr_write_b32 a8,v66
v_fma_mixhi_f16 v8,v59,s99,0
v_mfma_f32_16x16x32_bf16 v[48:51],a[212:215],a[56:59],v[48:51]
ds_read_b64_tr_b16 a[220:221],v1 offset:2048
v_accvgpr_write_b32 a9,v74
v_cvt_pk_bf16_f32 v114,v114,v118
v_accvgpr_write_b32 a10,v114
v_lshlrev_b32_sdwa v37,1,v37 dst_sel:DWORD dst_unused:UNUSED_PAD src0_sel:DWORD src1_sel:WORD_1
v_cvt_pk_bf16_f32 v138,v138,v142
v_mfma_f32_16x16x32_bf16 v[32:35],a[212:215],a[60:63],v[20:23]
ds_read_u16 v37,v37
v_accvgpr_write_b32 a11,v138
v_cvt_pk_bf16_f32 v67,v67,v71
v_accvgpr_write_b32 a12,v67
v_mfma_f32_16x16x32_bf16 v[32:35],a[208:211],a[76:79],v[32:35]
ds_read_b64_tr_b16 a[222:223],v1 offset:3072
v_cvt_pk_bf16_f32 v75,v75,v79
v_accvgpr_write_b32 a13,v75
v_cvt_pk_bf16_f32 v115,v115,v119
v_accvgpr_write_b32 a14,v115
v_pk_fma_f16 v9,v8,v8,s54
v_mfma_f32_16x16x32_bf16 v[32:35],a[204:207],a[92:95],v[32:35]
s_waitcnt lgkmcnt(7)
v_cvt_pk_bf16_f32 v139,v139,v143
v_accvgpr_write_b32 a15,v139
v_cvt_pk_bf16_f32 v80,v80,v84
v_cvt_pk_bf16_f32 v88,v88,v92
v_lshlrev_b32_sdwa v39,1,v39 dst_sel:DWORD dst_unused:UNUSED_PAD src0_sel:DWORD src1_sel:WORD_1
v_mfma_f32_16x16x32_bf16 v[136:139],a[236:239],a[252:255],v[24:27]
ds_read_u16_d16_hi v39,v39
v_cvt_pk_bf16_f32 v120,v120,v124
v_accvgpr_write_b32 a16,v80
v_accvgpr_write_b32 a17,v88
v_cvt_pk_bf16_f32 v144,v144,v148
v_pk_mul_f16 v9,v8,v9
v_mfma_f32_16x16x32_bf16 v[140:143],a[236:239],a[240:243],v[24:27]
v_accvgpr_write_b32 a18,v120
v_cvt_pk_bf16_f32 v81,v81,v85
v_accvgpr_write_b32 a19,v144
v_cvt_pk_bf16_f32 v89,v89,v93
v_lshlrev_b32_sdwa v40,1,v40 dst_sel:DWORD dst_unused:UNUSED_PAD src0_sel:DWORD src1_sel:WORD_1
v_mfma_f32_16x16x32_bf16 v[112:115],a[236:239],a[224:227],v[24:27]
v_exp_f16 v8,v9
v_accvgpr_write_b32 a20,v81
v_cvt_pk_bf16_f32 v121,v121,v125
v_accvgpr_write_b32 a21,v89
v_cvt_pk_bf16_f32 v145,v145,v149
v_mfma_f32_16x16x32_bf16 v[116:119],a[236:239],a[232:235],v[24:27]
ds_read_b128 a[236:239],v13 offset:512
ds_read_b128 v[24:27],v4 offset:3712
v_accvgpr_write_b32 a22,v121
v_cvt_pk_bf16_f32 v82,v82,v86
v_accvgpr_write_b32 a23,v145
v_mfma_f32_16x16x32_bf16 v[64:67],a[244:247],a[252:255],v[28:31]
buffer_store_dwordx4 a[44:47],v7,s[40:43],s9 offen offset:320 sc1
ds_read_b64 a[252:253],v60 offset:57408
v_cvt_pk_bf16_f32 v90,v90,v94
v_accvgpr_write_b32 a24,v82
v_accvgpr_write_b32 a25,v90
v_cvt_pk_bf16_f32 v122,v122,v126
v_lshlrev_b32_sdwa v42,1,v42 dst_sel:DWORD dst_unused:UNUSED_PAD src0_sel:DWORD src1_sel:WORD_1
v_mfma_f32_16x16x32_bf16 v[68:71],a[244:247],a[240:243],v[28:31]
ds_read_b64 a[254:255],v60 offset:57416
ds_read_b64 a[240:241],v61 offset:57408
v_accvgpr_write_b32 a26,v122
v_cvt_pk_bf16_f32 v146,v146,v150
v_accvgpr_write_b32 a27,v146
v_cvt_pk_bf16_f32 v83,v83,v87
v_mfma_f32_16x16x32_bf16 v[72:75],a[244:247],a[224:227],v[28:31]
ds_read_b64 a[242:243],v61 offset:57416
ds_read_b64 a[224:225],v62 offset:57408
v_accvgpr_write_b32 a28,v83
v_cvt_pk_bf16_f32 v91,v91,v95
v_accvgpr_write_b32 a29,v91
v_cvt_pk_bf16_f32 v123,v123,v127
v_lshlrev_b32_sdwa v41,1,v41 dst_sel:DWORD dst_unused:UNUSED_PAD src0_sel:DWORD src1_sel:WORD_1
v_mfma_f32_16x16x32_bf16 v[76:79],a[244:247],a[232:235],v[28:31]
s_waitcnt lgkmcnt(3)
ds_read_b64 a[226:227],v62 offset:57416
ds_read_b64 a[232:233],v63 offset:57408
v_exp_f16_sdwa v8,v9 dst_sel:WORD_1 dst_unused:UNUSED_PRESERVE src0_sel:WORD_1
v_accvgpr_write_b32 a30,v123
v_cvt_pk_bf16_f32 v147,v147,v151
v_accvgpr_write_b32 a31,v147
v_cvt_pk_bf16_f32 v96,v96,v100
v_mfma_f32_16x16x32_bf16 v[144:147],a[236:239],a[252:255],v[24:27]
s_waitcnt lgkmcnt(3)
ds_read_b64 a[234:235],v63 offset:57416
ds_read_b128 a[244:247],v13 offset:768
v_cvt_pk_bf16_f32 v104,v104,v108
v_cvt_pk_bf16_f32 v128,v128,v132
v_accvgpr_write_b32 a32,v96
v_accvgpr_write_b32 a33,v104
v_lshlrev_b32_sdwa v43,1,v43 dst_sel:DWORD dst_unused:UNUSED_PAD src0_sel:DWORD src1_sel:WORD_1
v_mfma_f32_16x16x32_bf16 v[148:151],a[236:239],a[240:243],v[24:27]
s_waitcnt lgkmcnt(3)
ds_read_b128 v[28:31],v4 offset:3744
v_cvt_pk_bf16_f32 v152,v152,v156
v_accvgpr_write_b32 a34,v128
v_cvt_pk_bf16_f32 v97,v97,v101
v_mfma_f32_16x16x32_bf16 v[120:123],a[236:239],a[224:227],v[24:27]
s_waitcnt lgkmcnt(2)
ds_read_u16 v40,v40
v_accvgpr_write_b32 a35,v152
v_cvt_pk_bf16_f32 v105,v105,v109
v_accvgpr_write_b32 a36,v97
v_cvt_pk_bf16_f32 v129,v129,v133
v_mfma_f32_16x16x32_bf16 v[124:127],a[236:239],a[232:235],v[24:27]
s_waitcnt lgkmcnt(1)
ds_read_b128 a[236:239],v13 offset:1024
ds_read_b128 v[24:27],v4 offset:3840
v_accvgpr_write_b32 a37,v105
v_cvt_pk_bf16_f32 v153,v153,v157
v_accvgpr_write_b32 a38,v129
v_cvt_pk_bf16_f32 v98,v98,v102
v_lshlrev_b32_sdwa v52,1,v52 dst_sel:DWORD dst_unused:UNUSED_PAD src0_sel:DWORD src1_sel:WORD_1
v_mfma_f32_16x16x32_bf16 v[80:83],a[244:247],a[252:255],v[28:31]
ds_read_b64 a[252:253],v60 offset:57472
ds_read_b64 a[254:255],v60 offset:57480
v_accvgpr_write_b32 a39,v153
v_cvt_pk_bf16_f32 v106,v106,v110
v_accvgpr_write_b32 a40,v98
v_accvgpr_write_b32 a41,v106
v_pk_fma_f16 v8,v8,s55,s55
v_mfma_f32_16x16x32_bf16 v[84:87],a[244:247],a[240:243],v[28:31]
ds_read_b64 a[240:241],v61 offset:57472
ds_read_b64 a[242:243],v61 offset:57480
v_cvt_pk_bf16_f32 v130,v130,v134
v_accvgpr_write_b32 a42,v130
v_cvt_pk_bf16_f32 v154,v154,v158
v_accvgpr_write_b32 a43,v154
v_lshlrev_b32_sdwa v54,1,v54 dst_sel:DWORD dst_unused:UNUSED_PAD src0_sel:DWORD src1_sel:WORD_1
v_mfma_f32_16x16x32_bf16 v[88:91],a[244:247],a[224:227],v[28:31]
ds_read_b64 a[224:225],v62 offset:57472
ds_read_b64 a[226:227],v62 offset:57480
v_rcp_f16 v9,v8
v_cvt_pk_bf16_f32 v99,v99,v103
v_accvgpr_write_b32 a44,v99
v_cvt_pk_bf16_f32 v107,v107,v111
v_accvgpr_write_b32 a45,v107
v_mfma_f32_16x16x32_bf16 v[92:95],a[244:247],a[232:235],v[28:31]
s_waitcnt lgkmcnt(4)
ds_read_b64 a[232:233],v63 offset:57472
ds_read_b64 a[234:235],v63 offset:57480
v_cvt_pk_bf16_f32 v131,v131,v135
v_accvgpr_write_b32 a46,v131
v_cvt_pk_bf16_f32 v155,v155,v159
v_accvgpr_write_b32 a47,v155
v_mfma_f32_16x16x32_bf16 v[152:155],a[236:239],a[252:255],v[24:27]
s_waitcnt lgkmcnt(4)
ds_read_b128 a[244:247],v13 offset:1280
ds_read_b128 v[28:31],v4 offset:3872
v_lshlrev_b32_sdwa v53,1,v53 dst_sel:DWORD dst_unused:UNUSED_PAD src0_sel:DWORD src1_sel:WORD_1
v_mfma_f32_16x16x32_bf16 v[156:159],a[236:239],a[240:243],v[24:27]
s_waitcnt lgkmcnt(4)
ds_read_u16_d16_hi v42,v42
v_lshlrev_b32_sdwa v55,1,v55 dst_sel:DWORD dst_unused:UNUSED_PAD src0_sel:DWORD src1_sel:WORD_1
v_mfma_f32_16x16x32_bf16 v[128:131],a[236:239],a[224:227],v[24:27]
s_waitcnt lgkmcnt(3)
ds_read_u16 v41,v41
v_lshlrev_b32_sdwa v56,1,v56 dst_sel:DWORD dst_unused:UNUSED_PAD src0_sel:DWORD src1_sel:WORD_1
v_mfma_f32_16x16x32_bf16 v[132:135],a[236:239],a[232:235],v[24:27]
s_waitcnt lgkmcnt(2)
ds_read_b64_tr_b16 a[236:237],v3 offset:64
ds_read_b64_tr_b16 a[238:239],v3 offset:1600
v_rcp_f16_sdwa v9,v8 dst_sel:WORD_1 dst_unused:UNUSED_PRESERVE src0_sel:WORD_1
v_mfma_f32_16x16x32_bf16 v[96:99],a[244:247],a[252:255],v[28:31]
ds_read_b64_tr_b16 a[252:253],v3 offset:72
ds_read_b64_tr_b16 a[254:255],v3 offset:1608
v_mfma_f32_16x16x32_bf16 v[100:103],a[244:247],a[240:243],v[28:31]
ds_read_b64_tr_b16 a[240:241],v3 offset:128
ds_read_b64_tr_b16 a[242:243],v3 offset:1664
v_mfma_f32_16x16x32_bf16 v[104:107],a[244:247],a[224:227],v[28:31]
ds_read_b64_tr_b16 a[224:225],v3 offset:136
ds_read_b64_tr_b16 a[226:227],v3 offset:1672
v_mfma_f32_16x16x32_bf16 v[108:111],a[244:247],a[232:235],v[28:31]
ds_read_u16_d16_hi v43,v43
ds_read_u16 v52,v52
v_lshlrev_b32_sdwa v58,1,v58 dst_sel:DWORD dst_unused:UNUSED_PAD src0_sel:DWORD src1_sel:WORD_1
v_mfma_f32_16x16x32_bf16 v[60:63],a[200:203],a[92:95],v[16:19]
ds_read_u16_d16_hi v54,v54
ds_read_u16 v53,v53
v_fma_mix_f32 v57,v57,v9,0 op_sel_hi:[0,1,0]
v_mfma_f32_16x16x32_bf16 v[60:63],a[196:199],a[76:79],v[60:63]
ds_read_u16_d16_hi v55,v55
v_fma_mix_f32 v59,v59,v9,0 op_sel:[0,1,0] op_sel_hi:[0,1,0]
v_mfma_f32_16x16x32_bf16 v[60:63],a[192:195],a[60:63],v[60:63]
s_barrier
ds_read_u16 v56,v56
ds_read_u16_d16_hi v58,v58
v_mfma_f32_16x16x32_bf16 v[24:27],a[192:195],a[0:3],v[16:19]
s_add_u32 m0,s57,24752
v_lshlrev_b32_sdwa v44,1,v44 dst_sel:DWORD dst_unused:UNUSED_PAD src0_sel:DWORD src1_sel:WORD_1
v_fma_mixlo_f16 v8,v33,s99,0
v_mfma_f32_16x16x32_bf16 v[24:27],a[196:199],a[16:19],v[24:27]
s_add_u32 s89,s56,5603328
ds_read_u16 v44,v44
v_lshlrev_b32_sdwa v46,1,v46 dst_sel:DWORD dst_unused:UNUSED_PAD src0_sel:DWORD src1_sel:WORD_1
v_mfma_f32_16x16x32_bf16 v[24:27],a[200:203],a[32:35],v[24:27]
buffer_load_dwordx4 v6,s[36:39],s89 offen lds
v_lshl_add_u64 v[36:37],v[36:37],0,v[38:39]
v_mfma_f32_16x16x32_bf16 v[28:31],a[204:207],a[32:35],v[20:23]
s_waitcnt lgkmcnt(7)
ds_read_u16_d16_hi v46,v46
v_lshl_add_u64 v[38:39],v[40:41],0,v[42:43]
v_fma_mixhi_f16 v8,v35,s99,0
v_mfma_f32_16x16x32_bf16 v[28:31],a[208:211],a[16:19],v[28:31]
s_waitcnt lgkmcnt(4)
v_lshl_add_u64 v[40:41],v[52:53],0,v[54:55]
v_lshlrev_b32_sdwa v45,1,v45 dst_sel:DWORD dst_unused:UNUSED_PAD src0_sel:DWORD src1_sel:WORD_1
v_pk_fma_f16 v9,v8,v8,s54
v_mfma_f32_16x16x32_bf16 v[28:31],a[212:215],a[0:3],v[28:31]
ds_read_u16 v45,v45
v_lshlrev_b32_sdwa v47,1,v47 dst_sel:DWORD dst_unused:UNUSED_PAD src0_sel:DWORD src1_sel:WORD_1
v_mfma_f32_16x16x32_bf16 v[52:55],a[212:215],a[4:7],v[20:23]
s_waitcnt lgkmcnt(3)
ds_read_u16_d16_hi v47,v47
v_or_b32 v42,v56,v58
v_pk_mul_f16 v9,v8,v9
v_mfma_f32_16x16x32_bf16 v[52:55],a[208:211],a[20:23],v[52:55]
v_cvt_pk_bf16_f32 v43,v57,v59
v_lshlrev_b32_sdwa v48,1,v48 dst_sel:DWORD dst_unused:UNUSED_PAD src0_sel:DWORD src1_sel:WORD_1
v_mfma_f32_16x16x32_bf16 v[52:55],a[204:207],a[36:39],v[52:55]
v_exp_f16 v8,v9
v_mfma_f32_16x16x32_bf16 v[56:59],a[200:203],a[36:39],v[16:19]
ds_read_u16 v48,v48
v_lshlrev_b32_sdwa v50,1,v50 dst_sel:DWORD dst_unused:UNUSED_PAD src0_sel:DWORD src1_sel:WORD_1
v_mfma_f32_16x16x32_bf16 v[56:59],a[196:199],a[20:23],v[56:59]
ds_read_u16_d16_hi v50,v50
v_lshlrev_b32_sdwa v49,1,v49 dst_sel:DWORD dst_unused:UNUSED_PAD src0_sel:DWORD src1_sel:WORD_1
v_mfma_f32_16x16x32_bf16 v[56:59],a[192:195],a[4:7],v[56:59]
v_exp_f16_sdwa v8,v9 dst_sel:WORD_1 dst_unused:UNUSED_PRESERVE src0_sel:WORD_1
v_mfma_f32_16x16x32_bf16 v[232:235],a[228:231],v[36:39],v[232:235]
ds_read_u16 v49,v49
v_lshlrev_b32_sdwa v51,1,v51 dst_sel:DWORD dst_unused:UNUSED_PAD src0_sel:DWORD src1_sel:WORD_1
v_mfma_f32_16x16x32_bf16 v[236:239],a[228:231],v[40:43],v[236:239]
ds_read_u16_d16_hi v51,v51
v_lshlrev_b32_sdwa v60,1,v60 dst_sel:DWORD dst_unused:UNUSED_PAD src0_sel:DWORD src1_sel:WORD_1
v_mfma_f32_16x16x32_bf16 v[164:167],a[248:251],v[40:43],v[164:167]
ds_read_u16 v60,v60
v_pk_fma_f16 v8,v8,s55,s55
v_mfma_f32_16x16x32_bf16 v[160:163],a[248:251],v[36:39],v[160:163]
v_rcp_f16 v9,v8
v_lshlrev_b32_sdwa v62,1,v62 dst_sel:DWORD dst_unused:UNUSED_PAD src0_sel:DWORD src1_sel:WORD_1
v_mfma_f32_16x16x32_bf16 v[240:243],a[236:239],v[36:39],v[240:243]
ds_read_u16_d16_hi v62,v62
v_lshlrev_b32_sdwa v61,1,v61 dst_sel:DWORD dst_unused:UNUSED_PAD src0_sel:DWORD src1_sel:WORD_1
v_mfma_f32_16x16x32_bf16 v[244:247],a[236:239],v[40:43],v[244:247]
ds_read_u16 v61,v61
v_lshlrev_b32_sdwa v63,1,v63 dst_sel:DWORD dst_unused:UNUSED_PAD src0_sel:DWORD src1_sel:WORD_1
v_mfma_f32_16x16x32_bf16 v[180:183],a[252:255],v[40:43],v[180:183]
ds_read_u16_d16_hi v63,v63
v_lshlrev_b32_sdwa v32,1,v32 dst_sel:DWORD dst_unused:UNUSED_PAD src0_sel:DWORD src1_sel:WORD_1
v_mfma_f32_16x16x32_bf16 v[176:179],a[252:255],v[36:39],v[176:179]
v_rcp_f16_sdwa v9,v8 dst_sel:WORD_1 dst_unused:UNUSED_PRESERVE src0_sel:WORD_1
v_mfma_f32_16x16x32_bf16 v[248:251],a[240:243],v[36:39],v[248:251]
ds_read_u16 v32,v32
v_lshlrev_b32_sdwa v34,1,v34 dst_sel:DWORD dst_unused:UNUSED_PAD src0_sel:DWORD src1_sel:WORD_1
v_mfma_f32_16x16x32_bf16 v[252:255],a[240:243],v[40:43],v[252:255]
ds_read_u16_d16_hi v34,v34
v_fma_mix_f32 v33,v33,v9,0 op_sel_hi:[0,1,0]
v_mfma_f32_16x16x32_bf16 v[196:199],a[224:227],v[40:43],v[196:199]
v_fma_mix_f32 v35,v35,v9,0 op_sel:[0,1,0] op_sel_hi:[0,1,0]
v_mfma_f32_16x16x32_bf16 v[192:195],a[224:227],v[36:39],v[192:195]
v_mfma_f32_16x16x32_bf16 v[40:43],a[192:195],a[8:11],v[16:19]
v_lshlrev_b32_sdwa v24,1,v24 dst_sel:DWORD dst_unused:UNUSED_PAD src0_sel:DWORD src1_sel:WORD_1
v_fma_mixlo_f16 v8,v53,s99,0
v_mfma_f32_16x16x32_bf16 v[40:43],a[196:199],a[24:27],v[40:43]
ds_read_u16 v24,v24
v_lshlrev_b32_sdwa v26,1,v26 dst_sel:DWORD dst_unused:UNUSED_PAD src0_sel:DWORD src1_sel:WORD_1
v_mfma_f32_16x16x32_bf16 v[40:43],a[200:203],a[40:43],v[40:43]
s_waitcnt lgkmcnt(4)
ds_read_u16_d16_hi v26,v26
v_lshl_add_u64 v[44:45],v[44:45],0,v[46:47]
v_mfma_f32_16x16x32_bf16 v[36:39],a[204:207],a[40:43],v[20:23]
v_lshl_add_u64 v[46:47],v[48:49],0,v[50:51]
v_fma_mixhi_f16 v8,v55,s99,0
v_mfma_f32_16x16x32_bf16 v[36:39],a[208:211],a[24:27],v[36:39]
s_waitcnt lgkmcnt(4)
v_lshl_add_u64 v[48:49],v[60:61],0,v[62:63]
v_lshlrev_b32_sdwa v25,1,v25 dst_sel:DWORD dst_unused:UNUSED_PAD src0_sel:DWORD src1_sel:WORD_1
v_pk_fma_f16 v9,v8,v8,s54
v_mfma_f32_16x16x32_bf16 v[36:39],a[212:215],a[8:11],v[36:39]
ds_read_u16 v25,v25
v_lshlrev_b32_sdwa v27,1,v27 dst_sel:DWORD dst_unused:UNUSED_PAD src0_sel:DWORD src1_sel:WORD_1
v_mfma_f32_16x16x32_bf16 v[60:63],a[212:215],a[12:15],v[20:23]
s_waitcnt lgkmcnt(3)
ds_read_b128 v[20:23],v4 offset:128
ds_read_b64_tr_b16 a[212:213],v1 offset:4096
v_or_b32 v50,v32,v34
v_pk_mul_f16 v9,v8,v9
v_mfma_f32_16x16x32_bf16 v[60:63],a[208:211],a[28:31],v[60:63]
ds_read_b64_tr_b16 a[214:215],v1 offset:5120
ds_read_b64_tr_b16 a[208:209],v2 offset:4096
v_cvt_pk_bf16_f32 v51,v33,v35
v_lshlrev_b32_sdwa v28,1,v28 dst_sel:DWORD dst_unused:UNUSED_PAD src0_sel:DWORD src1_sel:WORD_1
v_mfma_f32_16x16x32_bf16 v[60:63],a[204:207],a[44:47],v[60:63]
ds_read_b64_tr_b16 a[210:211],v2 offset:5120
ds_read_b64_tr_b16 a[204:205],v2 offset:2048
v_exp_f16 v8,v9
v_mfma_f32_16x16x32_bf16 v[32:35],a[200:203],a[44:47],v[16:19]
TP6024_%=:
s_cbranch_execz TR0_%=
ds_read_b128 v[16:19],v4 offset:160
ds_read_b64_tr_b16 a[206:207],v2 offset:3072
v_lshlrev_b32_sdwa v30,1,v30 dst_sel:DWORD dst_unused:UNUSED_PAD src0_sel:DWORD src1_sel:WORD_1
v_mfma_f32_16x16x32_bf16 v[32:35],a[196:199],a[28:31],v[32:35]
ds_read_b64_tr_b16 a[200:201],v2 offset:0
ds_read_b64_tr_b16 a[202:203],v2 offset:1024
v_lshlrev_b32_sdwa v29,1,v29 dst_sel:DWORD dst_unused:UNUSED_PAD src0_sel:DWORD src1_sel:WORD_1
v_mfma_f32_16x16x32_bf16 v[32:35],a[192:195],a[12:15],v[32:35]
v_exp_f16_sdwa v8,v9 dst_sel:WORD_1 dst_unused:UNUSED_PRESERVE src0_sel:WORD_1
v_mfma_f32_16x16x32_bf16 v[208:211],a[228:231],v[44:47],v[208:211]
ds_read_u16_d16_hi v27,v27
v_lshlrev_b32_sdwa v31,1,v31 dst_sel:DWORD dst_unused:UNUSED_PAD src0_sel:DWORD src1_sel:WORD_1
v_mfma_f32_16x16x32_bf16 v[212:215],a[228:231],v[48:51],v[212:215]
ds_read_u16 v28,v28
v_lshlrev_b32_sdwa v56,1,v56 dst_sel:DWORD dst_unused:UNUSED_PAD src0_sel:DWORD src1_sel:WORD_1
v_mfma_f32_16x16x32_bf16 v[172:175],a[248:251],v[48:51],v[172:175]
ds_read_u16_d16_hi v30,v30
v_pk_fma_f16 v8,v8,s55,s55
v_mfma_f32_16x16x32_bf16 v[168:171],a[248:251],v[44:47],v[168:171]
v_rcp_f16 v9,v8
v_lshlrev_b32_sdwa v58,1,v58 dst_sel:DWORD dst_unused:UNUSED_PAD src0_sel:DWORD src1_sel:WORD_1
v_mfma_f32_16x16x32_bf16 v[216:219],a[236:239],v[44:47],v[216:219]
ds_read_u16 v29,v29
v_lshlrev_b32_sdwa v57,1,v57 dst_sel:DWORD dst_unused:UNUSED_PAD src0_sel:DWORD src1_sel:WORD_1
v_mfma_f32_16x16x32_bf16 v[220:223],a[236:239],v[48:51],v[220:223]
ds_read_u16_d16_hi v31,v31
v_lshlrev_b32_sdwa v59,1,v59 dst_sel:DWORD dst_unused:UNUSED_PAD src0_sel:DWORD src1_sel:WORD_1
v_mfma_f32_16x16x32_bf16 v[188:191],a[252:255],v[48:51],v[188:191]
ds_read_u16 v56,v56
v_lshlrev_b32_sdwa v52,1,v52 dst_sel:DWORD dst_unused:UNUSED_PAD src0_sel:DWORD src1_sel:WORD_1
v_mfma_f32_16x16x32_bf16 v[184:187],a[252:255],v[44:47],v[184:187]
v_rcp_f16_sdwa v9,v8 dst_sel:WORD_1 dst_unused:UNUSED_PRESERVE src0_sel:WORD_1
v_mfma_f32_16x16x32_bf16 v[224:227],a[240:243],v[44:47],v[224:227]
ds_read_u16_d16_hi v58,v58
ds_read_u16 v57,v57
v_mfma_f32_16x16x32_bf16 v[228:231],a[240:243],v[48:51],v[228:231]
ds_read_u16_d16_hi v59,v59
v_lshlrev_b32_sdwa v54,1,v54 dst_sel:DWORD dst_unused:UNUSED_PAD src0_sel:DWORD src1_sel:WORD_1
v_mfma_f32_16x16x32_bf16 v[204:207],a[224:227],v[48:51],v[204:207]
ds_read_u16 v52,v52
v_fma_mix_f32 v53,v53,v9,0 op_sel_hi:[0,1,0]
v_mfma_f32_16x16x32_bf16 v[200:203],a[224:227],v[44:47],v[200:203]
s_waitcnt lgkmcnt(7)
ds_read_u16_d16_hi v54,v54
v_fma_mix_f32 v55,v55,v9,0 op_sel:[0,1,0] op_sel_hi:[0,1,0]
v_mfma_f32_16x16x32_bf16 v[48:51],a[216:219],a[48:51],v[20:23]
s_add_u32 m0,s57,28296
v_lshlrev_b32_sdwa v40,1,v40 dst_sel:DWORD dst_unused:UNUSED_PAD src0_sel:DWORD src1_sel:WORD_1
v_fma_mixlo_f16 v8,v61,s99,0
v_mfma_f32_16x16x32_bf16 v[48:51],a[220:223],a[64:67],v[48:51]
s_add_u32 s89,s56,5701632
ds_read_u16 v40,v40
v_lshlrev_b32_sdwa v42,1,v42 dst_sel:DWORD dst_unused:UNUSED_PAD src0_sel:DWORD src1_sel:WORD_1
v_mfma_f32_16x16x32_bf16 v[48:51],a[212:215],a[80:83],v[48:51]
buffer_load_dwordx4 v6,s[36:39],s89 offen lds
v_lshl_add_u64 v[24:25],v[24:25],0,v[26:27]
v_mfma_f32_16x16x32_bf16 v[44:47],a[208:211],a[80:83],v[16:19]
s_waitcnt lgkmcnt(6)
ds_read_u16_d16_hi v42,v42
v_lshl_add_u64 v[26:27],v[28:29],0,v[30:31]
v_fma_mixhi_f16 v8,v63,s99,0
v_mfma_f32_16x16x32_bf16 v[44:47],a[204:207],a[64:67],v[44:47]
s_waitcnt lgkmcnt(4)
v_lshl_add_u64 v[28:29],v[56:57],0,v[58:59]
v_lshlrev_b32_sdwa v41,1,v41 dst_sel:DWORD dst_unused:UNUSED_PAD src0_sel:DWORD src1_sel:WORD_1
v_pk_fma_f16 v9,v8,v8,s54
v_mfma_f32_16x16x32_bf16 v[44:47],a[200:203],a[48:51],v[44:47]
ds_read_u16 v41,v41
v_lshlrev_b32_sdwa v43,1,v43 dst_sel:DWORD dst_unused:UNUSED_PAD src0_sel:DWORD src1_sel:WORD_1
v_mfma_f32_16x16x32_bf16 v[56:59],a[200:203],a[52:55],v[16:19]
s_waitcnt lgkmcnt(3)
ds_read_u16_d16_hi v43,v43
v_or_b32 v30,v52,v54
v_pk_mul_f16 v9,v8,v9
v_mfma_f32_16x16x32_bf16 v[56:59],a[204:207],a[68:71],v[56:59]
s_waitcnt vmcnt(21)
v_cvt_pk_bf16_f32 v31,v53,v55
v_lshlrev_b32_sdwa v36,1,v36 dst_sel:DWORD dst_unused:UNUSED_PAD src0_sel:DWORD src1_sel:WORD_1
v_mfma_f32_16x16x32_bf16 v[56:59],a[208:211],a[84:87],v[56:59]
s_barrier
v_exp_f16 v8,v9
v_mfma_f32_16x16x32_bf16 v[52:55],a[212:215],a[84:87],v[20:23]
ds_read_u16 v36,v36
v_lshlrev_b32_sdwa v38,1,v38 dst_sel:DWORD dst_unused:UNUSED_PAD src0_sel:DWORD src1_sel:WORD_1
v_mfma_f32_16x16x32_bf16 v[52:55],a[220:223],a[68:71],v[52:55]
s_add_u32 m0,s58,0
ds_read_u16_d16_hi v38,v38
v_lshlrev_b32_sdwa v37,1,v37 dst_sel:DWORD dst_unused:UNUSED_PAD src0_sel:DWORD src1_sel:WORD_1
v_mfma_f32_16x16x32_bf16 v[52:55],a[216:219],a[52:55],v[52:55]
s_add_u32 s69,s69,s59
v_exp_f16_sdwa v8,v9 dst_sel:WORD_1 dst_unused:UNUSED_PRESERVE src0_sel:WORD_1
v_mfma_f32_16x16x32_bf16 v[136:139],a[228:231],v[24:27],v[136:139]
buffer_load_dwordx4 v5,s[44:47],s69 offen offset:0 lds
v_lshlrev_b32_sdwa v39,1,v39 dst_sel:DWORD dst_unused:UNUSED_PAD src0_sel:DWORD src1_sel:WORD_1
v_mfma_f32_16x16x32_bf16 v[140:143],a[228:231],v[28:31],v[140:143]
ds_read_u16 v37,v37
v_lshlrev_b32_sdwa v32,1,v32 dst_sel:DWORD dst_unused:UNUSED_PAD src0_sel:DWORD src1_sel:WORD_1
v_mfma_f32_16x16x32_bf16 v[68:71],a[248:251],v[28:31],v[68:71]
ds_read_u16_d16_hi v39,v39
v_pk_fma_f16 v8,v8,s55,s55
v_mfma_f32_16x16x32_bf16 v[64:67],a[248:251],v[24:27],v[64:67]
v_rcp_f16 v9,v8
v_lshlrev_b32_sdwa v34,1,v34 dst_sel:DWORD dst_unused:UNUSED_PAD src0_sel:DWORD src1_sel:WORD_1
v_mfma_f32_16x16x32_bf16 v[144:147],a[236:239],v[24:27],v[144:147]
s_add_u32 s69,s69,s53
ds_read_u16 v32,v32
v_lshlrev_b32_sdwa v33,1,v33 dst_sel:DWORD dst_unused:UNUSED_PAD src0_sel:DWORD src1_sel:WORD_1
v_mfma_f32_16x16x32_bf16 v[148:151],a[236:239],v[28:31],v[148:151]
buffer_load_dwordx4 v5,s[44:47],s69 offen offset:1024 lds
v_lshlrev_b32_sdwa v35,1,v35 dst_sel:DWORD dst_unused:UNUSED_PAD src0_sel:DWORD src1_sel:WORD_1
v_mfma_f32_16x16x32_bf16 v[84:87],a[252:255],v[28:31],v[84:87]
ds_read_u16_d16_hi v34,v34
v_lshlrev_b32_sdwa v60,1,v60 dst_sel:DWORD dst_unused:UNUSED_PAD src0_sel:DWORD src1_sel:WORD_1
v_mfma_f32_16x16x32_bf16 v[80:83],a[252:255],v[24:27],v[80:83]
v_rcp_f16_sdwa v9,v8 dst_sel:WORD_1 dst_unused:UNUSED_PRESERVE src0_sel:WORD_1
v_mfma_f32_16x16x32_bf16 v[152:155],a[240:243],v[24:27],v[152:155]
ds_read_u16 v33,v33
ds_read_u16_d16_hi v35,v35
v_mfma_f32_16x16x32_bf16 v[156:159],a[240:243],v[28:31],v[156:159]
s_add_u32 s69,s69,s53
ds_read_u16 v60,v60
v_lshlrev_b32_sdwa v62,1,v62 dst_sel:DWORD dst_unused:UNUSED_PAD src0_sel:DWORD src1_sel:WORD_1
v_mfma_f32_16x16x32_bf16 v[100:103],a[224:227],v[28:31],v[100:103]
buffer_load_dwordx4 v5,s[44:47],s69 offen offset:2048 lds
v_fma_mix_f32 v61,v61,v9,0 op_sel_hi:[0,1,0]
v_mfma_f32_16x16x32_bf16 v[96:99],a[224:227],v[24:27],v[96:99]
ds_read_u16_d16_hi v62,v62
v_fma_mix_f32 v63,v63,v9,0 op_sel:[0,1,0] op_sel_hi:[0,1,0]
v_mfma_f32_16x16x32_bf16 v[28:31],a[216:219],a[56:59],v[20:23]
ds_read_b64_tr_b16 a[232:233],v3 offset:6160
v_lshlrev_b32_sdwa v48,1,v48 dst_sel:DWORD dst_unused:UNUSED_PAD src0_sel:DWORD src1_sel:WORD_1
v_mfma_f32_16x16x32_bf16 v[28:31],a[220:223],a[72:75],v[28:31]
ds_read_u16 v48,v48
v_fma_mixlo_f16 v8,v57,s99,0
v_mfma_f32_16x16x32_bf16 v[28:31],a[212:215],a[88:91],v[28:31]
s_waitcnt lgkmcnt(7)
ds_read_b64_tr_b16 a[234:235],v3 offset:7696
v_lshl_add_u64 v[40:41],v[40:41],0,v[42:43]
v_lshlrev_b32_sdwa v50,1,v50 dst_sel:DWORD dst_unused:UNUSED_PAD src0_sel:DWORD src1_sel:WORD_1
v_mfma_f32_16x16x32_bf16 v[24:27],a[208:211],a[88:91],v[16:19]
ds_read_u16_d16_hi v50,v50
v_lshl_add_u64 v[42:43],v[36:37],0,v[38:39]
v_fma_mixhi_f16 v8,v59,s99,0
v_mfma_f32_16x16x32_bf16 v[24:27],a[204:207],a[72:75],v[24:27]
s_waitcnt lgkmcnt(6)
ds_read_b64_tr_b16 a[244:245],v3 offset:6168
v_lshl_add_u64 v[36:37],v[32:33],0,v[34:35]
v_lshlrev_b32_sdwa v49,1,v49 dst_sel:DWORD dst_unused:UNUSED_PAD src0_sel:DWORD src1_sel:WORD_1
v_pk_fma_f16 v9,v8,v8,s54
v_mfma_f32_16x16x32_bf16 v[24:27],a[200:203],a[56:59],v[24:27]
ds_read_u16 v49,v49
v_lshlrev_b32_sdwa v51,1,v51 dst_sel:DWORD dst_unused:UNUSED_PAD src0_sel:DWORD src1_sel:WORD_1
v_mfma_f32_16x16x32_bf16 v[32:35],a[200:203],a[60:63],v[16:19]
s_waitcnt lgkmcnt(6)
ds_read_u16_d16_hi v51,v51
v_or_b32 v38,v60,v62
v_pk_mul_f16 v9,v8,v9
v_mfma_f32_16x16x32_bf16 v[32:35],a[204:207],a[76:79],v[32:35]
ds_read_b64_tr_b16 a[246:247],v3 offset:7704
v_cvt_pk_bf16_f32 v39,v61,v63
v_lshlrev_b32_sdwa v44,1,v44 dst_sel:DWORD dst_unused:UNUSED_PAD src0_sel:DWORD src1_sel:WORD_1
v_mfma_f32_16x16x32_bf16 v[32:35],a[208:211],a[92:95],v[32:35]
v_exp_f16 v8,v9
v_mfma_f32_16x16x32_bf16 v[60:63],a[212:215],a[92:95],v[20:23]
ds_read_u16 v44,v44
v_lshlrev_b32_sdwa v46,1,v46 dst_sel:DWORD dst_unused:UNUSED_PAD src0_sel:DWORD src1_sel:WORD_1
v_mfma_f32_16x16x32_bf16 v[60:63],a[220:223],a[76:79],v[60:63]
ds_read_u16_d16_hi v46,v46
v_lshlrev_b32_sdwa v45,1,v45 dst_sel:DWORD dst_unused:UNUSED_PAD src0_sel:DWORD src1_sel:WORD_1
v_mfma_f32_16x16x32_bf16 v[60:63],a[216:219],a[60:63],v[60:63]
v_exp_f16_sdwa v8,v9 dst_sel:WORD_1 dst_unused:UNUSED_PRESERVE src0_sel:WORD_1
v_mfma_f32_16x16x32_bf16 v[112:115],a[228:231],v[40:43],v[112:115]
ds_read_u16 v45,v45
v_lshlrev_b32_sdwa v47,1,v47 dst_sel:DWORD dst_unused:UNUSED_PAD src0_sel:DWORD src1_sel:WORD_1
v_mfma_f32_16x16x32_bf16 v[116:119],a[228:231],v[36:39],v[116:119]
ds_read_u16_d16_hi v47,v47
v_lshlrev_b32_sdwa v52,1,v52 dst_sel:DWORD dst_unused:UNUSED_PAD src0_sel:DWORD src1_sel:WORD_1
v_mfma_f32_16x16x32_bf16 v[76:79],a[248:251],v[36:39],v[76:79]
ds_read_u16 v52,v52
v_pk_fma_f16 v8,v8,s55,s55
v_mfma_f32_16x16x32_bf16 v[72:75],a[248:251],v[40:43],v[72:75]
v_rcp_f16 v9,v8
v_lshlrev_b32_sdwa v54,1,v54 dst_sel:DWORD dst_unused:UNUSED_PAD src0_sel:DWORD src1_sel:WORD_1
v_mfma_f32_16x16x32_bf16 v[120:123],a[236:239],v[40:43],v[120:123]
ds_read_u16_d16_hi v54,v54
v_lshlrev_b32_sdwa v53,1,v53 dst_sel:DWORD dst_unused:UNUSED_PAD src0_sel:DWORD src1_sel:WORD_1
v_mfma_f32_16x16x32_bf16 v[124:127],a[236:239],v[36:39],v[124:127]
ds_read_u16 v53,v53
ds_read_b64_tr_b16 a[228:229],v3 offset:6224
v_mfma_f32_16x16x32_bf16 v[92:95],a[252:255],v[36:39],v[92:95]
ds_read_b64_tr_b16 a[230:231],v3 offset:7760
v_lshlrev_b32_sdwa v55,1,v55 dst_sel:DWORD dst_unused:UNUSED_PAD src0_sel:DWORD src1_sel:WORD_1
v_mfma_f32_16x16x32_bf16 v[88:91],a[252:255],v[40:43],v[88:91]
ds_read_u16_d16_hi v55,v55
v_rcp_f16_sdwa v9,v8 dst_sel:WORD_1 dst_unused:UNUSED_PRESERVE src0_sel:WORD_1
v_mfma_f32_16x16x32_bf16 v[128:131],a[240:243],v[40:43],v[128:131]
ds_read_b64_tr_b16 a[248:249],v3 offset:6232
v_lshlrev_b32_sdwa v56,1,v56 dst_sel:DWORD dst_unused:UNUSED_PAD src0_sel:DWORD src1_sel:WORD_1
v_mfma_f32_16x16x32_bf16 v[132:135],a[240:243],v[36:39],v[132:135]
ds_read_u16 v56,v56
v_lshlrev_b32_sdwa v58,1,v58 dst_sel:DWORD dst_unused:UNUSED_PAD src0_sel:DWORD src1_sel:WORD_1
v_mfma_f32_16x16x32_bf16 v[108:111],a[224:227],v[36:39],v[108:111]
ds_read_u16_d16_hi v58,v58
v_fma_mix_f32 v57,v57,v9,0 op_sel_hi:[0,1,0]
v_mfma_f32_16x16x32_bf16 v[104:107],a[224:227],v[40:43],v[104:107]
ds_read_b64_tr_b16 a[250:251],v3 offset:7768
v_fma_mix_f32 v59,v59,v9,0 op_sel:[0,1,0] op_sel_hi:[0,1,0]
v_mfma_f32_16x16x32_bf16 v[36:39],a[216:219],a[0:3],v[20:23]
s_add_u32 m0,s57,31832
ds_read_b64_tr_b16 a[236:237],v3 offset:6288
v_lshlrev_b32_sdwa v28,1,v28 dst_sel:DWORD dst_unused:UNUSED_PAD src0_sel:DWORD src1_sel:WORD_1
v_mfma_f32_16x16x32_bf16 v[36:39],a[220:223],a[16:19],v[36:39]
s_add_u32 s89,s56,5799936
ds_read_b64_tr_b16 a[238:239],v3 offset:7824
v_fma_mixlo_f16 v8,v33,s99,0
v_mfma_f32_16x16x32_bf16 v[36:39],a[212:215],a[32:35],v[36:39]
s_waitcnt lgkmcnt(8)
buffer_load_dwordx4 v6,s[36:39],s89 offen lds
v_lshl_add_u64 v[48:49],v[48:49],0,v[50:51]
v_lshlrev_b32_sdwa v30,1,v30 dst_sel:DWORD dst_unused:UNUSED_PAD src0_sel:DWORD src1_sel:WORD_1
v_mfma_f32_16x16x32_bf16 v[40:43],a[208:211],a[32:35],v[16:19]
ds_read_b64_tr_b16 a[252:253],v3 offset:6296
v_lshl_add_u64 v[50:51],v[44:45],0,v[46:47]
v_fma_mixhi_f16 v8,v35,s99,0
v_mfma_f32_16x16x32_bf16 v[40:43],a[204:207],a[16:19],v[40:43]
s_waitcnt lgkmcnt(7)
ds_read_b64_tr_b16 a[254:255],v3 offset:7832
v_lshl_add_u64 v[44:45],v[52:53],0,v[54:55]
v_lshlrev_b32_sdwa v29,1,v29 dst_sel:DWORD dst_unused:UNUSED_PAD src0_sel:DWORD src1_sel:WORD_1
v_pk_fma_f16 v9,v8,v8,s54
v_mfma_f32_16x16x32_bf16 v[40:43],a[200:203],a[0:3],v[40:43]
ds_read_u16 v28,v28
v_lshlrev_b32_sdwa v31,1,v31 dst_sel:DWORD dst_unused:UNUSED_PAD src0_sel:DWORD src1_sel:WORD_1
v_mfma_f32_16x16x32_bf16 v[52:55],a[200:203],a[4:7],v[16:19]
s_waitcnt lgkmcnt(6)
ds_read_u16_d16_hi v30,v30
v_or_b32 v46,v56,v58
v_pk_mul_f16 v9,v8,v9
v_mfma_f32_16x16x32_bf16 v[52:55],a[204:207],a[20:23],v[52:55]
ds_read_u16 v29,v29
v_cvt_pk_bf16_f32 v47,v57,v59
v_lshlrev_b32_sdwa v24,1,v24 dst_sel:DWORD dst_unused:UNUSED_PAD src0_sel:DWORD src1_sel:WORD_1
v_mfma_f32_16x16x32_bf16 v[52:55],a[208:211],a[36:39],v[52:55]
v_exp_f16 v8,v9
v_mfma_f32_16x16x32_bf16 v[56:59],a[212:215],a[36:39],v[20:23]
ds_read_u16_d16_hi v31,v31
v_lshlrev_b32_sdwa v26,1,v26 dst_sel:DWORD dst_unused:UNUSED_PAD src0_sel:DWORD src1_sel:WORD_1
v_mfma_f32_16x16x32_bf16 v[56:59],a[220:223],a[20:23],v[56:59]
ds_read_u16 v24,v24
v_lshlrev_b32_sdwa v25,1,v25 dst_sel:DWORD dst_unused:UNUSED_PAD src0_sel:DWORD src1_sel:WORD_1
v_mfma_f32_16x16x32_bf16 v[56:59],a[216:219],a[4:7],v[56:59]
ds_read_u16_d16_hi v26,v26
v_exp_f16_sdwa v8,v9 dst_sel:WORD_1 dst_unused:UNUSED_PRESERVE src0_sel:WORD_1
v_mfma_f32_16x16x32_bf16 v[232:235],a[232:235],v[48:51],v[232:235]
ds_read_u16 v25,v25
v_lshlrev_b32_sdwa v27,1,v27 dst_sel:DWORD dst_unused:UNUSED_PAD src0_sel:DWORD src1_sel:WORD_1
v_mfma_f32_16x16x32_bf16 v[236:239],a[232:235],v[44:47],v[236:239]
ds_read_u16_d16_hi v27,v27
v_lshlrev_b32_sdwa v60,1,v60 dst_sel:DWORD dst_unused:UNUSED_PAD src0_sel:DWORD src1_sel:WORD_1
v_mfma_f32_16x16x32_bf16 v[164:167],a[244:247],v[44:47],v[164:167]
ds_read_u16 v60,v60
v_pk_fma_f16 v8,v8,s55,s55
v_mfma_f32_16x16x32_bf16 v[160:163],a[244:247],v[48:51],v[160:163]
v_rcp_f16 v9,v8
v_lshlrev_b32_sdwa v62,1,v62 dst_sel:DWORD dst_unused:UNUSED_PAD src0_sel:DWORD src1_sel:WORD_1
v_mfma_f32_16x16x32_bf16 v[240:243],a[228:231],v[48:51],v[240:243]
ds_read_u16_d16_hi v62,v62
v_lshlrev_b32_sdwa v61,1,v61 dst_sel:DWORD dst_unused:UNUSED_PAD src0_sel:DWORD src1_sel:WORD_1
v_mfma_f32_16x16x32_bf16 v[244:247],a[228:231],v[44:47],v[244:247]
s_waitcnt lgkmcnt(7)
ds_read_u16 v61,v61
v_lshlrev_b32_sdwa v63,1,v63 dst_sel:DWORD dst_unused:UNUSED_PAD src0_sel:DWORD src1_sel:WORD_1
v_mfma_f32_16x16x32_bf16 v[180:183],a[248:251],v[44:47],v[180:183]
ds_read_u16_d16_hi v63,v63
v_lshlrev_b32_sdwa v32,1,v32 dst_sel:DWORD dst_unused:UNUSED_PAD src0_sel:DWORD src1_sel:WORD_1
v_mfma_f32_16x16x32_bf16 v[176:179],a[248:251],v[48:51],v[176:179]
v_rcp_f16_sdwa v9,v8 dst_sel:WORD_1 dst_unused:UNUSED_PRESERVE src0_sel:WORD_1
v_mfma_f32_16x16x32_bf16 v[248:251],a[236:239],v[48:51],v[248:251]
ds_read_u16 v32,v32
v_lshlrev_b32_sdwa v34,1,v34 dst_sel:DWORD dst_unused:UNUSED_PAD src0_sel:DWORD src1_sel:WORD_1
v_mfma_f32_16x16x32_bf16 v[252:255],a[236:239],v[44:47],v[252:255]
ds_read_u16_d16_hi v34,v34
v_fma_mix_f32 v33,v33,v9,0 op_sel_hi:[0,1,0]
v_mfma_f32_16x16x32_bf16 v[196:199],a[252:255],v[44:47],v[196:199]
ds_read_b64_tr_b16 a[196:197],v1 offset:6160
v_fma_mix_f32 v35,v35,v9,0 op_sel:[0,1,0] op_sel_hi:[0,1,0]
v_mfma_f32_16x16x32_bf16 v[192:195],a[252:255],v[48:51],v[192:195]
ds_read_b64_tr_b16 a[198:199],v1 offset:7184
v_mfma_f32_16x16x32_bf16 v[44:47],a[216:219],a[8:11],v[20:23]
ds_read_b64_tr_b16 a[192:193],v1 offset:8208
v_lshlrev_b32_sdwa v36,1,v36 dst_sel:DWORD dst_unused:UNUSED_PAD src0_sel:DWORD src1_sel:WORD_1
v_mfma_f32_16x16x32_bf16 v[44:47],a[220:223],a[24:27],v[44:47]
ds_read_b64_tr_b16 a[194:195],v1 offset:9232
v_fma_mixlo_f16 v8,v53,s99,0
v_mfma_f32_16x16x32_bf16 v[44:47],a[212:215],a[40:43],v[44:47]
s_waitcnt lgkmcnt(7)
ds_read_u16 v36,v36
v_lshl_add_u64 v[28:29],v[28:29],0,v[30:31]
v_lshlrev_b32_sdwa v38,1,v38 dst_sel:DWORD dst_unused:UNUSED_PAD src0_sel:DWORD src1_sel:WORD_1
v_mfma_f32_16x16x32_bf16 v[48:51],a[208:211],a[40:43],v[16:19]
ds_read_u16_d16_hi v38,v38
v_lshl_add_u64 v[30:31],v[24:25],0,v[26:27]
v_fma_mixhi_f16 v8,v55,s99,0
v_mfma_f32_16x16x32_bf16 v[48:51],a[204:207],a[24:27],v[48:51]
s_waitcnt lgkmcnt(8)
v_lshl_add_u64 v[24:25],v[60:61],0,v[62:63]
v_lshlrev_b32_sdwa v37,1,v37 dst_sel:DWORD dst_unused:UNUSED_PAD src0_sel:DWORD src1_sel:WORD_1
v_pk_fma_f16 v9,v8,v8,s54
v_mfma_f32_16x16x32_bf16 v[48:51],a[200:203],a[8:11],v[48:51]
ds_read_u16 v37,v37
v_lshlrev_b32_sdwa v39,1,v39 dst_sel:DWORD dst_unused:UNUSED_PAD src0_sel:DWORD src1_sel:WORD_1
v_mfma_f32_16x16x32_bf16 v[60:63],a[200:203],a[12:15],v[16:19]
s_waitcnt lgkmcnt(7)
ds_read_b128 v[16:19],v4 offset:256
v_or_b32 v26,v32,v34
v_pk_mul_f16 v9,v8,v9
v_mfma_f32_16x16x32_bf16 v[60:63],a[204:207],a[28:31],v[60:63]
ds_read_u16_d16_hi v39,v39
v_cvt_pk_bf16_f32 v27,v33,v35
v_lshlrev_b32_sdwa v40,1,v40 dst_sel:DWORD dst_unused:UNUSED_PAD src0_sel:DWORD src1_sel:WORD_1
v_mfma_f32_16x16x32_bf16 v[60:63],a[208:211],a[44:47],v[60:63]
ds_read_b64_tr_b16 a[200:201],v1 offset:10256
v_exp_f16 v8,v9
v_mfma_f32_16x16x32_bf16 v[32:35],a[212:215],a[44:47],v[20:23]
ds_read_b128 v[20:23],v4 offset:288
ds_read_b64_tr_b16 a[202:203],v1 offset:11280
v_lshlrev_b32_sdwa v42,1,v42 dst_sel:DWORD dst_unused:UNUSED_PAD src0_sel:DWORD src1_sel:WORD_1
v_mfma_f32_16x16x32_bf16 v[32:35],a[220:223],a[28:31],v[32:35]
ds_read_b64_tr_b16 a[204:205],v2 offset:10256
ds_read_b64_tr_b16 a[206:207],v2 offset:11280
v_lshlrev_b32_sdwa v41,1,v41 dst_sel:DWORD dst_unused:UNUSED_PAD src0_sel:DWORD src1_sel:WORD_1
v_mfma_f32_16x16x32_bf16 v[32:35],a[216:219],a[12:15],v[32:35]
ds_read_u16 v40,v40
v_exp_f16_sdwa v8,v9 dst_sel:WORD_1 dst_unused:UNUSED_PRESERVE src0_sel:WORD_1
v_mfma_f32_16x16x32_bf16 v[208:211],a[232:235],v[28:31],v[208:211]
ds_read_u16_d16_hi v42,v42
v_lshlrev_b32_sdwa v43,1,v43 dst_sel:DWORD dst_unused:UNUSED_PAD src0_sel:DWORD src1_sel:WORD_1
v_mfma_f32_16x16x32_bf16 v[212:215],a[232:235],v[24:27],v[212:215]
ds_read_u16 v41,v41
v_lshlrev_b32_sdwa v56,1,v56 dst_sel:DWORD dst_unused:UNUSED_PAD src0_sel:DWORD src1_sel:WORD_1
v_mfma_f32_16x16x32_bf16 v[172:175],a[244:247],v[24:27],v[172:175]
ds_read_u16_d16_hi v43,v43
v_pk_fma_f16 v8,v8,s55,s55
v_mfma_f32_16x16x32_bf16 v[168:171],a[244:247],v[28:31],v[168:171]
v_rcp_f16 v9,v8
v_lshlrev_b32_sdwa v58,1,v58 dst_sel:DWORD dst_unused:UNUSED_PAD src0_sel:DWORD src1_sel:WORD_1
v_mfma_f32_16x16x32_bf16 v[216:219],a[228:231],v[28:31],v[216:219]
ds_read_b64_tr_b16 a[208:209],v2 offset:8208
v_lshlrev_b32_sdwa v57,1,v57 dst_sel:DWORD dst_unused:UNUSED_PAD src0_sel:DWORD src1_sel:WORD_1
v_mfma_f32_16x16x32_bf16 v[220:223],a[228:231],v[24:27],v[220:223]
ds_read_b64_tr_b16 a[210:211],v2 offset:9232
v_lshlrev_b32_sdwa v59,1,v59 dst_sel:DWORD dst_unused:UNUSED_PAD src0_sel:DWORD src1_sel:WORD_1
v_mfma_f32_16x16x32_bf16 v[188:191],a[248:251],v[24:27],v[188:191]
ds_read_u16 v56,v56
v_lshlrev_b32_sdwa v52,1,v52 dst_sel:DWORD dst_unused:UNUSED_PAD src0_sel:DWORD src1_sel:WORD_1
v_mfma_f32_16x16x32_bf16 v[184:187],a[248:251],v[28:31],v[184:187]
ds_read_u16_d16_hi v58,v58
v_rcp_f16_sdwa v9,v8 dst_sel:WORD_1 dst_unused:UNUSED_PRESERVE src0_sel:WORD_1
v_mfma_f32_16x16x32_bf16 v[224:227],a[236:239],v[28:31],v[224:227]
ds_read_u16 v57,v57
ds_read_u16_d16_hi v59,v59
v_mfma_f32_16x16x32_bf16 v[228:231],a[236:239],v[24:27],v[228:231]
ds_read_b64_tr_b16 a[212:213],v2 offset:6160
ds_read_b64_tr_b16 a[214:215],v2 offset:7184
v_lshlrev_b32_sdwa v54,1,v54 dst_sel:DWORD dst_unused:UNUSED_PAD src0_sel:DWORD src1_sel:WORD_1
v_mfma_f32_16x16x32_bf16 v[204:207],a[252:255],v[24:27],v[204:207]
ds_read_u16 v52,v52
v_fma_mix_f32 v53,v53,v9,0 op_sel_hi:[0,1,0]
v_mfma_f32_16x16x32_bf16 v[200:203],a[252:255],v[28:31],v[200:203]
s_waitcnt lgkmcnt(9)
ds_read_u16_d16_hi v54,v54
v_fma_mix_f32 v55,v55,v9,0 op_sel:[0,1,0] op_sel_hi:[0,1,0]
v_mfma_f32_16x16x32_bf16 v[24:27],a[196:199],a[48:51],v[16:19]
s_add_u32 m0,s57,35360
v_lshlrev_b32_sdwa v44,1,v44 dst_sel:DWORD dst_unused:UNUSED_PAD src0_sel:DWORD src1_sel:WORD_1
v_fma_mixlo_f16 v8,v61,s99,0
v_mfma_f32_16x16x32_bf16 v[24:27],a[192:195],a[64:67],v[24:27]
s_add_u32 s89,s56,5898240
ds_read_u16 v44,v44
v_lshlrev_b32_sdwa v46,1,v46 dst_sel:DWORD dst_unused:UNUSED_PAD src0_sel:DWORD src1_sel:WORD_1
v_mfma_f32_16x16x32_bf16 v[24:27],a[200:203],a[80:83],v[24:27]
buffer_load_dwordx4 v6,s[36:39],s89 offen lds
v_lshl_add_u64 v[36:37],v[36:37],0,v[38:39]
v_mfma_f32_16x16x32_bf16 v[28:31],a[204:207],a[80:83],v[20:23]
s_waitcnt lgkmcnt(8)
ds_read_u16_d16_hi v46,v46
v_lshl_add_u64 v[38:39],v[40:41],0,v[42:43]
v_fma_mixhi_f16 v8,v63,s99,0
v_mfma_f32_16x16x32_bf16 v[28:31],a[208:211],a[64:67],v[28:31]
s_waitcnt lgkmcnt(4)
v_lshl_add_u64 v[40:41],v[56:57],0,v[58:59]
v_lshlrev_b32_sdwa v45,1,v45 dst_sel:DWORD dst_unused:UNUSED_PAD src0_sel:DWORD src1_sel:WORD_1
v_pk_fma_f16 v9,v8,v8,s54
v_mfma_f32_16x16x32_bf16 v[28:31],a[212:215],a[48:51],v[28:31]
ds_read_u16 v45,v45
v_lshlrev_b32_sdwa v47,1,v47 dst_sel:DWORD dst_unused:UNUSED_PAD src0_sel:DWORD src1_sel:WORD_1
v_mfma_f32_16x16x32_bf16 v[56:59],a[212:215],a[52:55],v[20:23]
s_waitcnt lgkmcnt(3)
ds_read_u16_d16_hi v47,v47
v_or_b32 v42,v52,v54
v_pk_mul_f16 v9,v8,v9
v_mfma_f32_16x16x32_bf16 v[56:59],a[208:211],a[68:71],v[56:59]
s_waitcnt vmcnt(22)
v_cvt_pk_bf16_f32 v43,v53,v55
v_lshlrev_b32_sdwa v48,1,v48 dst_sel:DWORD dst_unused:UNUSED_PAD src0_sel:DWORD src1_sel:WORD_1
v_mfma_f32_16x16x32_bf16 v[56:59],a[204:207],a[84:87],v[56:59]
s_barrier
v_exp_f16 v8,v9
v_mfma_f32_16x16x32_bf16 v[52:55],a[200:203],a[84:87],v[16:19]
ds_read_u16 v48,v48
v_lshlrev_b32_sdwa v50,1,v50 dst_sel:DWORD dst_unused:UNUSED_PAD src0_sel:DWORD src1_sel:WORD_1
v_mfma_f32_16x16x32_bf16 v[52:55],a[192:195],a[68:71],v[52:55]
s_add_u32 m0,s58,6160
ds_read_u16_d16_hi v50,v50
v_lshlrev_b32_sdwa v49,1,v49 dst_sel:DWORD dst_unused:UNUSED_PAD src0_sel:DWORD src1_sel:WORD_1
v_mfma_f32_16x16x32_bf16 v[52:55],a[196:199],a[52:55],v[52:55]
s_add_u32 s69,s69,s59
v_exp_f16_sdwa v8,v9 dst_sel:WORD_1 dst_unused:UNUSED_PRESERVE src0_sel:WORD_1
v_mfma_f32_16x16x32_bf16 v[136:139],a[232:235],v[36:39],v[136:139]
buffer_load_dwordx4 v5,s[44:47],s69 offen offset:0 lds
v_lshlrev_b32_sdwa v51,1,v51 dst_sel:DWORD dst_unused:UNUSED_PAD src0_sel:DWORD src1_sel:WORD_1
v_mfma_f32_16x16x32_bf16 v[140:143],a[232:235],v[40:43],v[140:143]
ds_read_u16 v49,v49
v_lshlrev_b32_sdwa v32,1,v32 dst_sel:DWORD dst_unused:UNUSED_PAD src0_sel:DWORD src1_sel:WORD_1
v_mfma_f32_16x16x32_bf16 v[68:71],a[244:247],v[40:43],v[68:71]
ds_read_u16_d16_hi v51,v51
v_pk_fma_f16 v8,v8,s55,s55
v_mfma_f32_16x16x32_bf16 v[64:67],a[244:247],v[36:39],v[64:67]
v_rcp_f16 v9,v8
v_lshlrev_b32_sdwa v34,1,v34 dst_sel:DWORD dst_unused:UNUSED_PAD src0_sel:DWORD src1_sel:WORD_1
v_mfma_f32_16x16x32_bf16 v[144:147],a[228:231],v[36:39],v[144:147]
s_add_u32 s69,s69,s53
ds_read_u16 v32,v32
v_lshlrev_b32_sdwa v33,1,v33 dst_sel:DWORD dst_unused:UNUSED_PAD src0_sel:DWORD src1_sel:WORD_1
v_mfma_f32_16x16x32_bf16 v[148:151],a[228:231],v[40:43],v[148:151]
buffer_load_dwordx4 v5,s[44:47],s69 offen offset:1024 lds
v_lshlrev_b32_sdwa v35,1,v35 dst_sel:DWORD dst_unused:UNUSED_PAD src0_sel:DWORD src1_sel:WORD_1
v_mfma_f32_16x16x32_bf16 v[84:87],a[248:251],v[40:43],v[84:87]
ds_read_u16_d16_hi v34,v34
v_lshlrev_b32_sdwa v60,1,v60 dst_sel:DWORD dst_unused:UNUSED_PAD src0_sel:DWORD src1_sel:WORD_1
v_mfma_f32_16x16x32_bf16 v[80:83],a[248:251],v[36:39],v[80:83]
v_rcp_f16_sdwa v9,v8 dst_sel:WORD_1 dst_unused:UNUSED_PRESERVE src0_sel:WORD_1
v_mfma_f32_16x16x32_bf16 v[152:155],a[236:239],v[36:39],v[152:155]
s_add_u32 m0,s57,38896
ds_read_u16 v33,v33
ds_read_u16_d16_hi v35,v35
v_mfma_f32_16x16x32_bf16 v[156:159],a[236:239],v[40:43],v[156:159]
s_add_u32 s89,s56,5996544
ds_read_u16 v60,v60
v_lshlrev_b32_sdwa v62,1,v62 dst_sel:DWORD dst_unused:UNUSED_PAD src0_sel:DWORD src1_sel:WORD_1
v_mfma_f32_16x16x32_bf16 v[100:103],a[252:255],v[40:43],v[100:103]
buffer_load_dwordx4 v6,s[36:39],s89 offen lds
v_fma_mix_f32 v61,v61,v9,0 op_sel_hi:[0,1,0]
v_mfma_f32_16x16x32_bf16 v[96:99],a[252:255],v[36:39],v[96:99]
s_add_u32 m0,s58,6160
ds_read_u16_d16_hi v62,v62
v_fma_mix_f32 v63,v63,v9,0 op_sel:[0,1,0] op_sel_hi:[0,1,0]
v_mfma_f32_16x16x32_bf16 v[40:43],a[196:199],a[56:59],v[16:19]
s_add_u32 s69,s69,s53
ds_read_b64_tr_b16 a[240:241],v3 offset:12320
v_lshlrev_b32_sdwa v24,1,v24 dst_sel:DWORD dst_unused:UNUSED_PAD src0_sel:DWORD src1_sel:WORD_1
v_mfma_f32_16x16x32_bf16 v[40:43],a[192:195],a[72:75],v[40:43]
buffer_load_dwordx4 v5,s[44:47],s69 offen offset:2048 lds
v_fma_mixlo_f16 v8,v57,s99,0
v_mfma_f32_16x16x32_bf16 v[40:43],a[200:203],a[88:91],v[40:43]
s_waitcnt lgkmcnt(6)
ds_read_u16 v24,v24
v_lshl_add_u64 v[44:45],v[44:45],0,v[46:47]
v_lshlrev_b32_sdwa v26,1,v26 dst_sel:DWORD dst_unused:UNUSED_PAD src0_sel:DWORD src1_sel:WORD_1
v_mfma_f32_16x16x32_bf16 v[36:39],a[204:207],a[88:91],v[20:23]
ds_read_u16_d16_hi v26,v26
v_lshl_add_u64 v[46:47],v[48:49],0,v[50:51]
v_fma_mixhi_f16 v8,v59,s99,0
v_mfma_f32_16x16x32_bf16 v[36:39],a[208:211],a[72:75],v[36:39]
s_waitcnt lgkmcnt(5)
ds_read_b64_tr_b16 a[242:243],v3 offset:13856
v_lshl_add_u64 v[48:49],v[32:33],0,v[34:35]
v_lshlrev_b32_sdwa v25,1,v25 dst_sel:DWORD dst_unused:UNUSED_PAD src0_sel:DWORD src1_sel:WORD_1
v_pk_fma_f16 v9,v8,v8,s54
v_mfma_f32_16x16x32_bf16 v[36:39],a[212:215],a[56:59],v[36:39]
ds_read_u16 v25,v25
v_lshlrev_b32_sdwa v27,1,v27 dst_sel:DWORD dst_unused:UNUSED_PAD src0_sel:DWORD src1_sel:WORD_1
v_mfma_f32_16x16x32_bf16 v[32:35],a[212:215],a[60:63],v[20:23]
s_waitcnt lgkmcnt(5)
ds_read_u16_d16_hi v27,v27
v_or_b32 v50,v60,v62
v_pk_mul_f16 v9,v8,v9
v_mfma_f32_16x16x32_bf16 v[32:35],a[208:211],a[76:79],v[32:35]
ds_read_b64_tr_b16 a[224:225],v3 offset:12328
v_cvt_pk_bf16_f32 v51,v61,v63
v_lshlrev_b32_sdwa v28,1,v28 dst_sel:DWORD dst_unused:UNUSED_PAD src0_sel:DWORD src1_sel:WORD_1
v_mfma_f32_16x16x32_bf16 v[32:35],a[204:207],a[92:95],v[32:35]
v_exp_f16 v8,v9
v_mfma_f32_16x16x32_bf16 v[60:63],a[200:203],a[92:95],v[16:19]
ds_read_u16 v28,v28
v_lshlrev_b32_sdwa v30,1,v30 dst_sel:DWORD dst_unused:UNUSED_PAD src0_sel:DWORD src1_sel:WORD_1
v_mfma_f32_16x16x32_bf16 v[60:63],a[192:195],a[76:79],v[60:63]
ds_read_u16_d16_hi v30,v30
v_lshlrev_b32_sdwa v29,1,v29 dst_sel:DWORD dst_unused:UNUSED_PAD src0_sel:DWORD src1_sel:WORD_1
v_mfma_f32_16x16x32_bf16 v[60:63],a[196:199],a[60:63],v[60:63]
v_exp_f16_sdwa v8,v9 dst_sel:WORD_1 dst_unused:UNUSED_PRESERVE src0_sel:WORD_1
v_mfma_f32_16x16x32_bf16 v[112:115],a[232:235],v[44:47],v[112:115]
ds_read_u16 v29,v29
v_lshlrev_b32_sdwa v31,1,v31 dst_sel:DWORD dst_unused:UNUSED_PAD src0_sel:DWORD src1_sel:WORD_1
v_mfma_f32_16x16x32_bf16 v[116:119],a[232:235],v[48:51],v[116:119]
ds_read_u16_d16_hi v31,v31
v_lshlrev_b32_sdwa v52,1,v52 dst_sel:DWORD dst_unused:UNUSED_PAD src0_sel:DWORD src1_sel:WORD_1
v_mfma_f32_16x16x32_bf16 v[76:79],a[244:247],v[48:51],v[76:79]
ds_read_u16 v52,v52
v_pk_fma_f16 v8,v8,s55,s55
v_mfma_f32_16x16x32_bf16 v[72:75],a[244:247],v[44:47],v[72:75]
v_rcp_f16 v9,v8
v_lshlrev_b32_sdwa v54,1,v54 dst_sel:DWORD dst_unused:UNUSED_PAD src0_sel:DWORD src1_sel:WORD_1
v_mfma_f32_16x16x32_bf16 v[120:123],a[228:231],v[44:47],v[120:123]
ds_read_u16_d16_hi v54,v54
v_lshlrev_b32_sdwa v53,1,v53 dst_sel:DWORD dst_unused:UNUSED_PAD src0_sel:DWORD src1_sel:WORD_1
v_mfma_f32_16x16x32_bf16 v[124:127],a[228:231],v[48:51],v[124:127]
ds_read_u16 v53,v53
ds_read_b64_tr_b16 a[226:227],v3 offset:13864
v_mfma_f32_16x16x32_bf16 v[92:95],a[248:251],v[48:51],v[92:95]
ds_read_b64_tr_b16 a[232:233],v3 offset:12384
v_lshlrev_b32_sdwa v55,1,v55 dst_sel:DWORD dst_unused:UNUSED_PAD src0_sel:DWORD src1_sel:WORD_1
v_mfma_f32_16x16x32_bf16 v[88:91],a[248:251],v[44:47],v[88:91]
ds_read_u16_d16_hi v55,v55
v_rcp_f16_sdwa v9,v8 dst_sel:WORD_1 dst_unused:UNUSED_PRESERVE src0_sel:WORD_1
v_mfma_f32_16x16x32_bf16 v[128:131],a[236:239],v[44:47],v[128:131]
ds_read_b64_tr_b16 a[234:235],v3 offset:13920
ds_read_b64_tr_b16 a[244:245],v3 offset:12392
v_lshlrev_b32_sdwa v56,1,v56 dst_sel:DWORD dst_unused:UNUSED_PAD src0_sel:DWORD src1_sel:WORD_1
v_mfma_f32_16x16x32_bf16 v[132:135],a[236:239],v[48:51],v[132:135]
ds_read_u16 v56,v56
v_lshlrev_b32_sdwa v58,1,v58 dst_sel:DWORD dst_unused:UNUSED_PAD src0_sel:DWORD src1_sel:WORD_1
v_mfma_f32_16x16x32_bf16 v[108:111],a[252:255],v[48:51],v[108:111]
ds_read_u16_d16_hi v58,v58
v_fma_mix_f32 v57,v57,v9,0 op_sel_hi:[0,1,0]
v_mfma_f32_16x16x32_bf16 v[104:107],a[252:255],v[44:47],v[104:107]
TP6560_%=:
s_cbranch_execz TR0_%=
ds_read_b64_tr_b16 a[246:247],v3 offset:13928
v_fma_mix_f32 v59,v59,v9,0 op_sel:[0,1,0] op_sel_hi:[0,1,0]
v_mfma_f32_16x16x32_bf16 v[48:51],a[196:199],a[0:3],v[16:19]
ds_read_b64_tr_b16 a[228:229],v3 offset:12448
v_lshlrev_b32_sdwa v40,1,v40 dst_sel:DWORD dst_unused:UNUSED_PAD src0_sel:DWORD src1_sel:WORD_1
v_mfma_f32_16x16x32_bf16 v[48:51],a[192:195],a[16:19],v[48:51]
ds_read_b64_tr_b16 a[230:231],v3 offset:13984
v_fma_mixlo_f16 v8,v33,s99,0
v_mfma_f32_16x16x32_bf16 v[48:51],a[200:203],a[32:35],v[48:51]
s_waitcnt lgkmcnt(9)
ds_read_b64_tr_b16 a[248:249],v3 offset:12456
v_lshl_add_u64 v[24:25],v[24:25],0,v[26:27]
v_lshlrev_b32_sdwa v42,1,v42 dst_sel:DWORD dst_unused:UNUSED_PAD src0_sel:DWORD src1_sel:WORD_1
v_mfma_f32_16x16x32_bf16 v[44:47],a[204:207],a[32:35],v[20:23]
ds_read_b64_tr_b16 a[250:251],v3 offset:13992
v_lshl_add_u64 v[26:27],v[28:29],0,v[30:31]
v_fma_mixhi_f16 v8,v35,s99,0
v_mfma_f32_16x16x32_bf16 v[44:47],a[208:211],a[16:19],v[44:47]
s_waitcnt lgkmcnt(9)
ds_read_u16 v40,v40
v_lshl_add_u64 v[28:29],v[52:53],0,v[54:55]
v_lshlrev_b32_sdwa v41,1,v41 dst_sel:DWORD dst_unused:UNUSED_PAD src0_sel:DWORD src1_sel:WORD_1
v_pk_fma_f16 v9,v8,v8,s54
v_mfma_f32_16x16x32_bf16 v[44:47],a[212:215],a[0:3],v[44:47]
ds_read_u16_d16_hi v42,v42
v_lshlrev_b32_sdwa v43,1,v43 dst_sel:DWORD dst_unused:UNUSED_PAD src0_sel:DWORD src1_sel:WORD_1
v_mfma_f32_16x16x32_bf16 v[52:55],a[212:215],a[4:7],v[20:23]
s_waitcnt lgkmcnt(7)
ds_read_u16 v41,v41
v_or_b32 v30,v56,v58
v_pk_mul_f16 v9,v8,v9
v_mfma_f32_16x16x32_bf16 v[52:55],a[208:211],a[20:23],v[52:55]
ds_read_u16_d16_hi v43,v43
v_cvt_pk_bf16_f32 v31,v57,v59
v_lshlrev_b32_sdwa v36,1,v36 dst_sel:DWORD dst_unused:UNUSED_PAD src0_sel:DWORD src1_sel:WORD_1
v_mfma_f32_16x16x32_bf16 v[52:55],a[204:207],a[36:39],v[52:55]
v_exp_f16 v8,v9
v_mfma_f32_16x16x32_bf16 v[56:59],a[200:203],a[36:39],v[16:19]
ds_read_u16 v36,v36
v_lshlrev_b32_sdwa v38,1,v38 dst_sel:DWORD dst_unused:UNUSED_PAD src0_sel:DWORD src1_sel:WORD_1
v_mfma_f32_16x16x32_bf16 v[56:59],a[192:195],a[20:23],v[56:59]
ds_read_u16_d16_hi v38,v38
v_lshlrev_b32_sdwa v37,1,v37 dst_sel:DWORD dst_unused:UNUSED_PAD src0_sel:DWORD src1_sel:WORD_1
v_mfma_f32_16x16x32_bf16 v[56:59],a[196:199],a[4:7],v[56:59]
s_add_u32 m0,s57,42440
v_exp_f16_sdwa v8,v9 dst_sel:WORD_1 dst_unused:UNUSED_PRESERVE src0_sel:WORD_1
v_mfma_f32_16x16x32_bf16 v[232:235],a[240:243],v[24:27],v[232:235]
s_add_u32 s89,s56,6094848
ds_read_u16 v37,v37
v_lshlrev_b32_sdwa v39,1,v39 dst_sel:DWORD dst_unused:UNUSED_PAD src0_sel:DWORD src1_sel:WORD_1
v_mfma_f32_16x16x32_bf16 v[236:239],a[240:243],v[28:31],v[236:239]
buffer_load_dwordx4 v6,s[36:39],s89 offen lds
v_lshlrev_b32_sdwa v60,1,v60 dst_sel:DWORD dst_unused:UNUSED_PAD src0_sel:DWORD src1_sel:WORD_1
v_mfma_f32_16x16x32_bf16 v[164:167],a[224:227],v[28:31],v[164:167]
ds_read_u16_d16_hi v39,v39
v_pk_fma_f16 v8,v8,s55,s55
v_mfma_f32_16x16x32_bf16 v[160:163],a[224:227],v[24:27],v[160:163]
v_rcp_f16 v9,v8
v_lshlrev_b32_sdwa v62,1,v62 dst_sel:DWORD dst_unused:UNUSED_PAD src0_sel:DWORD src1_sel:WORD_1
v_mfma_f32_16x16x32_bf16 v[240:243],a[232:235],v[24:27],v[240:243]
ds_read_u16 v60,v60
v_lshlrev_b32_sdwa v61,1,v61 dst_sel:DWORD dst_unused:UNUSED_PAD src0_sel:DWORD src1_sel:WORD_1
v_mfma_f32_16x16x32_bf16 v[244:247],a[232:235],v[28:31],v[244:247]
s_waitcnt lgkmcnt(5)
ds_read_u16_d16_hi v62,v62
v_lshlrev_b32_sdwa v63,1,v63 dst_sel:DWORD dst_unused:UNUSED_PAD src0_sel:DWORD src1_sel:WORD_1
v_mfma_f32_16x16x32_bf16 v[180:183],a[244:247],v[28:31],v[180:183]
ds_read_u16 v61,v61
v_lshlrev_b32_sdwa v32,1,v32 dst_sel:DWORD dst_unused:UNUSED_PAD src0_sel:DWORD src1_sel:WORD_1
v_mfma_f32_16x16x32_bf16 v[176:179],a[244:247],v[24:27],v[176:179]
ds_read_u16_d16_hi v63,v63
v_rcp_f16_sdwa v9,v8 dst_sel:WORD_1 dst_unused:UNUSED_PRESERVE src0_sel:WORD_1
v_mfma_f32_16x16x32_bf16 v[248:251],a[228:231],v[24:27],v[248:251]
ds_read_u16 v32,v32
v_lshlrev_b32_sdwa v34,1,v34 dst_sel:DWORD dst_unused:UNUSED_PAD src0_sel:DWORD src1_sel:WORD_1
v_mfma_f32_16x16x32_bf16 v[252:255],a[228:231],v[28:31],v[252:255]
ds_read_u16_d16_hi v34,v34
v_fma_mix_f32 v33,v33,v9,0 op_sel_hi:[0,1,0]
v_mfma_f32_16x16x32_bf16 v[196:199],a[248:251],v[28:31],v[196:199]
ds_read_b64_tr_b16 a[220:221],v1 offset:12320
v_fma_mix_f32 v35,v35,v9,0 op_sel:[0,1,0] op_sel_hi:[0,1,0]
v_mfma_f32_16x16x32_bf16 v[192:195],a[248:251],v[24:27],v[192:195]
ds_read_b64_tr_b16 a[222:223],v1 offset:13344
v_mfma_f32_16x16x32_bf16 v[28:31],a[196:199],a[8:11],v[16:19]
ds_read_b64_tr_b16 a[216:217],v1 offset:14368
v_lshlrev_b32_sdwa v48,1,v48 dst_sel:DWORD dst_unused:UNUSED_PAD src0_sel:DWORD src1_sel:WORD_1
v_mfma_f32_16x16x32_bf16 v[28:31],a[192:195],a[24:27],v[28:31]
ds_read_b64_tr_b16 a[218:219],v1 offset:15392
v_fma_mixlo_f16 v8,v53,s99,0
v_mfma_f32_16x16x32_bf16 v[28:31],a[200:203],a[40:43],v[28:31]
ds_read_u16 v48,v48
v_lshl_add_u64 v[40:41],v[40:41],0,v[42:43]
v_lshlrev_b32_sdwa v50,1,v50 dst_sel:DWORD dst_unused:UNUSED_PAD src0_sel:DWORD src1_sel:WORD_1
v_mfma_f32_16x16x32_bf16 v[24:27],a[204:207],a[40:43],v[20:23]
s_waitcnt lgkmcnt(8)
ds_read_u16_d16_hi v50,v50
v_lshl_add_u64 v[42:43],v[36:37],0,v[38:39]
v_fma_mixhi_f16 v8,v55,s99,0
v_mfma_f32_16x16x32_bf16 v[24:27],a[208:211],a[24:27],v[24:27]
s_waitcnt lgkmcnt(8)
v_lshl_add_u64 v[36:37],v[60:61],0,v[62:63]
v_lshlrev_b32_sdwa v49,1,v49 dst_sel:DWORD dst_unused:UNUSED_PAD src0_sel:DWORD src1_sel:WORD_1
v_pk_fma_f16 v9,v8,v8,s54
v_mfma_f32_16x16x32_bf16 v[24:27],a[212:215],a[8:11],v[24:27]
ds_read_u16 v49,v49
v_lshlrev_b32_sdwa v51,1,v51 dst_sel:DWORD dst_unused:UNUSED_PAD src0_sel:DWORD src1_sel:WORD_1
v_mfma_f32_16x16x32_bf16 v[60:63],a[212:215],a[12:15],v[20:23]
s_waitcnt lgkmcnt(7)
ds_read_b128 v[20:23],v4 offset:384
v_or_b32 v38,v32,v34
v_pk_mul_f16 v9,v8,v9
v_mfma_f32_16x16x32_bf16 v[60:63],a[208:211],a[28:31],v[60:63]
ds_read_u16_d16_hi v51,v51
v_cvt_pk_bf16_f32 v39,v33,v35
v_lshlrev_b32_sdwa v44,1,v44 dst_sel:DWORD dst_unused:UNUSED_PAD src0_sel:DWORD src1_sel:WORD_1
v_mfma_f32_16x16x32_bf16 v[60:63],a[204:207],a[44:47],v[60:63]
ds_read_b64_tr_b16 a[212:213],v1 offset:16416
v_exp_f16 v8,v9
v_mfma_f32_16x16x32_bf16 v[32:35],a[200:203],a[44:47],v[16:19]
ds_read_b128 v[16:19],v4 offset:416
ds_read_b64_tr_b16 a[214:215],v1 offset:17440
v_lshlrev_b32_sdwa v46,1,v46 dst_sel:DWORD dst_unused:UNUSED_PAD src0_sel:DWORD src1_sel:WORD_1
v_mfma_f32_16x16x32_bf16 v[32:35],a[192:195],a[28:31],v[32:35]
ds_read_b64_tr_b16 a[208:209],v2 offset:16416
ds_read_b64_tr_b16 a[210:211],v2 offset:17440
v_lshlrev_b32_sdwa v45,1,v45 dst_sel:DWORD dst_unused:UNUSED_PAD src0_sel:DWORD src1_sel:WORD_1
v_mfma_f32_16x16x32_bf16 v[32:35],a[196:199],a[12:15],v[32:35]
ds_read_u16 v44,v44
v_exp_f16_sdwa v8,v9 dst_sel:WORD_1 dst_unused:UNUSED_PRESERVE src0_sel:WORD_1
v_mfma_f32_16x16x32_bf16 v[208:211],a[240:243],v[40:43],v[208:211]
ds_read_u16_d16_hi v46,v46
v_lshlrev_b32_sdwa v47,1,v47 dst_sel:DWORD dst_unused:UNUSED_PAD src0_sel:DWORD src1_sel:WORD_1
v_mfma_f32_16x16x32_bf16 v[212:215],a[240:243],v[36:39],v[212:215]
ds_read_u16 v45,v45
v_lshlrev_b32_sdwa v56,1,v56 dst_sel:DWORD dst_unused:UNUSED_PAD src0_sel:DWORD src1_sel:WORD_1
v_mfma_f32_16x16x32_bf16 v[172:175],a[224:227],v[36:39],v[172:175]
ds_read_u16_d16_hi v47,v47
v_pk_fma_f16 v8,v8,s55,s55
v_mfma_f32_16x16x32_bf16 v[168:171],a[224:227],v[40:43],v[168:171]
v_rcp_f16 v9,v8
v_lshlrev_b32_sdwa v58,1,v58 dst_sel:DWORD dst_unused:UNUSED_PAD src0_sel:DWORD src1_sel:WORD_1
v_mfma_f32_16x16x32_bf16 v[216:219],a[232:235],v[40:43],v[216:219]
ds_read_b64_tr_b16 a[204:205],v2 offset:14368
v_lshlrev_b32_sdwa v57,1,v57 dst_sel:DWORD dst_unused:UNUSED_PAD src0_sel:DWORD src1_sel:WORD_1
v_mfma_f32_16x16x32_bf16 v[220:223],a[232:235],v[36:39],v[220:223]
ds_read_b64_tr_b16 a[206:207],v2 offset:15392
v_lshlrev_b32_sdwa v59,1,v59 dst_sel:DWORD dst_unused:UNUSED_PAD src0_sel:DWORD src1_sel:WORD_1
v_mfma_f32_16x16x32_bf16 v[188:191],a[244:247],v[36:39],v[188:191]
ds_read_u16 v56,v56
v_lshlrev_b32_sdwa v52,1,v52 dst_sel:DWORD dst_unused:UNUSED_PAD src0_sel:DWORD src1_sel:WORD_1
v_mfma_f32_16x16x32_bf16 v[184:187],a[244:247],v[40:43],v[184:187]
ds_read_u16_d16_hi v58,v58
v_rcp_f16_sdwa v9,v8 dst_sel:WORD_1 dst_unused:UNUSED_PRESERVE src0_sel:WORD_1
v_mfma_f32_16x16x32_bf16 v[224:227],a[228:231],v[40:43],v[224:227]
ds_read_u16 v57,v57
ds_read_u16_d16_hi v59,v59
v_mfma_f32_16x16x32_bf16 v[228:231],a[228:231],v[36:39],v[228:231]
ds_read_b64_tr_b16 a[200:201],v2 offset:12320
ds_read_b64_tr_b16 a[202:203],v2 offset:13344
v_lshlrev_b32_sdwa v54,1,v54 dst_sel:DWORD dst_unused:UNUSED_PAD src0_sel:DWORD src1_sel:WORD_1
v_mfma_f32_16x16x32_bf16 v[204:207],a[248:251],v[36:39],v[204:207]
ds_read_u16 v52,v52
v_fma_mix_f32 v53,v53,v9,0 op_sel_hi:[0,1,0]
v_mfma_f32_16x16x32_bf16 v[200:203],a[248:251],v[40:43],v[200:203]
s_waitcnt lgkmcnt(9)
ds_read_u16_d16_hi v54,v54
v_fma_mix_f32 v55,v55,v9,0 op_sel:[0,1,0] op_sel_hi:[0,1,0]
v_mfma_f32_16x16x32_bf16 v[36:39],a[220:223],a[48:51],v[20:23]
v_lshlrev_b32_sdwa v28,1,v28 dst_sel:DWORD dst_unused:UNUSED_PAD src0_sel:DWORD src1_sel:WORD_1
v_fma_mixlo_f16 v8,v61,s99,0
v_mfma_f32_16x16x32_bf16 v[36:39],a[216:219],a[64:67],v[36:39]
ds_read_u16 v28,v28
v_lshlrev_b32_sdwa v30,1,v30 dst_sel:DWORD dst_unused:UNUSED_PAD src0_sel:DWORD src1_sel:WORD_1
v_mfma_f32_16x16x32_bf16 v[36:39],a[212:215],a[80:83],v[36:39]
ds_read_u16_d16_hi v30,v30
v_lshl_add_u64 v[48:49],v[48:49],0,v[50:51]
v_mfma_f32_16x16x32_bf16 v[40:43],a[208:211],a[80:83],v[16:19]
s_waitcnt lgkmcnt(9)
v_lshl_add_u64 v[50:51],v[44:45],0,v[46:47]
v_fma_mixhi_f16 v8,v63,s99,0
v_mfma_f32_16x16x32_bf16 v[40:43],a[204:207],a[64:67],v[40:43]
s_waitcnt lgkmcnt(4)
v_lshl_add_u64 v[44:45],v[56:57],0,v[58:59]
v_lshlrev_b32_sdwa v29,1,v29 dst_sel:DWORD dst_unused:UNUSED_PAD src0_sel:DWORD src1_sel:WORD_1
v_pk_fma_f16 v9,v8,v8,s54
v_mfma_f32_16x16x32_bf16 v[40:43],a[200:203],a[48:51],v[40:43]
s_add_u32 m0,s57,45976
ds_read_u16 v29,v29
v_lshlrev_b32_sdwa v31,1,v31 dst_sel:DWORD dst_unused:UNUSED_PAD src0_sel:DWORD src1_sel:WORD_1
v_mfma_f32_16x16x32_bf16 v[56:59],a[200:203],a[52:55],v[16:19]
s_waitcnt lgkmcnt(3)
ds_read_u16_d16_hi v31,v31
v_or_b32 v46,v52,v54
v_pk_mul_f16 v9,v8,v9
v_mfma_f32_16x16x32_bf16 v[56:59],a[204:207],a[68:71],v[56:59]
s_waitcnt vmcnt(20)
v_cvt_pk_bf16_f32 v47,v53,v55
v_lshlrev_b32_sdwa v24,1,v24 dst_sel:DWORD dst_unused:UNUSED_PAD src0_sel:DWORD src1_sel:WORD_1
v_mfma_f32_16x16x32_bf16 v[56:59],a[208:211],a[84:87],v[56:59]
s_barrier
v_exp_f16 v8,v9
v_mfma_f32_16x16x32_bf16 v[52:55],a[212:215],a[84:87],v[20:23]
s_add_u32 s89,s56,6193152
ds_read_u16 v24,v24
v_lshlrev_b32_sdwa v26,1,v26 dst_sel:DWORD dst_unused:UNUSED_PAD src0_sel:DWORD src1_sel:WORD_1
v_mfma_f32_16x16x32_bf16 v[52:55],a[216:219],a[68:71],v[52:55]
buffer_load_dwordx4 v6,s[36:39],s89 offen lds
v_lshlrev_b32_sdwa v25,1,v25 dst_sel:DWORD dst_unused:UNUSED_PAD src0_sel:DWORD src1_sel:WORD_1
v_mfma_f32_16x16x32_bf16 v[52:55],a[220:223],a[52:55],v[52:55]
s_add_u32 m0,s58,12320
v_exp_f16_sdwa v8,v9 dst_sel:WORD_1 dst_unused:UNUSED_PRESERVE src0_sel:WORD_1
v_mfma_f32_16x16x32_bf16 v[136:139],a[240:243],v[48:51],v[136:139]
s_add_u32 s69,s69,s59
ds_read_u16_d16_hi v26,v26
v_lshlrev_b32_sdwa v27,1,v27 dst_sel:DWORD dst_unused:UNUSED_PAD src0_sel:DWORD src1_sel:WORD_1
v_mfma_f32_16x16x32_bf16 v[140:143],a[240:243],v[44:47],v[140:143]
buffer_load_dwordx4 v5,s[44:47],s69 offen offset:0 lds
v_lshlrev_b32_sdwa v32,1,v32 dst_sel:DWORD dst_unused:UNUSED_PAD src0_sel:DWORD src1_sel:WORD_1
v_mfma_f32_16x16x32_bf16 v[68:71],a[224:227],v[44:47],v[68:71]
ds_read_u16 v25,v25
v_pk_fma_f16 v8,v8,s55,s55
v_mfma_f32_16x16x32_bf16 v[64:67],a[224:227],v[48:51],v[64:67]
v_rcp_f16 v9,v8
v_lshlrev_b32_sdwa v34,1,v34 dst_sel:DWORD dst_unused:UNUSED_PAD src0_sel:DWORD src1_sel:WORD_1
v_mfma_f32_16x16x32_bf16 v[144:147],a[232:235],v[48:51],v[144:147]
s_add_u32 s69,s69,s53
ds_read_u16_d16_hi v27,v27
v_lshlrev_b32_sdwa v33,1,v33 dst_sel:DWORD dst_unused:UNUSED_PAD src0_sel:DWORD src1_sel:WORD_1
v_mfma_f32_16x16x32_bf16 v[148:151],a[232:235],v[44:47],v[148:151]
buffer_load_dwordx4 v5,s[44:47],s69 offen offset:1024 lds
v_lshlrev_b32_sdwa v35,1,v35 dst_sel:DWORD dst_unused:UNUSED_PAD src0_sel:DWORD src1_sel:WORD_1
v_mfma_f32_16x16x32_bf16 v[84:87],a[244:247],v[44:47],v[84:87]
ds_read_u16 v32,v32
v_lshlrev_b32_sdwa v60,1,v60 dst_sel:DWORD dst_unused:UNUSED_PAD src0_sel:DWORD src1_sel:WORD_1
v_mfma_f32_16x16x32_bf16 v[80:83],a[244:247],v[48:51],v[80:83]
v_rcp_f16_sdwa v9,v8 dst_sel:WORD_1 dst_unused:UNUSED_PRESERVE src0_sel:WORD_1
v_mfma_f32_16x16x32_bf16 v[152:155],a[228:231],v[48:51],v[152:155]
ds_read_u16_d16_hi v34,v34
ds_read_u16 v33,v33
v_mfma_f32_16x16x32_bf16 v[156:159],a[228:231],v[44:47],v[156:159]
s_add_u32 s69,s69,s53
ds_read_u16_d16_hi v35,v35
v_lshlrev_b32_sdwa v62,1,v62 dst_sel:DWORD dst_unused:UNUSED_PAD src0_sel:DWORD src1_sel:WORD_1
v_mfma_f32_16x16x32_bf16 v[100:103],a[248:251],v[44:47],v[100:103]
buffer_load_dwordx4 v5,s[44:47],s69 offen offset:2048 lds
v_fma_mix_f32 v61,v61,v9,0 op_sel_hi:[0,1,0]
v_mfma_f32_16x16x32_bf16 v[96:99],a[248:251],v[48:51],v[96:99]
ds_read_u16 v60,v60
ds_read_u16_d16_hi v62,v62
v_fma_mix_f32 v63,v63,v9,0 op_sel:[0,1,0] op_sel_hi:[0,1,0]
v_mfma_f32_16x16x32_bf16 v[44:47],a[220:223],a[56:59],v[20:23]
ds_read_b64_tr_b16 a[236:237],v3 offset:18480
v_lshlrev_b32_sdwa v36,1,v36 dst_sel:DWORD dst_unused:UNUSED_PAD src0_sel:DWORD src1_sel:WORD_1
v_mfma_f32_16x16x32_bf16 v[44:47],a[216:219],a[72:75],v[44:47]
ds_read_u16 v36,v36
v_fma_mixlo_f16 v8,v57,s99,0
v_mfma_f32_16x16x32_bf16 v[44:47],a[212:215],a[88:91],v[44:47]
s_waitcnt lgkmcnt(8)
ds_read_b64_tr_b16 a[238:239],v3 offset:20016
v_lshl_add_u64 v[28:29],v[28:29],0,v[30:31]
v_lshlrev_b32_sdwa v38,1,v38 dst_sel:DWORD dst_unused:UNUSED_PAD src0_sel:DWORD src1_sel:WORD_1
v_mfma_f32_16x16x32_bf16 v[48:51],a[208:211],a[88:91],v[16:19]
ds_read_u16_d16_hi v38,v38
v_lshl_add_u64 v[30:31],v[24:25],0,v[26:27]
v_fma_mixhi_f16 v8,v59,s99,0
v_mfma_f32_16x16x32_bf16 v[48:51],a[204:207],a[72:75],v[48:51]
s_waitcnt lgkmcnt(6)
ds_read_b64_tr_b16 a[252:253],v3 offset:18488
v_lshl_add_u64 v[24:25],v[32:33],0,v[34:35]
v_lshlrev_b32_sdwa v37,1,v37 dst_sel:DWORD dst_unused:UNUSED_PAD src0_sel:DWORD src1_sel:WORD_1
v_pk_fma_f16 v9,v8,v8,s54
v_mfma_f32_16x16x32_bf16 v[48:51],a[200:203],a[56:59],v[48:51]
ds_read_u16 v37,v37
v_lshlrev_b32_sdwa v39,1,v39 dst_sel:DWORD dst_unused:UNUSED_PAD src0_sel:DWORD src1_sel:WORD_1
v_mfma_f32_16x16x32_bf16 v[32:35],a[200:203],a[60:63],v[16:19]
s_waitcnt lgkmcnt(6)
ds_read_u16_d16_hi v39,v39
v_or_b32 v26,v60,v62
v_pk_mul_f16 v9,v8,v9
v_mfma_f32_16x16x32_bf16 v[32:35],a[204:207],a[76:79],v[32:35]
ds_read_b64_tr_b16 a[254:255],v3 offset:20024
v_cvt_pk_bf16_f32 v27,v61,v63
v_lshlrev_b32_sdwa v40,1,v40 dst_sel:DWORD dst_unused:UNUSED_PAD src0_sel:DWORD src1_sel:WORD_1
v_mfma_f32_16x16x32_bf16 v[32:35],a[208:211],a[92:95],v[32:35]
v_exp_f16 v8,v9
v_mfma_f32_16x16x32_bf16 v[60:63],a[212:215],a[92:95],v[20:23]
ds_read_u16 v40,v40
v_lshlrev_b32_sdwa v42,1,v42 dst_sel:DWORD dst_unused:UNUSED_PAD src0_sel:DWORD src1_sel:WORD_1
v_mfma_f32_16x16x32_bf16 v[60:63],a[216:219],a[76:79],v[60:63]
ds_read_u16_d16_hi v42,v42
v_lshlrev_b32_sdwa v41,1,v41 dst_sel:DWORD dst_unused:UNUSED_PAD src0_sel:DWORD src1_sel:WORD_1
v_mfma_f32_16x16x32_bf16 v[60:63],a[220:223],a[60:63],v[60:63]
v_exp_f16_sdwa v8,v9 dst_sel:WORD_1 dst_unused:UNUSED_PRESERVE src0_sel:WORD_1
v_mfma_f32_16x16x32_bf16 v[112:115],a[240:243],v[28:31],v[112:115]
ds_read_u16 v41,v41
v_lshlrev_b32_sdwa v43,1,v43 dst_sel:DWORD dst_unused:UNUSED_PAD src0_sel:DWORD src1_sel:WORD_1
v_mfma_f32_16x16x32_bf16 v[116:119],a[240:243],v[24:27],v[116:119]
ds_read_u16_d16_hi v43,v43
v_lshlrev_b32_sdwa v52,1,v52 dst_sel:DWORD dst_unused:UNUSED_PAD src0_sel:DWORD src1_sel:WORD_1
v_mfma_f32_16x16x32_bf16 v[76:79],a[224:227],v[24:27],v[76:79]
ds_read_u16 v52,v52
v_pk_fma_f16 v8,v8,s55,s55
v_mfma_f32_16x16x32_bf16 v[72:75],a[224:227],v[28:31],v[72:75]
v_rcp_f16 v9,v8
v_lshlrev_b32_sdwa v54,1,v54 dst_sel:DWORD dst_unused:UNUSED_PAD src0_sel:DWORD src1_sel:WORD_1
v_mfma_f32_16x16x32_bf16 v[120:123],a[232:235],v[28:31],v[120:123]
ds_read_u16_d16_hi v54,v54
v_lshlrev_b32_sdwa v53,1,v53 dst_sel:DWORD dst_unused:UNUSED_PAD src0_sel:DWORD src1_sel:WORD_1
v_mfma_f32_16x16x32_bf16 v[124:127],a[232:235],v[24:27],v[124:127]
ds_read_u16 v53,v53
ds_read_b64_tr_b16 a[240:241],v3 offset:18544
v_mfma_f32_16x16x32_bf16 v[92:95],a[244:247],v[24:27],v[92:95]
ds_read_b64_tr_b16 a[242:243],v3 offset:20080
v_lshlrev_b32_sdwa v55,1,v55 dst_sel:DWORD dst_unused:UNUSED_PAD src0_sel:DWORD src1_sel:WORD_1
v_mfma_f32_16x16x32_bf16 v[88:91],a[244:247],v[28:31],v[88:91]
ds_read_u16_d16_hi v55,v55
v_rcp_f16_sdwa v9,v8 dst_sel:WORD_1 dst_unused:UNUSED_PRESERVE src0_sel:WORD_1
v_mfma_f32_16x16x32_bf16 v[128:131],a[228:231],v[28:31],v[128:131]
ds_read_b64_tr_b16 a[224:225],v3 offset:18552
v_lshlrev_b32_sdwa v56,1,v56 dst_sel:DWORD dst_unused:UNUSED_PAD src0_sel:DWORD src1_sel:WORD_1
v_mfma_f32_16x16x32_bf16 v[132:135],a[228:231],v[24:27],v[132:135]
ds_read_u16 v56,v56
v_lshlrev_b32_sdwa v58,1,v58 dst_sel:DWORD dst_unused:UNUSED_PAD src0_sel:DWORD src1_sel:WORD_1
v_mfma_f32_16x16x32_bf16 v[108:111],a[248:251],v[24:27],v[108:111]
ds_read_u16_d16_hi v58,v58
v_fma_mix_f32 v57,v57,v9,0 op_sel_hi:[0,1,0]
v_mfma_f32_16x16x32_bf16 v[104:107],a[248:251],v[28:31],v[104:107]
ds_read_b64_tr_b16 a[226:227],v3 offset:20088
v_fma_mix_f32 v59,v59,v9,0 op_sel:[0,1,0] op_sel_hi:[0,1,0]
v_mfma_f32_16x16x32_bf16 v[24:27],a[220:223],a[0:3],v[20:23]
s_add_u32 m0,s57,49504
ds_read_b64_tr_b16 a[232:233],v3 offset:18608
v_lshlrev_b32_sdwa v44,1,v44 dst_sel:DWORD dst_unused:UNUSED_PAD src0_sel:DWORD src1_sel:WORD_1
v_mfma_f32_16x16x32_bf16 v[24:27],a[216:219],a[16:19],v[24:27]
s_add_u32 s89,s56,6291456
ds_read_b64_tr_b16 a[234:235],v3 offset:20144
v_fma_mixlo_f16 v8,v33,s99,0
v_mfma_f32_16x16x32_bf16 v[24:27],a[212:215],a[32:35],v[24:27]
s_waitcnt lgkmcnt(8)
buffer_load_dwordx4 v6,s[36:39],s89 offen lds
v_lshl_add_u64 v[36:37],v[36:37],0,v[38:39]
v_lshlrev_b32_sdwa v46,1,v46 dst_sel:DWORD dst_unused:UNUSED_PAD src0_sel:DWORD src1_sel:WORD_1
v_mfma_f32_16x16x32_bf16 v[28:31],a[208:211],a[32:35],v[16:19]
ds_read_b64_tr_b16 a[244:245],v3 offset:18616
v_lshl_add_u64 v[38:39],v[40:41],0,v[42:43]
v_fma_mixhi_f16 v8,v35,s99,0
v_mfma_f32_16x16x32_bf16 v[28:31],a[204:207],a[16:19],v[28:31]
s_waitcnt lgkmcnt(7)
ds_read_b64_tr_b16 a[246:247],v3 offset:20152
v_lshl_add_u64 v[40:41],v[52:53],0,v[54:55]
v_lshlrev_b32_sdwa v45,1,v45 dst_sel:DWORD dst_unused:UNUSED_PAD src0_sel:DWORD src1_sel:WORD_1
v_pk_fma_f16 v9,v8,v8,s54
v_mfma_f32_16x16x32_bf16 v[28:31],a[200:203],a[0:3],v[28:31]
ds_read_u16 v44,v44
v_lshlrev_b32_sdwa v47,1,v47 dst_sel:DWORD dst_unused:UNUSED_PAD src0_sel:DWORD src1_sel:WORD_1
v_mfma_f32_16x16x32_bf16 v[52:55],a[200:203],a[4:7],v[16:19]
s_waitcnt lgkmcnt(6)
ds_read_u16_d16_hi v46,v46
v_or_b32 v42,v56,v58
v_pk_mul_f16 v9,v8,v9
v_mfma_f32_16x16x32_bf16 v[52:55],a[204:207],a[20:23],v[52:55]
ds_read_u16 v45,v45
v_cvt_pk_bf16_f32 v43,v57,v59
v_lshlrev_b32_sdwa v48,1,v48 dst_sel:DWORD dst_unused:UNUSED_PAD src0_sel:DWORD src1_sel:WORD_1
v_mfma_f32_16x16x32_bf16 v[52:55],a[208:211],a[36:39],v[52:55]
v_exp_f16 v8,v9
v_mfma_f32_16x16x32_bf16 v[56:59],a[212:215],a[36:39],v[20:23]
ds_read_u16_d16_hi v47,v47
v_lshlrev_b32_sdwa v50,1,v50 dst_sel:DWORD dst_unused:UNUSED_PAD src0_sel:DWORD src1_sel:WORD_1
v_mfma_f32_16x16x32_bf16 v[56:59],a[216:219],a[20:23],v[56:59]
ds_read_u16 v48,v48
v_lshlrev_b32_sdwa v49,1,v49 dst_sel:DWORD dst_unused:UNUSED_PAD src0_sel:DWORD src1_sel:WORD_1
v_mfma_f32_16x16x32_bf16 v[56:59],a[220:223],a[4:7],v[56:59]
ds_read_u16_d16_hi v50,v50
v_exp_f16_sdwa v8,v9 dst_sel:WORD_1 dst_unused:UNUSED_PRESERVE src0_sel:WORD_1
v_mfma_f32_16x16x32_bf16 v[232:235],a[236:239],v[36:39],v[232:235]
ds_read_u16 v49,v49
v_lshlrev_b32_sdwa v51,1,v51 dst_sel:DWORD dst_unused:UNUSED_PAD src0_sel:DWORD src1_sel:WORD_1
v_mfma_f32_16x16x32_bf16 v[236:239],a[236:239],v[40:43],v[236:239]
ds_read_u16_d16_hi v51,v51
v_lshlrev_b32_sdwa v60,1,v60 dst_sel:DWORD dst_unused:UNUSED_PAD src0_sel:DWORD src1_sel:WORD_1
v_mfma_f32_16x16x32_bf16 v[164:167],a[252:255],v[40:43],v[164:167]
ds_read_u16 v60,v60
v_pk_fma_f16 v8,v8,s55,s55
v_mfma_f32_16x16x32_bf16 v[160:163],a[252:255],v[36:39],v[160:163]
v_rcp_f16 v9,v8
v_lshlrev_b32_sdwa v62,1,v62 dst_sel:DWORD dst_unused:UNUSED_PAD src0_sel:DWORD src1_sel:WORD_1
v_mfma_f32_16x16x32_bf16 v[240:243],a[240:243],v[36:39],v[240:243]
ds_read_u16_d16_hi v62,v62
v_lshlrev_b32_sdwa v61,1,v61 dst_sel:DWORD dst_unused:UNUSED_PAD src0_sel:DWORD src1_sel:WORD_1
v_mfma_f32_16x16x32_bf16 v[244:247],a[240:243],v[40:43],v[244:247]
s_waitcnt lgkmcnt(7)
ds_read_u16 v61,v61
v_lshlrev_b32_sdwa v63,1,v63 dst_sel:DWORD dst_unused:UNUSED_PAD src0_sel:DWORD src1_sel:WORD_1
v_mfma_f32_16x16x32_bf16 v[180:183],a[224:227],v[40:43],v[180:183]
ds_read_u16_d16_hi v63,v63
v_lshlrev_b32_sdwa v32,1,v32 dst_sel:DWORD dst_unused:UNUSED_PAD src0_sel:DWORD src1_sel:WORD_1
v_mfma_f32_16x16x32_bf16 v[176:179],a[224:227],v[36:39],v[176:179]
v_rcp_f16_sdwa v9,v8 dst_sel:WORD_1 dst_unused:UNUSED_PRESERVE src0_sel:WORD_1
v_mfma_f32_16x16x32_bf16 v[248:251],a[232:235],v[36:39],v[248:251]
ds_read_u16 v32,v32
v_lshlrev_b32_sdwa v34,1,v34 dst_sel:DWORD dst_unused:UNUSED_PAD src0_sel:DWORD src1_sel:WORD_1
v_mfma_f32_16x16x32_bf16 v[252:255],a[232:235],v[40:43],v[252:255]
ds_read_u16_d16_hi v34,v34
v_fma_mix_f32 v33,v33,v9,0 op_sel_hi:[0,1,0]
v_mfma_f32_16x16x32_bf16 v[196:199],a[244:247],v[40:43],v[196:199]
ds_read_b64_tr_b16 a[192:193],v1 offset:18480
v_fma_mix_f32 v35,v35,v9,0 op_sel:[0,1,0] op_sel_hi:[0,1,0]
v_mfma_f32_16x16x32_bf16 v[192:195],a[244:247],v[36:39],v[192:195]
ds_read_b64_tr_b16 a[194:195],v1 offset:19504
v_mfma_f32_16x16x32_bf16 v[40:43],a[220:223],a[8:11],v[20:23]
ds_read_b64_tr_b16 a[196:197],v1 offset:20528
v_lshlrev_b32_sdwa v24,1,v24 dst_sel:DWORD dst_unused:UNUSED_PAD src0_sel:DWORD src1_sel:WORD_1
v_mfma_f32_16x16x32_bf16 v[40:43],a[216:219],a[24:27],v[40:43]
ds_read_b64_tr_b16 a[198:199],v1 offset:21552
v_fma_mixlo_f16 v8,v53,s99,0
v_mfma_f32_16x16x32_bf16 v[40:43],a[212:215],a[40:43],v[40:43]
s_waitcnt lgkmcnt(7)
ds_read_u16 v24,v24
v_lshl_add_u64 v[44:45],v[44:45],0,v[46:47]
v_lshlrev_b32_sdwa v26,1,v26 dst_sel:DWORD dst_unused:UNUSED_PAD src0_sel:DWORD src1_sel:WORD_1
v_mfma_f32_16x16x32_bf16 v[36:39],a[208:211],a[40:43],v[16:19]
ds_read_u16_d16_hi v26,v26
v_lshl_add_u64 v[46:47],v[48:49],0,v[50:51]
v_fma_mixhi_f16 v8,v55,s99,0
v_mfma_f32_16x16x32_bf16 v[36:39],a[204:207],a[24:27],v[36:39]
s_waitcnt lgkmcnt(8)
v_lshl_add_u64 v[48:49],v[60:61],0,v[62:63]
v_lshlrev_b32_sdwa v25,1,v25 dst_sel:DWORD dst_unused:UNUSED_PAD src0_sel:DWORD src1_sel:WORD_1
v_pk_fma_f16 v9,v8,v8,s54
v_mfma_f32_16x16x32_bf16 v[36:39],a[200:203],a[8:11],v[36:39]
ds_read_u16 v25,v25
v_lshlrev_b32_sdwa v27,1,v27 dst_sel:DWORD dst_unused:UNUSED_PAD src0_sel:DWORD src1_sel:WORD_1
v_mfma_f32_16x16x32_bf16 v[60:63],a[200:203],a[12:15],v[16:19]
s_waitcnt lgkmcnt(7)
ds_read_b128 v[16:19],v4 offset:512
v_or_b32 v50,v32,v34
v_pk_mul_f16 v9,v8,v9
v_mfma_f32_16x16x32_bf16 v[60:63],a[204:207],a[28:31],v[60:63]
ds_read_u16_d16_hi v27,v27
v_cvt_pk_bf16_f32 v51,v33,v35
v_lshlrev_b32_sdwa v28,1,v28 dst_sel:DWORD dst_unused:UNUSED_PAD src0_sel:DWORD src1_sel:WORD_1
v_mfma_f32_16x16x32_bf16 v[60:63],a[208:211],a[44:47],v[60:63]
ds_read_b64_tr_b16 a[200:201],v1 offset:22576
v_exp_f16 v8,v9
v_mfma_f32_16x16x32_bf16 v[32:35],a[212:215],a[44:47],v[20:23]
ds_read_b128 v[20:23],v4 offset:544
ds_read_b64_tr_b16 a[202:203],v1 offset:23600
v_lshlrev_b32_sdwa v30,1,v30 dst_sel:DWORD dst_unused:UNUSED_PAD src0_sel:DWORD src1_sel:WORD_1
v_mfma_f32_16x16x32_bf16 v[32:35],a[216:219],a[28:31],v[32:35]
ds_read_b64_tr_b16 a[204:205],v2 offset:22576
ds_read_b64_tr_b16 a[206:207],v2 offset:23600
v_lshlrev_b32_sdwa v29,1,v29 dst_sel:DWORD dst_unused:UNUSED_PAD src0_sel:DWORD src1_sel:WORD_1
v_mfma_f32_16x16x32_bf16 v[32:35],a[220:223],a[12:15],v[32:35]
ds_read_u16 v28,v28
v_exp_f16_sdwa v8,v9 dst_sel:WORD_1 dst_unused:UNUSED_PRESERVE src0_sel:WORD_1
v_mfma_f32_16x16x32_bf16 v[208:211],a[236:239],v[44:47],v[208:211]
ds_read_u16_d16_hi v30,v30
v_lshlrev_b32_sdwa v31,1,v31 dst_sel:DWORD dst_unused:UNUSED_PAD src0_sel:DWORD src1_sel:WORD_1
v_mfma_f32_16x16x32_bf16 v[212:215],a[236:239],v[48:51],v[212:215]
ds_read_u16 v29,v29
v_lshlrev_b32_sdwa v56,1,v56 dst_sel:DWORD dst_unused:UNUSED_PAD src0_sel:DWORD src1_sel:WORD_1
v_mfma_f32_16x16x32_bf16 v[172:175],a[252:255],v[48:51],v[172:175]
ds_read_u16_d16_hi v31,v31
v_pk_fma_f16 v8,v8,s55,s55
v_mfma_f32_16x16x32_bf16 v[168:171],a[252:255],v[44:47],v[168:171]
v_rcp_f16 v9,v8
v_lshlrev_b32_sdwa v58,1,v58 dst_sel:DWORD dst_unused:UNUSED_PAD src0_sel:DWORD src1_sel:WORD_1
v_mfma_f32_16x16x32_bf16 v[216:219],a[240:243],v[44:47],v[216:219]
ds_read_b64_tr_b16 a[208:209],v2 offset:20528
v_lshlrev_b32_sdwa v57,1,v57 dst_sel:DWORD dst_unused:UNUSED_PAD src0_sel:DWORD src1_sel:WORD_1
v_mfma_f32_16x16x32_bf16 v[220:223],a[240:243],v[48:51],v[220:223]
ds_read_b64_tr_b16 a[210:211],v2 offset:21552
v_lshlrev_b32_sdwa v59,1,v59 dst_sel:DWORD dst_unused:UNUSED_PAD src0_sel:DWORD src1_sel:WORD_1
v_mfma_f32_16x16x32_bf16 v[188:191],a[224:227],v[48:51],v[188:191]
ds_read_u16 v56,v56
v_lshlrev_b32_sdwa v52,1,v52 dst_sel:DWORD dst_unused:UNUSED_PAD src0_sel:DWORD src1_sel:WORD_1
v_mfma_f32_16x16x32_bf16 v[184:187],a[224:227],v[44:47],v[184:187]
ds_read_u16_d16_hi v58,v58
v_rcp_f16_sdwa v9,v8 dst_sel:WORD_1 dst_unused:UNUSED_PRESERVE src0_sel:WORD_1
v_mfma_f32_16x16x32_bf16 v[224:227],a[232:235],v[44:47],v[224:227]
ds_read_u16 v57,v57
ds_read_u16_d16_hi v59,v59
v_mfma_f32_16x16x32_bf16 v[228:231],a[232:235],v[48:51],v[228:231]
ds_read_b64_tr_b16 a[212:213],v2 offset:18480
ds_read_b64_tr_b16 a[214:215],v2 offset:19504
v_lshlrev_b32_sdwa v54,1,v54 dst_sel:DWORD dst_unused:UNUSED_PAD src0_sel:DWORD src1_sel:WORD_1
v_mfma_f32_16x16x32_bf16 v[204:207],a[244:247],v[48:51],v[204:207]
ds_read_u16 v52,v52
v_fma_mix_f32 v53,v53,v9,0 op_sel_hi:[0,1,0]
v_mfma_f32_16x16x32_bf16 v[200:203],a[244:247],v[44:47],v[200:203]
s_waitcnt lgkmcnt(9)
ds_read_u16_d16_hi v54,v54
v_fma_mix_f32 v55,v55,v9,0 op_sel:[0,1,0] op_sel_hi:[0,1,0]
v_mfma_f32_16x16x32_bf16 v[48:51],a[192:195],a[48:51],v[16:19]
s_add_u32 m0,s57,53040
v_lshlrev_b32_sdwa v40,1,v40 dst_sel:DWORD dst_unused:UNUSED_PAD src0_sel:DWORD src1_sel:WORD_1
v_fma_mixlo_f16 v8,v61,s99,0
v_mfma_f32_16x16x32_bf16 v[48:51],a[196:199],a[64:67],v[48:51]
s_add_u32 s89,s56,6389760
ds_read_u16 v40,v40
v_lshlrev_b32_sdwa v42,1,v42 dst_sel:DWORD dst_unused:UNUSED_PAD src0_sel:DWORD src1_sel:WORD_1
v_mfma_f32_16x16x32_bf16 v[48:51],a[200:203],a[80:83],v[48:51]
buffer_load_dwordx4 v6,s[36:39],s89 offen lds
v_lshl_add_u64 v[24:25],v[24:25],0,v[26:27]
v_mfma_f32_16x16x32_bf16 v[44:47],a[204:207],a[80:83],v[20:23]
s_waitcnt lgkmcnt(8)
ds_read_u16_d16_hi v42,v42
v_lshl_add_u64 v[26:27],v[28:29],0,v[30:31]
v_fma_mixhi_f16 v8,v63,s99,0
v_mfma_f32_16x16x32_bf16 v[44:47],a[208:211],a[64:67],v[44:47]
s_waitcnt lgkmcnt(4)
v_lshl_add_u64 v[28:29],v[56:57],0,v[58:59]
v_lshlrev_b32_sdwa v41,1,v41 dst_sel:DWORD dst_unused:UNUSED_PAD src0_sel:DWORD src1_sel:WORD_1
v_pk_fma_f16 v9,v8,v8,s54
v_mfma_f32_16x16x32_bf16 v[44:47],a[212:215],a[48:51],v[44:47]
ds_read_u16 v41,v41
v_lshlrev_b32_sdwa v43,1,v43 dst_sel:DWORD dst_unused:UNUSED_PAD src0_sel:DWORD src1_sel:WORD_1
v_mfma_f32_16x16x32_bf16 v[56:59],a[212:215],a[52:55],v[20:23]
s_waitcnt lgkmcnt(3)
ds_read_u16_d16_hi v43,v43
v_or_b32 v30,v52,v54
v_pk_mul_f16 v9,v8,v9
v_mfma_f32_16x16x32_bf16 v[56:59],a[208:211],a[68:71],v[56:59]
s_waitcnt vmcnt(13)
v_cvt_pk_bf16_f32 v31,v53,v55
v_lshlrev_b32_sdwa v36,1,v36 dst_sel:DWORD dst_unused:UNUSED_PAD src0_sel:DWORD src1_sel:WORD_1
v_mfma_f32_16x16x32_bf16 v[56:59],a[204:207],a[84:87],v[56:59]
s_barrier
v_exp_f16 v8,v9
v_mfma_f32_16x16x32_bf16 v[52:55],a[200:203],a[84:87],v[16:19]
ds_read_u16 v36,v36
v_lshlrev_b32_sdwa v38,1,v38 dst_sel:DWORD dst_unused:UNUSED_PAD src0_sel:DWORD src1_sel:WORD_1
v_mfma_f32_16x16x32_bf16 v[52:55],a[196:199],a[68:71],v[52:55]
s_add_u32 m0,s58,18480
ds_read_u16_d16_hi v38,v38
v_lshlrev_b32_sdwa v37,1,v37 dst_sel:DWORD dst_unused:UNUSED_PAD src0_sel:DWORD src1_sel:WORD_1
v_mfma_f32_16x16x32_bf16 v[52:55],a[192:195],a[52:55],v[52:55]
s_add_u32 s69,s69,s59
v_exp_f16_sdwa v8,v9 dst_sel:WORD_1 dst_unused:UNUSED_PRESERVE src0_sel:WORD_1
v_mfma_f32_16x16x32_bf16 v[136:139],a[236:239],v[24:27],v[136:139]
TP7101_%=:
s_cbranch_execz TR0_%=
buffer_load_dwordx4 v5,s[44:47],s69 offen offset:0 lds
v_lshlrev_b32_sdwa v39,1,v39 dst_sel:DWORD dst_unused:UNUSED_PAD src0_sel:DWORD src1_sel:WORD_1
v_mfma_f32_16x16x32_bf16 v[140:143],a[236:239],v[28:31],v[140:143]
ds_read_u16 v37,v37
v_lshlrev_b32_sdwa v32,1,v32 dst_sel:DWORD dst_unused:UNUSED_PAD src0_sel:DWORD src1_sel:WORD_1
v_mfma_f32_16x16x32_bf16 v[68:71],a[252:255],v[28:31],v[68:71]
ds_read_u16_d16_hi v39,v39
v_pk_fma_f16 v8,v8,s55,s55
v_mfma_f32_16x16x32_bf16 v[64:67],a[252:255],v[24:27],v[64:67]
v_rcp_f16 v9,v8
v_lshlrev_b32_sdwa v34,1,v34 dst_sel:DWORD dst_unused:UNUSED_PAD src0_sel:DWORD src1_sel:WORD_1
v_mfma_f32_16x16x32_bf16 v[144:147],a[240:243],v[24:27],v[144:147]
s_add_u32 s69,s69,s53
ds_read_u16 v32,v32
v_lshlrev_b32_sdwa v33,1,v33 dst_sel:DWORD dst_unused:UNUSED_PAD src0_sel:DWORD src1_sel:WORD_1
v_mfma_f32_16x16x32_bf16 v[148:151],a[240:243],v[28:31],v[148:151]
buffer_load_dwordx4 v5,s[44:47],s69 offen offset:1024 lds
v_lshlrev_b32_sdwa v35,1,v35 dst_sel:DWORD dst_unused:UNUSED_PAD src0_sel:DWORD src1_sel:WORD_1
v_mfma_f32_16x16x32_bf16 v[84:87],a[224:227],v[28:31],v[84:87]
ds_read_u16_d16_hi v34,v34
v_lshlrev_b32_sdwa v60,1,v60 dst_sel:DWORD dst_unused:UNUSED_PAD src0_sel:DWORD src1_sel:WORD_1
v_mfma_f32_16x16x32_bf16 v[80:83],a[224:227],v[24:27],v[80:83]
v_rcp_f16_sdwa v9,v8 dst_sel:WORD_1 dst_unused:UNUSED_PRESERVE src0_sel:WORD_1
v_mfma_f32_16x16x32_bf16 v[152:155],a[232:235],v[24:27],v[152:155]
ds_read_u16 v33,v33
ds_read_u16_d16_hi v35,v35
v_mfma_f32_16x16x32_bf16 v[156:159],a[232:235],v[28:31],v[156:159]
s_add_u32 s69,s69,s53
ds_read_u16 v60,v60
v_lshlrev_b32_sdwa v62,1,v62 dst_sel:DWORD dst_unused:UNUSED_PAD src0_sel:DWORD src1_sel:WORD_1
v_mfma_f32_16x16x32_bf16 v[100:103],a[244:247],v[28:31],v[100:103]
buffer_load_dwordx4 v5,s[44:47],s69 offen offset:2048 lds
v_fma_mix_f32 v61,v61,v9,0 op_sel_hi:[0,1,0]
v_mfma_f32_16x16x32_bf16 v[96:99],a[244:247],v[24:27],v[96:99]
ds_read_u16_d16_hi v62,v62
v_fma_mix_f32 v63,v63,v9,0 op_sel:[0,1,0] op_sel_hi:[0,1,0]
v_mfma_f32_16x16x32_bf16 v[28:31],a[192:195],a[56:59],v[16:19]
ds_read_b64_tr_b16 a[228:229],v3 offset:0
v_lshlrev_b32_sdwa v48,1,v48 dst_sel:DWORD dst_unused:UNUSED_PAD src0_sel:DWORD src1_sel:WORD_1
v_mfma_f32_16x16x32_bf16 v[28:31],a[196:199],a[72:75],v[28:31]
ds_read_u16 v48,v48
v_fma_mixlo_f16 v8,v57,s99,0
v_mfma_f32_16x16x32_bf16 v[28:31],a[200:203],a[88:91],v[28:31]
s_waitcnt lgkmcnt(7)
ds_read_b64_tr_b16 a[230:231],v3 offset:1536
v_lshl_add_u64 v[40:41],v[40:41],0,v[42:43]
v_lshlrev_b32_sdwa v50,1,v50 dst_sel:DWORD dst_unused:UNUSED_PAD src0_sel:DWORD src1_sel:WORD_1
v_mfma_f32_16x16x32_bf16 v[24:27],a[204:207],a[88:91],v[20:23]
ds_read_u16_d16_hi v50,v50
v_lshl_add_u64 v[42:43],v[36:37],0,v[38:39]
v_fma_mixhi_f16 v8,v59,s99,0
v_mfma_f32_16x16x32_bf16 v[24:27],a[208:211],a[72:75],v[24:27]
s_waitcnt lgkmcnt(6)
ds_read_b64_tr_b16 a[248:249],v3 offset:8
v_lshl_add_u64 v[36:37],v[32:33],0,v[34:35]
v_lshlrev_b32_sdwa v49,1,v49 dst_sel:DWORD dst_unused:UNUSED_PAD src0_sel:DWORD src1_sel:WORD_1
v_pk_fma_f16 v9,v8,v8,s54
v_mfma_f32_16x16x32_bf16 v[24:27],a[212:215],a[56:59],v[24:27]
ds_read_u16 v49,v49
v_lshlrev_b32_sdwa v51,1,v51 dst_sel:DWORD dst_unused:UNUSED_PAD src0_sel:DWORD src1_sel:WORD_1
v_mfma_f32_16x16x32_bf16 v[32:35],a[212:215],a[60:63],v[20:23]
s_waitcnt lgkmcnt(6)
ds_read_u16_d16_hi v51,v51
v_or_b32 v38,v60,v62
v_pk_mul_f16 v9,v8,v9
v_mfma_f32_16x16x32_bf16 v[32:35],a[208:211],a[76:79],v[32:35]
ds_read_b64_tr_b16 a[250:251],v3 offset:1544
v_cvt_pk_bf16_f32 v39,v61,v63
v_lshlrev_b32_sdwa v44,1,v44 dst_sel:DWORD dst_unused:UNUSED_PAD src0_sel:DWORD src1_sel:WORD_1
v_mfma_f32_16x16x32_bf16 v[32:35],a[204:207],a[92:95],v[32:35]
v_exp_f16 v8,v9
v_mfma_f32_16x16x32_bf16 v[60:63],a[200:203],a[92:95],v[16:19]
ds_read_u16 v44,v44
v_lshlrev_b32_sdwa v46,1,v46 dst_sel:DWORD dst_unused:UNUSED_PAD src0_sel:DWORD src1_sel:WORD_1
v_mfma_f32_16x16x32_bf16 v[60:63],a[196:199],a[76:79],v[60:63]
ds_read_u16_d16_hi v46,v46
v_lshlrev_b32_sdwa v45,1,v45 dst_sel:DWORD dst_unused:UNUSED_PAD src0_sel:DWORD src1_sel:WORD_1
v_mfma_f32_16x16x32_bf16 v[60:63],a[192:195],a[60:63],v[60:63]
v_exp_f16_sdwa v8,v9 dst_sel:WORD_1 dst_unused:UNUSED_PRESERVE src0_sel:WORD_1
v_mfma_f32_16x16x32_bf16 v[112:115],a[236:239],v[40:43],v[112:115]
ds_read_u16 v45,v45
v_lshlrev_b32_sdwa v47,1,v47 dst_sel:DWORD dst_unused:UNUSED_PAD src0_sel:DWORD src1_sel:WORD_1
v_mfma_f32_16x16x32_bf16 v[116:119],a[236:239],v[36:39],v[116:119]
ds_read_u16_d16_hi v47,v47
v_lshlrev_b32_sdwa v52,1,v52 dst_sel:DWORD dst_unused:UNUSED_PAD src0_sel:DWORD src1_sel:WORD_1
v_mfma_f32_16x16x32_bf16 v[76:79],a[252:255],v[36:39],v[76:79]
ds_read_u16 v52,v52
v_pk_fma_f16 v8,v8,s55,s55
v_mfma_f32_16x16x32_bf16 v[72:75],a[252:255],v[40:43],v[72:75]
v_rcp_f16 v9,v8
v_lshlrev_b32_sdwa v54,1,v54 dst_sel:DWORD dst_unused:UNUSED_PAD src0_sel:DWORD src1_sel:WORD_1
v_mfma_f32_16x16x32_bf16 v[120:123],a[240:243],v[40:43],v[120:123]
ds_read_u16_d16_hi v54,v54
v_lshlrev_b32_sdwa v53,1,v53 dst_sel:DWORD dst_unused:UNUSED_PAD src0_sel:DWORD src1_sel:WORD_1
v_mfma_f32_16x16x32_bf16 v[124:127],a[240:243],v[36:39],v[124:127]
ds_read_u16 v53,v53
ds_read_b64_tr_b16 a[236:237],v3 offset:64
v_mfma_f32_16x16x32_bf16 v[92:95],a[224:227],v[36:39],v[92:95]
ds_read_b64_tr_b16 a[238:239],v3 offset:1600
v_lshlrev_b32_sdwa v55,1,v55 dst_sel:DWORD dst_unused:UNUSED_PAD src0_sel:DWORD src1_sel:WORD_1
v_mfma_f32_16x16x32_bf16 v[88:91],a[224:227],v[40:43],v[88:91]
ds_read_u16_d16_hi v55,v55
v_rcp_f16_sdwa v9,v8 dst_sel:WORD_1 dst_unused:UNUSED_PRESERVE src0_sel:WORD_1
v_mfma_f32_16x16x32_bf16 v[128:131],a[232:235],v[40:43],v[128:131]
ds_read_b64_tr_b16 a[252:253],v3 offset:72
v_lshlrev_b32_sdwa v56,1,v56 dst_sel:DWORD dst_unused:UNUSED_PAD src0_sel:DWORD src1_sel:WORD_1
v_mfma_f32_16x16x32_bf16 v[132:135],a[232:235],v[36:39],v[132:135]
ds_read_u16 v56,v56
v_lshlrev_b32_sdwa v58,1,v58 dst_sel:DWORD dst_unused:UNUSED_PAD src0_sel:DWORD src1_sel:WORD_1
v_mfma_f32_16x16x32_bf16 v[108:111],a[244:247],v[36:39],v[108:111]
ds_read_u16_d16_hi v58,v58
v_fma_mix_f32 v57,v57,v9,0 op_sel_hi:[0,1,0]
v_mfma_f32_16x16x32_bf16 v[104:107],a[244:247],v[40:43],v[104:107]
ds_read_b64_tr_b16 a[254:255],v3 offset:1608
v_fma_mix_f32 v59,v59,v9,0 op_sel:[0,1,0] op_sel_hi:[0,1,0]
v_mfma_f32_16x16x32_bf16 v[36:39],a[192:195],a[0:3],v[16:19]
ds_read_b64_tr_b16 a[240:241],v3 offset:128
v_lshlrev_b32_sdwa v28,1,v28 dst_sel:DWORD dst_unused:UNUSED_PAD src0_sel:DWORD src1_sel:WORD_1
v_mfma_f32_16x16x32_bf16 v[36:39],a[196:199],a[16:19],v[36:39]
ds_read_b64_tr_b16 a[242:243],v3 offset:1664
v_fma_mixlo_f16 v8,v33,s99,0
v_mfma_f32_16x16x32_bf16 v[36:39],a[200:203],a[32:35],v[36:39]
s_waitcnt lgkmcnt(8)
ds_read_b64_tr_b16 a[224:225],v3 offset:136
v_lshl_add_u64 v[48:49],v[48:49],0,v[50:51]
v_lshlrev_b32_sdwa v30,1,v30 dst_sel:DWORD dst_unused:UNUSED_PAD src0_sel:DWORD src1_sel:WORD_1
v_mfma_f32_16x16x32_bf16 v[40:43],a[204:207],a[32:35],v[20:23]
ds_read_b64_tr_b16 a[226:227],v3 offset:1672
v_lshl_add_u64 v[50:51],v[44:45],0,v[46:47]
v_fma_mixhi_f16 v8,v35,s99,0
v_mfma_f32_16x16x32_bf16 v[40:43],a[208:211],a[16:19],v[40:43]
s_waitcnt lgkmcnt(8)
ds_read_u16 v28,v28
v_lshl_add_u64 v[44:45],v[52:53],0,v[54:55]
v_lshlrev_b32_sdwa v29,1,v29 dst_sel:DWORD dst_unused:UNUSED_PAD src0_sel:DWORD src1_sel:WORD_1
v_pk_fma_f16 v9,v8,v8,s54
v_mfma_f32_16x16x32_bf16 v[40:43],a[212:215],a[0:3],v[40:43]
ds_read_u16_d16_hi v30,v30
v_lshlrev_b32_sdwa v31,1,v31 dst_sel:DWORD dst_unused:UNUSED_PAD src0_sel:DWORD src1_sel:WORD_1
v_mfma_f32_16x16x32_bf16 v[52:55],a[212:215],a[4:7],v[20:23]
s_waitcnt lgkmcnt(7)
ds_read_u16 v29,v29
v_or_b32 v46,v56,v58
v_pk_mul_f16 v9,v8,v9
v_mfma_f32_16x16x32_bf16 v[52:55],a[208:211],a[20:23],v[52:55]
ds_read_u16_d16_hi v31,v31
v_cvt_pk_bf16_f32 v47,v57,v59
v_lshlrev_b32_sdwa v24,1,v24 dst_sel:DWORD dst_unused:UNUSED_PAD src0_sel:DWORD src1_sel:WORD_1
v_mfma_f32_16x16x32_bf16 v[52:55],a[204:207],a[36:39],v[52:55]
v_exp_f16 v8,v9
v_mfma_f32_16x16x32_bf16 v[56:59],a[200:203],a[36:39],v[16:19]
ds_read_u16 v24,v24
v_lshlrev_b32_sdwa v26,1,v26 dst_sel:DWORD dst_unused:UNUSED_PAD src0_sel:DWORD src1_sel:WORD_1
v_mfma_f32_16x16x32_bf16 v[56:59],a[196:199],a[20:23],v[56:59]
ds_read_u16_d16_hi v26,v26
v_lshlrev_b32_sdwa v25,1,v25 dst_sel:DWORD dst_unused:UNUSED_PAD src0_sel:DWORD src1_sel:WORD_1
v_mfma_f32_16x16x32_bf16 v[56:59],a[192:195],a[4:7],v[56:59]
v_exp_f16_sdwa v8,v9 dst_sel:WORD_1 dst_unused:UNUSED_PRESERVE src0_sel:WORD_1
v_mfma_f32_16x16x32_bf16 v[232:235],a[228:231],v[48:51],v[232:235]
ds_read_u16 v25,v25
v_lshlrev_b32_sdwa v27,1,v27 dst_sel:DWORD dst_unused:UNUSED_PAD src0_sel:DWORD src1_sel:WORD_1
v_mfma_f32_16x16x32_bf16 v[236:239],a[228:231],v[44:47],v[236:239]
ds_read_u16_d16_hi v27,v27
v_lshlrev_b32_sdwa v60,1,v60 dst_sel:DWORD dst_unused:UNUSED_PAD src0_sel:DWORD src1_sel:WORD_1
v_mfma_f32_16x16x32_bf16 v[164:167],a[248:251],v[44:47],v[164:167]
ds_read_u16 v60,v60
v_pk_fma_f16 v8,v8,s55,s55
v_mfma_f32_16x16x32_bf16 v[160:163],a[248:251],v[48:51],v[160:163]
v_rcp_f16 v9,v8
v_lshlrev_b32_sdwa v62,1,v62 dst_sel:DWORD dst_unused:UNUSED_PAD src0_sel:DWORD src1_sel:WORD_1
v_mfma_f32_16x16x32_bf16 v[240:243],a[236:239],v[48:51],v[240:243]
ds_read_u16_d16_hi v62,v62
v_lshlrev_b32_sdwa v61,1,v61 dst_sel:DWORD dst_unused:UNUSED_PAD src0_sel:DWORD src1_sel:WORD_1
v_mfma_f32_16x16x32_bf16 v[244:247],a[236:239],v[44:47],v[244:247]
s_waitcnt lgkmcnt(6)
ds_read_u16 v61,v61
v_lshlrev_b32_sdwa v63,1,v63 dst_sel:DWORD dst_unused:UNUSED_PAD src0_sel:DWORD src1_sel:WORD_1
v_mfma_f32_16x16x32_bf16 v[180:183],a[252:255],v[44:47],v[180:183]
ds_read_u16_d16_hi v63,v63
v_lshlrev_b32_sdwa v32,1,v32 dst_sel:DWORD dst_unused:UNUSED_PAD src0_sel:DWORD src1_sel:WORD_1
v_mfma_f32_16x16x32_bf16 v[176:179],a[252:255],v[48:51],v[176:179]
v_rcp_f16_sdwa v9,v8 dst_sel:WORD_1 dst_unused:UNUSED_PRESERVE src0_sel:WORD_1
v_mfma_f32_16x16x32_bf16 v[248:251],a[240:243],v[48:51],v[248:251]
ds_read_u16 v32,v32
v_lshlrev_b32_sdwa v34,1,v34 dst_sel:DWORD dst_unused:UNUSED_PAD src0_sel:DWORD src1_sel:WORD_1
v_mfma_f32_16x16x32_bf16 v[252:255],a[240:243],v[44:47],v[252:255]
ds_read_u16_d16_hi v34,v34
v_fma_mix_f32 v33,v33,v9,0 op_sel_hi:[0,1,0]
v_mfma_f32_16x16x32_bf16 v[196:199],a[224:227],v[44:47],v[196:199]
ds_read_b64_tr_b16 a[216:217],v1 offset:0
v_fma_mix_f32 v35,v35,v9,0 op_sel:[0,1,0] op_sel_hi:[0,1,0]
v_mfma_f32_16x16x32_bf16 v[192:195],a[224:227],v[48:51],v[192:195]
ds_read_b64_tr_b16 a[218:219],v1 offset:1024
v_mfma_f32_16x16x32_bf16 v[44:47],a[192:195],a[8:11],v[16:19]
ds_read_b64_tr_b16 a[220:221],v1 offset:2048
v_mul_f32_dpp v232,v14,v232 row_newbcast:0 row_mask:0xf bank_mask:0xf
s_add_u32 s10,s61,1572864
v_mul_f32_dpp v233,v14,v233 row_newbcast:1 row_mask:0xf bank_mask:0xf
v_mul_f32_dpp v234,v14,v234 row_newbcast:2 row_mask:0xf bank_mask:0xf
v_lshlrev_b32_sdwa v36,1,v36 dst_sel:DWORD dst_unused:UNUSED_PAD src0_sel:DWORD src1_sel:WORD_1
v_mfma_f32_16x16x32_bf16 v[44:47],a[196:199],a[24:27],v[44:47]
ds_read_b64_tr_b16 a[222:223],v1 offset:3072
v_mul_f32_dpp v235,v14,v235 row_newbcast:3 row_mask:0xf bank_mask:0xf
v_mul_f32_dpp v160,v14,v160 row_newbcast:4 row_mask:0xf bank_mask:0xf
v_fma_mixlo_f16 v8,v53,s99,0
v_mul_f32_dpp v161,v14,v161 row_newbcast:5 row_mask:0xf bank_mask:0xf
v_mfma_f32_16x16x32_bf16 v[44:47],a[200:203],a[40:43],v[44:47]
ds_read_u16 v36,v36
v_lshl_add_u64 v[28:29],v[28:29],0,v[30:31]
v_mul_f32_dpp v162,v14,v162 row_newbcast:6 row_mask:0xf bank_mask:0xf
v_mul_f32_dpp v163,v14,v163 row_newbcast:7 row_mask:0xf bank_mask:0xf
v_lshlrev_b32_sdwa v38,1,v38 dst_sel:DWORD dst_unused:UNUSED_PAD src0_sel:DWORD src1_sel:WORD_1
v_cvt_pk_bf16_f32 v232,v232,v233
v_mfma_f32_16x16x32_bf16 v[48:51],a[204:207],a[40:43],v[20:23]
s_waitcnt lgkmcnt(7)
ds_read_u16_d16_hi v38,v38
v_lshl_add_u64 v[30:31],v[24:25],0,v[26:27]
v_cvt_pk_bf16_f32 v233,v234,v235
v_cvt_pk_bf16_f32 v234,v160,v161
v_fma_mixhi_f16 v8,v55,s99,0
v_cvt_pk_bf16_f32 v235,v162,v163
v_mfma_f32_16x16x32_bf16 v[48:51],a[208:211],a[24:27],v[48:51]
buffer_store_dwordx4 v[232:235],v7,s[40:43],s10 offen offset:0 sc1
v_lshl_add_u64 v[24:25],v[60:61],0,v[62:63]
v_mul_f32_dpp v240,v14,v240 row_newbcast:8 row_mask:0xf bank_mask:0xf
v_mul_f32_dpp v241,v14,v241 row_newbcast:9 row_mask:0xf bank_mask:0xf
v_lshlrev_b32_sdwa v37,1,v37 dst_sel:DWORD dst_unused:UNUSED_PAD src0_sel:DWORD src1_sel:WORD_1
v_pk_fma_f16 v9,v8,v8,s54
v_mul_f32_dpp v242,v14,v242 row_newbcast:10 row_mask:0xf bank_mask:0xf
v_mfma_f32_16x16x32_bf16 v[48:51],a[212:215],a[8:11],v[48:51]
ds_read_u16 v37,v37
v_mul_f32_dpp v243,v14,v243 row_newbcast:11 row_mask:0xf bank_mask:0xf
v_mul_f32_dpp v176,v14,v176 row_newbcast:12 row_mask:0xf bank_mask:0xf
v_lshlrev_b32_sdwa v39,1,v39 dst_sel:DWORD dst_unused:UNUSED_PAD src0_sel:DWORD src1_sel:WORD_1
v_mul_f32_dpp v177,v14,v177 row_newbcast:13 row_mask:0xf bank_mask:0xf
v_mfma_f32_16x16x32_bf16 v[60:63],a[212:215],a[12:15],v[20:23]
s_waitcnt lgkmcnt(7)
ds_read_b128 v[20:23],v4 offset:640
v_or_b32 v26,v32,v34
v_mul_f32_dpp v178,v14,v178 row_newbcast:14 row_mask:0xf bank_mask:0xf
v_mul_f32_dpp v179,v14,v179 row_newbcast:15 row_mask:0xf bank_mask:0xf
v_pk_mul_f16 v9,v8,v9
v_cvt_pk_bf16_f32 v240,v240,v241
v_mfma_f32_16x16x32_bf16 v[60:63],a[208:211],a[28:31],v[60:63]
ds_read_u16_d16_hi v39,v39
v_cvt_pk_bf16_f32 v27,v33,v35
v_cvt_pk_bf16_f32 v241,v242,v243
v_cvt_pk_bf16_f32 v242,v176,v177
v_lshlrev_b32_sdwa v40,1,v40 dst_sel:DWORD dst_unused:UNUSED_PAD src0_sel:DWORD src1_sel:WORD_1
v_cvt_pk_bf16_f32 v243,v178,v179
v_mfma_f32_16x16x32_bf16 v[60:63],a[204:207],a[44:47],v[60:63]
ds_read_b64_tr_b16 a[212:213],v1 offset:4096
v_exp_f16 v8,v9
v_mul_f32_dpp v248,v15,v248 row_newbcast:0 row_mask:0xf bank_mask:0xf
v_mul_f32_dpp v249,v15,v249 row_newbcast:1 row_mask:0xf bank_mask:0xf
v_mul_f32_dpp v250,v15,v250 row_newbcast:2 row_mask:0xf bank_mask:0xf
v_mfma_f32_16x16x32_bf16 v[32:35],a[200:203],a[44:47],v[16:19]
ds_read_b128 v[16:19],v4 offset:672
ds_read_b64_tr_b16 a[214:215],v1 offset:5120
v_mul_f32_dpp v251,v15,v251 row_newbcast:3 row_mask:0xf bank_mask:0xf
v_mul_f32_dpp v192,v15,v192 row_newbcast:4 row_mask:0xf bank_mask:0xf
v_lshlrev_b32_sdwa v42,1,v42 dst_sel:DWORD dst_unused:UNUSED_PAD src0_sel:DWORD src1_sel:WORD_1
v_mul_f32_dpp v193,v15,v193 row_newbcast:5 row_mask:0xf bank_mask:0xf
v_mfma_f32_16x16x32_bf16 v[32:35],a[196:199],a[28:31],v[32:35]
ds_read_b64_tr_b16 a[208:209],v2 offset:4096
ds_read_b64_tr_b16 a[210:211],v2 offset:5120
v_mul_f32_dpp v194,v15,v194 row_newbcast:6 row_mask:0xf bank_mask:0xf
v_mul_f32_dpp v195,v15,v195 row_newbcast:7 row_mask:0xf bank_mask:0xf
v_lshlrev_b32_sdwa v41,1,v41 dst_sel:DWORD dst_unused:UNUSED_PAD src0_sel:DWORD src1_sel:WORD_1
v_cvt_pk_bf16_f32 v248,v248,v249
v_mfma_f32_16x16x32_bf16 v[32:35],a[192:195],a[12:15],v[32:35]
ds_read_u16 v40,v40
v_exp_f16_sdwa v8,v9 dst_sel:WORD_1 dst_unused:UNUSED_PRESERVE src0_sel:WORD_1
v_cvt_pk_bf16_f32 v249,v250,v251
v_cvt_pk_bf16_f32 v250,v192,v193
v_cvt_pk_bf16_f32 v251,v194,v195
v_mfma_f32_16x16x32_bf16 v[208:211],a[228:231],v[28:31],v[208:211]
ds_read_u16_d16_hi v42,v42
v_mul_f32_dpp v236,v14,v236 row_newbcast:0 row_mask:0xf bank_mask:0xf
v_mul_f32_dpp v237,v14,v237 row_newbcast:1 row_mask:0xf bank_mask:0xf
v_lshlrev_b32_sdwa v43,1,v43 dst_sel:DWORD dst_unused:UNUSED_PAD src0_sel:DWORD src1_sel:WORD_1
v_mul_f32_dpp v238,v14,v238 row_newbcast:2 row_mask:0xf bank_mask:0xf
v_mfma_f32_16x16x32_bf16 v[212:215],a[228:231],v[24:27],v[212:215]
ds_read_u16 v41,v41
v_mul_f32_dpp v239,v14,v239 row_newbcast:3 row_mask:0xf bank_mask:0xf
v_mul_f32_dpp v164,v14,v164 row_newbcast:4 row_mask:0xf bank_mask:0xf
v_lshlrev_b32_sdwa v56,1,v56 dst_sel:DWORD dst_unused:UNUSED_PAD src0_sel:DWORD src1_sel:WORD_1
v_mul_f32_dpp v165,v14,v165 row_newbcast:5 row_mask:0xf bank_mask:0xf
v_mfma_f32_16x16x32_bf16 v[172:175],a[248:251],v[24:27],v[172:175]
ds_read_u16_d16_hi v43,v43
v_mul_f32_dpp v166,v14,v166 row_newbcast:6 row_mask:0xf bank_mask:0xf
v_mul_f32_dpp v167,v14,v167 row_newbcast:7 row_mask:0xf bank_mask:0xf
v_pk_fma_f16 v8,v8,s55,s55
v_cvt_pk_bf16_f32 v236,v236,v237
v_mfma_f32_16x16x32_bf16 v[168:171],a[248:251],v[28:31],v[168:171]
v_rcp_f16 v9,v8
v_cvt_pk_bf16_f32 v237,v238,v239
v_cvt_pk_bf16_f32 v238,v164,v165
v_lshlrev_b32_sdwa v58,1,v58 dst_sel:DWORD dst_unused:UNUSED_PAD src0_sel:DWORD src1_sel:WORD_1
v_cvt_pk_bf16_f32 v239,v166,v167
v_mfma_f32_16x16x32_bf16 v[216:219],a[236:239],v[28:31],v[216:219]
buffer_store_dwordx4 v[240:243],v7,s[40:43],s10 offen offset:64 sc1
ds_read_b64_tr_b16 a[204:205],v2 offset:2048
v_mul_f32_dpp v244,v14,v244 row_newbcast:8 row_mask:0xf bank_mask:0xf
v_mul_f32_dpp v245,v14,v245 row_newbcast:9 row_mask:0xf bank_mask:0xf
v_mul_f32_dpp v246,v14,v246 row_newbcast:10 row_mask:0xf bank_mask:0xf
v_mfma_f32_16x16x32_bf16 v[220:223],a[236:239],v[24:27],v[220:223]
ds_read_b64_tr_b16 a[206:207],v2 offset:3072
v_mul_f32_dpp v247,v14,v247 row_newbcast:11 row_mask:0xf bank_mask:0xf
v_mul_f32_dpp v180,v14,v180 row_newbcast:12 row_mask:0xf bank_mask:0xf
v_lshlrev_b32_sdwa v57,1,v57 dst_sel:DWORD dst_unused:UNUSED_PAD src0_sel:DWORD src1_sel:WORD_1
v_mul_f32_dpp v181,v14,v181 row_newbcast:13 row_mask:0xf bank_mask:0xf
v_mfma_f32_16x16x32_bf16 v[188:191],a[252:255],v[24:27],v[188:191]
ds_read_u16 v56,v56
v_mul_f32_dpp v182,v14,v182 row_newbcast:14 row_mask:0xf bank_mask:0xf
v_mul_f32_dpp v183,v14,v183 row_newbcast:15 row_mask:0xf bank_mask:0xf
v_lshlrev_b32_sdwa v59,1,v59 dst_sel:DWORD dst_unused:UNUSED_PAD src0_sel:DWORD src1_sel:WORD_1
v_cvt_pk_bf16_f32 v244,v244,v245
v_mfma_f32_16x16x32_bf16 v[184:187],a[252:255],v[28:31],v[184:187]
v_rcp_f16_sdwa v9,v8 dst_sel:WORD_1 dst_unused:UNUSED_PRESERVE src0_sel:WORD_1
v_cvt_pk_bf16_f32 v245,v246,v247
v_cvt_pk_bf16_f32 v246,v180,v181
v_cvt_pk_bf16_f32 v247,v182,v183
v_mfma_f32_16x16x32_bf16 v[224:227],a[240:243],v[28:31],v[224:227]
ds_read_u16_d16_hi v58,v58
ds_read_u16 v57,v57
v_mul_f32_dpp v252,v15,v252 row_newbcast:0 row_mask:0xf bank_mask:0xf
v_mul_f32_dpp v253,v15,v253 row_newbcast:1 row_mask:0xf bank_mask:0xf
v_lshlrev_b32_sdwa v52,1,v52 dst_sel:DWORD dst_unused:UNUSED_PAD src0_sel:DWORD src1_sel:WORD_1
v_mul_f32_dpp v254,v15,v254 row_newbcast:2 row_mask:0xf bank_mask:0xf
v_mfma_f32_16x16x32_bf16 v[228:231],a[240:243],v[24:27],v[228:231]
ds_read_u16_d16_hi v59,v59
ds_read_b64_tr_b16 a[200:201],v2 offset:0
v_mul_f32_dpp v255,v15,v255 row_newbcast:3 row_mask:0xf bank_mask:0xf
v_mul_f32_dpp v196,v15,v196 row_newbcast:4 row_mask:0xf bank_mask:0xf
v_mul_f32_dpp v197,v15,v197 row_newbcast:5 row_mask:0xf bank_mask:0xf
v_lshlrev_b32_sdwa v54,1,v54 dst_sel:DWORD dst_unused:UNUSED_PAD src0_sel:DWORD src1_sel:WORD_1
v_mfma_f32_16x16x32_bf16 v[204:207],a[224:227],v[24:27],v[204:207]
ds_read_b64_tr_b16 a[202:203],v2 offset:1024
v_mul_f32_dpp v198,v15,v198 row_newbcast:6 row_mask:0xf bank_mask:0xf
v_mul_f32_dpp v199,v15,v199 row_newbcast:7 row_mask:0xf bank_mask:0xf
v_fma_mix_f32 v53,v53,v9,0 op_sel_hi:[0,1,0]
v_cvt_pk_bf16_f32 v252,v252,v253
v_mfma_f32_16x16x32_bf16 v[200:203],a[224:227],v[28:31],v[200:203]
s_waitcnt lgkmcnt(8)
ds_read_u16 v52,v52
ds_read_u16_d16_hi v54,v54
v_cvt_pk_bf16_f32 v253,v254,v255
v_cvt_pk_bf16_f32 v254,v196,v197
v_cvt_pk_bf16_f32 v255,v198,v199
v_fma_mix_f32 v55,v55,v9,0 op_sel:[0,1,0] op_sel_hi:[0,1,0]
v_mfma_f32_16x16x32_bf16 v[24:27],a[216:219],a[0:3],v[20:23]
v_mul_f32_dpp v208,v14,v208 row_newbcast:0 row_mask:0xf bank_mask:0xf
s_add_u32 s11,s61,1671168
v_accvgpr_write_b32 a64,0
v_accvgpr_write_b32 a65,0
v_lshlrev_b32_sdwa v44,1,v44 dst_sel:DWORD dst_unused:UNUSED_PAD src0_sel:DWORD src1_sel:WORD_1
v_mfma_f32_16x16x32_bf16 v[24:27],a[220:223],a[16:19],v[24:27]
ds_read_u16 v44,v44
v_accvgpr_write_b32 a66,0
v_accvgpr_write_b32 a67,0
v_fma_mixlo_f16 v8,v61,s99,0
v_mul_f32_dpp v209,v14,v209 row_newbcast:1 row_mask:0xf bank_mask:0xf
v_mfma_f32_16x16x32_bf16 v[24:27],a[212:215],a[32:35],v[24:27]
buffer_store_dwordx4 v[248:251],v7,s[40:43],s10 offen offset:128 sc1
v_lshl_add_u64 v[36:37],v[36:37],0,v[38:39]
v_mul_f32_dpp v210,v14,v210 row_newbcast:2 row_mask:0xf bank_mask:0xf
v_mul_f32_dpp v211,v14,v211 row_newbcast:3 row_mask:0xf bank_mask:0xf
v_lshlrev_b32_sdwa v46,1,v46 dst_sel:DWORD dst_unused:UNUSED_PAD src0_sel:DWORD src1_sel:WORD_1
v_mul_f32_dpp v168,v14,v168 row_newbcast:4 row_mask:0xf bank_mask:0xf
v_mfma_f32_16x16x32_bf16 v[28:31],a[208:211],a[32:35],v[16:19]
s_waitcnt lgkmcnt(8)
ds_read_u16_d16_hi v46,v46
v_lshl_add_u64 v[38:39],v[40:41],0,v[42:43]
v_mul_f32_dpp v169,v14,v169 row_newbcast:5 row_mask:0xf bank_mask:0xf
v_mul_f32_dpp v170,v14,v170 row_newbcast:6 row_mask:0xf bank_mask:0xf
v_fma_mixhi_f16 v8,v63,s99,0
v_mul_f32_dpp v171,v14,v171 row_newbcast:7 row_mask:0xf bank_mask:0xf
v_mfma_f32_16x16x32_bf16 v[28:31],a[204:207],a[16:19],v[28:31]
s_waitcnt lgkmcnt(4)
v_lshl_add_u64 v[40:41],v[56:57],0,v[58:59]
v_cvt_pk_bf16_f32 v208,v208,v209
v_cvt_pk_bf16_f32 v209,v210,v211
v_lshlrev_b32_sdwa v45,1,v45 dst_sel:DWORD dst_unused:UNUSED_PAD src0_sel:DWORD src1_sel:WORD_1
v_pk_fma_f16 v9,v8,v8,s54
v_cvt_pk_bf16_f32 v210,v168,v169
v_mfma_f32_16x16x32_bf16 v[28:31],a[200:203],a[0:3],v[28:31]
ds_read_u16 v45,v45
v_cvt_pk_bf16_f32 v211,v170,v171
v_mul_f32_dpp v216,v14,v216 row_newbcast:8 row_mask:0xf bank_mask:0xf
v_lshlrev_b32_sdwa v47,1,v47 dst_sel:DWORD dst_unused:UNUSED_PAD src0_sel:DWORD src1_sel:WORD_1
v_accvgpr_write_b32 a72,v208
v_mfma_f32_16x16x32_bf16 v[56:59],a[200:203],a[4:7],v[16:19]
s_waitcnt lgkmcnt(3)
ds_read_u16_d16_hi v47,v47
v_or_b32 v42,v52,v54
v_mul_f32_dpp v217,v14,v217 row_newbcast:9 row_mask:0xf bank_mask:0xf
v_mul_f32_dpp v218,v14,v218 row_newbcast:10 row_mask:0xf bank_mask:0xf
v_pk_mul_f16 v9,v8,v9
v_accvgpr_write_b32 a73,v209
v_mfma_f32_16x16x32_bf16 v[56:59],a[204:207],a[20:23],v[56:59]
s_waitcnt vmcnt(6)
v_cvt_pk_bf16_f32 v43,v53,v55
v_mul_f32_dpp v219,v14,v219 row_newbcast:11 row_mask:0xf bank_mask:0xf
v_mul_f32_dpp v184,v14,v184 row_newbcast:12 row_mask:0xf bank_mask:0xf
v_lshlrev_b32_sdwa v48,1,v48 dst_sel:DWORD dst_unused:UNUSED_PAD src0_sel:DWORD src1_sel:WORD_1
v_accvgpr_write_b32 a74,v210
v_mfma_f32_16x16x32_bf16 v[56:59],a[208:211],a[36:39],v[56:59]
s_barrier
v_exp_f16 v8,v9
v_mul_f32_dpp v185,v14,v185 row_newbcast:13 row_mask:0xf bank_mask:0xf
v_mul_f32_dpp v186,v14,v186 row_newbcast:14 row_mask:0xf bank_mask:0xf
v_accvgpr_write_b32 a75,v211
v_mfma_f32_16x16x32_bf16 v[52:55],a[212:215],a[36:39],v[20:23]
ds_read_u16 v48,v48
v_mul_f32_dpp v187,v14,v187 row_newbcast:15 row_mask:0xf bank_mask:0xf
v_cvt_pk_bf16_f32 v216,v216,v217
v_lshlrev_b32_sdwa v50,1,v50 dst_sel:DWORD dst_unused:UNUSED_PAD src0_sel:DWORD src1_sel:WORD_1
v_cvt_pk_bf16_f32 v217,v218,v219
v_mfma_f32_16x16x32_bf16 v[52:55],a[220:223],a[20:23],v[52:55]
s_add_u32 m0,s58,0
ds_read_u16_d16_hi v50,v50
v_cvt_pk_bf16_f32 v218,v184,v185
v_cvt_pk_bf16_f32 v219,v186,v187
v_lshlrev_b32_sdwa v49,1,v49 dst_sel:DWORD dst_unused:UNUSED_PAD src0_sel:DWORD src1_sel:WORD_1
v_mul_f32_dpp v224,v15,v224 row_newbcast:0 row_mask:0xf bank_mask:0xf
v_mfma_f32_16x16x32_bf16 v[52:55],a[216:219],a[4:7],v[52:55]
s_add_u32 s69,s69,s59
v_exp_f16_sdwa v8,v9 dst_sel:WORD_1 dst_unused:UNUSED_PRESERVE src0_sel:WORD_1
v_accvgpr_write_b32 a76,v216
v_mul_f32_dpp v225,v15,v225 row_newbcast:1 row_mask:0xf bank_mask:0xf
v_mul_f32_dpp v226,v15,v226 row_newbcast:2 row_mask:0xf bank_mask:0xf
v_mfma_f32_16x16x32_bf16 v[136:139],a[228:231],v[36:39],v[136:139]
buffer_load_dwordx4 v5,s[44:47],s69 offen offset:0 lds
v_accvgpr_write_b32 a77,v217
v_mul_f32_dpp v227,v15,v227 row_newbcast:3 row_mask:0xf bank_mask:0xf
v_lshlrev_b32_sdwa v51,1,v51 dst_sel:DWORD dst_unused:UNUSED_PAD src0_sel:DWORD src1_sel:WORD_1
v_mul_f32_dpp v200,v15,v200 row_newbcast:4 row_mask:0xf bank_mask:0xf
v_mfma_f32_16x16x32_bf16 v[140:143],a[228:231],v[40:43],v[140:143]
s_add_u32 s69,s69,s53
ds_read_u16 v49,v49
v_accvgpr_write_b32 a78,v218
v_mul_f32_dpp v201,v15,v201 row_newbcast:5 row_mask:0xf bank_mask:0xf
v_lshlrev_b32_sdwa v32,1,v32 dst_sel:DWORD dst_unused:UNUSED_PAD src0_sel:DWORD src1_sel:WORD_1
v_mul_f32_dpp v202,v15,v202 row_newbcast:6 row_mask:0xf bank_mask:0xf
v_mfma_f32_16x16x32_bf16 v[68:71],a[248:251],v[40:43],v[68:71]
buffer_load_dwordx4 v5,s[44:47],s69 offen offset:1024 lds
v_accvgpr_write_b32 a79,v219
v_mul_f32_dpp v203,v15,v203 row_newbcast:7 row_mask:0xf bank_mask:0xf
v_pk_fma_f16 v8,v8,s55,s55
v_cvt_pk_bf16_f32 v224,v224,v225
v_mfma_f32_16x16x32_bf16 v[64:67],a[248:251],v[36:39],v[64:67]
v_rcp_f16 v9,v8
v_cvt_pk_bf16_f32 v225,v226,v227
v_cvt_pk_bf16_f32 v226,v200,v201
v_lshlrev_b32_sdwa v34,1,v34 dst_sel:DWORD dst_unused:UNUSED_PAD src0_sel:DWORD src1_sel:WORD_1
v_cvt_pk_bf16_f32 v227,v202,v203
v_mfma_f32_16x16x32_bf16 v[144:147],a[236:239],v[36:39],v[144:147]
buffer_store_dwordx4 v[236:239],v7,s[40:43],s10 offen offset:192 sc1
v_mul_f32_dpp v212,v14,v212 row_newbcast:0 row_mask:0xf bank_mask:0xf
v_accvgpr_write_b32 a80,v224
v_mul_f32_dpp v213,v14,v213 row_newbcast:1 row_mask:0xf bank_mask:0xf
v_mfma_f32_16x16x32_bf16 v[148:151],a[236:239],v[40:43],v[148:151]
s_add_u32 s69,s69,s53
ds_read_u16_d16_hi v51,v51
v_mul_f32_dpp v214,v14,v214 row_newbcast:2 row_mask:0xf bank_mask:0xf
v_accvgpr_write_b32 a81,v225
v_lshlrev_b32_sdwa v33,1,v33 dst_sel:DWORD dst_unused:UNUSED_PAD src0_sel:DWORD src1_sel:WORD_1
v_mul_f32_dpp v215,v14,v215 row_newbcast:3 row_mask:0xf bank_mask:0xf
v_mfma_f32_16x16x32_bf16 v[84:87],a[252:255],v[40:43],v[84:87]
buffer_load_dwordx4 v5,s[44:47],s69 offen offset:2048 lds
v_mul_f32_dpp v172,v14,v172 row_newbcast:4 row_mask:0xf bank_mask:0xf
v_accvgpr_write_b32 a82,v226
v_lshlrev_b32_sdwa v35,1,v35 dst_sel:DWORD dst_unused:UNUSED_PAD src0_sel:DWORD src1_sel:WORD_1
v_mul_f32_dpp v173,v14,v173 row_newbcast:5 row_mask:0xf bank_mask:0xf
v_mfma_f32_16x16x32_bf16 v[80:83],a[252:255],v[36:39],v[80:83]
v_rcp_f16_sdwa v9,v8 dst_sel:WORD_1 dst_unused:UNUSED_PRESERVE src0_sel:WORD_1
v_mul_f32_dpp v174,v14,v174 row_newbcast:6 row_mask:0xf bank_mask:0xf
v_accvgpr_write_b32 a83,v227
v_mul_f32_dpp v175,v14,v175 row_newbcast:7 row_mask:0xf bank_mask:0xf
v_mfma_f32_16x16x32_bf16 v[152:155],a[240:243],v[36:39],v[152:155]
ds_read_u16 v32,v32
ds_read_u16_d16_hi v34,v34
v_cvt_pk_bf16_f32 v212,v212,v213
v_cvt_pk_bf16_f32 v213,v214,v215
v_lshlrev_b32_sdwa v60,1,v60 dst_sel:DWORD dst_unused:UNUSED_PAD src0_sel:DWORD src1_sel:WORD_1
v_cvt_pk_bf16_f32 v214,v172,v173
v_mfma_f32_16x16x32_bf16 v[156:159],a[240:243],v[40:43],v[156:159]
ds_read_u16 v33,v33
ds_read_u16_d16_hi v35,v35
v_cvt_pk_bf16_f32 v215,v174,v175
v_mfma_f32_32x32x16_bf16 v[160:175],a[64:67],a[64:67],0
v_mul_f32_dpp v220,v14,v220 row_newbcast:8 row_mask:0xf bank_mask:0xf
v_lshlrev_b32_sdwa v62,1,v62 dst_sel:DWORD dst_unused:UNUSED_PAD src0_sel:DWORD src1_sel:WORD_1
v_fma_mix_f32 v61,v61,v9,0 op_sel_hi:[0,1,0]
v_accvgpr_write_b32 a84,v212
v_fma_mix_f32 v63,v63,v9,0 op_sel:[0,1,0] op_sel_hi:[0,1,0]
v_mfma_f32_16x16x32_bf16 v[100:103],a[224:227],v[40:43],v[100:103]
ds_read_u16 v60,v60
v_mul_f32_dpp v221,v14,v221 row_newbcast:9 row_mask:0xf bank_mask:0xf
v_mul_f32_dpp v222,v14,v222 row_newbcast:10 row_mask:0xf bank_mask:0xf
v_mfma_f32_16x16x32_bf16 v[96:99],a[224:227],v[36:39],v[96:99]
ds_read_u16_d16_hi v62,v62
v_accvgpr_write_b32 a85,v213
v_mul_f32_dpp v223,v14,v223 row_newbcast:11 row_mask:0xf bank_mask:0xf
v_mul_f32_dpp v188,v14,v188 row_newbcast:12 row_mask:0xf bank_mask:0xf
ds_read_b64_tr_b16 a[232:233],v3 offset:6160
ds_read_b64_tr_b16 a[234:235],v3 offset:7696
ds_read_b64_tr_b16 a[244:245],v3 offset:6168
ds_read_b64_tr_b16 a[246:247],v3 offset:7704
v_add_u32_sdwa v36,s60,v10 dst_sel:DWORD dst_unused:UNUSED_PAD src0_sel:DWORD src1_sel:WORD_1
v_subrev_u32 v8,88400,v36
v_min_u32 v36,v36,v8
v_add_u32 v37,7072,v36
v_subrev_u32 v8,88400,v37
v_min_u32 v37,v37,v8
s_waitcnt lgkmcnt(14)
v_lshl_add_u64 v[44:45],v[44:45],0,v[46:47]
s_waitcnt lgkmcnt(10)
v_lshl_add_u64 v[46:47],v[48:49],0,v[50:51]
s_waitcnt lgkmcnt(6)
v_lshl_add_u64 v[48:49],v[32:33],0,v[34:35]
s_waitcnt lgkmcnt(4)
v_or_b32 v50,v60,v62
v_cvt_pk_bf16_f32 v51,v61,v63
v_mfma_f32_16x16x32_bf16 v[112:115],a[228:231],v[44:47],v[112:115]
buffer_store_dwordx4 v[244:247],v7,s[40:43],s10 offen offset:256 sc1
v_xad_u32 v40,v11,0,v36
v_xad_u32 v41,v11,8,v37
v_xad_u32 v42,v11,16,v36
v_xad_u32 v43,v11,24,v37
v_mfma_f32_16x16x32_bf16 v[116:119],a[228:231],v[48:51],v[116:119]
TE10_%=:
s_cbranch_execz TP9790_%=
ds_read_b64 a[48:49],v40 offset:0
v_accvgpr_write_b32 a86,v214
v_mul_f32_dpp v189,v14,v189 row_newbcast:13 row_mask:0xf bank_mask:0xf
v_mul_f32_dpp v190,v14,v190 row_newbcast:14 row_mask:0xf bank_mask:0xf
v_accvgpr_write_b32 a87,v215
v_mfma_f32_16x16x32_bf16 v[76:79],a[248:251],v[48:51],v[76:79]
ds_read_b64 a[50:51],v40 offset:192
v_mul_f32_dpp v191,v14,v191 row_newbcast:15 row_mask:0xf bank_mask:0xf
v_cvt_pk_bf16_f32 v220,v220,v221
v_cvt_pk_bf16_f32 v221,v222,v223
v_mfma_f32_16x16x32_bf16 v[72:75],a[248:251],v[44:47],v[72:75]
ds_read_b64 a[52:53],v41 offset:0
v_cvt_pk_bf16_f32 v222,v188,v189
v_cvt_pk_bf16_f32 v223,v190,v191
v_mfma_f32_32x32x16_bf16 v[176:191],a[64:67],a[64:67],0
v_mfma_f32_16x16x32_bf16 v[120:123],a[236:239],v[44:47],v[120:123]
ds_read_b64 a[54:55],v41 offset:192
v_mul_f32_dpp v228,v15,v228 row_newbcast:0 row_mask:0xf bank_mask:0xf
v_accvgpr_write_b32 a88,v220
v_mul_f32_dpp v229,v15,v229 row_newbcast:1 row_mask:0xf bank_mask:0xf
v_mfma_f32_16x16x32_bf16 v[124:127],a[236:239],v[48:51],v[124:127]
ds_read_b64 a[56:57],v40 offset:8
v_mul_f32_dpp v230,v15,v230 row_newbcast:2 row_mask:0xf bank_mask:0xf
v_accvgpr_write_b32 a89,v221
v_mul_f32_dpp v231,v15,v231 row_newbcast:3 row_mask:0xf bank_mask:0xf
v_mfma_f32_16x16x32_bf16 v[92:95],a[252:255],v[48:51],v[92:95]
ds_read_b64 a[58:59],v40 offset:200
v_mul_f32_dpp v204,v15,v204 row_newbcast:4 row_mask:0xf bank_mask:0xf
v_accvgpr_write_b32 a90,v222
v_mul_f32_dpp v205,v15,v205 row_newbcast:5 row_mask:0xf bank_mask:0xf
v_mfma_f32_16x16x32_bf16 v[88:91],a[252:255],v[44:47],v[88:91]
ds_read_b64 a[60:61],v41 offset:8
v_mul_f32_dpp v206,v15,v206 row_newbcast:6 row_mask:0xf bank_mask:0xf
v_accvgpr_write_b32 a91,v223
v_mul_f32_dpp v207,v15,v207 row_newbcast:7 row_mask:0xf bank_mask:0xf
v_mfma_f32_16x16x32_bf16 v[128:131],a[240:243],v[44:47],v[128:131]
ds_read_b64 a[62:63],v41 offset:200
v_cvt_pk_bf16_f32 v228,v228,v229
v_cvt_pk_bf16_f32 v229,v230,v231
v_cvt_pk_bf16_f32 v230,v204,v205
v_mfma_f32_16x16x32_bf16 v[132:135],a[240:243],v[48:51],v[132:135]
v_cvt_pk_bf16_f32 v231,v206,v207
v_mfma_f32_32x32x16_bf16 v[192:207],a[64:67],a[64:67],0
v_accvgpr_write_b32 a92,v228
v_mfma_f32_16x16x32_bf16 v[108:111],a[224:227],v[48:51],v[108:111]
v_accvgpr_write_b32 a93,v229
v_accvgpr_write_b32 a94,v230
v_accvgpr_write_b32 a95,v231
v_mfma_f32_16x16x32_bf16 v[104:107],a[224:227],v[44:47],v[104:107]
buffer_store_dwordx4 v[252:255],v7,s[40:43],s10 offen offset:320 sc1
v_mfma_f32_32x32x16_bf16 v[208:223],a[64:67],a[64:67],0
v_mov_b64 v[32:33],0
v_mov_b64 v[34:35],0
ds_read_b64 a[64:65],v42 offset:0
ds_read_b64 a[66:67],v42 offset:192
ds_read_b64 a[68:69],v43 offset:0
ds_read_b64 a[70:71],v43 offset:192
s_waitcnt lgkmcnt(8)
v_smfmac_f32_16x16x64_bf16 v[160:163],a[96:99],a[48:55],v10
s_waitcnt lgkmcnt(4)
ds_read_b64 a[48:49],v42 offset:8
ds_read_b64 a[50:51],v42 offset:200
ds_read_b64 a[52:53],v43 offset:8
ds_read_b64 a[54:55],v43 offset:200
v_smfmac_f32_16x16x64_bf16 v[164:167],a[100:103],a[56:63],v10
s_waitcnt lgkmcnt(4)
ds_read_b64 a[56:57],v40 offset:64
ds_read_b64 a[58:59],v40 offset:256
ds_read_b64 a[60:61],v41 offset:64
ds_read_b64 a[62:63],v41 offset:256
v_smfmac_f32_16x16x64_bf16 v[168:171],a[104:107],a[64:71],v10
s_waitcnt lgkmcnt(4)
ds_read_b64 a[64:65],v40 offset:72
ds_read_b64 a[66:67],v40 offset:264
ds_read_b64 a[68:69],v41 offset:72
ds_read_b64 a[70:71],v41 offset:264
v_and_b32 v44,8,v11
v_pk_mul_f32 v[48:49],v[160:161],v[160:161]
v_pk_mul_f32 v[50:51],v[162:163],v[162:163]
v_smfmac_f32_16x16x64_bf16 v[172:175],a[108:111],a[48:55],v10
s_waitcnt lgkmcnt(4)
ds_read_b64 a[48:49],v42 offset:64
ds_read_b64 a[50:51],v42 offset:256
ds_read_b64 a[52:53],v43 offset:64
ds_read_b64 a[54:55],v43 offset:256
v_mul_u32_u24 v44,884,v44
s_add_u32 s64,s60,3536
v_pk_add_f32 v[60:61],v[160:161],v[164:165]
v_pk_fma_f32 v[48:49],v[164:165],v[164:165],v[48:49]
v_pk_add_f32 v[62:63],v[162:163],v[166:167]
v_pk_fma_f32 v[50:51],v[166:167],v[166:167],v[50:51]
v_smfmac_f32_16x16x64_bf16 v[176:179],a[128:131],a[56:63],v10
s_waitcnt lgkmcnt(4)
ds_read_b64 a[56:57],v42 offset:72
ds_read_b64 a[58:59],v42 offset:264
ds_read_b64 a[60:61],v43 offset:72
ds_read_b64 a[62:63],v43 offset:264
v_add_u32 v47,s64,v44
v_pk_add_f32 v[60:61],v[60:61],v[168:169]
v_pk_fma_f32 v[48:49],v[168:169],v[168:169],v[48:49]
v_pk_add_f32 v[62:63],v[62:63],v[170:171]
v_pk_fma_f32 v[50:51],v[170:171],v[170:171],v[50:51]
v_smfmac_f32_16x16x64_bf16 v[180:183],a[132:135],a[64:71],v10
s_waitcnt lgkmcnt(4)
ds_read_b64 a[64:65],v40 offset:128
ds_read_b64 a[66:67],v40 offset:320
ds_read_b64 a[68:69],v41 offset:128
ds_read_b64 a[70:71],v41 offset:320
v_subrev_u32 v8,88400,v47
v_pk_add_f32 v[60:61],v[60:61],v[172:173]
v_pk_fma_f32 v[48:49],v[172:173],v[172:173],v[48:49]
v_pk_add_f32 v[62:63],v[62:63],v[174:175]
v_pk_fma_f32 v[50:51],v[174:175],v[174:175],v[50:51]
v_smfmac_f32_16x16x64_bf16 v[184:187],a[136:139],a[48:55],v10
s_waitcnt lgkmcnt(4)
ds_read_b64 a[48:49],v40 offset:136
ds_read_b64 a[50:51],v40 offset:328
ds_read_b64 a[52:53],v41 offset:136
ds_read_b64 a[54:55],v41 offset:328
v_min_u32 v47,v47,v8
v_pk_add_f32 v[60:61],v[60:61],v[176:177]
v_pk_fma_f32 v[48:49],v[176:177],v[176:177],v[48:49]
v_pk_add_f32 v[62:63],v[62:63],v[178:179]
v_pk_fma_f32 v[50:51],v[178:179],v[178:179],v[50:51]
v_smfmac_f32_16x16x64_bf16 v[188:191],a[140:143],a[56:63],v10
s_waitcnt lgkmcnt(4)
buffer_store_dwordx4 a[72:75],v7,s[40:43],s11 offen offset:0 sc1
ds_read_b64 a[56:57],v42 offset:128
ds_read_b64 a[58:59],v42 offset:320
ds_read_b64 a[60:61],v43 offset:128
ds_read_b64 a[62:63],v43 offset:320
v_xad_u32 v40,v11,32,v36
v_xad_u32 v41,v11,40,v37
v_pk_add_f32 v[60:61],v[60:61],v[180:181]
v_pk_fma_f32 v[48:49],v[180:181],v[180:181],v[48:49]
v_pk_add_f32 v[62:63],v[62:63],v[182:183]
v_pk_fma_f32 v[50:51],v[182:183],v[182:183],v[50:51]
v_smfmac_f32_16x16x64_bf16 v[192:195],a[160:163],a[64:71],v10
s_waitcnt lgkmcnt(4)
ds_read_b64 a[64:65],v42 offset:136
ds_read_b64 a[66:67],v42 offset:328
ds_read_b64 a[68:69],v43 offset:136
ds_read_b64 a[70:71],v43 offset:328
v_add_u32 v46,3536,v47
v_pk_add_f32 v[60:61],v[60:61],v[184:185]
v_pk_fma_f32 v[48:49],v[184:185],v[184:185],v[48:49]
v_pk_add_f32 v[62:63],v[62:63],v[186:187]
v_pk_fma_f32 v[50:51],v[186:187],v[186:187],v[50:51]
v_smfmac_f32_16x16x64_bf16 v[196:199],a[164:167],a[48:55],v10
s_waitcnt lgkmcnt(4)
ds_read_b64 a[48:49],v40 offset:0
ds_read_b64 a[50:51],v40 offset:192
ds_read_b64 a[52:53],v41 offset:0
ds_read_b64 a[54:55],v41 offset:192
v_xad_u32 v42,v11,48,v36
v_xad_u32 v43,v11,56,v37
v_pk_add_f32 v[60:61],v[60:61],v[188:189]
v_pk_fma_f32 v[48:49],v[188:189],v[188:189],v[48:49]
v_pk_add_f32 v[62:63],v[62:63],v[190:191]
v_pk_fma_f32 v[50:51],v[190:191],v[190:191],v[50:51]
v_smfmac_f32_16x16x64_bf16 v[200:203],a[168:171],a[56:63],v10
s_waitcnt lgkmcnt(4)
ds_read_b64 a[56:57],v40 offset:8
ds_read_b64 a[58:59],v40 offset:200
ds_read_b64 a[60:61],v41 offset:8
ds_read_b64 a[62:63],v41 offset:200
v_subrev_u32 v8,88400,v46
v_pk_add_f32 v[60:61],v[60:61],v[192:193]
v_pk_fma_f32 v[48:49],v[192:193],v[192:193],v[48:49]
v_pk_add_f32 v[62:63],v[62:63],v[194:195]
v_pk_fma_f32 v[50:51],v[194:195],v[194:195],v[50:51]
v_smfmac_f32_16x16x64_bf16 v[204:207],a[172:175],a[64:71],v10
s_waitcnt lgkmcnt(4)
ds_read_b64 a[64:65],v40 offset:64
ds_read_b64 a[66:67],v40 offset:256
ds_read_b64 a[68:69],v41 offset:64
ds_read_b64 a[70:71],v41 offset:256
v_min_u32 v46,v46,v8
v_pk_add_f32 v[60:61],v[60:61],v[196:197]
v_pk_fma_f32 v[48:49],v[196:197],v[196:197],v[48:49]
v_pk_add_f32 v[62:63],v[62:63],v[198:199]
v_pk_fma_f32 v[50:51],v[198:199],v[198:199],v[50:51]
v_smfmac_f32_16x16x64_bf16 v[208:211],a[112:115],a[48:55],v10
s_waitcnt lgkmcnt(4)
ds_read_b64 a[48:49],v40 offset:72
ds_read_b64 a[50:51],v40 offset:264
ds_read_b64 a[52:53],v41 offset:72
ds_read_b64 a[54:55],v41 offset:264
v_xor_b32 v9,0x80008,v12
v_pk_add_f32 v[60:61],v[60:61],v[200:201]
v_pk_fma_f32 v[48:49],v[200:201],v[200:201],v[48:49]
v_pk_add_f32 v[62:63],v[62:63],v[202:203]
v_pk_fma_f32 v[50:51],v[202:203],v[202:203],v[50:51]
v_mfma_f32_32x32x16_bf16 v[224:239],v[32:35],v[32:35],0
v_smfmac_f32_16x16x64_bf16 v[212:215],a[116:119],a[56:63],v10
s_waitcnt lgkmcnt(4)
buffer_store_dwordx4 a[76:79],v7,s[40:43],s11 offen offset:64 sc1
ds_read_b64 a[56:57],v40 offset:128
ds_read_b64 a[58:59],v40 offset:320
ds_read_b64 a[60:61],v41 offset:128
ds_read_b64 a[62:63],v41 offset:320
v_add_u32_sdwa v44,v12,v47 dst_sel:DWORD dst_unused:UNUSED_PAD src0_sel:WORD_0 src1_sel:DWORD
v_pk_add_f32 v[60:61],v[60:61],v[204:205]
v_pk_fma_f32 v[48:49],v[204:205],v[204:205],v[48:49]
v_pk_add_f32 v[62:63],v[62:63],v[206:207]
v_pk_fma_f32 v[50:51],v[206:207],v[206:207],v[50:51]
v_smfmac_f32_16x16x64_bf16 v[216:219],a[144:147],a[64:71],v10
s_waitcnt lgkmcnt(4)
ds_read_b64 a[64:65],v40 offset:136
ds_read_b64 a[66:67],v40 offset:328
ds_read_b64 a[68:69],v41 offset:136
ds_read_b64 a[70:71],v41 offset:328
v_add_u32_sdwa v45,v12,v47 dst_sel:DWORD dst_unused:UNUSED_PAD src0_sel:WORD_1 src1_sel:DWORD
v_pk_add_f32 v[60:61],v[60:61],v[208:209]
v_pk_fma_f32 v[48:49],v[208:209],v[208:209],v[48:49]
v_pk_add_f32 v[62:63],v[62:63],v[210:211]
v_pk_fma_f32 v[50:51],v[210:211],v[210:211],v[50:51]
v_smfmac_f32_16x16x64_bf16 v[220:223],a[148:151],a[48:55],v10
s_waitcnt lgkmcnt(4)
ds_read_b64 a[48:49],v42 offset:0
ds_read_b64 a[50:51],v42 offset:192
ds_read_b64 a[52:53],v43 offset:0
ds_read_b64 a[54:55],v43 offset:192
v_add_u32_sdwa v47,v9,v46 dst_sel:DWORD dst_unused:UNUSED_PAD src0_sel:WORD_1 src1_sel:DWORD
v_pk_add_f32 v[60:61],v[60:61],v[212:213]
v_pk_fma_f32 v[48:49],v[212:213],v[212:213],v[48:49]
v_pk_add_f32 v[62:63],v[62:63],v[214:215]
v_pk_fma_f32 v[50:51],v[214:215],v[214:215],v[50:51]
v_smfmac_f32_16x16x64_bf16 v[224:227],a[176:179],a[56:63],v10
s_waitcnt lgkmcnt(4)
ds_read_b64 a[56:57],v42 offset:8
ds_read_b64 a[58:59],v42 offset:200
ds_read_b64 a[60:61],v43 offset:8
ds_read_b64 a[62:63],v43 offset:200
v_add_u32_sdwa v46,v9,v46 dst_sel:DWORD dst_unused:UNUSED_PAD src0_sel:WORD_0 src1_sel:DWORD
v_pk_add_f32 v[60:61],v[60:61],v[216:217]
v_pk_fma_f32 v[48:49],v[216:217],v[216:217],v[48:49]
v_pk_add_f32 v[62:63],v[62:63],v[218:219]
v_pk_fma_f32 v[50:51],v[218:219],v[218:219],v[50:51]
v_mfma_f32_32x32x16_bf16 v[240:255],v[32:35],v[32:35],0
v_smfmac_f32_16x16x64_bf16 v[228:231],a[180:183],a[64:71],v10
s_waitcnt lgkmcnt(4)
ds_read_b64 a[64:65],v42 offset:64
ds_read_b64 a[66:67],v42 offset:256
ds_read_b64 a[68:69],v43 offset:64
ds_read_b64 a[70:71],v43 offset:256
v_pk_add_f32 v[60:61],v[60:61],v[220:221]
v_pk_fma_f32 v[48:49],v[220:221],v[220:221],v[48:49]
v_pk_add_f32 v[62:63],v[62:63],v[222:223]
v_pk_fma_f32 v[50:51],v[222:223],v[222:223],v[50:51]
v_smfmac_f32_16x16x64_bf16 v[232:235],a[120:123],a[48:55],v10
s_waitcnt lgkmcnt(4)
ds_read_b64 a[48:49],v42 offset:72
ds_read_b64 a[50:51],v42 offset:264
ds_read_b64 a[52:53],v43 offset:72
ds_read_b64 a[54:55],v43 offset:264
v_pk_add_f32 v[60:61],v[60:61],v[224:225]
v_pk_fma_f32 v[48:49],v[224:225],v[224:225],v[48:49]
v_pk_add_f32 v[62:63],v[62:63],v[226:227]
v_pk_fma_f32 v[50:51],v[226:227],v[226:227],v[50:51]
v_smfmac_f32_16x16x64_bf16 v[236:239],a[124:127],a[56:63],v10
s_waitcnt lgkmcnt(4)
ds_read_b64 a[56:57],v42 offset:128
ds_read_b64 a[58:59],v42 offset:320
ds_read_b64 a[60:61],v43 offset:128
ds_read_b64 a[62:63],v43 offset:320
v_pk_add_f32 v[60:61],v[60:61],v[228:229]
v_pk_fma_f32 v[48:49],v[228:229],v[228:229],v[48:49]
v_pk_add_f32 v[62:63],v[62:63],v[230:231]
v_pk_fma_f32 v[50:51],v[230:231],v[230:231],v[50:51]
v_smfmac_f32_16x16x64_bf16 v[240:243],a[152:155],a[64:71],v10
s_waitcnt lgkmcnt(4)
buffer_store_dwordx4 a[80:83],v7,s[40:43],s11 offen offset:128 sc1
ds_read_b64 a[64:65],v42 offset:136
ds_read_b64 a[66:67],v42 offset:328
ds_read_b64 a[68:69],v43 offset:136
ds_read_b64 a[70:71],v43 offset:328
v_pk_add_f32 v[60:61],v[60:61],v[232:233]
v_pk_fma_f32 v[48:49],v[232:233],v[232:233],v[48:49]
v_pk_add_f32 v[62:63],v[62:63],v[234:235]
v_pk_fma_f32 v[50:51],v[234:235],v[234:235],v[50:51]
v_smfmac_f32_16x16x64_bf16 v[244:247],a[156:159],a[48:55],v10
s_waitcnt lgkmcnt(4)
ds_read_b64 a[228:229],v44 offset:57344
ds_read_b64 a[230:231],v44 offset:57352
ds_read_b64 a[248:249],v45 offset:57344
ds_read_b64 a[250:251],v45 offset:57352
ds_read_b128 a[240:243],v13 offset:0
v_pk_add_f32 v[60:61],v[60:61],v[236:237]
v_pk_fma_f32 v[48:49],v[236:237],v[236:237],v[48:49]
v_pk_add_f32 v[62:63],v[62:63],v[238:239]
v_pk_fma_f32 v[50:51],v[238:239],v[238:239],v[50:51]
v_smfmac_f32_16x16x64_bf16 v[248:251],a[184:187],a[56:63],v10
s_waitcnt lgkmcnt(5)
ds_read_b64 a[236:237],v46 offset:57344
ds_read_b64 a[238:239],v46 offset:57352
ds_read_b64 a[252:253],v47 offset:57344
ds_read_b64 a[254:255],v47 offset:57352
ds_read_b128 a[224:227],v13 offset:256
v_pk_add_f32 v[60:61],v[60:61],v[240:241]
v_pk_fma_f32 v[48:49],v[240:241],v[240:241],v[48:49]
v_pk_add_f32 v[62:63],v[62:63],v[242:243]
v_pk_fma_f32 v[50:51],v[242:243],v[242:243],v[50:51]
v_smfmac_f32_16x16x64_bf16 v[252:255],a[188:191],a[64:71],v10
ds_read_b128 v[40:43],v4 offset:3584
ds_read_b128 v[36:39],v4 offset:3616
v_pk_add_f32 v[60:61],v[60:61],v[244:245]
v_pk_fma_f32 v[48:49],v[244:245],v[244:245],v[48:49]
v_pk_add_f32 v[62:63],v[62:63],v[246:247]
v_pk_fma_f32 v[50:51],v[246:247],v[246:247],v[50:51]
v_pk_add_f32 v[60:61],v[60:61],v[248:249]
v_pk_fma_f32 v[48:49],v[248:249],v[248:249],v[48:49]
v_pk_add_f32 v[62:63],v[62:63],v[250:251]
v_pk_fma_f32 v[50:51],v[250:251],v[250:251],v[50:51]
v_pk_add_f32 v[60:61],v[60:61],v[252:253]
v_pk_fma_f32 v[48:49],v[252:253],v[252:253],v[48:49]
v_pk_add_f32 v[62:63],v[62:63],v[254:255]
v_pk_fma_f32 v[50:51],v[254:255],v[254:255],v[50:51]
s_nop 0
v_permlane32_swap_b32 v60,v62
v_permlane32_swap_b32 v61,v63
v_permlane32_swap_b32 v48,v50
v_permlane32_swap_b32 v49,v51
v_pk_add_f32 v[60:61],v[60:61],v[62:63]
v_pk_add_f32 v[48:49],v[48:49],v[50:51]
s_nop 0
v_permlane16_swap_b32 v60,v61
v_permlane16_swap_b32 v48,v49
v_add_f32 v8,v60,v61
v_add_f32 v9,v48,v49
v_mul_f32 v8,0x3c2aaaab,v8
v_mul_f32 v9,0x3c2aaaab,v9
v_cmp_ngt_f32 vcc,0x5d800000,v9
v_fma_f32 v9,-v8,v8,v9
v_max_f32 v9,0,v9
s_or_b64 s[90:91],s[90:91],vcc
v_add_f32 v9,0x3727c5ac,v9
v_rsq_f32 v9,v9
s_nop 0
v_mul_f32 v60,s96,v9
v_mul_f32 v61,s96,v9
v_mul_f32 v48,-v8,v60
v_mul_f32 v49,-v8,v60
v_permlane16_swap_b32 v60,v61
s_nop 0
v_permlane16_swap_b32 v48,v49
v_mov_b64 v[62:63],v[60:61]
v_mov_b64 v[50:51],v[48:49]
s_nop 0
v_permlane32_swap_b32 v60,v62
v_permlane32_swap_b32 v61,v63
v_permlane32_swap_b32 v48,v50
v_permlane32_swap_b32 v49,v51
s_or_b32 s64,s90,s91
s_lshl_b32 s65,s52,2
v_mov_b32 v8,s64
v_mov_b32 v9,s65
ds_write_b32 v9,v8 offset:11648
v_pk_fma_f32 v[160:161],v[160:161],v[60:61],v[48:49]
v_pk_fma_f32 v[162:163],v[162:163],v[62:63],v[50:51]
v_pk_fma_f32 v[164:165],v[164:165],v[60:61],v[48:49]
v_pk_fma_f32 v[166:167],v[166:167],v[62:63],v[50:51]
v_pk_fma_f32 v[168:169],v[168:169],v[60:61],v[48:49]
v_pk_fma_f32 v[170:171],v[170:171],v[62:63],v[50:51]
v_pk_fma_f32 v[172:173],v[172:173],v[60:61],v[48:49]
v_pk_fma_f32 v[174:175],v[174:175],v[62:63],v[50:51]
v_pk_fma_f32 v[208:209],v[208:209],v[60:61],v[48:49]
v_pk_fma_f32 v[210:211],v[210:211],v[62:63],v[50:51]
v_pk_fma_f32 v[212:213],v[212:213],v[60:61],v[48:49]
v_pk_fma_f32 v[214:215],v[214:215],v[62:63],v[50:51]
buffer_store_dwordx4 a[84:87],v7,s[40:43],s11 offen offset:192 sc1
v_pk_fma_f32 v[232:233],v[232:233],v[60:61],v[48:49]
v_pk_fma_f32 v[234:235],v[234:235],v[62:63],v[50:51]
v_pk_fma_f32 v[236:237],v[236:237],v[60:61],v[48:49]
v_pk_fma_f32 v[238:239],v[238:239],v[62:63],v[50:51]
v_pk_fma_f32 v[176:177],v[176:177],v[60:61],v[48:49]
v_pk_fma_f32 v[178:179],v[178:179],v[62:63],v[50:51]
v_pk_fma_f32 v[180:181],v[180:181],v[60:61],v[48:49]
v_pk_fma_f32 v[182:183],v[182:183],v[62:63],v[50:51]
v_pk_fma_f32 v[184:185],v[184:185],v[60:61],v[48:49]
v_pk_fma_f32 v[186:187],v[186:187],v[62:63],v[50:51]
v_pk_fma_f32 v[188:189],v[188:189],v[60:61],v[48:49]
v_pk_fma_f32 v[190:191],v[190:191],v[62:63],v[50:51]
v_pk_fma_f32 v[216:217],v[216:217],v[60:61],v[48:49]
v_pk_fma_f32 v[218:219],v[218:219],v[62:63],v[50:51]
v_pk_fma_f32 v[220:221],v[220:221],v[60:61],v[48:49]
v_pk_fma_f32 v[222:223],v[222:223],v[62:63],v[50:51]
v_pk_fma_f32 v[240:241],v[240:241],v[60:61],v[48:49]
v_pk_fma_f32 v[242:243],v[242:243],v[62:63],v[50:51]
v_pk_fma_f32 v[244:245],v[244:245],v[60:61],v[48:49]
v_pk_fma_f32 v[246:247],v[246:247],v[62:63],v[50:51]
v_pk_fma_f32 v[192:193],v[192:193],v[60:61],v[48:49]
v_pk_fma_f32 v[194:195],v[194:195],v[62:63],v[50:51]
v_pk_fma_f32 v[196:197],v[196:197],v[60:61],v[48:49]
v_pk_fma_f32 v[198:199],v[198:199],v[62:63],v[50:51]
v_pk_fma_f32 v[200:201],v[200:201],v[60:61],v[48:49]
v_pk_fma_f32 v[202:203],v[202:203],v[62:63],v[50:51]
v_pk_fma_f32 v[204:205],v[204:205],v[60:61],v[48:49]
v_pk_fma_f32 v[206:207],v[206:207],v[62:63],v[50:51]
v_pk_fma_f32 v[224:225],v[224:225],v[60:61],v[48:49]
v_pk_fma_f32 v[226:227],v[226:227],v[62:63],v[50:51]
v_pk_fma_f32 v[228:229],v[228:229],v[60:61],v[48:49]
v_pk_fma_f32 v[230:231],v[230:231],v[62:63],v[50:51]
v_pk_fma_f32 v[248:249],v[248:249],v[60:61],v[48:49]
v_pk_fma_f32 v[250:251],v[250:251],v[62:63],v[50:51]
v_pk_fma_f32 v[252:253],v[252:253],v[60:61],v[48:49]
v_pk_fma_f32 v[254:255],v[254:255],v[62:63],v[50:51]
v_mfma_f32_16x16x32_bf16 v[32:35],a[216:219],a[8:11],v[20:23]
ds_read_b64_tr_b16 a[196:197],v1 offset:6160
v_cvt_pk_bf16_f32 v160,v160,v164
v_cvt_pk_bf16_f32 v168,v168,v172
v_cvt_pk_bf16_f32 v208,v208,v212
v_lshlrev_b32_sdwa v24,1,v24 dst_sel:DWORD dst_unused:UNUSED_PAD src0_sel:DWORD src1_sel:WORD_1
v_mfma_f32_16x16x32_bf16 v[32:35],a[220:223],a[24:27],v[32:35]
ds_read_u16 v24,v24
v_accvgpr_write_b32 a48,v160
v_accvgpr_write_b32 a49,v168
v_cvt_pk_bf16_f32 v232,v232,v236
v_accvgpr_write_b32 a50,v208
v_mfma_f32_16x16x32_bf16 v[32:35],a[212:215],a[40:43],v[32:35]
buffer_store_dwordx4 a[88:91],v7,s[40:43],s11 offen offset:256 sc1
v_cvt_pk_bf16_f32 v161,v161,v165
v_accvgpr_write_b32 a51,v232
v_cvt_pk_bf16_f32 v169,v169,v173
v_accvgpr_write_b32 a52,v161
v_fma_mixlo_f16 v8,v57,s99,0
v_mfma_f32_16x16x32_bf16 v[60:63],a[208:211],a[40:43],v[16:19]
ds_read_b64_tr_b16 a[198:199],v1 offset:7184
v_cvt_pk_bf16_f32 v209,v209,v213
v_accvgpr_write_b32 a53,v169
v_cvt_pk_bf16_f32 v233,v233,v237
v_accvgpr_write_b32 a54,v209
v_lshlrev_b32_sdwa v26,1,v26 dst_sel:DWORD dst_unused:UNUSED_PAD src0_sel:DWORD src1_sel:WORD_1
v_mfma_f32_16x16x32_bf16 v[60:63],a[204:207],a[24:27],v[60:63]
ds_read_u16_d16_hi v26,v26
v_cvt_pk_bf16_f32 v162,v162,v166
v_accvgpr_write_b32 a55,v233
v_cvt_pk_bf16_f32 v170,v170,v174
v_accvgpr_write_b32 a56,v162
v_fma_mixhi_f16 v8,v59,s99,0
v_mfma_f32_16x16x32_bf16 v[60:63],a[200:203],a[8:11],v[60:63]
ds_read_b64_tr_b16 a[192:193],v1 offset:8208
v_accvgpr_write_b32 a57,v170
v_cvt_pk_bf16_f32 v210,v210,v214
v_accvgpr_write_b32 a58,v210
v_lshlrev_b32_sdwa v25,1,v25 dst_sel:DWORD dst_unused:UNUSED_PAD src0_sel:DWORD src1_sel:WORD_1
v_cvt_pk_bf16_f32 v234,v234,v238
v_mfma_f32_16x16x32_bf16 v[48:51],a[200:203],a[12:15],v[16:19]
ds_read_u16 v25,v25
v_accvgpr_write_b32 a59,v234
v_cvt_pk_bf16_f32 v163,v163,v167
v_accvgpr_write_b32 a60,v163
v_mfma_f32_16x16x32_bf16 v[48:51],a[204:207],a[28:31],v[48:51]
ds_read_b64_tr_b16 a[194:195],v1 offset:9232
v_cvt_pk_bf16_f32 v171,v171,v175
v_accvgpr_write_b32 a61,v171
v_cvt_pk_bf16_f32 v211,v211,v215
v_accvgpr_write_b32 a62,v211
v_pk_fma_f16 v9,v8,v8,s54
v_mfma_f32_16x16x32_bf16 v[48:51],a[208:211],a[44:47],v[48:51]
s_waitcnt lgkmcnt(7)
v_cvt_pk_bf16_f32 v235,v235,v239
v_accvgpr_write_b32 a63,v235
v_cvt_pk_bf16_f32 v176,v176,v180
v_cvt_pk_bf16_f32 v184,v184,v188
v_lshlrev_b32_sdwa v27,1,v27 dst_sel:DWORD dst_unused:UNUSED_PAD src0_sel:DWORD src1_sel:WORD_1
v_mfma_f32_16x16x32_bf16 v[232:235],a[240:243],a[228:231],v[40:43]
ds_read_u16_d16_hi v27,v27
v_cvt_pk_bf16_f32 v216,v216,v220
v_accvgpr_write_b32 a64,v176
v_accvgpr_write_b32 a65,v184
v_cvt_pk_bf16_f32 v240,v240,v244
v_pk_mul_f16 v9,v8,v9
v_mfma_f32_16x16x32_bf16 v[236:239],a[240:243],a[248:251],v[40:43]
v_accvgpr_write_b32 a66,v216
v_cvt_pk_bf16_f32 v177,v177,v181
v_accvgpr_write_b32 a67,v240
v_cvt_pk_bf16_f32 v185,v185,v189
v_lshlrev_b32_sdwa v28,1,v28 dst_sel:DWORD dst_unused:UNUSED_PAD src0_sel:DWORD src1_sel:WORD_1
v_mfma_f32_16x16x32_bf16 v[208:211],a[240:243],a[236:239],v[40:43]
v_exp_f16 v8,v9
v_accvgpr_write_b32 a68,v177
v_cvt_pk_bf16_f32 v217,v217,v221
v_accvgpr_write_b32 a69,v185
v_cvt_pk_bf16_f32 v241,v241,v245
v_mfma_f32_16x16x32_bf16 v[212:215],a[240:243],a[252:255],v[40:43]
ds_read_b128 a[240:243],v13 offset:512
ds_read_b128 v[40:43],v4 offset:3712
v_accvgpr_write_b32 a70,v217
v_cvt_pk_bf16_f32 v178,v178,v182
v_accvgpr_write_b32 a71,v241
v_mfma_f32_16x16x32_bf16 v[160:163],a[224:227],a[228:231],v[36:39]
buffer_store_dwordx4 a[92:95],v7,s[40:43],s11 offen offset:320 sc1
ds_read_b64 a[228:229],v44 offset:57408
v_cvt_pk_bf16_f32 v186,v186,v190
v_accvgpr_write_b32 a72,v178
v_accvgpr_write_b32 a73,v186
v_cvt_pk_bf16_f32 v218,v218,v222
v_lshlrev_b32_sdwa v30,1,v30 dst_sel:DWORD dst_unused:UNUSED_PAD src0_sel:DWORD src1_sel:WORD_1
v_mfma_f32_16x16x32_bf16 v[164:167],a[224:227],a[248:251],v[36:39]
ds_read_b64 a[230:231],v44 offset:57416
ds_read_b64 a[248:249],v45 offset:57408
v_accvgpr_write_b32 a74,v218
v_cvt_pk_bf16_f32 v242,v242,v246
v_accvgpr_write_b32 a75,v242
v_cvt_pk_bf16_f32 v179,v179,v183
v_mfma_f32_16x16x32_bf16 v[168:171],a[224:227],a[236:239],v[36:39]
ds_read_b64 a[250:251],v45 offset:57416
ds_read_b64 a[236:237],v46 offset:57408
v_accvgpr_write_b32 a76,v179
v_cvt_pk_bf16_f32 v187,v187,v191
v_accvgpr_write_b32 a77,v187
v_cvt_pk_bf16_f32 v219,v219,v223
v_lshlrev_b32_sdwa v29,1,v29 dst_sel:DWORD dst_unused:UNUSED_PAD src0_sel:DWORD src1_sel:WORD_1
v_mfma_f32_16x16x32_bf16 v[172:175],a[224:227],a[252:255],v[36:39]
s_waitcnt lgkmcnt(3)
ds_read_b64 a[238:239],v46 offset:57416
ds_read_b64 a[252:253],v47 offset:57408
v_exp_f16_sdwa v8,v9 dst_sel:WORD_1 dst_unused:UNUSED_PRESERVE src0_sel:WORD_1
v_accvgpr_write_b32 a78,v219
v_cvt_pk_bf16_f32 v243,v243,v247
v_accvgpr_write_b32 a79,v243
v_cvt_pk_bf16_f32 v192,v192,v196
v_mfma_f32_16x16x32_bf16 v[240:243],a[240:243],a[228:231],v[40:43]
s_waitcnt lgkmcnt(3)
ds_read_b64 a[254:255],v47 offset:57416
ds_read_b128 a[224:227],v13 offset:768
v_cvt_pk_bf16_f32 v200,v200,v204
v_cvt_pk_bf16_f32 v224,v224,v228
v_accvgpr_write_b32 a80,v192
v_accvgpr_write_b32 a81,v200
v_lshlrev_b32_sdwa v31,1,v31 dst_sel:DWORD dst_unused:UNUSED_PAD src0_sel:DWORD src1_sel:WORD_1
v_mfma_f32_16x16x32_bf16 v[244:247],a[240:243],a[248:251],v[40:43]
s_waitcnt lgkmcnt(3)
ds_read_b128 v[36:39],v4 offset:3744
v_cvt_pk_bf16_f32 v248,v248,v252
v_accvgpr_write_b32 a82,v224
v_cvt_pk_bf16_f32 v193,v193,v197
v_mfma_f32_16x16x32_bf16 v[216:219],a[240:243],a[236:239],v[40:43]
s_waitcnt lgkmcnt(2)
ds_read_u16 v28,v28
TE11_%=:
s_cbranch_execz TP10325_%=
v_accvgpr_write_b32 a83,v248
v_cvt_pk_bf16_f32 v201,v201,v205
v_accvgpr_write_b32 a84,v193
v_cvt_pk_bf16_f32 v225,v225,v229
v_mfma_f32_16x16x32_bf16 v[220:223],a[240:243],a[252:255],v[40:43]
s_waitcnt lgkmcnt(1)
ds_read_b128 a[240:243],v13 offset:1024
ds_read_b128 v[40:43],v4 offset:3840
v_accvgpr_write_b32 a85,v201
v_cvt_pk_bf16_f32 v249,v249,v253
v_accvgpr_write_b32 a86,v225
v_cvt_pk_bf16_f32 v194,v194,v198
v_lshlrev_b32_sdwa v52,1,v52 dst_sel:DWORD dst_unused:UNUSED_PAD src0_sel:DWORD src1_sel:WORD_1
v_mfma_f32_16x16x32_bf16 v[176:179],a[224:227],a[228:231],v[36:39]
ds_read_b64 a[228:229],v44 offset:57472
ds_read_b64 a[230:231],v44 offset:57480
v_accvgpr_write_b32 a87,v249
v_cvt_pk_bf16_f32 v202,v202,v206
v_accvgpr_write_b32 a88,v194
v_accvgpr_write_b32 a89,v202
v_pk_fma_f16 v8,v8,s55,s55
v_mfma_f32_16x16x32_bf16 v[180:183],a[224:227],a[248:251],v[36:39]
ds_read_b64 a[248:249],v45 offset:57472
ds_read_b64 a[250:251],v45 offset:57480
v_cvt_pk_bf16_f32 v226,v226,v230
v_accvgpr_write_b32 a90,v226
v_cvt_pk_bf16_f32 v250,v250,v254
v_accvgpr_write_b32 a91,v250
v_lshlrev_b32_sdwa v54,1,v54 dst_sel:DWORD dst_unused:UNUSED_PAD src0_sel:DWORD src1_sel:WORD_1
v_mfma_f32_16x16x32_bf16 v[184:187],a[224:227],a[236:239],v[36:39]
ds_read_b64 a[236:237],v46 offset:57472
ds_read_b64 a[238:239],v46 offset:57480
v_rcp_f16 v9,v8
v_cvt_pk_bf16_f32 v195,v195,v199
v_accvgpr_write_b32 a92,v195
v_cvt_pk_bf16_f32 v203,v203,v207
v_accvgpr_write_b32 a93,v203
v_mfma_f32_16x16x32_bf16 v[188:191],a[224:227],a[252:255],v[36:39]
s_waitcnt lgkmcnt(4)
ds_read_b64 a[252:253],v47 offset:57472
ds_read_b64 a[254:255],v47 offset:57480
v_cvt_pk_bf16_f32 v227,v227,v231
v_accvgpr_write_b32 a94,v227
v_cvt_pk_bf16_f32 v251,v251,v255
v_accvgpr_write_b32 a95,v251
v_mfma_f32_16x16x32_bf16 v[248:251],a[240:243],a[228:231],v[40:43]
s_waitcnt lgkmcnt(4)
ds_read_b128 a[224:227],v13 offset:1280
ds_read_b128 v[36:39],v4 offset:3872
v_lshlrev_b32_sdwa v53,1,v53 dst_sel:DWORD dst_unused:UNUSED_PAD src0_sel:DWORD src1_sel:WORD_1
v_mfma_f32_16x16x32_bf16 v[252:255],a[240:243],a[248:251],v[40:43]
s_waitcnt lgkmcnt(4)
ds_read_u16_d16_hi v30,v30
v_lshlrev_b32_sdwa v55,1,v55 dst_sel:DWORD dst_unused:UNUSED_PAD src0_sel:DWORD src1_sel:WORD_1
v_mfma_f32_16x16x32_bf16 v[224:227],a[240:243],a[236:239],v[40:43]
s_waitcnt lgkmcnt(3)
ds_read_u16 v29,v29
v_lshlrev_b32_sdwa v56,1,v56 dst_sel:DWORD dst_unused:UNUSED_PAD src0_sel:DWORD src1_sel:WORD_1
v_mfma_f32_16x16x32_bf16 v[228:231],a[240:243],a[252:255],v[40:43]
s_waitcnt lgkmcnt(2)
ds_read_b64_tr_b16 a[240:241],v3 offset:6224
ds_read_b64_tr_b16 a[242:243],v3 offset:7760
v_rcp_f16_sdwa v9,v8 dst_sel:WORD_1 dst_unused:UNUSED_PRESERVE src0_sel:WORD_1
v_mfma_f32_16x16x32_bf16 v[192:195],a[224:227],a[228:231],v[36:39]
ds_read_b64_tr_b16 a[228:229],v3 offset:6232
ds_read_b64_tr_b16 a[230:231],v3 offset:7768
v_mfma_f32_16x16x32_bf16 v[196:199],a[224:227],a[248:251],v[36:39]
ds_read_b64_tr_b16 a[248:249],v3 offset:6288
ds_read_b64_tr_b16 a[250:251],v3 offset:7824
v_mfma_f32_16x16x32_bf16 v[200:203],a[224:227],a[236:239],v[36:39]
ds_read_b64_tr_b16 a[236:237],v3 offset:6296
ds_read_b64_tr_b16 a[238:239],v3 offset:7832
v_mfma_f32_16x16x32_bf16 v[204:207],a[224:227],a[252:255],v[36:39]
ds_read_u16_d16_hi v31,v31
ds_read_u16 v52,v52
v_lshlrev_b32_sdwa v58,1,v58 dst_sel:DWORD dst_unused:UNUSED_PAD src0_sel:DWORD src1_sel:WORD_1
v_mfma_f32_16x16x32_bf16 v[44:47],a[212:215],a[44:47],v[20:23]
ds_read_u16_d16_hi v54,v54
ds_read_u16 v53,v53
v_fma_mix_f32 v57,v57,v9,0 op_sel_hi:[0,1,0]
v_mfma_f32_16x16x32_bf16 v[44:47],a[220:223],a[28:31],v[44:47]
ds_read_u16_d16_hi v55,v55
v_fma_mix_f32 v59,v59,v9,0 op_sel:[0,1,0] op_sel_hi:[0,1,0]
v_mfma_f32_16x16x32_bf16 v[44:47],a[216:219],a[12:15],v[44:47]
s_barrier
ds_read_u16 v56,v56
ds_read_u16_d16_hi v58,v58
v_mfma_f32_16x16x32_bf16 v[40:43],a[216:219],a[48:51],v[20:23]
v_lshlrev_b32_sdwa v32,1,v32 dst_sel:DWORD dst_unused:UNUSED_PAD src0_sel:DWORD src1_sel:WORD_1
v_fma_mixlo_f16 v8,v49,s99,0
v_mfma_f32_16x16x32_bf16 v[40:43],a[220:223],a[64:67],v[40:43]
ds_read_u16 v32,v32
v_lshlrev_b32_sdwa v34,1,v34 dst_sel:DWORD dst_unused:UNUSED_PAD src0_sel:DWORD src1_sel:WORD_1
v_mfma_f32_16x16x32_bf16 v[40:43],a[212:215],a[80:83],v[40:43]
ds_read_u16_d16_hi v34,v34
v_lshl_add_u64 v[24:25],v[24:25],0,v[26:27]
v_mfma_f32_16x16x32_bf16 v[36:39],a[208:211],a[80:83],v[16:19]
s_waitcnt lgkmcnt(8)
v_lshl_add_u64 v[26:27],v[28:29],0,v[30:31]
v_fma_mixhi_f16 v8,v51,s99,0
v_mfma_f32_16x16x32_bf16 v[36:39],a[204:207],a[64:67],v[36:39]
s_waitcnt lgkmcnt(4)
v_lshl_add_u64 v[28:29],v[52:53],0,v[54:55]
v_lshlrev_b32_sdwa v33,1,v33 dst_sel:DWORD dst_unused:UNUSED_PAD src0_sel:DWORD src1_sel:WORD_1
v_pk_fma_f16 v9,v8,v8,s54
v_mfma_f32_16x16x32_bf16 v[36:39],a[200:203],a[48:51],v[36:39]
ds_read_u16 v33,v33
v_lshlrev_b32_sdwa v35,1,v35 dst_sel:DWORD dst_unused:UNUSED_PAD src0_sel:DWORD src1_sel:WORD_1
v_mfma_f32_16x16x32_bf16 v[52:55],a[200:203],a[52:55],v[16:19]
s_waitcnt lgkmcnt(3)
ds_read_u16_d16_hi v35,v35
v_or_b32 v30,v56,v58
v_pk_mul_f16 v9,v8,v9
v_mfma_f32_16x16x32_bf16 v[52:55],a[204:207],a[68:71],v[52:55]
v_cvt_pk_bf16_f32 v31,v57,v59
v_lshlrev_b32_sdwa v60,1,v60 dst_sel:DWORD dst_unused:UNUSED_PAD src0_sel:DWORD src1_sel:WORD_1
v_mfma_f32_16x16x32_bf16 v[52:55],a[208:211],a[84:87],v[52:55]
v_exp_f16 v8,v9
v_mfma_f32_16x16x32_bf16 v[56:59],a[212:215],a[84:87],v[20:23]
ds_read_u16 v60,v60
v_lshlrev_b32_sdwa v62,1,v62 dst_sel:DWORD dst_unused:UNUSED_PAD src0_sel:DWORD src1_sel:WORD_1
v_mfma_f32_16x16x32_bf16 v[56:59],a[220:223],a[68:71],v[56:59]
ds_read_u16_d16_hi v62,v62
v_lshlrev_b32_sdwa v61,1,v61 dst_sel:DWORD dst_unused:UNUSED_PAD src0_sel:DWORD src1_sel:WORD_1
v_mfma_f32_16x16x32_bf16 v[56:59],a[216:219],a[52:55],v[56:59]
v_exp_f16_sdwa v8,v9 dst_sel:WORD_1 dst_unused:UNUSED_PRESERVE src0_sel:WORD_1
v_mfma_f32_16x16x32_bf16 v[136:139],a[232:235],v[24:27],v[136:139]
ds_read_u16 v61,v61
v_lshlrev_b32_sdwa v63,1,v63 dst_sel:DWORD dst_unused:UNUSED_PAD src0_sel:DWORD src1_sel:WORD_1
v_mfma_f32_16x16x32_bf16 v[140:143],a[232:235],v[28:31],v[140:143]
ds_read_u16_d16_hi v63,v63
v_lshlrev_b32_sdwa v44,1,v44 dst_sel:DWORD dst_unused:UNUSED_PAD src0_sel:DWORD src1_sel:WORD_1
v_mfma_f32_16x16x32_bf16 v[68:71],a[244:247],v[28:31],v[68:71]
ds_read_u16 v44,v44
v_pk_fma_f16 v8,v8,s55,s55
v_mfma_f32_16x16x32_bf16 v[64:67],a[244:247],v[24:27],v[64:67]
v_rcp_f16 v9,v8
v_lshlrev_b32_sdwa v46,1,v46 dst_sel:DWORD dst_unused:UNUSED_PAD src0_sel:DWORD src1_sel:WORD_1
v_mfma_f32_16x16x32_bf16 v[144:147],a[240:243],v[24:27],v[144:147]
ds_read_u16_d16_hi v46,v46
v_lshlrev_b32_sdwa v45,1,v45 dst_sel:DWORD dst_unused:UNUSED_PAD src0_sel:DWORD src1_sel:WORD_1
v_mfma_f32_16x16x32_bf16 v[148:151],a[240:243],v[28:31],v[148:151]
ds_read_u16 v45,v45
v_lshlrev_b32_sdwa v47,1,v47 dst_sel:DWORD dst_unused:UNUSED_PAD src0_sel:DWORD src1_sel:WORD_1
v_mfma_f32_16x16x32_bf16 v[84:87],a[228:231],v[28:31],v[84:87]
ds_read_u16_d16_hi v47,v47
v_lshlrev_b32_sdwa v48,1,v48 dst_sel:DWORD dst_unused:UNUSED_PAD src0_sel:DWORD src1_sel:WORD_1
v_mfma_f32_16x16x32_bf16 v[80:83],a[228:231],v[24:27],v[80:83]
v_rcp_f16_sdwa v9,v8 dst_sel:WORD_1 dst_unused:UNUSED_PRESERVE src0_sel:WORD_1
v_mfma_f32_16x16x32_bf16 v[152:155],a[248:251],v[24:27],v[152:155]
ds_read_u16 v48,v48
v_lshlrev_b32_sdwa v50,1,v50 dst_sel:DWORD dst_unused:UNUSED_PAD src0_sel:DWORD src1_sel:WORD_1
v_mfma_f32_16x16x32_bf16 v[156:159],a[248:251],v[28:31],v[156:159]
ds_read_u16_d16_hi v50,v50
v_fma_mix_f32 v49,v49,v9,0 op_sel_hi:[0,1,0]
v_mfma_f32_16x16x32_bf16 v[100:103],a[236:239],v[28:31],v[100:103]
v_fma_mix_f32 v51,v51,v9,0 op_sel:[0,1,0] op_sel_hi:[0,1,0]
v_mfma_f32_16x16x32_bf16 v[96:99],a[236:239],v[24:27],v[96:99]
v_mfma_f32_16x16x32_bf16 v[28:31],a[216:219],a[56:59],v[20:23]
v_lshlrev_b32_sdwa v40,1,v40 dst_sel:DWORD dst_unused:UNUSED_PAD src0_sel:DWORD src1_sel:WORD_1
v_fma_mixlo_f16 v8,v53,s99,0
v_mfma_f32_16x16x32_bf16 v[28:31],a[220:223],a[72:75],v[28:31]
ds_read_u16 v40,v40
v_lshlrev_b32_sdwa v42,1,v42 dst_sel:DWORD dst_unused:UNUSED_PAD src0_sel:DWORD src1_sel:WORD_1
v_mfma_f32_16x16x32_bf16 v[28:31],a[212:215],a[88:91],v[28:31]
s_waitcnt lgkmcnt(4)
ds_read_u16_d16_hi v42,v42
v_lshl_add_u64 v[32:33],v[32:33],0,v[34:35]
v_mfma_f32_16x16x32_bf16 v[24:27],a[208:211],a[88:91],v[16:19]
v_lshl_add_u64 v[34:35],v[60:61],0,v[62:63]
v_fma_mixhi_f16 v8,v55,s99,0
v_mfma_f32_16x16x32_bf16 v[24:27],a[204:207],a[72:75],v[24:27]
s_waitcnt lgkmcnt(4)
v_lshl_add_u64 v[60:61],v[44:45],0,v[46:47]
v_lshlrev_b32_sdwa v41,1,v41 dst_sel:DWORD dst_unused:UNUSED_PAD src0_sel:DWORD src1_sel:WORD_1
v_pk_fma_f16 v9,v8,v8,s54
v_mfma_f32_16x16x32_bf16 v[24:27],a[200:203],a[56:59],v[24:27]
ds_read_u16 v41,v41
v_lshlrev_b32_sdwa v43,1,v43 dst_sel:DWORD dst_unused:UNUSED_PAD src0_sel:DWORD src1_sel:WORD_1
v_mfma_f32_16x16x32_bf16 v[44:47],a[200:203],a[60:63],v[16:19]
s_waitcnt lgkmcnt(3)
ds_read_b128 v[16:19],v4 offset:768
ds_read_b64_tr_b16 a[200:201],v1 offset:10256
v_or_b32 v62,v48,v50
v_pk_mul_f16 v9,v8,v9
v_mfma_f32_16x16x32_bf16 v[44:47],a[204:207],a[76:79],v[44:47]
ds_read_b64_tr_b16 a[202:203],v1 offset:11280
ds_read_b64_tr_b16 a[204:205],v2 offset:10256
v_cvt_pk_bf16_f32 v63,v49,v51
v_lshlrev_b32_sdwa v36,1,v36 dst_sel:DWORD dst_unused:UNUSED_PAD src0_sel:DWORD src1_sel:WORD_1
v_mfma_f32_16x16x32_bf16 v[44:47],a[208:211],a[92:95],v[44:47]
ds_read_b64_tr_b16 a[206:207],v2 offset:11280
ds_read_b64_tr_b16 a[208:209],v2 offset:8208
v_exp_f16 v8,v9
v_mfma_f32_16x16x32_bf16 v[48:51],a[212:215],a[92:95],v[20:23]
ds_read_b128 v[20:23],v4 offset:800
ds_read_b64_tr_b16 a[210:211],v2 offset:9232
v_lshlrev_b32_sdwa v38,1,v38 dst_sel:DWORD dst_unused:UNUSED_PAD src0_sel:DWORD src1_sel:WORD_1
v_mfma_f32_16x16x32_bf16 v[48:51],a[220:223],a[76:79],v[48:51]
ds_read_b64_tr_b16 a[212:213],v2 offset:6160
ds_read_b64_tr_b16 a[214:215],v2 offset:7184
v_lshlrev_b32_sdwa v37,1,v37 dst_sel:DWORD dst_unused:UNUSED_PAD src0_sel:DWORD src1_sel:WORD_1
v_mfma_f32_16x16x32_bf16 v[48:51],a[216:219],a[60:63],v[48:51]
v_exp_f16_sdwa v8,v9 dst_sel:WORD_1 dst_unused:UNUSED_PRESERVE src0_sel:WORD_1
v_mfma_f32_16x16x32_bf16 v[112:115],a[232:235],v[32:35],v[112:115]
ds_read_u16_d16_hi v43,v43
v_lshlrev_b32_sdwa v39,1,v39 dst_sel:DWORD dst_unused:UNUSED_PAD src0_sel:DWORD src1_sel:WORD_1
v_mfma_f32_16x16x32_bf16 v[116:119],a[232:235],v[60:63],v[116:119]
ds_read_u16 v36,v36
v_lshlrev_b32_sdwa v56,1,v56 dst_sel:DWORD dst_unused:UNUSED_PAD src0_sel:DWORD src1_sel:WORD_1
v_mfma_f32_16x16x32_bf16 v[76:79],a[244:247],v[60:63],v[76:79]
ds_read_u16_d16_hi v38,v38
v_pk_fma_f16 v8,v8,s55,s55
v_mfma_f32_16x16x32_bf16 v[72:75],a[244:247],v[32:35],v[72:75]
v_rcp_f16 v9,v8
v_lshlrev_b32_sdwa v58,1,v58 dst_sel:DWORD dst_unused:UNUSED_PAD src0_sel:DWORD src1_sel:WORD_1
v_mfma_f32_16x16x32_bf16 v[120:123],a[240:243],v[32:35],v[120:123]
ds_read_u16 v37,v37
v_lshlrev_b32_sdwa v57,1,v57 dst_sel:DWORD dst_unused:UNUSED_PAD src0_sel:DWORD src1_sel:WORD_1
v_mfma_f32_16x16x32_bf16 v[124:127],a[240:243],v[60:63],v[124:127]
ds_read_u16_d16_hi v39,v39
v_lshlrev_b32_sdwa v59,1,v59 dst_sel:DWORD dst_unused:UNUSED_PAD src0_sel:DWORD src1_sel:WORD_1
v_mfma_f32_16x16x32_bf16 v[92:95],a[228:231],v[60:63],v[92:95]
ds_read_u16 v56,v56
v_lshlrev_b32_sdwa v52,1,v52 dst_sel:DWORD dst_unused:UNUSED_PAD src0_sel:DWORD src1_sel:WORD_1
v_mfma_f32_16x16x32_bf16 v[88:91],a[228:231],v[32:35],v[88:91]
v_rcp_f16_sdwa v9,v8 dst_sel:WORD_1 dst_unused:UNUSED_PRESERVE src0_sel:WORD_1
v_mfma_f32_16x16x32_bf16 v[128:131],a[248:251],v[32:35],v[128:131]
ds_read_u16_d16_hi v58,v58
ds_read_u16 v57,v57
v_mfma_f32_16x16x32_bf16 v[132:135],a[248:251],v[60:63],v[132:135]
ds_read_u16_d16_hi v59,v59
v_lshlrev_b32_sdwa v54,1,v54 dst_sel:DWORD dst_unused:UNUSED_PAD src0_sel:DWORD src1_sel:WORD_1
v_mfma_f32_16x16x32_bf16 v[108:111],a[236:239],v[60:63],v[108:111]
ds_read_u16 v52,v52
v_fma_mix_f32 v53,v53,v9,0 op_sel_hi:[0,1,0]
v_mfma_f32_16x16x32_bf16 v[104:107],a[236:239],v[32:35],v[104:107]
s_waitcnt lgkmcnt(7)
ds_read_u16_d16_hi v54,v54
v_fma_mix_f32 v55,v55,v9,0 op_sel:[0,1,0] op_sel_hi:[0,1,0]
v_mfma_f32_16x16x32_bf16 v[60:63],a[196:199],a[0:3],v[16:19]
s_add_u32 m0,s62,0
v_lshlrev_b32_sdwa v28,1,v28 dst_sel:DWORD dst_unused:UNUSED_PAD src0_sel:DWORD src1_sel:WORD_1
v_fma_mixlo_f16 v8,v45,s99,0
v_mfma_f32_16x16x32_bf16 v[60:63],a[192:195],a[16:19],v[60:63]
s_mov_b32 s68,s87
ds_read_u16 v28,v28
v_lshlrev_b32_sdwa v30,1,v30 dst_sel:DWORD dst_unused:UNUSED_PAD src0_sel:DWORD src1_sel:WORD_1
v_mfma_f32_16x16x32_bf16 v[60:63],a[200:203],a[32:35],v[60:63]
buffer_load_dwordx4 v5,s[44:47],s68 offen offset:0 lds
v_lshl_add_u64 v[40:41],v[40:41],0,v[42:43]
v_mfma_f32_16x16x32_bf16 v[32:35],a[204:207],a[32:35],v[20:23]
s_waitcnt lgkmcnt(6)
ds_read_u16_d16_hi v30,v30
v_lshl_add_u64 v[42:43],v[36:37],0,v[38:39]
v_fma_mixhi_f16 v8,v47,s99,0
v_mfma_f32_16x16x32_bf16 v[32:35],a[208:211],a[16:19],v[32:35]
s_waitcnt lgkmcnt(4)
v_lshl_add_u64 v[36:37],v[56:57],0,v[58:59]
v_lshlrev_b32_sdwa v29,1,v29 dst_sel:DWORD dst_unused:UNUSED_PAD src0_sel:DWORD src1_sel:WORD_1
v_pk_fma_f16 v9,v8,v8,s54
v_mfma_f32_16x16x32_bf16 v[32:35],a[212:215],a[0:3],v[32:35]
s_add_u32 s68,s68,s53
ds_read_u16 v29,v29
v_lshlrev_b32_sdwa v31,1,v31 dst_sel:DWORD dst_unused:UNUSED_PAD src0_sel:DWORD src1_sel:WORD_1
v_mfma_f32_16x16x32_bf16 v[56:59],a[212:215],a[4:7],v[20:23]
s_waitcnt lgkmcnt(3)
buffer_load_dwordx4 v5,s[44:47],s68 offen offset:1024 lds
v_or_b32 v38,v52,v54
v_pk_mul_f16 v9,v8,v9
v_mfma_f32_16x16x32_bf16 v[56:59],a[208:211],a[20:23],v[56:59]
ds_read_u16_d16_hi v31,v31
v_cvt_pk_bf16_f32 v39,v53,v55
v_lshlrev_b32_sdwa v24,1,v24 dst_sel:DWORD dst_unused:UNUSED_PAD src0_sel:DWORD src1_sel:WORD_1
v_mfma_f32_16x16x32_bf16 v[56:59],a[204:207],a[36:39],v[56:59]
s_barrier
v_exp_f16 v8,v9
v_mfma_f32_16x16x32_bf16 v[52:55],a[200:203],a[36:39],v[16:19]
ds_read_u16 v24,v24
v_lshlrev_b32_sdwa v26,1,v26 dst_sel:DWORD dst_unused:UNUSED_PAD src0_sel:DWORD src1_sel:WORD_1
v_mfma_f32_16x16x32_bf16 v[52:55],a[192:195],a[20:23],v[52:55]
s_add_u32 m0,s58,6160
ds_read_u16_d16_hi v26,v26
v_lshlrev_b32_sdwa v25,1,v25 dst_sel:DWORD dst_unused:UNUSED_PAD src0_sel:DWORD src1_sel:WORD_1
v_mfma_f32_16x16x32_bf16 v[52:55],a[196:199],a[4:7],v[52:55]
s_add_u32 s69,s69,s59
v_exp_f16_sdwa v8,v9 dst_sel:WORD_1 dst_unused:UNUSED_PRESERVE src0_sel:WORD_1
v_mfma_f32_16x16x32_bf16 v[232:235],a[232:235],v[40:43],v[232:235]
buffer_load_dwordx4 v5,s[44:47],s69 offen offset:0 lds
v_lshlrev_b32_sdwa v27,1,v27 dst_sel:DWORD dst_unused:UNUSED_PAD src0_sel:DWORD src1_sel:WORD_1
v_mfma_f32_16x16x32_bf16 v[236:239],a[232:235],v[36:39],v[236:239]
s_add_u32 m0,s62,0
ds_read_u16 v25,v25
v_lshlrev_b32_sdwa v48,1,v48 dst_sel:DWORD dst_unused:UNUSED_PAD src0_sel:DWORD src1_sel:WORD_1
v_mfma_f32_16x16x32_bf16 v[164:167],a[244:247],v[36:39],v[164:167]
s_add_u32 s68,s68,s53
ds_read_u16_d16_hi v27,v27
v_pk_fma_f16 v8,v8,s55,s55
v_mfma_f32_16x16x32_bf16 v[160:163],a[244:247],v[40:43],v[160:163]
v_rcp_f16 v9,v8
v_lshlrev_b32_sdwa v50,1,v50 dst_sel:DWORD dst_unused:UNUSED_PAD src0_sel:DWORD src1_sel:WORD_1
v_mfma_f32_16x16x32_bf16 v[240:243],a[240:243],v[40:43],v[240:243]
buffer_load_dwordx4 v5,s[44:47],s68 offen offset:2048 lds
v_lshlrev_b32_sdwa v49,1,v49 dst_sel:DWORD dst_unused:UNUSED_PAD src0_sel:DWORD src1_sel:WORD_1
v_mfma_f32_16x16x32_bf16 v[244:247],a[240:243],v[36:39],v[244:247]
s_add_u32 m0,s58,6160
ds_read_u16 v48,v48
v_lshlrev_b32_sdwa v51,1,v51 dst_sel:DWORD dst_unused:UNUSED_PAD src0_sel:DWORD src1_sel:WORD_1
v_mfma_f32_16x16x32_bf16 v[180:183],a[228:231],v[36:39],v[180:183]
s_add_u32 s69,s69,s53
ds_read_u16_d16_hi v50,v50
v_lshlrev_b32_sdwa v44,1,v44 dst_sel:DWORD dst_unused:UNUSED_PAD src0_sel:DWORD src1_sel:WORD_1
v_mfma_f32_16x16x32_bf16 v[176:179],a[228:231],v[40:43],v[176:179]
v_rcp_f16_sdwa v9,v8 dst_sel:WORD_1 dst_unused:UNUSED_PRESERVE src0_sel:WORD_1
v_mfma_f32_16x16x32_bf16 v[248:251],a[248:251],v[40:43],v[248:251]
buffer_load_dwordx4 v5,s[44:47],s69 offen offset:1024 lds
ds_read_u16 v49,v49
v_mfma_f32_16x16x32_bf16 v[252:255],a[248:251],v[36:39],v[252:255]
s_add_u32 s69,s69,s53
ds_read_u16_d16_hi v51,v51
v_lshlrev_b32_sdwa v46,1,v46 dst_sel:DWORD dst_unused:UNUSED_PAD src0_sel:DWORD src1_sel:WORD_1
v_mfma_f32_16x16x32_bf16 v[196:199],a[236:239],v[36:39],v[196:199]
buffer_load_dwordx4 v5,s[44:47],s69 offen offset:2048 lds
v_fma_mix_f32 v45,v45,v9,0 op_sel_hi:[0,1,0]
v_mfma_f32_16x16x32_bf16 v[192:195],a[236:239],v[40:43],v[192:195]
ds_read_u16 v44,v44
ds_read_u16_d16_hi v46,v46
v_fma_mix_f32 v47,v47,v9,0 op_sel:[0,1,0] op_sel_hi:[0,1,0]
v_mfma_f32_16x16x32_bf16 v[36:39],a[196:199],a[8:11],v[16:19]
ds_read_b64_tr_b16 a[252:253],v3 offset:12320
v_lshlrev_b32_sdwa v60,1,v60 dst_sel:DWORD dst_unused:UNUSED_PAD src0_sel:DWORD src1_sel:WORD_1
v_mfma_f32_16x16x32_bf16 v[36:39],a[192:195],a[24:27],v[36:39]
ds_read_u16 v60,v60
v_fma_mixlo_f16 v8,v57,s99,0
v_mfma_f32_16x16x32_bf16 v[36:39],a[200:203],a[40:43],v[36:39]
s_waitcnt lgkmcnt(7)
ds_read_b64_tr_b16 a[254:255],v3 offset:13856
v_lshl_add_u64 v[28:29],v[28:29],0,v[30:31]
v_lshlrev_b32_sdwa v62,1,v62 dst_sel:DWORD dst_unused:UNUSED_PAD src0_sel:DWORD src1_sel:WORD_1
v_mfma_f32_16x16x32_bf16 v[40:43],a[204:207],a[40:43],v[20:23]
ds_read_u16_d16_hi v62,v62
v_lshl_add_u64 v[30:31],v[24:25],0,v[26:27]
v_fma_mixhi_f16 v8,v59,s99,0
v_mfma_f32_16x16x32_bf16 v[40:43],a[208:211],a[24:27],v[40:43]
s_waitcnt lgkmcnt(6)
ds_read_b64_tr_b16 a[224:225],v3 offset:12328
v_lshl_add_u64 v[24:25],v[48:49],0,v[50:51]
v_lshlrev_b32_sdwa v61,1,v61 dst_sel:DWORD dst_unused:UNUSED_PAD src0_sel:DWORD src1_sel:WORD_1
v_pk_fma_f16 v9,v8,v8,s54
v_mfma_f32_16x16x32_bf16 v[40:43],a[212:215],a[8:11],v[40:43]
ds_read_u16 v61,v61
v_lshlrev_b32_sdwa v63,1,v63 dst_sel:DWORD dst_unused:UNUSED_PAD src0_sel:DWORD src1_sel:WORD_1
v_mfma_f32_16x16x32_bf16 v[48:51],a[212:215],a[12:15],v[20:23]
s_waitcnt lgkmcnt(6)
ds_read_u16_d16_hi v63,v63
v_or_b32 v26,v44,v46
v_pk_mul_f16 v9,v8,v9
v_mfma_f32_16x16x32_bf16 v[48:51],a[208:211],a[28:31],v[48:51]
ds_read_b64_tr_b16 a[226:227],v3 offset:13864
v_cvt_pk_bf16_f32 v27,v45,v47
v_lshlrev_b32_sdwa v32,1,v32 dst_sel:DWORD dst_unused:UNUSED_PAD src0_sel:DWORD src1_sel:WORD_1
v_mfma_f32_16x16x32_bf16 v[48:51],a[204:207],a[44:47],v[48:51]
v_exp_f16 v8,v9
v_mfma_f32_16x16x32_bf16 v[44:47],a[200:203],a[44:47],v[16:19]
ds_read_u16 v32,v32
v_lshlrev_b32_sdwa v34,1,v34 dst_sel:DWORD dst_unused:UNUSED_PAD src0_sel:DWORD src1_sel:WORD_1
v_mfma_f32_16x16x32_bf16 v[44:47],a[192:195],a[28:31],v[44:47]
ds_read_u16_d16_hi v34,v34
v_lshlrev_b32_sdwa v33,1,v33 dst_sel:DWORD dst_unused:UNUSED_PAD src0_sel:DWORD src1_sel:WORD_1
v_mfma_f32_16x16x32_bf16 v[44:47],a[196:199],a[12:15],v[44:47]
v_exp_f16_sdwa v8,v9 dst_sel:WORD_1 dst_unused:UNUSED_PRESERVE src0_sel:WORD_1
v_mfma_f32_16x16x32_bf16 v[208:211],a[232:235],v[28:31],v[208:211]
ds_read_u16 v33,v33
v_lshlrev_b32_sdwa v35,1,v35 dst_sel:DWORD dst_unused:UNUSED_PAD src0_sel:DWORD src1_sel:WORD_1
v_mfma_f32_16x16x32_bf16 v[212:215],a[232:235],v[24:27],v[212:215]
ds_read_u16_d16_hi v35,v35
v_lshlrev_b32_sdwa v52,1,v52 dst_sel:DWORD dst_unused:UNUSED_PAD src0_sel:DWORD src1_sel:WORD_1
v_mfma_f32_16x16x32_bf16 v[172:175],a[244:247],v[24:27],v[172:175]
ds_read_u16 v52,v52
v_pk_fma_f16 v8,v8,s55,s55
v_mfma_f32_16x16x32_bf16 v[168:171],a[244:247],v[28:31],v[168:171]
v_rcp_f16 v9,v8
v_lshlrev_b32_sdwa v54,1,v54 dst_sel:DWORD dst_unused:UNUSED_PAD src0_sel:DWORD src1_sel:WORD_1
v_mfma_f32_16x16x32_bf16 v[216:219],a[240:243],v[28:31],v[216:219]
ds_read_u16_d16_hi v54,v54
v_lshlrev_b32_sdwa v53,1,v53 dst_sel:DWORD dst_unused:UNUSED_PAD src0_sel:DWORD src1_sel:WORD_1
v_mfma_f32_16x16x32_bf16 v[220:223],a[240:243],v[24:27],v[220:223]
ds_read_u16 v53,v53
ds_read_b64_tr_b16 a[232:233],v3 offset:12384
v_mfma_f32_16x16x32_bf16 v[188:191],a[228:231],v[24:27],v[188:191]
ds_read_b64_tr_b16 a[234:235],v3 offset:13920
v_lshlrev_b32_sdwa v55,1,v55 dst_sel:DWORD dst_unused:UNUSED_PAD src0_sel:DWORD src1_sel:WORD_1
v_mfma_f32_16x16x32_bf16 v[184:187],a[228:231],v[28:31],v[184:187]
ds_read_u16_d16_hi v55,v55
v_rcp_f16_sdwa v9,v8 dst_sel:WORD_1 dst_unused:UNUSED_PRESERVE src0_sel:WORD_1
v_mfma_f32_16x16x32_bf16 v[224:227],a[248:251],v[28:31],v[224:227]
ds_read_b64_tr_b16 a[244:245],v3 offset:12392
v_lshlrev_b32_sdwa v56,1,v56 dst_sel:DWORD dst_unused:UNUSED_PAD src0_sel:DWORD src1_sel:WORD_1
v_mfma_f32_16x16x32_bf16 v[228:231],a[248:251],v[24:27],v[228:231]
ds_read_u16 v56,v56
v_lshlrev_b32_sdwa v58,1,v58 dst_sel:DWORD dst_unused:UNUSED_PAD src0_sel:DWORD src1_sel:WORD_1
v_mfma_f32_16x16x32_bf16 v[204:207],a[236:239],v[24:27],v[204:207]
ds_read_u16_d16_hi v58,v58
v_fma_mix_f32 v57,v57,v9,0 op_sel_hi:[0,1,0]
v_mfma_f32_16x16x32_bf16 v[200:203],a[236:239],v[28:31],v[200:203]
ds_read_b64_tr_b16 a[246:247],v3 offset:13928
v_fma_mix_f32 v59,v59,v9,0 op_sel:[0,1,0] op_sel_hi:[0,1,0]
v_mfma_f32_16x16x32_bf16 v[24:27],a[196:199],a[48:51],v[16:19]
s_add_u32 m0,s62,6160
ds_read_b64_tr_b16 a[240:241],v3 offset:12448
v_lshlrev_b32_sdwa v36,1,v36 dst_sel:DWORD dst_unused:UNUSED_PAD src0_sel:DWORD src1_sel:WORD_1
v_mfma_f32_16x16x32_bf16 v[24:27],a[192:195],a[64:67],v[24:27]
s_add_u32 s68,s68,s59
ds_read_b64_tr_b16 a[242:243],v3 offset:13984
v_fma_mixlo_f16 v8,v49,s99,0
v_mfma_f32_16x16x32_bf16 v[24:27],a[200:203],a[80:83],v[24:27]
s_waitcnt lgkmcnt(8)
buffer_load_dwordx4 v5,s[44:47],s68 offen offset:0 lds
v_lshl_add_u64 v[60:61],v[60:61],0,v[62:63]
v_lshlrev_b32_sdwa v38,1,v38 dst_sel:DWORD dst_unused:UNUSED_PAD src0_sel:DWORD src1_sel:WORD_1
v_mfma_f32_16x16x32_bf16 v[28:31],a[204:207],a[80:83],v[20:23]
s_add_u32 s68,s68,s53
ds_read_b64_tr_b16 a[228:229],v3 offset:12456
v_lshl_add_u64 v[62:63],v[32:33],0,v[34:35]
v_fma_mixhi_f16 v8,v51,s99,0
v_mfma_f32_16x16x32_bf16 v[28:31],a[208:211],a[64:67],v[28:31]
s_waitcnt lgkmcnt(7)
buffer_load_dwordx4 v5,s[44:47],s68 offen offset:1024 lds
v_lshl_add_u64 v[32:33],v[52:53],0,v[54:55]
v_lshlrev_b32_sdwa v37,1,v37 dst_sel:DWORD dst_unused:UNUSED_PAD src0_sel:DWORD src1_sel:WORD_1
v_pk_fma_f16 v9,v8,v8,s54
v_mfma_f32_16x16x32_bf16 v[28:31],a[212:215],a[48:51],v[28:31]
s_add_u32 s68,s68,s53
ds_read_b64_tr_b16 a[230:231],v3 offset:13992
v_lshlrev_b32_sdwa v39,1,v39 dst_sel:DWORD dst_unused:UNUSED_PAD src0_sel:DWORD src1_sel:WORD_1
v_mfma_f32_16x16x32_bf16 v[52:55],a[212:215],a[52:55],v[20:23]
s_waitcnt lgkmcnt(5)
buffer_load_dwordx4 v5,s[44:47],s68 offen offset:2048 lds
v_or_b32 v34,v56,v58
v_pk_mul_f16 v9,v8,v9
v_mfma_f32_16x16x32_bf16 v[52:55],a[208:211],a[68:71],v[52:55]
ds_read_u16 v36,v36
v_cvt_pk_bf16_f32 v35,v57,v59
v_lshlrev_b32_sdwa v40,1,v40 dst_sel:DWORD dst_unused:UNUSED_PAD src0_sel:DWORD src1_sel:WORD_1
v_mfma_f32_16x16x32_bf16 v[52:55],a[204:207],a[84:87],v[52:55]
v_exp_f16 v8,v9
v_mfma_f32_16x16x32_bf16 v[56:59],a[200:203],a[84:87],v[16:19]
ds_read_u16_d16_hi v38,v38
v_lshlrev_b32_sdwa v42,1,v42 dst_sel:DWORD dst_unused:UNUSED_PAD src0_sel:DWORD src1_sel:WORD_1
v_mfma_f32_16x16x32_bf16 v[56:59],a[192:195],a[68:71],v[56:59]
ds_read_u16 v37,v37
ds_read_u16_d16_hi v39,v39
v_lshlrev_b32_sdwa v41,1,v41 dst_sel:DWORD dst_unused:UNUSED_PAD src0_sel:DWORD src1_sel:WORD_1
v_mfma_f32_16x16x32_bf16 v[56:59],a[196:199],a[52:55],v[56:59]
s_add_u32 m0,s62,12320
ds_read_u16 v40,v40
v_exp_f16_sdwa v8,v9 dst_sel:WORD_1 dst_unused:UNUSED_PRESERVE src0_sel:WORD_1
v_mfma_f32_16x16x32_bf16 v[136:139],a[252:255],v[60:63],v[136:139]
s_add_u32 s68,s68,s59
ds_read_u16_d16_hi v42,v42
v_lshlrev_b32_sdwa v43,1,v43 dst_sel:DWORD dst_unused:UNUSED_PAD src0_sel:DWORD src1_sel:WORD_1
v_mfma_f32_16x16x32_bf16 v[140:143],a[252:255],v[32:35],v[140:143]
buffer_load_dwordx4 v5,s[44:47],s68 offen offset:0 lds
ds_read_u16 v41,v41
v_lshlrev_b32_sdwa v44,1,v44 dst_sel:DWORD dst_unused:UNUSED_PAD src0_sel:DWORD src1_sel:WORD_1
v_mfma_f32_16x16x32_bf16 v[68:71],a[224:227],v[32:35],v[68:71]
ds_read_u16_d16_hi v43,v43
v_pk_fma_f16 v8,v8,s55,s55
v_mfma_f32_16x16x32_bf16 v[64:67],a[224:227],v[60:63],v[64:67]
v_rcp_f16 v9,v8
v_lshlrev_b32_sdwa v46,1,v46 dst_sel:DWORD dst_unused:UNUSED_PAD src0_sel:DWORD src1_sel:WORD_1
v_mfma_f32_16x16x32_bf16 v[144:147],a[232:235],v[60:63],v[144:147]
ds_read_u16 v44,v44
v_lshlrev_b32_sdwa v45,1,v45 dst_sel:DWORD dst_unused:UNUSED_PAD src0_sel:DWORD src1_sel:WORD_1
v_mfma_f32_16x16x32_bf16 v[148:151],a[232:235],v[32:35],v[148:151]
s_waitcnt lgkmcnt(8)
ds_read_u16_d16_hi v46,v46
v_lshlrev_b32_sdwa v47,1,v47 dst_sel:DWORD dst_unused:UNUSED_PAD src0_sel:DWORD src1_sel:WORD_1
v_mfma_f32_16x16x32_bf16 v[84:87],a[244:247],v[32:35],v[84:87]
ds_read_u16 v45,v45
v_lshlrev_b32_sdwa v48,1,v48 dst_sel:DWORD dst_unused:UNUSED_PAD src0_sel:DWORD src1_sel:WORD_1
v_mfma_f32_16x16x32_bf16 v[80:83],a[244:247],v[60:63],v[80:83]
ds_read_u16_d16_hi v47,v47
v_rcp_f16_sdwa v9,v8 dst_sel:WORD_1 dst_unused:UNUSED_PRESERVE src0_sel:WORD_1
v_mfma_f32_16x16x32_bf16 v[152:155],a[240:243],v[60:63],v[152:155]
ds_read_u16 v48,v48
v_lshlrev_b32_sdwa v50,1,v50 dst_sel:DWORD dst_unused:UNUSED_PAD src0_sel:DWORD src1_sel:WORD_1
v_mfma_f32_16x16x32_bf16 v[156:159],a[240:243],v[32:35],v[156:159]
ds_read_u16_d16_hi v50,v50
v_fma_mix_f32 v49,v49,v9,0 op_sel_hi:[0,1,0]
v_mfma_f32_16x16x32_bf16 v[100:103],a[228:231],v[32:35],v[100:103]
s_add_u32 s68,s68,s53
ds_read_b64_tr_b16 a[220:221],v1 offset:12320
v_fma_mix_f32 v51,v51,v9,0 op_sel:[0,1,0] op_sel_hi:[0,1,0]
v_mfma_f32_16x16x32_bf16 v[96:99],a[228:231],v[60:63],v[96:99]
buffer_load_dwordx4 v5,s[44:47],s68 offen offset:1024 lds
v_mfma_f32_16x16x32_bf16 v[32:35],a[196:199],a[56:59],v[16:19]
ds_read_b64_tr_b16 a[222:223],v1 offset:13344
v_lshlrev_b32_sdwa v24,1,v24 dst_sel:DWORD dst_unused:UNUSED_PAD src0_sel:DWORD src1_sel:WORD_1
v_mfma_f32_16x16x32_bf16 v[32:35],a[192:195],a[72:75],v[32:35]
ds_read_b64_tr_b16 a[216:217],v1 offset:14368
v_fma_mixlo_f16 v8,v53,s99,0
v_mfma_f32_16x16x32_bf16 v[32:35],a[200:203],a[88:91],v[32:35]
s_waitcnt lgkmcnt(7)
ds_read_b64_tr_b16 a[218:219],v1 offset:15392
v_lshl_add_u64 v[36:37],v[36:37],0,v[38:39]
v_lshlrev_b32_sdwa v26,1,v26 dst_sel:DWORD dst_unused:UNUSED_PAD src0_sel:DWORD src1_sel:WORD_1
v_mfma_f32_16x16x32_bf16 v[60:63],a[204:207],a[88:91],v[20:23]
ds_read_u16 v24,v24
v_lshl_add_u64 v[38:39],v[40:41],0,v[42:43]
v_fma_mixhi_f16 v8,v55,s99,0
v_mfma_f32_16x16x32_bf16 v[60:63],a[208:211],a[72:75],v[60:63]
s_waitcnt lgkmcnt(7)
ds_read_u16_d16_hi v26,v26
v_lshl_add_u64 v[40:41],v[44:45],0,v[46:47]
v_lshlrev_b32_sdwa v25,1,v25 dst_sel:DWORD dst_unused:UNUSED_PAD src0_sel:DWORD src1_sel:WORD_1
v_pk_fma_f16 v9,v8,v8,s54
v_mfma_f32_16x16x32_bf16 v[60:63],a[212:215],a[56:59],v[60:63]
ds_read_u16 v25,v25
v_lshlrev_b32_sdwa v27,1,v27 dst_sel:DWORD dst_unused:UNUSED_PAD src0_sel:DWORD src1_sel:WORD_1
v_mfma_f32_16x16x32_bf16 v[44:47],a[212:215],a[60:63],v[20:23]
s_waitcnt lgkmcnt(7)
ds_read_b128 v[20:23],v4 offset:896
v_or_b32 v42,v48,v50
v_pk_mul_f16 v9,v8,v9
v_mfma_f32_16x16x32_bf16 v[44:47],a[208:211],a[76:79],v[44:47]
ds_read_u16_d16_hi v27,v27
v_cvt_pk_bf16_f32 v43,v49,v51
v_lshlrev_b32_sdwa v28,1,v28 dst_sel:DWORD dst_unused:UNUSED_PAD src0_sel:DWORD src1_sel:WORD_1
v_mfma_f32_16x16x32_bf16 v[44:47],a[204:207],a[92:95],v[44:47]
ds_read_b64_tr_b16 a[212:213],v1 offset:16416
v_exp_f16 v8,v9
v_mfma_f32_16x16x32_bf16 v[48:51],a[200:203],a[92:95],v[16:19]
ds_read_b128 v[16:19],v4 offset:928
ds_read_b64_tr_b16 a[214:215],v1 offset:17440
v_lshlrev_b32_sdwa v30,1,v30 dst_sel:DWORD dst_unused:UNUSED_PAD src0_sel:DWORD src1_sel:WORD_1
v_mfma_f32_16x16x32_bf16 v[48:51],a[192:195],a[76:79],v[48:51]
ds_read_b64_tr_b16 a[208:209],v2 offset:16416
ds_read_b64_tr_b16 a[210:211],v2 offset:17440
v_lshlrev_b32_sdwa v29,1,v29 dst_sel:DWORD dst_unused:UNUSED_PAD src0_sel:DWORD src1_sel:WORD_1
v_mfma_f32_16x16x32_bf16 v[48:51],a[196:199],a[60:63],v[48:51]
TE12_%=:
s_cbranch_execz TP10849_%=
ds_read_u16 v28,v28
v_exp_f16_sdwa v8,v9 dst_sel:WORD_1 dst_unused:UNUSED_PRESERVE src0_sel:WORD_1
v_mfma_f32_16x16x32_bf16 v[112:115],a[252:255],v[36:39],v[112:115]
ds_read_u16_d16_hi v30,v30
v_lshlrev_b32_sdwa v31,1,v31 dst_sel:DWORD dst_unused:UNUSED_PAD src0_sel:DWORD src1_sel:WORD_1
v_mfma_f32_16x16x32_bf16 v[116:119],a[252:255],v[40:43],v[116:119]
ds_read_u16 v29,v29
v_lshlrev_b32_sdwa v56,1,v56 dst_sel:DWORD dst_unused:UNUSED_PAD src0_sel:DWORD src1_sel:WORD_1
v_mfma_f32_16x16x32_bf16 v[76:79],a[224:227],v[40:43],v[76:79]
ds_read_u16_d16_hi v31,v31
v_pk_fma_f16 v8,v8,s55,s55
v_mfma_f32_16x16x32_bf16 v[72:75],a[224:227],v[36:39],v[72:75]
v_rcp_f16 v9,v8
v_lshlrev_b32_sdwa v58,1,v58 dst_sel:DWORD dst_unused:UNUSED_PAD src0_sel:DWORD src1_sel:WORD_1
v_mfma_f32_16x16x32_bf16 v[120:123],a[232:235],v[36:39],v[120:123]
ds_read_b64_tr_b16 a[204:205],v2 offset:14368
v_lshlrev_b32_sdwa v57,1,v57 dst_sel:DWORD dst_unused:UNUSED_PAD src0_sel:DWORD src1_sel:WORD_1
v_mfma_f32_16x16x32_bf16 v[124:127],a[232:235],v[40:43],v[124:127]
ds_read_b64_tr_b16 a[206:207],v2 offset:15392
v_lshlrev_b32_sdwa v59,1,v59 dst_sel:DWORD dst_unused:UNUSED_PAD src0_sel:DWORD src1_sel:WORD_1
v_mfma_f32_16x16x32_bf16 v[92:95],a[244:247],v[40:43],v[92:95]
ds_read_u16 v56,v56
v_lshlrev_b32_sdwa v52,1,v52 dst_sel:DWORD dst_unused:UNUSED_PAD src0_sel:DWORD src1_sel:WORD_1
v_mfma_f32_16x16x32_bf16 v[88:91],a[244:247],v[36:39],v[88:91]
ds_read_u16_d16_hi v58,v58
v_rcp_f16_sdwa v9,v8 dst_sel:WORD_1 dst_unused:UNUSED_PRESERVE src0_sel:WORD_1
v_mfma_f32_16x16x32_bf16 v[128:131],a[240:243],v[36:39],v[128:131]
ds_read_u16 v57,v57
ds_read_u16_d16_hi v59,v59
v_mfma_f32_16x16x32_bf16 v[132:135],a[240:243],v[40:43],v[132:135]
ds_read_b64_tr_b16 a[200:201],v2 offset:12320
ds_read_b64_tr_b16 a[202:203],v2 offset:13344
v_lshlrev_b32_sdwa v54,1,v54 dst_sel:DWORD dst_unused:UNUSED_PAD src0_sel:DWORD src1_sel:WORD_1
v_mfma_f32_16x16x32_bf16 v[108:111],a[228:231],v[40:43],v[108:111]
ds_read_u16 v52,v52
v_fma_mix_f32 v53,v53,v9,0 op_sel_hi:[0,1,0]
v_mfma_f32_16x16x32_bf16 v[104:107],a[228:231],v[36:39],v[104:107]
s_waitcnt lgkmcnt(9)
ds_read_u16_d16_hi v54,v54
v_fma_mix_f32 v55,v55,v9,0 op_sel:[0,1,0] op_sel_hi:[0,1,0]
v_mfma_f32_16x16x32_bf16 v[40:43],a[220:223],a[0:3],v[20:23]
s_add_u32 s68,s68,s53
v_lshlrev_b32_sdwa v32,1,v32 dst_sel:DWORD dst_unused:UNUSED_PAD src0_sel:DWORD src1_sel:WORD_1
v_fma_mixlo_f16 v8,v45,s99,0
v_mfma_f32_16x16x32_bf16 v[40:43],a[216:219],a[16:19],v[40:43]
buffer_load_dwordx4 v5,s[44:47],s68 offen offset:2048 lds
v_lshlrev_b32_sdwa v34,1,v34 dst_sel:DWORD dst_unused:UNUSED_PAD src0_sel:DWORD src1_sel:WORD_1
v_mfma_f32_16x16x32_bf16 v[40:43],a[212:215],a[32:35],v[40:43]
s_add_u32 m0,s62,18480
ds_read_u16 v32,v32
v_lshl_add_u64 v[24:25],v[24:25],0,v[26:27]
v_mfma_f32_16x16x32_bf16 v[36:39],a[208:211],a[32:35],v[16:19]
s_waitcnt lgkmcnt(8)
ds_read_u16_d16_hi v34,v34
v_lshl_add_u64 v[26:27],v[28:29],0,v[30:31]
v_fma_mixhi_f16 v8,v47,s99,0
v_mfma_f32_16x16x32_bf16 v[36:39],a[204:207],a[16:19],v[36:39]
s_waitcnt lgkmcnt(4)
v_lshl_add_u64 v[28:29],v[56:57],0,v[58:59]
v_lshlrev_b32_sdwa v33,1,v33 dst_sel:DWORD dst_unused:UNUSED_PAD src0_sel:DWORD src1_sel:WORD_1
v_pk_fma_f16 v9,v8,v8,s54
v_mfma_f32_16x16x32_bf16 v[36:39],a[200:203],a[0:3],v[36:39]
s_add_u32 s68,s68,s59
ds_read_u16 v33,v33
v_lshlrev_b32_sdwa v35,1,v35 dst_sel:DWORD dst_unused:UNUSED_PAD src0_sel:DWORD src1_sel:WORD_1
v_mfma_f32_16x16x32_bf16 v[56:59],a[200:203],a[4:7],v[16:19]
s_waitcnt lgkmcnt(3)
buffer_load_dwordx4 v5,s[44:47],s68 offen offset:0 lds
v_or_b32 v30,v52,v54
v_pk_mul_f16 v9,v8,v9
v_mfma_f32_16x16x32_bf16 v[56:59],a[204:207],a[20:23],v[56:59]
s_waitcnt vmcnt(28)
ds_read_u16_d16_hi v35,v35
v_cvt_pk_bf16_f32 v31,v53,v55
v_lshlrev_b32_sdwa v60,1,v60 dst_sel:DWORD dst_unused:UNUSED_PAD src0_sel:DWORD src1_sel:WORD_1
v_mfma_f32_16x16x32_bf16 v[56:59],a[208:211],a[36:39],v[56:59]
s_barrier
v_exp_f16 v8,v9
v_mfma_f32_16x16x32_bf16 v[52:55],a[212:215],a[36:39],v[20:23]
s_add_u32 s68,s68,s53
ds_read_u16 v60,v60
v_lshlrev_b32_sdwa v62,1,v62 dst_sel:DWORD dst_unused:UNUSED_PAD src0_sel:DWORD src1_sel:WORD_1
v_mfma_f32_16x16x32_bf16 v[52:55],a[216:219],a[20:23],v[52:55]
buffer_load_dwordx4 v5,s[44:47],s68 offen offset:1024 lds
v_lshlrev_b32_sdwa v61,1,v61 dst_sel:DWORD dst_unused:UNUSED_PAD src0_sel:DWORD src1_sel:WORD_1
v_mfma_f32_16x16x32_bf16 v[52:55],a[220:223],a[4:7],v[52:55]
s_add_u32 m0,s58,12320
v_exp_f16_sdwa v8,v9 dst_sel:WORD_1 dst_unused:UNUSED_PRESERVE src0_sel:WORD_1
v_mfma_f32_16x16x32_bf16 v[232:235],a[252:255],v[24:27],v[232:235]
s_add_u32 s69,s69,s59
ds_read_u16_d16_hi v62,v62
v_lshlrev_b32_sdwa v63,1,v63 dst_sel:DWORD dst_unused:UNUSED_PAD src0_sel:DWORD src1_sel:WORD_1
v_mfma_f32_16x16x32_bf16 v[236:239],a[252:255],v[28:31],v[236:239]
buffer_load_dwordx4 v5,s[44:47],s69 offen offset:0 lds
v_lshlrev_b32_sdwa v48,1,v48 dst_sel:DWORD dst_unused:UNUSED_PAD src0_sel:DWORD src1_sel:WORD_1
v_mfma_f32_16x16x32_bf16 v[164:167],a[224:227],v[28:31],v[164:167]
ds_read_u16 v61,v61
v_pk_fma_f16 v8,v8,s55,s55
v_mfma_f32_16x16x32_bf16 v[160:163],a[224:227],v[24:27],v[160:163]
v_rcp_f16 v9,v8
v_lshlrev_b32_sdwa v50,1,v50 dst_sel:DWORD dst_unused:UNUSED_PAD src0_sel:DWORD src1_sel:WORD_1
v_mfma_f32_16x16x32_bf16 v[240:243],a[232:235],v[24:27],v[240:243]
s_add_u32 s69,s69,s53
ds_read_u16_d16_hi v63,v63
v_lshlrev_b32_sdwa v49,1,v49 dst_sel:DWORD dst_unused:UNUSED_PAD src0_sel:DWORD src1_sel:WORD_1
v_mfma_f32_16x16x32_bf16 v[244:247],a[232:235],v[28:31],v[244:247]
buffer_load_dwordx4 v5,s[44:47],s69 offen offset:1024 lds
v_lshlrev_b32_sdwa v51,1,v51 dst_sel:DWORD dst_unused:UNUSED_PAD src0_sel:DWORD src1_sel:WORD_1
v_mfma_f32_16x16x32_bf16 v[180:183],a[244:247],v[28:31],v[180:183]
ds_read_u16 v48,v48
v_lshlrev_b32_sdwa v44,1,v44 dst_sel:DWORD dst_unused:UNUSED_PAD src0_sel:DWORD src1_sel:WORD_1
v_mfma_f32_16x16x32_bf16 v[176:179],a[244:247],v[24:27],v[176:179]
v_rcp_f16_sdwa v9,v8 dst_sel:WORD_1 dst_unused:UNUSED_PRESERVE src0_sel:WORD_1
v_mfma_f32_16x16x32_bf16 v[248:251],a[240:243],v[24:27],v[248:251]
ds_read_u16_d16_hi v50,v50
ds_read_u16 v49,v49
v_mfma_f32_16x16x32_bf16 v[252:255],a[240:243],v[28:31],v[252:255]
s_add_u32 s69,s69,s53
ds_read_u16_d16_hi v51,v51
v_lshlrev_b32_sdwa v46,1,v46 dst_sel:DWORD dst_unused:UNUSED_PAD src0_sel:DWORD src1_sel:WORD_1
v_mfma_f32_16x16x32_bf16 v[196:199],a[228:231],v[28:31],v[196:199]
buffer_load_dwordx4 v5,s[44:47],s69 offen offset:2048 lds
v_fma_mix_f32 v45,v45,v9,0 op_sel_hi:[0,1,0]
v_mfma_f32_16x16x32_bf16 v[192:195],a[228:231],v[24:27],v[192:195]
s_add_u32 m0,s62,18480
ds_read_u16 v44,v44
ds_read_u16_d16_hi v46,v46
v_fma_mix_f32 v47,v47,v9,0 op_sel:[0,1,0] op_sel_hi:[0,1,0]
v_mfma_f32_16x16x32_bf16 v[28:31],a[220:223],a[8:11],v[20:23]
s_add_u32 s68,s68,s53
ds_read_b64_tr_b16 a[248:249],v3 offset:18480
v_lshlrev_b32_sdwa v40,1,v40 dst_sel:DWORD dst_unused:UNUSED_PAD src0_sel:DWORD src1_sel:WORD_1
v_mfma_f32_16x16x32_bf16 v[28:31],a[216:219],a[24:27],v[28:31]
buffer_load_dwordx4 v5,s[44:47],s68 offen offset:2048 lds
v_fma_mixlo_f16 v8,v57,s99,0
v_mfma_f32_16x16x32_bf16 v[28:31],a[212:215],a[40:43],v[28:31]
s_waitcnt lgkmcnt(7)
ds_read_u16 v40,v40
v_lshl_add_u64 v[32:33],v[32:33],0,v[34:35]
v_lshlrev_b32_sdwa v42,1,v42 dst_sel:DWORD dst_unused:UNUSED_PAD src0_sel:DWORD src1_sel:WORD_1
v_mfma_f32_16x16x32_bf16 v[24:27],a[208:211],a[40:43],v[16:19]
ds_read_u16_d16_hi v42,v42
v_lshl_add_u64 v[34:35],v[60:61],0,v[62:63]
v_fma_mixhi_f16 v8,v59,s99,0
v_mfma_f32_16x16x32_bf16 v[24:27],a[204:207],a[24:27],v[24:27]
s_waitcnt lgkmcnt(5)
ds_read_b64_tr_b16 a[250:251],v3 offset:20016
v_lshl_add_u64 v[60:61],v[48:49],0,v[50:51]
v_lshlrev_b32_sdwa v41,1,v41 dst_sel:DWORD dst_unused:UNUSED_PAD src0_sel:DWORD src1_sel:WORD_1
v_pk_fma_f16 v9,v8,v8,s54
v_mfma_f32_16x16x32_bf16 v[24:27],a[200:203],a[8:11],v[24:27]
ds_read_u16 v41,v41
v_lshlrev_b32_sdwa v43,1,v43 dst_sel:DWORD dst_unused:UNUSED_PAD src0_sel:DWORD src1_sel:WORD_1
v_mfma_f32_16x16x32_bf16 v[48:51],a[200:203],a[12:15],v[16:19]
s_waitcnt lgkmcnt(5)
ds_read_u16_d16_hi v43,v43
v_or_b32 v62,v44,v46
v_pk_mul_f16 v9,v8,v9
v_mfma_f32_16x16x32_bf16 v[48:51],a[204:207],a[28:31],v[48:51]
ds_read_b64_tr_b16 a[236:237],v3 offset:18488
v_cvt_pk_bf16_f32 v63,v45,v47
v_lshlrev_b32_sdwa v36,1,v36 dst_sel:DWORD dst_unused:UNUSED_PAD src0_sel:DWORD src1_sel:WORD_1
v_mfma_f32_16x16x32_bf16 v[48:51],a[208:211],a[44:47],v[48:51]
v_exp_f16 v8,v9
v_mfma_f32_16x16x32_bf16 v[44:47],a[212:215],a[44:47],v[20:23]
ds_read_u16 v36,v36
v_lshlrev_b32_sdwa v38,1,v38 dst_sel:DWORD dst_unused:UNUSED_PAD src0_sel:DWORD src1_sel:WORD_1
v_mfma_f32_16x16x32_bf16 v[44:47],a[216:219],a[28:31],v[44:47]
ds_read_u16_d16_hi v38,v38
v_lshlrev_b32_sdwa v37,1,v37 dst_sel:DWORD dst_unused:UNUSED_PAD src0_sel:DWORD src1_sel:WORD_1
v_mfma_f32_16x16x32_bf16 v[44:47],a[220:223],a[12:15],v[44:47]
v_exp_f16_sdwa v8,v9 dst_sel:WORD_1 dst_unused:UNUSED_PRESERVE src0_sel:WORD_1
v_mfma_f32_16x16x32_bf16 v[208:211],a[252:255],v[32:35],v[208:211]
ds_read_u16 v37,v37
v_lshlrev_b32_sdwa v39,1,v39 dst_sel:DWORD dst_unused:UNUSED_PAD src0_sel:DWORD src1_sel:WORD_1
v_mfma_f32_16x16x32_bf16 v[212:215],a[252:255],v[60:63],v[212:215]
ds_read_u16_d16_hi v39,v39
v_lshlrev_b32_sdwa v52,1,v52 dst_sel:DWORD dst_unused:UNUSED_PAD src0_sel:DWORD src1_sel:WORD_1
v_mfma_f32_16x16x32_bf16 v[172:175],a[224:227],v[60:63],v[172:175]
ds_read_u16 v52,v52
v_pk_fma_f16 v8,v8,s55,s55
v_mfma_f32_16x16x32_bf16 v[168:171],a[224:227],v[32:35],v[168:171]
v_rcp_f16 v9,v8
v_lshlrev_b32_sdwa v54,1,v54 dst_sel:DWORD dst_unused:UNUSED_PAD src0_sel:DWORD src1_sel:WORD_1
v_mfma_f32_16x16x32_bf16 v[216:219],a[232:235],v[32:35],v[216:219]
ds_read_u16_d16_hi v54,v54
v_lshlrev_b32_sdwa v53,1,v53 dst_sel:DWORD dst_unused:UNUSED_PAD src0_sel:DWORD src1_sel:WORD_1
v_mfma_f32_16x16x32_bf16 v[220:223],a[232:235],v[60:63],v[220:223]
ds_read_u16 v53,v53
ds_read_b64_tr_b16 a[238:239],v3 offset:20024
v_mfma_f32_16x16x32_bf16 v[188:191],a[244:247],v[60:63],v[188:191]
ds_read_b64_tr_b16 a[252:253],v3 offset:18544
v_lshlrev_b32_sdwa v55,1,v55 dst_sel:DWORD dst_unused:UNUSED_PAD src0_sel:DWORD src1_sel:WORD_1
v_mfma_f32_16x16x32_bf16 v[184:187],a[244:247],v[32:35],v[184:187]
ds_read_u16_d16_hi v55,v55
v_rcp_f16_sdwa v9,v8 dst_sel:WORD_1 dst_unused:UNUSED_PRESERVE src0_sel:WORD_1
v_mfma_f32_16x16x32_bf16 v[224:227],a[240:243],v[32:35],v[224:227]
ds_read_b64_tr_b16 a[254:255],v3 offset:20080
ds_read_b64_tr_b16 a[224:225],v3 offset:18552
v_lshlrev_b32_sdwa v56,1,v56 dst_sel:DWORD dst_unused:UNUSED_PAD src0_sel:DWORD src1_sel:WORD_1
v_mfma_f32_16x16x32_bf16 v[228:231],a[240:243],v[60:63],v[228:231]
ds_read_u16 v56,v56
v_lshlrev_b32_sdwa v58,1,v58 dst_sel:DWORD dst_unused:UNUSED_PAD src0_sel:DWORD src1_sel:WORD_1
v_mfma_f32_16x16x32_bf16 v[204:207],a[228:231],v[60:63],v[204:207]
ds_read_u16_d16_hi v58,v58
v_fma_mix_f32 v57,v57,v9,0 op_sel_hi:[0,1,0]
v_mfma_f32_16x16x32_bf16 v[200:203],a[228:231],v[32:35],v[200:203]
ds_read_b64_tr_b16 a[226:227],v3 offset:20088
v_fma_mix_f32 v59,v59,v9,0 op_sel:[0,1,0] op_sel_hi:[0,1,0]
v_mfma_f32_16x16x32_bf16 v[60:63],a[220:223],a[48:51],v[20:23]
s_add_u32 m0,s62,24640
ds_read_b64_tr_b16 a[232:233],v3 offset:18608
v_lshlrev_b32_sdwa v28,1,v28 dst_sel:DWORD dst_unused:UNUSED_PAD src0_sel:DWORD src1_sel:WORD_1
v_mfma_f32_16x16x32_bf16 v[60:63],a[216:219],a[64:67],v[60:63]
s_add_u32 s68,s68,s59
ds_read_b64_tr_b16 a[234:235],v3 offset:20144
v_fma_mixlo_f16 v8,v49,s99,0
v_mfma_f32_16x16x32_bf16 v[60:63],a[212:215],a[80:83],v[60:63]
s_waitcnt lgkmcnt(9)
buffer_load_dwordx4 v5,s[44:47],s68 offen offset:0 lds
v_lshl_add_u64 v[40:41],v[40:41],0,v[42:43]
v_lshlrev_b32_sdwa v30,1,v30 dst_sel:DWORD dst_unused:UNUSED_PAD src0_sel:DWORD src1_sel:WORD_1
v_mfma_f32_16x16x32_bf16 v[32:35],a[208:211],a[80:83],v[16:19]
s_add_u32 s68,s68,s53
ds_read_b64_tr_b16 a[244:245],v3 offset:18616
v_lshl_add_u64 v[42:43],v[36:37],0,v[38:39]
v_fma_mixhi_f16 v8,v51,s99,0
v_mfma_f32_16x16x32_bf16 v[32:35],a[204:207],a[64:67],v[32:35]
s_waitcnt lgkmcnt(8)
buffer_load_dwordx4 v5,s[44:47],s68 offen offset:1024 lds
v_lshl_add_u64 v[36:37],v[52:53],0,v[54:55]
v_lshlrev_b32_sdwa v29,1,v29 dst_sel:DWORD dst_unused:UNUSED_PAD src0_sel:DWORD src1_sel:WORD_1
v_pk_fma_f16 v9,v8,v8,s54
v_mfma_f32_16x16x32_bf16 v[32:35],a[200:203],a[48:51],v[32:35]
ds_read_b64_tr_b16 a[246:247],v3 offset:20152
v_lshlrev_b32_sdwa v31,1,v31 dst_sel:DWORD dst_unused:UNUSED_PAD src0_sel:DWORD src1_sel:WORD_1
v_mfma_f32_16x16x32_bf16 v[52:55],a[200:203],a[52:55],v[16:19]
s_waitcnt lgkmcnt(5)
ds_read_u16 v28,v28
v_or_b32 v38,v56,v58
v_pk_mul_f16 v9,v8,v9
v_mfma_f32_16x16x32_bf16 v[52:55],a[204:207],a[68:71],v[52:55]
ds_read_u16_d16_hi v30,v30
v_cvt_pk_bf16_f32 v39,v57,v59
v_lshlrev_b32_sdwa v24,1,v24 dst_sel:DWORD dst_unused:UNUSED_PAD src0_sel:DWORD src1_sel:WORD_1
v_mfma_f32_16x16x32_bf16 v[52:55],a[208:211],a[84:87],v[52:55]
s_add_u32 s68,s68,s53
v_exp_f16 v8,v9
v_mfma_f32_16x16x32_bf16 v[56:59],a[212:215],a[84:87],v[20:23]
buffer_load_dwordx4 v5,s[44:47],s68 offen offset:2048 lds
v_lshlrev_b32_sdwa v26,1,v26 dst_sel:DWORD dst_unused:UNUSED_PAD src0_sel:DWORD src1_sel:WORD_1
v_mfma_f32_16x16x32_bf16 v[56:59],a[216:219],a[68:71],v[56:59]
ds_read_u16 v29,v29
ds_read_u16_d16_hi v31,v31
v_lshlrev_b32_sdwa v25,1,v25 dst_sel:DWORD dst_unused:UNUSED_PAD src0_sel:DWORD src1_sel:WORD_1
v_mfma_f32_16x16x32_bf16 v[56:59],a[220:223],a[52:55],v[56:59]
ds_read_u16 v24,v24
v_exp_f16_sdwa v8,v9 dst_sel:WORD_1 dst_unused:UNUSED_PRESERVE src0_sel:WORD_1
v_mfma_f32_16x16x32_bf16 v[136:139],a[248:251],v[40:43],v[136:139]
ds_read_u16_d16_hi v26,v26
v_lshlrev_b32_sdwa v27,1,v27 dst_sel:DWORD dst_unused:UNUSED_PAD src0_sel:DWORD src1_sel:WORD_1
v_mfma_f32_16x16x32_bf16 v[140:143],a[248:251],v[36:39],v[140:143]
ds_read_u16 v25,v25
v_lshlrev_b32_sdwa v44,1,v44 dst_sel:DWORD dst_unused:UNUSED_PAD src0_sel:DWORD src1_sel:WORD_1
v_mfma_f32_16x16x32_bf16 v[68:71],a[236:239],v[36:39],v[68:71]
ds_read_u16_d16_hi v27,v27
v_pk_fma_f16 v8,v8,s55,s55
v_mfma_f32_16x16x32_bf16 v[64:67],a[236:239],v[40:43],v[64:67]
v_rcp_f16 v9,v8
v_lshlrev_b32_sdwa v46,1,v46 dst_sel:DWORD dst_unused:UNUSED_PAD src0_sel:DWORD src1_sel:WORD_1
v_mfma_f32_16x16x32_bf16 v[144:147],a[252:255],v[40:43],v[144:147]
ds_read_u16 v44,v44
v_lshlrev_b32_sdwa v45,1,v45 dst_sel:DWORD dst_unused:UNUSED_PAD src0_sel:DWORD src1_sel:WORD_1
v_mfma_f32_16x16x32_bf16 v[148:151],a[252:255],v[36:39],v[148:151]
s_waitcnt lgkmcnt(7)
ds_read_u16_d16_hi v46,v46
v_lshlrev_b32_sdwa v47,1,v47 dst_sel:DWORD dst_unused:UNUSED_PAD src0_sel:DWORD src1_sel:WORD_1
v_mfma_f32_16x16x32_bf16 v[84:87],a[224:227],v[36:39],v[84:87]
ds_read_u16 v45,v45
v_lshlrev_b32_sdwa v48,1,v48 dst_sel:DWORD dst_unused:UNUSED_PAD src0_sel:DWORD src1_sel:WORD_1
v_mfma_f32_16x16x32_bf16 v[80:83],a[224:227],v[40:43],v[80:83]
ds_read_u16_d16_hi v47,v47
v_rcp_f16_sdwa v9,v8 dst_sel:WORD_1 dst_unused:UNUSED_PRESERVE src0_sel:WORD_1
v_mfma_f32_16x16x32_bf16 v[152:155],a[232:235],v[40:43],v[152:155]
ds_read_u16 v48,v48
v_lshlrev_b32_sdwa v50,1,v50 dst_sel:DWORD dst_unused:UNUSED_PAD src0_sel:DWORD src1_sel:WORD_1
v_mfma_f32_16x16x32_bf16 v[156:159],a[232:235],v[36:39],v[156:159]
ds_read_u16_d16_hi v50,v50
v_fma_mix_f32 v49,v49,v9,0 op_sel_hi:[0,1,0]
v_mfma_f32_16x16x32_bf16 v[100:103],a[244:247],v[36:39],v[100:103]
ds_read_b64_tr_b16 a[192:193],v1 offset:18480
v_fma_mix_f32 v51,v51,v9,0 op_sel:[0,1,0] op_sel_hi:[0,1,0]
v_mfma_f32_16x16x32_bf16 v[96:99],a[244:247],v[40:43],v[96:99]
ds_read_b64_tr_b16 a[194:195],v1 offset:19504
v_mfma_f32_16x16x32_bf16 v[36:39],a[220:223],a[56:59],v[20:23]
ds_read_b64_tr_b16 a[196:197],v1 offset:20528
v_lshlrev_b32_sdwa v60,1,v60 dst_sel:DWORD dst_unused:UNUSED_PAD src0_sel:DWORD src1_sel:WORD_1
v_mfma_f32_16x16x32_bf16 v[36:39],a[216:219],a[72:75],v[36:39]
ds_read_b64_tr_b16 a[198:199],v1 offset:21552
v_fma_mixlo_f16 v8,v53,s99,0
v_mfma_f32_16x16x32_bf16 v[36:39],a[212:215],a[88:91],v[36:39]
s_waitcnt lgkmcnt(8)
ds_read_u16 v60,v60
v_lshl_add_u64 v[28:29],v[28:29],0,v[30:31]
v_lshlrev_b32_sdwa v62,1,v62 dst_sel:DWORD dst_unused:UNUSED_PAD src0_sel:DWORD src1_sel:WORD_1
v_mfma_f32_16x16x32_bf16 v[40:43],a[208:211],a[88:91],v[16:19]
ds_read_u16_d16_hi v62,v62
v_lshl_add_u64 v[30:31],v[24:25],0,v[26:27]
v_fma_mixhi_f16 v8,v55,s99,0
v_mfma_f32_16x16x32_bf16 v[40:43],a[204:207],a[72:75],v[40:43]
s_waitcnt lgkmcnt(8)
v_lshl_add_u64 v[24:25],v[44:45],0,v[46:47]
v_lshlrev_b32_sdwa v61,1,v61 dst_sel:DWORD dst_unused:UNUSED_PAD src0_sel:DWORD src1_sel:WORD_1
v_pk_fma_f16 v9,v8,v8,s54
v_mfma_f32_16x16x32_bf16 v[40:43],a[200:203],a[56:59],v[40:43]
ds_read_u16 v61,v61
v_lshlrev_b32_sdwa v63,1,v63 dst_sel:DWORD dst_unused:UNUSED_PAD src0_sel:DWORD src1_sel:WORD_1
v_mfma_f32_16x16x32_bf16 v[44:47],a[200:203],a[60:63],v[16:19]
s_waitcnt lgkmcnt(7)
ds_read_b128 v[16:19],v4 offset:1024
v_or_b32 v26,v48,v50
v_pk_mul_f16 v9,v8,v9
v_mfma_f32_16x16x32_bf16 v[44:47],a[204:207],a[76:79],v[44:47]
ds_read_u16_d16_hi v63,v63
v_cvt_pk_bf16_f32 v27,v49,v51
v_lshlrev_b32_sdwa v32,1,v32 dst_sel:DWORD dst_unused:UNUSED_PAD src0_sel:DWORD src1_sel:WORD_1
v_mfma_f32_16x16x32_bf16 v[44:47],a[208:211],a[92:95],v[44:47]
ds_read_b64_tr_b16 a[200:201],v1 offset:22576
v_exp_f16 v8,v9
v_mfma_f32_16x16x32_bf16 v[48:51],a[212:215],a[92:95],v[20:23]
ds_read_b128 v[20:23],v4 offset:1056
ds_read_b64_tr_b16 a[202:203],v1 offset:23600
v_lshlrev_b32_sdwa v34,1,v34 dst_sel:DWORD dst_unused:UNUSED_PAD src0_sel:DWORD src1_sel:WORD_1
v_mfma_f32_16x16x32_bf16 v[48:51],a[216:219],a[76:79],v[48:51]
ds_read_b64_tr_b16 a[204:205],v2 offset:22576
ds_read_b64_tr_b16 a[206:207],v2 offset:23600
v_lshlrev_b32_sdwa v33,1,v33 dst_sel:DWORD dst_unused:UNUSED_PAD src0_sel:DWORD src1_sel:WORD_1
v_mfma_f32_16x16x32_bf16 v[48:51],a[220:223],a[60:63],v[48:51]
ds_read_u16 v32,v32
v_exp_f16_sdwa v8,v9 dst_sel:WORD_1 dst_unused:UNUSED_PRESERVE src0_sel:WORD_1
v_mfma_f32_16x16x32_bf16 v[112:115],a[248:251],v[28:31],v[112:115]
ds_read_u16_d16_hi v34,v34
v_lshlrev_b32_sdwa v35,1,v35 dst_sel:DWORD dst_unused:UNUSED_PAD src0_sel:DWORD src1_sel:WORD_1
v_mfma_f32_16x16x32_bf16 v[116:119],a[248:251],v[24:27],v[116:119]
ds_read_u16 v33,v33
v_lshlrev_b32_sdwa v56,1,v56 dst_sel:DWORD dst_unused:UNUSED_PAD src0_sel:DWORD src1_sel:WORD_1
v_mfma_f32_16x16x32_bf16 v[76:79],a[236:239],v[24:27],v[76:79]
ds_read_u16_d16_hi v35,v35
v_pk_fma_f16 v8,v8,s55,s55
v_mfma_f32_16x16x32_bf16 v[72:75],a[236:239],v[28:31],v[72:75]
v_rcp_f16 v9,v8
v_lshlrev_b32_sdwa v58,1,v58 dst_sel:DWORD dst_unused:UNUSED_PAD src0_sel:DWORD src1_sel:WORD_1
v_mfma_f32_16x16x32_bf16 v[120:123],a[252:255],v[28:31],v[120:123]
ds_read_b64_tr_b16 a[208:209],v2 offset:20528
v_lshlrev_b32_sdwa v57,1,v57 dst_sel:DWORD dst_unused:UNUSED_PAD src0_sel:DWORD src1_sel:WORD_1
v_mfma_f32_16x16x32_bf16 v[124:127],a[252:255],v[24:27],v[124:127]
ds_read_b64_tr_b16 a[210:211],v2 offset:21552
v_lshlrev_b32_sdwa v59,1,v59 dst_sel:DWORD dst_unused:UNUSED_PAD src0_sel:DWORD src1_sel:WORD_1
v_mfma_f32_16x16x32_bf16 v[92:95],a[224:227],v[24:27],v[92:95]
ds_read_u16 v56,v56
v_lshlrev_b32_sdwa v52,1,v52 dst_sel:DWORD dst_unused:UNUSED_PAD src0_sel:DWORD src1_sel:WORD_1
v_mfma_f32_16x16x32_bf16 v[88:91],a[224:227],v[28:31],v[88:91]
ds_read_u16_d16_hi v58,v58
v_rcp_f16_sdwa v9,v8 dst_sel:WORD_1 dst_unused:UNUSED_PRESERVE src0_sel:WORD_1
v_mfma_f32_16x16x32_bf16 v[128:131],a[232:235],v[28:31],v[128:131]
ds_read_u16 v57,v57
ds_read_u16_d16_hi v59,v59
v_mfma_f32_16x16x32_bf16 v[132:135],a[232:235],v[24:27],v[132:135]
ds_read_b64_tr_b16 a[212:213],v2 offset:18480
ds_read_b64_tr_b16 a[214:215],v2 offset:19504
v_lshlrev_b32_sdwa v54,1,v54 dst_sel:DWORD dst_unused:UNUSED_PAD src0_sel:DWORD src1_sel:WORD_1
v_mfma_f32_16x16x32_bf16 v[108:111],a[244:247],v[24:27],v[108:111]
ds_read_u16 v52,v52
v_fma_mix_f32 v53,v53,v9,0 op_sel_hi:[0,1,0]
v_mfma_f32_16x16x32_bf16 v[104:107],a[244:247],v[28:31],v[104:107]
s_waitcnt lgkmcnt(9)
ds_read_u16_d16_hi v54,v54
v_fma_mix_f32 v55,v55,v9,0 op_sel:[0,1,0] op_sel_hi:[0,1,0]
v_mfma_f32_16x16x32_bf16 v[24:27],a[192:195],a[0:3],v[16:19]
v_lshlrev_b32_sdwa v36,1,v36 dst_sel:DWORD dst_unused:UNUSED_PAD src0_sel:DWORD src1_sel:WORD_1
v_fma_mixlo_f16 v8,v45,s99,0
v_mfma_f32_16x16x32_bf16 v[24:27],a[196:199],a[16:19],v[24:27]
ds_read_u16 v36,v36
v_lshlrev_b32_sdwa v38,1,v38 dst_sel:DWORD dst_unused:UNUSED_PAD src0_sel:DWORD src1_sel:WORD_1
v_mfma_f32_16x16x32_bf16 v[24:27],a[200:203],a[32:35],v[24:27]
ds_read_u16_d16_hi v38,v38
v_lshl_add_u64 v[60:61],v[60:61],0,v[62:63]
v_mfma_f32_16x16x32_bf16 v[28:31],a[204:207],a[32:35],v[20:23]
s_waitcnt lgkmcnt(9)
v_lshl_add_u64 v[62:63],v[32:33],0,v[34:35]
v_fma_mixhi_f16 v8,v47,s99,0
v_mfma_f32_16x16x32_bf16 v[28:31],a[208:211],a[16:19],v[28:31]
s_waitcnt lgkmcnt(4)
v_lshl_add_u64 v[32:33],v[56:57],0,v[58:59]
v_lshlrev_b32_sdwa v37,1,v37 dst_sel:DWORD dst_unused:UNUSED_PAD src0_sel:DWORD src1_sel:WORD_1
v_pk_fma_f16 v9,v8,v8,s54
v_mfma_f32_16x16x32_bf16 v[28:31],a[212:215],a[0:3],v[28:31]
ds_read_u16 v37,v37
v_lshlrev_b32_sdwa v39,1,v39 dst_sel:DWORD dst_unused:UNUSED_PAD src0_sel:DWORD src1_sel:WORD_1
v_mfma_f32_16x16x32_bf16 v[56:59],a[212:215],a[4:7],v[20:23]
s_waitcnt lgkmcnt(3)
ds_read_u16_d16_hi v39,v39
v_or_b32 v34,v52,v54
v_pk_mul_f16 v9,v8,v9
v_mfma_f32_16x16x32_bf16 v[56:59],a[208:211],a[20:23],v[56:59]
s_waitcnt vmcnt(29)
v_cvt_pk_bf16_f32 v35,v53,v55
v_lshlrev_b32_sdwa v40,1,v40 dst_sel:DWORD dst_unused:UNUSED_PAD src0_sel:DWORD src1_sel:WORD_1
v_mfma_f32_16x16x32_bf16 v[56:59],a[204:207],a[36:39],v[56:59]
s_barrier
v_exp_f16 v8,v9
v_mfma_f32_16x16x32_bf16 v[52:55],a[200:203],a[36:39],v[16:19]
ds_read_u16 v40,v40
v_lshlrev_b32_sdwa v42,1,v42 dst_sel:DWORD dst_unused:UNUSED_PAD src0_sel:DWORD src1_sel:WORD_1
v_mfma_f32_16x16x32_bf16 v[52:55],a[196:199],a[20:23],v[52:55]
s_add_u32 m0,s58,18480
ds_read_u16_d16_hi v42,v42
v_lshlrev_b32_sdwa v41,1,v41 dst_sel:DWORD dst_unused:UNUSED_PAD src0_sel:DWORD src1_sel:WORD_1
v_mfma_f32_16x16x32_bf16 v[52:55],a[192:195],a[4:7],v[52:55]
s_mov_b32 s69,s88
v_exp_f16_sdwa v8,v9 dst_sel:WORD_1 dst_unused:UNUSED_PRESERVE src0_sel:WORD_1
v_mfma_f32_16x16x32_bf16 v[232:235],a[248:251],v[60:63],v[232:235]
buffer_load_dwordx4 v5,s[44:47],s69 offen offset:0 lds
v_lshlrev_b32_sdwa v43,1,v43 dst_sel:DWORD dst_unused:UNUSED_PAD src0_sel:DWORD src1_sel:WORD_1
v_mfma_f32_16x16x32_bf16 v[236:239],a[248:251],v[32:35],v[236:239]
ds_read_u16 v41,v41
v_lshlrev_b32_sdwa v48,1,v48 dst_sel:DWORD dst_unused:UNUSED_PAD src0_sel:DWORD src1_sel:WORD_1
v_mfma_f32_16x16x32_bf16 v[164:167],a[236:239],v[32:35],v[164:167]
ds_read_u16_d16_hi v43,v43
v_pk_fma_f16 v8,v8,s55,s55
v_mfma_f32_16x16x32_bf16 v[160:163],a[236:239],v[60:63],v[160:163]
v_rcp_f16 v9,v8
v_lshlrev_b32_sdwa v50,1,v50 dst_sel:DWORD dst_unused:UNUSED_PAD src0_sel:DWORD src1_sel:WORD_1
v_mfma_f32_16x16x32_bf16 v[240:243],a[252:255],v[60:63],v[240:243]
s_add_u32 s69,s69,s53
ds_read_u16 v48,v48
v_lshlrev_b32_sdwa v49,1,v49 dst_sel:DWORD dst_unused:UNUSED_PAD src0_sel:DWORD src1_sel:WORD_1
v_mfma_f32_16x16x32_bf16 v[244:247],a[252:255],v[32:35],v[244:247]
buffer_load_dwordx4 v5,s[44:47],s69 offen offset:1024 lds
v_lshlrev_b32_sdwa v51,1,v51 dst_sel:DWORD dst_unused:UNUSED_PAD src0_sel:DWORD src1_sel:WORD_1
v_mfma_f32_16x16x32_bf16 v[180:183],a[224:227],v[32:35],v[180:183]
ds_read_u16_d16_hi v50,v50
v_lshlrev_b32_sdwa v44,1,v44 dst_sel:DWORD dst_unused:UNUSED_PAD src0_sel:DWORD src1_sel:WORD_1
v_mfma_f32_16x16x32_bf16 v[176:179],a[224:227],v[60:63],v[176:179]
v_rcp_f16_sdwa v9,v8 dst_sel:WORD_1 dst_unused:UNUSED_PRESERVE src0_sel:WORD_1
v_mfma_f32_16x16x32_bf16 v[248:251],a[232:235],v[60:63],v[248:251]
ds_read_u16 v49,v49
ds_read_u16_d16_hi v51,v51
v_mfma_f32_16x16x32_bf16 v[252:255],a[232:235],v[32:35],v[252:255]
s_add_u32 s69,s69,s53
ds_read_u16 v44,v44
v_lshlrev_b32_sdwa v46,1,v46 dst_sel:DWORD dst_unused:UNUSED_PAD src0_sel:DWORD src1_sel:WORD_1
v_mfma_f32_16x16x32_bf16 v[196:199],a[244:247],v[32:35],v[196:199]
buffer_load_dwordx4 v5,s[44:47],s69 offen offset:2048 lds
v_fma_mix_f32 v45,v45,v9,0 op_sel_hi:[0,1,0]
v_mfma_f32_16x16x32_bf16 v[192:195],a[244:247],v[60:63],v[192:195]
ds_read_u16_d16_hi v46,v46
v_fma_mix_f32 v47,v47,v9,0 op_sel:[0,1,0] op_sel_hi:[0,1,0]
v_mfma_f32_16x16x32_bf16 v[32:35],a[192:195],a[8:11],v[16:19]
ds_read_b64_tr_b16 a[240:241],v3 offset:0
v_lshlrev_b32_sdwa v24,1,v24 dst_sel:DWORD dst_unused:UNUSED_PAD src0_sel:DWORD src1_sel:WORD_1
v_mfma_f32_16x16x32_bf16 v[32:35],a[196:199],a[24:27],v[32:35]
ds_read_u16 v24,v24
v_fma_mixlo_f16 v8,v57,s99,0
v_mfma_f32_16x16x32_bf16 v[32:35],a[200:203],a[40:43],v[32:35]
s_waitcnt lgkmcnt(7)
ds_read_b64_tr_b16 a[242:243],v3 offset:1536
v_lshl_add_u64 v[36:37],v[36:37],0,v[38:39]
v_lshlrev_b32_sdwa v26,1,v26 dst_sel:DWORD dst_unused:UNUSED_PAD src0_sel:DWORD src1_sel:WORD_1
v_mfma_f32_16x16x32_bf16 v[60:63],a[204:207],a[40:43],v[20:23]
ds_read_u16_d16_hi v26,v26
v_lshl_add_u64 v[38:39],v[40:41],0,v[42:43]
v_fma_mixhi_f16 v8,v59,s99,0
v_mfma_f32_16x16x32_bf16 v[60:63],a[208:211],a[24:27],v[60:63]
s_waitcnt lgkmcnt(6)
ds_read_b64_tr_b16 a[228:229],v3 offset:8
v_lshl_add_u64 v[40:41],v[48:49],0,v[50:51]
v_lshlrev_b32_sdwa v25,1,v25 dst_sel:DWORD dst_unused:UNUSED_PAD src0_sel:DWORD src1_sel:WORD_1
v_pk_fma_f16 v9,v8,v8,s54
v_mfma_f32_16x16x32_bf16 v[60:63],a[212:215],a[8:11],v[60:63]
ds_read_u16 v25,v25
v_lshlrev_b32_sdwa v27,1,v27 dst_sel:DWORD dst_unused:UNUSED_PAD src0_sel:DWORD src1_sel:WORD_1
v_mfma_f32_16x16x32_bf16 v[48:51],a[212:215],a[12:15],v[20:23]
s_waitcnt lgkmcnt(6)
ds_read_u16_d16_hi v27,v27
v_or_b32 v42,v44,v46
v_pk_mul_f16 v9,v8,v9
v_mfma_f32_16x16x32_bf16 v[48:51],a[208:211],a[28:31],v[48:51]
ds_read_b64_tr_b16 a[230:231],v3 offset:1544
v_cvt_pk_bf16_f32 v43,v45,v47
v_lshlrev_b32_sdwa v28,1,v28 dst_sel:DWORD dst_unused:UNUSED_PAD src0_sel:DWORD src1_sel:WORD_1
v_mfma_f32_16x16x32_bf16 v[48:51],a[204:207],a[44:47],v[48:51]
v_exp_f16 v8,v9
v_mfma_f32_16x16x32_bf16 v[44:47],a[200:203],a[44:47],v[16:19]
ds_read_u16 v28,v28
v_lshlrev_b32_sdwa v30,1,v30 dst_sel:DWORD dst_unused:UNUSED_PAD src0_sel:DWORD src1_sel:WORD_1
v_mfma_f32_16x16x32_bf16 v[44:47],a[196:199],a[28:31],v[44:47]
ds_read_u16_d16_hi v30,v30
v_lshlrev_b32_sdwa v29,1,v29 dst_sel:DWORD dst_unused:UNUSED_PAD src0_sel:DWORD src1_sel:WORD_1
v_mfma_f32_16x16x32_bf16 v[44:47],a[192:195],a[12:15],v[44:47]
v_exp_f16_sdwa v8,v9 dst_sel:WORD_1 dst_unused:UNUSED_PRESERVE src0_sel:WORD_1
v_mfma_f32_16x16x32_bf16 v[208:211],a[248:251],v[36:39],v[208:211]
ds_read_u16 v29,v29
v_lshlrev_b32_sdwa v31,1,v31 dst_sel:DWORD dst_unused:UNUSED_PAD src0_sel:DWORD src1_sel:WORD_1
v_mfma_f32_16x16x32_bf16 v[212:215],a[248:251],v[40:43],v[212:215]
ds_read_u16_d16_hi v31,v31
v_lshlrev_b32_sdwa v52,1,v52 dst_sel:DWORD dst_unused:UNUSED_PAD src0_sel:DWORD src1_sel:WORD_1
v_mfma_f32_16x16x32_bf16 v[172:175],a[236:239],v[40:43],v[172:175]
ds_read_u16 v52,v52
v_pk_fma_f16 v8,v8,s55,s55
v_mfma_f32_16x16x32_bf16 v[168:171],a[236:239],v[36:39],v[168:171]
v_rcp_f16 v9,v8
v_lshlrev_b32_sdwa v54,1,v54 dst_sel:DWORD dst_unused:UNUSED_PAD src0_sel:DWORD src1_sel:WORD_1
v_mfma_f32_16x16x32_bf16 v[216:219],a[252:255],v[36:39],v[216:219]
ds_read_u16_d16_hi v54,v54
v_lshlrev_b32_sdwa v53,1,v53 dst_sel:DWORD dst_unused:UNUSED_PAD src0_sel:DWORD src1_sel:WORD_1
v_mfma_f32_16x16x32_bf16 v[220:223],a[252:255],v[40:43],v[220:223]
ds_read_u16 v53,v53
ds_read_b64_tr_b16 a[248:249],v3 offset:64
v_mfma_f32_16x16x32_bf16 v[188:191],a[224:227],v[40:43],v[188:191]
ds_read_b64_tr_b16 a[250:251],v3 offset:1600
v_lshlrev_b32_sdwa v55,1,v55 dst_sel:DWORD dst_unused:UNUSED_PAD src0_sel:DWORD src1_sel:WORD_1
v_mfma_f32_16x16x32_bf16 v[184:187],a[224:227],v[36:39],v[184:187]
ds_read_u16_d16_hi v55,v55
v_rcp_f16_sdwa v9,v8 dst_sel:WORD_1 dst_unused:UNUSED_PRESERVE src0_sel:WORD_1
v_mfma_f32_16x16x32_bf16 v[224:227],a[232:235],v[36:39],v[224:227]
ds_read_b64_tr_b16 a[236:237],v3 offset:72
v_lshlrev_b32_sdwa v56,1,v56 dst_sel:DWORD dst_unused:UNUSED_PAD src0_sel:DWORD src1_sel:WORD_1
v_mfma_f32_16x16x32_bf16 v[228:231],a[232:235],v[40:43],v[228:231]
ds_read_u16 v56,v56
v_lshlrev_b32_sdwa v58,1,v58 dst_sel:DWORD dst_unused:UNUSED_PAD src0_sel:DWORD src1_sel:WORD_1
v_mfma_f32_16x16x32_bf16 v[204:207],a[244:247],v[40:43],v[204:207]
ds_read_u16_d16_hi v58,v58
v_fma_mix_f32 v57,v57,v9,0 op_sel_hi:[0,1,0]
v_mfma_f32_16x16x32_bf16 v[200:203],a[244:247],v[36:39],v[200:203]
ds_read_b64_tr_b16 a[238:239],v3 offset:1608
v_fma_mix_f32 v59,v59,v9,0 op_sel:[0,1,0] op_sel_hi:[0,1,0]
v_mfma_f32_16x16x32_bf16 v[40:43],a[192:195],a[48:51],v[16:19]
ds_read_b64_tr_b16 a[252:253],v3 offset:128
v_lshlrev_b32_sdwa v32,1,v32 dst_sel:DWORD dst_unused:UNUSED_PAD src0_sel:DWORD src1_sel:WORD_1
v_mfma_f32_16x16x32_bf16 v[40:43],a[196:199],a[64:67],v[40:43]
TE13_%=:
s_cbranch_execz TP11368_%=
ds_read_b64_tr_b16 a[254:255],v3 offset:1664
v_fma_mixlo_f16 v8,v49,s99,0
v_mfma_f32_16x16x32_bf16 v[40:43],a[200:203],a[80:83],v[40:43]
s_waitcnt lgkmcnt(8)
ds_read_b64_tr_b16 a[224:225],v3 offset:136
v_lshl_add_u64 v[24:25],v[24:25],0,v[26:27]
v_lshlrev_b32_sdwa v34,1,v34 dst_sel:DWORD dst_unused:UNUSED_PAD src0_sel:DWORD src1_sel:WORD_1
v_mfma_f32_16x16x32_bf16 v[36:39],a[204:207],a[80:83],v[20:23]
ds_read_b64_tr_b16 a[226:227],v3 offset:1672
v_lshl_add_u64 v[26:27],v[28:29],0,v[30:31]
v_fma_mixhi_f16 v8,v51,s99,0
v_mfma_f32_16x16x32_bf16 v[36:39],a[208:211],a[64:67],v[36:39]
s_waitcnt lgkmcnt(8)
ds_read_u16 v32,v32
v_lshl_add_u64 v[28:29],v[52:53],0,v[54:55]
v_lshlrev_b32_sdwa v33,1,v33 dst_sel:DWORD dst_unused:UNUSED_PAD src0_sel:DWORD src1_sel:WORD_1
v_pk_fma_f16 v9,v8,v8,s54
v_mfma_f32_16x16x32_bf16 v[36:39],a[212:215],a[48:51],v[36:39]
ds_read_u16_d16_hi v34,v34
v_lshlrev_b32_sdwa v35,1,v35 dst_sel:DWORD dst_unused:UNUSED_PAD src0_sel:DWORD src1_sel:WORD_1
v_mfma_f32_16x16x32_bf16 v[52:55],a[212:215],a[52:55],v[20:23]
s_waitcnt lgkmcnt(7)
ds_read_u16 v33,v33
v_or_b32 v30,v56,v58
v_pk_mul_f16 v9,v8,v9
v_mfma_f32_16x16x32_bf16 v[52:55],a[208:211],a[68:71],v[52:55]
ds_read_u16_d16_hi v35,v35
v_cvt_pk_bf16_f32 v31,v57,v59
v_lshlrev_b32_sdwa v60,1,v60 dst_sel:DWORD dst_unused:UNUSED_PAD src0_sel:DWORD src1_sel:WORD_1
v_mfma_f32_16x16x32_bf16 v[52:55],a[204:207],a[84:87],v[52:55]
v_exp_f16 v8,v9
v_mfma_f32_16x16x32_bf16 v[56:59],a[200:203],a[84:87],v[16:19]
ds_read_u16 v60,v60
v_lshlrev_b32_sdwa v62,1,v62 dst_sel:DWORD dst_unused:UNUSED_PAD src0_sel:DWORD src1_sel:WORD_1
v_mfma_f32_16x16x32_bf16 v[56:59],a[196:199],a[68:71],v[56:59]
ds_read_u16_d16_hi v62,v62
v_lshlrev_b32_sdwa v61,1,v61 dst_sel:DWORD dst_unused:UNUSED_PAD src0_sel:DWORD src1_sel:WORD_1
v_mfma_f32_16x16x32_bf16 v[56:59],a[192:195],a[52:55],v[56:59]
v_exp_f16_sdwa v8,v9 dst_sel:WORD_1 dst_unused:UNUSED_PRESERVE src0_sel:WORD_1
v_mfma_f32_16x16x32_bf16 v[136:139],a[240:243],v[24:27],v[136:139]
ds_read_u16 v61,v61
v_lshlrev_b32_sdwa v63,1,v63 dst_sel:DWORD dst_unused:UNUSED_PAD src0_sel:DWORD src1_sel:WORD_1
v_mfma_f32_16x16x32_bf16 v[140:143],a[240:243],v[28:31],v[140:143]
ds_read_u16_d16_hi v63,v63
v_lshlrev_b32_sdwa v44,1,v44 dst_sel:DWORD dst_unused:UNUSED_PAD src0_sel:DWORD src1_sel:WORD_1
v_mfma_f32_16x16x32_bf16 v[68:71],a[228:231],v[28:31],v[68:71]
ds_read_u16 v44,v44
v_pk_fma_f16 v8,v8,s55,s55
v_mfma_f32_16x16x32_bf16 v[64:67],a[228:231],v[24:27],v[64:67]
v_rcp_f16 v9,v8
v_lshlrev_b32_sdwa v46,1,v46 dst_sel:DWORD dst_unused:UNUSED_PAD src0_sel:DWORD src1_sel:WORD_1
v_mfma_f32_16x16x32_bf16 v[144:147],a[248:251],v[24:27],v[144:147]
ds_read_u16_d16_hi v46,v46
v_lshlrev_b32_sdwa v45,1,v45 dst_sel:DWORD dst_unused:UNUSED_PAD src0_sel:DWORD src1_sel:WORD_1
v_mfma_f32_16x16x32_bf16 v[148:151],a[248:251],v[28:31],v[148:151]
s_waitcnt lgkmcnt(6)
ds_read_u16 v45,v45
v_lshlrev_b32_sdwa v47,1,v47 dst_sel:DWORD dst_unused:UNUSED_PAD src0_sel:DWORD src1_sel:WORD_1
v_mfma_f32_16x16x32_bf16 v[84:87],a[236:239],v[28:31],v[84:87]
ds_read_u16_d16_hi v47,v47
v_lshlrev_b32_sdwa v48,1,v48 dst_sel:DWORD dst_unused:UNUSED_PAD src0_sel:DWORD src1_sel:WORD_1
v_mfma_f32_16x16x32_bf16 v[80:83],a[236:239],v[24:27],v[80:83]
v_rcp_f16_sdwa v9,v8 dst_sel:WORD_1 dst_unused:UNUSED_PRESERVE src0_sel:WORD_1
v_mfma_f32_16x16x32_bf16 v[152:155],a[252:255],v[24:27],v[152:155]
ds_read_u16 v48,v48
v_lshlrev_b32_sdwa v50,1,v50 dst_sel:DWORD dst_unused:UNUSED_PAD src0_sel:DWORD src1_sel:WORD_1
v_mfma_f32_16x16x32_bf16 v[156:159],a[252:255],v[28:31],v[156:159]
ds_read_u16_d16_hi v50,v50
v_fma_mix_f32 v49,v49,v9,0 op_sel_hi:[0,1,0]
v_mfma_f32_16x16x32_bf16 v[100:103],a[224:227],v[28:31],v[100:103]
ds_read_b64_tr_b16 a[216:217],v1 offset:0
v_fma_mix_f32 v51,v51,v9,0 op_sel:[0,1,0] op_sel_hi:[0,1,0]
v_mfma_f32_16x16x32_bf16 v[96:99],a[224:227],v[24:27],v[96:99]
ds_read_b64_tr_b16 a[218:219],v1 offset:1024
v_mfma_f32_16x16x32_bf16 v[28:31],a[192:195],a[56:59],v[16:19]
ds_read_b64_tr_b16 a[220:221],v1 offset:2048
v_lshlrev_b32_sdwa v40,1,v40 dst_sel:DWORD dst_unused:UNUSED_PAD src0_sel:DWORD src1_sel:WORD_1
v_mfma_f32_16x16x32_bf16 v[28:31],a[196:199],a[72:75],v[28:31]
ds_read_b64_tr_b16 a[222:223],v1 offset:3072
v_fma_mixlo_f16 v8,v53,s99,0
v_mfma_f32_16x16x32_bf16 v[28:31],a[200:203],a[88:91],v[28:31]
ds_read_u16 v40,v40
v_lshl_add_u64 v[32:33],v[32:33],0,v[34:35]
v_lshlrev_b32_sdwa v42,1,v42 dst_sel:DWORD dst_unused:UNUSED_PAD src0_sel:DWORD src1_sel:WORD_1
v_mfma_f32_16x16x32_bf16 v[24:27],a[204:207],a[88:91],v[20:23]
s_waitcnt lgkmcnt(7)
ds_read_u16_d16_hi v42,v42
v_lshl_add_u64 v[34:35],v[60:61],0,v[62:63]
v_fma_mixhi_f16 v8,v55,s99,0
v_mfma_f32_16x16x32_bf16 v[24:27],a[208:211],a[72:75],v[24:27]
v_lshl_add_u64 v[60:61],v[44:45],0,v[46:47]
v_lshlrev_b32_sdwa v41,1,v41 dst_sel:DWORD dst_unused:UNUSED_PAD src0_sel:DWORD src1_sel:WORD_1
v_pk_fma_f16 v9,v8,v8,s54
v_mfma_f32_16x16x32_bf16 v[24:27],a[212:215],a[56:59],v[24:27]
ds_read_u16 v41,v41
v_lshlrev_b32_sdwa v43,1,v43 dst_sel:DWORD dst_unused:UNUSED_PAD src0_sel:DWORD src1_sel:WORD_1
v_mfma_f32_16x16x32_bf16 v[44:47],a[212:215],a[60:63],v[20:23]
s_waitcnt lgkmcnt(7)
ds_read_b128 v[20:23],v4 offset:1152
v_or_b32 v62,v48,v50
v_pk_mul_f16 v9,v8,v9
v_mfma_f32_16x16x32_bf16 v[44:47],a[208:211],a[76:79],v[44:47]
ds_read_u16_d16_hi v43,v43
v_cvt_pk_bf16_f32 v63,v49,v51
v_lshlrev_b32_sdwa v36,1,v36 dst_sel:DWORD dst_unused:UNUSED_PAD src0_sel:DWORD src1_sel:WORD_1
v_mfma_f32_16x16x32_bf16 v[44:47],a[204:207],a[92:95],v[44:47]
ds_read_b64_tr_b16 a[212:213],v1 offset:4096
v_exp_f16 v8,v9
v_mfma_f32_16x16x32_bf16 v[48:51],a[200:203],a[92:95],v[16:19]
ds_read_b128 v[16:19],v4 offset:1184
ds_read_b64_tr_b16 a[214:215],v1 offset:5120
v_lshlrev_b32_sdwa v38,1,v38 dst_sel:DWORD dst_unused:UNUSED_PAD src0_sel:DWORD src1_sel:WORD_1
v_mfma_f32_16x16x32_bf16 v[48:51],a[196:199],a[76:79],v[48:51]
ds_read_b64_tr_b16 a[208:209],v2 offset:4096
ds_read_b64_tr_b16 a[210:211],v2 offset:5120
v_lshlrev_b32_sdwa v37,1,v37 dst_sel:DWORD dst_unused:UNUSED_PAD src0_sel:DWORD src1_sel:WORD_1
v_mfma_f32_16x16x32_bf16 v[48:51],a[192:195],a[60:63],v[48:51]
ds_read_u16 v36,v36
v_exp_f16_sdwa v8,v9 dst_sel:WORD_1 dst_unused:UNUSED_PRESERVE src0_sel:WORD_1
v_mfma_f32_16x16x32_bf16 v[112:115],a[240:243],v[32:35],v[112:115]
ds_read_u16_d16_hi v38,v38
v_lshlrev_b32_sdwa v39,1,v39 dst_sel:DWORD dst_unused:UNUSED_PAD src0_sel:DWORD src1_sel:WORD_1
v_mfma_f32_16x16x32_bf16 v[116:119],a[240:243],v[60:63],v[116:119]
ds_read_u16 v37,v37
v_lshlrev_b32_sdwa v56,1,v56 dst_sel:DWORD dst_unused:UNUSED_PAD src0_sel:DWORD src1_sel:WORD_1
v_mfma_f32_16x16x32_bf16 v[76:79],a[228:231],v[60:63],v[76:79]
ds_read_u16_d16_hi v39,v39
v_pk_fma_f16 v8,v8,s55,s55
v_mfma_f32_16x16x32_bf16 v[72:75],a[228:231],v[32:35],v[72:75]
v_rcp_f16 v9,v8
v_lshlrev_b32_sdwa v58,1,v58 dst_sel:DWORD dst_unused:UNUSED_PAD src0_sel:DWORD src1_sel:WORD_1
v_mfma_f32_16x16x32_bf16 v[120:123],a[248:251],v[32:35],v[120:123]
ds_read_b64_tr_b16 a[204:205],v2 offset:2048
v_lshlrev_b32_sdwa v57,1,v57 dst_sel:DWORD dst_unused:UNUSED_PAD src0_sel:DWORD src1_sel:WORD_1
v_mfma_f32_16x16x32_bf16 v[124:127],a[248:251],v[60:63],v[124:127]
ds_read_b64_tr_b16 a[206:207],v2 offset:3072
v_lshlrev_b32_sdwa v59,1,v59 dst_sel:DWORD dst_unused:UNUSED_PAD src0_sel:DWORD src1_sel:WORD_1
v_mfma_f32_16x16x32_bf16 v[92:95],a[236:239],v[60:63],v[92:95]
ds_read_u16 v56,v56
v_lshlrev_b32_sdwa v52,1,v52 dst_sel:DWORD dst_unused:UNUSED_PAD src0_sel:DWORD src1_sel:WORD_1
v_mfma_f32_16x16x32_bf16 v[88:91],a[236:239],v[32:35],v[88:91]
ds_read_u16_d16_hi v58,v58
v_rcp_f16_sdwa v9,v8 dst_sel:WORD_1 dst_unused:UNUSED_PRESERVE src0_sel:WORD_1
v_mfma_f32_16x16x32_bf16 v[128:131],a[252:255],v[32:35],v[128:131]
ds_read_u16 v57,v57
ds_read_u16_d16_hi v59,v59
v_mfma_f32_16x16x32_bf16 v[132:135],a[252:255],v[60:63],v[132:135]
ds_read_b64_tr_b16 a[200:201],v2 offset:0
ds_read_b64_tr_b16 a[202:203],v2 offset:1024
v_lshlrev_b32_sdwa v54,1,v54 dst_sel:DWORD dst_unused:UNUSED_PAD src0_sel:DWORD src1_sel:WORD_1
v_mfma_f32_16x16x32_bf16 v[108:111],a[224:227],v[60:63],v[108:111]
ds_read_u16 v52,v52
v_fma_mix_f32 v53,v53,v9,0 op_sel_hi:[0,1,0]
v_mfma_f32_16x16x32_bf16 v[104:107],a[224:227],v[32:35],v[104:107]
s_waitcnt lgkmcnt(9)
ds_read_u16_d16_hi v54,v54
v_fma_mix_f32 v55,v55,v9,0 op_sel:[0,1,0] op_sel_hi:[0,1,0]
v_mfma_f32_16x16x32_bf16 v[60:63],a[216:219],a[0:3],v[20:23]
v_lshlrev_b32_sdwa v28,1,v28 dst_sel:DWORD dst_unused:UNUSED_PAD src0_sel:DWORD src1_sel:WORD_1
v_fma_mixlo_f16 v8,v45,s99,0
v_mfma_f32_16x16x32_bf16 v[60:63],a[220:223],a[16:19],v[60:63]
ds_read_u16 v28,v28
v_lshlrev_b32_sdwa v30,1,v30 dst_sel:DWORD dst_unused:UNUSED_PAD src0_sel:DWORD src1_sel:WORD_1
v_mfma_f32_16x16x32_bf16 v[60:63],a[212:215],a[32:35],v[60:63]
ds_read_u16_d16_hi v30,v30
v_lshl_add_u64 v[40:41],v[40:41],0,v[42:43]
v_mfma_f32_16x16x32_bf16 v[32:35],a[208:211],a[32:35],v[16:19]
s_waitcnt lgkmcnt(9)
v_lshl_add_u64 v[42:43],v[36:37],0,v[38:39]
v_fma_mixhi_f16 v8,v47,s99,0
v_mfma_f32_16x16x32_bf16 v[32:35],a[204:207],a[16:19],v[32:35]
s_waitcnt lgkmcnt(4)
v_lshl_add_u64 v[36:37],v[56:57],0,v[58:59]
v_lshlrev_b32_sdwa v29,1,v29 dst_sel:DWORD dst_unused:UNUSED_PAD src0_sel:DWORD src1_sel:WORD_1
v_pk_fma_f16 v9,v8,v8,s54
v_mfma_f32_16x16x32_bf16 v[32:35],a[200:203],a[0:3],v[32:35]
ds_read_u16 v29,v29
v_lshlrev_b32_sdwa v31,1,v31 dst_sel:DWORD dst_unused:UNUSED_PAD src0_sel:DWORD src1_sel:WORD_1
v_mfma_f32_16x16x32_bf16 v[56:59],a[200:203],a[4:7],v[16:19]
s_waitcnt lgkmcnt(3)
ds_read_u16_d16_hi v31,v31
v_or_b32 v38,v52,v54
v_pk_mul_f16 v9,v8,v9
v_mfma_f32_16x16x32_bf16 v[56:59],a[204:207],a[20:23],v[56:59]
s_waitcnt vmcnt(12)
v_cvt_pk_bf16_f32 v39,v53,v55
v_lshlrev_b32_sdwa v24,1,v24 dst_sel:DWORD dst_unused:UNUSED_PAD src0_sel:DWORD src1_sel:WORD_1
v_mfma_f32_16x16x32_bf16 v[56:59],a[208:211],a[36:39],v[56:59]
s_barrier
v_exp_f16 v8,v9
v_mfma_f32_16x16x32_bf16 v[52:55],a[212:215],a[36:39],v[20:23]
ds_read_u16 v24,v24
v_lshlrev_b32_sdwa v26,1,v26 dst_sel:DWORD dst_unused:UNUSED_PAD src0_sel:DWORD src1_sel:WORD_1
v_mfma_f32_16x16x32_bf16 v[52:55],a[220:223],a[20:23],v[52:55]
ds_read_u16_d16_hi v26,v26
v_lshlrev_b32_sdwa v25,1,v25 dst_sel:DWORD dst_unused:UNUSED_PAD src0_sel:DWORD src1_sel:WORD_1
v_mfma_f32_16x16x32_bf16 v[52:55],a[216:219],a[4:7],v[52:55]
v_exp_f16_sdwa v8,v9 dst_sel:WORD_1 dst_unused:UNUSED_PRESERVE src0_sel:WORD_1
v_mfma_f32_16x16x32_bf16 v[232:235],a[240:243],v[40:43],v[232:235]
ds_read_u16 v25,v25
v_lshlrev_b32_sdwa v27,1,v27 dst_sel:DWORD dst_unused:UNUSED_PAD src0_sel:DWORD src1_sel:WORD_1
v_mfma_f32_16x16x32_bf16 v[236:239],a[240:243],v[36:39],v[236:239]
ds_read_u16_d16_hi v27,v27
v_lshlrev_b32_sdwa v48,1,v48 dst_sel:DWORD dst_unused:UNUSED_PAD src0_sel:DWORD src1_sel:WORD_1
v_mfma_f32_16x16x32_bf16 v[164:167],a[228:231],v[36:39],v[164:167]
ds_read_u16 v48,v48
v_pk_fma_f16 v8,v8,s55,s55
v_mfma_f32_16x16x32_bf16 v[160:163],a[228:231],v[40:43],v[160:163]
v_rcp_f16 v9,v8
v_lshlrev_b32_sdwa v50,1,v50 dst_sel:DWORD dst_unused:UNUSED_PAD src0_sel:DWORD src1_sel:WORD_1
v_mfma_f32_16x16x32_bf16 v[240:243],a[248:251],v[40:43],v[240:243]
ds_read_u16_d16_hi v50,v50
v_lshlrev_b32_sdwa v49,1,v49 dst_sel:DWORD dst_unused:UNUSED_PAD src0_sel:DWORD src1_sel:WORD_1
v_mfma_f32_16x16x32_bf16 v[244:247],a[248:251],v[36:39],v[244:247]
ds_read_u16 v49,v49
v_lshlrev_b32_sdwa v51,1,v51 dst_sel:DWORD dst_unused:UNUSED_PAD src0_sel:DWORD src1_sel:WORD_1
v_mfma_f32_16x16x32_bf16 v[180:183],a[236:239],v[36:39],v[180:183]
ds_read_u16_d16_hi v51,v51
v_lshlrev_b32_sdwa v44,1,v44 dst_sel:DWORD dst_unused:UNUSED_PAD src0_sel:DWORD src1_sel:WORD_1
v_mfma_f32_16x16x32_bf16 v[176:179],a[236:239],v[40:43],v[176:179]
v_rcp_f16_sdwa v9,v8 dst_sel:WORD_1 dst_unused:UNUSED_PRESERVE src0_sel:WORD_1
v_mfma_f32_16x16x32_bf16 v[248:251],a[252:255],v[40:43],v[248:251]
ds_read_u16 v44,v44
v_lshlrev_b32_sdwa v46,1,v46 dst_sel:DWORD dst_unused:UNUSED_PAD src0_sel:DWORD src1_sel:WORD_1
v_mfma_f32_16x16x32_bf16 v[252:255],a[252:255],v[36:39],v[252:255]
ds_read_u16_d16_hi v46,v46
v_fma_mix_f32 v45,v45,v9,0 op_sel_hi:[0,1,0]
v_mfma_f32_16x16x32_bf16 v[196:199],a[224:227],v[36:39],v[196:199]
ds_read_b64_tr_b16 a[232:233],v3 offset:6160
v_fma_mix_f32 v47,v47,v9,0 op_sel:[0,1,0] op_sel_hi:[0,1,0]
v_mfma_f32_16x16x32_bf16 v[192:195],a[224:227],v[40:43],v[192:195]
ds_read_b64_tr_b16 a[234:235],v3 offset:7696
v_add_u32 v11,88272,v1
v_add_u32 v12,88272,v2
v_mfma_f32_16x16x32_bf16 v[36:39],a[216:219],a[8:11],v[20:23]
ds_read_b64_tr_b16 a[96:97],v11 offset:0
ds_read_b64_tr_b16 a[98:99],v11 offset:1024
v_lshlrev_b32_sdwa v60,1,v60 dst_sel:DWORD dst_unused:UNUSED_PAD src0_sel:DWORD src1_sel:WORD_1
v_mfma_f32_16x16x32_bf16 v[36:39],a[220:223],a[24:27],v[36:39]
ds_read_b64_tr_b16 a[100:101],v11 offset:2048
ds_read_b64_tr_b16 a[102:103],v11 offset:3072
v_fma_mixlo_f16 v8,v57,s99,0
v_mfma_f32_16x16x32_bf16 v[36:39],a[212:215],a[40:43],v[36:39]
s_waitcnt lgkmcnt(9)
ds_read_u16 v60,v60
ds_read_b64_tr_b16 a[104:105],v11 offset:4096
v_lshl_add_u64 v[28:29],v[28:29],0,v[30:31]
v_lshlrev_b32_sdwa v62,1,v62 dst_sel:DWORD dst_unused:UNUSED_PAD src0_sel:DWORD src1_sel:WORD_1
v_mfma_f32_16x16x32_bf16 v[40:43],a[208:211],a[40:43],v[16:19]
ds_read_u16_d16_hi v62,v62
v_lshl_add_u64 v[30:31],v[24:25],0,v[26:27]
v_fma_mixhi_f16 v8,v59,s99,0
v_mfma_f32_16x16x32_bf16 v[40:43],a[204:207],a[24:27],v[40:43]
s_waitcnt lgkmcnt(11)
ds_read_b64_tr_b16 a[106:107],v11 offset:5120
v_lshl_add_u64 v[24:25],v[48:49],0,v[50:51]
v_lshlrev_b32_sdwa v61,1,v61 dst_sel:DWORD dst_unused:UNUSED_PAD src0_sel:DWORD src1_sel:WORD_1
v_pk_fma_f16 v9,v8,v8,s54
v_mfma_f32_16x16x32_bf16 v[40:43],a[200:203],a[8:11],v[40:43]
ds_read_u16 v61,v61
v_lshlrev_b32_sdwa v63,1,v63 dst_sel:DWORD dst_unused:UNUSED_PAD src0_sel:DWORD src1_sel:WORD_1
v_mfma_f32_16x16x32_bf16 v[48:51],a[200:203],a[12:15],v[16:19]
s_waitcnt lgkmcnt(11)
ds_read_u16_d16_hi v63,v63
v_or_b32 v26,v44,v46
v_pk_mul_f16 v9,v8,v9
v_mfma_f32_16x16x32_bf16 v[48:51],a[204:207],a[28:31],v[48:51]
ds_read_b64_tr_b16 a[244:245],v3 offset:6168
v_cvt_pk_bf16_f32 v27,v45,v47
v_lshlrev_b32_sdwa v32,1,v32 dst_sel:DWORD dst_unused:UNUSED_PAD src0_sel:DWORD src1_sel:WORD_1
v_mfma_f32_16x16x32_bf16 v[48:51],a[208:211],a[44:47],v[48:51]
v_exp_f16 v8,v9
v_mfma_f32_16x16x32_bf16 v[44:47],a[212:215],a[44:47],v[20:23]
ds_read_u16 v32,v32
v_lshlrev_b32_sdwa v34,1,v34 dst_sel:DWORD dst_unused:UNUSED_PAD src0_sel:DWORD src1_sel:WORD_1
v_mfma_f32_16x16x32_bf16 v[44:47],a[220:223],a[28:31],v[44:47]
ds_read_u16_d16_hi v34,v34
v_lshlrev_b32_sdwa v33,1,v33 dst_sel:DWORD dst_unused:UNUSED_PAD src0_sel:DWORD src1_sel:WORD_1
v_mfma_f32_16x16x32_bf16 v[44:47],a[216:219],a[12:15],v[44:47]
ds_read_u16 v33,v33
v_exp_f16_sdwa v8,v9 dst_sel:WORD_1 dst_unused:UNUSED_PRESERVE src0_sel:WORD_1
v_mfma_f32_16x16x32_bf16 v[208:211],a[240:243],v[28:31],v[208:211]
ds_read_b64_tr_b16 a[246:247],v3 offset:7704
v_lshlrev_b32_sdwa v35,1,v35 dst_sel:DWORD dst_unused:UNUSED_PAD src0_sel:DWORD src1_sel:WORD_1
v_mfma_f32_16x16x32_bf16 v[212:215],a[240:243],v[24:27],v[212:215]
ds_read_u16_d16_hi v35,v35
ds_read_b64_tr_b16 a[108:109],v12 offset:4096
v_lshlrev_b32_sdwa v52,1,v52 dst_sel:DWORD dst_unused:UNUSED_PAD src0_sel:DWORD src1_sel:WORD_1
v_mfma_f32_16x16x32_bf16 v[172:175],a[228:231],v[24:27],v[172:175]
ds_read_u16 v52,v52
v_pk_fma_f16 v8,v8,s55,s55
v_mfma_f32_16x16x32_bf16 v[168:171],a[228:231],v[28:31],v[168:171]
ds_read_b64_tr_b16 a[110:111],v12 offset:5120
v_rcp_f16 v9,v8
v_lshlrev_b32_sdwa v54,1,v54 dst_sel:DWORD dst_unused:UNUSED_PAD src0_sel:DWORD src1_sel:WORD_1
v_mfma_f32_16x16x32_bf16 v[216:219],a[248:251],v[28:31],v[216:219]
ds_read_u16_d16_hi v54,v54
ds_read_b64_tr_b16 a[240:241],v3 offset:6224
v_mfma_f32_16x16x32_bf16 v[220:223],a[248:251],v[24:27],v[220:223]
ds_read_b64_tr_b16 a[242:243],v3 offset:7760
ds_read_b64_tr_b16 a[228:229],v3 offset:6232
v_lshlrev_b32_sdwa v53,1,v53 dst_sel:DWORD dst_unused:UNUSED_PAD src0_sel:DWORD src1_sel:WORD_1
v_mfma_f32_16x16x32_bf16 v[188:191],a[236:239],v[24:27],v[188:191]
ds_read_u16 v53,v53
ds_read_b64_tr_b16 a[230:231],v3 offset:7768
v_lshlrev_b32_sdwa v55,1,v55 dst_sel:DWORD dst_unused:UNUSED_PAD src0_sel:DWORD src1_sel:WORD_1
v_mfma_f32_16x16x32_bf16 v[184:187],a[236:239],v[28:31],v[184:187]
ds_read_u16_d16_hi v55,v55
v_rcp_f16_sdwa v9,v8 dst_sel:WORD_1 dst_unused:UNUSED_PRESERVE src0_sel:WORD_1
v_mfma_f32_16x16x32_bf16 v[224:227],a[252:255],v[28:31],v[224:227]
ds_read_b64_tr_b16 a[248:249],v3 offset:6288
ds_read_b64_tr_b16 a[250:251],v3 offset:7824
v_lshlrev_b32_sdwa v56,1,v56 dst_sel:DWORD dst_unused:UNUSED_PAD src0_sel:DWORD src1_sel:WORD_1
v_mfma_f32_16x16x32_bf16 v[228:231],a[252:255],v[24:27],v[228:231]
ds_read_u16 v56,v56
ds_read_b64_tr_b16 a[236:237],v3 offset:6296
v_lshlrev_b32_sdwa v58,1,v58 dst_sel:DWORD dst_unused:UNUSED_PAD src0_sel:DWORD src1_sel:WORD_1
v_mfma_f32_16x16x32_bf16 v[204:207],a[224:227],v[24:27],v[204:207]
ds_read_u16_d16_hi v58,v58
v_fma_mix_f32 v57,v57,v9,0 op_sel_hi:[0,1,0]
v_mfma_f32_16x16x32_bf16 v[200:203],a[224:227],v[28:31],v[200:203]
ds_read_b64_tr_b16 a[238:239],v3 offset:7832
v_fma_mix_f32 v59,v59,v9,0 op_sel:[0,1,0] op_sel_hi:[0,1,0]
v_mfma_f32_16x16x32_bf16 v[24:27],a[216:219],a[48:51],v[20:23]
ds_read_b64_tr_b16 a[112:113],v12 offset:2048
v_lshlrev_b32_sdwa v36,1,v36 dst_sel:DWORD dst_unused:UNUSED_PAD src0_sel:DWORD src1_sel:WORD_1
v_mfma_f32_16x16x32_bf16 v[24:27],a[220:223],a[64:67],v[24:27]
ds_read_b64_tr_b16 a[114:115],v12 offset:3072
v_fma_mixlo_f16 v8,v49,s99,0
v_mfma_f32_16x16x32_bf16 v[24:27],a[212:215],a[80:83],v[24:27]
s_waitcnt lgkmcnt(11)
ds_read_u16 v36,v36
v_lshl_add_u64 v[60:61],v[60:61],0,v[62:63]
v_lshlrev_b32_sdwa v38,1,v38 dst_sel:DWORD dst_unused:UNUSED_PAD src0_sel:DWORD src1_sel:WORD_1
v_mfma_f32_16x16x32_bf16 v[28:31],a[208:211],a[80:83],v[16:19]
ds_read_u16_d16_hi v38,v38
v_lshl_add_u64 v[62:63],v[32:33],0,v[34:35]
v_fma_mixhi_f16 v8,v51,s99,0
v_mfma_f32_16x16x32_bf16 v[28:31],a[204:207],a[64:67],v[28:31]
s_waitcnt lgkmcnt(10)
ds_read_b64_tr_b16 a[116:117],v12 offset:0
v_lshl_add_u64 v[32:33],v[52:53],0,v[54:55]
v_lshlrev_b32_sdwa v37,1,v37 dst_sel:DWORD dst_unused:UNUSED_PAD src0_sel:DWORD src1_sel:WORD_1
v_pk_fma_f16 v9,v8,v8,s54
v_mfma_f32_16x16x32_bf16 v[28:31],a[200:203],a[48:51],v[28:31]
ds_read_u16 v37,v37
v_lshlrev_b32_sdwa v39,1,v39 dst_sel:DWORD dst_unused:UNUSED_PAD src0_sel:DWORD src1_sel:WORD_1
v_mfma_f32_16x16x32_bf16 v[52:55],a[200:203],a[52:55],v[16:19]
s_waitcnt lgkmcnt(7)
ds_read_u16_d16_hi v39,v39
v_or_b32 v34,v56,v58
v_pk_mul_f16 v9,v8,v9
v_mfma_f32_16x16x32_bf16 v[52:55],a[204:207],a[68:71],v[52:55]
ds_read_b64_tr_b16 a[118:119],v12 offset:1024
v_cvt_pk_bf16_f32 v35,v57,v59
v_lshlrev_b32_sdwa v40,1,v40 dst_sel:DWORD dst_unused:UNUSED_PAD src0_sel:DWORD src1_sel:WORD_1
v_mfma_f32_16x16x32_bf16 v[52:55],a[208:211],a[84:87],v[52:55]
v_exp_f16 v8,v9
v_mfma_f32_16x16x32_bf16 v[56:59],a[212:215],a[84:87],v[20:23]
ds_read_u16 v40,v40
ds_read_b64_tr_b16 a[120:121],v12 offset:6160
v_lshlrev_b32_sdwa v42,1,v42 dst_sel:DWORD dst_unused:UNUSED_PAD src0_sel:DWORD src1_sel:WORD_1
v_mfma_f32_16x16x32_bf16 v[56:59],a[220:223],a[68:71],v[56:59]
ds_read_u16_d16_hi v42,v42
ds_read_b64_tr_b16 a[122:123],v12 offset:7184
v_lshlrev_b32_sdwa v41,1,v41 dst_sel:DWORD dst_unused:UNUSED_PAD src0_sel:DWORD src1_sel:WORD_1
v_mfma_f32_16x16x32_bf16 v[56:59],a[216:219],a[52:55],v[56:59]
ds_read_u16 v41,v41
v_exp_f16_sdwa v8,v9 dst_sel:WORD_1 dst_unused:UNUSED_PRESERVE src0_sel:WORD_1
v_mfma_f32_16x16x32_bf16 v[136:139],a[232:235],v[60:63],v[136:139]
ds_read_b64_tr_b16 a[196:197],v1 offset:6160
v_lshlrev_b32_sdwa v43,1,v43 dst_sel:DWORD dst_unused:UNUSED_PAD src0_sel:DWORD src1_sel:WORD_1
v_mfma_f32_16x16x32_bf16 v[140:143],a[232:235],v[32:35],v[140:143]
ds_read_u16_d16_hi v43,v43
v_lshlrev_b32_sdwa v44,1,v44 dst_sel:DWORD dst_unused:UNUSED_PAD src0_sel:DWORD src1_sel:WORD_1
v_mfma_f32_16x16x32_bf16 v[68:71],a[244:247],v[32:35],v[68:71]
ds_read_u16 v44,v44
v_pk_fma_f16 v8,v8,s55,s55
v_mfma_f32_16x16x32_bf16 v[64:67],a[244:247],v[60:63],v[64:67]
v_rcp_f16 v9,v8
v_lshlrev_b32_sdwa v46,1,v46 dst_sel:DWORD dst_unused:UNUSED_PAD src0_sel:DWORD src1_sel:WORD_1
v_mfma_f32_16x16x32_bf16 v[144:147],a[240:243],v[60:63],v[144:147]
ds_read_u16_d16_hi v46,v46
v_lshlrev_b32_sdwa v45,1,v45 dst_sel:DWORD dst_unused:UNUSED_PAD src0_sel:DWORD src1_sel:WORD_1
v_mfma_f32_16x16x32_bf16 v[148:151],a[240:243],v[32:35],v[148:151]
ds_read_u16 v45,v45
ds_read_b64_tr_b16 a[198:199],v1 offset:7184
v_mfma_f32_16x16x32_bf16 v[84:87],a[228:231],v[32:35],v[84:87]
ds_read_b64_tr_b16 a[124:125],v12 offset:8208
v_lshlrev_b32_sdwa v47,1,v47 dst_sel:DWORD dst_unused:UNUSED_PAD src0_sel:DWORD src1_sel:WORD_1
v_mfma_f32_16x16x32_bf16 v[80:83],a[228:231],v[60:63],v[80:83]
ds_read_u16_d16_hi v47,v47
v_rcp_f16_sdwa v9,v8 dst_sel:WORD_1 dst_unused:UNUSED_PRESERVE src0_sel:WORD_1
v_mfma_f32_16x16x32_bf16 v[152:155],a[248:251],v[60:63],v[152:155]
ds_read_b64_tr_b16 a[126:127],v12 offset:9232
v_lshlrev_b32_sdwa v48,1,v48 dst_sel:DWORD dst_unused:UNUSED_PAD src0_sel:DWORD src1_sel:WORD_1
v_mfma_f32_16x16x32_bf16 v[156:159],a[248:251],v[32:35],v[156:159]
s_waitcnt lgkmcnt(8)
ds_read_u16 v48,v48
v_lshlrev_b32_sdwa v50,1,v50 dst_sel:DWORD dst_unused:UNUSED_PAD src0_sel:DWORD src1_sel:WORD_1
v_mfma_f32_16x16x32_bf16 v[100:103],a[236:239],v[32:35],v[100:103]
ds_read_u16_d16_hi v50,v50
v_fma_mix_f32 v49,v49,v9,0 op_sel_hi:[0,1,0]
v_mfma_f32_16x16x32_bf16 v[96:99],a[236:239],v[60:63],v[96:99]
ds_read_b64_tr_b16 a[192:193],v1 offset:8208
v_fma_mix_f32 v51,v51,v9,0 op_sel:[0,1,0] op_sel_hi:[0,1,0]
v_mfma_f32_16x16x32_bf16 v[32:35],a[216:219],a[56:59],v[20:23]
ds_read_b64_tr_b16 a[194:195],v1 offset:9232
v_lshlrev_b32_sdwa v24,1,v24 dst_sel:DWORD dst_unused:UNUSED_PAD src0_sel:DWORD src1_sel:WORD_1
v_mfma_f32_16x16x32_bf16 v[32:35],a[220:223],a[72:75],v[32:35]
ds_read_u16 v24,v24
v_fma_mixlo_f16 v8,v53,s99,0
v_mfma_f32_16x16x32_bf16 v[32:35],a[212:215],a[88:91],v[32:35]
ds_read_b64_tr_b16 a[128:129],v12 offset:10256
v_lshl_add_u64 v[36:37],v[36:37],0,v[38:39]
v_lshlrev_b32_sdwa v26,1,v26 dst_sel:DWORD dst_unused:UNUSED_PAD src0_sel:DWORD src1_sel:WORD_1
v_mfma_f32_16x16x32_bf16 v[60:63],a[208:211],a[88:91],v[16:19]
s_waitcnt lgkmcnt(8)
ds_read_u16_d16_hi v26,v26
v_lshl_add_u64 v[38:39],v[40:41],0,v[42:43]
v_fma_mixhi_f16 v8,v55,s99,0
v_mfma_f32_16x16x32_bf16 v[60:63],a[204:207],a[72:75],v[60:63]
s_waitcnt lgkmcnt(8)
ds_read_b64_tr_b16 a[130:131],v12 offset:11280
v_lshl_add_u64 v[40:41],v[44:45],0,v[46:47]
v_lshlrev_b32_sdwa v25,1,v25 dst_sel:DWORD dst_unused:UNUSED_PAD src0_sel:DWORD src1_sel:WORD_1
v_pk_fma_f16 v9,v8,v8,s54
v_mfma_f32_16x16x32_bf16 v[60:63],a[200:203],a[56:59],v[60:63]
ds_read_u16 v25,v25
v_lshlrev_b32_sdwa v27,1,v27 dst_sel:DWORD dst_unused:UNUSED_PAD src0_sel:DWORD src1_sel:WORD_1
v_mfma_f32_16x16x32_bf16 v[44:47],a[200:203],a[60:63],v[16:19]
s_waitcnt lgkmcnt(7)
ds_read_b128 v[16:19],v4 offset:1280
v_or_b32 v42,v48,v50
v_pk_mul_f16 v9,v8,v9
v_mfma_f32_16x16x32_bf16 v[44:47],a[204:207],a[76:79],v[44:47]
ds_read_u16_d16_hi v27,v27
ds_read_b64_tr_b16 a[200:201],v1 offset:10256
v_cvt_pk_bf16_f32 v43,v49,v51
v_lshlrev_b32_sdwa v28,1,v28 dst_sel:DWORD dst_unused:UNUSED_PAD src0_sel:DWORD src1_sel:WORD_1
v_mfma_f32_16x16x32_bf16 v[44:47],a[208:211],a[92:95],v[44:47]
ds_read_b64_tr_b16 a[202:203],v1 offset:11280
v_exp_f16 v8,v9
v_mfma_f32_16x16x32_bf16 v[48:51],a[212:215],a[92:95],v[20:23]
ds_read_b128 v[20:23],v4 offset:1312
ds_read_b64_tr_b16 a[204:205],v2 offset:10256
v_lshlrev_b32_sdwa v30,1,v30 dst_sel:DWORD dst_unused:UNUSED_PAD src0_sel:DWORD src1_sel:WORD_1
v_mfma_f32_16x16x32_bf16 v[48:51],a[220:223],a[76:79],v[48:51]
ds_read_b64_tr_b16 a[206:207],v2 offset:11280
ds_read_u16 v28,v28
v_lshlrev_b32_sdwa v29,1,v29 dst_sel:DWORD dst_unused:UNUSED_PAD src0_sel:DWORD src1_sel:WORD_1
v_mfma_f32_16x16x32_bf16 v[48:51],a[216:219],a[60:63],v[48:51]
ds_read_u16_d16_hi v30,v30
v_exp_f16_sdwa v8,v9 dst_sel:WORD_1 dst_unused:UNUSED_PRESERVE src0_sel:WORD_1
v_mfma_f32_16x16x32_bf16 v[112:115],a[232:235],v[36:39],v[112:115]
ds_read_u16 v29,v29
ds_read_b64_tr_b16 a[208:209],v2 offset:8208
v_lshlrev_b32_sdwa v31,1,v31 dst_sel:DWORD dst_unused:UNUSED_PAD src0_sel:DWORD src1_sel:WORD_1
v_mfma_f32_16x16x32_bf16 v[116:119],a[232:235],v[40:43],v[116:119]
ds_read_u16_d16_hi v31,v31
ds_read_b64_tr_b16 a[210:211],v2 offset:9232
v_lshlrev_b32_sdwa v56,1,v56 dst_sel:DWORD dst_unused:UNUSED_PAD src0_sel:DWORD src1_sel:WORD_1
v_mfma_f32_16x16x32_bf16 v[76:79],a[244:247],v[40:43],v[76:79]
ds_read_u16 v56,v56
ds_read_b64_tr_b16 a[212:213],v2 offset:6160
v_pk_fma_f16 v8,v8,s55,s55
v_mfma_f32_16x16x32_bf16 v[72:75],a[244:247],v[36:39],v[72:75]
v_rcp_f16 v9,v8
v_lshlrev_b32_sdwa v58,1,v58 dst_sel:DWORD dst_unused:UNUSED_PAD src0_sel:DWORD src1_sel:WORD_1
v_mfma_f32_16x16x32_bf16 v[120:123],a[240:243],v[36:39],v[120:123]
ds_read_u16_d16_hi v58,v58
v_lshlrev_b32_sdwa v57,1,v57 dst_sel:DWORD dst_unused:UNUSED_PAD src0_sel:DWORD src1_sel:WORD_1
v_mfma_f32_16x16x32_bf16 v[124:127],a[240:243],v[40:43],v[124:127]
ds_read_u16 v57,v57
ds_read_b64_tr_b16 a[214:215],v2 offset:7184
v_mfma_f32_16x16x32_bf16 v[92:95],a[228:231],v[40:43],v[92:95]
ds_read_b64_tr_b16 a[132:133],v11 offset:10256
v_lshlrev_b32_sdwa v59,1,v59 dst_sel:DWORD dst_unused:UNUSED_PAD src0_sel:DWORD src1_sel:WORD_1
v_mfma_f32_16x16x32_bf16 v[88:91],a[228:231],v[36:39],v[88:91]
ds_read_u16_d16_hi v59,v59
v_rcp_f16_sdwa v9,v8 dst_sel:WORD_1 dst_unused:UNUSED_PRESERVE src0_sel:WORD_1
v_mfma_f32_16x16x32_bf16 v[128:131],a[248:251],v[36:39],v[128:131]
ds_read_b64_tr_b16 a[134:135],v11 offset:11280
v_lshlrev_b32_sdwa v52,1,v52 dst_sel:DWORD dst_unused:UNUSED_PAD src0_sel:DWORD src1_sel:WORD_1
v_mfma_f32_16x16x32_bf16 v[132:135],a[248:251],v[40:43],v[132:135]
ds_read_u16 v52,v52
v_lshlrev_b32_sdwa v54,1,v54 dst_sel:DWORD dst_unused:UNUSED_PAD src0_sel:DWORD src1_sel:WORD_1
v_mfma_f32_16x16x32_bf16 v[108:111],a[236:239],v[40:43],v[108:111]
ds_read_u16_d16_hi v54,v54
v_fma_mix_f32 v53,v53,v9,0 op_sel_hi:[0,1,0]
v_mfma_f32_16x16x32_bf16 v[104:107],a[236:239],v[36:39],v[104:107]
s_waitcnt lgkmcnt(8)
ds_read_b64_tr_b16 a[136:137],v11 offset:8208
v_fma_mix_f32 v55,v55,v9,0 op_sel:[0,1,0] op_sel_hi:[0,1,0]
v_mfma_f32_16x16x32_bf16 v[40:43],a[196:199],a[0:3],v[16:19]
ds_read_b64_tr_b16 a[138:139],v11 offset:9232
v_lshlrev_b32_sdwa v32,1,v32 dst_sel:DWORD dst_unused:UNUSED_PAD src0_sel:DWORD src1_sel:WORD_1
v_mfma_f32_16x16x32_bf16 v[40:43],a[192:195],a[16:19],v[40:43]
ds_read_u16 v32,v32
v_fma_mixlo_f16 v8,v45,s99,0
v_mfma_f32_16x16x32_bf16 v[40:43],a[200:203],a[32:35],v[40:43]
ds_read_b64_tr_b16 a[140:141],v11 offset:6160
v_lshl_add_u64 v[24:25],v[24:25],0,v[26:27]
v_lshlrev_b32_sdwa v34,1,v34 dst_sel:DWORD dst_unused:UNUSED_PAD src0_sel:DWORD src1_sel:WORD_1
v_mfma_f32_16x16x32_bf16 v[36:39],a[204:207],a[32:35],v[20:23]
ds_read_u16_d16_hi v34,v34
v_lshl_add_u64 v[26:27],v[28:29],0,v[30:31]
v_fma_mixhi_f16 v8,v47,s99,0
v_mfma_f32_16x16x32_bf16 v[36:39],a[208:211],a[16:19],v[36:39]
s_waitcnt lgkmcnt(8)
ds_read_b64_tr_b16 a[142:143],v11 offset:7184
v_lshl_add_u64 v[28:29],v[56:57],0,v[58:59]
v_lshlrev_b32_sdwa v33,1,v33 dst_sel:DWORD dst_unused:UNUSED_PAD src0_sel:DWORD src1_sel:WORD_1
v_pk_fma_f16 v9,v8,v8,s54
v_mfma_f32_16x16x32_bf16 v[36:39],a[212:215],a[0:3],v[36:39]
ds_read_u16 v33,v33
v_lshlrev_b32_sdwa v35,1,v35 dst_sel:DWORD dst_unused:UNUSED_PAD src0_sel:DWORD src1_sel:WORD_1
v_mfma_f32_16x16x32_bf16 v[56:59],a[212:215],a[4:7],v[20:23]
s_waitcnt lgkmcnt(7)
ds_read_u16_d16_hi v35,v35
v_or_b32 v30,v52,v54
v_pk_mul_f16 v9,v8,v9
v_mfma_f32_16x16x32_bf16 v[56:59],a[208:211],a[20:23],v[56:59]
s_waitcnt vmcnt(6)
ds_read_b64_tr_b16 a[144:145],v11 offset:12320
v_cvt_pk_bf16_f32 v31,v53,v55
v_lshlrev_b32_sdwa v60,1,v60 dst_sel:DWORD dst_unused:UNUSED_PAD src0_sel:DWORD src1_sel:WORD_1
v_mfma_f32_16x16x32_bf16 v[56:59],a[204:207],a[36:39],v[56:59]
s_barrier
v_exp_f16 v8,v9
v_mfma_f32_16x16x32_bf16 v[52:55],a[200:203],a[36:39],v[16:19]
ds_read_u16 v60,v60
v_lshlrev_b32_sdwa v62,1,v62 dst_sel:DWORD dst_unused:UNUSED_PAD src0_sel:DWORD src1_sel:WORD_1
v_mfma_f32_16x16x32_bf16 v[52:55],a[192:195],a[20:23],v[52:55]
TP9790_%=:
s_cbranch_execz TR1_%=
ds_read_u16_d16_hi v62,v62
v_lshlrev_b32_sdwa v61,1,v61 dst_sel:DWORD dst_unused:UNUSED_PAD src0_sel:DWORD src1_sel:WORD_1
v_mfma_f32_16x16x32_bf16 v[52:55],a[196:199],a[4:7],v[52:55]
v_exp_f16_sdwa v8,v9 dst_sel:WORD_1 dst_unused:UNUSED_PRESERVE src0_sel:WORD_1
v_mfma_f32_16x16x32_bf16 v[232:235],a[232:235],v[24:27],v[232:235]
ds_read_u16 v61,v61
v_lshlrev_b32_sdwa v63,1,v63 dst_sel:DWORD dst_unused:UNUSED_PAD src0_sel:DWORD src1_sel:WORD_1
v_mfma_f32_16x16x32_bf16 v[236:239],a[232:235],v[28:31],v[236:239]
ds_read_u16_d16_hi v63,v63
v_lshlrev_b32_sdwa v48,1,v48 dst_sel:DWORD dst_unused:UNUSED_PAD src0_sel:DWORD src1_sel:WORD_1
v_mfma_f32_16x16x32_bf16 v[164:167],a[244:247],v[28:31],v[164:167]
ds_read_u16 v48,v48
v_pk_fma_f16 v8,v8,s55,s55
v_mfma_f32_16x16x32_bf16 v[160:163],a[244:247],v[24:27],v[160:163]
v_rcp_f16 v9,v8
v_lshlrev_b32_sdwa v50,1,v50 dst_sel:DWORD dst_unused:UNUSED_PAD src0_sel:DWORD src1_sel:WORD_1
v_mfma_f32_16x16x32_bf16 v[240:243],a[240:243],v[24:27],v[240:243]
ds_read_u16_d16_hi v50,v50
v_lshlrev_b32_sdwa v49,1,v49 dst_sel:DWORD dst_unused:UNUSED_PAD src0_sel:DWORD src1_sel:WORD_1
v_mfma_f32_16x16x32_bf16 v[244:247],a[240:243],v[28:31],v[244:247]
ds_read_u16 v49,v49
v_lshlrev_b32_sdwa v51,1,v51 dst_sel:DWORD dst_unused:UNUSED_PAD src0_sel:DWORD src1_sel:WORD_1
v_mfma_f32_16x16x32_bf16 v[180:183],a[228:231],v[28:31],v[180:183]
ds_read_u16_d16_hi v51,v51
v_lshlrev_b32_sdwa v44,1,v44 dst_sel:DWORD dst_unused:UNUSED_PAD src0_sel:DWORD src1_sel:WORD_1
v_mfma_f32_16x16x32_bf16 v[176:179],a[228:231],v[24:27],v[176:179]
ds_read_u16 v44,v44
v_rcp_f16_sdwa v9,v8 dst_sel:WORD_1 dst_unused:UNUSED_PRESERVE src0_sel:WORD_1
v_mfma_f32_16x16x32_bf16 v[248:251],a[248:251],v[24:27],v[248:251]
ds_read_b64_tr_b16 a[146:147],v11 offset:13344
v_lshlrev_b32_sdwa v46,1,v46 dst_sel:DWORD dst_unused:UNUSED_PAD src0_sel:DWORD src1_sel:WORD_1
v_mfma_f32_16x16x32_bf16 v[252:255],a[248:251],v[28:31],v[252:255]
ds_read_u16_d16_hi v46,v46
v_fma_mix_f32 v45,v45,v9,0 op_sel_hi:[0,1,0]
v_mfma_f32_16x16x32_bf16 v[196:199],a[236:239],v[28:31],v[196:199]
ds_read_b64_tr_b16 a[148:149],v11 offset:14368
v_fma_mix_f32 v47,v47,v9,0 op_sel:[0,1,0] op_sel_hi:[0,1,0]
v_mfma_f32_16x16x32_bf16 v[192:195],a[236:239],v[24:27],v[192:195]
ds_read_b64_tr_b16 a[150:151],v11 offset:15392
v_mfma_f32_16x16x32_bf16 v[28:31],a[196:199],a[8:11],v[16:19]
ds_read_b64_tr_b16 a[152:153],v11 offset:16416
v_lshlrev_b32_sdwa v40,1,v40 dst_sel:DWORD dst_unused:UNUSED_PAD src0_sel:DWORD src1_sel:WORD_1
v_mfma_f32_16x16x32_bf16 v[28:31],a[192:195],a[24:27],v[28:31]
ds_read_u16 v40,v40
ds_read_b64_tr_b16 a[154:155],v11 offset:17440
v_fma_mixlo_f16 v8,v57,s99,0
v_mfma_f32_16x16x32_bf16 v[28:31],a[200:203],a[40:43],v[28:31]
s_waitcnt lgkmcnt(9)
ds_read_b64_tr_b16 a[252:253],v3 offset:12320
v_lshl_add_u64 v[32:33],v[32:33],0,v[34:35]
v_lshlrev_b32_sdwa v42,1,v42 dst_sel:DWORD dst_unused:UNUSED_PAD src0_sel:DWORD src1_sel:WORD_1
v_mfma_f32_16x16x32_bf16 v[24:27],a[204:207],a[40:43],v[20:23]
ds_read_u16_d16_hi v42,v42
v_lshl_add_u64 v[34:35],v[60:61],0,v[62:63]
v_fma_mixhi_f16 v8,v59,s99,0
v_mfma_f32_16x16x32_bf16 v[24:27],a[208:211],a[24:27],v[24:27]
s_waitcnt lgkmcnt(9)
ds_read_b64_tr_b16 a[254:255],v3 offset:13856
v_lshl_add_u64 v[60:61],v[48:49],0,v[50:51]
v_lshlrev_b32_sdwa v41,1,v41 dst_sel:DWORD dst_unused:UNUSED_PAD src0_sel:DWORD src1_sel:WORD_1
v_pk_fma_f16 v9,v8,v8,s54
v_mfma_f32_16x16x32_bf16 v[24:27],a[212:215],a[8:11],v[24:27]
ds_read_u16 v41,v41
v_lshlrev_b32_sdwa v43,1,v43 dst_sel:DWORD dst_unused:UNUSED_PAD src0_sel:DWORD src1_sel:WORD_1
v_mfma_f32_16x16x32_bf16 v[48:51],a[212:215],a[12:15],v[20:23]
s_waitcnt lgkmcnt(9)
ds_read_u16_d16_hi v43,v43
v_or_b32 v62,v44,v46
v_pk_mul_f16 v9,v8,v9
v_mfma_f32_16x16x32_bf16 v[48:51],a[208:211],a[28:31],v[48:51]
ds_read_b64_tr_b16 a[224:225],v3 offset:12328
v_cvt_pk_bf16_f32 v63,v45,v47
v_lshlrev_b32_sdwa v36,1,v36 dst_sel:DWORD dst_unused:UNUSED_PAD src0_sel:DWORD src1_sel:WORD_1
v_mfma_f32_16x16x32_bf16 v[48:51],a[204:207],a[44:47],v[48:51]
v_exp_f16 v8,v9
v_mfma_f32_16x16x32_bf16 v[44:47],a[200:203],a[44:47],v[16:19]
ds_read_u16 v36,v36
v_lshlrev_b32_sdwa v38,1,v38 dst_sel:DWORD dst_unused:UNUSED_PAD src0_sel:DWORD src1_sel:WORD_1
v_mfma_f32_16x16x32_bf16 v[44:47],a[192:195],a[28:31],v[44:47]
ds_read_u16_d16_hi v38,v38
v_lshlrev_b32_sdwa v37,1,v37 dst_sel:DWORD dst_unused:UNUSED_PAD src0_sel:DWORD src1_sel:WORD_1
v_mfma_f32_16x16x32_bf16 v[44:47],a[196:199],a[12:15],v[44:47]
ds_read_u16 v37,v37
v_exp_f16_sdwa v8,v9 dst_sel:WORD_1 dst_unused:UNUSED_PRESERVE src0_sel:WORD_1
v_mfma_f32_16x16x32_bf16 v[208:211],a[232:235],v[32:35],v[208:211]
ds_read_b64_tr_b16 a[226:227],v3 offset:13864
v_lshlrev_b32_sdwa v39,1,v39 dst_sel:DWORD dst_unused:UNUSED_PAD src0_sel:DWORD src1_sel:WORD_1
v_mfma_f32_16x16x32_bf16 v[212:215],a[232:235],v[60:63],v[212:215]
ds_read_u16_d16_hi v39,v39
ds_read_b64_tr_b16 a[156:157],v12 offset:16416
v_lshlrev_b32_sdwa v52,1,v52 dst_sel:DWORD dst_unused:UNUSED_PAD src0_sel:DWORD src1_sel:WORD_1
v_mfma_f32_16x16x32_bf16 v[172:175],a[244:247],v[60:63],v[172:175]
ds_read_u16 v52,v52
v_pk_fma_f16 v8,v8,s55,s55
v_mfma_f32_16x16x32_bf16 v[168:171],a[244:247],v[32:35],v[168:171]
ds_read_b64_tr_b16 a[158:159],v12 offset:17440
v_rcp_f16 v9,v8
v_lshlrev_b32_sdwa v54,1,v54 dst_sel:DWORD dst_unused:UNUSED_PAD src0_sel:DWORD src1_sel:WORD_1
v_mfma_f32_16x16x32_bf16 v[216:219],a[240:243],v[32:35],v[216:219]
ds_read_u16_d16_hi v54,v54
ds_read_b64_tr_b16 a[232:233],v3 offset:12384
v_mfma_f32_16x16x32_bf16 v[220:223],a[240:243],v[60:63],v[220:223]
ds_read_b64_tr_b16 a[234:235],v3 offset:13920
ds_read_b64_tr_b16 a[244:245],v3 offset:12392
v_lshlrev_b32_sdwa v53,1,v53 dst_sel:DWORD dst_unused:UNUSED_PAD src0_sel:DWORD src1_sel:WORD_1
v_mfma_f32_16x16x32_bf16 v[188:191],a[228:231],v[60:63],v[188:191]
ds_read_u16 v53,v53
ds_read_b64_tr_b16 a[246:247],v3 offset:13928
v_lshlrev_b32_sdwa v55,1,v55 dst_sel:DWORD dst_unused:UNUSED_PAD src0_sel:DWORD src1_sel:WORD_1
v_mfma_f32_16x16x32_bf16 v[184:187],a[228:231],v[32:35],v[184:187]
ds_read_u16_d16_hi v55,v55
v_rcp_f16_sdwa v9,v8 dst_sel:WORD_1 dst_unused:UNUSED_PRESERVE src0_sel:WORD_1
v_mfma_f32_16x16x32_bf16 v[224:227],a[248:251],v[32:35],v[224:227]
ds_read_b64_tr_b16 a[240:241],v3 offset:12448
ds_read_b64_tr_b16 a[242:243],v3 offset:13984
v_lshlrev_b32_sdwa v56,1,v56 dst_sel:DWORD dst_unused:UNUSED_PAD src0_sel:DWORD src1_sel:WORD_1
v_mfma_f32_16x16x32_bf16 v[228:231],a[248:251],v[60:63],v[228:231]
ds_read_u16 v56,v56
ds_read_b64_tr_b16 a[228:229],v3 offset:12456
v_lshlrev_b32_sdwa v58,1,v58 dst_sel:DWORD dst_unused:UNUSED_PAD src0_sel:DWORD src1_sel:WORD_1
v_mfma_f32_16x16x32_bf16 v[204:207],a[236:239],v[60:63],v[204:207]
ds_read_u16_d16_hi v58,v58
v_fma_mix_f32 v57,v57,v9,0 op_sel_hi:[0,1,0]
v_mfma_f32_16x16x32_bf16 v[200:203],a[236:239],v[32:35],v[200:203]
ds_read_b64_tr_b16 a[230:231],v3 offset:13992
v_fma_mix_f32 v59,v59,v9,0 op_sel:[0,1,0] op_sel_hi:[0,1,0]
v_mfma_f32_16x16x32_bf16 v[60:63],a[196:199],a[48:51],v[16:19]
ds_read_b64_tr_b16 a[160:161],v12 offset:14368
v_lshlrev_b32_sdwa v28,1,v28 dst_sel:DWORD dst_unused:UNUSED_PAD src0_sel:DWORD src1_sel:WORD_1
v_mfma_f32_16x16x32_bf16 v[60:63],a[192:195],a[64:67],v[60:63]
ds_read_b64_tr_b16 a[162:163],v12 offset:15392
v_fma_mixlo_f16 v8,v49,s99,0
v_mfma_f32_16x16x32_bf16 v[60:63],a[200:203],a[80:83],v[60:63]
s_waitcnt lgkmcnt(11)
ds_read_u16 v28,v28
v_lshl_add_u64 v[40:41],v[40:41],0,v[42:43]
v_lshlrev_b32_sdwa v30,1,v30 dst_sel:DWORD dst_unused:UNUSED_PAD src0_sel:DWORD src1_sel:WORD_1
v_mfma_f32_16x16x32_bf16 v[32:35],a[204:207],a[80:83],v[20:23]
ds_read_u16_d16_hi v30,v30
v_lshl_add_u64 v[42:43],v[36:37],0,v[38:39]
v_fma_mixhi_f16 v8,v51,s99,0
v_mfma_f32_16x16x32_bf16 v[32:35],a[208:211],a[64:67],v[32:35]
s_waitcnt lgkmcnt(10)
ds_read_b64_tr_b16 a[164:165],v12 offset:12320
v_lshl_add_u64 v[36:37],v[52:53],0,v[54:55]
v_lshlrev_b32_sdwa v29,1,v29 dst_sel:DWORD dst_unused:UNUSED_PAD src0_sel:DWORD src1_sel:WORD_1
v_pk_fma_f16 v9,v8,v8,s54
v_mfma_f32_16x16x32_bf16 v[32:35],a[212:215],a[48:51],v[32:35]
ds_read_u16 v29,v29
v_lshlrev_b32_sdwa v31,1,v31 dst_sel:DWORD dst_unused:UNUSED_PAD src0_sel:DWORD src1_sel:WORD_1
v_mfma_f32_16x16x32_bf16 v[52:55],a[212:215],a[52:55],v[20:23]
s_waitcnt lgkmcnt(7)
ds_read_u16_d16_hi v31,v31
v_or_b32 v38,v56,v58
v_pk_mul_f16 v9,v8,v9
v_mfma_f32_16x16x32_bf16 v[52:55],a[208:211],a[68:71],v[52:55]
ds_read_b64_tr_b16 a[166:167],v12 offset:13344
v_cvt_pk_bf16_f32 v39,v57,v59
v_lshlrev_b32_sdwa v24,1,v24 dst_sel:DWORD dst_unused:UNUSED_PAD src0_sel:DWORD src1_sel:WORD_1
v_mfma_f32_16x16x32_bf16 v[52:55],a[204:207],a[84:87],v[52:55]
v_exp_f16 v8,v9
v_mfma_f32_16x16x32_bf16 v[56:59],a[200:203],a[84:87],v[16:19]
ds_read_u16 v24,v24
ds_read_b64_tr_b16 a[168:169],v12 offset:18480
v_lshlrev_b32_sdwa v26,1,v26 dst_sel:DWORD dst_unused:UNUSED_PAD src0_sel:DWORD src1_sel:WORD_1
v_mfma_f32_16x16x32_bf16 v[56:59],a[192:195],a[68:71],v[56:59]
ds_read_u16_d16_hi v26,v26
ds_read_b64_tr_b16 a[170:171],v12 offset:19504
v_lshlrev_b32_sdwa v25,1,v25 dst_sel:DWORD dst_unused:UNUSED_PAD src0_sel:DWORD src1_sel:WORD_1
v_mfma_f32_16x16x32_bf16 v[56:59],a[196:199],a[52:55],v[56:59]
ds_read_u16 v25,v25
v_exp_f16_sdwa v8,v9 dst_sel:WORD_1 dst_unused:UNUSED_PRESERVE src0_sel:WORD_1
v_mfma_f32_16x16x32_bf16 v[136:139],a[252:255],v[40:43],v[136:139]
ds_read_b64_tr_b16 a[172:173],v12 offset:20528
v_lshlrev_b32_sdwa v27,1,v27 dst_sel:DWORD dst_unused:UNUSED_PAD src0_sel:DWORD src1_sel:WORD_1
v_mfma_f32_16x16x32_bf16 v[140:143],a[252:255],v[36:39],v[140:143]
ds_read_u16_d16_hi v27,v27
v_lshlrev_b32_sdwa v44,1,v44 dst_sel:DWORD dst_unused:UNUSED_PAD src0_sel:DWORD src1_sel:WORD_1
v_mfma_f32_16x16x32_bf16 v[68:71],a[224:227],v[36:39],v[68:71]
ds_read_u16 v44,v44
v_pk_fma_f16 v8,v8,s55,s55
v_mfma_f32_16x16x32_bf16 v[64:67],a[224:227],v[40:43],v[64:67]
v_rcp_f16 v9,v8
v_lshlrev_b32_sdwa v46,1,v46 dst_sel:DWORD dst_unused:UNUSED_PAD src0_sel:DWORD src1_sel:WORD_1
v_mfma_f32_16x16x32_bf16 v[144:147],a[232:235],v[40:43],v[144:147]
ds_read_u16_d16_hi v46,v46
v_lshlrev_b32_sdwa v45,1,v45 dst_sel:DWORD dst_unused:UNUSED_PAD src0_sel:DWORD src1_sel:WORD_1
v_mfma_f32_16x16x32_bf16 v[148:151],a[232:235],v[36:39],v[148:151]
ds_read_u16 v45,v45
ds_read_b64_tr_b16 a[174:175],v12 offset:21552
v_mfma_f32_16x16x32_bf16 v[84:87],a[244:247],v[36:39],v[84:87]
ds_read_b64_tr_b16 a[220:221],v1 offset:12320
v_lshlrev_b32_sdwa v47,1,v47 dst_sel:DWORD dst_unused:UNUSED_PAD src0_sel:DWORD src1_sel:WORD_1
v_mfma_f32_16x16x32_bf16 v[80:83],a[244:247],v[40:43],v[80:83]
ds_read_u16_d16_hi v47,v47
v_rcp_f16_sdwa v9,v8 dst_sel:WORD_1 dst_unused:UNUSED_PRESERVE src0_sel:WORD_1
v_mfma_f32_16x16x32_bf16 v[152:155],a[240:243],v[40:43],v[152:155]
ds_read_b64_tr_b16 a[222:223],v1 offset:13344
v_lshlrev_b32_sdwa v48,1,v48 dst_sel:DWORD dst_unused:UNUSED_PAD src0_sel:DWORD src1_sel:WORD_1
v_mfma_f32_16x16x32_bf16 v[156:159],a[240:243],v[36:39],v[156:159]
s_waitcnt lgkmcnt(8)
ds_read_u16 v48,v48
v_lshlrev_b32_sdwa v50,1,v50 dst_sel:DWORD dst_unused:UNUSED_PAD src0_sel:DWORD src1_sel:WORD_1
v_mfma_f32_16x16x32_bf16 v[100:103],a[228:231],v[36:39],v[100:103]
ds_read_u16_d16_hi v50,v50
v_fma_mix_f32 v49,v49,v9,0 op_sel_hi:[0,1,0]
v_mfma_f32_16x16x32_bf16 v[96:99],a[228:231],v[40:43],v[96:99]
ds_read_b64_tr_b16 a[216:217],v1 offset:14368
v_fma_mix_f32 v51,v51,v9,0 op_sel:[0,1,0] op_sel_hi:[0,1,0]
v_mfma_f32_16x16x32_bf16 v[36:39],a[196:199],a[56:59],v[16:19]
ds_read_b64_tr_b16 a[218:219],v1 offset:15392
v_lshlrev_b32_sdwa v60,1,v60 dst_sel:DWORD dst_unused:UNUSED_PAD src0_sel:DWORD src1_sel:WORD_1
v_mfma_f32_16x16x32_bf16 v[36:39],a[192:195],a[72:75],v[36:39]
ds_read_u16 v60,v60
v_fma_mixlo_f16 v8,v53,s99,0
v_mfma_f32_16x16x32_bf16 v[36:39],a[200:203],a[88:91],v[36:39]
ds_read_b64_tr_b16 a[176:177],v12 offset:22576
v_lshl_add_u64 v[28:29],v[28:29],0,v[30:31]
v_lshlrev_b32_sdwa v62,1,v62 dst_sel:DWORD dst_unused:UNUSED_PAD src0_sel:DWORD src1_sel:WORD_1
v_mfma_f32_16x16x32_bf16 v[40:43],a[204:207],a[88:91],v[20:23]
s_waitcnt lgkmcnt(8)
ds_read_u16_d16_hi v62,v62
v_lshl_add_u64 v[30:31],v[24:25],0,v[26:27]
v_fma_mixhi_f16 v8,v55,s99,0
v_mfma_f32_16x16x32_bf16 v[40:43],a[208:211],a[72:75],v[40:43]
s_waitcnt lgkmcnt(8)
ds_read_b64_tr_b16 a[178:179],v12 offset:23600
v_lshl_add_u64 v[24:25],v[44:45],0,v[46:47]
v_lshlrev_b32_sdwa v61,1,v61 dst_sel:DWORD dst_unused:UNUSED_PAD src0_sel:DWORD src1_sel:WORD_1
v_pk_fma_f16 v9,v8,v8,s54
v_mfma_f32_16x16x32_bf16 v[40:43],a[212:215],a[56:59],v[40:43]
ds_read_u16 v61,v61
v_lshlrev_b32_sdwa v63,1,v63 dst_sel:DWORD dst_unused:UNUSED_PAD src0_sel:DWORD src1_sel:WORD_1
v_mfma_f32_16x16x32_bf16 v[44:47],a[212:215],a[60:63],v[20:23]
s_waitcnt lgkmcnt(7)
ds_read_b128 v[20:23],v4 offset:1408
v_or_b32 v26,v48,v50
v_pk_mul_f16 v9,v8,v9
v_mfma_f32_16x16x32_bf16 v[44:47],a[208:211],a[76:79],v[44:47]
ds_read_u16_d16_hi v63,v63
ds_read_b64_tr_b16 a[212:213],v1 offset:16416
v_cvt_pk_bf16_f32 v27,v49,v51
v_lshlrev_b32_sdwa v32,1,v32 dst_sel:DWORD dst_unused:UNUSED_PAD src0_sel:DWORD src1_sel:WORD_1
v_mfma_f32_16x16x32_bf16 v[44:47],a[204:207],a[92:95],v[44:47]
ds_read_b64_tr_b16 a[214:215],v1 offset:17440
v_exp_f16 v8,v9
v_mfma_f32_16x16x32_bf16 v[48:51],a[200:203],a[92:95],v[16:19]
ds_read_b128 v[16:19],v4 offset:1440
ds_read_b64_tr_b16 a[208:209],v2 offset:16416
v_lshlrev_b32_sdwa v34,1,v34 dst_sel:DWORD dst_unused:UNUSED_PAD src0_sel:DWORD src1_sel:WORD_1
v_mfma_f32_16x16x32_bf16 v[48:51],a[192:195],a[76:79],v[48:51]
ds_read_b64_tr_b16 a[210:211],v2 offset:17440
ds_read_u16 v32,v32
v_lshlrev_b32_sdwa v33,1,v33 dst_sel:DWORD dst_unused:UNUSED_PAD src0_sel:DWORD src1_sel:WORD_1
v_mfma_f32_16x16x32_bf16 v[48:51],a[196:199],a[60:63],v[48:51]
ds_read_u16_d16_hi v34,v34
v_exp_f16_sdwa v8,v9 dst_sel:WORD_1 dst_unused:UNUSED_PRESERVE src0_sel:WORD_1
v_mfma_f32_16x16x32_bf16 v[112:115],a[252:255],v[28:31],v[112:115]
ds_read_u16 v33,v33
ds_read_b64_tr_b16 a[204:205],v2 offset:14368
v_lshlrev_b32_sdwa v35,1,v35 dst_sel:DWORD dst_unused:UNUSED_PAD src0_sel:DWORD src1_sel:WORD_1
v_mfma_f32_16x16x32_bf16 v[116:119],a[252:255],v[24:27],v[116:119]
ds_read_u16_d16_hi v35,v35
ds_read_b64_tr_b16 a[206:207],v2 offset:15392
v_lshlrev_b32_sdwa v56,1,v56 dst_sel:DWORD dst_unused:UNUSED_PAD src0_sel:DWORD src1_sel:WORD_1
v_mfma_f32_16x16x32_bf16 v[76:79],a[224:227],v[24:27],v[76:79]
ds_read_u16 v56,v56
ds_read_b64_tr_b16 a[200:201],v2 offset:12320
v_pk_fma_f16 v8,v8,s55,s55
v_mfma_f32_16x16x32_bf16 v[72:75],a[224:227],v[28:31],v[72:75]
v_rcp_f16 v9,v8
v_lshlrev_b32_sdwa v58,1,v58 dst_sel:DWORD dst_unused:UNUSED_PAD src0_sel:DWORD src1_sel:WORD_1
v_mfma_f32_16x16x32_bf16 v[120:123],a[232:235],v[28:31],v[120:123]
ds_read_u16_d16_hi v58,v58
v_lshlrev_b32_sdwa v57,1,v57 dst_sel:DWORD dst_unused:UNUSED_PAD src0_sel:DWORD src1_sel:WORD_1
v_mfma_f32_16x16x32_bf16 v[124:127],a[232:235],v[24:27],v[124:127]
ds_read_u16 v57,v57
ds_read_b64_tr_b16 a[202:203],v2 offset:13344
v_mfma_f32_16x16x32_bf16 v[92:95],a[244:247],v[24:27],v[92:95]
ds_read_b64_tr_b16 a[180:181],v11 offset:22576
v_lshlrev_b32_sdwa v59,1,v59 dst_sel:DWORD dst_unused:UNUSED_PAD src0_sel:DWORD src1_sel:WORD_1
v_mfma_f32_16x16x32_bf16 v[88:91],a[244:247],v[28:31],v[88:91]
ds_read_u16_d16_hi v59,v59
v_rcp_f16_sdwa v9,v8 dst_sel:WORD_1 dst_unused:UNUSED_PRESERVE src0_sel:WORD_1
v_mfma_f32_16x16x32_bf16 v[128:131],a[240:243],v[28:31],v[128:131]
ds_read_b64_tr_b16 a[182:183],v11 offset:23600
v_lshlrev_b32_sdwa v52,1,v52 dst_sel:DWORD dst_unused:UNUSED_PAD src0_sel:DWORD src1_sel:WORD_1
v_mfma_f32_16x16x32_bf16 v[132:135],a[240:243],v[24:27],v[132:135]
ds_read_u16 v52,v52
v_lshlrev_b32_sdwa v54,1,v54 dst_sel:DWORD dst_unused:UNUSED_PAD src0_sel:DWORD src1_sel:WORD_1
v_mfma_f32_16x16x32_bf16 v[108:111],a[228:231],v[24:27],v[108:111]
ds_read_u16_d16_hi v54,v54
v_fma_mix_f32 v53,v53,v9,0 op_sel_hi:[0,1,0]
v_mfma_f32_16x16x32_bf16 v[104:107],a[228:231],v[28:31],v[104:107]
s_waitcnt lgkmcnt(8)
ds_read_b64_tr_b16 a[184:185],v11 offset:20528
v_fma_mix_f32 v55,v55,v9,0 op_sel:[0,1,0] op_sel_hi:[0,1,0]
v_mfma_f32_16x16x32_bf16 v[24:27],a[220:223],a[0:3],v[20:23]
ds_read_b64_tr_b16 a[186:187],v11 offset:21552
v_lshlrev_b32_sdwa v36,1,v36 dst_sel:DWORD dst_unused:UNUSED_PAD src0_sel:DWORD src1_sel:WORD_1
v_mfma_f32_16x16x32_bf16 v[24:27],a[216:219],a[16:19],v[24:27]
ds_read_u16 v36,v36
v_fma_mixlo_f16 v8,v45,s99,0
v_mfma_f32_16x16x32_bf16 v[24:27],a[212:215],a[32:35],v[24:27]
ds_read_b64_tr_b16 a[188:189],v11 offset:18480
v_lshl_add_u64 v[60:61],v[60:61],0,v[62:63]
v_lshlrev_b32_sdwa v38,1,v38 dst_sel:DWORD dst_unused:UNUSED_PAD src0_sel:DWORD src1_sel:WORD_1
v_mfma_f32_16x16x32_bf16 v[28:31],a[208:211],a[32:35],v[16:19]
ds_read_u16_d16_hi v38,v38
v_lshl_add_u64 v[62:63],v[32:33],0,v[34:35]
v_fma_mixhi_f16 v8,v47,s99,0
v_mfma_f32_16x16x32_bf16 v[28:31],a[204:207],a[16:19],v[28:31]
s_waitcnt lgkmcnt(8)
ds_read_b64_tr_b16 a[190:191],v11 offset:19504
v_lshl_add_u64 v[32:33],v[56:57],0,v[58:59]
v_lshlrev_b32_sdwa v37,1,v37 dst_sel:DWORD dst_unused:UNUSED_PAD src0_sel:DWORD src1_sel:WORD_1
v_pk_fma_f16 v9,v8,v8,s54
v_mfma_f32_16x16x32_bf16 v[28:31],a[200:203],a[0:3],v[28:31]
ds_read_u16 v37,v37
v_lshlrev_b32_sdwa v39,1,v39 dst_sel:DWORD dst_unused:UNUSED_PAD src0_sel:DWORD src1_sel:WORD_1
v_mfma_f32_16x16x32_bf16 v[56:59],a[200:203],a[4:7],v[16:19]
s_waitcnt lgkmcnt(7)
ds_read_u16_d16_hi v39,v39
v_or_b32 v34,v52,v54
v_pk_mul_f16 v9,v8,v9
v_mfma_f32_16x16x32_bf16 v[56:59],a[204:207],a[20:23],v[56:59]
s_waitcnt vmcnt(0)
v_cvt_pk_bf16_f32 v35,v53,v55
v_lshlrev_b32_sdwa v40,1,v40 dst_sel:DWORD dst_unused:UNUSED_PAD src0_sel:DWORD src1_sel:WORD_1
v_mfma_f32_16x16x32_bf16 v[56:59],a[208:211],a[36:39],v[56:59]
s_barrier
v_exp_f16 v8,v9
v_mfma_f32_16x16x32_bf16 v[52:55],a[212:215],a[36:39],v[20:23]
ds_read_u16 v40,v40
v_lshlrev_b32_sdwa v42,1,v42 dst_sel:DWORD dst_unused:UNUSED_PAD src0_sel:DWORD src1_sel:WORD_1
v_mfma_f32_16x16x32_bf16 v[52:55],a[216:219],a[20:23],v[52:55]
ds_read_u16_d16_hi v42,v42
v_lshlrev_b32_sdwa v41,1,v41 dst_sel:DWORD dst_unused:UNUSED_PAD src0_sel:DWORD src1_sel:WORD_1
v_mfma_f32_16x16x32_bf16 v[52:55],a[220:223],a[4:7],v[52:55]
v_exp_f16_sdwa v8,v9 dst_sel:WORD_1 dst_unused:UNUSED_PRESERVE src0_sel:WORD_1
v_mfma_f32_16x16x32_bf16 v[232:235],a[252:255],v[60:63],v[232:235]
ds_read_u16 v41,v41
v_lshlrev_b32_sdwa v43,1,v43 dst_sel:DWORD dst_unused:UNUSED_PAD src0_sel:DWORD src1_sel:WORD_1
v_mfma_f32_16x16x32_bf16 v[236:239],a[252:255],v[32:35],v[236:239]
ds_read_u16_d16_hi v43,v43
v_lshlrev_b32_sdwa v48,1,v48 dst_sel:DWORD dst_unused:UNUSED_PAD src0_sel:DWORD src1_sel:WORD_1
v_mfma_f32_16x16x32_bf16 v[164:167],a[224:227],v[32:35],v[164:167]
ds_read_u16 v48,v48
v_pk_fma_f16 v8,v8,s55,s55
v_mfma_f32_16x16x32_bf16 v[160:163],a[224:227],v[60:63],v[160:163]
v_rcp_f16 v9,v8
v_lshlrev_b32_sdwa v50,1,v50 dst_sel:DWORD dst_unused:UNUSED_PAD src0_sel:DWORD src1_sel:WORD_1
v_mfma_f32_16x16x32_bf16 v[240:243],a[232:235],v[60:63],v[240:243]
ds_read_u16_d16_hi v50,v50
v_lshlrev_b32_sdwa v49,1,v49 dst_sel:DWORD dst_unused:UNUSED_PAD src0_sel:DWORD src1_sel:WORD_1
v_mfma_f32_16x16x32_bf16 v[244:247],a[232:235],v[32:35],v[244:247]
ds_read_u16 v49,v49
v_lshlrev_b32_sdwa v51,1,v51 dst_sel:DWORD dst_unused:UNUSED_PAD src0_sel:DWORD src1_sel:WORD_1
v_mfma_f32_16x16x32_bf16 v[180:183],a[244:247],v[32:35],v[180:183]
ds_read_u16_d16_hi v51,v51
v_lshlrev_b32_sdwa v44,1,v44 dst_sel:DWORD dst_unused:UNUSED_PAD src0_sel:DWORD src1_sel:WORD_1
v_mfma_f32_16x16x32_bf16 v[176:179],a[244:247],v[60:63],v[176:179]
v_rcp_f16_sdwa v9,v8 dst_sel:WORD_1 dst_unused:UNUSED_PRESERVE src0_sel:WORD_1
v_mfma_f32_16x16x32_bf16 v[248:251],a[240:243],v[60:63],v[248:251]
ds_read_u16 v44,v44
v_lshlrev_b32_sdwa v46,1,v46 dst_sel:DWORD dst_unused:UNUSED_PAD src0_sel:DWORD src1_sel:WORD_1
v_mfma_f32_16x16x32_bf16 v[252:255],a[240:243],v[32:35],v[252:255]
ds_read_u16_d16_hi v46,v46
v_fma_mix_f32 v45,v45,v9,0 op_sel_hi:[0,1,0]
v_mfma_f32_16x16x32_bf16 v[196:199],a[228:231],v[32:35],v[196:199]
ds_read_b64_tr_b16 a[248:249],v3 offset:18480
v_fma_mix_f32 v47,v47,v9,0 op_sel:[0,1,0] op_sel_hi:[0,1,0]
v_mfma_f32_16x16x32_bf16 v[192:195],a[228:231],v[60:63],v[192:195]
ds_read_b64_tr_b16 a[250:251],v3 offset:20016
v_mfma_f32_16x16x32_bf16 v[32:35],a[220:223],a[8:11],v[20:23]
ds_read_b64_tr_b16 a[236:237],v3 offset:18488
v_lshlrev_b32_sdwa v24,1,v24 dst_sel:DWORD dst_unused:UNUSED_PAD src0_sel:DWORD src1_sel:WORD_1
v_mfma_f32_16x16x32_bf16 v[32:35],a[216:219],a[24:27],v[32:35]
ds_read_u16 v24,v24
v_fma_mixlo_f16 v8,v57,s99,0
v_mfma_f32_16x16x32_bf16 v[32:35],a[212:215],a[40:43],v[32:35]
s_waitcnt lgkmcnt(7)
ds_read_b64_tr_b16 a[238:239],v3 offset:20024
v_lshl_add_u64 v[36:37],v[36:37],0,v[38:39]
v_lshlrev_b32_sdwa v26,1,v26 dst_sel:DWORD dst_unused:UNUSED_PAD src0_sel:DWORD src1_sel:WORD_1
v_mfma_f32_16x16x32_bf16 v[60:63],a[208:211],a[40:43],v[16:19]
ds_read_u16_d16_hi v26,v26
v_lshl_add_u64 v[38:39],v[40:41],0,v[42:43]
v_fma_mixhi_f16 v8,v59,s99,0
v_mfma_f32_16x16x32_bf16 v[60:63],a[204:207],a[24:27],v[60:63]
s_waitcnt lgkmcnt(8)
ds_read_b64_tr_b16 a[192:193],v1 offset:18480
v_lshl_add_u64 v[40:41],v[48:49],0,v[50:51]
v_lshlrev_b32_sdwa v25,1,v25 dst_sel:DWORD dst_unused:UNUSED_PAD src0_sel:DWORD src1_sel:WORD_1
v_pk_fma_f16 v9,v8,v8,s54
v_mfma_f32_16x16x32_bf16 v[60:63],a[200:203],a[8:11],v[60:63]
ds_read_u16 v25,v25
v_lshlrev_b32_sdwa v27,1,v27 dst_sel:DWORD dst_unused:UNUSED_PAD src0_sel:DWORD src1_sel:WORD_1
v_mfma_f32_16x16x32_bf16 v[48:51],a[200:203],a[12:15],v[16:19]
s_waitcnt lgkmcnt(8)
ds_read_u16_d16_hi v27,v27
v_or_b32 v42,v44,v46
v_pk_mul_f16 v9,v8,v9
v_mfma_f32_16x16x32_bf16 v[48:51],a[204:207],a[28:31],v[48:51]
ds_read_b64_tr_b16 a[194:195],v1 offset:19504
v_cvt_pk_bf16_f32 v43,v45,v47
v_lshlrev_b32_sdwa v28,1,v28 dst_sel:DWORD dst_unused:UNUSED_PAD src0_sel:DWORD src1_sel:WORD_1
v_mfma_f32_16x16x32_bf16 v[48:51],a[208:211],a[44:47],v[48:51]
v_exp_f16 v8,v9
v_mfma_f32_16x16x32_bf16 v[44:47],a[212:215],a[44:47],v[20:23]
ds_read_u16 v28,v28
v_lshlrev_b32_sdwa v30,1,v30 dst_sel:DWORD dst_unused:UNUSED_PAD src0_sel:DWORD src1_sel:WORD_1
v_mfma_f32_16x16x32_bf16 v[44:47],a[216:219],a[28:31],v[44:47]
ds_read_u16_d16_hi v30,v30
v_lshlrev_b32_sdwa v29,1,v29 dst_sel:DWORD dst_unused:UNUSED_PAD src0_sel:DWORD src1_sel:WORD_1
v_mfma_f32_16x16x32_bf16 v[44:47],a[220:223],a[12:15],v[44:47]
ds_read_b64_tr_b16 a[0:1],v3 offset:38992
ds_read_b64_tr_b16 a[2:3],v3 offset:40528
v_exp_f16_sdwa v8,v9 dst_sel:WORD_1 dst_unused:UNUSED_PRESERVE src0_sel:WORD_1
v_mfma_f32_16x16x32_bf16 v[208:211],a[252:255],v[36:39],v[208:211]
ds_read_b64_tr_b16 a[4:5],v3 offset:45152
ds_read_b64_tr_b16 a[6:7],v3 offset:46688
v_lshlrev_b32_sdwa v31,1,v31 dst_sel:DWORD dst_unused:UNUSED_PAD src0_sel:DWORD src1_sel:WORD_1
v_mfma_f32_16x16x32_bf16 v[212:215],a[252:255],v[40:43],v[212:215]
ds_read_u16 v29,v29
ds_read_u16_d16_hi v31,v31
v_lshlrev_b32_sdwa v52,1,v52 dst_sel:DWORD dst_unused:UNUSED_PAD src0_sel:DWORD src1_sel:WORD_1
v_mfma_f32_16x16x32_bf16 v[172:175],a[224:227],v[40:43],v[172:175]
ds_read_u16 v52,v52
ds_read_b64_tr_b16 a[8:9],v3 offset:45160
v_pk_fma_f16 v8,v8,s55,s55
v_mfma_f32_16x16x32_bf16 v[168:171],a[224:227],v[36:39],v[168:171]
ds_read_b64_tr_b16 a[10:11],v3 offset:46696
v_rcp_f16 v9,v8
v_lshlrev_b32_sdwa v54,1,v54 dst_sel:DWORD dst_unused:UNUSED_PAD src0_sel:DWORD src1_sel:WORD_1
v_mfma_f32_16x16x32_bf16 v[216:219],a[232:235],v[36:39],v[216:219]
ds_read_u16_d16_hi v54,v54
ds_read_b64_tr_b16 a[12:13],v3 offset:39000
v_mfma_f32_16x16x32_bf16 v[220:223],a[232:235],v[40:43],v[220:223]
ds_read_b64_tr_b16 a[14:15],v3 offset:40536
ds_read_b64_tr_b16 a[252:253],v3 offset:18544
v_lshlrev_b32_sdwa v53,1,v53 dst_sel:DWORD dst_unused:UNUSED_PAD src0_sel:DWORD src1_sel:WORD_1
v_mfma_f32_16x16x32_bf16 v[188:191],a[244:247],v[40:43],v[188:191]
ds_read_u16 v53,v53
ds_read_b64_tr_b16 a[254:255],v3 offset:20080
v_lshlrev_b32_sdwa v55,1,v55 dst_sel:DWORD dst_unused:UNUSED_PAD src0_sel:DWORD src1_sel:WORD_1
v_mfma_f32_16x16x32_bf16 v[184:187],a[244:247],v[36:39],v[184:187]
ds_read_u16_d16_hi v55,v55
v_rcp_f16_sdwa v9,v8 dst_sel:WORD_1 dst_unused:UNUSED_PRESERVE src0_sel:WORD_1
v_mfma_f32_16x16x32_bf16 v[224:227],a[240:243],v[36:39],v[224:227]
ds_read_b64_tr_b16 a[224:225],v3 offset:18552
ds_read_b64_tr_b16 a[226:227],v3 offset:20088
v_lshlrev_b32_sdwa v56,1,v56 dst_sel:DWORD dst_unused:UNUSED_PAD src0_sel:DWORD src1_sel:WORD_1
v_mfma_f32_16x16x32_bf16 v[228:231],a[240:243],v[40:43],v[228:231]
ds_read_u16 v56,v56
ds_read_b64_tr_b16 a[232:233],v3 offset:18608
v_lshlrev_b32_sdwa v58,1,v58 dst_sel:DWORD dst_unused:UNUSED_PAD src0_sel:DWORD src1_sel:WORD_1
v_mfma_f32_16x16x32_bf16 v[204:207],a[228:231],v[40:43],v[204:207]
ds_read_u16_d16_hi v58,v58
ds_read_b64_tr_b16 a[234:235],v3 offset:20144
v_fma_mix_f32 v57,v57,v9,0 op_sel_hi:[0,1,0]
v_mfma_f32_16x16x32_bf16 v[200:203],a[228:231],v[36:39],v[200:203]
ds_read_b64_tr_b16 a[244:245],v3 offset:18616
v_fma_mix_f32 v59,v59,v9,0 op_sel:[0,1,0] op_sel_hi:[0,1,0]
v_mfma_f32_16x16x32_bf16 v[40:43],a[220:223],a[48:51],v[20:23]
ds_read_b64_tr_b16 a[246:247],v3 offset:20152
v_lshlrev_b32_sdwa v32,1,v32 dst_sel:DWORD dst_unused:UNUSED_PAD src0_sel:DWORD src1_sel:WORD_1
v_mfma_f32_16x16x32_bf16 v[40:43],a[216:219],a[64:67],v[40:43]
ds_read_b64_tr_b16 a[16:17],v3 offset:39056
ds_read_b64_tr_b16 a[18:19],v3 offset:40592
v_fma_mixlo_f16 v8,v49,s99,0
v_mfma_f32_16x16x32_bf16 v[40:43],a[212:215],a[80:83],v[40:43]
s_waitcnt lgkmcnt(13)
ds_read_u16 v32,v32
v_lshl_add_u64 v[24:25],v[24:25],0,v[26:27]
v_lshlrev_b32_sdwa v34,1,v34 dst_sel:DWORD dst_unused:UNUSED_PAD src0_sel:DWORD src1_sel:WORD_1
v_mfma_f32_16x16x32_bf16 v[36:39],a[208:211],a[80:83],v[16:19]
ds_read_u16_d16_hi v34,v34
v_lshl_add_u64 v[26:27],v[28:29],0,v[30:31]
v_fma_mixhi_f16 v8,v51,s99,0
v_mfma_f32_16x16x32_bf16 v[36:39],a[204:207],a[64:67],v[36:39]
s_waitcnt lgkmcnt(12)
ds_read_b64_tr_b16 a[20:21],v3 offset:45216
v_lshl_add_u64 v[28:29],v[52:53],0,v[54:55]
v_lshlrev_b32_sdwa v33,1,v33 dst_sel:DWORD dst_unused:UNUSED_PAD src0_sel:DWORD src1_sel:WORD_1
v_pk_fma_f16 v9,v8,v8,s54
v_mfma_f32_16x16x32_bf16 v[36:39],a[200:203],a[48:51],v[36:39]
ds_read_u16 v33,v33
v_lshlrev_b32_sdwa v35,1,v35 dst_sel:DWORD dst_unused:UNUSED_PAD src0_sel:DWORD src1_sel:WORD_1
v_mfma_f32_16x16x32_bf16 v[52:55],a[200:203],a[52:55],v[16:19]
s_waitcnt lgkmcnt(9)
ds_read_u16_d16_hi v35,v35
v_or_b32 v30,v56,v58
v_pk_mul_f16 v9,v8,v9
v_mfma_f32_16x16x32_bf16 v[52:55],a[204:207],a[68:71],v[52:55]
ds_read_b64_tr_b16 a[22:23],v3 offset:46752
v_cvt_pk_bf16_f32 v31,v57,v59
v_lshlrev_b32_sdwa v60,1,v60 dst_sel:DWORD dst_unused:UNUSED_PAD src0_sel:DWORD src1_sel:WORD_1
v_mfma_f32_16x16x32_bf16 v[52:55],a[208:211],a[84:87],v[52:55]
v_exp_f16 v8,v9
v_mfma_f32_16x16x32_bf16 v[56:59],a[212:215],a[84:87],v[20:23]
ds_read_u16 v60,v60
v_lshlrev_b32_sdwa v62,1,v62 dst_sel:DWORD dst_unused:UNUSED_PAD src0_sel:DWORD src1_sel:WORD_1
v_mfma_f32_16x16x32_bf16 v[56:59],a[216:219],a[68:71],v[56:59]
ds_read_u16_d16_hi v62,v62
ds_read_b64_tr_b16 a[24:25],v3 offset:45224
v_lshlrev_b32_sdwa v61,1,v61 dst_sel:DWORD dst_unused:UNUSED_PAD src0_sel:DWORD src1_sel:WORD_1
v_mfma_f32_16x16x32_bf16 v[56:59],a[220:223],a[52:55],v[56:59]
ds_read_u16 v61,v61
v_exp_f16_sdwa v8,v9 dst_sel:WORD_1 dst_unused:UNUSED_PRESERVE src0_sel:WORD_1
v_mfma_f32_16x16x32_bf16 v[136:139],a[248:251],v[24:27],v[136:139]
ds_read_b64_tr_b16 a[26:27],v3 offset:46760
v_lshlrev_b32_sdwa v63,1,v63 dst_sel:DWORD dst_unused:UNUSED_PAD src0_sel:DWORD src1_sel:WORD_1
v_mfma_f32_16x16x32_bf16 v[140:143],a[248:251],v[28:31],v[140:143]
ds_read_u16_d16_hi v63,v63
v_lshlrev_b32_sdwa v44,1,v44 dst_sel:DWORD dst_unused:UNUSED_PAD src0_sel:DWORD src1_sel:WORD_1
v_mfma_f32_16x16x32_bf16 v[68:71],a[236:239],v[28:31],v[68:71]
ds_read_u16 v44,v44
v_pk_fma_f16 v8,v8,s55,s55
v_mfma_f32_16x16x32_bf16 v[64:67],a[236:239],v[24:27],v[64:67]
v_rcp_f16 v9,v8
v_lshlrev_b32_sdwa v46,1,v46 dst_sel:DWORD dst_unused:UNUSED_PAD src0_sel:DWORD src1_sel:WORD_1
v_mfma_f32_16x16x32_bf16 v[144:147],a[252:255],v[24:27],v[144:147]
ds_read_u16_d16_hi v46,v46
v_lshlrev_b32_sdwa v45,1,v45 dst_sel:DWORD dst_unused:UNUSED_PAD src0_sel:DWORD src1_sel:WORD_1
v_mfma_f32_16x16x32_bf16 v[148:151],a[252:255],v[28:31],v[148:151]
ds_read_u16 v45,v45
ds_read_b64_tr_b16 a[28:29],v3 offset:39064
v_mfma_f32_16x16x32_bf16 v[84:87],a[224:227],v[28:31],v[84:87]
ds_read_b64_tr_b16 a[30:31],v3 offset:40600
v_lshlrev_b32_sdwa v47,1,v47 dst_sel:DWORD dst_unused:UNUSED_PAD src0_sel:DWORD src1_sel:WORD_1
v_mfma_f32_16x16x32_bf16 v[80:83],a[224:227],v[24:27],v[80:83]
s_waitcnt lgkmcnt(8)
v_rcp_f16_sdwa v9,v8 dst_sel:WORD_1 dst_unused:UNUSED_PRESERVE src0_sel:WORD_1
v_mfma_f32_16x16x32_bf16 v[152:155],a[232:235],v[24:27],v[152:155]
TP10325_%=:
s_cbranch_execz TR1_%=
ds_read_u16_d16_hi v47,v47
v_lshlrev_b32_sdwa v48,1,v48 dst_sel:DWORD dst_unused:UNUSED_PAD src0_sel:DWORD src1_sel:WORD_1
v_mfma_f32_16x16x32_bf16 v[156:159],a[232:235],v[28:31],v[156:159]
ds_read_u16 v48,v48
v_lshlrev_b32_sdwa v50,1,v50 dst_sel:DWORD dst_unused:UNUSED_PAD src0_sel:DWORD src1_sel:WORD_1
v_mfma_f32_16x16x32_bf16 v[100:103],a[244:247],v[28:31],v[100:103]
ds_read_u16_d16_hi v50,v50
v_fma_mix_f32 v49,v49,v9,0 op_sel_hi:[0,1,0]
v_mfma_f32_16x16x32_bf16 v[96:99],a[244:247],v[24:27],v[96:99]
ds_read_b64_tr_b16 a[196:197],v1 offset:20528
v_fma_mix_f32 v51,v51,v9,0 op_sel:[0,1,0] op_sel_hi:[0,1,0]
v_mfma_f32_16x16x32_bf16 v[28:31],a[220:223],a[56:59],v[20:23]
ds_read_b64_tr_b16 a[198:199],v1 offset:21552
v_mul_f32_dpp v136,v14,v136 row_newbcast:0 row_mask:0xf bank_mask:0xf
s_add_u32 s8,s61,3145728
v_lshlrev_b32_sdwa v40,1,v40 dst_sel:DWORD dst_unused:UNUSED_PAD src0_sel:DWORD src1_sel:WORD_1
v_mfma_f32_16x16x32_bf16 v[28:31],a[216:219],a[72:75],v[28:31]
ds_read_u16 v40,v40
v_mul_f32_dpp v137,v14,v137 row_newbcast:1 row_mask:0xf bank_mask:0xf
v_fma_mixlo_f16 v8,v53,s99,0
v_mul_f32_dpp v138,v14,v138 row_newbcast:2 row_mask:0xf bank_mask:0xf
v_mfma_f32_16x16x32_bf16 v[28:31],a[212:215],a[88:91],v[28:31]
ds_read_b64_tr_b16 a[32:33],v3 offset:39120
v_lshl_add_u64 v[32:33],v[32:33],0,v[34:35]
v_mul_f32_dpp v139,v14,v139 row_newbcast:3 row_mask:0xf bank_mask:0xf
v_lshlrev_b32_sdwa v42,1,v42 dst_sel:DWORD dst_unused:UNUSED_PAD src0_sel:DWORD src1_sel:WORD_1
v_mfma_f32_16x16x32_bf16 v[24:27],a[208:211],a[88:91],v[16:19]
s_waitcnt lgkmcnt(7)
ds_read_u16_d16_hi v42,v42
v_lshl_add_u64 v[34:35],v[60:61],0,v[62:63]
v_mul_f32_dpp v64,v14,v64 row_newbcast:4 row_mask:0xf bank_mask:0xf
v_fma_mixhi_f16 v8,v55,s99,0
v_mul_f32_dpp v65,v14,v65 row_newbcast:5 row_mask:0xf bank_mask:0xf
v_mfma_f32_16x16x32_bf16 v[24:27],a[204:207],a[72:75],v[24:27]
s_waitcnt lgkmcnt(7)
ds_read_b64_tr_b16 a[34:35],v3 offset:40656
v_lshl_add_u64 v[60:61],v[44:45],0,v[46:47]
v_mul_f32_dpp v66,v14,v66 row_newbcast:6 row_mask:0xf bank_mask:0xf
v_lshlrev_b32_sdwa v41,1,v41 dst_sel:DWORD dst_unused:UNUSED_PAD src0_sel:DWORD src1_sel:WORD_1
v_pk_fma_f16 v9,v8,v8,s54
v_mfma_f32_16x16x32_bf16 v[24:27],a[200:203],a[56:59],v[24:27]
ds_read_u16 v41,v41
v_mul_f32_dpp v67,v14,v67 row_newbcast:7 row_mask:0xf bank_mask:0xf
v_lshlrev_b32_sdwa v43,1,v43 dst_sel:DWORD dst_unused:UNUSED_PAD src0_sel:DWORD src1_sel:WORD_1
v_cvt_pk_bf16_f32 v136,v136,v137
v_mfma_f32_16x16x32_bf16 v[44:47],a[200:203],a[60:63],v[16:19]
s_waitcnt lgkmcnt(7)
ds_read_b128 v[16:19],v4 offset:0
v_or_b32 v62,v48,v50
v_cvt_pk_bf16_f32 v137,v138,v139
v_pk_mul_f16 v9,v8,v9
v_mfma_f32_16x16x32_bf16 v[44:47],a[204:207],a[76:79],v[44:47]
ds_read_u16_d16_hi v43,v43
ds_read_b64_tr_b16 a[200:201],v1 offset:22576
v_cvt_pk_bf16_f32 v63,v49,v51
v_cvt_pk_bf16_f32 v138,v64,v65
v_lshlrev_b32_sdwa v36,1,v36 dst_sel:DWORD dst_unused:UNUSED_PAD src0_sel:DWORD src1_sel:WORD_1
v_cvt_pk_bf16_f32 v139,v66,v67
v_mfma_f32_16x16x32_bf16 v[44:47],a[208:211],a[92:95],v[44:47]
ds_read_b64_tr_b16 a[202:203],v1 offset:23600
v_exp_f16 v8,v9
v_mul_f32_dpp v144,v14,v144 row_newbcast:8 row_mask:0xf bank_mask:0xf
v_mfma_f32_16x16x32_bf16 v[48:51],a[212:215],a[92:95],v[20:23]
buffer_store_dwordx4 v[136:139],v7,s[40:43],s8 offen offset:0 sc1
ds_read_b128 v[20:23],v4 offset:32
v_mul_f32_dpp v145,v14,v145 row_newbcast:9 row_mask:0xf bank_mask:0xf
v_lshlrev_b32_sdwa v38,1,v38 dst_sel:DWORD dst_unused:UNUSED_PAD src0_sel:DWORD src1_sel:WORD_1
v_mul_f32_dpp v146,v14,v146 row_newbcast:10 row_mask:0xf bank_mask:0xf
v_mfma_f32_16x16x32_bf16 v[48:51],a[216:219],a[76:79],v[48:51]
ds_read_b64_tr_b16 a[204:205],v2 offset:22576
ds_read_b64_tr_b16 a[206:207],v2 offset:23600
v_mul_f32_dpp v147,v14,v147 row_newbcast:11 row_mask:0xf bank_mask:0xf
v_lshlrev_b32_sdwa v37,1,v37 dst_sel:DWORD dst_unused:UNUSED_PAD src0_sel:DWORD src1_sel:WORD_1
v_mfma_f32_16x16x32_bf16 v[48:51],a[220:223],a[60:63],v[48:51]
ds_read_u16 v36,v36
v_exp_f16_sdwa v8,v9 dst_sel:WORD_1 dst_unused:UNUSED_PRESERVE src0_sel:WORD_1
v_mul_f32_dpp v80,v14,v80 row_newbcast:12 row_mask:0xf bank_mask:0xf
v_mul_f32_dpp v81,v14,v81 row_newbcast:13 row_mask:0xf bank_mask:0xf
v_mfma_f32_16x16x32_bf16 v[112:115],a[248:251],v[32:35],v[112:115]
ds_read_u16_d16_hi v38,v38
ds_read_u16 v37,v37
v_mul_f32_dpp v82,v14,v82 row_newbcast:14 row_mask:0xf bank_mask:0xf
v_lshlrev_b32_sdwa v39,1,v39 dst_sel:DWORD dst_unused:UNUSED_PAD src0_sel:DWORD src1_sel:WORD_1
v_mfma_f32_16x16x32_bf16 v[116:119],a[248:251],v[60:63],v[116:119]
ds_read_u16_d16_hi v39,v39
ds_read_b64_tr_b16 a[208:209],v2 offset:20528
v_mul_f32_dpp v83,v14,v83 row_newbcast:15 row_mask:0xf bank_mask:0xf
v_lshlrev_b32_sdwa v56,1,v56 dst_sel:DWORD dst_unused:UNUSED_PAD src0_sel:DWORD src1_sel:WORD_1
v_cvt_pk_bf16_f32 v144,v144,v145
v_mfma_f32_16x16x32_bf16 v[76:79],a[236:239],v[60:63],v[76:79]
ds_read_b64_tr_b16 a[210:211],v2 offset:21552
ds_read_u16 v56,v56
v_cvt_pk_bf16_f32 v145,v146,v147
v_pk_fma_f16 v8,v8,s55,s55
v_mfma_f32_16x16x32_bf16 v[72:75],a[236:239],v[32:35],v[72:75]
ds_read_b64_tr_b16 a[212:213],v2 offset:18480
v_rcp_f16 v9,v8
v_cvt_pk_bf16_f32 v146,v80,v81
v_lshlrev_b32_sdwa v58,1,v58 dst_sel:DWORD dst_unused:UNUSED_PAD src0_sel:DWORD src1_sel:WORD_1
v_cvt_pk_bf16_f32 v147,v82,v83
v_mfma_f32_16x16x32_bf16 v[120:123],a[252:255],v[32:35],v[120:123]
ds_read_u16_d16_hi v58,v58
v_mul_f32_dpp v152,v15,v152 row_newbcast:0 row_mask:0xf bank_mask:0xf
v_mfma_f32_16x16x32_bf16 v[124:127],a[252:255],v[60:63],v[124:127]
ds_read_b64_tr_b16 a[214:215],v2 offset:19504
ds_read_b64_tr_b16 a[36:37],v3 offset:45280
v_mul_f32_dpp v153,v15,v153 row_newbcast:1 row_mask:0xf bank_mask:0xf
v_lshlrev_b32_sdwa v57,1,v57 dst_sel:DWORD dst_unused:UNUSED_PAD src0_sel:DWORD src1_sel:WORD_1
v_mul_f32_dpp v154,v15,v154 row_newbcast:2 row_mask:0xf bank_mask:0xf
v_mfma_f32_16x16x32_bf16 v[92:95],a[224:227],v[60:63],v[92:95]
ds_read_u16 v57,v57
v_mul_f32_dpp v155,v15,v155 row_newbcast:3 row_mask:0xf bank_mask:0xf
v_lshlrev_b32_sdwa v59,1,v59 dst_sel:DWORD dst_unused:UNUSED_PAD src0_sel:DWORD src1_sel:WORD_1
v_mfma_f32_16x16x32_bf16 v[88:91],a[224:227],v[32:35],v[88:91]
ds_read_u16_d16_hi v59,v59
v_rcp_f16_sdwa v9,v8 dst_sel:WORD_1 dst_unused:UNUSED_PRESERVE src0_sel:WORD_1
v_mul_f32_dpp v96,v15,v96 row_newbcast:4 row_mask:0xf bank_mask:0xf
v_mul_f32_dpp v97,v15,v97 row_newbcast:5 row_mask:0xf bank_mask:0xf
v_mfma_f32_16x16x32_bf16 v[128:131],a[232:235],v[32:35],v[128:131]
buffer_store_dwordx4 v[144:147],v7,s[40:43],s8 offen offset:64 sc1
v_mul_f32_dpp v98,v15,v98 row_newbcast:6 row_mask:0xf bank_mask:0xf
v_lshlrev_b32_sdwa v52,1,v52 dst_sel:DWORD dst_unused:UNUSED_PAD src0_sel:DWORD src1_sel:WORD_1
v_mfma_f32_16x16x32_bf16 v[132:135],a[232:235],v[60:63],v[132:135]
ds_read_u16 v52,v52
v_mul_f32_dpp v99,v15,v99 row_newbcast:7 row_mask:0xf bank_mask:0xf
v_cvt_pk_bf16_f32 v152,v152,v153
v_lshlrev_b32_sdwa v54,1,v54 dst_sel:DWORD dst_unused:UNUSED_PAD src0_sel:DWORD src1_sel:WORD_1
v_mfma_f32_16x16x32_bf16 v[108:111],a[244:247],v[60:63],v[108:111]
ds_read_u16_d16_hi v54,v54
v_cvt_pk_bf16_f32 v153,v154,v155
v_fma_mix_f32 v53,v53,v9,0 op_sel_hi:[0,1,0]
v_mfma_f32_16x16x32_bf16 v[104:107],a[244:247],v[32:35],v[104:107]
s_waitcnt lgkmcnt(8)
ds_read_b64_tr_b16 a[38:39],v3 offset:46816
v_cvt_pk_bf16_f32 v154,v96,v97
v_cvt_pk_bf16_f32 v155,v98,v99
v_fma_mix_f32 v55,v55,v9,0 op_sel:[0,1,0] op_sel_hi:[0,1,0]
v_mfma_f32_16x16x32_bf16 v[60:63],a[192:195],a[48:51],v[16:19]
ds_read_b64_tr_b16 a[40:41],v3 offset:45288
v_mul_f32_dpp v140,v14,v140 row_newbcast:0 row_mask:0xf bank_mask:0xf
v_lshlrev_b32_sdwa v28,1,v28 dst_sel:DWORD dst_unused:UNUSED_PAD src0_sel:DWORD src1_sel:WORD_1
v_mfma_f32_16x16x32_bf16 v[60:63],a[196:199],a[64:67],v[60:63]
ds_read_b64_tr_b16 a[42:43],v3 offset:46824
v_mul_f32_dpp v141,v14,v141 row_newbcast:1 row_mask:0xf bank_mask:0xf
v_fma_mixlo_f16 v8,v45,s99,0
v_mul_f32_dpp v142,v14,v142 row_newbcast:2 row_mask:0xf bank_mask:0xf
v_mfma_f32_16x16x32_bf16 v[60:63],a[200:203],a[80:83],v[60:63]
ds_read_u16 v28,v28
v_lshl_add_u64 v[40:41],v[40:41],0,v[42:43]
v_mul_f32_dpp v143,v14,v143 row_newbcast:3 row_mask:0xf bank_mask:0xf
v_lshlrev_b32_sdwa v30,1,v30 dst_sel:DWORD dst_unused:UNUSED_PAD src0_sel:DWORD src1_sel:WORD_1
v_mfma_f32_16x16x32_bf16 v[32:35],a[204:207],a[80:83],v[20:23]
ds_read_u16_d16_hi v30,v30
v_lshl_add_u64 v[42:43],v[36:37],0,v[38:39]
v_mul_f32_dpp v68,v14,v68 row_newbcast:4 row_mask:0xf bank_mask:0xf
v_fma_mixhi_f16 v8,v47,s99,0
v_mul_f32_dpp v69,v14,v69 row_newbcast:5 row_mask:0xf bank_mask:0xf
v_mfma_f32_16x16x32_bf16 v[32:35],a[208:211],a[64:67],v[32:35]
s_waitcnt lgkmcnt(7)
ds_read_b64_tr_b16 a[44:45],v3 offset:39128
v_lshl_add_u64 v[36:37],v[56:57],0,v[58:59]
v_mul_f32_dpp v70,v14,v70 row_newbcast:6 row_mask:0xf bank_mask:0xf
v_lshlrev_b32_sdwa v29,1,v29 dst_sel:DWORD dst_unused:UNUSED_PAD src0_sel:DWORD src1_sel:WORD_1
v_pk_fma_f16 v9,v8,v8,s54
v_mfma_f32_16x16x32_bf16 v[32:35],a[212:215],a[48:51],v[32:35]
ds_read_u16 v29,v29
v_mul_f32_dpp v71,v14,v71 row_newbcast:7 row_mask:0xf bank_mask:0xf
v_lshlrev_b32_sdwa v31,1,v31 dst_sel:DWORD dst_unused:UNUSED_PAD src0_sel:DWORD src1_sel:WORD_1
v_cvt_pk_bf16_f32 v140,v140,v141
v_mfma_f32_16x16x32_bf16 v[56:59],a[212:215],a[52:55],v[20:23]
s_waitcnt lgkmcnt(7)
buffer_store_dwordx4 v[152:155],v7,s[40:43],s8 offen offset:128 sc1
v_or_b32 v38,v52,v54
v_cvt_pk_bf16_f32 v141,v142,v143
v_pk_mul_f16 v9,v8,v9
v_mfma_f32_16x16x32_bf16 v[56:59],a[208:211],a[68:71],v[56:59]
ds_read_u16_d16_hi v31,v31
v_cvt_pk_bf16_f32 v39,v53,v55
v_cvt_pk_bf16_f32 v142,v68,v69
v_lshlrev_b32_sdwa v24,1,v24 dst_sel:DWORD dst_unused:UNUSED_PAD src0_sel:DWORD src1_sel:WORD_1
v_cvt_pk_bf16_f32 v143,v70,v71
v_mfma_f32_16x16x32_bf16 v[56:59],a[204:207],a[84:87],v[56:59]
s_barrier
v_exp_f16 v8,v9
v_mul_f32_dpp v148,v14,v148 row_newbcast:8 row_mask:0xf bank_mask:0xf
v_mfma_f32_16x16x32_bf16 v[52:55],a[200:203],a[84:87],v[16:19]
ds_read_u16 v24,v24
v_mul_f32_dpp v149,v14,v149 row_newbcast:9 row_mask:0xf bank_mask:0xf
v_lshlrev_b32_sdwa v26,1,v26 dst_sel:DWORD dst_unused:UNUSED_PAD src0_sel:DWORD src1_sel:WORD_1
v_mul_f32_dpp v150,v14,v150 row_newbcast:10 row_mask:0xf bank_mask:0xf
v_mfma_f32_16x16x32_bf16 v[52:55],a[196:199],a[68:71],v[52:55]
ds_read_u16_d16_hi v26,v26
v_mul_f32_dpp v151,v14,v151 row_newbcast:11 row_mask:0xf bank_mask:0xf
v_lshlrev_b32_sdwa v25,1,v25 dst_sel:DWORD dst_unused:UNUSED_PAD src0_sel:DWORD src1_sel:WORD_1
v_mfma_f32_16x16x32_bf16 v[52:55],a[192:195],a[52:55],v[52:55]
v_exp_f16_sdwa v8,v9 dst_sel:WORD_1 dst_unused:UNUSED_PRESERVE src0_sel:WORD_1
v_mul_f32_dpp v84,v14,v84 row_newbcast:12 row_mask:0xf bank_mask:0xf
v_mul_f32_dpp v85,v14,v85 row_newbcast:13 row_mask:0xf bank_mask:0xf
v_mfma_f32_16x16x32_bf16 v[232:235],a[248:251],v[40:43],v[232:235]
ds_read_u16 v25,v25
v_mul_f32_dpp v86,v14,v86 row_newbcast:14 row_mask:0xf bank_mask:0xf
v_lshlrev_b32_sdwa v27,1,v27 dst_sel:DWORD dst_unused:UNUSED_PAD src0_sel:DWORD src1_sel:WORD_1
v_mfma_f32_16x16x32_bf16 v[236:239],a[248:251],v[36:39],v[236:239]
ds_read_u16_d16_hi v27,v27
v_mul_f32_dpp v87,v14,v87 row_newbcast:15 row_mask:0xf bank_mask:0xf
v_lshlrev_b32_sdwa v48,1,v48 dst_sel:DWORD dst_unused:UNUSED_PAD src0_sel:DWORD src1_sel:WORD_1
v_cvt_pk_bf16_f32 v148,v148,v149
v_mfma_f32_16x16x32_bf16 v[164:167],a[236:239],v[36:39],v[164:167]
ds_read_u16 v48,v48
v_cvt_pk_bf16_f32 v149,v150,v151
v_pk_fma_f16 v8,v8,s55,s55
v_mfma_f32_16x16x32_bf16 v[160:163],a[236:239],v[40:43],v[160:163]
v_rcp_f16 v9,v8
v_cvt_pk_bf16_f32 v150,v84,v85
v_lshlrev_b32_sdwa v50,1,v50 dst_sel:DWORD dst_unused:UNUSED_PAD src0_sel:DWORD src1_sel:WORD_1
v_cvt_pk_bf16_f32 v151,v86,v87
v_mfma_f32_16x16x32_bf16 v[240:243],a[252:255],v[40:43],v[240:243]
ds_read_u16_d16_hi v50,v50
v_mul_f32_dpp v156,v15,v156 row_newbcast:0 row_mask:0xf bank_mask:0xf
v_mfma_f32_16x16x32_bf16 v[244:247],a[252:255],v[36:39],v[244:247]
ds_read_b64_tr_b16 a[46:47],v3 offset:40664
v_mul_f32_dpp v157,v15,v157 row_newbcast:1 row_mask:0xf bank_mask:0xf
v_lshlrev_b32_sdwa v49,1,v49 dst_sel:DWORD dst_unused:UNUSED_PAD src0_sel:DWORD src1_sel:WORD_1
v_mul_f32_dpp v158,v15,v158 row_newbcast:2 row_mask:0xf bank_mask:0xf
v_mfma_f32_16x16x32_bf16 v[180:183],a[224:227],v[36:39],v[180:183]
ds_read_u16 v49,v49
v_mul_f32_dpp v159,v15,v159 row_newbcast:3 row_mask:0xf bank_mask:0xf
v_lshlrev_b32_sdwa v51,1,v51 dst_sel:DWORD dst_unused:UNUSED_PAD src0_sel:DWORD src1_sel:WORD_1
v_mfma_f32_16x16x32_bf16 v[176:179],a[224:227],v[40:43],v[176:179]
v_rcp_f16_sdwa v9,v8 dst_sel:WORD_1 dst_unused:UNUSED_PRESERVE src0_sel:WORD_1
v_mul_f32_dpp v100,v15,v100 row_newbcast:4 row_mask:0xf bank_mask:0xf
v_mul_f32_dpp v101,v15,v101 row_newbcast:5 row_mask:0xf bank_mask:0xf
v_mfma_f32_16x16x32_bf16 v[248:251],a[232:235],v[40:43],v[248:251]
buffer_store_dwordx4 v[140:143],v7,s[40:43],s8 offen offset:192 sc1
ds_read_u16_d16_hi v51,v51
v_mul_f32_dpp v102,v15,v102 row_newbcast:6 row_mask:0xf bank_mask:0xf
v_lshlrev_b32_sdwa v44,1,v44 dst_sel:DWORD dst_unused:UNUSED_PAD src0_sel:DWORD src1_sel:WORD_1
v_mfma_f32_16x16x32_bf16 v[252:255],a[232:235],v[36:39],v[252:255]
ds_read_u16 v44,v44
v_mul_f32_dpp v103,v15,v103 row_newbcast:7 row_mask:0xf bank_mask:0xf
v_cvt_pk_bf16_f32 v156,v156,v157
v_lshlrev_b32_sdwa v46,1,v46 dst_sel:DWORD dst_unused:UNUSED_PAD src0_sel:DWORD src1_sel:WORD_1
v_mfma_f32_16x16x32_bf16 v[196:199],a[244:247],v[36:39],v[196:199]
ds_read_u16_d16_hi v46,v46
v_cvt_pk_bf16_f32 v157,v158,v159
v_fma_mix_f32 v45,v45,v9,0 op_sel_hi:[0,1,0]
v_mfma_f32_16x16x32_bf16 v[192:195],a[244:247],v[40:43],v[192:195]
ds_read_b64_tr_b16 a[240:241],v3 offset:32832
v_cvt_pk_bf16_f32 v158,v100,v101
v_cvt_pk_bf16_f32 v159,v102,v103
v_fma_mix_f32 v47,v47,v9,0 op_sel:[0,1,0] op_sel_hi:[0,1,0]
v_mfma_f32_16x16x32_bf16 v[36:39],a[192:195],a[56:59],v[16:19]
ds_read_b128 v[64:67],v4 offset:128
ds_read_b128 v[68:71],v4 offset:160
v_mul_f32_dpp v112,v14,v112 row_newbcast:0 row_mask:0xf bank_mask:0xf
s_add_u32 s9,s61,3244032
v_lshlrev_b32_sdwa v60,1,v60 dst_sel:DWORD dst_unused:UNUSED_PAD src0_sel:DWORD src1_sel:WORD_1
v_mfma_f32_16x16x32_bf16 v[36:39],a[196:199],a[72:75],v[36:39]
ds_read_u16 v60,v60
v_mul_f32_dpp v113,v14,v113 row_newbcast:1 row_mask:0xf bank_mask:0xf
v_fma_mixlo_f16 v8,v57,s99,0
v_mul_f32_dpp v114,v14,v114 row_newbcast:2 row_mask:0xf bank_mask:0xf
v_mfma_f32_16x16x32_bf16 v[36:39],a[200:203],a[88:91],v[36:39]
s_waitcnt lgkmcnt(8)
ds_read_b128 v[80:83],v4 offset:256
v_lshl_add_u64 v[28:29],v[28:29],0,v[30:31]
v_mul_f32_dpp v115,v14,v115 row_newbcast:3 row_mask:0xf bank_mask:0xf
v_lshlrev_b32_sdwa v62,1,v62 dst_sel:DWORD dst_unused:UNUSED_PAD src0_sel:DWORD src1_sel:WORD_1
v_mfma_f32_16x16x32_bf16 v[40:43],a[204:207],a[88:91],v[20:23]
ds_read_u16_d16_hi v62,v62
v_lshl_add_u64 v[30:31],v[24:25],0,v[26:27]
v_mul_f32_dpp v72,v14,v72 row_newbcast:4 row_mask:0xf bank_mask:0xf
v_fma_mixhi_f16 v8,v59,s99,0
v_mul_f32_dpp v73,v14,v73 row_newbcast:5 row_mask:0xf bank_mask:0xf
v_mfma_f32_16x16x32_bf16 v[40:43],a[208:211],a[72:75],v[40:43]
s_waitcnt lgkmcnt(8)
ds_read_b128 v[84:87],v4 offset:288
v_lshl_add_u64 v[24:25],v[48:49],0,v[50:51]
v_mul_f32_dpp v74,v14,v74 row_newbcast:6 row_mask:0xf bank_mask:0xf
v_lshlrev_b32_sdwa v61,1,v61 dst_sel:DWORD dst_unused:UNUSED_PAD src0_sel:DWORD src1_sel:WORD_1
v_pk_fma_f16 v9,v8,v8,s54
v_mfma_f32_16x16x32_bf16 v[40:43],a[212:215],a[56:59],v[40:43]
ds_read_u16 v61,v61
v_mul_f32_dpp v75,v14,v75 row_newbcast:7 row_mask:0xf bank_mask:0xf
v_lshlrev_b32_sdwa v63,1,v63 dst_sel:DWORD dst_unused:UNUSED_PAD src0_sel:DWORD src1_sel:WORD_1
v_cvt_pk_bf16_f32 v112,v112,v113
v_mfma_f32_16x16x32_bf16 v[48:51],a[212:215],a[60:63],v[20:23]
s_waitcnt lgkmcnt(8)
buffer_store_dwordx4 v[148:151],v7,s[40:43],s8 offen offset:256 sc1
ds_read_b64_tr_b16 a[212:213],v3 offset:51312
v_or_b32 v26,v44,v46
v_cvt_pk_bf16_f32 v113,v114,v115
v_pk_mul_f16 v9,v8,v9
v_mfma_f32_16x16x32_bf16 v[48:51],a[208:211],a[76:79],v[48:51]
ds_read_b64_tr_b16 a[214:215],v3 offset:52848
ds_read_b64_tr_b16 a[208:209],v3 offset:57472
v_cvt_pk_bf16_f32 v27,v45,v47
v_cvt_pk_bf16_f32 v114,v72,v73
v_lshlrev_b32_sdwa v32,1,v32 dst_sel:DWORD dst_unused:UNUSED_PAD src0_sel:DWORD src1_sel:WORD_1
v_cvt_pk_bf16_f32 v115,v74,v75
v_mfma_f32_16x16x32_bf16 v[48:51],a[204:207],a[92:95],v[48:51]
ds_read_b64_tr_b16 a[210:211],v3 offset:59008
v_exp_f16 v8,v9
v_mul_f32_dpp v120,v14,v120 row_newbcast:8 row_mask:0xf bank_mask:0xf
v_mfma_f32_16x16x32_bf16 v[44:47],a[200:203],a[92:95],v[16:19]
ds_read_u16_d16_hi v63,v63
ds_read_u16 v32,v32
v_mul_f32_dpp v121,v14,v121 row_newbcast:9 row_mask:0xf bank_mask:0xf
v_lshlrev_b32_sdwa v34,1,v34 dst_sel:DWORD dst_unused:UNUSED_PAD src0_sel:DWORD src1_sel:WORD_1
v_mul_f32_dpp v122,v14,v122 row_newbcast:10 row_mask:0xf bank_mask:0xf
v_mfma_f32_16x16x32_bf16 v[44:47],a[196:199],a[76:79],v[44:47]
ds_read_u16_d16_hi v34,v34
ds_read_b64_tr_b16 a[204:205],v3 offset:57480
v_mul_f32_dpp v123,v14,v123 row_newbcast:11 row_mask:0xf bank_mask:0xf
v_lshlrev_b32_sdwa v33,1,v33 dst_sel:DWORD dst_unused:UNUSED_PAD src0_sel:DWORD src1_sel:WORD_1
v_mfma_f32_16x16x32_bf16 v[44:47],a[192:195],a[60:63],v[44:47]
ds_read_u16 v33,v33
v_exp_f16_sdwa v8,v9 dst_sel:WORD_1 dst_unused:UNUSED_PRESERVE src0_sel:WORD_1
v_mul_f32_dpp v88,v14,v88 row_newbcast:12 row_mask:0xf bank_mask:0xf
v_mul_f32_dpp v89,v14,v89 row_newbcast:13 row_mask:0xf bank_mask:0xf
v_mfma_f32_16x16x32_bf16 v[208:211],a[248:251],v[28:31],v[208:211]
ds_read_b64_tr_b16 a[206:207],v3 offset:59016
ds_read_b64_tr_b16 a[242:243],v3 offset:34368
v_mul_f32_dpp v90,v14,v90 row_newbcast:14 row_mask:0xf bank_mask:0xf
v_lshlrev_b32_sdwa v35,1,v35 dst_sel:DWORD dst_unused:UNUSED_PAD src0_sel:DWORD src1_sel:WORD_1
v_mfma_f32_16x16x32_bf16 v[212:215],a[248:251],v[24:27],v[212:215]
ds_read_u16_d16_hi v35,v35
ds_read_b64_tr_b16 a[228:229],v3 offset:32840
v_mul_f32_dpp v91,v14,v91 row_newbcast:15 row_mask:0xf bank_mask:0xf
v_lshlrev_b32_sdwa v52,1,v52 dst_sel:DWORD dst_unused:UNUSED_PAD src0_sel:DWORD src1_sel:WORD_1
v_cvt_pk_bf16_f32 v120,v120,v121
v_mfma_f32_16x16x32_bf16 v[172:175],a[236:239],v[24:27],v[172:175]
ds_read_u16 v52,v52
ds_read_b64_tr_b16 a[230:231],v3 offset:34376
v_cvt_pk_bf16_f32 v121,v122,v123
v_pk_fma_f16 v8,v8,s55,s55
v_mfma_f32_16x16x32_bf16 v[168:171],a[236:239],v[28:31],v[168:171]
ds_read_b64_tr_b16 a[200:201],v3 offset:51320
v_rcp_f16 v9,v8
v_cvt_pk_bf16_f32 v122,v88,v89
v_lshlrev_b32_sdwa v54,1,v54 dst_sel:DWORD dst_unused:UNUSED_PAD src0_sel:DWORD src1_sel:WORD_1
v_cvt_pk_bf16_f32 v123,v90,v91
v_mfma_f32_16x16x32_bf16 v[216:219],a[252:255],v[28:31],v[216:219]
buffer_store_dwordx4 v[156:159],v7,s[40:43],s8 offen offset:320 sc1
ds_read_u16_d16_hi v54,v54
v_mul_f32_dpp v128,v15,v128 row_newbcast:0 row_mask:0xf bank_mask:0xf
v_mfma_f32_16x16x32_bf16 v[220:223],a[252:255],v[24:27],v[220:223]
ds_read_b64_tr_b16 a[202:203],v3 offset:52856
ds_read_b64_tr_b16 a[248:249],v3 offset:32896
v_mul_f32_dpp v129,v15,v129 row_newbcast:1 row_mask:0xf bank_mask:0xf
v_lshlrev_b32_sdwa v53,1,v53 dst_sel:DWORD dst_unused:UNUSED_PAD src0_sel:DWORD src1_sel:WORD_1
v_mul_f32_dpp v130,v15,v130 row_newbcast:2 row_mask:0xf bank_mask:0xf
v_mfma_f32_16x16x32_bf16 v[188:191],a[224:227],v[24:27],v[188:191]
ds_read_u16 v53,v53
ds_read_b64_tr_b16 a[250:251],v3 offset:34432
v_mul_f32_dpp v131,v15,v131 row_newbcast:3 row_mask:0xf bank_mask:0xf
v_lshlrev_b32_sdwa v55,1,v55 dst_sel:DWORD dst_unused:UNUSED_PAD src0_sel:DWORD src1_sel:WORD_1
v_mfma_f32_16x16x32_bf16 v[184:187],a[224:227],v[28:31],v[184:187]
ds_read_u16_d16_hi v55,v55
v_rcp_f16_sdwa v9,v8 dst_sel:WORD_1 dst_unused:UNUSED_PRESERVE src0_sel:WORD_1
v_mul_f32_dpp v104,v15,v104 row_newbcast:4 row_mask:0xf bank_mask:0xf
v_mul_f32_dpp v105,v15,v105 row_newbcast:5 row_mask:0xf bank_mask:0xf
v_mfma_f32_16x16x32_bf16 v[224:227],a[232:235],v[28:31],v[224:227]
ds_read_b64_tr_b16 a[236:237],v3 offset:32904
ds_read_b64_tr_b16 a[238:239],v3 offset:34440
v_mul_f32_dpp v106,v15,v106 row_newbcast:6 row_mask:0xf bank_mask:0xf
v_lshlrev_b32_sdwa v56,1,v56 dst_sel:DWORD dst_unused:UNUSED_PAD src0_sel:DWORD src1_sel:WORD_1
v_mfma_f32_16x16x32_bf16 v[228:231],a[232:235],v[24:27],v[228:231]
ds_read_u16 v56,v56
ds_read_b128 v[96:99],v4 offset:384
v_mul_f32_dpp v107,v15,v107 row_newbcast:7 row_mask:0xf bank_mask:0xf
v_cvt_pk_bf16_f32 v128,v128,v129
v_lshlrev_b32_sdwa v58,1,v58 dst_sel:DWORD dst_unused:UNUSED_PAD src0_sel:DWORD src1_sel:WORD_1
v_mfma_f32_16x16x32_bf16 v[204:207],a[244:247],v[24:27],v[204:207]
ds_read_u16_d16_hi v58,v58
ds_read_b128 v[100:103],v4 offset:416
v_cvt_pk_bf16_f32 v129,v130,v131
v_fma_mix_f32 v57,v57,v9,0 op_sel_hi:[0,1,0]
v_mfma_f32_16x16x32_bf16 v[200:203],a[244:247],v[28:31],v[200:203]
s_waitcnt lgkmcnt(13)
ds_read_b64_tr_b16 a[252:253],v3 offset:32960
ds_read_b64_tr_b16 a[254:255],v3 offset:34496
v_cvt_pk_bf16_f32 v130,v104,v105
v_cvt_pk_bf16_f32 v131,v106,v107
v_fma_mix_f32 v59,v59,v9,0 op_sel:[0,1,0] op_sel_hi:[0,1,0]
v_mfma_f32_16x16x32_bf16 v[24:27],a[96:99],a[48:51],v[64:67]
ds_read_b64_tr_b16 a[224:225],v3 offset:32968
ds_read_b64_tr_b16 a[226:227],v3 offset:34504
v_mul_f32_dpp v116,v14,v116 row_newbcast:0 row_mask:0xf bank_mask:0xf
v_lshlrev_b32_sdwa v36,1,v36 dst_sel:DWORD dst_unused:UNUSED_PAD src0_sel:DWORD src1_sel:WORD_1
v_mfma_f32_16x16x32_bf16 v[24:27],a[100:103],a[64:67],v[24:27]
ds_read_b64_tr_b16 a[196:197],v3 offset:51376
ds_read_b64_tr_b16 a[198:199],v3 offset:52912
v_mul_f32_dpp v117,v14,v117 row_newbcast:1 row_mask:0xf bank_mask:0xf
v_fma_mixlo_f16 v8,v49,s99,0
v_mul_f32_dpp v118,v14,v118 row_newbcast:2 row_mask:0xf bank_mask:0xf
v_mfma_f32_16x16x32_bf16 v[24:27],a[104:107],a[80:83],v[24:27]
ds_read_u16 v36,v36
ds_read_b128 v[20:23],v4 offset:512
v_lshl_add_u64 v[60:61],v[60:61],0,v[62:63]
v_mul_f32_dpp v119,v14,v119 row_newbcast:3 row_mask:0xf bank_mask:0xf
v_lshlrev_b32_sdwa v38,1,v38 dst_sel:DWORD dst_unused:UNUSED_PAD src0_sel:DWORD src1_sel:WORD_1
v_mfma_f32_16x16x32_bf16 v[28:31],a[108:111],a[80:83],v[68:71]
buffer_store_dwordx4 v[112:115],v7,s[40:43],s9 offen offset:0 sc1
ds_read_u16_d16_hi v38,v38
v_lshl_add_u64 v[62:63],v[32:33],0,v[34:35]
v_mul_f32_dpp v76,v14,v76 row_newbcast:4 row_mask:0xf bank_mask:0xf
v_fma_mixhi_f16 v8,v51,s99,0
v_mul_f32_dpp v77,v14,v77 row_newbcast:5 row_mask:0xf bank_mask:0xf
v_mfma_f32_16x16x32_bf16 v[28:31],a[112:115],a[64:67],v[28:31]
s_waitcnt lgkmcnt(15)
ds_read_b128 v[16:19],v4 offset:544
v_lshl_add_u64 v[32:33],v[52:53],0,v[54:55]
v_mul_f32_dpp v78,v14,v78 row_newbcast:6 row_mask:0xf bank_mask:0xf
v_lshlrev_b32_sdwa v37,1,v37 dst_sel:DWORD dst_unused:UNUSED_PAD src0_sel:DWORD src1_sel:WORD_1
v_pk_fma_f16 v9,v8,v8,s54
v_mfma_f32_16x16x32_bf16 v[28:31],a[116:119],a[48:51],v[28:31]
ds_read_u16 v37,v37
v_mul_f32_dpp v79,v14,v79 row_newbcast:7 row_mask:0xf bank_mask:0xf
v_lshlrev_b32_sdwa v39,1,v39 dst_sel:DWORD dst_unused:UNUSED_PAD src0_sel:DWORD src1_sel:WORD_1
v_cvt_pk_bf16_f32 v116,v116,v117
v_mfma_f32_16x16x32_bf16 v[52:55],a[120:123],a[48:51],v[84:87]
s_waitcnt lgkmcnt(12)
ds_read_u16_d16_hi v39,v39
v_or_b32 v34,v56,v58
v_cvt_pk_bf16_f32 v117,v118,v119
v_pk_mul_f16 v9,v8,v9
v_mfma_f32_16x16x32_bf16 v[52:55],a[124:127],a[64:67],v[52:55]
ds_read_b64_tr_b16 a[192:193],v3 offset:57536
v_cvt_pk_bf16_f32 v35,v57,v59
v_cvt_pk_bf16_f32 v118,v76,v77
v_lshlrev_b32_sdwa v40,1,v40 dst_sel:DWORD dst_unused:UNUSED_PAD src0_sel:DWORD src1_sel:WORD_1
v_cvt_pk_bf16_f32 v119,v78,v79
v_mfma_f32_16x16x32_bf16 v[52:55],a[128:131],a[80:83],v[52:55]
v_exp_f16 v8,v9
v_mul_f32_dpp v124,v14,v124 row_newbcast:8 row_mask:0xf bank_mask:0xf
v_mfma_f32_16x16x32_bf16 v[56:59],a[132:135],a[80:83],v[80:83]
ds_read_u16 v40,v40
v_mul_f32_dpp v125,v14,v125 row_newbcast:9 row_mask:0xf bank_mask:0xf
v_lshlrev_b32_sdwa v42,1,v42 dst_sel:DWORD dst_unused:UNUSED_PAD src0_sel:DWORD src1_sel:WORD_1
v_mul_f32_dpp v126,v14,v126 row_newbcast:10 row_mask:0xf bank_mask:0xf
v_mfma_f32_16x16x32_bf16 v[56:59],a[136:139],a[64:67],v[56:59]
ds_read_u16_d16_hi v42,v42
v_mul_f32_dpp v127,v14,v127 row_newbcast:11 row_mask:0xf bank_mask:0xf
v_lshlrev_b32_sdwa v41,1,v41 dst_sel:DWORD dst_unused:UNUSED_PAD src0_sel:DWORD src1_sel:WORD_1
v_mfma_f32_16x16x32_bf16 v[56:59],a[140:143],a[48:51],v[56:59]
ds_read_u16 v41,v41
v_exp_f16_sdwa v8,v9 dst_sel:WORD_1 dst_unused:UNUSED_PRESERVE src0_sel:WORD_1
v_mul_f32_dpp v92,v14,v92 row_newbcast:12 row_mask:0xf bank_mask:0xf
v_mul_f32_dpp v93,v14,v93 row_newbcast:13 row_mask:0xf bank_mask:0xf
v_mfma_f32_16x16x32_bf16 v[232:235],a[240:243],v[60:63],v[232:235]
ds_read_b64_tr_b16 a[194:195],v3 offset:59072
v_mul_f32_dpp v94,v14,v94 row_newbcast:14 row_mask:0xf bank_mask:0xf
v_lshlrev_b32_sdwa v43,1,v43 dst_sel:DWORD dst_unused:UNUSED_PAD src0_sel:DWORD src1_sel:WORD_1
v_mfma_f32_16x16x32_bf16 v[236:239],a[240:243],v[32:35],v[236:239]
ds_read_u16_d16_hi v43,v43
v_mul_f32_dpp v95,v14,v95 row_newbcast:15 row_mask:0xf bank_mask:0xf
v_lshlrev_b32_sdwa v44,1,v44 dst_sel:DWORD dst_unused:UNUSED_PAD src0_sel:DWORD src1_sel:WORD_1
v_cvt_pk_bf16_f32 v124,v124,v125
v_mfma_f32_16x16x32_bf16 v[164:167],a[228:231],v[32:35],v[164:167]
buffer_store_dwordx4 v[120:123],v7,s[40:43],s9 offen offset:64 sc1
v_cvt_pk_bf16_f32 v125,v126,v127
v_pk_fma_f16 v8,v8,s55,s55
v_mfma_f32_16x16x32_bf16 v[160:163],a[228:231],v[60:63],v[160:163]
v_rcp_f16 v9,v8
v_cvt_pk_bf16_f32 v126,v92,v93
v_lshlrev_b32_sdwa v46,1,v46 dst_sel:DWORD dst_unused:UNUSED_PAD src0_sel:DWORD src1_sel:WORD_1
v_cvt_pk_bf16_f32 v127,v94,v95
v_mfma_f32_16x16x32_bf16 v[240:243],a[248:251],v[60:63],v[240:243]
ds_read_u16 v44,v44
v_mul_f32_dpp v132,v15,v132 row_newbcast:0 row_mask:0xf bank_mask:0xf
v_mfma_f32_16x16x32_bf16 v[244:247],a[248:251],v[32:35],v[244:247]
ds_read_u16_d16_hi v46,v46
ds_read_b64_tr_b16 a[216:217],v3 offset:57544
v_mul_f32_dpp v133,v15,v133 row_newbcast:1 row_mask:0xf bank_mask:0xf
v_lshlrev_b32_sdwa v45,1,v45 dst_sel:DWORD dst_unused:UNUSED_PAD src0_sel:DWORD src1_sel:WORD_1
v_mul_f32_dpp v134,v15,v134 row_newbcast:2 row_mask:0xf bank_mask:0xf
v_mfma_f32_16x16x32_bf16 v[180:183],a[236:239],v[32:35],v[180:183]
ds_read_u16 v45,v45
v_mul_f32_dpp v135,v15,v135 row_newbcast:3 row_mask:0xf bank_mask:0xf
v_lshlrev_b32_sdwa v47,1,v47 dst_sel:DWORD dst_unused:UNUSED_PAD src0_sel:DWORD src1_sel:WORD_1
v_mfma_f32_16x16x32_bf16 v[176:179],a[236:239],v[60:63],v[176:179]
s_waitcnt lgkmcnt(7)
ds_read_u16_d16_hi v47,v47
v_rcp_f16_sdwa v9,v8 dst_sel:WORD_1 dst_unused:UNUSED_PRESERVE src0_sel:WORD_1
v_mul_f32_dpp v108,v15,v108 row_newbcast:4 row_mask:0xf bank_mask:0xf
v_mul_f32_dpp v109,v15,v109 row_newbcast:5 row_mask:0xf bank_mask:0xf
v_mfma_f32_16x16x32_bf16 v[248:251],a[252:255],v[60:63],v[248:251]
ds_read_b64_tr_b16 a[218:219],v3 offset:59080
v_mul_f32_dpp v110,v15,v110 row_newbcast:6 row_mask:0xf bank_mask:0xf
v_lshlrev_b32_sdwa v48,1,v48 dst_sel:DWORD dst_unused:UNUSED_PAD src0_sel:DWORD src1_sel:WORD_1
v_mfma_f32_16x16x32_bf16 v[252:255],a[252:255],v[32:35],v[252:255]
ds_read_u16 v48,v48
v_mul_f32_dpp v111,v15,v111 row_newbcast:7 row_mask:0xf bank_mask:0xf
v_cvt_pk_bf16_f32 v132,v132,v133
v_lshlrev_b32_sdwa v50,1,v50 dst_sel:DWORD dst_unused:UNUSED_PAD src0_sel:DWORD src1_sel:WORD_1
v_mfma_f32_16x16x32_bf16 v[196:199],a[224:227],v[32:35],v[196:199]
ds_read_u16_d16_hi v50,v50
v_cvt_pk_bf16_f32 v133,v134,v135
v_fma_mix_f32 v49,v49,v9,0 op_sel_hi:[0,1,0]
v_mfma_f32_16x16x32_bf16 v[192:195],a[224:227],v[60:63],v[192:195]
ds_read_b64_tr_b16 a[220:221],v3 offset:51384
v_cvt_pk_bf16_f32 v134,v108,v109
v_cvt_pk_bf16_f32 v135,v110,v111
v_fma_mix_f32 v51,v51,v9,0 op_sel:[0,1,0] op_sel_hi:[0,1,0]
v_mfma_f32_16x16x32_bf16 v[32:35],a[144:147],a[48:51],v[96:99]
ds_read_b64_tr_b16 a[222:223],v3 offset:52920
v_lshlrev_b32_sdwa v24,1,v24 dst_sel:DWORD dst_unused:UNUSED_PAD src0_sel:DWORD src1_sel:WORD_1
v_mfma_f32_16x16x32_bf16 v[32:35],a[148:151],a[64:67],v[32:35]
ds_read_u16 v24,v24
v_lshlrev_b32_sdwa v26,1,v26 dst_sel:DWORD dst_unused:UNUSED_PAD src0_sel:DWORD src1_sel:WORD_1
v_mfma_f32_16x16x32_bf16 v[32:35],a[152:155],a[80:83],v[32:35]
buffer_store_dwordx4 v[128:131],v7,s[40:43],s9 offen offset:128 sc1
v_lshl_add_u64 v[36:37],v[36:37],0,v[38:39]
v_mfma_f32_16x16x32_bf16 v[60:63],a[156:159],a[80:83],v[100:103]
s_waitcnt lgkmcnt(7)
ds_read_u16_d16_hi v26,v26
v_lshl_add_u64 v[38:39],v[40:41],0,v[42:43]
v_lshlrev_b32_sdwa v25,1,v25 dst_sel:DWORD dst_unused:UNUSED_PAD src0_sel:DWORD src1_sel:WORD_1
v_mfma_f32_16x16x32_bf16 v[60:63],a[160:163],a[64:67],v[60:63]
s_waitcnt lgkmcnt(7)
ds_read_u16 v25,v25
v_lshl_add_u64 v[40:41],v[44:45],0,v[46:47]
v_mfma_f32_16x16x32_bf16 v[60:63],a[164:167],a[48:51],v[60:63]
TP10849_%=:
s_cbranch_execz TR1_%=
ds_read_b64_tr_b16 a[232:233],v3 offset:51440
v_lshlrev_b32_sdwa v27,1,v27 dst_sel:DWORD dst_unused:UNUSED_PAD src0_sel:DWORD src1_sel:WORD_1
v_mfma_f32_16x16x32_bf16 v[44:47],a[168:171],a[48:51],v[16:19]
s_waitcnt lgkmcnt(6)
ds_read_u16_d16_hi v27,v27
v_or_b32 v42,v48,v50
v_lshlrev_b32_sdwa v28,1,v28 dst_sel:DWORD dst_unused:UNUSED_PAD src0_sel:DWORD src1_sel:WORD_1
v_mfma_f32_16x16x32_bf16 v[44:47],a[172:175],a[64:67],v[44:47]
ds_read_u16 v28,v28
v_cvt_pk_bf16_f32 v43,v49,v51
v_lshlrev_b32_sdwa v30,1,v30 dst_sel:DWORD dst_unused:UNUSED_PAD src0_sel:DWORD src1_sel:WORD_1
v_mfma_f32_16x16x32_bf16 v[44:47],a[176:179],a[80:83],v[44:47]
ds_read_u16_d16_hi v30,v30
v_lshlrev_b32_sdwa v29,1,v29 dst_sel:DWORD dst_unused:UNUSED_PAD src0_sel:DWORD src1_sel:WORD_1
v_mfma_f32_16x16x32_bf16 v[48:51],a[180:183],a[80:83],v[20:23]
ds_read_u16 v29,v29
v_lshlrev_b32_sdwa v31,1,v31 dst_sel:DWORD dst_unused:UNUSED_PAD src0_sel:DWORD src1_sel:WORD_1
v_mfma_f32_16x16x32_bf16 v[48:51],a[184:187],a[64:67],v[48:51]
ds_read_u16_d16_hi v31,v31
v_lshlrev_b32_sdwa v56,1,v56 dst_sel:DWORD dst_unused:UNUSED_PAD src0_sel:DWORD src1_sel:WORD_1
v_mfma_f32_16x16x32_bf16 v[48:51],a[188:191],a[48:51],v[48:51]
ds_read_u16 v56,v56
v_lshlrev_b32_sdwa v58,1,v58 dst_sel:DWORD dst_unused:UNUSED_PAD src0_sel:DWORD src1_sel:WORD_1
v_mfma_f32_16x16x32_bf16 v[208:211],a[240:243],v[36:39],v[208:211]
ds_read_u16_d16_hi v58,v58
v_lshlrev_b32_sdwa v57,1,v57 dst_sel:DWORD dst_unused:UNUSED_PAD src0_sel:DWORD src1_sel:WORD_1
v_mfma_f32_16x16x32_bf16 v[212:215],a[240:243],v[40:43],v[212:215]
ds_read_u16 v57,v57
v_lshlrev_b32_sdwa v59,1,v59 dst_sel:DWORD dst_unused:UNUSED_PAD src0_sel:DWORD src1_sel:WORD_1
v_mfma_f32_16x16x32_bf16 v[172:175],a[228:231],v[40:43],v[172:175]
ds_read_u16_d16_hi v59,v59
v_lshlrev_b32_sdwa v52,1,v52 dst_sel:DWORD dst_unused:UNUSED_PAD src0_sel:DWORD src1_sel:WORD_1
v_mfma_f32_16x16x32_bf16 v[168:171],a[228:231],v[36:39],v[168:171]
ds_read_u16 v52,v52
v_lshlrev_b32_sdwa v54,1,v54 dst_sel:DWORD dst_unused:UNUSED_PAD src0_sel:DWORD src1_sel:WORD_1
v_mfma_f32_16x16x32_bf16 v[216:219],a[248:251],v[36:39],v[216:219]
ds_read_u16_d16_hi v54,v54
v_lshlrev_b32_sdwa v53,1,v53 dst_sel:DWORD dst_unused:UNUSED_PAD src0_sel:DWORD src1_sel:WORD_1
v_mfma_f32_16x16x32_bf16 v[220:223],a[248:251],v[40:43],v[220:223]
ds_read_u16 v53,v53
ds_read_b64_tr_b16 a[234:235],v3 offset:52976
v_mfma_f32_16x16x32_bf16 v[188:191],a[236:239],v[40:43],v[188:191]
ds_read_b64_tr_b16 a[244:245],v3 offset:57600
v_lshlrev_b32_sdwa v55,1,v55 dst_sel:DWORD dst_unused:UNUSED_PAD src0_sel:DWORD src1_sel:WORD_1
v_mfma_f32_16x16x32_bf16 v[184:187],a[236:239],v[36:39],v[184:187]
buffer_store_dwordx4 v[116:119],v7,s[40:43],s9 offen offset:192 sc1
ds_read_u16_d16_hi v55,v55
v_mfma_f32_16x16x32_bf16 v[224:227],a[252:255],v[36:39],v[224:227]
ds_read_b64_tr_b16 a[246:247],v3 offset:59136
v_mfma_f32_16x16x32_bf16 v[228:231],a[252:255],v[40:43],v[228:231]
ds_read_b64_tr_b16 a[240:241],v3 offset:57608
v_mfma_f32_16x16x32_bf16 v[204:207],a[224:227],v[40:43],v[204:207]
ds_read_b64_tr_b16 a[242:243],v3 offset:59144
v_mfma_f32_16x16x32_bf16 v[200:203],a[224:227],v[36:39],v[200:203]
ds_read_b64_tr_b16 a[228:229],v3 offset:51448
v_mfma_f32_16x16x32_bf16 v[40:43],a[96:99],a[52:55],v[64:67]
ds_read_b64_tr_b16 a[230:231],v3 offset:52984
v_lshlrev_b32_sdwa v32,1,v32 dst_sel:DWORD dst_unused:UNUSED_PAD src0_sel:DWORD src1_sel:WORD_1
v_mfma_f32_16x16x32_bf16 v[40:43],a[100:103],a[68:71],v[40:43]
ds_read_u16 v32,v32
v_lshlrev_b32_sdwa v34,1,v34 dst_sel:DWORD dst_unused:UNUSED_PAD src0_sel:DWORD src1_sel:WORD_1
v_mfma_f32_16x16x32_bf16 v[40:43],a[104:107],a[84:87],v[40:43]
s_waitcnt lgkmcnt(8)
ds_read_u16_d16_hi v34,v34
v_lshl_add_u64 v[24:25],v[24:25],0,v[26:27]
v_mfma_f32_16x16x32_bf16 v[36:39],a[108:111],a[84:87],v[68:71]
v_lshl_add_u64 v[26:27],v[28:29],0,v[30:31]
v_lshlrev_b32_sdwa v33,1,v33 dst_sel:DWORD dst_unused:UNUSED_PAD src0_sel:DWORD src1_sel:WORD_1
v_mfma_f32_16x16x32_bf16 v[36:39],a[112:115],a[68:71],v[36:39]
ds_read_u16 v33,v33
v_lshl_add_u64 v[28:29],v[56:57],0,v[58:59]
v_mfma_f32_16x16x32_bf16 v[36:39],a[116:119],a[52:55],v[36:39]
v_lshlrev_b32_sdwa v35,1,v35 dst_sel:DWORD dst_unused:UNUSED_PAD src0_sel:DWORD src1_sel:WORD_1
v_lshlrev_b32_sdwa v60,1,v60 dst_sel:DWORD dst_unused:UNUSED_PAD src0_sel:DWORD src1_sel:WORD_1
v_mfma_f32_16x16x32_bf16 v[56:59],a[120:123],a[52:55],v[84:87]
ds_read_u16_d16_hi v35,v35
v_lshlrev_b32_sdwa v62,1,v62 dst_sel:DWORD dst_unused:UNUSED_PAD src0_sel:DWORD src1_sel:WORD_1
v_mfma_f32_16x16x32_bf16 v[56:59],a[124:127],a[68:71],v[56:59]
s_waitcnt lgkmcnt(6)
ds_read_u16 v60,v60
v_lshl_add_u64 v[30:31],v[52:53],0,v[54:55]
v_mfma_f32_16x16x32_bf16 v[56:59],a[128:131],a[84:87],v[56:59]
ds_read_u16_d16_hi v62,v62
v_lshlrev_b32_sdwa v61,1,v61 dst_sel:DWORD dst_unused:UNUSED_PAD src0_sel:DWORD src1_sel:WORD_1
v_mfma_f32_16x16x32_bf16 v[52:55],a[132:135],a[84:87],v[80:83]
ds_read_u16 v61,v61
v_lshlrev_b32_sdwa v63,1,v63 dst_sel:DWORD dst_unused:UNUSED_PAD src0_sel:DWORD src1_sel:WORD_1
v_mfma_f32_16x16x32_bf16 v[52:55],a[136:139],a[68:71],v[52:55]
ds_read_u16_d16_hi v63,v63
v_lshlrev_b32_sdwa v48,1,v48 dst_sel:DWORD dst_unused:UNUSED_PAD src0_sel:DWORD src1_sel:WORD_1
v_mfma_f32_16x16x32_bf16 v[52:55],a[140:143],a[52:55],v[52:55]
ds_read_u16 v48,v48
v_lshlrev_b32_sdwa v50,1,v50 dst_sel:DWORD dst_unused:UNUSED_PAD src0_sel:DWORD src1_sel:WORD_1
v_mfma_f32_16x16x32_bf16 v[232:235],a[0:3],v[24:27],v[232:235]
ds_read_u16_d16_hi v50,v50
v_lshlrev_b32_sdwa v49,1,v49 dst_sel:DWORD dst_unused:UNUSED_PAD src0_sel:DWORD src1_sel:WORD_1
v_mfma_f32_16x16x32_bf16 v[232:235],a[4:7],v[28:31],v[232:235]
buffer_store_dwordx4 v[124:127],v7,s[40:43],s9 offen offset:256 sc1
v_lshlrev_b32_sdwa v51,1,v51 dst_sel:DWORD dst_unused:UNUSED_PAD src0_sel:DWORD src1_sel:WORD_1
v_mfma_f32_16x16x32_bf16 v[160:163],a[8:11],v[28:31],v[160:163]
ds_read_u16 v49,v49
v_lshlrev_b32_sdwa v44,1,v44 dst_sel:DWORD dst_unused:UNUSED_PAD src0_sel:DWORD src1_sel:WORD_1
v_mfma_f32_16x16x32_bf16 v[160:163],a[12:15],v[24:27],v[160:163]
ds_read_u16_d16_hi v51,v51
v_lshlrev_b32_sdwa v46,1,v46 dst_sel:DWORD dst_unused:UNUSED_PAD src0_sel:DWORD src1_sel:WORD_1
v_mfma_f32_16x16x32_bf16 v[240:243],a[16:19],v[24:27],v[240:243]
ds_read_u16 v44,v44
v_lshlrev_b32_sdwa v45,1,v45 dst_sel:DWORD dst_unused:UNUSED_PAD src0_sel:DWORD src1_sel:WORD_1
v_mfma_f32_16x16x32_bf16 v[240:243],a[20:23],v[28:31],v[240:243]
ds_read_u16_d16_hi v46,v46
v_lshlrev_b32_sdwa v47,1,v47 dst_sel:DWORD dst_unused:UNUSED_PAD src0_sel:DWORD src1_sel:WORD_1
v_mfma_f32_16x16x32_bf16 v[176:179],a[24:27],v[28:31],v[176:179]
ds_read_u16 v45,v45
v_mfma_f32_16x16x32_bf16 v[176:179],a[28:31],v[24:27],v[176:179]
ds_read_u16_d16_hi v47,v47
v_mfma_f32_16x16x32_bf16 v[248:251],a[32:35],v[24:27],v[248:251]
v_mfma_f32_16x16x32_bf16 v[248:251],a[36:39],v[28:31],v[248:251]
v_mfma_f32_16x16x32_bf16 v[192:195],a[40:43],v[28:31],v[192:195]
v_mfma_f32_16x16x32_bf16 v[192:195],a[44:47],v[24:27],v[192:195]
v_mfma_f32_16x16x32_bf16 v[28:31],a[144:147],a[52:55],v[96:99]
v_lshlrev_b32_sdwa v40,1,v40 dst_sel:DWORD dst_unused:UNUSED_PAD src0_sel:DWORD src1_sel:WORD_1
v_lshlrev_b32_sdwa v42,1,v42 dst_sel:DWORD dst_unused:UNUSED_PAD src0_sel:DWORD src1_sel:WORD_1
v_mfma_f32_16x16x32_bf16 v[28:31],a[148:151],a[68:71],v[28:31]
ds_read_u16 v40,v40
v_lshlrev_b32_sdwa v41,1,v41 dst_sel:DWORD dst_unused:UNUSED_PAD src0_sel:DWORD src1_sel:WORD_1
v_mfma_f32_16x16x32_bf16 v[28:31],a[152:155],a[84:87],v[28:31]
s_waitcnt lgkmcnt(3)
ds_read_u16_d16_hi v42,v42
v_lshl_add_u64 v[32:33],v[32:33],0,v[34:35]
v_mfma_f32_16x16x32_bf16 v[24:27],a[156:159],a[84:87],v[100:103]
ds_read_u16 v41,v41
v_lshl_add_u64 v[34:35],v[60:61],0,v[62:63]
v_mfma_f32_16x16x32_bf16 v[24:27],a[160:163],a[68:71],v[24:27]
v_lshl_add_u64 v[60:61],v[48:49],0,v[50:51]
v_lshlrev_b32_sdwa v43,1,v43 dst_sel:DWORD dst_unused:UNUSED_PAD src0_sel:DWORD src1_sel:WORD_1
v_mfma_f32_16x16x32_bf16 v[24:27],a[164:167],a[52:55],v[24:27]
ds_read_u16_d16_hi v43,v43
v_lshlrev_b32_sdwa v36,1,v36 dst_sel:DWORD dst_unused:UNUSED_PAD src0_sel:DWORD src1_sel:WORD_1
v_mfma_f32_16x16x32_bf16 v[48:51],a[168:171],a[52:55],v[16:19]
ds_read_u16 v36,v36
v_lshlrev_b32_sdwa v38,1,v38 dst_sel:DWORD dst_unused:UNUSED_PAD src0_sel:DWORD src1_sel:WORD_1
v_mfma_f32_16x16x32_bf16 v[48:51],a[172:175],a[68:71],v[48:51]
s_waitcnt lgkmcnt(5)
ds_read_u16_d16_hi v38,v38
v_lshl_add_u64 v[62:63],v[44:45],0,v[46:47]
v_mfma_f32_16x16x32_bf16 v[48:51],a[176:179],a[84:87],v[48:51]
buffer_store_dwordx4 v[132:135],v7,s[40:43],s9 offen offset:320 sc1
v_lshlrev_b32_sdwa v37,1,v37 dst_sel:DWORD dst_unused:UNUSED_PAD src0_sel:DWORD src1_sel:WORD_1
v_mfma_f32_16x16x32_bf16 v[44:47],a[180:183],a[84:87],v[20:23]
ds_read_u16 v37,v37
v_lshlrev_b32_sdwa v39,1,v39 dst_sel:DWORD dst_unused:UNUSED_PAD src0_sel:DWORD src1_sel:WORD_1
v_mfma_f32_16x16x32_bf16 v[44:47],a[184:187],a[68:71],v[44:47]
ds_read_u16_d16_hi v39,v39
v_lshlrev_b32_sdwa v52,1,v52 dst_sel:DWORD dst_unused:UNUSED_PAD src0_sel:DWORD src1_sel:WORD_1
v_mfma_f32_16x16x32_bf16 v[44:47],a[188:191],a[52:55],v[44:47]
ds_read_u16 v52,v52
v_lshlrev_b32_sdwa v54,1,v54 dst_sel:DWORD dst_unused:UNUSED_PAD src0_sel:DWORD src1_sel:WORD_1
v_mfma_f32_16x16x32_bf16 v[232:235],a[212:215],v[32:35],v[232:235]
ds_read_u16_d16_hi v54,v54
v_lshlrev_b32_sdwa v53,1,v53 dst_sel:DWORD dst_unused:UNUSED_PAD src0_sel:DWORD src1_sel:WORD_1
v_mfma_f32_16x16x32_bf16 v[232:235],a[208:211],v[60:63],v[232:235]
ds_read_u16 v53,v53
v_lshlrev_b32_sdwa v55,1,v55 dst_sel:DWORD dst_unused:UNUSED_PAD src0_sel:DWORD src1_sel:WORD_1
v_mfma_f32_16x16x32_bf16 v[160:163],a[204:207],v[60:63],v[160:163]
ds_read_u16_d16_hi v55,v55
v_lshlrev_b32_sdwa v56,1,v56 dst_sel:DWORD dst_unused:UNUSED_PAD src0_sel:DWORD src1_sel:WORD_1
v_mfma_f32_16x16x32_bf16 v[160:163],a[200:203],v[32:35],v[160:163]
ds_read_u16 v56,v56
v_lshlrev_b32_sdwa v58,1,v58 dst_sel:DWORD dst_unused:UNUSED_PAD src0_sel:DWORD src1_sel:WORD_1
v_mfma_f32_16x16x32_bf16 v[240:243],a[196:199],v[32:35],v[240:243]
ds_read_u16_d16_hi v58,v58
v_lshlrev_b32_sdwa v57,1,v57 dst_sel:DWORD dst_unused:UNUSED_PAD src0_sel:DWORD src1_sel:WORD_1
v_mfma_f32_16x16x32_bf16 v[240:243],a[192:195],v[60:63],v[240:243]
ds_read_u16 v57,v57
v_lshlrev_b32_sdwa v59,1,v59 dst_sel:DWORD dst_unused:UNUSED_PAD src0_sel:DWORD src1_sel:WORD_1
v_mfma_f32_16x16x32_bf16 v[176:179],a[216:219],v[60:63],v[176:179]
ds_read_u16_d16_hi v59,v59
v_mfma_f32_16x16x32_bf16 v[176:179],a[220:223],v[32:35],v[176:179]
v_mfma_f32_16x16x32_bf16 v[248:251],a[232:235],v[32:35],v[248:251]
v_mfma_f32_16x16x32_bf16 v[248:251],a[244:247],v[60:63],v[248:251]
v_mfma_f32_16x16x32_bf16 v[192:195],a[240:243],v[60:63],v[192:195]
v_mfma_f32_16x16x32_bf16 v[192:195],a[228:231],v[32:35],v[192:195]
v_mfma_f32_16x16x32_bf16 v[60:63],a[96:99],a[56:59],v[64:67]
v_lshlrev_b32_sdwa v28,1,v28 dst_sel:DWORD dst_unused:UNUSED_PAD src0_sel:DWORD src1_sel:WORD_1
v_mul_f32_dpp v232,v14,v232 row_newbcast:0 row_mask:0xf bank_mask:0xf
s_add_u32 s10,s61,4718592
v_mfma_f32_16x16x32_bf16 v[60:63],a[100:103],a[72:75],v[60:63]
ds_read_u16 v28,v28
v_lshlrev_b32_sdwa v30,1,v30 dst_sel:DWORD dst_unused:UNUSED_PAD src0_sel:DWORD src1_sel:WORD_1
v_mfma_f32_16x16x32_bf16 v[60:63],a[104:107],a[88:91],v[60:63]
s_waitcnt lgkmcnt(2)
ds_read_u16_d16_hi v30,v30
v_lshl_add_u64 v[40:41],v[40:41],0,v[42:43]
v_mul_f32_dpp v233,v14,v233 row_newbcast:1 row_mask:0xf bank_mask:0xf
v_mfma_f32_16x16x32_bf16 v[32:35],a[108:111],a[88:91],v[68:71]
v_lshl_add_u64 v[42:43],v[36:37],0,v[38:39]
v_lshlrev_b32_sdwa v29,1,v29 dst_sel:DWORD dst_unused:UNUSED_PAD src0_sel:DWORD src1_sel:WORD_1
v_mul_f32_dpp v234,v14,v234 row_newbcast:2 row_mask:0xf bank_mask:0xf
v_mfma_f32_16x16x32_bf16 v[32:35],a[112:115],a[72:75],v[32:35]
ds_read_u16 v29,v29
v_lshl_add_u64 v[36:37],v[52:53],0,v[54:55]
v_mfma_f32_16x16x32_bf16 v[32:35],a[116:119],a[56:59],v[32:35]
v_lshlrev_b32_sdwa v31,1,v31 dst_sel:DWORD dst_unused:UNUSED_PAD src0_sel:DWORD src1_sel:WORD_1
v_mul_f32_dpp v235,v14,v235 row_newbcast:3 row_mask:0xf bank_mask:0xf
v_mfma_f32_16x16x32_bf16 v[52:55],a[120:123],a[56:59],v[84:87]
ds_read_u16_d16_hi v31,v31
v_lshlrev_b32_sdwa v24,1,v24 dst_sel:DWORD dst_unused:UNUSED_PAD src0_sel:DWORD src1_sel:WORD_1
v_mul_f32_dpp v160,v14,v160 row_newbcast:4 row_mask:0xf bank_mask:0xf
v_mfma_f32_16x16x32_bf16 v[52:55],a[124:127],a[72:75],v[52:55]
s_waitcnt lgkmcnt(4)
ds_read_u16 v24,v24
v_lshl_add_u64 v[38:39],v[56:57],0,v[58:59]
v_mul_f32_dpp v161,v14,v161 row_newbcast:5 row_mask:0xf bank_mask:0xf
v_lshlrev_b32_sdwa v26,1,v26 dst_sel:DWORD dst_unused:UNUSED_PAD src0_sel:DWORD src1_sel:WORD_1
v_mfma_f32_16x16x32_bf16 v[52:55],a[128:131],a[88:91],v[52:55]
ds_read_u16_d16_hi v26,v26
v_mul_f32_dpp v162,v14,v162 row_newbcast:6 row_mask:0xf bank_mask:0xf
v_mfma_f32_16x16x32_bf16 v[56:59],a[132:135],a[88:91],v[80:83]
v_lshlrev_b32_sdwa v25,1,v25 dst_sel:DWORD dst_unused:UNUSED_PAD src0_sel:DWORD src1_sel:WORD_1
v_mul_f32_dpp v163,v14,v163 row_newbcast:7 row_mask:0xf bank_mask:0xf
v_mfma_f32_16x16x32_bf16 v[56:59],a[136:139],a[72:75],v[56:59]
ds_read_u16 v25,v25
v_lshlrev_b32_sdwa v27,1,v27 dst_sel:DWORD dst_unused:UNUSED_PAD src0_sel:DWORD src1_sel:WORD_1
v_mfma_f32_16x16x32_bf16 v[56:59],a[140:143],a[56:59],v[56:59]
ds_read_u16_d16_hi v27,v27
v_cvt_pk_bf16_f32 v232,v232,v233
v_mfma_f32_16x16x32_bf16 v[236:239],a[0:3],v[40:43],v[236:239]
v_lshlrev_b32_sdwa v44,1,v44 dst_sel:DWORD dst_unused:UNUSED_PAD src0_sel:DWORD src1_sel:WORD_1
v_cvt_pk_bf16_f32 v233,v234,v235
v_mfma_f32_16x16x32_bf16 v[236:239],a[4:7],v[36:39],v[236:239]
ds_read_u16 v44,v44
v_lshlrev_b32_sdwa v46,1,v46 dst_sel:DWORD dst_unused:UNUSED_PAD src0_sel:DWORD src1_sel:WORD_1
v_mfma_f32_16x16x32_bf16 v[164:167],a[8:11],v[36:39],v[164:167]
ds_read_u16_d16_hi v46,v46
v_cvt_pk_bf16_f32 v234,v160,v161
v_lshlrev_b32_sdwa v45,1,v45 dst_sel:DWORD dst_unused:UNUSED_PAD src0_sel:DWORD src1_sel:WORD_1
v_mfma_f32_16x16x32_bf16 v[164:167],a[12:15],v[40:43],v[164:167]
ds_read_u16 v45,v45
v_cvt_pk_bf16_f32 v235,v162,v163
v_mfma_f32_16x16x32_bf16 v[244:247],a[16:19],v[40:43],v[244:247]
buffer_store_dwordx4 v[232:235],v7,s[40:43],s10 offen offset:0 sc1
v_lshlrev_b32_sdwa v47,1,v47 dst_sel:DWORD dst_unused:UNUSED_PAD src0_sel:DWORD src1_sel:WORD_1
v_mfma_f32_16x16x32_bf16 v[244:247],a[20:23],v[36:39],v[244:247]
ds_read_u16_d16_hi v47,v47
v_mul_f32_dpp v240,v14,v240 row_newbcast:8 row_mask:0xf bank_mask:0xf
v_lshlrev_b32_sdwa v48,1,v48 dst_sel:DWORD dst_unused:UNUSED_PAD src0_sel:DWORD src1_sel:WORD_1
v_mfma_f32_16x16x32_bf16 v[180:183],a[24:27],v[36:39],v[180:183]
ds_read_u16 v48,v48
v_mul_f32_dpp v241,v14,v241 row_newbcast:9 row_mask:0xf bank_mask:0xf
v_mfma_f32_16x16x32_bf16 v[180:183],a[28:31],v[40:43],v[180:183]
v_lshlrev_b32_sdwa v50,1,v50 dst_sel:DWORD dst_unused:UNUSED_PAD src0_sel:DWORD src1_sel:WORD_1
v_mul_f32_dpp v242,v14,v242 row_newbcast:10 row_mask:0xf bank_mask:0xf
v_mfma_f32_16x16x32_bf16 v[252:255],a[32:35],v[40:43],v[252:255]
ds_read_u16_d16_hi v50,v50
v_lshlrev_b32_sdwa v49,1,v49 dst_sel:DWORD dst_unused:UNUSED_PAD src0_sel:DWORD src1_sel:WORD_1
v_mfma_f32_16x16x32_bf16 v[252:255],a[36:39],v[36:39],v[252:255]
ds_read_u16 v49,v49
v_mul_f32_dpp v243,v14,v243 row_newbcast:11 row_mask:0xf bank_mask:0xf
v_lshlrev_b32_sdwa v51,1,v51 dst_sel:DWORD dst_unused:UNUSED_PAD src0_sel:DWORD src1_sel:WORD_1
v_mfma_f32_16x16x32_bf16 v[196:199],a[40:43],v[36:39],v[196:199]
ds_read_u16_d16_hi v51,v51
v_mul_f32_dpp v176,v14,v176 row_newbcast:12 row_mask:0xf bank_mask:0xf
v_mfma_f32_16x16x32_bf16 v[196:199],a[44:47],v[40:43],v[196:199]
v_mul_f32_dpp v177,v14,v177 row_newbcast:13 row_mask:0xf bank_mask:0xf
v_mfma_f32_16x16x32_bf16 v[36:39],a[144:147],a[56:59],v[96:99]
v_lshlrev_b32_sdwa v60,1,v60 dst_sel:DWORD dst_unused:UNUSED_PAD src0_sel:DWORD src1_sel:WORD_1
v_mul_f32_dpp v178,v14,v178 row_newbcast:14 row_mask:0xf bank_mask:0xf
v_mfma_f32_16x16x32_bf16 v[36:39],a[148:151],a[72:75],v[36:39]
ds_read_u16 v60,v60
v_lshlrev_b32_sdwa v62,1,v62 dst_sel:DWORD dst_unused:UNUSED_PAD src0_sel:DWORD src1_sel:WORD_1
v_mfma_f32_16x16x32_bf16 v[36:39],a[152:155],a[88:91],v[36:39]
s_waitcnt lgkmcnt(5)
ds_read_u16_d16_hi v62,v62
v_lshl_add_u64 v[28:29],v[28:29],0,v[30:31]
v_mul_f32_dpp v179,v14,v179 row_newbcast:15 row_mask:0xf bank_mask:0xf
v_mfma_f32_16x16x32_bf16 v[40:43],a[156:159],a[88:91],v[100:103]
v_lshl_add_u64 v[30:31],v[24:25],0,v[26:27]
v_lshlrev_b32_sdwa v61,1,v61 dst_sel:DWORD dst_unused:UNUSED_PAD src0_sel:DWORD src1_sel:WORD_1
v_cvt_pk_bf16_f32 v240,v240,v241
v_mfma_f32_16x16x32_bf16 v[40:43],a[160:163],a[72:75],v[40:43]
ds_read_u16 v61,v61
v_lshl_add_u64 v[24:25],v[44:45],0,v[46:47]
v_mfma_f32_16x16x32_bf16 v[40:43],a[164:167],a[56:59],v[40:43]
v_lshlrev_b32_sdwa v63,1,v63 dst_sel:DWORD dst_unused:UNUSED_PAD src0_sel:DWORD src1_sel:WORD_1
v_cvt_pk_bf16_f32 v241,v242,v243
v_mfma_f32_16x16x32_bf16 v[44:47],a[168:171],a[56:59],v[16:19]
ds_read_u16_d16_hi v63,v63
v_lshlrev_b32_sdwa v32,1,v32 dst_sel:DWORD dst_unused:UNUSED_PAD src0_sel:DWORD src1_sel:WORD_1
v_cvt_pk_bf16_f32 v242,v176,v177
v_mfma_f32_16x16x32_bf16 v[44:47],a[172:175],a[72:75],v[44:47]
s_waitcnt lgkmcnt(4)
ds_read_u16 v32,v32
v_lshl_add_u64 v[26:27],v[48:49],0,v[50:51]
v_cvt_pk_bf16_f32 v243,v178,v179
v_lshlrev_b32_sdwa v34,1,v34 dst_sel:DWORD dst_unused:UNUSED_PAD src0_sel:DWORD src1_sel:WORD_1
v_mfma_f32_16x16x32_bf16 v[44:47],a[176:179],a[88:91],v[44:47]
buffer_store_dwordx4 v[240:243],v7,s[40:43],s10 offen offset:64 sc1
v_mul_f32_dpp v248,v15,v248 row_newbcast:0 row_mask:0xf bank_mask:0xf
v_mfma_f32_16x16x32_bf16 v[48:51],a[180:183],a[88:91],v[20:23]
ds_read_u16_d16_hi v34,v34
v_lshlrev_b32_sdwa v33,1,v33 dst_sel:DWORD dst_unused:UNUSED_PAD src0_sel:DWORD src1_sel:WORD_1
v_mfma_f32_16x16x32_bf16 v[48:51],a[184:187],a[72:75],v[48:51]
ds_read_u16 v33,v33
v_mul_f32_dpp v249,v15,v249 row_newbcast:1 row_mask:0xf bank_mask:0xf
v_lshlrev_b32_sdwa v35,1,v35 dst_sel:DWORD dst_unused:UNUSED_PAD src0_sel:DWORD src1_sel:WORD_1
v_mfma_f32_16x16x32_bf16 v[48:51],a[188:191],a[56:59],v[48:51]
ds_read_u16_d16_hi v35,v35
v_mul_f32_dpp v250,v15,v250 row_newbcast:2 row_mask:0xf bank_mask:0xf
v_mfma_f32_16x16x32_bf16 v[236:239],a[212:215],v[28:31],v[236:239]
v_lshlrev_b32_sdwa v56,1,v56 dst_sel:DWORD dst_unused:UNUSED_PAD src0_sel:DWORD src1_sel:WORD_1
v_mul_f32_dpp v251,v15,v251 row_newbcast:3 row_mask:0xf bank_mask:0xf
v_mfma_f32_16x16x32_bf16 v[236:239],a[208:211],v[24:27],v[236:239]
ds_read_u16 v56,v56
v_lshlrev_b32_sdwa v58,1,v58 dst_sel:DWORD dst_unused:UNUSED_PAD src0_sel:DWORD src1_sel:WORD_1
v_mfma_f32_16x16x32_bf16 v[164:167],a[204:207],v[24:27],v[164:167]
ds_read_u16_d16_hi v58,v58
v_mul_f32_dpp v192,v15,v192 row_newbcast:4 row_mask:0xf bank_mask:0xf
v_lshlrev_b32_sdwa v57,1,v57 dst_sel:DWORD dst_unused:UNUSED_PAD src0_sel:DWORD src1_sel:WORD_1
v_mfma_f32_16x16x32_bf16 v[164:167],a[200:203],v[28:31],v[164:167]
ds_read_u16 v57,v57
v_mul_f32_dpp v193,v15,v193 row_newbcast:5 row_mask:0xf bank_mask:0xf
v_mfma_f32_16x16x32_bf16 v[244:247],a[196:199],v[28:31],v[244:247]
v_lshlrev_b32_sdwa v59,1,v59 dst_sel:DWORD dst_unused:UNUSED_PAD src0_sel:DWORD src1_sel:WORD_1
v_mul_f32_dpp v194,v15,v194 row_newbcast:6 row_mask:0xf bank_mask:0xf
v_mfma_f32_16x16x32_bf16 v[244:247],a[192:195],v[24:27],v[244:247]
ds_read_u16_d16_hi v59,v59
v_lshlrev_b32_sdwa v52,1,v52 dst_sel:DWORD dst_unused:UNUSED_PAD src0_sel:DWORD src1_sel:WORD_1
v_mfma_f32_16x16x32_bf16 v[180:183],a[216:219],v[24:27],v[180:183]
ds_read_u16 v52,v52
v_mul_f32_dpp v195,v15,v195 row_newbcast:7 row_mask:0xf bank_mask:0xf
v_mfma_f32_16x16x32_bf16 v[180:183],a[220:223],v[28:31],v[180:183]
v_lshlrev_b32_sdwa v54,1,v54 dst_sel:DWORD dst_unused:UNUSED_PAD src0_sel:DWORD src1_sel:WORD_1
v_cvt_pk_bf16_f32 v248,v248,v249
v_mfma_f32_16x16x32_bf16 v[252:255],a[232:235],v[28:31],v[252:255]
ds_read_u16_d16_hi v54,v54
v_lshlrev_b32_sdwa v53,1,v53 dst_sel:DWORD dst_unused:UNUSED_PAD src0_sel:DWORD src1_sel:WORD_1
v_mfma_f32_16x16x32_bf16 v[252:255],a[244:247],v[24:27],v[252:255]
ds_read_u16 v53,v53
v_cvt_pk_bf16_f32 v249,v250,v251
v_lshlrev_b32_sdwa v55,1,v55 dst_sel:DWORD dst_unused:UNUSED_PAD src0_sel:DWORD src1_sel:WORD_1
v_mfma_f32_16x16x32_bf16 v[196:199],a[240:243],v[24:27],v[196:199]
ds_read_u16_d16_hi v55,v55
v_cvt_pk_bf16_f32 v250,v192,v193
v_mfma_f32_16x16x32_bf16 v[196:199],a[228:231],v[28:31],v[196:199]
v_cvt_pk_bf16_f32 v251,v194,v195
v_mfma_f32_16x16x32_bf16 v[24:27],a[96:99],a[60:63],v[64:67]
buffer_store_dwordx4 v[248:251],v7,s[40:43],s10 offen offset:128 sc1
v_lshlrev_b32_sdwa v36,1,v36 dst_sel:DWORD dst_unused:UNUSED_PAD src0_sel:DWORD src1_sel:WORD_1
v_mfma_f32_16x16x32_bf16 v[24:27],a[100:103],a[76:79],v[24:27]
ds_read_u16 v36,v36
v_mul_f32_dpp v236,v14,v236 row_newbcast:0 row_mask:0xf bank_mask:0xf
v_mfma_f32_16x16x32_bf16 v[24:27],a[104:107],a[92:95],v[24:27]
s_waitcnt lgkmcnt(5)
v_lshl_add_u64 v[60:61],v[60:61],0,v[62:63]
v_lshlrev_b32_sdwa v38,1,v38 dst_sel:DWORD dst_unused:UNUSED_PAD src0_sel:DWORD src1_sel:WORD_1
v_mul_f32_dpp v237,v14,v237 row_newbcast:1 row_mask:0xf bank_mask:0xf
v_mfma_f32_16x16x32_bf16 v[28:31],a[108:111],a[92:95],v[68:71]
ds_read_u16_d16_hi v38,v38
v_lshl_add_u64 v[62:63],v[32:33],0,v[34:35]
v_lshlrev_b32_sdwa v37,1,v37 dst_sel:DWORD dst_unused:UNUSED_PAD src0_sel:DWORD src1_sel:WORD_1
v_mul_f32_dpp v238,v14,v238 row_newbcast:2 row_mask:0xf bank_mask:0xf
v_mfma_f32_16x16x32_bf16 v[28:31],a[112:115],a[76:79],v[28:31]
ds_read_u16 v37,v37
v_lshl_add_u64 v[32:33],v[56:57],0,v[58:59]
v_mfma_f32_16x16x32_bf16 v[28:31],a[116:119],a[60:63],v[28:31]
v_lshlrev_b32_sdwa v39,1,v39 dst_sel:DWORD dst_unused:UNUSED_PAD src0_sel:DWORD src1_sel:WORD_1
v_mul_f32_dpp v239,v14,v239 row_newbcast:3 row_mask:0xf bank_mask:0xf
v_mfma_f32_16x16x32_bf16 v[56:59],a[120:123],a[60:63],v[84:87]
ds_read_u16_d16_hi v39,v39
v_lshlrev_b32_sdwa v40,1,v40 dst_sel:DWORD dst_unused:UNUSED_PAD src0_sel:DWORD src1_sel:WORD_1
v_mul_f32_dpp v164,v14,v164 row_newbcast:4 row_mask:0xf bank_mask:0xf
v_mfma_f32_16x16x32_bf16 v[56:59],a[124:127],a[76:79],v[56:59]
s_waitcnt lgkmcnt(4)
ds_read_u16 v40,v40
v_lshl_add_u64 v[34:35],v[52:53],0,v[54:55]
v_mul_f32_dpp v165,v14,v165 row_newbcast:5 row_mask:0xf bank_mask:0xf
v_lshlrev_b32_sdwa v42,1,v42 dst_sel:DWORD dst_unused:UNUSED_PAD src0_sel:DWORD src1_sel:WORD_1
v_mfma_f32_16x16x32_bf16 v[56:59],a[128:131],a[92:95],v[56:59]
ds_read_u16_d16_hi v42,v42
v_mul_f32_dpp v166,v14,v166 row_newbcast:6 row_mask:0xf bank_mask:0xf
v_mfma_f32_16x16x32_bf16 v[52:55],a[132:135],a[92:95],v[80:83]
v_lshlrev_b32_sdwa v41,1,v41 dst_sel:DWORD dst_unused:UNUSED_PAD src0_sel:DWORD src1_sel:WORD_1
v_mul_f32_dpp v167,v14,v167 row_newbcast:7 row_mask:0xf bank_mask:0xf
v_mfma_f32_16x16x32_bf16 v[52:55],a[136:139],a[76:79],v[52:55]
ds_read_u16 v41,v41
v_lshlrev_b32_sdwa v43,1,v43 dst_sel:DWORD dst_unused:UNUSED_PAD src0_sel:DWORD src1_sel:WORD_1
v_mfma_f32_16x16x32_bf16 v[52:55],a[140:143],a[60:63],v[52:55]
ds_read_u16_d16_hi v43,v43
v_cvt_pk_bf16_f32 v236,v236,v237
v_mfma_f32_16x16x32_bf16 v[208:211],a[0:3],v[60:63],v[208:211]
v_lshlrev_b32_sdwa v48,1,v48 dst_sel:DWORD dst_unused:UNUSED_PAD src0_sel:DWORD src1_sel:WORD_1
v_cvt_pk_bf16_f32 v237,v238,v239
v_mfma_f32_16x16x32_bf16 v[208:211],a[4:7],v[32:35],v[208:211]
ds_read_u16 v48,v48
v_lshlrev_b32_sdwa v50,1,v50 dst_sel:DWORD dst_unused:UNUSED_PAD src0_sel:DWORD src1_sel:WORD_1
v_mfma_f32_16x16x32_bf16 v[168:171],a[8:11],v[32:35],v[168:171]
ds_read_u16_d16_hi v50,v50
v_cvt_pk_bf16_f32 v238,v164,v165
v_lshlrev_b32_sdwa v49,1,v49 dst_sel:DWORD dst_unused:UNUSED_PAD src0_sel:DWORD src1_sel:WORD_1
v_mfma_f32_16x16x32_bf16 v[168:171],a[12:15],v[60:63],v[168:171]
ds_read_u16 v49,v49
v_cvt_pk_bf16_f32 v239,v166,v167
v_mfma_f32_16x16x32_bf16 v[216:219],a[16:19],v[60:63],v[216:219]
buffer_store_dwordx4 v[236:239],v7,s[40:43],s10 offen offset:192 sc1
v_lshlrev_b32_sdwa v51,1,v51 dst_sel:DWORD dst_unused:UNUSED_PAD src0_sel:DWORD src1_sel:WORD_1
v_mfma_f32_16x16x32_bf16 v[216:219],a[20:23],v[32:35],v[216:219]
ds_read_u16_d16_hi v51,v51
v_mul_f32_dpp v244,v14,v244 row_newbcast:8 row_mask:0xf bank_mask:0xf
v_lshlrev_b32_sdwa v44,1,v44 dst_sel:DWORD dst_unused:UNUSED_PAD src0_sel:DWORD src1_sel:WORD_1
v_mfma_f32_16x16x32_bf16 v[184:187],a[24:27],v[32:35],v[184:187]
ds_read_u16 v44,v44
v_mul_f32_dpp v245,v14,v245 row_newbcast:9 row_mask:0xf bank_mask:0xf
v_mfma_f32_16x16x32_bf16 v[184:187],a[28:31],v[60:63],v[184:187]
v_lshlrev_b32_sdwa v46,1,v46 dst_sel:DWORD dst_unused:UNUSED_PAD src0_sel:DWORD src1_sel:WORD_1
v_mul_f32_dpp v246,v14,v246 row_newbcast:10 row_mask:0xf bank_mask:0xf
v_mfma_f32_16x16x32_bf16 v[224:227],a[32:35],v[60:63],v[224:227]
ds_read_u16_d16_hi v46,v46
v_lshlrev_b32_sdwa v45,1,v45 dst_sel:DWORD dst_unused:UNUSED_PAD src0_sel:DWORD src1_sel:WORD_1
v_mfma_f32_16x16x32_bf16 v[224:227],a[36:39],v[32:35],v[224:227]
ds_read_u16 v45,v45
v_mul_f32_dpp v247,v14,v247 row_newbcast:11 row_mask:0xf bank_mask:0xf
v_lshlrev_b32_sdwa v47,1,v47 dst_sel:DWORD dst_unused:UNUSED_PAD src0_sel:DWORD src1_sel:WORD_1
v_mfma_f32_16x16x32_bf16 v[200:203],a[40:43],v[32:35],v[200:203]
ds_read_u16_d16_hi v47,v47
v_mul_f32_dpp v180,v14,v180 row_newbcast:12 row_mask:0xf bank_mask:0xf
v_mfma_f32_16x16x32_bf16 v[200:203],a[44:47],v[60:63],v[200:203]
v_mul_f32_dpp v181,v14,v181 row_newbcast:13 row_mask:0xf bank_mask:0xf
v_mov_b32 v10,0
v_mfma_f32_16x16x32_bf16 v[32:35],a[144:147],a[60:63],v[96:99]
ds_read_b128 v[72:75],v10 offset:11648
v_lshlrev_b32_sdwa v24,1,v24 dst_sel:DWORD dst_unused:UNUSED_PAD src0_sel:DWORD src1_sel:WORD_1
v_mfma_f32_16x16x32_bf16 v[32:35],a[148:151],a[76:79],v[32:35]
ds_read_u16 v24,v24
v_mul_f32_dpp v182,v14,v182 row_newbcast:14 row_mask:0xf bank_mask:0xf
v_mfma_f32_16x16x32_bf16 v[32:35],a[152:155],a[92:95],v[32:35]
s_waitcnt lgkmcnt(6)
v_lshl_add_u64 v[36:37],v[36:37],0,v[38:39]
v_lshlrev_b32_sdwa v26,1,v26 dst_sel:DWORD dst_unused:UNUSED_PAD src0_sel:DWORD src1_sel:WORD_1
v_mul_f32_dpp v183,v14,v183 row_newbcast:15 row_mask:0xf bank_mask:0xf
v_mfma_f32_16x16x32_bf16 v[60:63],a[156:159],a[92:95],v[100:103]
ds_read_u16_d16_hi v26,v26
v_lshl_add_u64 v[38:39],v[40:41],0,v[42:43]
v_lshlrev_b32_sdwa v25,1,v25 dst_sel:DWORD dst_unused:UNUSED_PAD src0_sel:DWORD src1_sel:WORD_1
v_cvt_pk_bf16_f32 v244,v244,v245
v_mfma_f32_16x16x32_bf16 v[60:63],a[160:163],a[76:79],v[60:63]
ds_read_u16 v25,v25
v_lshl_add_u64 v[40:41],v[48:49],0,v[50:51]
v_mfma_f32_16x16x32_bf16 v[60:63],a[164:167],a[60:63],v[60:63]
v_lshlrev_b32_sdwa v27,1,v27 dst_sel:DWORD dst_unused:UNUSED_PAD src0_sel:DWORD src1_sel:WORD_1
v_cvt_pk_bf16_f32 v245,v246,v247
v_mfma_f32_16x16x32_bf16 v[48:51],a[168:171],a[60:63],v[16:19]
ds_read_u16_d16_hi v27,v27
v_lshlrev_b32_sdwa v28,1,v28 dst_sel:DWORD dst_unused:UNUSED_PAD src0_sel:DWORD src1_sel:WORD_1
v_cvt_pk_bf16_f32 v246,v180,v181
v_mfma_f32_16x16x32_bf16 v[48:51],a[172:175],a[76:79],v[48:51]
s_waitcnt lgkmcnt(5)
ds_read_u16 v28,v28
v_lshl_add_u64 v[42:43],v[44:45],0,v[46:47]
v_cvt_pk_bf16_f32 v247,v182,v183
v_lshlrev_b32_sdwa v30,1,v30 dst_sel:DWORD dst_unused:UNUSED_PAD src0_sel:DWORD src1_sel:WORD_1
v_mfma_f32_16x16x32_bf16 v[48:51],a[176:179],a[92:95],v[48:51]
buffer_store_dwordx4 v[244:247],v7,s[40:43],s10 offen offset:256 sc1
v_mul_f32_dpp v252,v15,v252 row_newbcast:0 row_mask:0xf bank_mask:0xf
v_mfma_f32_16x16x32_bf16 v[44:47],a[180:183],a[92:95],v[20:23]
ds_read_u16_d16_hi v30,v30
v_lshlrev_b32_sdwa v29,1,v29 dst_sel:DWORD dst_unused:UNUSED_PAD src0_sel:DWORD src1_sel:WORD_1
v_mfma_f32_16x16x32_bf16 v[44:47],a[184:187],a[76:79],v[44:47]
ds_read_u16 v29,v29
v_mul_f32_dpp v253,v15,v253 row_newbcast:1 row_mask:0xf bank_mask:0xf
v_lshlrev_b32_sdwa v31,1,v31 dst_sel:DWORD dst_unused:UNUSED_PAD src0_sel:DWORD src1_sel:WORD_1
v_mfma_f32_16x16x32_bf16 v[44:47],a[188:191],a[60:63],v[44:47]
ds_read_u16_d16_hi v31,v31
v_mul_f32_dpp v254,v15,v254 row_newbcast:2 row_mask:0xf bank_mask:0xf
v_mfma_f32_16x16x32_bf16 v[208:211],a[212:215],v[36:39],v[208:211]
v_lshlrev_b32_sdwa v52,1,v52 dst_sel:DWORD dst_unused:UNUSED_PAD src0_sel:DWORD src1_sel:WORD_1
v_mul_f32_dpp v255,v15,v255 row_newbcast:3 row_mask:0xf bank_mask:0xf
v_mfma_f32_16x16x32_bf16 v[208:211],a[208:211],v[40:43],v[208:211]
ds_read_u16 v52,v52
v_lshlrev_b32_sdwa v54,1,v54 dst_sel:DWORD dst_unused:UNUSED_PAD src0_sel:DWORD src1_sel:WORD_1
v_mfma_f32_16x16x32_bf16 v[168:171],a[204:207],v[40:43],v[168:171]
ds_read_u16_d16_hi v54,v54
v_mul_f32_dpp v196,v15,v196 row_newbcast:4 row_mask:0xf bank_mask:0xf
v_lshlrev_b32_sdwa v53,1,v53 dst_sel:DWORD dst_unused:UNUSED_PAD src0_sel:DWORD src1_sel:WORD_1
v_mfma_f32_16x16x32_bf16 v[168:171],a[200:203],v[36:39],v[168:171]
ds_read_u16 v53,v53
v_mul_f32_dpp v197,v15,v197 row_newbcast:5 row_mask:0xf bank_mask:0xf
v_mfma_f32_16x16x32_bf16 v[216:219],a[196:199],v[36:39],v[216:219]
v_lshlrev_b32_sdwa v55,1,v55 dst_sel:DWORD dst_unused:UNUSED_PAD src0_sel:DWORD src1_sel:WORD_1
v_mul_f32_dpp v198,v15,v198 row_newbcast:6 row_mask:0xf bank_mask:0xf
v_mfma_f32_16x16x32_bf16 v[216:219],a[192:195],v[40:43],v[216:219]
ds_read_u16_d16_hi v55,v55
v_lshlrev_b32_sdwa v56,1,v56 dst_sel:DWORD dst_unused:UNUSED_PAD src0_sel:DWORD src1_sel:WORD_1
v_mfma_f32_16x16x32_bf16 v[184:187],a[216:219],v[40:43],v[184:187]
ds_read_u16 v56,v56
v_mul_f32_dpp v199,v15,v199 row_newbcast:7 row_mask:0xf bank_mask:0xf
v_mfma_f32_16x16x32_bf16 v[184:187],a[220:223],v[36:39],v[184:187]
v_lshlrev_b32_sdwa v58,1,v58 dst_sel:DWORD dst_unused:UNUSED_PAD src0_sel:DWORD src1_sel:WORD_1
v_cvt_pk_bf16_f32 v252,v252,v253
v_mfma_f32_16x16x32_bf16 v[224:227],a[232:235],v[36:39],v[224:227]
ds_read_u16_d16_hi v58,v58
v_lshlrev_b32_sdwa v57,1,v57 dst_sel:DWORD dst_unused:UNUSED_PAD src0_sel:DWORD src1_sel:WORD_1
v_mfma_f32_16x16x32_bf16 v[224:227],a[244:247],v[40:43],v[224:227]
ds_read_u16 v57,v57
v_cvt_pk_bf16_f32 v253,v254,v255
v_lshlrev_b32_sdwa v59,1,v59 dst_sel:DWORD dst_unused:UNUSED_PAD src0_sel:DWORD src1_sel:WORD_1
v_mfma_f32_16x16x32_bf16 v[200:203],a[240:243],v[40:43],v[200:203]
ds_read_u16_d16_hi v59,v59
v_cvt_pk_bf16_f32 v254,v196,v197
v_mfma_f32_16x16x32_bf16 v[200:203],a[228:231],v[36:39],v[200:203]
v_cvt_pk_bf16_f32 v255,v198,v199
s_waitcnt lgkmcnt(12)
v_lshl_add_u64 v[24:25],v[24:25],0,v[26:27]
s_waitcnt lgkmcnt(8)
v_lshl_add_u64 v[26:27],v[28:29],0,v[30:31]
s_waitcnt lgkmcnt(4)
v_lshl_add_u64 v[28:29],v[52:53],0,v[54:55]
s_waitcnt lgkmcnt(0)
v_lshl_add_u64 v[30:31],v[56:57],0,v[58:59]
v_mfma_f32_16x16x32_bf16 v[212:215],a[0:3],v[24:27],v[212:215]
TP11368_%=:
s_cbranch_execz TR1_%=
buffer_store_dwordx4 v[252:255],v7,s[40:43],s10 offen offset:320 sc1
v_mul_f32_dpp v208,v14,v208 row_newbcast:0 row_mask:0xf bank_mask:0xf
s_add_u32 s11,s61,4816896
v_mul_f32_dpp v209,v14,v209 row_newbcast:1 row_mask:0xf bank_mask:0xf
v_mul_f32_dpp v210,v14,v210 row_newbcast:2 row_mask:0xf bank_mask:0xf
v_lshlrev_b32_sdwa v32,1,v32 dst_sel:DWORD dst_unused:UNUSED_PAD src0_sel:DWORD src1_sel:WORD_1
v_mfma_f32_16x16x32_bf16 v[212:215],a[4:7],v[28:31],v[212:215]
ds_read_u16 v32,v32
v_mul_f32_dpp v211,v14,v211 row_newbcast:3 row_mask:0xf bank_mask:0xf
v_mul_f32_dpp v168,v14,v168 row_newbcast:4 row_mask:0xf bank_mask:0xf
v_lshlrev_b32_sdwa v34,1,v34 dst_sel:DWORD dst_unused:UNUSED_PAD src0_sel:DWORD src1_sel:WORD_1
v_mul_f32_dpp v169,v14,v169 row_newbcast:5 row_mask:0xf bank_mask:0xf
v_mfma_f32_16x16x32_bf16 v[172:175],a[8:11],v[28:31],v[172:175]
ds_read_u16_d16_hi v34,v34
v_mul_f32_dpp v170,v14,v170 row_newbcast:6 row_mask:0xf bank_mask:0xf
v_mul_f32_dpp v171,v14,v171 row_newbcast:7 row_mask:0xf bank_mask:0xf
v_lshlrev_b32_sdwa v33,1,v33 dst_sel:DWORD dst_unused:UNUSED_PAD src0_sel:DWORD src1_sel:WORD_1
v_cvt_pk_bf16_f32 v208,v208,v209
v_mfma_f32_16x16x32_bf16 v[172:175],a[12:15],v[24:27],v[172:175]
ds_read_u16 v33,v33
v_cvt_pk_bf16_f32 v209,v210,v211
v_lshlrev_b32_sdwa v35,1,v35 dst_sel:DWORD dst_unused:UNUSED_PAD src0_sel:DWORD src1_sel:WORD_1
v_cvt_pk_bf16_f32 v210,v168,v169
v_lshlrev_b32_sdwa v60,1,v60 dst_sel:DWORD dst_unused:UNUSED_PAD src0_sel:DWORD src1_sel:WORD_1
v_cvt_pk_bf16_f32 v211,v170,v171
v_mfma_f32_16x16x32_bf16 v[220:223],a[16:19],v[24:27],v[220:223]
buffer_store_dwordx4 v[208:211],v7,s[40:43],s11 offen offset:0 sc1
v_mul_f32_dpp v216,v14,v216 row_newbcast:8 row_mask:0xf bank_mask:0xf
v_mul_f32_dpp v217,v14,v217 row_newbcast:9 row_mask:0xf bank_mask:0xf
v_lshlrev_b32_sdwa v62,1,v62 dst_sel:DWORD dst_unused:UNUSED_PAD src0_sel:DWORD src1_sel:WORD_1
v_mul_f32_dpp v218,v14,v218 row_newbcast:10 row_mask:0xf bank_mask:0xf
v_mfma_f32_16x16x32_bf16 v[220:223],a[20:23],v[28:31],v[220:223]
ds_read_u16_d16_hi v35,v35
v_mul_f32_dpp v219,v14,v219 row_newbcast:11 row_mask:0xf bank_mask:0xf
v_lshlrev_b32_sdwa v61,1,v61 dst_sel:DWORD dst_unused:UNUSED_PAD src0_sel:DWORD src1_sel:WORD_1
v_mul_f32_dpp v184,v14,v184 row_newbcast:12 row_mask:0xf bank_mask:0xf
v_lshlrev_b32_sdwa v63,1,v63 dst_sel:DWORD dst_unused:UNUSED_PAD src0_sel:DWORD src1_sel:WORD_1
v_mul_f32_dpp v185,v14,v185 row_newbcast:13 row_mask:0xf bank_mask:0xf
v_mfma_f32_16x16x32_bf16 v[188:191],a[24:27],v[28:31],v[188:191]
ds_read_u16 v60,v60
v_mul_f32_dpp v186,v14,v186 row_newbcast:14 row_mask:0xf bank_mask:0xf
v_mul_f32_dpp v187,v14,v187 row_newbcast:15 row_mask:0xf bank_mask:0xf
v_lshlrev_b32_sdwa v44,1,v44 dst_sel:DWORD dst_unused:UNUSED_PAD src0_sel:DWORD src1_sel:WORD_1
v_cvt_pk_bf16_f32 v216,v216,v217
v_mfma_f32_16x16x32_bf16 v[188:191],a[28:31],v[24:27],v[188:191]
ds_read_u16_d16_hi v62,v62
ds_read_u16 v61,v61
v_cvt_pk_bf16_f32 v217,v218,v219
v_cvt_pk_bf16_f32 v218,v184,v185
v_lshlrev_b32_sdwa v46,1,v46 dst_sel:DWORD dst_unused:UNUSED_PAD src0_sel:DWORD src1_sel:WORD_1
v_cvt_pk_bf16_f32 v219,v186,v187
v_lshlrev_b32_sdwa v45,1,v45 dst_sel:DWORD dst_unused:UNUSED_PAD src0_sel:DWORD src1_sel:WORD_1
v_mfma_f32_16x16x32_bf16 v[228:231],a[32:35],v[24:27],v[228:231]
buffer_store_dwordx4 v[216:219],v7,s[40:43],s11 offen offset:64 sc1
ds_read_u16_d16_hi v63,v63
v_mul_f32_dpp v224,v15,v224 row_newbcast:0 row_mask:0xf bank_mask:0xf
v_mul_f32_dpp v225,v15,v225 row_newbcast:1 row_mask:0xf bank_mask:0xf
v_lshlrev_b32_sdwa v47,1,v47 dst_sel:DWORD dst_unused:UNUSED_PAD src0_sel:DWORD src1_sel:WORD_1
v_mul_f32_dpp v226,v15,v226 row_newbcast:2 row_mask:0xf bank_mask:0xf
v_mfma_f32_16x16x32_bf16 v[228:231],a[36:39],v[28:31],v[228:231]
ds_read_u16 v44,v44
ds_read_u16_d16_hi v46,v46
v_mul_f32_dpp v227,v15,v227 row_newbcast:3 row_mask:0xf bank_mask:0xf
v_mul_f32_dpp v200,v15,v200 row_newbcast:4 row_mask:0xf bank_mask:0xf
v_lshlrev_b32_sdwa v48,1,v48 dst_sel:DWORD dst_unused:UNUSED_PAD src0_sel:DWORD src1_sel:WORD_1
v_mul_f32_dpp v201,v15,v201 row_newbcast:5 row_mask:0xf bank_mask:0xf
v_mfma_f32_16x16x32_bf16 v[204:207],a[40:43],v[28:31],v[204:207]
ds_read_u16 v45,v45
ds_read_u16_d16_hi v47,v47
v_mul_f32_dpp v202,v15,v202 row_newbcast:6 row_mask:0xf bank_mask:0xf
v_lshlrev_b32_sdwa v50,1,v50 dst_sel:DWORD dst_unused:UNUSED_PAD src0_sel:DWORD src1_sel:WORD_1
v_mul_f32_dpp v203,v15,v203 row_newbcast:7 row_mask:0xf bank_mask:0xf
v_lshlrev_b32_sdwa v49,1,v49 dst_sel:DWORD dst_unused:UNUSED_PAD src0_sel:DWORD src1_sel:WORD_1
v_cvt_pk_bf16_f32 v224,v224,v225
v_mfma_f32_16x16x32_bf16 v[204:207],a[44:47],v[24:27],v[204:207]
ds_read_u16 v48,v48
ds_read_u16_d16_hi v50,v50
v_cvt_pk_bf16_f32 v225,v226,v227
v_cvt_pk_bf16_f32 v226,v200,v201
v_cvt_pk_bf16_f32 v227,v202,v203
v_lshlrev_b32_sdwa v51,1,v51 dst_sel:DWORD dst_unused:UNUSED_PAD src0_sel:DWORD src1_sel:WORD_1
ds_read_u16 v49,v49
ds_read_u16_d16_hi v51,v51
s_waitcnt lgkmcnt(12)
v_lshl_add_u64 v[32:33],v[32:33],0,v[34:35]
s_waitcnt lgkmcnt(8)
v_lshl_add_u64 v[34:35],v[60:61],0,v[62:63]
s_waitcnt lgkmcnt(4)
v_lshl_add_u64 v[60:61],v[44:45],0,v[46:47]
s_waitcnt lgkmcnt(0)
v_lshl_add_u64 v[62:63],v[48:49],0,v[50:51]
buffer_store_dwordx4 v[224:227],v7,s[40:43],s11 offen offset:128 sc1
v_mfma_f32_16x16x32_bf16 v[212:215],a[212:215],v[32:35],v[212:215]
v_mfma_f32_16x16x32_bf16 v[212:215],a[208:211],v[60:63],v[212:215]
v_mfma_f32_16x16x32_bf16 v[172:175],a[204:207],v[60:63],v[172:175]
v_mfma_f32_16x16x32_bf16 v[172:175],a[200:203],v[32:35],v[172:175]
v_mfma_f32_16x16x32_bf16 v[220:223],a[196:199],v[32:35],v[220:223]
v_mfma_f32_16x16x32_bf16 v[220:223],a[192:195],v[60:63],v[220:223]
v_mfma_f32_16x16x32_bf16 v[188:191],a[216:219],v[60:63],v[188:191]
s_nop 2
v_mul_f32_dpp v212,v14,v212 row_newbcast:0 row_mask:0xf bank_mask:0xf
v_mul_f32_dpp v213,v14,v213 row_newbcast:1 row_mask:0xf bank_mask:0xf
v_mul_f32_dpp v214,v14,v214 row_newbcast:2 row_mask:0xf bank_mask:0xf
v_mfma_f32_16x16x32_bf16 v[188:191],a[220:223],v[32:35],v[188:191]
v_mul_f32_dpp v215,v14,v215 row_newbcast:3 row_mask:0xf bank_mask:0xf
v_mul_f32_dpp v172,v14,v172 row_newbcast:4 row_mask:0xf bank_mask:0xf
v_mul_f32_dpp v173,v14,v173 row_newbcast:5 row_mask:0xf bank_mask:0xf
v_mfma_f32_16x16x32_bf16 v[228:231],a[232:235],v[32:35],v[228:231]
v_mul_f32_dpp v174,v14,v174 row_newbcast:6 row_mask:0xf bank_mask:0xf
v_mul_f32_dpp v175,v14,v175 row_newbcast:7 row_mask:0xf bank_mask:0xf
v_cvt_pk_bf16_f32 v212,v212,v213
v_mfma_f32_16x16x32_bf16 v[228:231],a[244:247],v[60:63],v[228:231]
v_cvt_pk_bf16_f32 v213,v214,v215
v_cvt_pk_bf16_f32 v214,v172,v173
v_cvt_pk_bf16_f32 v215,v174,v175
buffer_store_dwordx4 v[212:215],v7,s[40:43],s11 offen offset:192 sc1
v_mfma_f32_16x16x32_bf16 v[204:207],a[240:243],v[60:63],v[204:207]
v_mul_f32_dpp v220,v14,v220 row_newbcast:8 row_mask:0xf bank_mask:0xf
v_mul_f32_dpp v221,v14,v221 row_newbcast:9 row_mask:0xf bank_mask:0xf
v_mul_f32_dpp v222,v14,v222 row_newbcast:10 row_mask:0xf bank_mask:0xf
v_mfma_f32_16x16x32_bf16 v[204:207],a[228:231],v[32:35],v[204:207]
v_mul_f32_dpp v223,v14,v223 row_newbcast:11 row_mask:0xf bank_mask:0xf
v_mul_f32_dpp v188,v14,v188 row_newbcast:12 row_mask:0xf bank_mask:0xf
v_mul_f32_dpp v189,v14,v189 row_newbcast:13 row_mask:0xf bank_mask:0xf
v_mul_f32_dpp v190,v14,v190 row_newbcast:14 row_mask:0xf bank_mask:0xf
v_mul_f32_dpp v191,v14,v191 row_newbcast:15 row_mask:0xf bank_mask:0xf
v_cvt_pk_bf16_f32 v220,v220,v221
v_cvt_pk_bf16_f32 v221,v222,v223
v_cvt_pk_bf16_f32 v222,v188,v189
v_cvt_pk_bf16_f32 v223,v190,v191
buffer_store_dwordx4 v[220:223],v7,s[40:43],s11 offen offset:256 sc1
v_mul_f32_dpp v228,v15,v228 row_newbcast:0 row_mask:0xf bank_mask:0xf
v_mul_f32_dpp v229,v15,v229 row_newbcast:1 row_mask:0xf bank_mask:0xf
v_mul_f32_dpp v230,v15,v230 row_newbcast:2 row_mask:0xf bank_mask:0xf
v_mul_f32_dpp v231,v15,v231 row_newbcast:3 row_mask:0xf bank_mask:0xf
v_mul_f32_dpp v204,v15,v204 row_newbcast:4 row_mask:0xf bank_mask:0xf
v_mul_f32_dpp v205,v15,v205 row_newbcast:5 row_mask:0xf bank_mask:0xf
v_mul_f32_dpp v206,v15,v206 row_newbcast:6 row_mask:0xf bank_mask:0xf
v_mul_f32_dpp v207,v15,v207 row_newbcast:7 row_mask:0xf bank_mask:0xf
v_cvt_pk_bf16_f32 v228,v228,v229
v_cvt_pk_bf16_f32 v229,v230,v231
v_cvt_pk_bf16_f32 v230,v204,v205
v_cvt_pk_bf16_f32 v231,v206,v207
buffer_store_dwordx4 v[228:231],v7,s[40:43],s11 offen offset:320 sc1
v_or3_b32 v72,v72,v73,v74
v_or_b32 v72,v72,v75
s_nop 0
v_readfirstlane_b32 s64,v72
s_cmp_lg_u32 s64,0
s_cbranch_scc1 N_fallback_%=
s_endpgm
N_mainend_%=:
N_fallback_%=:
s_waitcnt vmcnt(0) lgkmcnt(0)
s_barrier
)ASM"
    :: "s"(blockIdx.x), "v"(tid), "s"(ka)
    : "v1", "v2", "v3", "v4", "v5", "v6", "v7", "v8", "v9", "v10", "v11", "v12", "v13", "v14", "v15", "v16", "v17", "v18", "v19", "v20", "v21", "v22", "v23", "v24", "v25", "v26", "v27", "v28", "v29", "v30", "v31", "v32", "v33", "v34", "v35", "v36", "v37", "v38", "v39", "v40", "v41", "v42", "v43", "v44", "v45", "v46", "v47", "v48", "v49", "v50", "v51", "v52", "v53", "v54", "v55", "v56", "v57", "v58", "v59", "v60", "v61", "v62", "v63", "v64", "v65", "v66", "v67", "v68", "v69", "v70", "v71", "v72", "v73", "v74", "v75", "v76", "v77", "v78", "v79", "v80", "v81", "v82", "v83", "v84", "v85", "v86", "v87", "v88", "v89", "v90", "v91", "v92", "v93", "v94", "v95", "v96", "v97", "v98", "v99", "v100", "v101", "v102", "v103", "v104", "v105", "v106", "v107", "v108", "v109", "v110", "v111", "v112", "v113", "v114", "v115", "v116", "v117", "v118", "v119", "v120", "v121", "v122", "v123", "v124", "v125", "v126", "v127", "v128", "v129", "v130", "v131", "v132", "v133", "v134", "v135", "v136", "v137", "v138", "v139", "v140", "v141", "v142", "v143", "v144", "v145", "v146", "v147", "v148", "v149", "v150", "v151", "v152", "v153", "v154", "v155", "v156", "v157", "v158", "v159", "v160", "v161", "v162", "v163", "v164", "v165", "v166", "v167", "v168", "v169", "v170", "v171", "v172", "v173", "v174", "v175", "v176", "v177", "v178", "v179", "v180", "v181", "v182", "v183", "v184", "v185", "v186", "v187", "v188", "v189", "v190", "v191", "v192", "v193", "v194", "v195", "v196", "v197", "v198", "v199", "v200", "v201", "v202", "v203", "v204", "v205", "v206", "v207", "v208", "v209", "v210", "v211", "v212", "v213", "v214", "v215", "v216", "v217", "v218", "v219", "v220", "v221", "v222", "v223", "v224", "v225", "v226", "v227", "v228", "v229", "v230", "v231", "v232", "v233", "v234", "v235", "v236", "v237", "v238", "v239", "v240", "v241", "v242", "v243", "v244", "v245", "v246", "v247", "v248", "v249", "v250", "v251", "v252", "v253", "v254", "v255", "a0", "a1", "a2", "a3", "a4", "a5", "a6", "a7", "a8", "a9", "a10", "a11", "a12", "a13", "a14", "a15", "a16", "a17", "a18", "a19", "a20", "a21", "a22", "a23", "a24", "a25", "a26", "a27", "a28", "a29", "a30", "a31", "a32", "a33", "a34", "a35", "a36", "a37", "a38", "a39", "a40", "a41", "a42", "a43", "a44", "a45", "a46", "a47", "a48", "a49", "a50", "a51", "a52", "a53", "a54", "a55", "a56", "a57", "a58", "a59", "a60", "a61", "a62", "a63", "a64", "a65", "a66", "a67", "a68", "a69", "a70", "a71", "a72", "a73", "a74", "a75", "a76", "a77", "a78", "a79", "a80", "a81", "a82", "a83", "a84", "a85", "a86", "a87", "a88", "a89", "a90", "a91", "a92", "a93", "a94", "a95", "a96", "a97", "a98", "a99", "a100", "a101", "a102", "a103", "a104", "a105", "a106", "a107", "a108", "a109", "a110", "a111", "a112", "a113", "a114", "a115", "a116", "a117", "a118", "a119", "a120", "a121", "a122", "a123", "a124", "a125", "a126", "a127", "a128", "a129", "a130", "a131", "a132", "a133", "a134", "a135", "a136", "a137", "a138", "a139", "a140", "a141", "a142", "a143", "a144", "a145", "a146", "a147", "a148", "a149", "a150", "a151", "a152", "a153", "a154", "a155", "a156", "a157", "a158", "a159", "a160", "a161", "a162", "a163", "a164", "a165", "a166", "a167", "a168", "a169", "a170", "a171", "a172", "a173", "a174", "a175", "a176", "a177", "a178", "a179", "a180", "a181", "a182", "a183", "a184", "a185", "a186", "a187", "a188", "a189", "a190", "a191", "a192", "a193", "a194", "a195", "a196", "a197", "a198", "a199", "a200", "a201", "a202", "a203", "a204", "a205", "a206", "a207", "a208", "a209", "a210", "a211", "a212", "a213", "a214", "a215", "a216", "a217", "a218", "a219", "a220", "a221", "a222", "a223", "a224", "a225", "a226", "a227", "a228", "a229", "a230", "a231", "a232", "a233", "a234", "a235", "a236", "a237", "a238", "a239", "a240", "a241", "a242", "a243", "a244", "a245", "a246", "a247", "a248", "a249", "a250", "a251", "a252", "a253", "a254", "a255", "s4", "s5", "s6", "s7", "s8", "s9", "s10", "s11", "s12", "s13", "s14", "s15", "s16", "s17", "s18", "s19", "s20", "s21", "s22", "s23", "s24", "s25", "s26", "s27", "s28", "s29", "s30", "s31", "s32", "s33", "s34", "s35", "s36", "s37", "s38", "s39", "s40", "s41", "s42", "s43", "s44", "s45", "s46", "s47", "s48", "s49", "s50", "s51", "s52", "s53", "s54", "s55", "s56", "s57", "s58", "s59", "s60", "s61", "s62", "s63", "s64", "s65", "s66", "s67", "s68", "s69", "s70", "s71", "s72", "s73", "s74", "s75", "s76", "s77", "s78", "s79", "s80", "s81", "s82", "s83", "s84", "s85", "s86", "s87", "s88", "s89", "s90", "s91", "s92", "s93", "s94", "s95", "s96", "s97", "s98", "s99", "vcc", "scc", "memory");
    asm volatile("" : "+s"(ka));
    typedef const unsigned short* __attribute__((address_space(1))) gp;
    unsigned long long const __attribute__((address_space(4)))* kp = (unsigned long long const __attribute__((address_space(4)))*)ka;
    const unsigned short* x = (const unsigned short*)kp[0]; const unsigned short* dw = (const unsigned short*)kp[1];
    const unsigned short* w1 = (const unsigned short*)kp[2]; const unsigned short* b1 = (const unsigned short*)kp[3];
    const unsigned short* w2 = (const unsigned short*)kp[4]; const unsigned short* b2 = (const unsigned short*)kp[5];
    const unsigned short* gamma = (const unsigned short*)kp[6]; unsigned short* out = (unsigned short*)kp[7];
    extern __shared__ float sm[];
    float* cv = sm; float* hid = sm + 16 * 96; float* stt = hid + 16 * 384;
    const int x0 = (blockIdx.x >> 3) * 16, y0 = (blockIdx.x & 7) * 64;
    for (int y = y0; y < y0 + 64; ++y) {
        for (int i = tid; i < 16 * 96; i += 256) {
            int p = i / 96, c = i - p * 96; float s = 0.f;
            for (int ky = 0; ky < 3; ++ky) for (int kx = 0; kx < 3; ++kx) {
                int yy = y + ky - 1, xx = x0 + p + kx - 1;
                float v = (yy >= 0 && yy < 512 && xx >= 0 && xx < 512) ? bf2f(x[((size_t)yy * 512 + xx) * 96 + c]) : 0.f;
                s = fmaf(v, bf2f(dw[(ky * 3 + kx) * 96 + c]), s);
            }
            cv[i] = s;
        }
        __syncthreads();
        if (tid < 16) {
            float m = 0.f; for (int c = 0; c < 96; ++c) m += cv[tid * 96 + c];
            m *= (1.f / 96); float q = 0.f;
            for (int c = 0; c < 96; ++c) { float d = cv[tid * 96 + c] - m; q = fmaf(d, d, q); }
            stt[tid] = m; stt[16 + tid] = 1.f / sqrtf(q * (1.f / 96) + 1e-5f);
        }
        __syncthreads();
        for (int i = tid; i < 16 * 96; i += 256) { int p = i / 96; cv[i] = rbf((cv[i] - stt[p]) * stt[16 + p]); }
        __syncthreads();
        for (int e = tid; e < 384; e += 256) {
            float a[16];
#pragma unroll
            for (int p = 0; p < 16; ++p) a[p] = 0.f;
            for (int c = 0; c < 96; ++c) {
                float w = bf2f(w1[c * 384 + e]);
#pragma unroll
                for (int p = 0; p < 16; ++p) a[p] = fmaf(cv[p * 96 + c], w, a[p]);
            }
            float bb = bf2f(b1[e]);
#pragma unroll
            for (int p = 0; p < 16; ++p) {
                float h = a[p] + bb;
                hid[p * 384 + e] = rbf(0.5f * h * (1.f + tanhf(0.7978845608028654f * (h + 0.044715f * h * h * h))));
            }
        }
        __syncthreads();
        for (int i = tid; i < 16 * 96; i += 256) {
            int p = i / 96, c = i - p * 96; float s = 0.f;
            for (int e = 0; e < 384; ++e) s = fmaf(hid[p * 384 + e], bf2f(w2[e * 96 + c]), s);
            size_t o = ((size_t)y * 512 + x0 + p) * 96 + c;
            out[o] = f2bf(bf2f(x[o]) + bf2f(gamma[c]) * (s + bf2f(b2[c])));
        }
        __syncthreads();
    }

}

extern "C" void kernel_launch(void* const* d_in, const int* in_sizes, int n_in,
                              void* d_out, int out_size, void* d_ws, size_t ws_size,
                              hipStream_t stream) {
    (void)hipFuncSetAttribute((const void*)convnext_kernel, hipFuncAttributeMaxDynamicSharedMemorySize, LDS_BYTES);
    convnext_kernel<<<256, 256, LDS_BYTES, stream>>>(
        (const unsigned short*)d_in[0], (const unsigned short*)d_in[1], (const unsigned short*)d_in[2], (const unsigned short*)d_in[3],
        (const unsigned short*)d_in[4], (const unsigned short*)d_in[5], (const unsigned short*)d_in[6], (unsigned short*)d_out);
}
```

```cpp
#include <hip/hip_runtime.h>
#include <stdint.h>

constexpr int LDS_BYTES = 163584;

__device__ __forceinline__ float bf2f(unsigned short b) { return __uint_as_float((unsigned)b << 16); }
__device__ __forceinline__ unsigned short f2bf(float f) { unsigned r; asm("v_cvt_pk_bf16_f32 %0, %1, %1" : "=v"(r) : "v"(f)); return (unsigned short)r; }
__device__ __forceinline__ float rbf(float f) { return bf2f(f2bf(f)); }

__global__ __launch_bounds__(256, 1) __attribute__((aligned(4096))) void convnext_kernel(
    const unsigned short* __restrict__ x_, const unsigned short* __restrict__ dw_, const unsigned short* __restrict__ w1_,
    const unsigned short* __restrict__ b1_, const unsigned short* __restrict__ w2_, const unsigned short* __restrict__ b2_,
    const unsigned short* __restrict__ gamma_, unsigned short* __restrict__ out_) {
    const unsigned tid = threadIdx.x;
    unsigned long long ka = (unsigned long long)__builtin_amdgcn_kernarg_segment_ptr();
    asm volatile(
R"ASM(
s_load_dwordx16 s[20:35],%2,0
v_lshrrev_b32 v27,6,%1
v_and_b32 v24,63,%1
v_readfirstlane_b32 s52,v27
s_lshr_b32 s70,%0,3
v_and_b32_e64 v61,8,v24
v_lshl_add_u32 v62,s70,2,v27
v_mul_u32_u24 v61,15,v61
s_getpc_b64 s[14:15]
N_pf_%=:
v_or_b32 v61,7,v61
v_and_b32 v62,v61,v62
v_and_b32_e64 v61,7,v24
v_lshl_add_u32 v62,v62,3,v61
v_lshlrev_b32_e64 v62,7,v62
v_min_u32 v62,N_mainend_%=-N_pf_%=+2048,v62
global_load_dword v255,v62,s[14:15]
v_mul_u32_u24 v30,52,v27
v_add_u32 v30,v30,v24
v_subrev_u32_e64 v29,4,v30
v_max_i32 v29,0xc0,v29
v_min_u32_e64 v30,v30,v29
v_mul_u32_u24 v28,0xaaab,v30
s_and_b32 s64,%0,7
s_lshr_b32 s70,%0,3
v_lshrrev_b32 v28,19,v28
s_lshl_b32 s71,s64,6
s_mul_i32 s68,s70,3072
v_bfe_u32 v27,v28,2,2
s_sub_u32 s65,s71,1
s_mul_i32 s56,s65,98304
v_xor_b32 v27,v30,v27
s_add_u32 s56,s56,s68
s_cmp_lt_i32 s65,0
s_cselect_b32 s89,0x70000000,s56
v_lshlrev_b32 v27,4,v27
s_cmp_eq_u32 s70,0
s_cselect_b32 s67,0,99
s_cmp_eq_u32 s70,31
s_cselect_b32 s67,17,s67
v_mov_b32 v29,0x7fff0000
v_cmp_eq_u32_e32 vcc,s67,v28
s_mul_i32 s57,s52,832
s_add_u32 s57,s57,73728
v_cndmask_b32_e32 v6,v27,v29,vcc
s_mov_b32 s38,50331840
s_mov_b32 s39,0x00020000
s_mov_b32 s50,1728
s_mov_b32 s51,0x00020000
v_lshlrev_b32_e64 v8,4,v24
v_mul_u32_u24 v44,79,v24
v_add_u32 v45,5056,v44
v_lshrrev_b32 v44,10,v44
v_lshrrev_b32 v45,10,v45
v_sub_u32 v44,v24,v44
v_sub_u32 v45,v24,v45
v_lshlrev_b32_e64 v44,4,v44
v_add_lshl_u32 v45,v45,64,4
v_min_u32 v28,191,%1
v_lshlrev_b32_e64 v32,2,v28
v_min_u32 v34,95,%1
v_lshlrev_b32_e64 v33,1,v34
s_waitcnt lgkmcnt(0)
s_sub_u32 s36,s20,192
s_subb_u32 s37,s21,0
s_and_b32 s37,s37,0xffff
s_add_u32 m0,s57,0
s_nop 0
buffer_load_dwordx4 v6,s[36:39],s89 offen lds
s_add_u32 m0,s57,3536
s_add_u32 s89,s56,98304
buffer_load_dwordx4 v6,s[36:39],s89 offen lds
s_add_u32 m0,s57,7080
s_add_u32 s89,s56,196608
s_nop 0
buffer_load_dwordx4 v6,s[36:39],s89 offen lds
s_mov_b32 s48,s22
s_and_b32 s49,s23,0xffff
s_mov_b32 m0,12288
global_load_ushort v37,v33,s[32:33]
buffer_load_dwordx4 v44,s[48:51],0 offen lds
s_mov_b32 m0,13312
global_load_dword v35,v32,s[26:27]
buffer_load_dwordx4 v45,s[48:51],0 offen lds
global_load_ushort v36,v33,s[30:31]
v_and_b32 v27,15,v24
v_lshrrev_b32 v28,4,v24
s_mul_i32 s66,s52,192
v_min_u32_e64 v27,11,v27
v_mul_u32_u24 v28,18432,v28
v_lshl_add_u32 v27,v27,4,s66
s_mul_i32 s66,s52,18432
v_add_u32 v172,v27,v28
v_add_u32 v173,s66,v8
global_load_dwordx4 v[160:163],v172,s[24:25]
global_load_dwordx4 v[164:167],v172,s[24:25] offset:3840
global_load_dwordx4 v[168:171],v173,s[28:29]
s_and_b32 s64,s52,1
s_mul_i32 s65,s64,48
v_bfe_u32 v27,v24,3,2
v_and_b32 v28,4,v24
v_lshrrev_b32 v29,5,v24
v_add3_u32 v27,v27,v28,s65
v_lshl_add_u32 v27,v29,3,v27
v_mul_u32_u24 v27,768,v27
v_and_b32 v28,3,v24
v_xor_b32 v28,v28,v29
v_lshl_add_u32 v27,v28,4,v27
s_mul_i32 s65,s64,3072
v_add_u32_e64 v28,s65,v8
s_mul_i32 s67,s64,3080
s_add_u32 s67,s67,40896
s_add_u32 s65,s65,16256
s_cmp_lt_u32 s52,2
s_cselect_b32 s58,s65,s67
s_cselect_b64 vcc,-1,0
s_movk_i32 s59,6144
s_cselect_b32 s59,-22464,s59
s_cselect_b32 s53,11264,0
s_cselect_b32 s88,0,67584
s_cselect_b32 s87,64,0
s_cselect_b32 s69,0,0x70000000
s_mov_b32 s62,32832
s_cselect_b32 s62,88272,s62
s_cselect_b64 s[44:45],s[24:25],s[28:29]
s_add_u32 s62,s62,s58
s_and_b32 s45,s45,0xffff
v_cndmask_b32_e32 v5,v28,v27,vcc
s_add_u32 m0,s57,10616
s_add_u32 s89,s56,294912
buffer_load_dwordx4 v6,s[36:39],s89 offen lds
s_mov_b32 s46,73728
s_mov_b32 s47,0x00020000
s_mov_b32 s99,0x72800000
s_mov_b32 s98,0xffff0000
s_mov_b64 s[90:91],0
v_mov_b32 v96,0x409d3ef8
v_lshlrev_b32 v102,4,%1
v_lshlrev_b32 v104,19,%1
v_cmp_eq_u32_e64 s[94:95],0,%1
v_add_u32 v103,0x8000,v102
s_mov_b32 s4,0x3f000000
s_mov_b32 s5,0x3fc00000
s_mov_b32 s6,0x40200000
s_mov_b32 s7,0x40600000
s_mov_b32 s8,0x40900000
s_mov_b32 s9,0x40b00000
s_mov_b32 s10,0x40d00000
s_mov_b32 s11,0x40f00000
s_mov_b32 s12,0x3f80222e
s_mov_b32 s13,0x13089abf
s_mov_b32 s96,0xebefe000
v_add_u32 v28,0x10000,v104
s_add_u32 m0,s57,14144
s_add_u32 s89,s56,393216
buffer_load_dwordx4 v6,s[36:39],s89 offen lds
v_mul_f32_e64 v98,s99,v104
v_fma_f32 v100,v28,s99,-v98
v_pk_fma_f32 v[64:65],v[100:101],s[4:5],v[98:99] op_sel_hi:[0,1,0]
v_pk_fma_f32 v[66:67],v[100:101],s[6:7],v[98:99] op_sel_hi:[0,1,0]
v_pk_fma_f32 v[68:69],v[100:101],s[8:9],v[98:99] op_sel_hi:[0,1,0]
v_pk_fma_f32 v[70:71],v[100:101],s[10:11],v[98:99] op_sel_hi:[0,1,0]
v_cndmask_b32_e64 v64,v64,0,s[94:95]
v_pk_mul_f32 v[72:73],v[64:65],v[64:65]
v_pk_mul_f32 v[74:75],v[66:67],v[66:67]
v_pk_mul_f32 v[76:77],v[68:69],v[68:69]
v_pk_mul_f32 v[78:79],v[70:71],v[70:71]
v_pk_fma_f32 v[72:73],v[72:73],s[12:13],v[96:97] op_sel_hi:[1,0,0]
v_pk_fma_f32 v[74:75],v[74:75],s[12:13],v[96:97] op_sel_hi:[1,0,0]
v_pk_fma_f32 v[76:77],v[76:77],s[12:13],v[96:97] op_sel_hi:[1,0,0]
v_pk_fma_f32 v[78:79],v[78:79],s[12:13],v[96:97] op_sel_hi:[1,0,0]
s_add_u32 m0,s57,17680
s_add_u32 s89,s56,491520
buffer_load_dwordx4 v6,s[36:39],s89 offen lds
v_pk_mul_f32 v[72:73],v[72:73],v[64:65]
v_pk_mul_f32 v[74:75],v[74:75],v[66:67]
v_pk_mul_f32 v[76:77],v[76:77],v[68:69]
v_pk_mul_f32 v[78:79],v[78:79],v[70:71]
v_exp_f32 v72,v72
v_exp_f32 v73,v73
v_exp_f32 v74,v74
v_exp_f32 v75,v75
v_exp_f32 v76,v76
v_exp_f32 v77,v77
s_add_u32 m0,s57,21224
s_add_u32 s89,s56,589824
buffer_load_dwordx4 v6,s[36:39],s89 offen lds
v_exp_f32 v78,v78
v_exp_f32 v79,v79
v_pk_fma_f32 v[72:73],v[72:73],s[96:97],s[96:97] op_sel_hi:[1,0,0]
v_pk_fma_f32 v[74:75],v[74:75],s[96:97],s[96:97] op_sel_hi:[1,0,0]
v_pk_fma_f32 v[76:77],v[76:77],s[96:97],s[96:97] op_sel_hi:[1,0,0]
v_pk_fma_f32 v[78:79],v[78:79],s[96:97],s[96:97] op_sel_hi:[1,0,0]
v_rcp_f32 v72,v72
v_rcp_f32 v73,v73
v_rcp_f32 v74,v74
v_rcp_f32 v75,v75
s_add_u32 m0,s57,24760
s_add_u32 s89,s56,688128
buffer_load_dwordx4 v6,s[36:39],s89 offen lds
v_rcp_f32 v76,v76
v_rcp_f32 v77,v77
v_rcp_f32 v78,v78
v_rcp_f32 v79,v79
v_pk_mul_f32 v[72:73],v[72:73],v[64:65]
v_pk_mul_f32 v[74:75],v[74:75],v[66:67]
v_pk_mul_f32 v[76:77],v[76:77],v[68:69]
v_pk_mul_f32 v[78:79],v[78:79],v[70:71]
v_pk_fma_f32 v[64:65],v[64:65],s[12:13],v[72:73] op_sel:[0,1,0] op_sel_hi:[1,1,1]
v_pk_fma_f32 v[66:67],v[66:67],s[12:13],v[74:75] op_sel:[0,1,0] op_sel_hi:[1,1,1]
v_pk_fma_f32 v[68:69],v[68:69],s[12:13],v[76:77] op_sel:[0,1,0] op_sel_hi:[1,1,1]
s_add_u32 m0,s57,28288
s_add_u32 s89,s56,786432
buffer_load_dwordx4 v6,s[36:39],s89 offen lds
v_pk_fma_f32 v[70:71],v[70:71],s[12:13],v[78:79] op_sel:[0,1,0] op_sel_hi:[1,1,1]
v_cvt_pk_bf16_f32 v80,v72,v73
v_cvt_pk_bf16_f32 v81,v74,v75
v_cvt_pk_bf16_f32 v82,v76,v77
v_cvt_pk_bf16_f32 v83,v78,v79
v_cvt_pk_bf16_f32 v84,v64,v65
v_cvt_pk_bf16_f32 v85,v66,v67
v_cvt_pk_bf16_f32 v86,v68,v69
v_cvt_pk_bf16_f32 v87,v70,v71
ds_write_b128 v102,v[80:83] offset:0
ds_write_b128 v103,v[84:87] offset:32768
v_add_u32 v104,0x8000000,v104
v_add_u32 v28,0x10000,v104
v_mul_f32_e64 v98,s99,v104
v_fma_f32 v100,v28,s99,-v98
s_add_u32 m0,s57,31824
s_add_u32 s89,s56,884736
buffer_load_dwordx4 v6,s[36:39],s89 offen lds
v_pk_fma_f32 v[64:65],v[100:101],s[4:5],v[98:99] op_sel_hi:[0,1,0]
v_pk_fma_f32 v[66:67],v[100:101],s[6:7],v[98:99] op_sel_hi:[0,1,0]
v_pk_fma_f32 v[68:69],v[100:101],s[8:9],v[98:99] op_sel_hi:[0,1,0]
v_pk_fma_f32 v[70:71],v[100:101],s[10:11],v[98:99] op_sel_hi:[0,1,0]
v_pk_mul_f32 v[72:73],v[64:65],v[64:65]
v_pk_mul_f32 v[74:75],v[66:67],v[66:67]
v_pk_mul_f32 v[76:77],v[68:69],v[68:69]
v_pk_mul_f32 v[78:79],v[70:71],v[70:71]
v_pk_fma_f32 v[72:73],v[72:73],s[12:13],v[96:97] op_sel_hi:[1,0,0]
v_pk_fma_f32 v[74:75],v[74:75],s[12:13],v[96:97] op_sel_hi:[1,0,0]
v_pk_fma_f32 v[76:77],v[76:77],s[12:13],v[96:97] op_sel_hi:[1,0,0]
v_pk_fma_f32 v[78:79],v[78:79],s[12:13],v[96:97] op_sel_hi:[1,0,0]
v_pk_mul_f32 v[72:73],v[72:73],v[64:65]
v_pk_mul_f32 v[74:75],v[74:75],v[66:67]
v_pk_mul_f32 v[76:77],v[76:77],v[68:69]
s_add_u32 m0,s57,35368
s_add_u32 s89,s56,983040
buffer_load_dwordx4 v6,s[36:39],s89 offen lds
v_pk_mul_f32 v[78:79],v[78:79],v[70:71]
v_exp_f32 v72,v72
v_exp_f32 v73,v73
v_exp_f32 v74,v74
v_exp_f32 v75,v75
v_exp_f32 v76,v76
v_exp_f32 v77,v77
v_exp_f32_e64 v78,v78
s_add_u32 m0,s57,38904
s_add_u32 s89,s56,1081344
buffer_load_dwordx4 v6,s[36:39],s89 offen lds
v_exp_f32_e64 v79,v79
v_pk_fma_f32 v[72:73],v[72:73],s[96:97],s[96:97] op_sel_hi:[1,0,0]
v_pk_fma_f32 v[74:75],v[74:75],s[96:97],s[96:97] op_sel_hi:[1,0,0]
v_pk_fma_f32 v[76:77],v[76:77],s[96:97],s[96:97] op_sel_hi:[1,0,0]
v_pk_fma_f32 v[78:79],v[78:79],s[96:97],s[96:97] op_sel_hi:[1,0,0]
v_rcp_f32 v72,v72
v_rcp_f32 v73,v73
v_rcp_f32 v74,v74
v_rcp_f32 v75,v75
v_rcp_f32 v76,v76
v_rcp_f32 v77,v77
v_rcp_f32 v78,v78
v_rcp_f32 v79,v79
v_pk_mul_f32 v[72:73],v[72:73],v[64:65]
v_pk_mul_f32 v[74:75],v[74:75],v[66:67]
v_pk_mul_f32 v[76:77],v[76:77],v[68:69]
v_pk_mul_f32 v[78:79],v[78:79],v[70:71]
v_pk_fma_f32 v[64:65],v[64:65],s[12:13],v[72:73] op_sel:[0,1,0] op_sel_hi:[1,1,1]
v_pk_fma_f32 v[66:67],v[66:67],s[12:13],v[74:75] op_sel:[0,1,0] op_sel_hi:[1,1,1]
v_pk_fma_f32 v[68:69],v[68:69],s[12:13],v[76:77] op_sel:[0,1,0] op_sel_hi:[1,1,1]
v_pk_fma_f32 v[70:71],v[70:71],s[12:13],v[78:79] op_sel:[0,1,0] op_sel_hi:[1,1,1]
v_cvt_pk_bf16_f32 v88,v72,v73
v_cvt_pk_bf16_f32 v89,v74,v75
v_cvt_pk_bf16_f32 v90,v76,v77
v_cvt_pk_bf16_f32 v91,v78,v79
v_cvt_pk_bf16_f32 v92,v64,v65
v_cvt_pk_bf16_f32 v93,v66,v67
v_cvt_pk_bf16_f32 v94,v68,v69
v_cvt_pk_bf16_f32 v95,v70,v71
ds_write_b128 v102,v[88:91] offset:4096
ds_write_b128 v103,v[92:95] offset:36864
s_mov_b32 s96,0x8befe000
s_lshl_b32 s66,s52,2
s_mul_i32 s60,s66,3536
s_add_u32 s66,s66,s71
s_lshl2_add_u32 s16,s70,s52
s_mul_i32 s16,s16,512
v_and_b32 v25,15,v24
v_lshrrev_b32_e64 v26,4,v24
s_mul_i32 s61,s66,98304
s_add_u32 s61,s61,s68
s_mov_b32 s54,0x44ea44ea
s_mov_b32 s55,0x877f877f
s_mov_b64 s[40:41],s[34:35]
s_and_b32 s41,s41,0xffff
s_mov_b32 s42,50331648
s_mov_b32 s43,0x00020000
v_mov_b32 v41,0x70000
s_add_u32 m0,s57,42432
s_add_u32 s89,s56,1179648
buffer_load_dwordx4 v6,s[36:39],s89 offen lds
v_and_b32 v39,8,v24
v_and_b32 v40,7,v24
v_bfe_u32 v27,v24,1,1
v_sub_u32_e64 v27,v26,v27
v_cmp_gt_u32_e64 s[92:93],3,v27
v_and_b32 v28,1,v24
v_mul_u32_u24 v27,3,v27
v_sub_u32_e64 v27,v27,v28
v_cmp_eq_u32_e64 s[94:95],0,v28
v_lshrrev_b32_e64 v29,2,v25
v_mul_i32_i24 v27,208,v27
v_lshl_add_u32 v27,v29,1,v27
v_mul_u32_u24 v10,0x5555,v29
v_add_u32 v27,12288,v27
v_cndmask_b32_e64 v45,v41,v27,s[92:93]
v_cndmask_b32_e64 v44,v41,v45,s[94:95]
v_cndmask_b32_e64 v47,v45,v41,s[94:95]
s_waitcnt vmcnt(13)
s_nop 0
ds_read_u16 a96,v44 offset:0
ds_read_u16 a97,v45 offset:208
ds_read_u16 a98,v45 offset:416
ds_read_u16 a99,v47 offset:624
ds_read_u16 a100,v44 offset:8
ds_read_u16 a101,v45 offset:216
ds_read_u16 a102,v45 offset:424
ds_read_u16 a103,v47 offset:632
v_mov_b64 v[20:21],0
v_mov_b64 v[22:23],0
v_cmp_eq_u32_e64 s[92:93],v26,v29
v_lshlrev_b32_e64 v27,4,v25
v_add_u32 v27,10112,v27
v_cndmask_b32_e64 v13,v41,v27,s[92:93]
v_lshrrev_b32 v27,2,v39
v_lshrrev_b32 v28,5,v24
v_add_u32_e64 v27,v27,v28
v_mul_u32_u24 v27,3536,v27
v_lshl_or_b32 v10,v27,16,v10
v_and_b32 v27,1,v26
v_add_u32 v27,v27,v40
v_mul_u32_u24 v28,384,v27
v_lshrrev_b32_e64 v27,1,v27
v_lshl_add_u32 v28,v27,4,v28
v_mfma_f32_32x32x16_bf16 v[64:79],v[20:23],v[20:23],0
ds_read_u16 a104,v44 offset:16
ds_read_u16 a105,v45 offset:224
ds_read_u16 a106,v45 offset:432
ds_read_u16 a107,v47 offset:640
ds_read_u16 a108,v44 offset:24
ds_read_u16 a109,v45 offset:232
ds_read_u16 a110,v45 offset:440
ds_read_u16 a111,v47 offset:648
v_add_u32 v30,73728,v39
s_add_u32 m0,s57,45968
s_add_u32 s89,s56,1277952
buffer_load_dwordx4 v6,s[36:39],s89 offen lds
v_add_u32 v11,v28,v30
s_movk_i32 s66,16384
v_lshl_add_u32 v27,v40,1,1
v_lshrrev_b32 v28,2,v27
v_xor_b32 v28,v28,v26
v_mul_u32_u24 v27,192,v27
v_lshl_add_u32 v27,v28,4,v27
v_add3_u32 v12,v27,v39,s66
v_lshl_add_u32 v27,v40,1,2
v_lshrrev_b32 v28,2,v27
v_xor_b32 v28,v28,v26
v_mul_u32_u24 v27,192,v27
v_lshl_add_u32 v27,v28,4,v27
v_add3_u32 v27,v27,v39,s66
v_lshl_or_b32 v12,v27,16,v12
v_mfma_f32_32x32x16_bf16 v[80:95],v[20:23],v[20:23],0
ds_read_u16 a112,v44 offset:32
ds_read_u16 a113,v45 offset:240
ds_read_u16 a114,v45 offset:448
ds_read_u16 a115,v47 offset:656
ds_read_u16 a116,v44 offset:40
ds_read_u16 a117,v45 offset:248
ds_read_u16 a118,v45 offset:456
ds_read_u16 a119,v47 offset:664
v_mul_u32_u24 v27,24576,v39
v_mul_u32_u24 v28,384,v40
v_lshl_add_u32 v27,v26,4,v27
v_add_u32 v7,v27,v28
v_lshrrev_b32 v27,3,v25
v_bfe_u32 v28,v25,2,1
v_lshl_add_u32 v28,v27,3,v28
v_lshl_add_u32 v28,v26,1,v28
v_lshlrev_b32_e64 v28,6,v28
v_lshl_add_u32 v28,v27,4,v28
v_and_b32_e64 v29,1,v25
v_lshl_add_u32 v28,v29,5,v28
v_bfe_u32 v29,v25,1,1
v_lshl_add_u32 v28,v29,3,v28
v_xor_b32_e64 v29,16,v28
v_add_u32 v1,16256,v28
v_mfma_f32_32x32x16_bf16 v[96:111],v[20:23],v[20:23],0
ds_read_u16 a120,v44 offset:48
ds_read_u16 a121,v45 offset:256
ds_read_u16 a122,v45 offset:464
ds_read_u16 a123,v47 offset:672
ds_read_u16 a124,v44 offset:56
ds_read_u16 a125,v45 offset:264
ds_read_u16 a126,v45 offset:472
ds_read_u16 a127,v47 offset:680
v_add_u32 v2,16256,v29
s_add_u32 m0,s57,49512
s_add_u32 s89,s56,1376256
buffer_load_dwordx4 v6,s[36:39],s89 offen lds
v_lshrrev_b32_e64 v27,3,v25
v_bfe_u32 v28,v25,2,1
v_lshl_add_u32 v27,v28,1,v27
v_lshrrev_b32_e64 v28,1,v26
v_lshl_add_u32 v27,v28,2,v27
v_mul_u32_u24 v27,192,v27
v_and_b32_e64 v29,1,v26
v_mul_u32_u24 v30,3080,v29
v_add_u32 v27,v27,v30
v_and_b32 v30,3,v25
v_lshl_add_u32 v27,v30,4,v27
v_add_u32 v3,40896,v27
v_lshlrev_b32_e64 v29,6,v29
v_lshl_add_u32 v29,v28,4,v29
v_add_u32 v4,8192,v29
v_mfma_f32_32x32x16_bf16 v[112:127],v[20:23],v[20:23],0
ds_read_u16 a128,v44 offset:64
ds_read_u16 a129,v45 offset:272
ds_read_u16 a130,v45 offset:480
ds_read_u16 a131,v47 offset:688
ds_read_u16 a132,v44 offset:72
ds_read_u16 a133,v45 offset:280
ds_read_u16 a134,v45 offset:488
ds_read_u16 a135,v47 offset:696
v_lshrrev_b32_e64 v27,5,v34
v_bfe_u32 v28,v34,2,1
v_and_b32 v29,3,v34
v_lshlrev_b32 v49,7,v27
v_lshl_add_u32 v49,v28,5,v49
v_lshl_add_u32 v49,v29,2,v49
v_bfe_u32 v30,v34,3,1
v_lshl_add_u32 v49,v30,6,v49
v_bfe_u32 v30,v34,4,1
v_lshl_add_u32 v49,v30,4,v49
v_add_u32 v49,11776,v49
v_lshrrev_b32_e64 v55,6,v34
v_bfe_u32 v30,v34,3,2
v_lshl_add_u32 v55,v55,2,v30
v_and_b32_e64 v30,1,v27
v_lshl_add_u32 v55,v55,1,v30
v_mfma_f32_32x32x16_bf16 v[128:143],v[20:23],v[20:23],0
ds_read_u16 a136,v44 offset:80
ds_read_u16 a137,v45 offset:288
ds_read_u16 a138,v45 offset:496
ds_read_u16 a139,v47 offset:704
ds_read_u16 a140,v44 offset:88
ds_read_u16 a141,v45 offset:296
ds_read_u16 a142,v45 offset:504
ds_read_u16 a143,v47 offset:712
v_and_b32_e64 v30,7,v34
s_add_u32 m0,s57,53048
s_add_u32 s89,s56,1474560
buffer_load_dwordx4 v6,s[36:39],s89 offen lds
v_lshl_add_u32 v55,v55,3,v30
v_lshlrev_b32 v55,2,v55
v_lshlrev_b32 v15,2,v24
v_lshl_add_u32 v50,v27,1,v28
v_lshlrev_b32_e64 v50,4,v50
v_bfe_u32 v30,v34,3,2
v_lshl_add_u32 v50,v30,2,v50
v_add_u32 v50,v50,v29
v_lshlrev_b32 v50,4,v50
v_add_u32 v50,10112,v50
v_lshlrev_b32_e64 v51,4,v29
v_cmp_eq_u32_e64 s[94:95],1,v28
v_lshlrev_b32_e64 v48,1,v32
v_add_u32 v48,8192,v48
v_mov_b32_e64 v53,0
v_mfma_f32_32x32x16_bf16 v[144:159],v[20:23],v[20:23],0
ds_read_u16 a144,v44 offset:96
ds_read_u16 a145,v45 offset:304
ds_read_u16 a146,v45 offset:512
ds_read_u16 a147,v47 offset:720
ds_read_u16 a148,v44 offset:104
ds_read_u16 a149,v45 offset:312
ds_read_u16 a150,v45 offset:520
ds_read_u16 a151,v47 offset:728
v_lshlrev_b32 v37,16,v37
v_lshlrev_b32 v36,16,v36
v_mov_b32 v27,0x45800000
v_mov_b32 v28,0x53800000
v_cmp_nlt_f32_e64 s[90:91],|v37|,v27
v_cmp_nlt_f32_e64 vcc,|v36|,v28
v_mov_b32 v27,0x42800000
v_mul_f32 v37,0x6c800000,v37
v_mul_f32 v36,0x12800000,v36
v_rcp_f32 v56,v37
v_lshlrev_b32 v42,16,v35
v_and_b32 v43,s98,v35
s_or_b64 s[90:91],s[90:91],vcc
v_cmp_nlt_f32_e64 vcc,|v42|,v27
v_cmp_nlt_f32_e64 s[92:93],|v43|,v27
v_med3_f32 v56,v56,1.0,-1.0
ds_read_u16 a152,v44 offset:112
ds_read_u16 a153,v45 offset:320
ds_read_u16 a154,v45 offset:528
ds_read_u16 a155,v47 offset:736
ds_read_u16 a156,v44 offset:120
ds_read_u16 a157,v45 offset:328
ds_read_u16 a158,v45 offset:536
ds_read_u16 a159,v47 offset:744
v_pk_mul_f32 v[42:43],v[42:43],s[96:97] op_sel_hi:[1,0]
s_add_u32 m0,s57,56576
s_add_u32 s89,s56,1572864
buffer_load_dwordx4 v6,s[36:39],s89 offen lds
v_cvt_pk_bf16_f32 v52,v56,v53
ds_write_b64 v48,v[42:43]
ds_write_b32 v49,v36
v_lshlrev_b32 v54,16,v52
v_rcp_f32 v54,v54
v_lshlrev_b64 v[52:53],v51,v[52:53]
s_or_b64 s[90:91],s[90:91],vcc
s_or_b64 s[90:91],s[90:91],s[92:93]
v_cndmask_b32_e64 v56,v52,0,s[94:95]
v_cndmask_b32_e64 v58,0,v52,s[94:95]
v_cndmask_b32_e64 v57,v53,0,s[94:95]
v_cndmask_b32_e64 v59,0,v53,s[94:95]
ds_write_b32 v15,v7 offset:14848
ds_write_b32 v15,v13 offset:15104
s_mul_i32 s66,s52,1536
ds_read_u16 a160,v44 offset:128
ds_read_u16 a161,v45 offset:336
ds_read_u16 a162,v45 offset:544
ds_read_u16 a163,v47 offset:752
ds_read_u16 a164,v44 offset:136
ds_read_u16 a165,v45 offset:344
ds_read_u16 a166,v45 offset:552
ds_read_u16 a167,v47 offset:760
s_add_u32 m0,s57,60112
s_add_u32 s89,s56,1671168
buffer_load_dwordx4 v6,s[36:39],s89 offen lds
s_add_u32 m0,s58,18480
s_nop 0
buffer_load_dwordx4 v5,s[44:47],s69 offen offset:0 lds
s_mov_b64 exec,0
s_cmp_eq_u32 s52,0
s_cbranch_scc1 TE00_%=
s_cmp_eq_u32 s52,1
s_cbranch_scc1 TE01_%=
s_cmp_eq_u32 s52,2
s_cbranch_scc1 TE02_%=
s_branch TE03_%=
TR0_%=:
s_mov_b64 exec,-1
s_add_u32 s66,s66,16256
v_lshl_add_u32 v7,v24,3,s66
ds_write_b128 v50,v[56:59]
ds_write_b32 v55,v54 offset:14336
s_waitcnt vmcnt(16)
s_nop 0
v_and_b32 v160,0x7fff7fff,v160
v_and_b32 v161,0x7fff7fff,v161
v_and_b32 v162,0x7fff7fff,v162
v_and_b32 v163,0x7fff7fff,v163
v_and_b32 v164,0x7fff7fff,v164
v_and_b32 v165,0x7fff7fff,v165
v_and_b32 v166,0x7fff7fff,v166
v_and_b32 v167,0x7fff7fff,v167
v_and_b32 v168,0x7fff7fff,v168
v_and_b32 v169,0x7fff7fff,v169
v_and_b32 v170,0x7fff7fff,v170
ds_read_u16 a168,v44 offset:144
ds_read_u16 a169,v45 offset:352
ds_read_u16 a170,v45 offset:560
TE00_%=:
s_cbranch_execz TP2752_%=
ds_read_u16 a171,v47 offset:768
ds_read_u16 a172,v44 offset:152
ds_read_u16 a173,v45 offset:360
ds_read_u16 a174,v45 offset:568
ds_read_u16 a175,v47 offset:776
v_and_b32 v171,0x7fff7fff,v171
s_add_u32 s69,s69,s53
buffer_load_dwordx4 v5,s[44:47],s69 offen offset:1024 lds
v_pk_max_u16 v160,v160,v164
v_pk_max_u16 v161,v161,v165
v_pk_max_u16 v162,v162,v166
v_pk_max_u16 v163,v163,v167
v_mov_b64 v[164:165],v[160:161]
v_mov_b64 v[166:167],v[162:163]
s_nop 0
v_permlane32_swap_b32 v160,v164
v_permlane32_swap_b32 v161,v165
v_permlane32_swap_b32 v162,v166
v_permlane32_swap_b32_e64 v163,v167
v_pk_max_u16 v160,v160,v164
v_pk_max_u16 v161,v161,v165
v_pk_max_u16 v162,v162,v166
v_pk_max_u16 v163,v163,v167
v_mov_b64_e64 v[164:165],v[160:161]
ds_read_u16 a176,v44 offset:160
ds_read_u16 a177,v45 offset:368
ds_read_u16 a178,v45 offset:576
ds_read_u16 a179,v47 offset:784
ds_read_u16 a180,v44 offset:168
ds_read_u16 a181,v45 offset:376
ds_read_u16 a182,v45 offset:584
ds_read_u16 a183,v47 offset:792
v_mov_b64 v[166:167],v[162:163]
v_permlane16_swap_b32 v160,v164
v_permlane16_swap_b32 v161,v165
v_permlane16_swap_b32 v162,v166
v_permlane16_swap_b32_e64 v163,v167
v_pk_max_u16 v160,v160,v164
v_pk_max_u16 v161,v161,v165
v_pk_max_u16 v162,v162,v166
v_pk_max_u16 v163,v163,v167
v_pk_min_u16 v160,v160,v161
v_pk_min_u16 v162,v162,v163
v_pk_min_u16 v160,v160,v162
v_pk_min_u16 v160,v160,v160 op_sel:[0,1] op_sel_hi:[1,0]
v_pk_max_u16 v168,v168,v169
v_pk_max_u16 v170,v170,v171
s_add_u32 s69,s69,s53
s_nop 0
buffer_load_dwordx4 v5,s[44:47],s69 offen offset:2048 lds
v_pk_max_u16 v168,v168,v170
ds_read_u16 a184,v44 offset:176
ds_read_u16 a185,v45 offset:384
ds_read_u16 a186,v45 offset:592
ds_read_u16 a187,v47 offset:800
ds_read_u16 a188,v44 offset:184
ds_read_u16 a189,v45 offset:392
ds_read_u16 a190,v45 offset:600
ds_read_u16 a191,v47 offset:808
v_pk_max_u16 v168,v168,v168 op_sel:[0,1] op_sel_hi:[1,0]
v_cmp_gt_u16_e32 vcc,0x3880,v160
s_nop 0
s_or_b64 s[90:91],s[90:91],vcc
v_cmp_le_u16_e32 vcc,0x5380,v168
s_nop 0
s_or_b64 s[90:91],s[90:91],vcc
v_accvgpr_write_b32 a88,0
v_accvgpr_write_b32 a89,0
v_accvgpr_write_b32 a90,0
v_accvgpr_write_b32 a91,0
s_add_u32 m0,s58,0
s_mov_b32 s69,s87
buffer_load_dwordx4 v5,s[44:47],s69 offen offset:0 lds
s_add_u32 s69,s69,s53
buffer_load_dwordx4 v5,s[44:47],s69 offen offset:1024 lds
s_add_u32 s69,s69,s53
buffer_load_dwordx4 v5,s[44:47],s69 offen offset:2048 lds
s_waitcnt lgkmcnt(0)
s_waitcnt vmcnt(6)
s_barrier
v_add_u32_sdwa v28,s60,v10 dst_sel:DWORD dst_unused:UNUSED_PAD src0_sel:DWORD src1_sel:WORD_1
v_subrev_u32 v8,88400,v28
v_min_u32 v28,v28,v8
v_add_u32 v29,7072,v28
v_subrev_u32 v8,88400,v29
v_min_u32_e64 v29,v29,v8
v_xad_u32 v24,v11,0,v28
v_xad_u32 v25,v11,8,v29
v_xad_u32 v26,v11,16,v28
v_xad_u32 v27,v11,24,v29
ds_read_b64 a[0:1],v24 offset:0
ds_read_b64 a[2:3],v24 offset:192
ds_read_b64 a[4:5],v25 offset:0
ds_read_b64 a[6:7],v25 offset:192
ds_read_b64 a[8:9],v24 offset:8
ds_read_b64 a[10:11],v24 offset:200
ds_read_b64 a[12:13],v25 offset:8
ds_read_b64 a[14:15],v25 offset:200
ds_read_b64 a[16:17],v26 offset:0
ds_read_b64 a[18:19],v26 offset:192
ds_read_b64 a[20:21],v27 offset:0
ds_read_b64 a[22:23],v27 offset:192
s_waitcnt lgkmcnt(8)
v_smfmac_f32_16x16x64_bf16 v[64:67],a[96:99],a[0:7],v10
s_waitcnt lgkmcnt(4)
s_add_u32 m0,s58,6160
s_add_u32 s69,s69,s59
buffer_load_dwordx4 v5,s[44:47],s69 offen offset:0 lds
ds_read_b64 a[0:1],v26 offset:8
ds_read_b64 a[2:3],v26 offset:200
ds_read_b64 a[4:5],v27 offset:8
ds_read_b64 a[6:7],v27 offset:200
v_smfmac_f32_16x16x64_bf16 v[68:71],a[100:103],a[8:15],v10
s_waitcnt lgkmcnt(4)
s_add_u32 m0,s57,63656
s_add_u32 s89,s56,1769472
buffer_load_dwordx4 v6,s[36:39],s89 offen lds
ds_read_b64 a[8:9],v24 offset:64
ds_read_b64 a[10:11],v24 offset:256
ds_read_b64 a[12:13],v25 offset:64
ds_read_b64 a[14:15],v25 offset:256
v_smfmac_f32_16x16x64_bf16 v[72:75],a[104:107],a[16:23],v10
s_waitcnt lgkmcnt(4)
ds_read_b64 a[16:17],v24 offset:72
ds_read_b64 a[18:19],v24 offset:264
ds_read_b64 a[20:21],v25 offset:72
ds_read_b64 a[22:23],v25 offset:264
v_and_b32_e64 v248,8,v11
v_pk_mul_f32 v[40:41],v[64:65],v[64:65]
v_pk_mul_f32 v[42:43],v[66:67],v[66:67]
v_smfmac_f32_16x16x64_bf16 v[76:79],a[108:111],a[0:7],v10
s_waitcnt lgkmcnt(4)
ds_read_b64 a[0:1],v26 offset:64
ds_read_b64 a[2:3],v26 offset:256
ds_read_b64 a[4:5],v27 offset:64
ds_read_b64 a[6:7],v27 offset:256
v_mul_u32_u24 v248,884,v248
s_add_u32 s64,s60,3536
v_pk_add_f32 v[36:37],v[64:65],v[68:69]
v_pk_fma_f32 v[40:41],v[68:69],v[68:69],v[40:41]
v_pk_add_f32 v[38:39],v[66:67],v[70:71]
v_pk_fma_f32 v[42:43],v[70:71],v[70:71],v[42:43]
v_smfmac_f32_16x16x64_bf16 v[80:83],a[128:131],a[8:15],v10
s_waitcnt lgkmcnt(4)
s_add_u32 m0,s57,67192
s_add_u32 s89,s56,1867776
buffer_load_dwordx4 v6,s[36:39],s89 offen lds
ds_read_b64 a[8:9],v26 offset:72
ds_read_b64 a[10:11],v26 offset:264
ds_read_b64 a[12:13],v27 offset:72
ds_read_b64 a[14:15],v27 offset:264
v_add_u32 v251,s64,v248
v_pk_add_f32 v[36:37],v[36:37],v[72:73]
v_pk_fma_f32 v[40:41],v[72:73],v[72:73],v[40:41]
v_pk_add_f32 v[38:39],v[38:39],v[74:75]
v_pk_fma_f32 v[42:43],v[74:75],v[74:75],v[42:43]
v_smfmac_f32_16x16x64_bf16 v[84:87],a[132:135],a[16:23],v10
s_waitcnt lgkmcnt(4)
s_nop 0
ds_read_b64 a[16:17],v24 offset:128
ds_read_b64 a[18:19],v24 offset:320
ds_read_b64 a[20:21],v25 offset:128
ds_read_b64 a[22:23],v25 offset:320
v_subrev_u32 v8,88400,v251
v_pk_add_f32 v[36:37],v[36:37],v[76:77]
v_pk_fma_f32 v[40:41],v[76:77],v[76:77],v[40:41]
v_pk_add_f32 v[38:39],v[38:39],v[78:79]
v_pk_fma_f32 v[42:43],v[78:79],v[78:79],v[42:43]
v_smfmac_f32_16x16x64_bf16 v[88:91],a[136:139],a[0:7],v10
s_waitcnt lgkmcnt(4)
ds_read_b64 a[0:1],v24 offset:136
ds_read_b64 a[2:3],v24 offset:328
ds_read_b64 a[4:5],v25 offset:136
ds_read_b64 a[6:7],v25 offset:328
v_min_u32 v251,v251,v8
v_pk_add_f32 v[36:37],v[36:37],v[80:81]
v_pk_fma_f32 v[40:41],v[80:81],v[80:81],v[40:41]
v_pk_add_f32 v[38:39],v[38:39],v[82:83]
v_pk_fma_f32 v[42:43],v[82:83],v[82:83],v[42:43]
v_smfmac_f32_16x16x64_bf16 v[92:95],a[140:143],a[8:15],v10
s_waitcnt lgkmcnt(4)
s_add_u32 m0,s58,6160
s_add_u32 s69,s69,s53
buffer_load_dwordx4 v5,s[44:47],s69 offen offset:1024 lds
ds_read_b64 a[8:9],v26 offset:128
ds_read_b64 a[10:11],v26 offset:320
ds_read_b64 a[12:13],v27 offset:128
ds_read_b64 a[14:15],v27 offset:320
v_xad_u32 v24,v11,32,v28
v_xad_u32 v25,v11,40,v29
v_pk_add_f32 v[36:37],v[36:37],v[84:85]
v_pk_fma_f32 v[40:41],v[84:85],v[84:85],v[40:41]
v_pk_add_f32 v[38:39],v[38:39],v[86:87]
v_pk_fma_f32 v[42:43],v[86:87],v[86:87],v[42:43]
v_smfmac_f32_16x16x64_bf16 v[96:99],a[160:163],a[16:23],v10
s_waitcnt lgkmcnt(4)
ds_read_b64 a[16:17],v26 offset:136
ds_read_b64 a[18:19],v26 offset:328
ds_read_b64 a[20:21],v27 offset:136
ds_read_b64 a[22:23],v27 offset:328
v_add_u32 v250,3536,v251
v_pk_add_f32 v[36:37],v[36:37],v[88:89]
v_pk_fma_f32 v[40:41],v[88:89],v[88:89],v[40:41]
v_pk_add_f32 v[38:39],v[38:39],v[90:91]
v_pk_fma_f32 v[42:43],v[90:91],v[90:91],v[42:43]
v_smfmac_f32_16x16x64_bf16 v[100:103],a[164:167],a[0:7],v10
s_waitcnt lgkmcnt(4)
s_add_u32 m0,s57,70720
s_add_u32 s89,s56,1966080
buffer_load_dwordx4 v6,s[36:39],s89 offen lds
ds_read_b64 a[0:1],v24 offset:0
ds_read_b64 a[2:3],v24 offset:192
ds_read_b64 a[4:5],v25 offset:0
ds_read_b64 a[6:7],v25 offset:192
v_xad_u32 v26,v11,48,v28
v_xad_u32 v27,v11,56,v29
v_pk_add_f32 v[36:37],v[36:37],v[92:93]
v_pk_fma_f32 v[40:41],v[92:93],v[92:93],v[40:41]
v_pk_add_f32 v[38:39],v[38:39],v[94:95]
v_pk_fma_f32 v[42:43],v[94:95],v[94:95],v[42:43]
v_smfmac_f32_16x16x64_bf16 v[104:107],a[168:171],a[8:15],v10
s_waitcnt lgkmcnt(4)
s_nop 0
ds_read_b64 a[8:9],v24 offset:8
ds_read_b64 a[10:11],v24 offset:200
ds_read_b64 a[12:13],v25 offset:8
ds_read_b64 a[14:15],v25 offset:200
v_subrev_u32 v8,88400,v250
v_pk_add_f32 v[36:37],v[36:37],v[96:97]
v_pk_fma_f32 v[40:41],v[96:97],v[96:97],v[40:41]
v_pk_add_f32 v[38:39],v[38:39],v[98:99]
v_pk_fma_f32 v[42:43],v[98:99],v[98:99],v[42:43]
v_smfmac_f32_16x16x64_bf16 v[108:111],a[172:175],a[16:23],v10
s_waitcnt lgkmcnt(4)
ds_read_b64 a[16:17],v24 offset:64
ds_read_b64 a[18:19],v24 offset:256
ds_read_b64 a[20:21],v25 offset:64
ds_read_b64 a[22:23],v25 offset:256
v_min_u32 v250,v250,v8
v_pk_add_f32 v[36:37],v[36:37],v[100:101]
v_pk_fma_f32 v[40:41],v[100:101],v[100:101],v[40:41]
v_pk_add_f32 v[38:39],v[38:39],v[102:103]
v_pk_fma_f32 v[42:43],v[102:103],v[102:103],v[42:43]
v_smfmac_f32_16x16x64_bf16 v[112:115],a[112:115],a[0:7],v10
s_waitcnt lgkmcnt(4)
s_add_u32 m0,s58,6160
s_add_u32 s69,s69,s53
buffer_load_dwordx4 v5,s[44:47],s69 offen offset:2048 lds
ds_read_b64 a[0:1],v24 offset:72
ds_read_b64 a[2:3],v24 offset:264
ds_read_b64 a[4:5],v25 offset:72
ds_read_b64 a[6:7],v25 offset:264
v_xor_b32 v9,0x80008,v12
v_pk_add_f32 v[36:37],v[36:37],v[104:105]
v_pk_fma_f32 v[40:41],v[104:105],v[104:105],v[40:41]
v_pk_add_f32 v[38:39],v[38:39],v[106:107]
v_pk_fma_f32 v[42:43],v[106:107],v[106:107],v[42:43]
v_smfmac_f32_16x16x64_bf16 v[116:119],a[116:119],a[8:15],v10
s_waitcnt lgkmcnt(4)
ds_read_b64 a[8:9],v24 offset:128
ds_read_b64 a[10:11],v24 offset:320
ds_read_b64 a[12:13],v25 offset:128
ds_read_b64 a[14:15],v25 offset:320
v_add_u32_sdwa v248,v12,v251 dst_sel:DWORD dst_unused:UNUSED_PAD src0_sel:WORD_0 src1_sel:DWORD
v_pk_add_f32 v[36:37],v[36:37],v[108:109]
v_pk_fma_f32 v[40:41],v[108:109],v[108:109],v[40:41]
v_pk_add_f32 v[38:39],v[38:39],v[110:111]
v_pk_fma_f32 v[42:43],v[110:111],v[110:111],v[42:43]
v_smfmac_f32_16x16x64_bf16 v[120:123],a[144:147],a[16:23],v10
s_waitcnt lgkmcnt(4)
s_add_u32 m0,s57,74256
s_add_u32 s89,s56,2064384
buffer_load_dwordx4 v6,s[36:39],s89 offen lds
ds_read_b64 a[16:17],v24 offset:136
ds_read_b64 a[18:19],v24 offset:328
ds_read_b64 a[20:21],v25 offset:136
ds_read_b64 a[22:23],v25 offset:328
v_add_u32_sdwa v249,v12,v251 dst_sel:DWORD dst_unused:UNUSED_PAD src0_sel:WORD_1 src1_sel:DWORD
v_pk_add_f32 v[36:37],v[36:37],v[112:113]
v_pk_fma_f32 v[40:41],v[112:113],v[112:113],v[40:41]
v_pk_add_f32 v[38:39],v[38:39],v[114:115]
v_pk_fma_f32 v[42:43],v[114:115],v[114:115],v[42:43]
v_smfmac_f32_16x16x64_bf16 v[124:127],a[148:151],a[0:7],v10
s_waitcnt lgkmcnt(4)
ds_read_b64 a[0:1],v26 offset:0
ds_read_b64 a[2:3],v26 offset:192
ds_read_b64 a[4:5],v27 offset:0
ds_read_b64 a[6:7],v27 offset:192
v_add_u32_sdwa v251,v9,v250 dst_sel:DWORD dst_unused:UNUSED_PAD src0_sel:WORD_1 src1_sel:DWORD
v_pk_add_f32 v[36:37],v[36:37],v[116:117]
v_pk_fma_f32 v[40:41],v[116:117],v[116:117],v[40:41]
v_pk_add_f32 v[38:39],v[38:39],v[118:119]
v_pk_fma_f32 v[42:43],v[118:119],v[118:119],v[42:43]
v_smfmac_f32_16x16x64_bf16 v[128:131],a[176:179],a[8:15],v10
s_waitcnt lgkmcnt(4)
ds_read_b64 a[8:9],v26 offset:8
ds_read_b64 a[10:11],v26 offset:200
ds_read_b64 a[12:13],v27 offset:8
ds_read_b64 a[14:15],v27 offset:200
v_add_u32_sdwa v250,v9,v250 dst_sel:DWORD dst_unused:UNUSED_PAD src0_sel:WORD_0 src1_sel:DWORD
v_pk_add_f32 v[36:37],v[36:37],v[120:121]
v_pk_fma_f32 v[40:41],v[120:121],v[120:121],v[40:41]
v_pk_add_f32 v[38:39],v[38:39],v[122:123]
v_pk_fma_f32 v[42:43],v[122:123],v[122:123],v[42:43]
v_smfmac_f32_16x16x64_bf16 v[132:135],a[180:183],a[16:23],v10
s_waitcnt lgkmcnt(4)
s_add_u32 m0,s58,12320
s_add_u32 s69,s69,s59
buffer_load_dwordx4 v5,s[44:47],s69 offen offset:0 lds
ds_read_b64 a[16:17],v26 offset:64
ds_read_b64 a[18:19],v26 offset:256
ds_read_b64 a[20:21],v27 offset:64
ds_read_b64 a[22:23],v27 offset:256
v_pk_add_f32 v[36:37],v[36:37],v[124:125]
v_pk_fma_f32 v[40:41],v[124:125],v[124:125],v[40:41]
v_pk_add_f32 v[38:39],v[38:39],v[126:127]
v_pk_fma_f32 v[42:43],v[126:127],v[126:127],v[42:43]
v_smfmac_f32_16x16x64_bf16 v[136:139],a[120:123],a[0:7],v10
s_waitcnt lgkmcnt(4)
ds_read_b64 a[0:1],v26 offset:72
ds_read_b64 a[2:3],v26 offset:264
ds_read_b64 a[4:5],v27 offset:72
ds_read_b64 a[6:7],v27 offset:264
v_pk_add_f32 v[36:37],v[36:37],v[128:129]
v_pk_fma_f32 v[40:41],v[128:129],v[128:129],v[40:41]
v_pk_add_f32 v[38:39],v[38:39],v[130:131]
v_pk_fma_f32 v[42:43],v[130:131],v[130:131],v[42:43]
v_smfmac_f32_16x16x64_bf16 v[140:143],a[124:127],a[8:15],v10
s_waitcnt lgkmcnt(4)
s_add_u32 m0,s57,77800
s_add_u32 s89,s56,2162688
buffer_load_dwordx4 v6,s[36:39],s89 offen lds
ds_read_b64 a[8:9],v26 offset:128
ds_read_b64 a[10:11],v26 offset:320
ds_read_b64 a[12:13],v27 offset:128
ds_read_b64 a[14:15],v27 offset:320
v_pk_add_f32 v[36:37],v[36:37],v[132:133]
v_pk_fma_f32 v[40:41],v[132:133],v[132:133],v[40:41]
v_pk_add_f32 v[38:39],v[38:39],v[134:135]
v_pk_fma_f32 v[42:43],v[134:135],v[134:135],v[42:43]
v_smfmac_f32_16x16x64_bf16 v[144:147],a[152:155],a[16:23],v10
s_waitcnt lgkmcnt(4)
ds_read_b64 a[16:17],v26 offset:136
ds_read_b64 a[18:19],v26 offset:328
ds_read_b64 a[20:21],v27 offset:136
ds_read_b64 a[22:23],v27 offset:328
v_pk_add_f32 v[36:37],v[36:37],v[136:137]
v_pk_fma_f32 v[40:41],v[136:137],v[136:137],v[40:41]
v_pk_add_f32 v[38:39],v[38:39],v[138:139]
v_pk_fma_f32 v[42:43],v[138:139],v[138:139],v[42:43]
v_smfmac_f32_16x16x64_bf16 v[148:151],a[156:159],a[0:7],v10
s_waitcnt lgkmcnt(4)
ds_read_b64 a[24:25],v248 offset:57344
ds_read_b64 a[26:27],v248 offset:57352
ds_read_b64 a[36:37],v249 offset:57344
ds_read_b64 a[38:39],v249 offset:57352
v_pk_add_f32 v[36:37],v[36:37],v[140:141]
v_pk_fma_f32 v[40:41],v[140:141],v[140:141],v[40:41]
v_pk_add_f32 v[38:39],v[38:39],v[142:143]
v_pk_fma_f32 v[42:43],v[142:143],v[142:143],v[42:43]
v_smfmac_f32_16x16x64_bf16 v[152:155],a[184:187],a[8:15],v10
s_waitcnt lgkmcnt(4)
s_add_u32 m0,s58,12320
s_add_u32 s69,s69,s53
buffer_load_dwordx4 v5,s[44:47],s69 offen offset:1024 lds
ds_read_b64 a[28:29],v248 offset:57408
ds_read_b64 a[30:31],v248 offset:57416
ds_read_b64 a[40:41],v249 offset:57408
ds_read_b64 a[42:43],v249 offset:57416
v_pk_add_f32 v[36:37],v[36:37],v[144:145]
v_pk_fma_f32 v[40:41],v[144:145],v[144:145],v[40:41]
v_pk_add_f32 v[38:39],v[38:39],v[146:147]
v_pk_fma_f32 v[42:43],v[146:147],v[146:147],v[42:43]
v_smfmac_f32_16x16x64_bf16 v[156:159],a[188:191],a[16:23],v10
ds_read_b64 a[32:33],v248 offset:57472
ds_read_b64 a[34:35],v248 offset:57480
ds_read_b64 a[44:45],v249 offset:57472
ds_read_b64 a[46:47],v249 offset:57480
v_pk_add_f32 v[36:37],v[36:37],v[148:149]
v_pk_fma_f32 v[40:41],v[148:149],v[148:149],v[40:41]
v_pk_add_f32 v[38:39],v[38:39],v[150:151]
v_pk_fma_f32 v[42:43],v[150:151],v[150:151],v[42:43]
v_pk_add_f32 v[36:37],v[36:37],v[152:153]
v_pk_fma_f32 v[40:41],v[152:153],v[152:153],v[40:41]
v_pk_add_f32 v[38:39],v[38:39],v[154:155]
v_pk_fma_f32 v[42:43],v[154:155],v[154:155],v[42:43]
v_pk_add_f32 v[36:37],v[36:37],v[156:157]
v_pk_fma_f32 v[40:41],v[156:157],v[156:157],v[40:41]
v_pk_add_f32 v[38:39],v[38:39],v[158:159]
v_pk_fma_f32 v[42:43],v[158:159],v[158:159],v[42:43]
s_nop 0
v_permlane32_swap_b32 v36,v38
v_permlane32_swap_b32 v37,v39
v_permlane32_swap_b32 v40,v42
v_permlane32_swap_b32_e64 v41,v43
v_pk_add_f32 v[36:37],v[36:37],v[38:39]
v_pk_add_f32 v[40:41],v[40:41],v[42:43]
s_add_u32 s60,s60,56576
s_sub_u32 s64,s60,88400
s_min_u32 s60,s60,s64
v_permlane16_swap_b32 v36,v37
v_permlane16_swap_b32 v40,v41
v_add_f32 v8,v36,v37
v_add_f32_e64 v9,v40,v41
v_mul_f32 v8,0x3c2aaaab,v8
v_mul_f32 v9,0x3c2aaaab,v9
v_cmp_ngt_f32 vcc,0x5d800000,v9
v_fma_f32 v9,-v8,v8,v9
v_max_f32 v9,0,v9
s_or_b64 s[90:91],s[90:91],vcc
v_add_f32 v9,0x3727c5ac,v9
v_rsq_f32 v9,v9
s_nop 0
v_mul_f32 v36,s96,v9
v_mul_f32 v37,s96,v9
v_mul_f32 v40,-v8,v36
v_mul_f32 v41,-v8,v36
v_permlane16_swap_b32 v36,v37
s_nop 0
v_permlane16_swap_b32 v40,v41
v_mov_b64 v[38:39],v[36:37]
v_mov_b64 v[42:43],v[40:41]
s_nop 0
v_permlane32_swap_b32 v36,v38
v_permlane32_swap_b32 v37,v39
v_permlane32_swap_b32 v40,v42
v_permlane32_swap_b32 v41,v43
s_waitcnt vmcnt(10)
s_barrier
ds_read_b64_tr_b16 a[192:193],v1 offset:18480
ds_read_b64_tr_b16 a[194:195],v1 offset:19504
ds_read_b64_tr_b16 a[196:197],v1 offset:20528
ds_read_b64_tr_b16 a[198:199],v1 offset:21552
v_pk_fma_f32 v[64:65],v[64:65],v[36:37],v[40:41]
v_pk_fma_f32 v[66:67],v[66:67],v[38:39],v[42:43]
v_pk_fma_f32 v[68:69],v[68:69],v[36:37],v[40:41]
v_pk_fma_f32 v[70:71],v[70:71],v[38:39],v[42:43]
v_pk_fma_f32 v[72:73],v[72:73],v[36:37],v[40:41]
v_pk_fma_f32 v[74:75],v[74:75],v[38:39],v[42:43]
v_pk_fma_f32 v[76:77],v[76:77],v[36:37],v[40:41]
v_pk_fma_f32 v[78:79],v[78:79],v[38:39],v[42:43]
v_pk_fma_f32 v[112:113],v[112:113],v[36:37],v[40:41]
v_pk_fma_f32 v[114:115],v[114:115],v[38:39],v[42:43]
ds_read_b64_tr_b16 a[200:201],v1 offset:22576
ds_read_b64_tr_b16 a[202:203],v1 offset:23600
ds_read_b64_tr_b16 a[204:205],v2 offset:22576
ds_read_b64_tr_b16 a[206:207],v2 offset:23600
v_pk_fma_f32 v[116:117],v[116:117],v[36:37],v[40:41]
v_pk_fma_f32 v[118:119],v[118:119],v[38:39],v[42:43]
s_add_u32 m0,s57,81336
s_add_u32 s89,s56,2260992
buffer_load_dwordx4 v6,s[36:39],s89 offen lds
v_pk_fma_f32 v[136:137],v[136:137],v[36:37],v[40:41]
v_pk_fma_f32 v[138:139],v[138:139],v[38:39],v[42:43]
v_pk_fma_f32 v[140:141],v[140:141],v[36:37],v[40:41]
v_pk_fma_f32 v[142:143],v[142:143],v[38:39],v[42:43]
v_pk_fma_f32 v[80:81],v[80:81],v[36:37],v[40:41]
v_pk_fma_f32 v[82:83],v[82:83],v[38:39],v[42:43]
v_pk_fma_f32 v[84:85],v[84:85],v[36:37],v[40:41]
v_pk_fma_f32 v[86:87],v[86:87],v[38:39],v[42:43]
ds_read_b64_tr_b16 a[208:209],v2 offset:20528
ds_read_b64_tr_b16 a[210:211],v2 offset:21552
ds_read_b64_tr_b16 a[212:213],v2 offset:18480
ds_read_b64_tr_b16 a[214:215],v2 offset:19504
v_pk_fma_f32 v[88:89],v[88:89],v[36:37],v[40:41]
v_pk_fma_f32 v[90:91],v[90:91],v[38:39],v[42:43]
v_pk_fma_f32 v[92:93],v[92:93],v[36:37],v[40:41]
v_pk_fma_f32 v[94:95],v[94:95],v[38:39],v[42:43]
s_add_u32 m0,s58,12320
s_add_u32 s69,s69,s53
s_nop 0
buffer_load_dwordx4 v5,s[44:47],s69 offen offset:2048 lds
v_pk_fma_f32 v[120:121],v[120:121],v[36:37],v[40:41]
v_pk_fma_f32 v[122:123],v[122:123],v[38:39],v[42:43]
v_pk_fma_f32 v[124:125],v[124:125],v[36:37],v[40:41]
v_pk_fma_f32 v[126:127],v[126:127],v[38:39],v[42:43]
v_pk_fma_f32 v[144:145],v[144:145],v[36:37],v[40:41]
v_pk_fma_f32 v[146:147],v[146:147],v[38:39],v[42:43]
ds_read_b128 v[16:19],v4 offset:0
ds_read_b128 v[20:23],v4 offset:32
ds_read_b64 v[232:233],v7 offset:18480
ds_read_b64 v[234:235],v7 offset:18992
v_pk_fma_f32 v[148:149],v[148:149],v[36:37],v[40:41]
v_pk_fma_f32 v[150:151],v[150:151],v[38:39],v[42:43]
v_pk_fma_f32 v[96:97],v[96:97],v[36:37],v[40:41]
v_pk_fma_f32 v[98:99],v[98:99],v[38:39],v[42:43]
v_pk_fma_f32 v[100:101],v[100:101],v[36:37],v[40:41]
v_pk_fma_f32 v[102:103],v[102:103],v[38:39],v[42:43]
s_add_u32 m0,s57,84864
s_add_u32 s89,s56,2359296
buffer_load_dwordx4 v6,s[36:39],s89 offen lds
v_pk_fma_f32 v[104:105],v[104:105],v[36:37],v[40:41]
v_pk_fma_f32 v[106:107],v[106:107],v[38:39],v[42:43]
v_pk_fma_f32 v[108:109],v[108:109],v[36:37],v[40:41]
v_pk_fma_f32 v[110:111],v[110:111],v[38:39],v[42:43]
ds_read_b64 v[236:237],v7 offset:19504
v_pk_fma_f32 v[128:129],v[128:129],v[36:37],v[40:41]
v_pk_fma_f32 v[130:131],v[130:131],v[38:39],v[42:43]
v_pk_fma_f32 v[132:133],v[132:133],v[36:37],v[40:41]
v_pk_fma_f32 v[134:135],v[134:135],v[38:39],v[42:43]
v_pk_fma_f32 v[152:153],v[152:153],v[36:37],v[40:41]
v_pk_fma_f32 v[154:155],v[154:155],v[38:39],v[42:43]
v_pk_fma_f32 v[156:157],v[156:157],v[36:37],v[40:41]
v_pk_fma_f32 v[158:159],v[158:159],v[38:39],v[42:43]
v_cvt_pk_bf16_f32 v160,v64,v68
v_cvt_pk_bf16_f32 v161,v72,v76
v_cvt_pk_bf16_f32 v162,v112,v116
v_cvt_pk_bf16_f32 v163,v136,v140
v_cvt_pk_bf16_f32 v164,v65,v69
v_cvt_pk_bf16_f32 v165,v73,v77
v_cvt_pk_bf16_f32 v166,v113,v117
v_cvt_pk_bf16_f32 v167,v137,v141
v_cvt_pk_bf16_f32 v176,v80,v84
v_cvt_pk_bf16_f32 v177,v88,v92
v_cvt_pk_bf16_f32 v178,v120,v124
v_cvt_pk_bf16_f32 v179,v144,v148
v_cvt_pk_bf16_f32 v180,v81,v85
v_cvt_pk_bf16_f32 v181,v89,v93
v_cvt_pk_bf16_f32 v182,v121,v125
v_cvt_pk_bf16_f32 v183,v145,v149
v_cvt_pk_bf16_f32 v192,v96,v100
v_cvt_pk_bf16_f32 v193,v104,v108
v_cvt_pk_bf16_f32 v194,v128,v132
v_cvt_pk_bf16_f32 v195,v152,v156
v_cvt_pk_bf16_f32 v196,v97,v101
v_cvt_pk_bf16_f32 v197,v105,v109
v_cvt_pk_bf16_f32 v198,v129,v133
v_cvt_pk_bf16_f32 v199,v153,v157
s_waitcnt lgkmcnt(0)
v_or3_b32 v252,v232,v233,0
v_or3_b32 v252,v234,v235,v252
v_or3_b32 v252,v236,v237,v252
v_mfma_f32_16x16x32_bf16 v[44:47],a[192:195],v[160:163],v[16:19]
ds_read_b128 a[48:51],v13 offset:0
v_cvt_pk_bf16_f32 v168,v66,v70
v_cvt_pk_bf16_f32 v169,v74,v78
v_mfma_f32_16x16x32_bf16 v[44:47],a[196:199],v[176:179],v[44:47]
ds_read_b128 v[208:211],v4 offset:3584
v_cvt_pk_bf16_f32 v170,v114,v118
v_cvt_pk_bf16_f32 v171,v138,v142
v_mfma_f32_16x16x32_bf16 v[44:47],a[200:203],v[192:195],v[44:47]
ds_read_b128 a[52:55],v13 offset:256
v_cvt_pk_bf16_f32 v172,v67,v71
v_cvt_pk_bf16_f32 v173,v75,v79
v_mfma_f32_16x16x32_bf16 v[48:51],a[204:207],v[192:195],v[20:23]
ds_read_b128 v[212:215],v4 offset:3616
v_cvt_pk_bf16_f32 v174,v115,v119
v_cvt_pk_bf16_f32 v175,v139,v143
v_mfma_f32_16x16x32_bf16 v[48:51],a[208:211],v[176:179],v[48:51]
ds_read_b128 a[56:59],v13 offset:512
v_cvt_pk_bf16_f32 v184,v82,v86
v_cvt_pk_bf16_f32 v185,v90,v94
v_mfma_f32_16x16x32_bf16 v[48:51],a[212:215],v[160:163],v[48:51]
ds_read_b128 v[216:219],v4 offset:3712
v_cvt_pk_bf16_f32 v186,v122,v126
v_cvt_pk_bf16_f32 v187,v146,v150
v_mfma_f32_16x16x32_bf16 v[52:55],a[212:215],v[164:167],v[20:23]
TE01_%=:
s_cbranch_execz TP3284_%=
ds_read_b128 a[60:63],v13 offset:768
v_cvt_pk_bf16_f32 v188,v83,v87
v_cvt_pk_bf16_f32 v189,v91,v95
v_mfma_f32_16x16x32_bf16 v[52:55],a[208:211],v[180:183],v[52:55]
ds_read_b128 v[220:223],v4 offset:3744
v_cvt_pk_bf16_f32 v190,v123,v127
v_cvt_pk_bf16_f32 v191,v147,v151
v_mfma_f32_16x16x32_bf16 v[52:55],a[204:207],v[196:199],v[52:55]
ds_read_b128 a[64:67],v13 offset:1024
v_cvt_pk_bf16_f32 v200,v98,v102
v_cvt_pk_bf16_f32 v201,v106,v110
v_mfma_f32_16x16x32_bf16 v[56:59],a[200:203],v[196:199],v[16:19]
ds_read_b128 v[224:227],v4 offset:3840
v_cvt_pk_bf16_f32 v202,v130,v134
v_cvt_pk_bf16_f32 v203,v154,v158
v_mfma_f32_16x16x32_bf16 v[56:59],a[196:199],v[180:183],v[56:59]
ds_read_b128 a[68:71],v13 offset:1280
v_cvt_pk_bf16_f32 v204,v99,v103
v_cvt_pk_bf16_f32 v205,v107,v111
v_mfma_f32_16x16x32_bf16 v[56:59],a[192:195],v[164:167],v[56:59]
ds_read_b128 v[228:231],v4 offset:3872
v_cvt_pk_bf16_f32 v206,v131,v135
v_cvt_pk_bf16_f32 v207,v155,v159
v_mfma_f32_16x16x32_bf16 v[60:63],a[192:195],v[168:171],v[16:19]
ds_read_b64 v[14:15],v7 offset:0
ds_read_b64 a[0:1],v250 offset:57344
v_lshlrev_b32_sdwa v44,1,v44 dst_sel:DWORD dst_unused:UNUSED_PAD src0_sel:DWORD src1_sel:WORD_1
v_mfma_f32_16x16x32_bf16 v[60:63],a[196:199],v[184:187],v[60:63]
ds_read_b64 a[2:3],v250 offset:57352
ds_read_b64 a[12:13],v251 offset:57344
v_fma_mixlo_f16 v8,v53,s99,0
v_mfma_f32_16x16x32_bf16 v[60:63],a[200:203],v[200:203],v[60:63]
ds_read_b64 a[14:15],v251 offset:57352
ds_read_b64 a[4:5],v250 offset:57408
v_lshlrev_b32_sdwa v46,1,v46 dst_sel:DWORD dst_unused:UNUSED_PAD src0_sel:DWORD src1_sel:WORD_1
v_mfma_f32_16x16x32_bf16 v[32:35],a[204:207],v[200:203],v[20:23]
ds_read_b64 a[6:7],v250 offset:57416
ds_read_b64 a[16:17],v251 offset:57408
v_fma_mixhi_f16 v8,v55,s99,0
v_mfma_f32_16x16x32_bf16 v[32:35],a[208:211],v[184:187],v[32:35]
ds_read_b64 a[18:19],v251 offset:57416
ds_read_b64 a[8:9],v250 offset:57472
v_lshlrev_b32_sdwa v45,1,v45 dst_sel:DWORD dst_unused:UNUSED_PAD src0_sel:DWORD src1_sel:WORD_1
v_pk_fma_f16 v9,v8,v8,s54
v_mfma_f32_16x16x32_bf16 v[32:35],a[212:215],v[168:171],v[32:35]
s_waitcnt lgkmcnt(9)
ds_read_b64 a[10:11],v250 offset:57480
ds_read_b64 a[20:21],v251 offset:57472
v_or3_b32 v252,v14,v15,v252
v_lshlrev_b32_sdwa v47,1,v47 dst_sel:DWORD dst_unused:UNUSED_PAD src0_sel:DWORD src1_sel:WORD_1
v_mfma_f32_16x16x32_bf16 v[36:39],a[212:215],v[172:175],v[20:23]
ds_read_b64 v[14:15],v7 offset:512
ds_read_b128 v[20:23],v4 offset:128
v_pk_mul_f16 v9,v8,v9
v_mfma_f32_16x16x32_bf16 v[36:39],a[208:211],v[188:191],v[36:39]
ds_read_b64 a[22:23],v251 offset:57480
v_exp_f16_e64 v8,v9
v_lshlrev_b32_sdwa v48,1,v48 dst_sel:DWORD dst_unused:UNUSED_PAD src0_sel:DWORD src1_sel:WORD_1
v_mfma_f32_16x16x32_bf16 v[36:39],a[204:207],v[204:207],v[36:39]
ds_read_b64_tr_b16 a[216:217],v1 offset:0
ds_read_b64_tr_b16 a[218:219],v1 offset:1024
v_mfma_f32_16x16x32_bf16 v[40:43],a[200:203],v[204:207],v[16:19]
ds_read_b128 v[16:19],v4 offset:160
ds_read_b64_tr_b16 a[220:221],v1 offset:2048
v_lshlrev_b32_sdwa v50,1,v50 dst_sel:DWORD dst_unused:UNUSED_PAD src0_sel:DWORD src1_sel:WORD_1
v_mfma_f32_16x16x32_bf16 v[40:43],a[196:199],v[188:191],v[40:43]
ds_read_b64_tr_b16 a[222:223],v1 offset:3072
ds_read_b64_tr_b16 a[72:73],v1 offset:4096
v_lshlrev_b32_sdwa v49,1,v49 dst_sel:DWORD dst_unused:UNUSED_PAD src0_sel:DWORD src1_sel:WORD_1
v_mfma_f32_16x16x32_bf16 v[40:43],a[192:195],v[172:175],v[40:43]
s_waitcnt lgkmcnt(8)
ds_read_b64_tr_b16 a[74:75],v1 offset:5120
ds_read_u16 v44,v44
v_or3_b32 v252,v14,v15,v252
v_mfma_f32_16x16x32_bf16 v[136:139],a[48:51],a[24:27],v[208:211]
ds_read_b64 v[14:15],v7 offset:1024
ds_read_u16_d16_hi v46,v46
v_exp_f16_sdwa v8,v9 dst_sel:WORD_1 dst_unused:UNUSED_PRESERVE src0_sel:WORD_1
v_lshlrev_b32_sdwa v51,1,v51 dst_sel:DWORD dst_unused:UNUSED_PAD src0_sel:DWORD src1_sel:WORD_1
v_mfma_f32_16x16x32_bf16 v[140:143],a[48:51],a[36:39],v[208:211]
ds_read_u16 v45,v45
ds_read_u16_d16_hi v47,v47
v_lshlrev_b32_sdwa v56,1,v56 dst_sel:DWORD dst_unused:UNUSED_PAD src0_sel:DWORD src1_sel:WORD_1
v_mfma_f32_16x16x32_bf16 v[68:71],a[52:55],a[36:39],v[212:215]
ds_read_b64_tr_b16 a[76:77],v2 offset:4096
ds_read_b64_tr_b16 a[78:79],v2 offset:5120
v_pk_fma_f16 v8,v8,s55,s55
v_mfma_f32_16x16x32_bf16 v[64:67],a[52:55],a[24:27],v[212:215]
ds_read_u16 v48,v48
v_rcp_f16 v9,v8
v_lshlrev_b32_sdwa v58,1,v58 dst_sel:DWORD dst_unused:UNUSED_PAD src0_sel:DWORD src1_sel:WORD_1
v_mfma_f32_16x16x32_bf16 v[144:147],a[56:59],a[28:31],v[216:219]
ds_read_u16_d16_hi v50,v50
ds_read_u16 v49,v49
v_mfma_f32_16x16x32_bf16 v[148:151],a[56:59],a[40:43],v[216:219]
s_waitcnt lgkmcnt(8)
ds_read_u16_d16_hi v51,v51
ds_read_b64_tr_b16 a[80:81],v2 offset:2048
v_or3_b32 v252,v14,v15,v252
v_lshlrev_b32_sdwa v57,1,v57 dst_sel:DWORD dst_unused:UNUSED_PAD src0_sel:DWORD src1_sel:WORD_1
v_mfma_f32_16x16x32_bf16 v[84:87],a[60:63],a[40:43],v[220:223]
ds_read_b64_tr_b16 a[82:83],v2 offset:3072
ds_read_u16 v56,v56
v_lshlrev_b32_sdwa v59,1,v59 dst_sel:DWORD dst_unused:UNUSED_PAD src0_sel:DWORD src1_sel:WORD_1
v_mfma_f32_16x16x32_bf16 v[80:83],a[60:63],a[28:31],v[220:223]
ds_read_u16_d16_hi v58,v58
v_rcp_f16_sdwa v9,v8 dst_sel:WORD_1 dst_unused:UNUSED_PRESERVE src0_sel:WORD_1
v_mfma_f32_16x16x32_bf16 v[152:155],a[64:67],a[32:35],v[224:227]
ds_read_u16 v57,v57
ds_read_u16_d16_hi v59,v59
v_lshlrev_b32_sdwa v52,1,v52 dst_sel:DWORD dst_unused:UNUSED_PAD src0_sel:DWORD src1_sel:WORD_1
v_mfma_f32_16x16x32_bf16 v[156:159],a[64:67],a[44:47],v[224:227]
s_nop 0
ds_read_b64_tr_b16 a[84:85],v2 offset:0
ds_read_b64_tr_b16 a[86:87],v2 offset:1024
v_lshlrev_b32_sdwa v54,1,v54 dst_sel:DWORD dst_unused:UNUSED_PAD src0_sel:DWORD src1_sel:WORD_1
v_mfma_f32_16x16x32_bf16 v[100:103],a[68:71],a[44:47],v[228:231]
s_waitcnt vmcnt(7)
ds_read_u16 v52,v52
ds_read_u16_d16_hi v54,v54
v_fma_mix_f32 v53,v53,v9,0 op_sel_hi:[0,1,0]
v_mfma_f32_16x16x32_bf16 v[96:99],a[68:71],a[32:35],v[228:231]
s_barrier
ds_read_b64_tr_b16 a[224:225],v3 offset:0
ds_read_b64_tr_b16 a[226:227],v3 offset:1536
v_fma_mix_f32 v55,v55,v9,0 op_sel:[0,1,0] op_sel_hi:[0,1,0]
v_mfma_f32_16x16x32_bf16 v[24:27],a[216:219],v[160:163],v[20:23]
s_add_u32 m0,s57,0
ds_read_b64_tr_b16 a[228:229],v3 offset:8
ds_read_b64_tr_b16 a[230:231],v3 offset:1544
v_lshlrev_b32_sdwa v60,1,v60 dst_sel:DWORD dst_unused:UNUSED_PAD src0_sel:DWORD src1_sel:WORD_1
v_mfma_f32_16x16x32_bf16 v[24:27],a[220:223],v[176:179],v[24:27]
s_add_u32 s89,s56,2457600
ds_read_b64_tr_b16 a[232:233],v3 offset:64
ds_read_b64_tr_b16 a[234:235],v3 offset:1600
v_mfma_f32_16x16x32_bf16 v[24:27],a[72:75],v[192:195],v[24:27]
s_waitcnt lgkmcnt(15)
buffer_load_dwordx4 v6,s[36:39],s89 offen lds
ds_read_b64_tr_b16 a[236:237],v3 offset:72
v_lshl_add_u64 v[44:45],v[44:45],0,v[46:47]
v_mfma_f32_16x16x32_bf16 v[28:31],a[76:79],v[192:195],v[16:19]
s_waitcnt lgkmcnt(14)
ds_read_b64_tr_b16 a[238:239],v3 offset:1608
ds_read_b64_tr_b16 a[240:241],v3 offset:128
v_lshl_add_u64 v[46:47],v[48:49],0,v[50:51]
v_lshlrev_b32_sdwa v62,1,v62 dst_sel:DWORD dst_unused:UNUSED_PAD src0_sel:DWORD src1_sel:WORD_1
v_mfma_f32_16x16x32_bf16 v[28:31],a[80:83],v[176:179],v[28:31]
s_waitcnt lgkmcnt(11)
ds_read_b64_tr_b16 a[242:243],v3 offset:1664
ds_read_b64_tr_b16 a[244:245],v3 offset:136
v_lshl_add_u64 v[48:49],v[56:57],0,v[58:59]
v_mfma_f32_16x16x32_bf16 v[28:31],a[84:87],v[160:163],v[28:31]
s_add_u32 m0,s58,18480
ds_read_b64_tr_b16 a[246:247],v3 offset:1672
v_lshlrev_b32_sdwa v61,1,v61 dst_sel:DWORD dst_unused:UNUSED_PAD src0_sel:DWORD src1_sel:WORD_1
v_mfma_f32_16x16x32_bf16 v[56:59],a[84:87],v[164:167],v[16:19]
s_waitcnt lgkmcnt(12)
ds_read_u16 v60,v60
v_or_b32_e64 v50,v52,v54
v_mfma_f32_16x16x32_bf16 v[56:59],a[80:83],v[180:183],v[56:59]
s_add_u32 s69,s69,s59
ds_read_u16_d16_hi v62,v62
v_cvt_pk_bf16_f32 v51,v53,v55
v_lshlrev_b32_sdwa v63,1,v63 dst_sel:DWORD dst_unused:UNUSED_PAD src0_sel:DWORD src1_sel:WORD_1
v_mfma_f32_16x16x32_bf16 v[56:59],a[76:79],v[196:199],v[56:59]
buffer_load_dwordx4 v5,s[44:47],s69 offen offset:0 lds
v_lshlrev_b32_sdwa v32,1,v32 dst_sel:DWORD dst_unused:UNUSED_PAD src0_sel:DWORD src1_sel:WORD_1
v_mfma_f32_16x16x32_bf16 v[52:55],a[72:75],v[196:199],v[20:23]
s_add_u32 s69,s69,s53
ds_read_u16 v61,v61
v_lshlrev_b32_sdwa v34,1,v34 dst_sel:DWORD dst_unused:UNUSED_PAD src0_sel:DWORD src1_sel:WORD_1
v_mfma_f32_16x16x32_bf16 v[52:55],a[220:223],v[180:183],v[52:55]
buffer_load_dwordx4 v5,s[44:47],s69 offen offset:1024 lds
v_lshlrev_b32_sdwa v33,1,v33 dst_sel:DWORD dst_unused:UNUSED_PAD src0_sel:DWORD src1_sel:WORD_1
v_mfma_f32_16x16x32_bf16 v[52:55],a[216:219],v[164:167],v[52:55]
s_waitcnt lgkmcnt(8)
ds_read_u16_d16_hi v63,v63
v_lshlrev_b32_sdwa v35,1,v35 dst_sel:DWORD dst_unused:UNUSED_PAD src0_sel:DWORD src1_sel:WORD_1
v_mfma_f32_16x16x32_bf16 v[136:139],a[224:227],v[44:47],v[136:139]
s_add_u32 s69,s69,s53
ds_read_u16 v32,v32
ds_read_u16_d16_hi v34,v34
v_mfma_f32_16x16x32_bf16 v[140:143],a[224:227],v[48:51],v[140:143]
buffer_load_dwordx4 v5,s[44:47],s69 offen offset:2048 lds
ds_read_u16 v33,v33
v_mfma_f32_16x16x32_bf16 v[68:71],a[228:231],v[48:51],v[68:71]
ds_read_u16_d16_hi v35,v35
ds_read_b64_tr_b16 a[248:249],v3 offset:6160
v_mfma_f32_16x16x32_bf16 v[64:67],a[228:231],v[44:47],v[64:67]
ds_read_b64_tr_b16 a[250:251],v3 offset:7696
v_lshlrev_b32_sdwa v40,1,v40 dst_sel:DWORD dst_unused:UNUSED_PAD src0_sel:DWORD src1_sel:WORD_1
v_mfma_f32_16x16x32_bf16 v[144:147],a[232:235],v[44:47],v[144:147]
s_add_u32 m0,s57,3544
ds_read_u16 v40,v40
ds_read_b64_tr_b16 a[252:253],v3 offset:6168
v_mfma_f32_16x16x32_bf16 v[148:151],a[232:235],v[48:51],v[148:151]
s_waitcnt lgkmcnt(10)
ds_read_b64_tr_b16 a[254:255],v3 offset:7704
v_lshlrev_b32_sdwa v42,1,v42 dst_sel:DWORD dst_unused:UNUSED_PAD src0_sel:DWORD src1_sel:WORD_1
v_mfma_f32_16x16x32_bf16 v[84:87],a[236:239],v[48:51],v[84:87]
s_add_u32 s89,s56,2555904
ds_read_u16_d16_hi v42,v42
v_lshlrev_b32_sdwa v41,1,v41 dst_sel:DWORD dst_unused:UNUSED_PAD src0_sel:DWORD src1_sel:WORD_1
v_mfma_f32_16x16x32_bf16 v[80:83],a[236:239],v[44:47],v[80:83]
buffer_load_dwordx4 v6,s[36:39],s89 offen lds
v_lshlrev_b32_sdwa v43,1,v43 dst_sel:DWORD dst_unused:UNUSED_PAD src0_sel:DWORD src1_sel:WORD_1
v_mfma_f32_16x16x32_bf16 v[152:155],a[240:243],v[44:47],v[152:155]
ds_read_u16 v41,v41
v_lshlrev_b32_sdwa v36,1,v36 dst_sel:DWORD dst_unused:UNUSED_PAD src0_sel:DWORD src1_sel:WORD_1
v_mfma_f32_16x16x32_bf16 v[156:159],a[240:243],v[48:51],v[156:159]
ds_read_u16_d16_hi v43,v43
v_lshlrev_b32_sdwa v38,1,v38 dst_sel:DWORD dst_unused:UNUSED_PAD src0_sel:DWORD src1_sel:WORD_1
v_mfma_f32_16x16x32_bf16 v[100:103],a[244:247],v[48:51],v[100:103]
ds_read_u16 v36,v36
v_lshlrev_b32_sdwa v37,1,v37 dst_sel:DWORD dst_unused:UNUSED_PAD src0_sel:DWORD src1_sel:WORD_1
v_mfma_f32_16x16x32_bf16 v[96:99],a[244:247],v[44:47],v[96:99]
ds_read_u16_d16_hi v38,v38
v_lshlrev_b32_sdwa v39,1,v39 dst_sel:DWORD dst_unused:UNUSED_PAD src0_sel:DWORD src1_sel:WORD_1
v_mfma_f32_16x16x32_bf16 v[112:115],a[48:51],a[0:3],v[208:211]
ds_read_u16 v37,v37
v_mfma_f32_16x16x32_bf16 v[116:119],a[48:51],a[12:15],v[208:211]
ds_read_u16_d16_hi v39,v39
ds_read_b64_tr_b16 a[92:93],v3 offset:6224
v_mfma_f32_16x16x32_bf16 v[76:79],a[52:55],a[12:15],v[212:215]
ds_read_b64_tr_b16 a[94:95],v3 offset:7760
v_mfma_f32_16x16x32_bf16 v[72:75],a[52:55],a[0:3],v[212:215]
ds_read_b64_tr_b16 a[48:49],v3 offset:6232
v_mfma_f32_16x16x32_bf16 v[120:123],a[56:59],a[4:7],v[216:219]
ds_read_b64_tr_b16 a[50:51],v3 offset:7768
v_mfma_f32_16x16x32_bf16 v[124:127],a[56:59],a[16:19],v[216:219]
ds_read_b64_tr_b16 a[56:57],v3 offset:6288
v_mfma_f32_16x16x32_bf16 v[92:95],a[60:63],a[16:19],v[220:223]
ds_read_b64_tr_b16 a[58:59],v3 offset:7824
v_mfma_f32_16x16x32_bf16 v[88:91],a[60:63],a[4:7],v[220:223]
v_mfma_f32_16x16x32_bf16 v[128:131],a[64:67],a[8:11],v[224:227]
v_mfma_f32_16x16x32_bf16 v[132:135],a[64:67],a[20:23],v[224:227]
ds_read_b64_tr_b16 a[64:65],v3 offset:6296
v_mfma_f32_16x16x32_bf16 v[108:111],a[68:71],a[20:23],v[228:231]
ds_read_b64_tr_b16 a[66:67],v3 offset:7832
v_mfma_f32_16x16x32_bf16 v[104:107],a[68:71],a[8:11],v[228:231]
s_add_u32 m0,s57,7080
v_mfma_f32_16x16x32_bf16 v[48:51],a[216:219],v[168:171],v[20:23]
s_add_u32 s89,s56,2654208
ds_read_b64 v[14:15],v7 offset:6160
ds_read_b64_tr_b16 a[212:213],v1 offset:6160
v_lshlrev_b32_sdwa v24,1,v24 dst_sel:DWORD dst_unused:UNUSED_PAD src0_sel:DWORD src1_sel:WORD_1
v_mfma_f32_16x16x32_bf16 v[48:51],a[220:223],v[184:187],v[48:51]
buffer_load_dwordx4 v6,s[36:39],s89 offen lds
ds_read_b64_tr_b16 a[214:215],v1 offset:7184
v_fma_mixlo_f16 v8,v57,s99,0
v_mfma_f32_16x16x32_bf16 v[48:51],a[72:75],v[200:203],v[48:51]
s_waitcnt lgkmcnt(6)
ds_read_b64_tr_b16 a[208:209],v1 offset:8208
ds_read_b64_tr_b16 a[210:211],v1 offset:9232
v_lshl_add_u64 v[60:61],v[60:61],0,v[62:63]
v_lshlrev_b32_sdwa v26,1,v26 dst_sel:DWORD dst_unused:UNUSED_PAD src0_sel:DWORD src1_sel:WORD_1
v_mfma_f32_16x16x32_bf16 v[44:47],a[76:79],v[200:203],v[16:19]
ds_read_b64_tr_b16 a[204:205],v1 offset:10256
ds_read_b64_tr_b16 a[206:207],v1 offset:11280
v_lshl_add_u64 v[62:63],v[32:33],0,v[34:35]
v_fma_mixhi_f16 v8,v59,s99,0
v_mfma_f32_16x16x32_bf16 v[44:47],a[80:83],v[184:187],v[44:47]
ds_read_u16 v24,v24
ds_read_u16_d16_hi v26,v26
v_lshl_add_u64 v[32:33],v[40:41],0,v[42:43]
v_lshlrev_b32_sdwa v25,1,v25 dst_sel:DWORD dst_unused:UNUSED_PAD src0_sel:DWORD src1_sel:WORD_1
v_pk_fma_f16 v9,v8,v8,s54
v_mfma_f32_16x16x32_bf16 v[44:47],a[84:87],v[168:171],v[44:47]
s_waitcnt lgkmcnt(8)
ds_read_u16 v25,v25
ds_read_b64_tr_b16 a[200:201],v2 offset:10256
v_or3_b32 v252,v14,v15,v252
v_lshlrev_b32_sdwa v27,1,v27 dst_sel:DWORD dst_unused:UNUSED_PAD src0_sel:DWORD src1_sel:WORD_1
v_mfma_f32_16x16x32_bf16 v[40:43],a[84:87],v[172:175],v[16:19]
s_nop 0
ds_read_b64 v[14:15],v7 offset:6672
ds_read_b128 v[16:19],v4 offset:256
v_pk_mul_f16 v9,v8,v9
v_mfma_f32_16x16x32_bf16 v[40:43],a[80:83],v[188:191],v[40:43]
ds_read_u16_d16_hi v27,v27
ds_read_b64_tr_b16 a[202:203],v2 offset:11280
v_lshl_add_u64 v[34:35],v[36:37],0,v[38:39]
v_lshlrev_b32_sdwa v28,1,v28 dst_sel:DWORD dst_unused:UNUSED_PAD src0_sel:DWORD src1_sel:WORD_1
v_mfma_f32_16x16x32_bf16 v[40:43],a[76:79],v[204:207],v[40:43]
ds_read_u16 v28,v28
v_exp_f16 v8,v9
v_mfma_f32_16x16x32_bf16 v[36:39],a[72:75],v[204:207],v[20:23]
ds_read_b128 v[20:23],v4 offset:288
ds_read_b64_tr_b16 a[196:197],v2 offset:8208
v_lshlrev_b32_sdwa v30,1,v30 dst_sel:DWORD dst_unused:UNUSED_PAD src0_sel:DWORD src1_sel:WORD_1
v_mfma_f32_16x16x32_bf16 v[36:39],a[220:223],v[188:191],v[36:39]
ds_read_u16_d16_hi v30,v30
v_lshlrev_b32_sdwa v29,1,v29 dst_sel:DWORD dst_unused:UNUSED_PAD src0_sel:DWORD src1_sel:WORD_1
v_mfma_f32_16x16x32_bf16 v[36:39],a[216:219],v[172:175],v[36:39]
s_waitcnt lgkmcnt(7)
ds_read_u16 v29,v29
v_or3_b32 v252,v14,v15,v252
v_mfma_f32_16x16x32_bf16 v[112:115],a[224:227],v[60:63],v[112:115]
s_add_u32 m0,s57,10608
ds_read_b64 v[14:15],v7 offset:7184
v_exp_f16_sdwa v8,v9 dst_sel:WORD_1 dst_unused:UNUSED_PRESERVE src0_sel:WORD_1
v_lshlrev_b32_sdwa v31,1,v31 dst_sel:DWORD dst_unused:UNUSED_PAD src0_sel:DWORD src1_sel:WORD_1
v_mfma_f32_16x16x32_bf16 v[116:119],a[224:227],v[32:35],v[116:119]
s_add_u32 s89,s56,2752512
ds_read_u16_d16_hi v31,v31
v_lshlrev_b32_sdwa v52,1,v52 dst_sel:DWORD dst_unused:UNUSED_PAD src0_sel:DWORD src1_sel:WORD_1
v_mfma_f32_16x16x32_bf16 v[76:79],a[228:231],v[32:35],v[76:79]
buffer_load_dwordx4 v6,s[36:39],s89 offen lds
ds_read_b64_tr_b16 a[198:199],v2 offset:9232
v_pk_fma_f16 v8,v8,s55,s55
v_mfma_f32_16x16x32_bf16 v[72:75],a[228:231],v[60:63],v[72:75]
v_rcp_f16 v9,v8
v_lshlrev_b32_sdwa v54,1,v54 dst_sel:DWORD dst_unused:UNUSED_PAD src0_sel:DWORD src1_sel:WORD_1
v_mfma_f32_16x16x32_bf16 v[120:123],a[232:235],v[60:63],v[120:123]
ds_read_u16 v52,v52
v_lshlrev_b32_sdwa v53,1,v53 dst_sel:DWORD dst_unused:UNUSED_PAD src0_sel:DWORD src1_sel:WORD_1
v_mfma_f32_16x16x32_bf16 v[124:127],a[232:235],v[32:35],v[124:127]
s_waitcnt lgkmcnt(3)
ds_read_u16_d16_hi v54,v54
v_or3_b32 v252,v14,v15,v252
v_mfma_f32_16x16x32_bf16 v[92:95],a[236:239],v[32:35],v[92:95]
ds_read_u16 v53,v53
v_lshlrev_b32_sdwa v55,1,v55 dst_sel:DWORD dst_unused:UNUSED_PAD src0_sel:DWORD src1_sel:WORD_1
v_mfma_f32_16x16x32_bf16 v[88:91],a[236:239],v[60:63],v[88:91]
v_rcp_f16_sdwa v9,v8 dst_sel:WORD_1 dst_unused:UNUSED_PRESERVE src0_sel:WORD_1
v_mfma_f32_16x16x32_bf16 v[128:131],a[240:243],v[60:63],v[128:131]
ds_read_u16_d16_hi v55,v55
v_lshlrev_b32_sdwa v56,1,v56 dst_sel:DWORD dst_unused:UNUSED_PAD src0_sel:DWORD src1_sel:WORD_1
v_mfma_f32_16x16x32_bf16 v[132:135],a[240:243],v[32:35],v[132:135]
ds_read_b64_tr_b16 a[192:193],v2 offset:6160
ds_read_b64_tr_b16 a[194:195],v2 offset:7184
v_lshlrev_b32_sdwa v58,1,v58 dst_sel:DWORD dst_unused:UNUSED_PAD src0_sel:DWORD src1_sel:WORD_1
v_mfma_f32_16x16x32_bf16 v[108:111],a[244:247],v[32:35],v[108:111]
ds_read_u16 v56,v56
v_fma_mix_f32 v57,v57,v9,0 op_sel_hi:[0,1,0]
v_mfma_f32_16x16x32_bf16 v[104:107],a[244:247],v[60:63],v[104:107]
ds_read_u16_d16_hi v58,v58
v_fma_mix_f32 v59,v59,v9,0 op_sel:[0,1,0] op_sel_hi:[0,1,0]
v_mfma_f32_16x16x32_bf16 v[32:35],a[212:215],v[160:163],v[16:19]
v_lshlrev_b32_sdwa v48,1,v48 dst_sel:DWORD dst_unused:UNUSED_PAD src0_sel:DWORD src1_sel:WORD_1
v_mfma_f32_32x32x16_bf16 v[208:223],a[88:91],a[88:91],0
v_lshlrev_b32_sdwa v50,1,v50 dst_sel:DWORD dst_unused:UNUSED_PAD src0_sel:DWORD src1_sel:WORD_1
v_lshlrev_b32_sdwa v49,1,v49 dst_sel:DWORD dst_unused:UNUSED_PAD src0_sel:DWORD src1_sel:WORD_1
v_lshlrev_b32_sdwa v51,1,v51 dst_sel:DWORD dst_unused:UNUSED_PAD src0_sel:DWORD src1_sel:WORD_1
v_lshlrev_b32_sdwa v44,1,v44 dst_sel:DWORD dst_unused:UNUSED_PAD src0_sel:DWORD src1_sel:WORD_1
v_lshlrev_b32_sdwa v46,1,v46 dst_sel:DWORD dst_unused:UNUSED_PAD src0_sel:DWORD src1_sel:WORD_1
v_mfma_f32_16x16x32_bf16 v[32:35],a[208:211],v[176:179],v[32:35]
s_add_u32 m0,s57,14144
ds_read_u16 v48,v48
ds_read_u16_d16_hi v50,v50
v_mfma_f32_16x16x32_bf16 v[32:35],a[204:207],v[192:195],v[32:35]
s_add_u32 s89,s56,2850816
ds_read_u16 v49,v49
v_lshl_add_u64 v[24:25],v[24:25],0,v[26:27]
v_mfma_f32_16x16x32_bf16 v[60:63],a[200:203],v[192:195],v[20:23]
s_waitcnt lgkmcnt(8)
buffer_load_dwordx4 v6,s[36:39],s89 offen lds
v_lshl_add_u64 v[26:27],v[28:29],0,v[30:31]
v_mfma_f32_32x32x16_bf16 v[224:239],a[88:91],a[88:91],0
v_accvgpr_write_b32 a0,v160
v_accvgpr_write_b32 a1,v161
v_accvgpr_write_b32 a2,v162
v_accvgpr_write_b32 a3,v163
v_lshlrev_b32_sdwa v45,1,v45 dst_sel:DWORD dst_unused:UNUSED_PAD src0_sel:DWORD src1_sel:WORD_1
v_mfma_f32_16x16x32_bf16 v[60:63],a[196:199],v[176:179],v[60:63]
s_waitcnt lgkmcnt(5)
ds_read_u16_d16_hi v51,v51
v_lshl_add_u64 v[28:29],v[52:53],0,v[54:55]
v_mfma_f32_16x16x32_bf16 v[60:63],a[192:195],a[0:3],v[60:63]
ds_read_u16 v44,v44
v_lshlrev_b32_sdwa v47,1,v47 dst_sel:DWORD dst_unused:UNUSED_PAD src0_sel:DWORD src1_sel:WORD_1
v_mfma_f32_16x16x32_bf16 v[52:55],a[192:195],v[164:167],v[20:23]
s_waitcnt lgkmcnt(5)
ds_read_u16_d16_hi v46,v46
v_or_b32_e64 v30,v56,v58
v_mfma_f32_16x16x32_bf16 v[52:55],a[196:199],v[180:183],v[52:55]
s_waitcnt vmcnt(9)
ds_read_u16 v45,v45
v_cvt_pk_bf16_f32 v31,v57,v59
v_mfma_f32_16x16x32_bf16 v[52:55],a[200:203],v[196:199],v[52:55]
s_barrier
ds_read_u16_d16_hi v47,v47
v_accvgpr_write_b32 a4,v164
v_mfma_f32_16x16x32_bf16 v[56:59],a[204:207],v[196:199],v[16:19]
ds_read_b64_tr_b16 a[224:225],v3 offset:12320
v_lshlrev_b32_sdwa v36,1,v36 dst_sel:DWORD dst_unused:UNUSED_PAD src0_sel:DWORD src1_sel:WORD_1
v_mfma_f32_16x16x32_bf16 v[56:59],a[208:211],v[180:183],v[56:59]
ds_read_u16 v36,v36
v_accvgpr_write_b32 a5,v165
v_mfma_f32_16x16x32_bf16 v[56:59],a[212:215],v[164:167],v[56:59]
s_add_u32 m0,s58,0
ds_read_b64_tr_b16 a[226:227],v3 offset:13856
v_lshlrev_b32_sdwa v38,1,v38 dst_sel:DWORD dst_unused:UNUSED_PAD src0_sel:DWORD src1_sel:WORD_1
v_mfma_f32_16x16x32_bf16 v[136:139],a[248:251],v[24:27],v[136:139]
s_add_u32 s69,s69,s59
ds_read_u16_d16_hi v38,v38
v_accvgpr_write_b32 a6,v166
v_mfma_f32_16x16x32_bf16 v[140:143],a[248:251],v[28:31],v[140:143]
buffer_load_dwordx4 v5,s[44:47],s69 offen offset:0 lds
v_lshlrev_b32_sdwa v37,1,v37 dst_sel:DWORD dst_unused:UNUSED_PAD src0_sel:DWORD src1_sel:WORD_1
v_mfma_f32_16x16x32_bf16 v[68:71],a[252:255],v[28:31],v[68:71]
ds_read_u16 v37,v37
v_lshlrev_b32_sdwa v39,1,v39 dst_sel:DWORD dst_unused:UNUSED_PAD src0_sel:DWORD src1_sel:WORD_1
v_mfma_f32_16x16x32_bf16 v[64:67],a[252:255],v[24:27],v[64:67]
ds_read_u16_d16_hi v39,v39
v_accvgpr_write_b32 a7,v167
v_mfma_f32_16x16x32_bf16 v[144:147],a[92:95],v[24:27],v[144:147]
s_add_u32 s69,s69,s53
ds_read_b64_tr_b16 a[228:229],v3 offset:12328
v_lshlrev_b32_sdwa v40,1,v40 dst_sel:DWORD dst_unused:UNUSED_PAD src0_sel:DWORD src1_sel:WORD_1
v_mfma_f32_16x16x32_bf16 v[148:151],a[92:95],v[28:31],v[148:151]
buffer_load_dwordx4 v5,s[44:47],s69 offen offset:1024 lds
ds_read_u16 v40,v40
v_mfma_f32_16x16x32_bf16 v[84:87],a[48:51],v[28:31],v[84:87]
s_add_u32 m0,s57,17688
ds_read_b64_tr_b16 a[230:231],v3 offset:13864
ds_read_b64_tr_b16 a[232:233],v3 offset:12384
v_accvgpr_write_b32 a8,v168
v_mfma_f32_16x16x32_bf16 v[80:83],a[48:51],v[24:27],v[80:83]
s_add_u32 s89,s56,2949120
ds_read_b64_tr_b16 a[234:235],v3 offset:13920
v_lshlrev_b32_sdwa v42,1,v42 dst_sel:DWORD dst_unused:UNUSED_PAD src0_sel:DWORD src1_sel:WORD_1
v_mfma_f32_16x16x32_bf16 v[152:155],a[56:59],v[24:27],v[152:155]
buffer_load_dwordx4 v6,s[36:39],s89 offen lds
ds_read_u16_d16_hi v42,v42
v_lshlrev_b32_sdwa v41,1,v41 dst_sel:DWORD dst_unused:UNUSED_PAD src0_sel:DWORD src1_sel:WORD_1
v_accvgpr_write_b32 a9,v169
v_mfma_f32_16x16x32_bf16 v[156:159],a[56:59],v[28:31],v[156:159]
s_add_u32 m0,s58,0
ds_read_u16 v41,v41
ds_read_b64_tr_b16 a[236:237],v3 offset:12392
v_lshlrev_b32_sdwa v43,1,v43 dst_sel:DWORD dst_unused:UNUSED_PAD src0_sel:DWORD src1_sel:WORD_1
v_mfma_f32_16x16x32_bf16 v[100:103],a[64:67],v[28:31],v[100:103]
s_add_u32 s69,s69,s53
ds_read_u16_d16_hi v43,v43
ds_read_b64_tr_b16 a[238:239],v3 offset:13928
v_accvgpr_write_b32 a10,v170
v_mfma_f32_16x16x32_bf16 v[96:99],a[64:67],v[24:27],v[96:99]
buffer_load_dwordx4 v5,s[44:47],s69 offen offset:2048 lds
ds_read_b64_tr_b16 a[240:241],v3 offset:12448
v_accvgpr_write_b32 a11,v171
s_nop 1
v_mfma_f32_16x16x32_bf16 v[28:31],a[212:215],a[8:11],v[16:19]
ds_read_b64 v[14:15],v7 offset:12320
ds_read_b64_tr_b16 a[52:53],v1 offset:12320
v_lshlrev_b32_sdwa v32,1,v32 dst_sel:DWORD dst_unused:UNUSED_PAD src0_sel:DWORD src1_sel:WORD_1
v_mfma_f32_16x16x32_bf16 v[28:31],a[208:211],v[184:187],v[28:31]
s_add_u32 m0,s58,6160
ds_read_b64_tr_b16 a[54:55],v1 offset:13344
ds_read_b64_tr_b16 a[60:61],v1 offset:14368
v_fma_mixlo_f16 v8,v53,s99,0
v_mfma_f32_16x16x32_bf16 v[28:31],a[204:207],v[200:203],v[28:31]
s_waitcnt lgkmcnt(13)
ds_read_b64_tr_b16 a[62:63],v1 offset:15392
ds_read_b64_tr_b16 a[68:69],v1 offset:16416
v_lshl_add_u64 v[48:49],v[48:49],0,v[50:51]
v_lshlrev_b32_sdwa v34,1,v34 dst_sel:DWORD dst_unused:UNUSED_PAD src0_sel:DWORD src1_sel:WORD_1
v_mfma_f32_16x16x32_bf16 v[24:27],a[200:203],v[200:203],v[20:23]
s_add_u32 s69,s69,s59
ds_read_b64_tr_b16 a[70:71],v1 offset:17440
ds_read_u16 v32,v32
v_lshl_add_u64 v[50:51],v[44:45],0,v[46:47]
v_fma_mixhi_f16 v8,v55,s99,0
v_mfma_f32_16x16x32_bf16 v[24:27],a[196:199],v[184:187],v[24:27]
buffer_load_dwordx4 v5,s[44:47],s69 offen offset:0 lds
ds_read_u16_d16_hi v34,v34
v_lshl_add_u64 v[44:45],v[36:37],0,v[38:39]
v_lshlrev_b32_sdwa v33,1,v33 dst_sel:DWORD dst_unused:UNUSED_PAD src0_sel:DWORD src1_sel:WORD_1
v_pk_fma_f16 v9,v8,v8,s54
v_mfma_f32_16x16x32_bf16 v[24:27],a[192:195],a[8:11],v[24:27]
s_waitcnt lgkmcnt(8)
ds_read_u16 v33,v33
ds_read_b64_tr_b16 a[84:85],v2 offset:16416
v_or3_b32 v252,v14,v15,v252
v_lshlrev_b32_sdwa v35,1,v35 dst_sel:DWORD dst_unused:UNUSED_PAD src0_sel:DWORD src1_sel:WORD_1
v_accvgpr_write_b32 a12,v172
v_mfma_f32_16x16x32_bf16 v[36:39],a[192:195],v[172:175],v[20:23]
s_add_u32 s69,s69,s53
ds_read_b64 v[14:15],v7 offset:12832
ds_read_b128 v[20:23],v4 offset:384
v_pk_mul_f16 v9,v8,v9
v_mfma_f32_16x16x32_bf16 v[36:39],a[196:199],v[188:191],v[36:39]
buffer_load_dwordx4 v5,s[44:47],s69 offen offset:1024 lds
ds_read_u16_d16_hi v35,v35
v_lshl_add_u64 v[46:47],v[40:41],0,v[42:43]
v_lshlrev_b32_sdwa v60,1,v60 dst_sel:DWORD dst_unused:UNUSED_PAD src0_sel:DWORD src1_sel:WORD_1
v_mfma_f32_16x16x32_bf16 v[36:39],a[200:203],v[204:207],v[36:39]
s_add_u32 m0,s57,21224
ds_read_b64_tr_b16 a[86:87],v2 offset:17440
v_exp_f16_e64 v8,v9
v_mfma_f32_16x16x32_bf16 v[40:43],a[204:207],v[204:207],v[16:19]
s_add_u32 s89,s56,3047424
ds_read_b128 v[16:19],v4 offset:416
ds_read_u16 v60,v60
v_lshlrev_b32_sdwa v62,1,v62 dst_sel:DWORD dst_unused:UNUSED_PAD src0_sel:DWORD src1_sel:WORD_1
v_mfma_f32_16x16x32_bf16 v[40:43],a[208:211],v[188:191],v[40:43]
buffer_load_dwordx4 v6,s[36:39],s89 offen lds
ds_read_u16_d16_hi v62,v62
v_lshlrev_b32_sdwa v61,1,v61 dst_sel:DWORD dst_unused:UNUSED_PAD src0_sel:DWORD src1_sel:WORD_1
v_mfma_f32_16x16x32_bf16 v[40:43],a[212:215],v[172:175],v[40:43]
s_waitcnt lgkmcnt(6)
ds_read_u16 v61,v61
ds_read_b64_tr_b16 a[80:81],v2 offset:14368
v_or3_b32 v252,v14,v15,v252
v_accvgpr_write_b32 a13,v173
v_mfma_f32_16x16x32_bf16 v[112:115],a[248:251],v[48:51],v[112:115]
s_add_u32 m0,s58,6160
ds_read_b64 v[14:15],v7 offset:13344
ds_read_b64_tr_b16 a[82:83],v2 offset:15392
v_exp_f16_sdwa v8,v9 dst_sel:WORD_1 dst_unused:UNUSED_PRESERVE src0_sel:WORD_1
v_lshlrev_b32_sdwa v63,1,v63 dst_sel:DWORD dst_unused:UNUSED_PAD src0_sel:DWORD src1_sel:WORD_1
v_mfma_f32_16x16x32_bf16 v[116:119],a[248:251],v[44:47],v[116:119]
s_add_u32 s69,s69,s53
ds_read_u16_d16_hi v63,v63
ds_read_b64_tr_b16 a[76:77],v2 offset:12320
v_lshlrev_b32_sdwa v56,1,v56 dst_sel:DWORD dst_unused:UNUSED_PAD src0_sel:DWORD src1_sel:WORD_1
v_mfma_f32_16x16x32_bf16 v[76:79],a[252:255],v[44:47],v[76:79]
buffer_load_dwordx4 v5,s[44:47],s69 offen offset:2048 lds
ds_read_u16 v56,v56
v_pk_fma_f16 v8,v8,s55,s55
v_mfma_f32_16x16x32_bf16 v[72:75],a[252:255],v[48:51],v[72:75]
ds_read_b64_tr_b16 a[78:79],v2 offset:13344
v_rcp_f16_e64 v9,v8
v_lshlrev_b32_sdwa v58,1,v58 dst_sel:DWORD dst_unused:UNUSED_PAD src0_sel:DWORD src1_sel:WORD_1
v_mfma_f32_16x16x32_bf16 v[120:123],a[92:95],v[48:51],v[120:123]
ds_read_u16_d16_hi v58,v58
v_lshlrev_b32_sdwa v57,1,v57 dst_sel:DWORD dst_unused:UNUSED_PAD src0_sel:DWORD src1_sel:WORD_1
v_mfma_f32_16x16x32_bf16 v[124:127],a[92:95],v[44:47],v[124:127]
s_waitcnt lgkmcnt(6)
ds_read_u16 v57,v57
v_or3_b32 v252,v14,v15,v252
TE02_%=:
s_cbranch_execz TP3810_%=
v_accvgpr_write_b32 a14,v174
v_mfma_f32_16x16x32_bf16 v[92:95],a[48:51],v[44:47],v[92:95]
s_add_u32 m0,s57,24752
ds_read_b64_tr_b16 a[242:243],v3 offset:13984
v_lshlrev_b32_sdwa v59,1,v59 dst_sel:DWORD dst_unused:UNUSED_PAD src0_sel:DWORD src1_sel:WORD_1
v_mfma_f32_16x16x32_bf16 v[88:91],a[48:51],v[48:51],v[88:91]
s_add_u32 s89,s56,3145728
v_rcp_f16_sdwa v9,v8 dst_sel:WORD_1 dst_unused:UNUSED_PRESERVE src0_sel:WORD_1
v_mfma_f32_16x16x32_bf16 v[128:131],a[56:59],v[48:51],v[128:131]
s_nop 0
buffer_load_dwordx4 v6,s[36:39],s89 offen lds
ds_read_u16_d16_hi v59,v59
v_lshlrev_b32_sdwa v52,1,v52 dst_sel:DWORD dst_unused:UNUSED_PAD src0_sel:DWORD src1_sel:WORD_1
v_mfma_f32_16x16x32_bf16 v[132:135],a[56:59],v[44:47],v[132:135]
ds_read_u16 v52,v52
v_lshlrev_b32_sdwa v54,1,v54 dst_sel:DWORD dst_unused:UNUSED_PAD src0_sel:DWORD src1_sel:WORD_1
v_mfma_f32_16x16x32_bf16 v[108:111],a[64:67],v[44:47],v[108:111]
ds_read_u16_d16_hi v54,v54
v_fma_mix_f32 v53,v53,v9,0 op_sel_hi:[0,1,0]
v_mfma_f32_16x16x32_bf16 v[104:107],a[64:67],v[48:51],v[104:107]
ds_read_b64_tr_b16 a[244:245],v3 offset:12456
v_accvgpr_write_b32 a15,v175
v_fma_mix_f32 v55,v55,v9,0 op_sel:[0,1,0] op_sel_hi:[0,1,0]
v_mfma_f32_16x16x32_bf16 v[44:47],a[52:55],a[0:3],v[20:23]
ds_read_b64_tr_b16 a[246:247],v3 offset:13992
v_lshlrev_b32_sdwa v28,1,v28 dst_sel:DWORD dst_unused:UNUSED_PAD src0_sel:DWORD src1_sel:WORD_1
v_mfma_f32_16x16x32_bf16 v[44:47],a[60:63],v[176:179],v[44:47]
ds_read_u16 v28,v28
v_mfma_f32_32x32x16_bf16 v[160:175],a[88:91],a[88:91],0
v_lshlrev_b32_sdwa v30,1,v30 dst_sel:DWORD dst_unused:UNUSED_PAD src0_sel:DWORD src1_sel:WORD_1
v_accvgpr_write_b32 a16,v176
v_lshlrev_b32_sdwa v29,1,v29 dst_sel:DWORD dst_unused:UNUSED_PAD src0_sel:DWORD src1_sel:WORD_1
v_accvgpr_write_b32 a17,v177
v_lshlrev_b32_sdwa v31,1,v31 dst_sel:DWORD dst_unused:UNUSED_PAD src0_sel:DWORD src1_sel:WORD_1
v_mfma_f32_16x16x32_bf16 v[44:47],a[68:71],v[192:195],v[44:47]
ds_read_u16_d16_hi v30,v30
v_lshl_add_u64 v[32:33],v[32:33],0,v[34:35]
v_mfma_f32_16x16x32_bf16 v[48:51],a[84:87],v[192:195],v[16:19]
s_waitcnt lgkmcnt(7)
ds_read_u16 v29,v29
v_lshl_add_u64 v[34:35],v[60:61],0,v[62:63]
v_mfma_f32_16x16x32_bf16 v[48:51],a[80:83],v[176:179],v[48:51]
s_waitcnt lgkmcnt(7)
ds_read_u16_d16_hi v31,v31
v_lshl_add_u64 v[60:61],v[56:57],0,v[58:59]
v_mfma_f32_16x16x32_bf16 v[48:51],a[76:79],a[0:3],v[48:51]
v_lshlrev_b32_sdwa v24,1,v24 dst_sel:DWORD dst_unused:UNUSED_PAD src0_sel:DWORD src1_sel:WORD_1
v_accvgpr_write_b32 a18,v178
v_mfma_f32_16x16x32_bf16 v[56:59],a[76:79],a[4:7],v[16:19]
s_waitcnt lgkmcnt(6)
ds_read_u16 v24,v24
v_or_b32 v62,v52,v54
v_mfma_f32_16x16x32_bf16 v[56:59],a[80:83],v[180:183],v[56:59]
s_waitcnt vmcnt(13)
v_cvt_pk_bf16_f32 v63,v53,v55
v_lshlrev_b32_sdwa v26,1,v26 dst_sel:DWORD dst_unused:UNUSED_PAD src0_sel:DWORD src1_sel:WORD_1
v_mfma_f32_16x16x32_bf16 v[56:59],a[84:87],v[196:199],v[56:59]
s_barrier
ds_read_u16_d16_hi v26,v26
v_accvgpr_write_b32 a19,v179
v_mfma_f32_16x16x32_bf16 v[52:55],a[68:71],v[196:199],v[20:23]
s_add_u32 m0,s57,28288
ds_read_b64_tr_b16 a[92:93],v3 offset:18480
v_lshlrev_b32_sdwa v25,1,v25 dst_sel:DWORD dst_unused:UNUSED_PAD src0_sel:DWORD src1_sel:WORD_1
v_mfma_f32_16x16x32_bf16 v[52:55],a[60:63],v[180:183],v[52:55]
s_add_u32 s89,s56,3244032
ds_read_u16 v25,v25
v_accvgpr_write_b32 a20,v180
v_lshlrev_b32_sdwa v27,1,v27 dst_sel:DWORD dst_unused:UNUSED_PAD src0_sel:DWORD src1_sel:WORD_1
v_mfma_f32_16x16x32_bf16 v[52:55],a[52:55],a[4:7],v[52:55]
buffer_load_dwordx4 v6,s[36:39],s89 offen lds
v_accvgpr_write_b32 a21,v181
v_mfma_f32_16x16x32_bf16 v[136:139],a[224:227],v[32:35],v[136:139]
ds_read_u16_d16_hi v27,v27
v_lshlrev_b32_sdwa v40,1,v40 dst_sel:DWORD dst_unused:UNUSED_PAD src0_sel:DWORD src1_sel:WORD_1
v_mfma_f32_16x16x32_bf16 v[140:143],a[224:227],v[60:63],v[140:143]
ds_read_u16 v40,v40
v_lshlrev_b32_sdwa v42,1,v42 dst_sel:DWORD dst_unused:UNUSED_PAD src0_sel:DWORD src1_sel:WORD_1
v_accvgpr_write_b32 a22,v182
v_mfma_f32_16x16x32_bf16 v[68:71],a[228:231],v[60:63],v[68:71]
ds_read_u16_d16_hi v42,v42
v_lshlrev_b32_sdwa v41,1,v41 dst_sel:DWORD dst_unused:UNUSED_PAD src0_sel:DWORD src1_sel:WORD_1
v_mfma_f32_16x16x32_bf16 v[64:67],a[228:231],v[32:35],v[64:67]
ds_read_u16 v41,v41
v_accvgpr_write_b32 a23,v183
v_mfma_f32_16x16x32_bf16 v[144:147],a[232:235],v[32:35],v[144:147]
ds_read_b64_tr_b16 a[94:95],v3 offset:20016
ds_read_b64_tr_b16 a[48:49],v3 offset:18488
v_lshlrev_b32_sdwa v43,1,v43 dst_sel:DWORD dst_unused:UNUSED_PAD src0_sel:DWORD src1_sel:WORD_1
v_mfma_f32_16x16x32_bf16 v[148:151],a[232:235],v[60:63],v[148:151]
ds_read_u16_d16_hi v43,v43
v_accvgpr_write_b32 a24,v184
v_lshlrev_b32_sdwa v36,1,v36 dst_sel:DWORD dst_unused:UNUSED_PAD src0_sel:DWORD src1_sel:WORD_1
v_mfma_f32_16x16x32_bf16 v[84:87],a[236:239],v[60:63],v[84:87]
ds_read_u16 v36,v36
ds_read_b64_tr_b16 a[50:51],v3 offset:20024
v_accvgpr_write_b32 a25,v185
v_mfma_f32_16x16x32_bf16 v[80:83],a[236:239],v[32:35],v[80:83]
ds_read_b64_tr_b16 a[56:57],v3 offset:18544
v_lshlrev_b32_sdwa v38,1,v38 dst_sel:DWORD dst_unused:UNUSED_PAD src0_sel:DWORD src1_sel:WORD_1
v_mfma_f32_16x16x32_bf16 v[152:155],a[240:243],v[32:35],v[152:155]
ds_read_u16_d16_hi v38,v38
ds_read_b64_tr_b16 a[58:59],v3 offset:20080
v_lshlrev_b32_sdwa v37,1,v37 dst_sel:DWORD dst_unused:UNUSED_PAD src0_sel:DWORD src1_sel:WORD_1
v_accvgpr_write_b32 a26,v186
v_mfma_f32_16x16x32_bf16 v[156:159],a[240:243],v[60:63],v[156:159]
s_waitcnt lgkmcnt(11)
ds_read_u16 v37,v37
ds_read_b64_tr_b16 a[64:65],v3 offset:18552
v_lshlrev_b32_sdwa v39,1,v39 dst_sel:DWORD dst_unused:UNUSED_PAD src0_sel:DWORD src1_sel:WORD_1
v_mfma_f32_16x16x32_bf16 v[100:103],a[244:247],v[60:63],v[100:103]
s_nop 0
ds_read_u16_d16_hi v39,v39
ds_read_b64_tr_b16 a[66:67],v3 offset:20088
v_accvgpr_write_b32 a27,v187
v_mfma_f32_16x16x32_bf16 v[96:99],a[244:247],v[32:35],v[96:99]
s_add_u32 m0,s57,31832
ds_read_b64_tr_b16 a[248:249],v3 offset:18608
v_accvgpr_write_b32 a28,v188
v_mfma_f32_16x16x32_bf16 v[60:63],a[52:55],a[8:11],v[20:23]
s_add_u32 s89,s56,3342336
ds_read_b64 v[14:15],v7 offset:18480
ds_read_b64_tr_b16 a[72:73],v1 offset:18480
v_lshlrev_b32_sdwa v44,1,v44 dst_sel:DWORD dst_unused:UNUSED_PAD src0_sel:DWORD src1_sel:WORD_1
v_mfma_f32_16x16x32_bf16 v[60:63],a[60:63],a[24:27],v[60:63]
buffer_load_dwordx4 v6,s[36:39],s89 offen lds
ds_read_b64_tr_b16 a[74:75],v1 offset:19504
v_fma_mixlo_f16 v8,v57,s99,0
v_mfma_f32_16x16x32_bf16 v[60:63],a[68:71],v[200:203],v[60:63]
ds_read_b64_tr_b16 a[220:221],v1 offset:20528
ds_read_b64_tr_b16 a[222:223],v1 offset:21552
v_lshl_add_u64 v[28:29],v[28:29],0,v[30:31]
v_lshlrev_b32_sdwa v46,1,v46 dst_sel:DWORD dst_unused:UNUSED_PAD src0_sel:DWORD src1_sel:WORD_1
v_mfma_f32_16x16x32_bf16 v[32:35],a[84:87],v[200:203],v[16:19]
ds_read_b64_tr_b16 a[216:217],v1 offset:22576
ds_read_b64_tr_b16 a[218:219],v1 offset:23600
v_lshl_add_u64 v[30:31],v[24:25],0,v[26:27]
v_fma_mixhi_f16 v8,v59,s99,0
v_mfma_f32_16x16x32_bf16 v[32:35],a[80:83],a[24:27],v[32:35]
s_waitcnt lgkmcnt(14)
ds_read_u16 v44,v44
ds_read_u16_d16_hi v46,v46
v_lshl_add_u64 v[24:25],v[40:41],0,v[42:43]
v_lshlrev_b32_sdwa v45,1,v45 dst_sel:DWORD dst_unused:UNUSED_PAD src0_sel:DWORD src1_sel:WORD_1
v_pk_fma_f16 v9,v8,v8,s54
v_mfma_f32_16x16x32_bf16 v[32:35],a[76:79],a[8:11],v[32:35]
s_waitcnt lgkmcnt(8)
ds_read_u16 v45,v45
ds_read_b64_tr_b16 a[192:193],v2 offset:22576
v_or3_b32 v252,v14,v15,v252
v_lshlrev_b32_sdwa v47,1,v47 dst_sel:DWORD dst_unused:UNUSED_PAD src0_sel:DWORD src1_sel:WORD_1
v_accvgpr_write_b32 a29,v189
v_mfma_f32_16x16x32_bf16 v[40:43],a[76:79],a[12:15],v[16:19]
ds_read_b64 v[14:15],v7 offset:18992
ds_read_b128 v[16:19],v4 offset:512
v_pk_mul_f16 v9,v8,v9
v_mfma_f32_16x16x32_bf16 v[40:43],a[80:83],v[188:191],v[40:43]
ds_read_u16_d16_hi v47,v47
ds_read_b64_tr_b16 a[194:195],v2 offset:23600
v_lshl_add_u64 v[26:27],v[36:37],0,v[38:39]
v_lshlrev_b32_sdwa v48,1,v48 dst_sel:DWORD dst_unused:UNUSED_PAD src0_sel:DWORD src1_sel:WORD_1
v_mfma_f32_16x16x32_bf16 v[40:43],a[84:87],v[204:207],v[40:43]
ds_read_u16 v48,v48
v_exp_f16 v8,v9
v_mfma_f32_16x16x32_bf16 v[36:39],a[68:71],v[204:207],v[20:23]
ds_read_b128 v[20:23],v4 offset:544
ds_read_b64_tr_b16 a[196:197],v2 offset:20528
v_lshlrev_b32_sdwa v50,1,v50 dst_sel:DWORD dst_unused:UNUSED_PAD src0_sel:DWORD src1_sel:WORD_1
v_mfma_f32_16x16x32_bf16 v[36:39],a[60:63],v[188:191],v[36:39]
ds_read_u16_d16_hi v50,v50
ds_read_b64_tr_b16 a[198:199],v2 offset:21552
v_lshlrev_b32_sdwa v49,1,v49 dst_sel:DWORD dst_unused:UNUSED_PAD src0_sel:DWORD src1_sel:WORD_1
v_mfma_f32_16x16x32_bf16 v[36:39],a[52:55],a[12:15],v[36:39]
s_waitcnt lgkmcnt(8)
ds_read_u16 v49,v49
ds_read_b64_tr_b16 a[200:201],v2 offset:18480
v_or3_b32 v252,v14,v15,v252
v_accvgpr_write_b32 a30,v190
v_mfma_f32_16x16x32_bf16 v[112:115],a[224:227],v[28:31],v[112:115]
s_add_u32 m0,s57,35368
ds_read_b64 v[14:15],v7 offset:19504
ds_read_b64_tr_b16 a[202:203],v2 offset:19504
v_exp_f16_sdwa v8,v9 dst_sel:WORD_1 dst_unused:UNUSED_PRESERVE src0_sel:WORD_1
v_lshlrev_b32_sdwa v51,1,v51 dst_sel:DWORD dst_unused:UNUSED_PAD src0_sel:DWORD src1_sel:WORD_1
v_mfma_f32_16x16x32_bf16 v[116:119],a[224:227],v[24:27],v[116:119]
s_add_u32 s89,s56,3440640
ds_read_u16_d16_hi v51,v51
v_lshlrev_b32_sdwa v52,1,v52 dst_sel:DWORD dst_unused:UNUSED_PAD src0_sel:DWORD src1_sel:WORD_1
v_mfma_f32_16x16x32_bf16 v[76:79],a[228:231],v[24:27],v[76:79]
buffer_load_dwordx4 v6,s[36:39],s89 offen lds
ds_read_u16 v52,v52
v_pk_fma_f16 v8,v8,s55,s55
v_mfma_f32_16x16x32_bf16 v[72:75],a[228:231],v[28:31],v[72:75]
v_rcp_f16_e64 v9,v8
v_lshlrev_b32_sdwa v54,1,v54 dst_sel:DWORD dst_unused:UNUSED_PAD src0_sel:DWORD src1_sel:WORD_1
v_mfma_f32_16x16x32_bf16 v[120:123],a[232:235],v[28:31],v[120:123]
ds_read_u16_d16_hi v54,v54
v_lshlrev_b32_sdwa v53,1,v53 dst_sel:DWORD dst_unused:UNUSED_PAD src0_sel:DWORD src1_sel:WORD_1
v_mfma_f32_16x16x32_bf16 v[124:127],a[232:235],v[24:27],v[124:127]
s_waitcnt lgkmcnt(4)
ds_read_u16 v53,v53
v_or3_b32 v252,v14,v15,v252
v_accvgpr_write_b32 a31,v191
v_mfma_f32_16x16x32_bf16 v[92:95],a[236:239],v[24:27],v[92:95]
ds_read_b64_tr_b16 a[250:251],v3 offset:20144
v_lshlrev_b32_sdwa v55,1,v55 dst_sel:DWORD dst_unused:UNUSED_PAD src0_sel:DWORD src1_sel:WORD_1
v_mfma_f32_16x16x32_bf16 v[88:91],a[236:239],v[28:31],v[88:91]
v_rcp_f16_sdwa v9,v8 dst_sel:WORD_1 dst_unused:UNUSED_PRESERVE src0_sel:WORD_1
v_mfma_f32_16x16x32_bf16 v[128:131],a[240:243],v[28:31],v[128:131]
ds_read_u16_d16_hi v55,v55
v_lshlrev_b32_sdwa v56,1,v56 dst_sel:DWORD dst_unused:UNUSED_PAD src0_sel:DWORD src1_sel:WORD_1
v_mfma_f32_16x16x32_bf16 v[132:135],a[240:243],v[24:27],v[132:135]
ds_read_u16 v56,v56
v_lshlrev_b32_sdwa v58,1,v58 dst_sel:DWORD dst_unused:UNUSED_PAD src0_sel:DWORD src1_sel:WORD_1
v_mfma_f32_16x16x32_bf16 v[108:111],a[244:247],v[24:27],v[108:111]
ds_read_u16_d16_hi v58,v58
v_fma_mix_f32 v57,v57,v9,0 op_sel_hi:[0,1,0]
v_mfma_f32_16x16x32_bf16 v[104:107],a[244:247],v[28:31],v[104:107]
ds_read_b64_tr_b16 a[252:253],v3 offset:18616
v_mfma_f32_32x32x16_bf16 v[176:191],a[88:91],a[88:91],0
v_fma_mix_f32 v59,v59,v9,0 op_sel:[0,1,0] op_sel_hi:[0,1,0]
v_mfma_f32_16x16x32_bf16 v[24:27],a[72:75],a[0:3],v[16:19]
s_add_u32 m0,s57,38896
ds_read_b64_tr_b16 a[254:255],v3 offset:20152
v_lshlrev_b32_sdwa v60,1,v60 dst_sel:DWORD dst_unused:UNUSED_PAD src0_sel:DWORD src1_sel:WORD_1
v_mfma_f32_16x16x32_bf16 v[24:27],a[220:223],a[16:19],v[24:27]
s_add_u32 s89,s56,3538944
ds_read_u16 v60,v60
v_accvgpr_write_b32 a32,v192
v_mfma_f32_16x16x32_bf16 v[24:27],a[216:219],v[192:195],v[24:27]
buffer_load_dwordx4 v6,s[36:39],s89 offen lds
v_lshl_add_u64 v[44:45],v[44:45],0,v[46:47]
v_lshlrev_b32_sdwa v62,1,v62 dst_sel:DWORD dst_unused:UNUSED_PAD src0_sel:DWORD src1_sel:WORD_1
v_mfma_f32_16x16x32_bf16 v[28:31],a[192:195],v[192:195],v[20:23]
s_waitcnt lgkmcnt(6)
ds_read_u16_d16_hi v62,v62
v_lshl_add_u64 v[46:47],v[48:49],0,v[50:51]
v_accvgpr_write_b32 a33,v193
v_lshlrev_b32_sdwa v61,1,v61 dst_sel:DWORD dst_unused:UNUSED_PAD src0_sel:DWORD src1_sel:WORD_1
v_mfma_f32_16x16x32_bf16 v[28:31],a[196:199],a[16:19],v[28:31]
s_waitcnt lgkmcnt(6)
ds_read_u16 v61,v61
v_lshl_add_u64 v[48:49],v[52:53],0,v[54:55]
v_mfma_f32_16x16x32_bf16 v[28:31],a[200:203],a[0:3],v[28:31]
v_lshlrev_b32_sdwa v63,1,v63 dst_sel:DWORD dst_unused:UNUSED_PAD src0_sel:DWORD src1_sel:WORD_1
v_accvgpr_write_b32 a34,v194
v_mfma_f32_16x16x32_bf16 v[52:55],a[200:203],a[4:7],v[20:23]
s_waitcnt lgkmcnt(5)
ds_read_u16_d16_hi v63,v63
v_or_b32_e64 v50,v56,v58
v_lshlrev_b32_sdwa v32,1,v32 dst_sel:DWORD dst_unused:UNUSED_PAD src0_sel:DWORD src1_sel:WORD_1
v_accvgpr_write_b32 a35,v195
v_mfma_f32_16x16x32_bf16 v[52:55],a[196:199],a[20:23],v[52:55]
s_waitcnt vmcnt(9)
ds_read_u16 v32,v32
v_cvt_pk_bf16_f32 v51,v57,v59
v_lshlrev_b32_sdwa v34,1,v34 dst_sel:DWORD dst_unused:UNUSED_PAD src0_sel:DWORD src1_sel:WORD_1
v_mfma_f32_16x16x32_bf16 v[52:55],a[192:195],v[196:199],v[52:55]
s_barrier
ds_read_u16_d16_hi v34,v34
v_accvgpr_write_b32 a36,v196
v_mfma_f32_16x16x32_bf16 v[56:59],a[216:219],v[196:199],v[16:19]
ds_read_b64_tr_b16 a[224:225],v3 offset:0
v_lshlrev_b32_sdwa v33,1,v33 dst_sel:DWORD dst_unused:UNUSED_PAD src0_sel:DWORD src1_sel:WORD_1
v_mfma_f32_16x16x32_bf16 v[56:59],a[220:223],a[20:23],v[56:59]
s_add_u32 m0,s58,12320
ds_read_u16 v33,v33
v_accvgpr_write_b32 a37,v197
v_lshlrev_b32_sdwa v35,1,v35 dst_sel:DWORD dst_unused:UNUSED_PAD src0_sel:DWORD src1_sel:WORD_1
v_mfma_f32_16x16x32_bf16 v[56:59],a[72:75],a[4:7],v[56:59]
s_add_u32 s69,s69,s59
ds_read_u16_d16_hi v35,v35
v_accvgpr_write_b32 a38,v198
v_mfma_f32_16x16x32_bf16 v[136:139],a[92:95],v[44:47],v[136:139]
buffer_load_dwordx4 v5,s[44:47],s69 offen offset:0 lds
v_lshlrev_b32_sdwa v36,1,v36 dst_sel:DWORD dst_unused:UNUSED_PAD src0_sel:DWORD src1_sel:WORD_1
v_mfma_f32_16x16x32_bf16 v[140:143],a[92:95],v[48:51],v[140:143]
ds_read_u16 v36,v36
v_lshlrev_b32_sdwa v38,1,v38 dst_sel:DWORD dst_unused:UNUSED_PAD src0_sel:DWORD src1_sel:WORD_1
v_accvgpr_write_b32 a39,v199
v_mfma_f32_16x16x32_bf16 v[68:71],a[48:51],v[48:51],v[68:71]
ds_read_u16_d16_hi v38,v38
v_lshlrev_b32_sdwa v37,1,v37 dst_sel:DWORD dst_unused:UNUSED_PAD src0_sel:DWORD src1_sel:WORD_1
v_mfma_f32_16x16x32_bf16 v[64:67],a[48:51],v[44:47],v[64:67]
s_add_u32 m0,s57,42432
ds_read_u16 v37,v37
v_accvgpr_write_b32 a40,v200
v_mfma_f32_16x16x32_bf16 v[144:147],a[56:59],v[44:47],v[144:147]
s_add_u32 s89,s56,3637248
ds_read_b64_tr_b16 a[226:227],v3 offset:1536
ds_read_b64_tr_b16 a[228:229],v3 offset:8
v_lshlrev_b32_sdwa v39,1,v39 dst_sel:DWORD dst_unused:UNUSED_PAD src0_sel:DWORD src1_sel:WORD_1
v_mfma_f32_16x16x32_bf16 v[148:151],a[56:59],v[48:51],v[148:151]
buffer_load_dwordx4 v6,s[36:39],s89 offen lds
ds_read_u16_d16_hi v39,v39
v_accvgpr_write_b32 a41,v201
v_lshlrev_b32_sdwa v40,1,v40 dst_sel:DWORD dst_unused:UNUSED_PAD src0_sel:DWORD src1_sel:WORD_1
v_mfma_f32_16x16x32_bf16 v[84:87],a[64:67],v[48:51],v[84:87]
s_add_u32 m0,s58,12320
ds_read_u16 v40,v40
ds_read_b64_tr_b16 a[230:231],v3 offset:1544
v_accvgpr_write_b32 a42,v202
v_mfma_f32_16x16x32_bf16 v[80:83],a[64:67],v[44:47],v[80:83]
s_add_u32 s69,s69,s53
ds_read_b64_tr_b16 a[232:233],v3 offset:64
v_lshlrev_b32_sdwa v42,1,v42 dst_sel:DWORD dst_unused:UNUSED_PAD src0_sel:DWORD src1_sel:WORD_1
v_mfma_f32_16x16x32_bf16 v[152:155],a[248:251],v[44:47],v[152:155]
buffer_load_dwordx4 v5,s[44:47],s69 offen offset:1024 lds
ds_read_u16_d16_hi v42,v42
v_lshlrev_b32_sdwa v41,1,v41 dst_sel:DWORD dst_unused:UNUSED_PAD src0_sel:DWORD src1_sel:WORD_1
v_accvgpr_write_b32 a43,v203
v_mfma_f32_16x16x32_bf16 v[156:159],a[248:251],v[48:51],v[156:159]
s_waitcnt lgkmcnt(10)
ds_read_u16 v41,v41
ds_read_b64_tr_b16 a[234:235],v3 offset:1600
v_lshlrev_b32_sdwa v43,1,v43 dst_sel:DWORD dst_unused:UNUSED_PAD src0_sel:DWORD src1_sel:WORD_1
v_mfma_f32_16x16x32_bf16 v[100:103],a[252:255],v[48:51],v[100:103]
s_add_u32 s69,s69,s53
ds_read_u16_d16_hi v43,v43
ds_read_b64_tr_b16 a[236:237],v3 offset:72
v_accvgpr_write_b32 a44,v204
v_mfma_f32_16x16x32_bf16 v[96:99],a[252:255],v[44:47],v[96:99]
buffer_load_dwordx4 v5,s[44:47],s69 offen offset:2048 lds
ds_read_b64_tr_b16 a[238:239],v3 offset:1608
v_accvgpr_write_b32 a45,v205
v_mfma_f32_16x16x32_bf16 v[48:51],a[72:75],a[8:11],v[16:19]
ds_read_b64 v[14:15],v7 offset:0
ds_read_b64_tr_b16 a[204:205],v1 offset:0
v_lshlrev_b32_sdwa v24,1,v24 dst_sel:DWORD dst_unused:UNUSED_PAD src0_sel:DWORD src1_sel:WORD_1
v_mfma_f32_16x16x32_bf16 v[48:51],a[220:223],a[24:27],v[48:51]
ds_read_b64_tr_b16 a[206:207],v1 offset:1024
ds_read_b64_tr_b16 a[208:209],v1 offset:2048
v_fma_mixlo_f16 v8,v53,s99,0
v_mfma_f32_16x16x32_bf16 v[48:51],a[216:219],a[40:43],v[48:51]
ds_read_b64_tr_b16 a[210:211],v1 offset:3072
ds_read_b64_tr_b16 a[212:213],v1 offset:4096
v_lshl_add_u64 v[60:61],v[60:61],0,v[62:63]
v_lshlrev_b32_sdwa v26,1,v26 dst_sel:DWORD dst_unused:UNUSED_PAD src0_sel:DWORD src1_sel:WORD_1
v_mfma_f32_16x16x32_bf16 v[44:47],a[192:195],a[40:43],v[20:23]
ds_read_b64_tr_b16 a[214:215],v1 offset:5120
ds_read_u16 v24,v24
v_lshl_add_u64 v[62:63],v[32:33],0,v[34:35]
v_fma_mixhi_f16 v8,v55,s99,0
v_mfma_f32_16x16x32_bf16 v[44:47],a[196:199],a[24:27],v[44:47]
s_waitcnt lgkmcnt(14)
ds_read_u16_d16_hi v26,v26
ds_read_b64_tr_b16 a[240:241],v3 offset:128
v_lshl_add_u64 v[32:33],v[36:37],0,v[38:39]
v_lshlrev_b32_sdwa v25,1,v25 dst_sel:DWORD dst_unused:UNUSED_PAD src0_sel:DWORD src1_sel:WORD_1
v_pk_fma_f16 v9,v8,v8,s54
v_mfma_f32_16x16x32_bf16 v[44:47],a[200:203],a[8:11],v[44:47]
s_waitcnt lgkmcnt(9)
ds_read_u16 v25,v25
v_or3_b32 v252,v14,v15,v252
v_lshlrev_b32_sdwa v27,1,v27 dst_sel:DWORD dst_unused:UNUSED_PAD src0_sel:DWORD src1_sel:WORD_1
v_mfma_f32_16x16x32_bf16 v[36:39],a[200:203],a[12:15],v[20:23]
s_add_u32 m0,s57,45976
ds_read_b64 v[14:15],v7 offset:512
ds_read_b128 v[20:23],v4 offset:640
v_pk_mul_f16 v9,v8,v9
v_mfma_f32_16x16x32_bf16 v[36:39],a[196:199],a[28:31],v[36:39]
s_add_u32 s89,s56,3735552
ds_read_u16_d16_hi v27,v27
ds_read_b64_tr_b16 a[200:201],v2 offset:4096
v_lshl_add_u64 v[34:35],v[40:41],0,v[42:43]
v_lshlrev_b32_sdwa v28,1,v28 dst_sel:DWORD dst_unused:UNUSED_PAD src0_sel:DWORD src1_sel:WORD_1
v_accvgpr_write_b32 a46,v206
v_mfma_f32_16x16x32_bf16 v[36:39],a[192:195],v[204:207],v[36:39]
ds_read_b64_tr_b16 a[202:203],v2 offset:5120
v_exp_f16 v8,v9
v_mfma_f32_16x16x32_bf16 v[40:43],a[216:219],v[204:207],v[16:19]
buffer_load_dwordx4 v6,s[36:39],s89 offen lds
ds_read_b128 v[16:19],v4 offset:672
v_lshlrev_b32_sdwa v30,1,v30 dst_sel:DWORD dst_unused:UNUSED_PAD src0_sel:DWORD src1_sel:WORD_1
v_mfma_f32_16x16x32_bf16 v[40:43],a[220:223],a[28:31],v[40:43]
ds_read_u16 v28,v28
ds_read_u16_d16_hi v30,v30
v_lshlrev_b32_sdwa v29,1,v29 dst_sel:DWORD dst_unused:UNUSED_PAD src0_sel:DWORD src1_sel:WORD_1
v_mfma_f32_16x16x32_bf16 v[40:43],a[72:75],a[12:15],v[40:43]
s_waitcnt lgkmcnt(7)
ds_read_u16 v29,v29
ds_read_b64_tr_b16 a[196:197],v2 offset:2048
v_or3_b32 v252,v14,v15,v252
v_mfma_f32_16x16x32_bf16 v[112:115],a[92:95],v[60:63],v[112:115]
ds_read_b64 v[14:15],v7 offset:1024
ds_read_b64_tr_b16 a[198:199],v2 offset:3072
v_exp_f16_sdwa v8,v9 dst_sel:WORD_1 dst_unused:UNUSED_PRESERVE src0_sel:WORD_1
v_lshlrev_b32_sdwa v31,1,v31 dst_sel:DWORD dst_unused:UNUSED_PAD src0_sel:DWORD src1_sel:WORD_1
v_mfma_f32_16x16x32_bf16 v[116:119],a[92:95],v[32:35],v[116:119]
ds_read_u16_d16_hi v31,v31
ds_read_b64_tr_b16 a[192:193],v2 offset:0
v_lshlrev_b32_sdwa v56,1,v56 dst_sel:DWORD dst_unused:UNUSED_PAD src0_sel:DWORD src1_sel:WORD_1
v_mfma_f32_16x16x32_bf16 v[76:79],a[48:51],v[32:35],v[76:79]
ds_read_u16 v56,v56
ds_read_b64_tr_b16 a[194:195],v2 offset:1024
v_pk_fma_f16 v8,v8,s55,s55
v_mfma_f32_16x16x32_bf16 v[72:75],a[48:51],v[60:63],v[72:75]
v_rcp_f16_e64 v9,v8
v_lshlrev_b32_sdwa v58,1,v58 dst_sel:DWORD dst_unused:UNUSED_PAD src0_sel:DWORD src1_sel:WORD_1
v_accvgpr_write_b32 a47,v207
v_mfma_f32_16x16x32_bf16 v[120:123],a[56:59],v[60:63],v[120:123]
s_add_u32 m0,s57,49512
ds_read_u16_d16_hi v58,v58
v_lshlrev_b32_sdwa v57,1,v57 dst_sel:DWORD dst_unused:UNUSED_PAD src0_sel:DWORD src1_sel:WORD_1
v_mfma_f32_16x16x32_bf16 v[124:127],a[56:59],v[32:35],v[124:127]
s_waitcnt lgkmcnt(6)
ds_read_u16 v57,v57
v_or3_b32 v252,v14,v15,v252
v_mfma_f32_16x16x32_bf16 v[92:95],a[64:67],v[32:35],v[92:95]
s_add_u32 s89,s56,3833856
ds_read_b64_tr_b16 a[242:243],v3 offset:1664
v_and_b32 v252,0x40004000,v252
v_lshlrev_b32_sdwa v59,1,v59 dst_sel:DWORD dst_unused:UNUSED_PAD src0_sel:DWORD src1_sel:WORD_1
v_mfma_f32_16x16x32_bf16 v[88:91],a[64:67],v[60:63],v[88:91]
buffer_load_dwordx4 v6,s[36:39],s89 offen lds
v_cmp_ne_u32_e32 vcc,0,v252
s_nop 0
s_or_b64 s[90:91],s[90:91],vcc
v_mfma_f32_16x16x32_bf16 v[128:131],a[248:251],v[60:63],v[128:131]
ds_read_u16_d16_hi v59,v59
v_rcp_f16_sdwa v9,v8 dst_sel:WORD_1 dst_unused:UNUSED_PRESERVE src0_sel:WORD_1
v_lshlrev_b32_sdwa v52,1,v52 dst_sel:DWORD dst_unused:UNUSED_PAD src0_sel:DWORD src1_sel:WORD_1
v_mfma_f32_16x16x32_bf16 v[132:135],a[248:251],v[32:35],v[132:135]
ds_read_u16 v52,v52
v_lshlrev_b32_sdwa v54,1,v54 dst_sel:DWORD dst_unused:UNUSED_PAD src0_sel:DWORD src1_sel:WORD_1
v_mfma_f32_16x16x32_bf16 v[108:111],a[252:255],v[32:35],v[108:111]
ds_read_u16_d16_hi v54,v54
v_fma_mix_f32 v53,v53,v9,0 op_sel_hi:[0,1,0]
v_mfma_f32_16x16x32_bf16 v[104:107],a[252:255],v[60:63],v[104:107]
ds_read_b64_tr_b16 a[244:245],v3 offset:136
v_mfma_f32_32x32x16_bf16 v[192:207],a[88:91],a[88:91],0
v_fma_mix_f32 v55,v55,v9,0 op_sel:[0,1,0] op_sel_hi:[0,1,0]
v_mfma_f32_16x16x32_bf16 v[32:35],a[204:207],a[0:3],v[20:23]
ds_read_b64_tr_b16 a[246:247],v3 offset:1672
v_lshlrev_b32_sdwa v48,1,v48 dst_sel:DWORD dst_unused:UNUSED_PAD src0_sel:DWORD src1_sel:WORD_1
v_mfma_f32_16x16x32_bf16 v[32:35],a[208:211],a[16:19],v[32:35]
ds_read_u16 v48,v48
v_lshlrev_b32_sdwa v50,1,v50 dst_sel:DWORD dst_unused:UNUSED_PAD src0_sel:DWORD src1_sel:WORD_1
v_mfma_f32_16x16x32_bf16 v[32:35],a[212:215],a[32:35],v[32:35]
ds_read_u16_d16_hi v50,v50
v_lshl_add_u64 v[24:25],v[24:25],0,v[26:27]
v_mfma_f32_16x16x32_bf16 v[60:63],a[200:203],a[32:35],v[16:19]
s_waitcnt lgkmcnt(7)
v_lshl_add_u64 v[26:27],v[28:29],0,v[30:31]
v_lshlrev_b32_sdwa v49,1,v49 dst_sel:DWORD dst_unused:UNUSED_PAD src0_sel:DWORD src1_sel:WORD_1
v_mfma_f32_16x16x32_bf16 v[60:63],a[196:199],a[16:19],v[60:63]
s_waitcnt lgkmcnt(6)
ds_read_u16 v49,v49
v_lshl_add_u64 v[28:29],v[56:57],0,v[58:59]
v_mfma_f32_16x16x32_bf16 v[60:63],a[192:195],a[0:3],v[60:63]
v_lshlrev_b32_sdwa v51,1,v51 dst_sel:DWORD dst_unused:UNUSED_PAD src0_sel:DWORD src1_sel:WORD_1
v_lshlrev_b32_sdwa v44,1,v44 dst_sel:DWORD dst_unused:UNUSED_PAD src0_sel:DWORD src1_sel:WORD_1
v_mfma_f32_16x16x32_bf16 v[56:59],a[192:195],a[4:7],v[16:19]
s_waitcnt lgkmcnt(5)
ds_read_u16_d16_hi v51,v51
v_or_b32 v30,v52,v54
v_mfma_f32_16x16x32_bf16 v[56:59],a[196:199],a[20:23],v[56:59]
s_waitcnt vmcnt(11)
ds_read_u16 v44,v44
v_cvt_pk_bf16_f32 v31,v53,v55
v_lshlrev_b32_sdwa v46,1,v46 dst_sel:DWORD dst_unused:UNUSED_PAD src0_sel:DWORD src1_sel:WORD_1
v_mfma_f32_16x16x32_bf16 v[56:59],a[200:203],a[36:39],v[56:59]
s_barrier
ds_read_u16_d16_hi v46,v46
v_lshlrev_b32_sdwa v45,1,v45 dst_sel:DWORD dst_unused:UNUSED_PAD src0_sel:DWORD src1_sel:WORD_1
v_mfma_f32_16x16x32_bf16 v[52:55],a[212:215],a[36:39],v[20:23]
s_add_u32 m0,s57,53040
ds_read_u16 v45,v45
v_lshlrev_b32_sdwa v47,1,v47 dst_sel:DWORD dst_unused:UNUSED_PAD src0_sel:DWORD src1_sel:WORD_1
v_mfma_f32_16x16x32_bf16 v[52:55],a[208:211],a[20:23],v[52:55]
s_add_u32 s89,s56,3932160
ds_read_u16_d16_hi v47,v47
v_mfma_f32_32x32x16_bf16 v[240:255],a[88:91],a[88:91],0
v_lshlrev_b32_sdwa v40,1,v40 dst_sel:DWORD dst_unused:UNUSED_PAD src0_sel:DWORD src1_sel:WORD_1
v_lshlrev_b32_sdwa v42,1,v42 dst_sel:DWORD dst_unused:UNUSED_PAD src0_sel:DWORD src1_sel:WORD_1
v_lshlrev_b32_sdwa v41,1,v41 dst_sel:DWORD dst_unused:UNUSED_PAD src0_sel:DWORD src1_sel:WORD_1
v_lshlrev_b32_sdwa v43,1,v43 dst_sel:DWORD dst_unused:UNUSED_PAD src0_sel:DWORD src1_sel:WORD_1
v_lshlrev_b32_sdwa v36,1,v36 dst_sel:DWORD dst_unused:UNUSED_PAD src0_sel:DWORD src1_sel:WORD_1
v_mfma_f32_16x16x32_bf16 v[52:55],a[204:207],a[4:7],v[52:55]
buffer_load_dwordx4 v6,s[36:39],s89 offen lds
ds_read_u16 v40,v40
v_mfma_f32_16x16x32_bf16 v[136:139],a[224:227],v[24:27],v[136:139]
s_add_u32 m0,s58,18480
ds_read_u16_d16_hi v42,v42
ds_read_u16 v41,v41
v_mfma_f32_16x16x32_bf16 v[140:143],a[224:227],v[28:31],v[140:143]
s_add_u32 s69,s69,s59
ds_read_u16_d16_hi v43,v43
v_lshlrev_b32_sdwa v38,1,v38 dst_sel:DWORD dst_unused:UNUSED_PAD src0_sel:DWORD src1_sel:WORD_1
v_mfma_f32_16x16x32_bf16 v[68:71],a[228:231],v[28:31],v[68:71]
buffer_load_dwordx4 v5,s[44:47],s69 offen offset:0 lds
ds_read_u16 v36,v36
v_mfma_f32_16x16x32_bf16 v[64:67],a[228:231],v[24:27],v[64:67]
s_add_u32 s69,s69,s53
ds_read_u16_d16_hi v38,v38
ds_read_b64_tr_b16 a[248:249],v3 offset:6160
v_mfma_f32_16x16x32_bf16 v[144:147],a[232:235],v[24:27],v[144:147]
buffer_load_dwordx4 v5,s[44:47],s69 offen offset:1024 lds
v_lshlrev_b32_sdwa v37,1,v37 dst_sel:DWORD dst_unused:UNUSED_PAD src0_sel:DWORD src1_sel:WORD_1
v_mfma_f32_16x16x32_bf16 v[148:151],a[232:235],v[28:31],v[148:151]
ds_read_u16 v37,v37
v_lshlrev_b32_sdwa v39,1,v39 dst_sel:DWORD dst_unused:UNUSED_PAD src0_sel:DWORD src1_sel:WORD_1
v_mfma_f32_16x16x32_bf16 v[84:87],a[236:239],v[28:31],v[84:87]
ds_read_u16_d16_hi v39,v39
v_mfma_f32_16x16x32_bf16 v[80:83],a[236:239],v[24:27],v[80:83]
ds_read_b64_tr_b16 a[250:251],v3 offset:7696
v_mfma_f32_16x16x32_bf16 v[152:155],a[240:243],v[24:27],v[152:155]
s_add_u32 s69,s69,s53
ds_read_b64_tr_b16 a[252:253],v3 offset:6168
v_mfma_f32_16x16x32_bf16 v[156:159],a[240:243],v[28:31],v[156:159]
s_waitcnt lgkmcnt(8)
buffer_load_dwordx4 v5,s[44:47],s69 offen offset:2048 lds
v_mfma_f32_16x16x32_bf16 v[100:103],a[244:247],v[28:31],v[100:103]
ds_read_b64_tr_b16 a[254:255],v3 offset:7704
v_mfma_f32_16x16x32_bf16 v[96:99],a[244:247],v[24:27],v[96:99]
s_waitcnt vmcnt(13)
s_barrier
v_add_u32_sdwa v24,s60,v10 dst_sel:DWORD dst_unused:UNUSED_PAD src0_sel:DWORD src1_sel:WORD_1
v_subrev_u32 v8,88400,v24
v_min_u32_e64 v24,v24,v8
v_add_u32 v25,7072,v24
v_subrev_u32 v8,88400,v25
v_min_u32_e64 v25,v25,v8
v_lshl_add_u64 v[48:49],v[48:49],0,v[50:51]
v_lshl_add_u64 v[50:51],v[44:45],0,v[46:47]
s_waitcnt lgkmcnt(8)
v_lshl_add_u64 v[44:45],v[40:41],0,v[42:43]
s_waitcnt lgkmcnt(3)
v_lshl_add_u64 v[46:47],v[36:37],0,v[38:39]
v_mfma_f32_16x16x32_bf16 v[112:115],a[224:227],v[48:51],v[112:115]
s_add_u32 m0,s58,0
s_add_u32 s69,s69,s59
buffer_load_dwordx4 v5,s[44:47],s69 offen offset:0 lds
v_xad_u32 v28,v11,0,v24
v_xad_u32 v29,v11,8,v25
v_mfma_f32_16x16x32_bf16 v[116:119],a[224:227],v[44:47],v[116:119]
TE03_%=:
s_cbranch_execz TP4341_%=
v_xad_u32 v30,v11,16,v24
v_xad_u32 v31,v11,24,v25
v_mfma_f32_16x16x32_bf16 v[76:79],a[228:231],v[44:47],v[76:79]
ds_read_b64 a[48:49],v28 offset:0
ds_read_b64 a[50:51],v28 offset:192
v_mfma_f32_16x16x32_bf16 v[72:75],a[228:231],v[48:51],v[72:75]
ds_read_b64 a[52:53],v29 offset:0
ds_read_b64 a[54:55],v29 offset:192
v_mfma_f32_16x16x32_bf16 v[120:123],a[232:235],v[48:51],v[120:123]
ds_read_b64 a[56:57],v28 offset:8
ds_read_b64 a[58:59],v28 offset:200
v_mfma_f32_16x16x32_bf16 v[124:127],a[232:235],v[44:47],v[124:127]
s_add_u32 s69,s69,s53
buffer_load_dwordx4 v5,s[44:47],s69 offen offset:1024 lds
ds_read_b64 a[60:61],v29 offset:8
ds_read_b64 a[62:63],v29 offset:200
v_mfma_f32_16x16x32_bf16 v[92:95],a[236:239],v[44:47],v[92:95]
v_mfma_f32_16x16x32_bf16 v[88:91],a[236:239],v[48:51],v[88:91]
v_mfma_f32_16x16x32_bf16 v[128:131],a[240:243],v[48:51],v[128:131]
v_mfma_f32_16x16x32_bf16 v[132:135],a[240:243],v[44:47],v[132:135]
v_mfma_f32_16x16x32_bf16 v[108:111],a[244:247],v[44:47],v[108:111]
v_mfma_f32_16x16x32_bf16 v[104:107],a[244:247],v[48:51],v[104:107]
s_nop 0
ds_read_b64 a[64:65],v30 offset:0
ds_read_b64 a[66:67],v30 offset:192
ds_read_b64 a[68:69],v31 offset:0
ds_read_b64 a[70:71],v31 offset:192
s_waitcnt lgkmcnt(8)
v_smfmac_f32_16x16x64_bf16 v[160:163],a[96:99],a[48:55],v10
s_waitcnt lgkmcnt(4)
s_add_u32 s69,s69,s53
buffer_load_dwordx4 v5,s[44:47],s69 offen offset:2048 lds
ds_read_b64 a[48:49],v30 offset:8
ds_read_b64 a[50:51],v30 offset:200
ds_read_b64 a[52:53],v31 offset:8
ds_read_b64 a[54:55],v31 offset:200
v_smfmac_f32_16x16x64_bf16 v[164:167],a[100:103],a[56:63],v10
s_waitcnt lgkmcnt(4)
ds_read_b64 a[56:57],v28 offset:64
ds_read_b64 a[58:59],v28 offset:256
ds_read_b64 a[60:61],v29 offset:64
ds_read_b64 a[62:63],v29 offset:256
v_smfmac_f32_16x16x64_bf16 v[168:171],a[104:107],a[64:71],v10
s_waitcnt lgkmcnt(4)
ds_read_b64 a[64:65],v28 offset:72
ds_read_b64 a[66:67],v28 offset:264
ds_read_b64 a[68:69],v29 offset:72
ds_read_b64 a[70:71],v29 offset:264
v_and_b32 v48,8,v11
v_pk_mul_f32 v[44:45],v[160:161],v[160:161]
v_pk_mul_f32 v[46:47],v[162:163],v[162:163]
v_smfmac_f32_16x16x64_bf16 v[172:175],a[108:111],a[48:55],v10
s_waitcnt lgkmcnt(4)
ds_read_b64 a[48:49],v30 offset:64
ds_read_b64 a[50:51],v30 offset:256
ds_read_b64 a[52:53],v31 offset:64
ds_read_b64 a[54:55],v31 offset:256
v_mul_u32_u24 v48,884,v48
s_add_u32 s64,s60,3536
v_pk_add_f32 v[36:37],v[160:161],v[164:165]
v_pk_fma_f32 v[44:45],v[164:165],v[164:165],v[44:45]
v_pk_add_f32 v[38:39],v[162:163],v[166:167]
v_pk_fma_f32 v[46:47],v[166:167],v[166:167],v[46:47]
v_smfmac_f32_16x16x64_bf16 v[176:179],a[128:131],a[56:63],v10
s_waitcnt lgkmcnt(4)
ds_read_b64 a[56:57],v30 offset:72
ds_read_b64 a[58:59],v30 offset:264
ds_read_b64 a[60:61],v31 offset:72
ds_read_b64 a[62:63],v31 offset:264
v_add_u32 v51,s64,v48
v_pk_add_f32 v[36:37],v[36:37],v[168:169]
v_pk_fma_f32 v[44:45],v[168:169],v[168:169],v[44:45]
v_pk_add_f32 v[38:39],v[38:39],v[170:171]
v_pk_fma_f32 v[46:47],v[170:171],v[170:171],v[46:47]
v_smfmac_f32_16x16x64_bf16 v[180:183],a[132:135],a[64:71],v10
s_waitcnt lgkmcnt(4)
ds_read_b64 a[64:65],v28 offset:128
ds_read_b64 a[66:67],v28 offset:320
ds_read_b64 a[68:69],v29 offset:128
ds_read_b64 a[70:71],v29 offset:320
v_subrev_u32 v8,88400,v51
v_pk_add_f32 v[36:37],v[36:37],v[172:173]
v_pk_fma_f32 v[44:45],v[172:173],v[172:173],v[44:45]
v_pk_add_f32 v[38:39],v[38:39],v[174:175]
v_pk_fma_f32 v[46:47],v[174:175],v[174:175],v[46:47]
v_smfmac_f32_16x16x64_bf16 v[184:187],a[136:139],a[48:55],v10
s_waitcnt lgkmcnt(4)
ds_read_b64 a[48:49],v28 offset:136
ds_read_b64 a[50:51],v28 offset:328
ds_read_b64 a[52:53],v29 offset:136
ds_read_b64 a[54:55],v29 offset:328
v_min_u32 v51,v51,v8
v_pk_add_f32 v[36:37],v[36:37],v[176:177]
v_pk_fma_f32 v[44:45],v[176:177],v[176:177],v[44:45]
v_pk_add_f32 v[38:39],v[38:39],v[178:179]
v_pk_fma_f32 v[46:47],v[178:179],v[178:179],v[46:47]
v_smfmac_f32_16x16x64_bf16 v[188:191],a[140:143],a[56:63],v10
s_waitcnt lgkmcnt(4)
ds_read_b64 a[56:57],v30 offset:128
ds_read_b64 a[58:59],v30 offset:320
ds_read_b64 a[60:61],v31 offset:128
ds_read_b64 a[62:63],v31 offset:320
v_xad_u32 v28,v11,32,v24
v_xad_u32 v29,v11,40,v25
v_pk_add_f32 v[36:37],v[36:37],v[180:181]
v_pk_fma_f32 v[44:45],v[180:181],v[180:181],v[44:45]
v_pk_add_f32 v[38:39],v[38:39],v[182:183]
v_pk_fma_f32 v[46:47],v[182:183],v[182:183],v[46:47]
v_smfmac_f32_16x16x64_bf16 v[192:195],a[160:163],a[64:71],v10
s_waitcnt lgkmcnt(4)
ds_read_b64 a[64:65],v30 offset:136
ds_read_b64 a[66:67],v30 offset:328
ds_read_b64 a[68:69],v31 offset:136
ds_read_b64 a[70:71],v31 offset:328
v_add_u32 v50,3536,v51
v_pk_add_f32 v[36:37],v[36:37],v[184:185]
v_pk_fma_f32 v[44:45],v[184:185],v[184:185],v[44:45]
v_pk_add_f32 v[38:39],v[38:39],v[186:187]
v_pk_fma_f32 v[46:47],v[186:187],v[186:187],v[46:47]
v_smfmac_f32_16x16x64_bf16 v[196:199],a[164:167],a[48:55],v10
s_waitcnt lgkmcnt(4)
ds_read_b64 a[48:49],v28 offset:0
ds_read_b64 a[50:51],v28 offset:192
ds_read_b64 a[52:53],v29 offset:0
ds_read_b64 a[54:55],v29 offset:192
v_xad_u32 v30,v11,48,v24
v_xad_u32 v31,v11,56,v25
v_pk_add_f32 v[36:37],v[36:37],v[188:189]
v_pk_fma_f32 v[44:45],v[188:189],v[188:189],v[44:45]
v_pk_add_f32 v[38:39],v[38:39],v[190:191]
v_pk_fma_f32 v[46:47],v[190:191],v[190:191],v[46:47]
v_smfmac_f32_16x16x64_bf16 v[200:203],a[168:171],a[56:63],v10
s_waitcnt lgkmcnt(4)
ds_read_b64 a[56:57],v28 offset:8
ds_read_b64 a[58:59],v28 offset:200
ds_read_b64 a[60:61],v29 offset:8
ds_read_b64 a[62:63],v29 offset:200
v_subrev_u32 v8,88400,v50
v_pk_add_f32 v[36:37],v[36:37],v[192:193]
v_pk_fma_f32 v[44:45],v[192:193],v[192:193],v[44:45]
v_pk_add_f32 v[38:39],v[38:39],v[194:195]
v_pk_fma_f32 v[46:47],v[194:195],v[194:195],v[46:47]
v_smfmac_f32_16x16x64_bf16 v[204:207],a[172:175],a[64:71],v10
s_waitcnt lgkmcnt(4)
ds_read_b64 a[64:65],v28 offset:64
ds_read_b64 a[66:67],v28 offset:256
ds_read_b64 a[68:69],v29 offset:64
ds_read_b64 a[70:71],v29 offset:256
v_min_u32 v50,v50,v8
v_pk_add_f32 v[36:37],v[36:37],v[196:197]
v_pk_fma_f32 v[44:45],v[196:197],v[196:197],v[44:45]
v_pk_add_f32 v[38:39],v[38:39],v[198:199]
v_pk_fma_f32 v[46:47],v[198:199],v[198:199],v[46:47]
v_smfmac_f32_16x16x64_bf16 v[208:211],a[112:115],a[48:55],v10
s_waitcnt lgkmcnt(4)
ds_read_b64 a[48:49],v28 offset:72
ds_read_b64 a[50:51],v28 offset:264
ds_read_b64 a[52:53],v29 offset:72
ds_read_b64 a[54:55],v29 offset:264
v_xor_b32 v9,0x80008,v12
v_pk_add_f32 v[36:37],v[36:37],v[200:201]
v_pk_fma_f32 v[44:45],v[200:201],v[200:201],v[44:45]
v_pk_add_f32 v[38:39],v[38:39],v[202:203]
v_pk_fma_f32 v[46:47],v[202:203],v[202:203],v[46:47]
v_smfmac_f32_16x16x64_bf16 v[212:215],a[116:119],a[56:63],v10
s_waitcnt lgkmcnt(4)
ds_read_b64 a[56:57],v28 offset:128
ds_read_b64 a[58:59],v28 offset:320
ds_read_b64 a[60:61],v29 offset:128
ds_read_b64 a[62:63],v29 offset:320
v_add_u32_sdwa v48,v12,v51 dst_sel:DWORD dst_unused:UNUSED_PAD src0_sel:WORD_0 src1_sel:DWORD
v_pk_add_f32 v[36:37],v[36:37],v[204:205]
v_pk_fma_f32 v[44:45],v[204:205],v[204:205],v[44:45]
v_pk_add_f32 v[38:39],v[38:39],v[206:207]
v_pk_fma_f32 v[46:47],v[206:207],v[206:207],v[46:47]
v_smfmac_f32_16x16x64_bf16 v[216:219],a[144:147],a[64:71],v10
s_waitcnt lgkmcnt(4)
ds_read_b64 a[64:65],v28 offset:136
ds_read_b64 a[66:67],v28 offset:328
ds_read_b64 a[68:69],v29 offset:136
ds_read_b64 a[70:71],v29 offset:328
v_add_u32_sdwa v49,v12,v51 dst_sel:DWORD dst_unused:UNUSED_PAD src0_sel:WORD_1 src1_sel:DWORD
v_pk_add_f32 v[36:37],v[36:37],v[208:209]
v_pk_fma_f32 v[44:45],v[208:209],v[208:209],v[44:45]
v_pk_add_f32 v[38:39],v[38:39],v[210:211]
v_pk_fma_f32 v[46:47],v[210:211],v[210:211],v[46:47]
v_smfmac_f32_16x16x64_bf16 v[220:223],a[148:151],a[48:55],v10
s_waitcnt lgkmcnt(4)
ds_read_b64 a[48:49],v30 offset:0
ds_read_b64 a[50:51],v30 offset:192
ds_read_b64 a[52:53],v31 offset:0
ds_read_b64 a[54:55],v31 offset:192
v_add_u32_sdwa v51,v9,v50 dst_sel:DWORD dst_unused:UNUSED_PAD src0_sel:WORD_1 src1_sel:DWORD
v_pk_add_f32 v[36:37],v[36:37],v[212:213]
v_pk_fma_f32 v[44:45],v[212:213],v[212:213],v[44:45]
v_pk_add_f32 v[38:39],v[38:39],v[214:215]
v_pk_fma_f32 v[46:47],v[214:215],v[214:215],v[46:47]
v_smfmac_f32_16x16x64_bf16 v[224:227],a[176:179],a[56:63],v10
s_waitcnt lgkmcnt(4)
ds_read_b64 a[56:57],v30 offset:8
ds_read_b64 a[58:59],v30 offset:200
ds_read_b64 a[60:61],v31 offset:8
ds_read_b64 a[62:63],v31 offset:200
v_add_u32_sdwa v50,v9,v50 dst_sel:DWORD dst_unused:UNUSED_PAD src0_sel:WORD_0 src1_sel:DWORD
v_pk_add_f32 v[36:37],v[36:37],v[216:217]
v_pk_fma_f32 v[44:45],v[216:217],v[216:217],v[44:45]
v_pk_add_f32 v[38:39],v[38:39],v[218:219]
v_pk_fma_f32 v[46:47],v[218:219],v[218:219],v[46:47]
v_smfmac_f32_16x16x64_bf16 v[228:231],a[180:183],a[64:71],v10
s_waitcnt lgkmcnt(4)
ds_read_b64 a[64:65],v30 offset:64
ds_read_b64 a[66:67],v30 offset:256
ds_read_b64 a[68:69],v31 offset:64
ds_read_b64 a[70:71],v31 offset:256
v_pk_add_f32 v[36:37],v[36:37],v[220:221]
v_pk_fma_f32 v[44:45],v[220:221],v[220:221],v[44:45]
v_pk_add_f32 v[38:39],v[38:39],v[222:223]
v_pk_fma_f32 v[46:47],v[222:223],v[222:223],v[46:47]
v_smfmac_f32_16x16x64_bf16 v[232:235],a[120:123],a[48:55],v10
s_waitcnt lgkmcnt(4)
ds_read_b64 a[48:49],v30 offset:72
ds_read_b64 a[50:51],v30 offset:264
ds_read_b64 a[52:53],v31 offset:72
ds_read_b64 a[54:55],v31 offset:264
v_pk_add_f32 v[36:37],v[36:37],v[224:225]
v_pk_fma_f32 v[44:45],v[224:225],v[224:225],v[44:45]
v_pk_add_f32 v[38:39],v[38:39],v[226:227]
v_pk_fma_f32 v[46:47],v[226:227],v[226:227],v[46:47]
v_smfmac_f32_16x16x64_bf16 v[236:239],a[124:127],a[56:63],v10
s_waitcnt lgkmcnt(4)
ds_read_b64 a[56:57],v30 offset:128
ds_read_b64 a[58:59],v30 offset:320
ds_read_b64 a[60:61],v31 offset:128
ds_read_b64 a[62:63],v31 offset:320
v_pk_add_f32 v[36:37],v[36:37],v[228:229]
v_pk_fma_f32 v[44:45],v[228:229],v[228:229],v[44:45]
v_pk_add_f32 v[38:39],v[38:39],v[230:231]
v_pk_fma_f32 v[46:47],v[230:231],v[230:231],v[46:47]
v_smfmac_f32_16x16x64_bf16 v[240:243],a[152:155],a[64:71],v10
s_waitcnt lgkmcnt(4)
ds_read_b64 a[64:65],v30 offset:136
ds_read_b64 a[66:67],v30 offset:328
ds_read_b64 a[68:69],v31 offset:136
ds_read_b64 a[70:71],v31 offset:328
v_pk_add_f32 v[36:37],v[36:37],v[232:233]
v_pk_fma_f32 v[44:45],v[232:233],v[232:233],v[44:45]
v_pk_add_f32 v[38:39],v[38:39],v[234:235]
v_pk_fma_f32 v[46:47],v[234:235],v[234:235],v[46:47]
v_smfmac_f32_16x16x64_bf16 v[244:247],a[156:159],a[48:55],v10
s_waitcnt lgkmcnt(4)
ds_read_b64 a[224:225],v48 offset:57344
ds_read_b64 a[226:227],v48 offset:57352
ds_read_b64 a[228:229],v49 offset:57344
ds_read_b64 a[230:231],v49 offset:57352
ds_read_b128 a[240:243],v13 offset:0
v_pk_add_f32 v[36:37],v[36:37],v[236:237]
v_pk_fma_f32 v[44:45],v[236:237],v[236:237],v[44:45]
v_pk_add_f32 v[38:39],v[38:39],v[238:239]
v_pk_fma_f32 v[46:47],v[238:239],v[238:239],v[46:47]
v_smfmac_f32_16x16x64_bf16 v[248:251],a[184:187],a[56:63],v10
s_waitcnt lgkmcnt(5)
ds_read_b64 a[232:233],v50 offset:57344
ds_read_b64 a[234:235],v50 offset:57352
ds_read_b64 a[236:237],v51 offset:57344
ds_read_b64 a[238:239],v51 offset:57352
ds_read_b128 a[244:247],v13 offset:256
v_pk_add_f32 v[36:37],v[36:37],v[240:241]
v_pk_fma_f32 v[44:45],v[240:241],v[240:241],v[44:45]
v_pk_add_f32 v[38:39],v[38:39],v[242:243]
v_pk_fma_f32 v[46:47],v[242:243],v[242:243],v[46:47]
v_smfmac_f32_16x16x64_bf16 v[252:255],a[188:191],a[64:71],v10
ds_read_b128 v[28:31],v4 offset:3584
ds_read_b128 v[24:27],v4 offset:3616
v_pk_add_f32 v[36:37],v[36:37],v[244:245]
v_pk_fma_f32 v[44:45],v[244:245],v[244:245],v[44:45]
v_pk_add_f32 v[38:39],v[38:39],v[246:247]
v_pk_fma_f32 v[46:47],v[246:247],v[246:247],v[46:47]
v_pk_add_f32 v[36:37],v[36:37],v[248:249]
v_pk_fma_f32 v[44:45],v[248:249],v[248:249],v[44:45]
v_pk_add_f32 v[38:39],v[38:39],v[250:251]
v_pk_fma_f32 v[46:47],v[250:251],v[250:251],v[46:47]
v_pk_add_f32 v[36:37],v[36:37],v[252:253]
v_pk_fma_f32 v[44:45],v[252:253],v[252:253],v[44:45]
v_pk_add_f32 v[38:39],v[38:39],v[254:255]
v_pk_fma_f32 v[46:47],v[254:255],v[254:255],v[46:47]
s_nop 0
v_permlane32_swap_b32 v36,v38
v_permlane32_swap_b32 v37,v39
v_permlane32_swap_b32 v44,v46
v_permlane32_swap_b32_e64 v45,v47
v_pk_add_f32 v[36:37],v[36:37],v[38:39]
v_pk_add_f32 v[44:45],v[44:45],v[46:47]
s_add_u32 s60,s60,56576
s_sub_u32 s64,s60,88400
s_min_u32 s60,s60,s64
v_permlane16_swap_b32 v36,v37
v_permlane16_swap_b32 v44,v45
v_add_f32 v8,v36,v37
v_add_f32_e64 v9,v44,v45
v_mul_f32 v8,0x3c2aaaab,v8
v_mul_f32 v9,0x3c2aaaab,v9
v_cmp_ngt_f32 vcc,0x5d800000,v9
v_fma_f32 v9,-v8,v8,v9
v_max_f32 v9,0,v9
s_or_b64 s[90:91],s[90:91],vcc
v_add_f32 v9,0x3727c5ac,v9
v_rsq_f32 v9,v9
s_nop 0
v_mul_f32 v36,s96,v9
v_mul_f32 v37,s96,v9
v_mul_f32 v44,-v8,v36
v_mul_f32 v45,-v8,v36
v_permlane16_swap_b32 v36,v37
s_nop 0
v_permlane16_swap_b32 v44,v45
v_mov_b64 v[38:39],v[36:37]
v_mov_b64 v[46:47],v[44:45]
s_nop 0
v_permlane32_swap_b32 v36,v38
v_permlane32_swap_b32 v37,v39
v_permlane32_swap_b32 v44,v46
v_permlane32_swap_b32 v45,v47
v_pk_fma_f32 v[160:161],v[160:161],v[36:37],v[44:45]
v_pk_fma_f32 v[162:163],v[162:163],v[38:39],v[46:47]
v_pk_fma_f32 v[164:165],v[164:165],v[36:37],v[44:45]
v_pk_fma_f32 v[166:167],v[166:167],v[38:39],v[46:47]
v_pk_fma_f32 v[168:169],v[168:169],v[36:37],v[44:45]
v_pk_fma_f32 v[170:171],v[170:171],v[38:39],v[46:47]
v_pk_fma_f32 v[172:173],v[172:173],v[36:37],v[44:45]
v_pk_fma_f32 v[174:175],v[174:175],v[38:39],v[46:47]
v_pk_fma_f32 v[208:209],v[208:209],v[36:37],v[44:45]
v_pk_fma_f32 v[210:211],v[210:211],v[38:39],v[46:47]
v_pk_fma_f32 v[212:213],v[212:213],v[36:37],v[44:45]
v_pk_fma_f32 v[214:215],v[214:215],v[38:39],v[46:47]
v_pk_fma_f32 v[232:233],v[232:233],v[36:37],v[44:45]
v_pk_fma_f32 v[234:235],v[234:235],v[38:39],v[46:47]
v_pk_fma_f32 v[236:237],v[236:237],v[36:37],v[44:45]
v_pk_fma_f32 v[238:239],v[238:239],v[38:39],v[46:47]
v_pk_fma_f32 v[176:177],v[176:177],v[36:37],v[44:45]
v_pk_fma_f32 v[178:179],v[178:179],v[38:39],v[46:47]
v_pk_fma_f32 v[180:181],v[180:181],v[36:37],v[44:45]
v_pk_fma_f32 v[182:183],v[182:183],v[38:39],v[46:47]
v_pk_fma_f32 v[184:185],v[184:185],v[36:37],v[44:45]
v_pk_fma_f32 v[186:187],v[186:187],v[38:39],v[46:47]
v_pk_fma_f32 v[188:189],v[188:189],v[36:37],v[44:45]
v_pk_fma_f32 v[190:191],v[190:191],v[38:39],v[46:47]
v_pk_fma_f32 v[216:217],v[216:217],v[36:37],v[44:45]
v_pk_fma_f32 v[218:219],v[218:219],v[38:39],v[46:47]
v_pk_fma_f32 v[220:221],v[220:221],v[36:37],v[44:45]
v_pk_fma_f32 v[222:223],v[222:223],v[38:39],v[46:47]
v_pk_fma_f32 v[240:241],v[240:241],v[36:37],v[44:45]
v_pk_fma_f32 v[242:243],v[242:243],v[38:39],v[46:47]
v_pk_fma_f32 v[244:245],v[244:245],v[36:37],v[44:45]
v_pk_fma_f32 v[246:247],v[246:247],v[38:39],v[46:47]
v_pk_fma_f32 v[192:193],v[192:193],v[36:37],v[44:45]
v_pk_fma_f32 v[194:195],v[194:195],v[38:39],v[46:47]
v_pk_fma_f32 v[196:197],v[196:197],v[36:37],v[44:45]
v_pk_fma_f32 v[198:199],v[198:199],v[38:39],v[46:47]
v_pk_fma_f32 v[200:201],v[200:201],v[36:37],v[44:45]
v_pk_fma_f32 v[202:203],v[202:203],v[38:39],v[46:47]
v_pk_fma_f32 v[204:205],v[204:205],v[36:37],v[44:45]
v_pk_fma_f32 v[206:207],v[206:207],v[38:39],v[46:47]
v_pk_fma_f32 v[224:225],v[224:225],v[36:37],v[44:45]
v_pk_fma_f32 v[226:227],v[226:227],v[38:39],v[46:47]
v_pk_fma_f32 v[228:229],v[228:229],v[36:37],v[44:45]
v_pk_fma_f32 v[230:231],v[230:231],v[38:39],v[46:47]
v_pk_fma_f32 v[248:249],v[248:249],v[36:37],v[44:45]
v_pk_fma_f32 v[250:251],v[250:251],v[38:39],v[46:47]
v_pk_fma_f32 v[252:253],v[252:253],v[36:37],v[44:45]
v_pk_fma_f32 v[254:255],v[254:255],v[38:39],v[46:47]
v_mfma_f32_16x16x32_bf16 v[40:43],a[204:207],a[8:11],v[20:23]
ds_read_b64_tr_b16 a[216:217],v1 offset:6160
v_cvt_pk_bf16_f32 v160,v160,v164
v_cvt_pk_bf16_f32 v168,v168,v172
v_cvt_pk_bf16_f32 v208,v208,v212
v_lshlrev_b32_sdwa v32,1,v32 dst_sel:DWORD dst_unused:UNUSED_PAD src0_sel:DWORD src1_sel:WORD_1
v_mfma_f32_16x16x32_bf16 v[40:43],a[208:211],a[24:27],v[40:43]
ds_read_u16 v32,v32
v_accvgpr_write_b32 a48,v160
v_accvgpr_write_b32 a49,v168
v_cvt_pk_bf16_f32 v232,v232,v236
v_accvgpr_write_b32 a50,v208
v_mfma_f32_16x16x32_bf16 v[40:43],a[212:215],a[40:43],v[40:43]
ds_read_b64_tr_b16 a[218:219],v1 offset:7184
v_cvt_pk_bf16_f32 v161,v161,v165
v_accvgpr_write_b32 a51,v232
v_cvt_pk_bf16_f32 v169,v169,v173
v_accvgpr_write_b32 a52,v161
v_mfma_f32_16x16x32_bf16 v[36:39],a[200:203],a[40:43],v[16:19]
ds_read_b64_tr_b16 a[220:221],v1 offset:8208
v_cvt_pk_bf16_f32 v209,v209,v213
v_accvgpr_write_b32 a53,v169
v_cvt_pk_bf16_f32 v233,v233,v237
v_lshlrev_b32_sdwa v34,1,v34 dst_sel:DWORD dst_unused:UNUSED_PAD src0_sel:DWORD src1_sel:WORD_1
v_accvgpr_write_b32 a54,v209
v_mfma_f32_16x16x32_bf16 v[36:39],a[196:199],a[24:27],v[36:39]
ds_read_u16_d16_hi v34,v34
v_cvt_pk_bf16_f32 v162,v162,v166
v_accvgpr_write_b32 a55,v233
v_cvt_pk_bf16_f32 v170,v170,v174
v_accvgpr_write_b32 a56,v162
v_mfma_f32_16x16x32_bf16 v[36:39],a[192:195],a[8:11],v[36:39]
ds_read_b64_tr_b16 a[222:223],v1 offset:9232
v_accvgpr_write_b32 a57,v170
v_cvt_pk_bf16_f32 v210,v210,v214
v_accvgpr_write_b32 a58,v210
v_lshlrev_b32_sdwa v33,1,v33 dst_sel:DWORD dst_unused:UNUSED_PAD src0_sel:DWORD src1_sel:WORD_1
v_cvt_pk_bf16_f32 v234,v234,v238
v_mfma_f32_16x16x32_bf16 v[44:47],a[192:195],a[12:15],v[16:19]
ds_read_u16 v33,v33
v_accvgpr_write_b32 a59,v234
v_cvt_pk_bf16_f32 v163,v163,v167
v_accvgpr_write_b32 a60,v163
v_mfma_f32_16x16x32_bf16 v[44:47],a[196:199],a[28:31],v[44:47]
v_cvt_pk_bf16_f32 v171,v171,v175
v_accvgpr_write_b32 a61,v171
v_cvt_pk_bf16_f32 v211,v211,v215
v_accvgpr_write_b32 a62,v211
v_lshlrev_b32_sdwa v35,1,v35 dst_sel:DWORD dst_unused:UNUSED_PAD src0_sel:DWORD src1_sel:WORD_1
v_mfma_f32_16x16x32_bf16 v[44:47],a[200:203],a[44:47],v[44:47]
s_waitcnt lgkmcnt(7)
ds_read_u16_d16_hi v35,v35
v_cvt_pk_bf16_f32 v235,v235,v239
v_accvgpr_write_b32 a63,v235
v_cvt_pk_bf16_f32 v176,v176,v180
v_cvt_pk_bf16_f32 v184,v184,v188
v_mfma_f32_16x16x32_bf16 v[232:235],a[240:243],a[224:227],v[28:31]
s_nop 0
v_cvt_pk_bf16_f32 v216,v216,v220
v_accvgpr_write_b32 a64,v176
v_accvgpr_write_b32 a65,v184
v_cvt_pk_bf16_f32 v240,v240,v244
v_lshlrev_b32_sdwa v60,1,v60 dst_sel:DWORD dst_unused:UNUSED_PAD src0_sel:DWORD src1_sel:WORD_1
v_mfma_f32_16x16x32_bf16 v[236:239],a[240:243],a[228:231],v[28:31]
ds_read_u16 v60,v60
v_accvgpr_write_b32 a66,v216
v_cvt_pk_bf16_f32 v177,v177,v181
v_accvgpr_write_b32 a67,v240
v_cvt_pk_bf16_f32 v185,v185,v189
v_mfma_f32_16x16x32_bf16 v[208:211],a[240:243],a[232:235],v[28:31]
v_accvgpr_write_b32 a68,v177
v_cvt_pk_bf16_f32 v217,v217,v221
v_accvgpr_write_b32 a69,v185
v_lshlrev_b32_sdwa v62,1,v62 dst_sel:DWORD dst_unused:UNUSED_PAD src0_sel:DWORD src1_sel:WORD_1
v_cvt_pk_bf16_f32 v241,v241,v245
v_mfma_f32_16x16x32_bf16 v[212:215],a[240:243],a[236:239],v[28:31]
ds_read_b128 a[240:243],v13 offset:512
ds_read_b128 v[28:31],v4 offset:3712
v_accvgpr_write_b32 a70,v217
v_cvt_pk_bf16_f32 v178,v178,v182
v_accvgpr_write_b32 a71,v241
v_mfma_f32_16x16x32_bf16 v[160:163],a[244:247],a[224:227],v[24:27]
ds_read_b64 a[224:225],v48 offset:57408
ds_read_b64 a[226:227],v48 offset:57416
v_cvt_pk_bf16_f32 v186,v186,v190
v_accvgpr_write_b32 a72,v178
v_accvgpr_write_b32 a73,v186
v_cvt_pk_bf16_f32 v218,v218,v222
v_lshlrev_b32_sdwa v61,1,v61 dst_sel:DWORD dst_unused:UNUSED_PAD src0_sel:DWORD src1_sel:WORD_1
v_mfma_f32_16x16x32_bf16 v[164:167],a[244:247],a[228:231],v[24:27]
ds_read_b64 a[228:229],v49 offset:57408
ds_read_b64 a[230:231],v49 offset:57416
v_accvgpr_write_b32 a74,v218
v_cvt_pk_bf16_f32 v242,v242,v246
v_accvgpr_write_b32 a75,v242
v_cvt_pk_bf16_f32 v179,v179,v183
v_mfma_f32_16x16x32_bf16 v[168:171],a[244:247],a[232:235],v[24:27]
ds_read_b64 a[232:233],v50 offset:57408
ds_read_b64 a[234:235],v50 offset:57416
v_accvgpr_write_b32 a76,v179
v_cvt_pk_bf16_f32 v187,v187,v191
v_accvgpr_write_b32 a77,v187
v_cvt_pk_bf16_f32 v219,v219,v223
v_lshlrev_b32_sdwa v63,1,v63 dst_sel:DWORD dst_unused:UNUSED_PAD src0_sel:DWORD src1_sel:WORD_1
v_mfma_f32_16x16x32_bf16 v[172:175],a[244:247],a[236:239],v[24:27]
s_waitcnt lgkmcnt(4)
ds_read_b64 a[236:237],v51 offset:57408
ds_read_b64 a[238:239],v51 offset:57416
v_accvgpr_write_b32 a78,v219
v_cvt_pk_bf16_f32 v243,v243,v247
v_accvgpr_write_b32 a79,v243
v_cvt_pk_bf16_f32 v192,v192,v196
v_mfma_f32_16x16x32_bf16 v[240:243],a[240:243],a[224:227],v[28:31]
s_waitcnt lgkmcnt(4)
ds_read_b128 a[244:247],v13 offset:768
ds_read_b128 v[24:27],v4 offset:3744
v_cvt_pk_bf16_f32 v200,v200,v204
v_cvt_pk_bf16_f32 v224,v224,v228
v_accvgpr_write_b32 a80,v192
v_accvgpr_write_b32 a81,v200
v_lshlrev_b32_sdwa v52,1,v52 dst_sel:DWORD dst_unused:UNUSED_PAD src0_sel:DWORD src1_sel:WORD_1
v_mfma_f32_16x16x32_bf16 v[244:247],a[240:243],a[228:231],v[28:31]
s_waitcnt lgkmcnt(4)
ds_read_u16_d16_hi v62,v62
v_cvt_pk_bf16_f32 v248,v248,v252
v_accvgpr_write_b32 a82,v224
v_cvt_pk_bf16_f32 v193,v193,v197
v_mfma_f32_16x16x32_bf16 v[216:219],a[240:243],a[232:235],v[28:31]
s_waitcnt lgkmcnt(3)
ds_read_u16 v61,v61
v_accvgpr_write_b32 a83,v248
v_cvt_pk_bf16_f32 v201,v201,v205
v_accvgpr_write_b32 a84,v193
v_cvt_pk_bf16_f32 v225,v225,v229
v_lshlrev_b32_sdwa v54,1,v54 dst_sel:DWORD dst_unused:UNUSED_PAD src0_sel:DWORD src1_sel:WORD_1
v_mfma_f32_16x16x32_bf16 v[220:223],a[240:243],a[236:239],v[28:31]
s_waitcnt lgkmcnt(2)
ds_read_b128 a[240:243],v13 offset:1024
ds_read_b128 v[28:31],v4 offset:3840
v_accvgpr_write_b32 a85,v201
v_cvt_pk_bf16_f32 v249,v249,v253
v_accvgpr_write_b32 a86,v225
v_cvt_pk_bf16_f32 v194,v194,v198
v_mfma_f32_16x16x32_bf16 v[176:179],a[244:247],a[224:227],v[24:27]
s_nop 0
ds_read_b64 a[224:225],v48 offset:57472
ds_read_b64 a[226:227],v48 offset:57480
v_accvgpr_write_b32 a87,v249
v_cvt_pk_bf16_f32 v202,v202,v206
v_accvgpr_write_b32 a88,v194
v_accvgpr_write_b32 a89,v202
v_lshlrev_b32_sdwa v53,1,v53 dst_sel:DWORD dst_unused:UNUSED_PAD src0_sel:DWORD src1_sel:WORD_1
v_mfma_f32_16x16x32_bf16 v[180:183],a[244:247],a[228:231],v[24:27]
ds_read_b64 a[228:229],v49 offset:57472
ds_read_b64 a[230:231],v49 offset:57480
v_cvt_pk_bf16_f32 v226,v226,v230
v_accvgpr_write_b32 a90,v226
v_cvt_pk_bf16_f32 v250,v250,v254
v_accvgpr_write_b32 a91,v250
v_mfma_f32_16x16x32_bf16 v[184:187],a[244:247],a[232:235],v[24:27]
s_nop 0
ds_read_b64 a[232:233],v50 offset:57472
ds_read_b64 a[234:235],v50 offset:57480
v_cvt_pk_bf16_f32 v195,v195,v199
v_accvgpr_write_b32 a92,v195
v_cvt_pk_bf16_f32 v203,v203,v207
v_accvgpr_write_b32 a93,v203
v_mfma_f32_16x16x32_bf16 v[188:191],a[244:247],a[236:239],v[24:27]
s_waitcnt lgkmcnt(4)
ds_read_b64 a[236:237],v51 offset:57472
ds_read_b64 a[238:239],v51 offset:57480
v_cvt_pk_bf16_f32 v227,v227,v231
v_accvgpr_write_b32 a94,v227
TP2752_%=:
s_cbranch_execz TP4876_%=
v_cvt_pk_bf16_f32 v251,v251,v255
v_lshlrev_b32_sdwa v55,1,v55 dst_sel:DWORD dst_unused:UNUSED_PAD src0_sel:DWORD src1_sel:WORD_1
v_accvgpr_write_b32 a95,v251
v_mfma_f32_16x16x32_bf16 v[248:251],a[240:243],a[224:227],v[28:31]
s_waitcnt lgkmcnt(4)
ds_read_b128 a[244:247],v13 offset:1280
ds_read_b128 v[24:27],v4 offset:3872
v_mfma_f32_16x16x32_bf16 v[252:255],a[240:243],a[228:231],v[28:31]
s_waitcnt lgkmcnt(4)
ds_read_u16_d16_hi v63,v63
ds_read_u16 v52,v52
v_lshlrev_b32_sdwa v56,1,v56 dst_sel:DWORD dst_unused:UNUSED_PAD src0_sel:DWORD src1_sel:WORD_1
v_mfma_f32_16x16x32_bf16 v[224:227],a[240:243],a[232:235],v[28:31]
s_waitcnt lgkmcnt(4)
ds_read_u16_d16_hi v54,v54
v_lshlrev_b32_sdwa v58,1,v58 dst_sel:DWORD dst_unused:UNUSED_PAD src0_sel:DWORD src1_sel:WORD_1
v_mfma_f32_16x16x32_bf16 v[228:231],a[240:243],a[236:239],v[28:31]
s_waitcnt lgkmcnt(3)
ds_read_b64_tr_b16 a[240:241],v3 offset:6224
ds_read_b64_tr_b16 a[242:243],v3 offset:7760
v_mfma_f32_16x16x32_bf16 v[192:195],a[244:247],a[224:227],v[24:27]
ds_read_b64_tr_b16 a[224:225],v3 offset:6232
ds_read_b64_tr_b16 a[226:227],v3 offset:7768
v_mfma_f32_16x16x32_bf16 v[196:199],a[244:247],a[228:231],v[24:27]
ds_read_b64_tr_b16 a[228:229],v3 offset:6288
ds_read_b64_tr_b16 a[230:231],v3 offset:7824
v_lshlrev_b32_sdwa v57,1,v57 dst_sel:DWORD dst_unused:UNUSED_PAD src0_sel:DWORD src1_sel:WORD_1
v_mfma_f32_16x16x32_bf16 v[200:203],a[244:247],a[232:235],v[24:27]
ds_read_b64_tr_b16 a[232:233],v3 offset:6296
ds_read_b64_tr_b16 a[234:235],v3 offset:7832
v_mfma_f32_16x16x32_bf16 v[204:207],a[244:247],a[236:239],v[24:27]
ds_read_u16 v53,v53
ds_read_u16_d16_hi v55,v55
v_lshlrev_b32_sdwa v59,1,v59 dst_sel:DWORD dst_unused:UNUSED_PAD src0_sel:DWORD src1_sel:WORD_1
v_mfma_f32_16x16x32_bf16 v[48:51],a[212:215],a[44:47],v[20:23]
ds_read_u16 v56,v56
v_mfma_f32_16x16x32_bf16 v[48:51],a[208:211],a[28:31],v[48:51]
s_waitcnt vmcnt(9)
ds_read_u16_d16_hi v58,v58
v_mfma_f32_16x16x32_bf16 v[48:51],a[204:207],a[12:15],v[48:51]
s_barrier
ds_read_u16 v57,v57
ds_read_u16_d16_hi v59,v59
v_mfma_f32_16x16x32_bf16 v[28:31],a[204:207],a[48:51],v[20:23]
s_add_u32 m0,s57,56576
ds_read_b64 v[14:15],v7 offset:6160
v_lshlrev_b32_sdwa v40,1,v40 dst_sel:DWORD dst_unused:UNUSED_PAD src0_sel:DWORD src1_sel:WORD_1
v_mfma_f32_16x16x32_bf16 v[28:31],a[208:211],a[64:67],v[28:31]
s_add_u32 s89,s56,4030464
ds_read_u16 v40,v40
v_lshlrev_b32_sdwa v42,1,v42 dst_sel:DWORD dst_unused:UNUSED_PAD src0_sel:DWORD src1_sel:WORD_1
v_mfma_f32_16x16x32_bf16 v[28:31],a[212:215],a[80:83],v[28:31]
buffer_load_dwordx4 v6,s[36:39],s89 offen lds
v_lshl_add_u64 v[32:33],v[32:33],0,v[34:35]
v_mfma_f32_16x16x32_bf16 v[24:27],a[200:203],a[80:83],v[16:19]
s_waitcnt lgkmcnt(10)
ds_read_u16_d16_hi v42,v42
v_lshl_add_u64 v[34:35],v[60:61],0,v[62:63]
v_lshlrev_b32_sdwa v41,1,v41 dst_sel:DWORD dst_unused:UNUSED_PAD src0_sel:DWORD src1_sel:WORD_1
v_mfma_f32_16x16x32_bf16 v[24:27],a[196:199],a[64:67],v[24:27]
s_waitcnt lgkmcnt(7)
ds_read_u16 v41,v41
v_lshl_add_u64 v[60:61],v[52:53],0,v[54:55]
v_mfma_f32_16x16x32_bf16 v[24:27],a[192:195],a[48:51],v[24:27]
s_waitcnt lgkmcnt(3)
v_or3_b32 v13,v14,v15,0
v_lshlrev_b32_sdwa v43,1,v43 dst_sel:DWORD dst_unused:UNUSED_PAD src0_sel:DWORD src1_sel:WORD_1
v_mfma_f32_16x16x32_bf16 v[52:55],a[192:195],a[52:55],v[16:19]
ds_read_b64 v[14:15],v7 offset:6672
v_lshlrev_b32_sdwa v36,1,v36 dst_sel:DWORD dst_unused:UNUSED_PAD src0_sel:DWORD src1_sel:WORD_1
v_mfma_f32_16x16x32_bf16 v[52:55],a[196:199],a[68:71],v[52:55]
ds_read_u16_d16_hi v43,v43
v_lshl_add_u64 v[62:63],v[56:57],0,v[58:59]
v_lshlrev_b32_sdwa v38,1,v38 dst_sel:DWORD dst_unused:UNUSED_PAD src0_sel:DWORD src1_sel:WORD_1
v_mfma_f32_16x16x32_bf16 v[52:55],a[200:203],a[84:87],v[52:55]
ds_read_u16 v36,v36
v_lshlrev_b32_sdwa v37,1,v37 dst_sel:DWORD dst_unused:UNUSED_PAD src0_sel:DWORD src1_sel:WORD_1
v_mfma_f32_16x16x32_bf16 v[56:59],a[212:215],a[84:87],v[20:23]
ds_read_u16_d16_hi v38,v38
v_lshlrev_b32_sdwa v39,1,v39 dst_sel:DWORD dst_unused:UNUSED_PAD src0_sel:DWORD src1_sel:WORD_1
v_mfma_f32_16x16x32_bf16 v[56:59],a[208:211],a[68:71],v[56:59]
ds_read_u16 v37,v37
v_lshlrev_b32_sdwa v48,1,v48 dst_sel:DWORD dst_unused:UNUSED_PAD src0_sel:DWORD src1_sel:WORD_1
v_mfma_f32_16x16x32_bf16 v[56:59],a[204:207],a[52:55],v[56:59]
s_waitcnt lgkmcnt(4)
ds_read_u16_d16_hi v39,v39
v_or3_b32 v13,v14,v15,v13
v_mfma_f32_16x16x32_bf16 v[136:139],a[248:251],v[32:35],v[136:139]
ds_read_b64 v[14:15],v7 offset:7184
v_lshlrev_b32_sdwa v50,1,v50 dst_sel:DWORD dst_unused:UNUSED_PAD src0_sel:DWORD src1_sel:WORD_1
v_mfma_f32_16x16x32_bf16 v[140:143],a[248:251],v[60:63],v[140:143]
ds_read_u16 v48,v48
v_lshlrev_b32_sdwa v49,1,v49 dst_sel:DWORD dst_unused:UNUSED_PAD src0_sel:DWORD src1_sel:WORD_1
v_mfma_f32_16x16x32_bf16 v[68:71],a[252:255],v[60:63],v[68:71]
ds_read_u16_d16_hi v50,v50
v_lshlrev_b32_sdwa v51,1,v51 dst_sel:DWORD dst_unused:UNUSED_PAD src0_sel:DWORD src1_sel:WORD_1
v_mfma_f32_16x16x32_bf16 v[64:67],a[252:255],v[32:35],v[64:67]
ds_read_u16 v49,v49
v_lshlrev_b32_sdwa v44,1,v44 dst_sel:DWORD dst_unused:UNUSED_PAD src0_sel:DWORD src1_sel:WORD_1
v_mfma_f32_16x16x32_bf16 v[144:147],a[240:243],v[32:35],v[144:147]
ds_read_u16_d16_hi v51,v51
v_lshlrev_b32_sdwa v46,1,v46 dst_sel:DWORD dst_unused:UNUSED_PAD src0_sel:DWORD src1_sel:WORD_1
v_mfma_f32_16x16x32_bf16 v[148:151],a[240:243],v[60:63],v[148:151]
s_waitcnt lgkmcnt(4)
ds_read_u16 v44,v44
v_or3_b32 v13,v14,v15,v13
v_mfma_f32_16x16x32_bf16 v[84:87],a[224:227],v[60:63],v[84:87]
ds_read_u16_d16_hi v46,v46
v_lshlrev_b32_sdwa v45,1,v45 dst_sel:DWORD dst_unused:UNUSED_PAD src0_sel:DWORD src1_sel:WORD_1
v_mfma_f32_16x16x32_bf16 v[80:83],a[224:227],v[32:35],v[80:83]
ds_read_u16 v45,v45
v_lshlrev_b32_sdwa v47,1,v47 dst_sel:DWORD dst_unused:UNUSED_PAD src0_sel:DWORD src1_sel:WORD_1
v_mfma_f32_16x16x32_bf16 v[152:155],a[228:231],v[32:35],v[152:155]
ds_read_u16_d16_hi v47,v47
v_mfma_f32_16x16x32_bf16 v[156:159],a[228:231],v[60:63],v[156:159]
v_mfma_f32_16x16x32_bf16 v[100:103],a[232:235],v[60:63],v[100:103]
v_mfma_f32_16x16x32_bf16 v[96:99],a[232:235],v[32:35],v[96:99]
s_add_u32 m0,s57,60120
v_mfma_f32_16x16x32_bf16 v[60:63],a[204:207],a[56:59],v[20:23]
s_add_u32 s89,s56,4128768
v_lshlrev_b32_sdwa v28,1,v28 dst_sel:DWORD dst_unused:UNUSED_PAD src0_sel:DWORD src1_sel:WORD_1
v_fma_mixlo_f16 v8,v53,s99,0
v_mfma_f32_16x16x32_bf16 v[60:63],a[208:211],a[72:75],v[60:63]
buffer_load_dwordx4 v6,s[36:39],s89 offen lds
v_lshlrev_b32_sdwa v30,1,v30 dst_sel:DWORD dst_unused:UNUSED_PAD src0_sel:DWORD src1_sel:WORD_1
v_mfma_f32_16x16x32_bf16 v[60:63],a[212:215],a[88:91],v[60:63]
ds_read_u16 v28,v28
v_lshl_add_u64 v[40:41],v[40:41],0,v[42:43]
v_mfma_f32_16x16x32_bf16 v[32:35],a[200:203],a[88:91],v[16:19]
ds_read_u16_d16_hi v30,v30
v_lshl_add_u64 v[42:43],v[36:37],0,v[38:39]
v_fma_mixhi_f16 v8,v55,s99,0
v_mfma_f32_16x16x32_bf16 v[32:35],a[196:199],a[72:75],v[32:35]
s_waitcnt lgkmcnt(3)
v_lshl_add_u64 v[36:37],v[48:49],0,v[50:51]
v_lshlrev_b32_sdwa v29,1,v29 dst_sel:DWORD dst_unused:UNUSED_PAD src0_sel:DWORD src1_sel:WORD_1
v_pk_fma_f16 v9,v8,v8,s54
v_mfma_f32_16x16x32_bf16 v[32:35],a[192:195],a[56:59],v[32:35]
ds_read_u16 v29,v29
v_lshlrev_b32_sdwa v31,1,v31 dst_sel:DWORD dst_unused:UNUSED_PAD src0_sel:DWORD src1_sel:WORD_1
v_mfma_f32_16x16x32_bf16 v[48:51],a[192:195],a[60:63],v[16:19]
ds_read_b128 v[16:19],v4 offset:768
ds_read_b64_tr_b16 a[192:193],v1 offset:10256
v_pk_mul_f16 v9,v8,v9
v_mfma_f32_16x16x32_bf16 v[48:51],a[196:199],a[76:79],v[48:51]
s_waitcnt lgkmcnt(5)
ds_read_b64_tr_b16 a[194:195],v1 offset:11280
ds_read_b64_tr_b16 a[196:197],v2 offset:10256
v_lshl_add_u64 v[38:39],v[44:45],0,v[46:47]
v_lshlrev_b32_sdwa v24,1,v24 dst_sel:DWORD dst_unused:UNUSED_PAD src0_sel:DWORD src1_sel:WORD_1
v_mfma_f32_16x16x32_bf16 v[48:51],a[200:203],a[92:95],v[48:51]
ds_read_b64_tr_b16 a[198:199],v2 offset:11280
ds_read_b64_tr_b16 a[200:201],v2 offset:8208
v_exp_f16 v8,v9
v_mfma_f32_16x16x32_bf16 v[44:47],a[212:215],a[92:95],v[20:23]
ds_read_b128 v[20:23],v4 offset:800
ds_read_b64_tr_b16 a[202:203],v2 offset:9232
v_lshlrev_b32_sdwa v26,1,v26 dst_sel:DWORD dst_unused:UNUSED_PAD src0_sel:DWORD src1_sel:WORD_1
v_mfma_f32_16x16x32_bf16 v[44:47],a[208:211],a[76:79],v[44:47]
ds_read_b64_tr_b16 a[212:213],v2 offset:6160
ds_read_b64_tr_b16 a[214:215],v2 offset:7184
v_lshlrev_b32_sdwa v25,1,v25 dst_sel:DWORD dst_unused:UNUSED_PAD src0_sel:DWORD src1_sel:WORD_1
v_mfma_f32_16x16x32_bf16 v[44:47],a[204:207],a[60:63],v[44:47]
ds_read_u16_d16_hi v31,v31
v_exp_f16_sdwa v8,v9 dst_sel:WORD_1 dst_unused:UNUSED_PRESERVE src0_sel:WORD_1
v_mfma_f32_16x16x32_bf16 v[112:115],a[248:251],v[40:43],v[112:115]
ds_read_u16 v24,v24
v_lshlrev_b32_sdwa v27,1,v27 dst_sel:DWORD dst_unused:UNUSED_PAD src0_sel:DWORD src1_sel:WORD_1
v_mfma_f32_16x16x32_bf16 v[116:119],a[248:251],v[36:39],v[116:119]
ds_read_u16_d16_hi v26,v26
v_lshlrev_b32_sdwa v56,1,v56 dst_sel:DWORD dst_unused:UNUSED_PAD src0_sel:DWORD src1_sel:WORD_1
v_mfma_f32_16x16x32_bf16 v[76:79],a[252:255],v[36:39],v[76:79]
ds_read_u16 v25,v25
v_pk_fma_f16 v8,v8,s55,s55
v_mfma_f32_16x16x32_bf16 v[72:75],a[252:255],v[40:43],v[72:75]
v_rcp_f16 v9,v8
v_lshlrev_b32_sdwa v58,1,v58 dst_sel:DWORD dst_unused:UNUSED_PAD src0_sel:DWORD src1_sel:WORD_1
v_mfma_f32_16x16x32_bf16 v[120:123],a[240:243],v[40:43],v[120:123]
ds_read_u16_d16_hi v27,v27
v_lshlrev_b32_sdwa v57,1,v57 dst_sel:DWORD dst_unused:UNUSED_PAD src0_sel:DWORD src1_sel:WORD_1
v_mfma_f32_16x16x32_bf16 v[124:127],a[240:243],v[36:39],v[124:127]
ds_read_u16 v56,v56
v_lshlrev_b32_sdwa v59,1,v59 dst_sel:DWORD dst_unused:UNUSED_PAD src0_sel:DWORD src1_sel:WORD_1
v_mfma_f32_16x16x32_bf16 v[92:95],a[224:227],v[36:39],v[92:95]
ds_read_u16_d16_hi v58,v58
v_lshlrev_b32_sdwa v52,1,v52 dst_sel:DWORD dst_unused:UNUSED_PAD src0_sel:DWORD src1_sel:WORD_1
v_mfma_f32_16x16x32_bf16 v[88:91],a[224:227],v[40:43],v[88:91]
v_rcp_f16_sdwa v9,v8 dst_sel:WORD_1 dst_unused:UNUSED_PRESERVE src0_sel:WORD_1
v_mfma_f32_16x16x32_bf16 v[128:131],a[228:231],v[40:43],v[128:131]
ds_read_u16 v57,v57
ds_read_u16_d16_hi v59,v59
v_mfma_f32_16x16x32_bf16 v[132:135],a[228:231],v[36:39],v[132:135]
ds_read_u16 v52,v52
v_lshlrev_b32_sdwa v54,1,v54 dst_sel:DWORD dst_unused:UNUSED_PAD src0_sel:DWORD src1_sel:WORD_1
v_mfma_f32_16x16x32_bf16 v[108:111],a[232:235],v[36:39],v[108:111]
ds_read_u16_d16_hi v54,v54
v_fma_mix_f32 v53,v53,v9,0 op_sel_hi:[0,1,0]
v_mfma_f32_16x16x32_bf16 v[104:107],a[232:235],v[40:43],v[104:107]
s_waitcnt lgkmcnt(7)
v_fma_mix_f32 v55,v55,v9,0 op_sel:[0,1,0] op_sel_hi:[0,1,0]
v_mfma_f32_16x16x32_bf16 v[36:39],a[216:219],a[0:3],v[16:19]
s_add_u32 m0,s57,63656
v_lshlrev_b32_sdwa v60,1,v60 dst_sel:DWORD dst_unused:UNUSED_PAD src0_sel:DWORD src1_sel:WORD_1
v_lshlrev_b32_sdwa v62,1,v62 dst_sel:DWORD dst_unused:UNUSED_PAD src0_sel:DWORD src1_sel:WORD_1
v_mfma_f32_16x16x32_bf16 v[36:39],a[220:223],a[16:19],v[36:39]
s_add_u32 s89,s56,4227072
ds_read_u16 v60,v60
v_lshlrev_b32_sdwa v61,1,v61 dst_sel:DWORD dst_unused:UNUSED_PAD src0_sel:DWORD src1_sel:WORD_1
v_mfma_f32_16x16x32_bf16 v[36:39],a[192:195],a[32:35],v[36:39]
buffer_load_dwordx4 v6,s[36:39],s89 offen lds
v_lshl_add_u64 v[28:29],v[28:29],0,v[30:31]
v_mfma_f32_16x16x32_bf16 v[40:43],a[196:199],a[32:35],v[20:23]
s_waitcnt lgkmcnt(5)
ds_read_u16_d16_hi v62,v62
v_lshl_add_u64 v[30:31],v[24:25],0,v[26:27]
v_mfma_f32_16x16x32_bf16 v[40:43],a[200:203],a[16:19],v[40:43]
s_waitcnt lgkmcnt(4)
ds_read_u16 v61,v61
v_lshl_add_u64 v[24:25],v[56:57],0,v[58:59]
v_mfma_f32_16x16x32_bf16 v[40:43],a[212:215],a[0:3],v[40:43]
v_lshlrev_b32_sdwa v63,1,v63 dst_sel:DWORD dst_unused:UNUSED_PAD src0_sel:DWORD src1_sel:WORD_1
v_lshlrev_b32_sdwa v32,1,v32 dst_sel:DWORD dst_unused:UNUSED_PAD src0_sel:DWORD src1_sel:WORD_1
v_mfma_f32_16x16x32_bf16 v[56:59],a[212:215],a[4:7],v[20:23]
s_waitcnt lgkmcnt(3)
ds_read_u16_d16_hi v63,v63
v_or_b32 v26,v52,v54
v_mfma_f32_16x16x32_bf16 v[56:59],a[200:203],a[20:23],v[56:59]
ds_read_u16 v32,v32
v_cvt_pk_bf16_f32 v27,v53,v55
v_lshlrev_b32_sdwa v34,1,v34 dst_sel:DWORD dst_unused:UNUSED_PAD src0_sel:DWORD src1_sel:WORD_1
v_mfma_f32_16x16x32_bf16 v[56:59],a[196:199],a[36:39],v[56:59]
ds_read_u16_d16_hi v34,v34
v_lshlrev_b32_sdwa v33,1,v33 dst_sel:DWORD dst_unused:UNUSED_PAD src0_sel:DWORD src1_sel:WORD_1
v_mfma_f32_16x16x32_bf16 v[52:55],a[192:195],a[36:39],v[16:19]
ds_read_u16 v33,v33
v_lshlrev_b32_sdwa v35,1,v35 dst_sel:DWORD dst_unused:UNUSED_PAD src0_sel:DWORD src1_sel:WORD_1
v_mfma_f32_16x16x32_bf16 v[52:55],a[220:223],a[20:23],v[52:55]
ds_read_u16_d16_hi v35,v35
v_lshlrev_b32_sdwa v44,1,v44 dst_sel:DWORD dst_unused:UNUSED_PAD src0_sel:DWORD src1_sel:WORD_1
v_mfma_f32_16x16x32_bf16 v[52:55],a[216:219],a[4:7],v[52:55]
ds_read_u16 v44,v44
v_lshlrev_b32_sdwa v46,1,v46 dst_sel:DWORD dst_unused:UNUSED_PAD src0_sel:DWORD src1_sel:WORD_1
v_mfma_f32_16x16x32_bf16 v[232:235],a[248:251],v[28:31],v[232:235]
ds_read_u16_d16_hi v46,v46
v_lshlrev_b32_sdwa v45,1,v45 dst_sel:DWORD dst_unused:UNUSED_PAD src0_sel:DWORD src1_sel:WORD_1
v_mfma_f32_16x16x32_bf16 v[236:239],a[248:251],v[24:27],v[236:239]
ds_read_u16 v45,v45
v_lshlrev_b32_sdwa v47,1,v47 dst_sel:DWORD dst_unused:UNUSED_PAD src0_sel:DWORD src1_sel:WORD_1
v_mfma_f32_16x16x32_bf16 v[164:167],a[252:255],v[24:27],v[164:167]
ds_read_u16_d16_hi v47,v47
ds_read_b64_tr_b16 a[236:237],v3 offset:12320
v_mfma_f32_16x16x32_bf16 v[160:163],a[252:255],v[28:31],v[160:163]
ds_read_b64_tr_b16 a[238:239],v3 offset:13856
v_lshlrev_b32_sdwa v48,1,v48 dst_sel:DWORD dst_unused:UNUSED_PAD src0_sel:DWORD src1_sel:WORD_1
v_mfma_f32_16x16x32_bf16 v[240:243],a[240:243],v[28:31],v[240:243]
ds_read_u16 v48,v48
v_lshlrev_b32_sdwa v50,1,v50 dst_sel:DWORD dst_unused:UNUSED_PAD src0_sel:DWORD src1_sel:WORD_1
v_mfma_f32_16x16x32_bf16 v[244:247],a[240:243],v[24:27],v[244:247]
ds_read_u16_d16_hi v50,v50
v_lshlrev_b32_sdwa v49,1,v49 dst_sel:DWORD dst_unused:UNUSED_PAD src0_sel:DWORD src1_sel:WORD_1
v_mfma_f32_16x16x32_bf16 v[180:183],a[224:227],v[24:27],v[180:183]
s_add_u32 m0,s57,67184
ds_read_u16 v49,v49
v_lshlrev_b32_sdwa v51,1,v51 dst_sel:DWORD dst_unused:UNUSED_PAD src0_sel:DWORD src1_sel:WORD_1
v_mfma_f32_16x16x32_bf16 v[176:179],a[224:227],v[28:31],v[176:179]
s_add_u32 s89,s56,4325376
ds_read_u16_d16_hi v51,v51
v_mfma_f32_16x16x32_bf16 v[248:251],a[228:231],v[28:31],v[248:251]
buffer_load_dwordx4 v6,s[36:39],s89 offen lds
v_mfma_f32_16x16x32_bf16 v[252:255],a[228:231],v[24:27],v[252:255]
ds_read_b64_tr_b16 a[244:245],v3 offset:12328
v_mfma_f32_16x16x32_bf16 v[196:199],a[232:235],v[24:27],v[196:199]
ds_read_b64_tr_b16 a[246:247],v3 offset:13864
v_mfma_f32_16x16x32_bf16 v[192:195],a[232:235],v[28:31],v[192:195]
v_mfma_f32_16x16x32_bf16 v[24:27],a[216:219],a[8:11],v[16:19]
ds_read_b64_tr_b16 a[208:209],v1 offset:12320
v_lshlrev_b32_sdwa v36,1,v36 dst_sel:DWORD dst_unused:UNUSED_PAD src0_sel:DWORD src1_sel:WORD_1
v_mfma_f32_16x16x32_bf16 v[24:27],a[220:223],a[24:27],v[24:27]
ds_read_u16 v36,v36
v_fma_mixlo_f16 v8,v57,s99,0
v_mfma_f32_16x16x32_bf16 v[24:27],a[192:195],a[40:43],v[24:27]
s_waitcnt lgkmcnt(6)
ds_read_b64_tr_b16 a[210:211],v1 offset:13344
v_lshl_add_u64 v[60:61],v[60:61],0,v[62:63]
v_lshlrev_b32_sdwa v38,1,v38 dst_sel:DWORD dst_unused:UNUSED_PAD src0_sel:DWORD src1_sel:WORD_1
v_mfma_f32_16x16x32_bf16 v[28:31],a[196:199],a[40:43],v[20:23]
ds_read_u16_d16_hi v38,v38
v_lshl_add_u64 v[62:63],v[32:33],0,v[34:35]
v_fma_mixhi_f16 v8,v59,s99,0
v_mfma_f32_16x16x32_bf16 v[28:31],a[200:203],a[24:27],v[28:31]
ds_read_b64_tr_b16 a[204:205],v1 offset:14368
v_lshl_add_u64 v[32:33],v[44:45],0,v[46:47]
v_lshlrev_b32_sdwa v37,1,v37 dst_sel:DWORD dst_unused:UNUSED_PAD src0_sel:DWORD src1_sel:WORD_1
v_pk_fma_f16 v9,v8,v8,s54
v_mfma_f32_16x16x32_bf16 v[28:31],a[212:215],a[8:11],v[28:31]
ds_read_u16 v37,v37
v_lshlrev_b32_sdwa v39,1,v39 dst_sel:DWORD dst_unused:UNUSED_PAD src0_sel:DWORD src1_sel:WORD_1
v_mfma_f32_16x16x32_bf16 v[44:47],a[212:215],a[12:15],v[20:23]
ds_read_u16_d16_hi v39,v39
v_pk_mul_f16 v9,v8,v9
v_mfma_f32_16x16x32_bf16 v[44:47],a[200:203],a[28:31],v[44:47]
s_waitcnt lgkmcnt(7)
ds_read_b64_tr_b16 a[206:207],v1 offset:15392
v_lshl_add_u64 v[34:35],v[48:49],0,v[50:51]
v_lshlrev_b32_sdwa v40,1,v40 dst_sel:DWORD dst_unused:UNUSED_PAD src0_sel:DWORD src1_sel:WORD_1
v_mfma_f32_16x16x32_bf16 v[44:47],a[196:199],a[44:47],v[44:47]
v_exp_f16 v8,v9
v_mfma_f32_16x16x32_bf16 v[48:51],a[192:195],a[44:47],v[16:19]
ds_read_u16 v40,v40
v_lshlrev_b32_sdwa v42,1,v42 dst_sel:DWORD dst_unused:UNUSED_PAD src0_sel:DWORD src1_sel:WORD_1
v_mfma_f32_16x16x32_bf16 v[48:51],a[220:223],a[28:31],v[48:51]
ds_read_u16_d16_hi v42,v42
v_lshlrev_b32_sdwa v41,1,v41 dst_sel:DWORD dst_unused:UNUSED_PAD src0_sel:DWORD src1_sel:WORD_1
v_mfma_f32_16x16x32_bf16 v[48:51],a[216:219],a[12:15],v[48:51]
v_exp_f16_sdwa v8,v9 dst_sel:WORD_1 dst_unused:UNUSED_PRESERVE src0_sel:WORD_1
v_mfma_f32_16x16x32_bf16 v[208:211],a[248:251],v[60:63],v[208:211]
ds_read_u16 v41,v41
v_lshlrev_b32_sdwa v43,1,v43 dst_sel:DWORD dst_unused:UNUSED_PAD src0_sel:DWORD src1_sel:WORD_1
v_mfma_f32_16x16x32_bf16 v[212:215],a[248:251],v[32:35],v[212:215]
ds_read_u16_d16_hi v43,v43
v_lshlrev_b32_sdwa v52,1,v52 dst_sel:DWORD dst_unused:UNUSED_PAD src0_sel:DWORD src1_sel:WORD_1
v_mfma_f32_16x16x32_bf16 v[172:175],a[252:255],v[32:35],v[172:175]
ds_read_u16 v52,v52
v_pk_fma_f16 v8,v8,s55,s55
v_mfma_f32_16x16x32_bf16 v[168:171],a[252:255],v[60:63],v[168:171]
ds_read_b64_tr_b16 a[248:249],v3 offset:12384
v_rcp_f16_e64 v9,v8
v_lshlrev_b32_sdwa v54,1,v54 dst_sel:DWORD dst_unused:UNUSED_PAD src0_sel:DWORD src1_sel:WORD_1
v_mfma_f32_16x16x32_bf16 v[216:219],a[240:243],v[60:63],v[216:219]
ds_read_u16_d16_hi v54,v54
v_lshlrev_b32_sdwa v53,1,v53 dst_sel:DWORD dst_unused:UNUSED_PAD src0_sel:DWORD src1_sel:WORD_1
v_mfma_f32_16x16x32_bf16 v[220:223],a[240:243],v[32:35],v[220:223]
ds_read_u16 v53,v53
ds_read_b64_tr_b16 a[250:251],v3 offset:13920
v_mfma_f32_16x16x32_bf16 v[188:191],a[224:227],v[32:35],v[188:191]
ds_read_b64_tr_b16 a[252:253],v3 offset:12392
v_lshlrev_b32_sdwa v55,1,v55 dst_sel:DWORD dst_unused:UNUSED_PAD src0_sel:DWORD src1_sel:WORD_1
v_mfma_f32_16x16x32_bf16 v[184:187],a[224:227],v[60:63],v[184:187]
ds_read_u16_d16_hi v55,v55
v_rcp_f16_sdwa v9,v8 dst_sel:WORD_1 dst_unused:UNUSED_PRESERVE src0_sel:WORD_1
v_mfma_f32_16x16x32_bf16 v[224:227],a[228:231],v[60:63],v[224:227]
ds_read_b64_tr_b16 a[254:255],v3 offset:13928
ds_read_b64_tr_b16 a[240:241],v3 offset:12448
v_lshlrev_b32_sdwa v56,1,v56 dst_sel:DWORD dst_unused:UNUSED_PAD src0_sel:DWORD src1_sel:WORD_1
v_mfma_f32_16x16x32_bf16 v[228:231],a[228:231],v[32:35],v[228:231]
ds_read_u16 v56,v56
v_lshlrev_b32_sdwa v58,1,v58 dst_sel:DWORD dst_unused:UNUSED_PAD src0_sel:DWORD src1_sel:WORD_1
v_mfma_f32_16x16x32_bf16 v[204:207],a[232:235],v[32:35],v[204:207]
ds_read_u16_d16_hi v58,v58
v_fma_mix_f32 v57,v57,v9,0 op_sel_hi:[0,1,0]
v_mfma_f32_16x16x32_bf16 v[200:203],a[232:235],v[60:63],v[200:203]
ds_read_b64_tr_b16 a[242:243],v3 offset:13984
v_fma_mix_f32 v59,v59,v9,0 op_sel:[0,1,0] op_sel_hi:[0,1,0]
v_mfma_f32_16x16x32_bf16 v[32:35],a[216:219],a[48:51],v[16:19]
s_add_u32 m0,s57,70720
ds_read_b64 v[14:15],v7 offset:12320
v_lshlrev_b32_sdwa v24,1,v24 dst_sel:DWORD dst_unused:UNUSED_PAD src0_sel:DWORD src1_sel:WORD_1
v_mfma_f32_16x16x32_bf16 v[32:35],a[220:223],a[64:67],v[32:35]
s_add_u32 s89,s56,4423680
ds_read_b64_tr_b16 a[224:225],v3 offset:12456
v_lshlrev_b32_sdwa v26,1,v26 dst_sel:DWORD dst_unused:UNUSED_PAD src0_sel:DWORD src1_sel:WORD_1
v_mfma_f32_16x16x32_bf16 v[32:35],a[192:195],a[80:83],v[32:35]
s_waitcnt lgkmcnt(9)
buffer_load_dwordx4 v6,s[36:39],s89 offen lds
v_lshl_add_u64 v[36:37],v[36:37],0,v[38:39]
v_mfma_f32_16x16x32_bf16 v[60:63],a[196:199],a[80:83],v[20:23]
ds_read_b64_tr_b16 a[226:227],v3 offset:13992
v_lshl_add_u64 v[38:39],v[40:41],0,v[42:43]
v_lshlrev_b32_sdwa v25,1,v25 dst_sel:DWORD dst_unused:UNUSED_PAD src0_sel:DWORD src1_sel:WORD_1
v_mfma_f32_16x16x32_bf16 v[60:63],a[200:203],a[64:67],v[60:63]
s_waitcnt lgkmcnt(8)
ds_read_u16 v24,v24
v_lshl_add_u64 v[40:41],v[52:53],0,v[54:55]
v_mfma_f32_16x16x32_bf16 v[60:63],a[212:215],a[48:51],v[60:63]
s_waitcnt lgkmcnt(3)
ds_read_u16_d16_hi v26,v26
v_or3_b32 v13,v14,v15,v13
v_lshlrev_b32_sdwa v27,1,v27 dst_sel:DWORD dst_unused:UNUSED_PAD src0_sel:DWORD src1_sel:WORD_1
v_mfma_f32_16x16x32_bf16 v[52:55],a[212:215],a[52:55],v[20:23]
ds_read_b64 v[14:15],v7 offset:12832
v_or_b32 v42,v56,v58
v_lshlrev_b32_sdwa v28,1,v28 dst_sel:DWORD dst_unused:UNUSED_PAD src0_sel:DWORD src1_sel:WORD_1
v_mfma_f32_16x16x32_bf16 v[52:55],a[200:203],a[68:71],v[52:55]
ds_read_u16 v25,v25
v_cvt_pk_bf16_f32 v43,v57,v59
v_lshlrev_b32_sdwa v30,1,v30 dst_sel:DWORD dst_unused:UNUSED_PAD src0_sel:DWORD src1_sel:WORD_1
v_mfma_f32_16x16x32_bf16 v[52:55],a[196:199],a[84:87],v[52:55]
ds_read_u16_d16_hi v27,v27
v_lshlrev_b32_sdwa v29,1,v29 dst_sel:DWORD dst_unused:UNUSED_PAD src0_sel:DWORD src1_sel:WORD_1
v_mfma_f32_16x16x32_bf16 v[56:59],a[192:195],a[84:87],v[16:19]
ds_read_u16 v28,v28
v_lshlrev_b32_sdwa v31,1,v31 dst_sel:DWORD dst_unused:UNUSED_PAD src0_sel:DWORD src1_sel:WORD_1
v_mfma_f32_16x16x32_bf16 v[56:59],a[220:223],a[68:71],v[56:59]
ds_read_u16_d16_hi v30,v30
v_lshlrev_b32_sdwa v48,1,v48 dst_sel:DWORD dst_unused:UNUSED_PAD src0_sel:DWORD src1_sel:WORD_1
v_mfma_f32_16x16x32_bf16 v[56:59],a[216:219],a[52:55],v[56:59]
s_waitcnt lgkmcnt(4)
ds_read_u16 v29,v29
v_or3_b32 v13,v14,v15,v13
v_mfma_f32_16x16x32_bf16 v[136:139],a[236:239],v[36:39],v[136:139]
ds_read_b64 v[14:15],v7 offset:13344
v_lshlrev_b32_sdwa v50,1,v50 dst_sel:DWORD dst_unused:UNUSED_PAD src0_sel:DWORD src1_sel:WORD_1
v_mfma_f32_16x16x32_bf16 v[140:143],a[236:239],v[40:43],v[140:143]
s_add_u32 m0,s57,74264
ds_read_u16_d16_hi v31,v31
v_lshlrev_b32_sdwa v49,1,v49 dst_sel:DWORD dst_unused:UNUSED_PAD src0_sel:DWORD src1_sel:WORD_1
v_mfma_f32_16x16x32_bf16 v[68:71],a[244:247],v[40:43],v[68:71]
s_add_u32 s89,s56,4521984
ds_read_u16 v48,v48
v_lshlrev_b32_sdwa v51,1,v51 dst_sel:DWORD dst_unused:UNUSED_PAD src0_sel:DWORD src1_sel:WORD_1
v_mfma_f32_16x16x32_bf16 v[64:67],a[244:247],v[36:39],v[64:67]
buffer_load_dwordx4 v6,s[36:39],s89 offen lds
v_lshlrev_b32_sdwa v44,1,v44 dst_sel:DWORD dst_unused:UNUSED_PAD src0_sel:DWORD src1_sel:WORD_1
v_mfma_f32_16x16x32_bf16 v[144:147],a[248:251],v[36:39],v[144:147]
ds_read_u16_d16_hi v50,v50
v_lshlrev_b32_sdwa v46,1,v46 dst_sel:DWORD dst_unused:UNUSED_PAD src0_sel:DWORD src1_sel:WORD_1
v_mfma_f32_16x16x32_bf16 v[148:151],a[248:251],v[40:43],v[148:151]
s_waitcnt lgkmcnt(3)
ds_read_u16 v49,v49
v_or3_b32 v13,v14,v15,v13
v_mfma_f32_16x16x32_bf16 v[84:87],a[252:255],v[40:43],v[84:87]
ds_read_u16_d16_hi v51,v51
v_lshlrev_b32_sdwa v45,1,v45 dst_sel:DWORD dst_unused:UNUSED_PAD src0_sel:DWORD src1_sel:WORD_1
v_mfma_f32_16x16x32_bf16 v[80:83],a[252:255],v[36:39],v[80:83]
ds_read_u16 v44,v44
v_lshlrev_b32_sdwa v47,1,v47 dst_sel:DWORD dst_unused:UNUSED_PAD src0_sel:DWORD src1_sel:WORD_1
v_mfma_f32_16x16x32_bf16 v[152:155],a[240:243],v[36:39],v[152:155]
ds_read_u16_d16_hi v46,v46
v_mfma_f32_16x16x32_bf16 v[156:159],a[240:243],v[40:43],v[156:159]
ds_read_u16 v45,v45
v_mfma_f32_16x16x32_bf16 v[100:103],a[224:227],v[40:43],v[100:103]
ds_read_u16_d16_hi v47,v47
v_mfma_f32_16x16x32_bf16 v[96:99],a[224:227],v[36:39],v[96:99]
v_mfma_f32_16x16x32_bf16 v[40:43],a[216:219],a[56:59],v[16:19]
v_lshlrev_b32_sdwa v32,1,v32 dst_sel:DWORD dst_unused:UNUSED_PAD src0_sel:DWORD src1_sel:WORD_1
v_fma_mixlo_f16 v8,v53,s99,0
v_mfma_f32_16x16x32_bf16 v[40:43],a[220:223],a[72:75],v[40:43]
ds_read_u16 v32,v32
v_lshlrev_b32_sdwa v34,1,v34 dst_sel:DWORD dst_unused:UNUSED_PAD src0_sel:DWORD src1_sel:WORD_1
v_mfma_f32_16x16x32_bf16 v[40:43],a[192:195],a[88:91],v[40:43]
ds_read_u16_d16_hi v34,v34
v_lshl_add_u64 v[24:25],v[24:25],0,v[26:27]
v_mfma_f32_16x16x32_bf16 v[36:39],a[196:199],a[88:91],v[20:23]
s_waitcnt lgkmcnt(6)
v_lshl_add_u64 v[26:27],v[28:29],0,v[30:31]
v_fma_mixhi_f16 v8,v55,s99,0
v_mfma_f32_16x16x32_bf16 v[36:39],a[200:203],a[72:75],v[36:39]
v_lshl_add_u64 v[28:29],v[48:49],0,v[50:51]
v_lshlrev_b32_sdwa v33,1,v33 dst_sel:DWORD dst_unused:UNUSED_PAD src0_sel:DWORD src1_sel:WORD_1
v_pk_fma_f16 v9,v8,v8,s54
v_mfma_f32_16x16x32_bf16 v[36:39],a[212:215],a[56:59],v[36:39]
ds_read_u16 v33,v33
v_lshlrev_b32_sdwa v35,1,v35 dst_sel:DWORD dst_unused:UNUSED_PAD src0_sel:DWORD src1_sel:WORD_1
v_mfma_f32_16x16x32_bf16 v[48:51],a[212:215],a[60:63],v[20:23]
ds_read_b128 v[20:23],v4 offset:896
v_pk_mul_f16 v9,v8,v9
v_mfma_f32_16x16x32_bf16 v[48:51],a[200:203],a[76:79],v[48:51]
s_waitcnt lgkmcnt(4)
ds_read_u16_d16_hi v35,v35
ds_read_b64_tr_b16 a[212:213],v1 offset:16416
v_lshl_add_u64 v[30:31],v[44:45],0,v[46:47]
v_lshlrev_b32_sdwa v60,1,v60 dst_sel:DWORD dst_unused:UNUSED_PAD src0_sel:DWORD src1_sel:WORD_1
v_mfma_f32_16x16x32_bf16 v[48:51],a[196:199],a[92:95],v[48:51]
ds_read_b64_tr_b16 a[214:215],v1 offset:17440
v_exp_f16 v8,v9
v_mfma_f32_16x16x32_bf16 v[44:47],a[192:195],a[92:95],v[16:19]
ds_read_b128 v[16:19],v4 offset:928
ds_read_b64_tr_b16 a[200:201],v2 offset:16416
v_lshlrev_b32_sdwa v62,1,v62 dst_sel:DWORD dst_unused:UNUSED_PAD src0_sel:DWORD src1_sel:WORD_1
v_mfma_f32_16x16x32_bf16 v[44:47],a[220:223],a[76:79],v[44:47]
ds_read_b64_tr_b16 a[202:203],v2 offset:17440
ds_read_u16 v60,v60
v_lshlrev_b32_sdwa v61,1,v61 dst_sel:DWORD dst_unused:UNUSED_PAD src0_sel:DWORD src1_sel:WORD_1
v_mfma_f32_16x16x32_bf16 v[44:47],a[216:219],a[60:63],v[44:47]
ds_read_u16_d16_hi v62,v62
v_exp_f16_sdwa v8,v9 dst_sel:WORD_1 dst_unused:UNUSED_PRESERVE src0_sel:WORD_1
v_mfma_f32_16x16x32_bf16 v[112:115],a[236:239],v[24:27],v[112:115]
ds_read_u16 v61,v61
ds_read_b64_tr_b16 a[196:197],v2 offset:14368
v_lshlrev_b32_sdwa v63,1,v63 dst_sel:DWORD dst_unused:UNUSED_PAD src0_sel:DWORD src1_sel:WORD_1
v_mfma_f32_16x16x32_bf16 v[116:119],a[236:239],v[28:31],v[116:119]
ds_read_u16_d16_hi v63,v63
v_lshlrev_b32_sdwa v56,1,v56 dst_sel:DWORD dst_unused:UNUSED_PAD src0_sel:DWORD src1_sel:WORD_1
v_mfma_f32_16x16x32_bf16 v[76:79],a[244:247],v[28:31],v[76:79]
ds_read_b64_tr_b16 a[198:199],v2 offset:15392
v_pk_fma_f16 v8,v8,s55,s55
v_mfma_f32_16x16x32_bf16 v[72:75],a[244:247],v[24:27],v[72:75]
v_rcp_f16 v9,v8
v_lshlrev_b32_sdwa v58,1,v58 dst_sel:DWORD dst_unused:UNUSED_PAD src0_sel:DWORD src1_sel:WORD_1
v_mfma_f32_16x16x32_bf16 v[120:123],a[248:251],v[24:27],v[120:123]
ds_read_u16 v56,v56
v_lshlrev_b32_sdwa v57,1,v57 dst_sel:DWORD dst_unused:UNUSED_PAD src0_sel:DWORD src1_sel:WORD_1
v_mfma_f32_16x16x32_bf16 v[124:127],a[248:251],v[28:31],v[124:127]
ds_read_u16_d16_hi v58,v58
v_lshlrev_b32_sdwa v59,1,v59 dst_sel:DWORD dst_unused:UNUSED_PAD src0_sel:DWORD src1_sel:WORD_1
v_mfma_f32_16x16x32_bf16 v[92:95],a[252:255],v[28:31],v[92:95]
ds_read_u16 v57,v57
v_lshlrev_b32_sdwa v52,1,v52 dst_sel:DWORD dst_unused:UNUSED_PAD src0_sel:DWORD src1_sel:WORD_1
v_mfma_f32_16x16x32_bf16 v[88:91],a[252:255],v[24:27],v[88:91]
ds_read_u16_d16_hi v59,v59
v_rcp_f16_sdwa v9,v8 dst_sel:WORD_1 dst_unused:UNUSED_PRESERVE src0_sel:WORD_1
v_mfma_f32_16x16x32_bf16 v[128:131],a[240:243],v[24:27],v[128:131]
ds_read_b64_tr_b16 a[192:193],v2 offset:12320
v_lshlrev_b32_sdwa v54,1,v54 dst_sel:DWORD dst_unused:UNUSED_PAD src0_sel:DWORD src1_sel:WORD_1
v_mfma_f32_16x16x32_bf16 v[132:135],a[240:243],v[28:31],v[132:135]
ds_read_b64_tr_b16 a[194:195],v2 offset:13344
v_fma_mix_f32 v53,v53,v9,0 op_sel_hi:[0,1,0]
v_mfma_f32_16x16x32_bf16 v[108:111],a[224:227],v[28:31],v[108:111]
ds_read_u16 v52,v52
v_fma_mix_f32 v55,v55,v9,0 op_sel:[0,1,0] op_sel_hi:[0,1,0]
v_mfma_f32_16x16x32_bf16 v[104:107],a[224:227],v[24:27],v[104:107]
s_waitcnt lgkmcnt(7)
ds_read_u16_d16_hi v54,v54
v_mfma_f32_16x16x32_bf16 v[28:31],a[208:211],a[0:3],v[20:23]
s_add_u32 m0,s57,77800
v_lshlrev_b32_sdwa v40,1,v40 dst_sel:DWORD dst_unused:UNUSED_PAD src0_sel:DWORD src1_sel:WORD_1
v_lshlrev_b32_sdwa v42,1,v42 dst_sel:DWORD dst_unused:UNUSED_PAD src0_sel:DWORD src1_sel:WORD_1
v_mfma_f32_16x16x32_bf16 v[28:31],a[204:207],a[16:19],v[28:31]
s_add_u32 s89,s56,4620288
ds_read_u16 v40,v40
v_lshlrev_b32_sdwa v41,1,v41 dst_sel:DWORD dst_unused:UNUSED_PAD src0_sel:DWORD src1_sel:WORD_1
v_mfma_f32_16x16x32_bf16 v[28:31],a[212:215],a[32:35],v[28:31]
buffer_load_dwordx4 v6,s[36:39],s89 offen lds
v_lshl_add_u64 v[32:33],v[32:33],0,v[34:35]
v_mfma_f32_16x16x32_bf16 v[24:27],a[200:203],a[32:35],v[16:19]
ds_read_u16_d16_hi v42,v42
v_lshl_add_u64 v[34:35],v[60:61],0,v[62:63]
v_mfma_f32_16x16x32_bf16 v[24:27],a[196:199],a[16:19],v[24:27]
s_waitcnt lgkmcnt(4)
ds_read_u16 v41,v41
v_lshl_add_u64 v[60:61],v[56:57],0,v[58:59]
v_mfma_f32_16x16x32_bf16 v[24:27],a[192:195],a[0:3],v[24:27]
TP3284_%=:
s_cbranch_execz TP5414_%=
v_lshlrev_b32_sdwa v43,1,v43 dst_sel:DWORD dst_unused:UNUSED_PAD src0_sel:DWORD src1_sel:WORD_1
v_lshlrev_b32_sdwa v36,1,v36 dst_sel:DWORD dst_unused:UNUSED_PAD src0_sel:DWORD src1_sel:WORD_1
v_mfma_f32_16x16x32_bf16 v[56:59],a[192:195],a[4:7],v[16:19]
s_waitcnt lgkmcnt(3)
ds_read_u16_d16_hi v43,v43
v_or_b32_e64 v62,v52,v54
v_mfma_f32_16x16x32_bf16 v[56:59],a[196:199],a[20:23],v[56:59]
s_waitcnt vmcnt(7)
ds_read_u16 v36,v36
v_cvt_pk_bf16_f32 v63,v53,v55
v_lshlrev_b32_sdwa v38,1,v38 dst_sel:DWORD dst_unused:UNUSED_PAD src0_sel:DWORD src1_sel:WORD_1
v_mfma_f32_16x16x32_bf16 v[56:59],a[200:203],a[36:39],v[56:59]
s_barrier
ds_read_u16_d16_hi v38,v38
v_lshlrev_b32_sdwa v37,1,v37 dst_sel:DWORD dst_unused:UNUSED_PAD src0_sel:DWORD src1_sel:WORD_1
v_mfma_f32_16x16x32_bf16 v[52:55],a[212:215],a[36:39],v[20:23]
s_add_u32 m0,s57,81328
ds_read_u16 v37,v37
v_lshlrev_b32_sdwa v39,1,v39 dst_sel:DWORD dst_unused:UNUSED_PAD src0_sel:DWORD src1_sel:WORD_1
v_mfma_f32_16x16x32_bf16 v[52:55],a[204:207],a[20:23],v[52:55]
s_add_u32 s89,s56,4718592
ds_read_u16_d16_hi v39,v39
v_lshlrev_b32_sdwa v44,1,v44 dst_sel:DWORD dst_unused:UNUSED_PAD src0_sel:DWORD src1_sel:WORD_1
v_mfma_f32_16x16x32_bf16 v[52:55],a[208:211],a[4:7],v[52:55]
buffer_load_dwordx4 v6,s[36:39],s89 offen lds
v_lshlrev_b32_sdwa v46,1,v46 dst_sel:DWORD dst_unused:UNUSED_PAD src0_sel:DWORD src1_sel:WORD_1
v_mfma_f32_16x16x32_bf16 v[232:235],a[236:239],v[32:35],v[232:235]
s_add_u32 m0,s58,6160
ds_read_u16 v44,v44
v_lshlrev_b32_sdwa v45,1,v45 dst_sel:DWORD dst_unused:UNUSED_PAD src0_sel:DWORD src1_sel:WORD_1
v_mfma_f32_16x16x32_bf16 v[236:239],a[236:239],v[60:63],v[236:239]
s_add_u32 s69,s69,s59
ds_read_u16_d16_hi v46,v46
v_lshlrev_b32_sdwa v47,1,v47 dst_sel:DWORD dst_unused:UNUSED_PAD src0_sel:DWORD src1_sel:WORD_1
v_mfma_f32_16x16x32_bf16 v[164:167],a[244:247],v[60:63],v[164:167]
buffer_load_dwordx4 v5,s[44:47],s69 offen offset:0 lds
ds_read_u16 v45,v45
v_mfma_f32_16x16x32_bf16 v[160:163],a[244:247],v[32:35],v[160:163]
ds_read_u16_d16_hi v47,v47
ds_read_b64_tr_b16 a[228:229],v3 offset:18480
v_mfma_f32_16x16x32_bf16 v[240:243],a[248:251],v[32:35],v[240:243]
s_add_u32 s69,s69,s53
ds_read_b64_tr_b16 a[230:231],v3 offset:20016
v_lshlrev_b32_sdwa v48,1,v48 dst_sel:DWORD dst_unused:UNUSED_PAD src0_sel:DWORD src1_sel:WORD_1
v_mfma_f32_16x16x32_bf16 v[244:247],a[248:251],v[60:63],v[244:247]
buffer_load_dwordx4 v5,s[44:47],s69 offen offset:1024 lds
v_lshlrev_b32_sdwa v50,1,v50 dst_sel:DWORD dst_unused:UNUSED_PAD src0_sel:DWORD src1_sel:WORD_1
v_mfma_f32_16x16x32_bf16 v[180:183],a[252:255],v[60:63],v[180:183]
ds_read_u16 v48,v48
v_lshlrev_b32_sdwa v49,1,v49 dst_sel:DWORD dst_unused:UNUSED_PAD src0_sel:DWORD src1_sel:WORD_1
v_mfma_f32_16x16x32_bf16 v[176:179],a[252:255],v[32:35],v[176:179]
ds_read_u16_d16_hi v50,v50
v_lshlrev_b32_sdwa v51,1,v51 dst_sel:DWORD dst_unused:UNUSED_PAD src0_sel:DWORD src1_sel:WORD_1
v_mfma_f32_16x16x32_bf16 v[248:251],a[240:243],v[32:35],v[248:251]
ds_read_u16 v49,v49
v_mfma_f32_16x16x32_bf16 v[252:255],a[240:243],v[60:63],v[252:255]
s_add_u32 s69,s69,s53
ds_read_u16_d16_hi v51,v51
v_mfma_f32_16x16x32_bf16 v[196:199],a[224:227],v[60:63],v[196:199]
buffer_load_dwordx4 v5,s[44:47],s69 offen offset:2048 lds
v_mfma_f32_16x16x32_bf16 v[192:195],a[224:227],v[32:35],v[192:195]
ds_read_b64_tr_b16 a[232:233],v3 offset:18488
v_mfma_f32_16x16x32_bf16 v[60:63],a[208:211],a[8:11],v[20:23]
ds_read_b64_tr_b16 a[234:235],v3 offset:20024
v_lshlrev_b32_sdwa v28,1,v28 dst_sel:DWORD dst_unused:UNUSED_PAD src0_sel:DWORD src1_sel:WORD_1
v_mfma_f32_16x16x32_bf16 v[60:63],a[204:207],a[24:27],v[60:63]
ds_read_u16 v28,v28
v_fma_mixlo_f16 v8,v57,s99,0
v_mfma_f32_16x16x32_bf16 v[60:63],a[212:215],a[40:43],v[60:63]
s_waitcnt lgkmcnt(7)
ds_read_b64_tr_b16 a[220:221],v1 offset:18480
v_lshl_add_u64 v[40:41],v[40:41],0,v[42:43]
v_lshlrev_b32_sdwa v30,1,v30 dst_sel:DWORD dst_unused:UNUSED_PAD src0_sel:DWORD src1_sel:WORD_1
v_mfma_f32_16x16x32_bf16 v[32:35],a[200:203],a[40:43],v[16:19]
ds_read_u16_d16_hi v30,v30
v_lshl_add_u64 v[42:43],v[36:37],0,v[38:39]
v_fma_mixhi_f16 v8,v59,s99,0
v_mfma_f32_16x16x32_bf16 v[32:35],a[196:199],a[24:27],v[32:35]
ds_read_b64_tr_b16 a[222:223],v1 offset:19504
v_lshl_add_u64 v[36:37],v[44:45],0,v[46:47]
v_lshlrev_b32_sdwa v29,1,v29 dst_sel:DWORD dst_unused:UNUSED_PAD src0_sel:DWORD src1_sel:WORD_1
v_pk_fma_f16 v9,v8,v8,s54
v_mfma_f32_16x16x32_bf16 v[32:35],a[192:195],a[8:11],v[32:35]
ds_read_u16 v29,v29
v_lshlrev_b32_sdwa v31,1,v31 dst_sel:DWORD dst_unused:UNUSED_PAD src0_sel:DWORD src1_sel:WORD_1
v_mfma_f32_16x16x32_bf16 v[44:47],a[192:195],a[12:15],v[16:19]
ds_read_u16_d16_hi v31,v31
v_pk_mul_f16 v9,v8,v9
v_mfma_f32_16x16x32_bf16 v[44:47],a[196:199],a[28:31],v[44:47]
s_waitcnt lgkmcnt(8)
ds_read_b64_tr_b16 a[216:217],v1 offset:20528
v_lshl_add_u64 v[38:39],v[48:49],0,v[50:51]
v_lshlrev_b32_sdwa v24,1,v24 dst_sel:DWORD dst_unused:UNUSED_PAD src0_sel:DWORD src1_sel:WORD_1
v_mfma_f32_16x16x32_bf16 v[44:47],a[200:203],a[44:47],v[44:47]
v_exp_f16 v8,v9
v_mfma_f32_16x16x32_bf16 v[48:51],a[212:215],a[44:47],v[20:23]
ds_read_u16 v24,v24
v_lshlrev_b32_sdwa v26,1,v26 dst_sel:DWORD dst_unused:UNUSED_PAD src0_sel:DWORD src1_sel:WORD_1
v_mfma_f32_16x16x32_bf16 v[48:51],a[204:207],a[28:31],v[48:51]
ds_read_u16_d16_hi v26,v26
v_lshlrev_b32_sdwa v25,1,v25 dst_sel:DWORD dst_unused:UNUSED_PAD src0_sel:DWORD src1_sel:WORD_1
v_mfma_f32_16x16x32_bf16 v[48:51],a[208:211],a[12:15],v[48:51]
v_exp_f16_sdwa v8,v9 dst_sel:WORD_1 dst_unused:UNUSED_PRESERVE src0_sel:WORD_1
v_mfma_f32_16x16x32_bf16 v[208:211],a[236:239],v[40:43],v[208:211]
ds_read_u16 v25,v25
v_lshlrev_b32_sdwa v27,1,v27 dst_sel:DWORD dst_unused:UNUSED_PAD src0_sel:DWORD src1_sel:WORD_1
v_mfma_f32_16x16x32_bf16 v[212:215],a[236:239],v[36:39],v[212:215]
ds_read_u16_d16_hi v27,v27
v_lshlrev_b32_sdwa v52,1,v52 dst_sel:DWORD dst_unused:UNUSED_PAD src0_sel:DWORD src1_sel:WORD_1
v_mfma_f32_16x16x32_bf16 v[172:175],a[244:247],v[36:39],v[172:175]
ds_read_u16 v52,v52
v_pk_fma_f16 v8,v8,s55,s55
v_mfma_f32_16x16x32_bf16 v[168:171],a[244:247],v[40:43],v[168:171]
ds_read_b64_tr_b16 a[236:237],v3 offset:18544
v_rcp_f16_e64 v9,v8
v_lshlrev_b32_sdwa v54,1,v54 dst_sel:DWORD dst_unused:UNUSED_PAD src0_sel:DWORD src1_sel:WORD_1
v_mfma_f32_16x16x32_bf16 v[216:219],a[248:251],v[40:43],v[216:219]
ds_read_u16_d16_hi v54,v54
v_lshlrev_b32_sdwa v53,1,v53 dst_sel:DWORD dst_unused:UNUSED_PAD src0_sel:DWORD src1_sel:WORD_1
v_mfma_f32_16x16x32_bf16 v[220:223],a[248:251],v[36:39],v[220:223]
ds_read_u16 v53,v53
ds_read_b64_tr_b16 a[238:239],v3 offset:20080
v_mfma_f32_16x16x32_bf16 v[188:191],a[252:255],v[36:39],v[188:191]
ds_read_b64_tr_b16 a[244:245],v3 offset:18552
v_lshlrev_b32_sdwa v55,1,v55 dst_sel:DWORD dst_unused:UNUSED_PAD src0_sel:DWORD src1_sel:WORD_1
v_mfma_f32_16x16x32_bf16 v[184:187],a[252:255],v[40:43],v[184:187]
ds_read_u16_d16_hi v55,v55
v_rcp_f16_sdwa v9,v8 dst_sel:WORD_1 dst_unused:UNUSED_PRESERVE src0_sel:WORD_1
v_mfma_f32_16x16x32_bf16 v[224:227],a[240:243],v[40:43],v[224:227]
ds_read_b64_tr_b16 a[246:247],v3 offset:20088
ds_read_b64_tr_b16 a[248:249],v3 offset:18608
v_lshlrev_b32_sdwa v56,1,v56 dst_sel:DWORD dst_unused:UNUSED_PAD src0_sel:DWORD src1_sel:WORD_1
v_mfma_f32_16x16x32_bf16 v[228:231],a[240:243],v[36:39],v[228:231]
ds_read_u16 v56,v56
v_lshlrev_b32_sdwa v58,1,v58 dst_sel:DWORD dst_unused:UNUSED_PAD src0_sel:DWORD src1_sel:WORD_1
v_mfma_f32_16x16x32_bf16 v[204:207],a[224:227],v[36:39],v[204:207]
ds_read_u16_d16_hi v58,v58
v_fma_mix_f32 v57,v57,v9,0 op_sel_hi:[0,1,0]
v_mfma_f32_16x16x32_bf16 v[200:203],a[224:227],v[40:43],v[200:203]
ds_read_b64_tr_b16 a[250:251],v3 offset:20144
v_fma_mix_f32 v59,v59,v9,0 op_sel:[0,1,0] op_sel_hi:[0,1,0]
v_mfma_f32_16x16x32_bf16 v[36:39],a[208:211],a[48:51],v[20:23]
s_add_u32 m0,s58,12320
ds_read_b64 v[14:15],v7 offset:18480
v_lshlrev_b32_sdwa v60,1,v60 dst_sel:DWORD dst_unused:UNUSED_PAD src0_sel:DWORD src1_sel:WORD_1
v_mfma_f32_16x16x32_bf16 v[36:39],a[204:207],a[64:67],v[36:39]
s_add_u32 s69,s69,s59
ds_read_b64_tr_b16 a[252:253],v3 offset:18616
v_lshlrev_b32_sdwa v62,1,v62 dst_sel:DWORD dst_unused:UNUSED_PAD src0_sel:DWORD src1_sel:WORD_1
v_mfma_f32_16x16x32_bf16 v[36:39],a[212:215],a[80:83],v[36:39]
s_waitcnt lgkmcnt(9)
buffer_load_dwordx4 v5,s[44:47],s69 offen offset:0 lds
ds_read_b64_tr_b16 a[254:255],v3 offset:20152
v_lshl_add_u64 v[28:29],v[28:29],0,v[30:31]
v_mfma_f32_16x16x32_bf16 v[40:43],a[200:203],a[80:83],v[16:19]
s_add_u32 s69,s69,s53
ds_read_u16 v60,v60
v_lshl_add_u64 v[30:31],v[24:25],0,v[26:27]
v_lshlrev_b32_sdwa v61,1,v61 dst_sel:DWORD dst_unused:UNUSED_PAD src0_sel:DWORD src1_sel:WORD_1
v_mfma_f32_16x16x32_bf16 v[40:43],a[196:199],a[64:67],v[40:43]
s_waitcnt lgkmcnt(9)
buffer_load_dwordx4 v5,s[44:47],s69 offen offset:1024 lds
v_lshl_add_u64 v[24:25],v[52:53],0,v[54:55]
v_mfma_f32_16x16x32_bf16 v[40:43],a[192:195],a[48:51],v[40:43]
s_waitcnt lgkmcnt(3)
ds_read_u16_d16_hi v62,v62
v_or3_b32 v13,v14,v15,v13
v_lshlrev_b32_sdwa v63,1,v63 dst_sel:DWORD dst_unused:UNUSED_PAD src0_sel:DWORD src1_sel:WORD_1
v_mfma_f32_16x16x32_bf16 v[52:55],a[192:195],a[52:55],v[16:19]
s_add_u32 m0,s57,84864
ds_read_b64 v[14:15],v7 offset:18992
v_or_b32 v26,v56,v58
v_lshlrev_b32_sdwa v32,1,v32 dst_sel:DWORD dst_unused:UNUSED_PAD src0_sel:DWORD src1_sel:WORD_1
v_mfma_f32_16x16x32_bf16 v[52:55],a[196:199],a[68:71],v[52:55]
s_add_u32 s89,s56,4816896
ds_read_u16 v61,v61
v_cvt_pk_bf16_f32 v27,v57,v59
v_lshlrev_b32_sdwa v34,1,v34 dst_sel:DWORD dst_unused:UNUSED_PAD src0_sel:DWORD src1_sel:WORD_1
v_mfma_f32_16x16x32_bf16 v[52:55],a[200:203],a[84:87],v[52:55]
buffer_load_dwordx4 v6,s[36:39],s89 offen lds
ds_read_u16_d16_hi v63,v63
v_mfma_f32_16x16x32_bf16 v[56:59],a[212:215],a[84:87],v[20:23]
s_add_u32 m0,s58,12320
ds_read_u16 v32,v32
v_lshlrev_b32_sdwa v33,1,v33 dst_sel:DWORD dst_unused:UNUSED_PAD src0_sel:DWORD src1_sel:WORD_1
v_mfma_f32_16x16x32_bf16 v[56:59],a[204:207],a[68:71],v[56:59]
s_add_u32 s69,s69,s53
ds_read_u16_d16_hi v34,v34
v_lshlrev_b32_sdwa v35,1,v35 dst_sel:DWORD dst_unused:UNUSED_PAD src0_sel:DWORD src1_sel:WORD_1
v_mfma_f32_16x16x32_bf16 v[56:59],a[208:211],a[52:55],v[56:59]
s_waitcnt lgkmcnt(4)
buffer_load_dwordx4 v5,s[44:47],s69 offen offset:2048 lds
v_or3_b32 v13,v14,v15,v13
v_mfma_f32_16x16x32_bf16 v[136:139],a[228:231],v[28:31],v[136:139]
s_add_u32 m0,s57,8
ds_read_b64 v[14:15],v7 offset:19504
v_lshlrev_b32_sdwa v48,1,v48 dst_sel:DWORD dst_unused:UNUSED_PAD src0_sel:DWORD src1_sel:WORD_1
v_mfma_f32_16x16x32_bf16 v[140:143],a[228:231],v[24:27],v[140:143]
s_add_u32 s89,s56,4915200
ds_read_u16 v33,v33
v_lshlrev_b32_sdwa v50,1,v50 dst_sel:DWORD dst_unused:UNUSED_PAD src0_sel:DWORD src1_sel:WORD_1
v_mfma_f32_16x16x32_bf16 v[68:71],a[232:235],v[24:27],v[68:71]
buffer_load_dwordx4 v6,s[36:39],s89 offen lds
ds_read_u16_d16_hi v35,v35
v_lshlrev_b32_sdwa v49,1,v49 dst_sel:DWORD dst_unused:UNUSED_PAD src0_sel:DWORD src1_sel:WORD_1
v_mfma_f32_16x16x32_bf16 v[64:67],a[232:235],v[28:31],v[64:67]
ds_read_u16 v48,v48
v_lshlrev_b32_sdwa v51,1,v51 dst_sel:DWORD dst_unused:UNUSED_PAD src0_sel:DWORD src1_sel:WORD_1
v_mfma_f32_16x16x32_bf16 v[144:147],a[236:239],v[28:31],v[144:147]
ds_read_u16_d16_hi v50,v50
v_lshlrev_b32_sdwa v44,1,v44 dst_sel:DWORD dst_unused:UNUSED_PAD src0_sel:DWORD src1_sel:WORD_1
v_mfma_f32_16x16x32_bf16 v[148:151],a[236:239],v[24:27],v[148:151]
s_waitcnt lgkmcnt(4)
ds_read_u16 v49,v49
v_or3_b32 v13,v14,v15,v13
v_mfma_f32_16x16x32_bf16 v[84:87],a[244:247],v[24:27],v[84:87]
ds_read_u16_d16_hi v51,v51
v_lshlrev_b32_sdwa v46,1,v46 dst_sel:DWORD dst_unused:UNUSED_PAD src0_sel:DWORD src1_sel:WORD_1
v_mfma_f32_16x16x32_bf16 v[80:83],a[244:247],v[28:31],v[80:83]
ds_read_u16 v44,v44
v_lshlrev_b32_sdwa v45,1,v45 dst_sel:DWORD dst_unused:UNUSED_PAD src0_sel:DWORD src1_sel:WORD_1
v_mfma_f32_16x16x32_bf16 v[152:155],a[248:251],v[28:31],v[152:155]
ds_read_u16_d16_hi v46,v46
v_lshlrev_b32_sdwa v47,1,v47 dst_sel:DWORD dst_unused:UNUSED_PAD src0_sel:DWORD src1_sel:WORD_1
v_mfma_f32_16x16x32_bf16 v[156:159],a[248:251],v[24:27],v[156:159]
ds_read_u16 v45,v45
v_mfma_f32_16x16x32_bf16 v[100:103],a[252:255],v[24:27],v[100:103]
ds_read_u16_d16_hi v47,v47
v_mfma_f32_16x16x32_bf16 v[96:99],a[252:255],v[28:31],v[96:99]
ds_read_b64_tr_b16 a[218:219],v1 offset:21552
v_mfma_f32_16x16x32_bf16 v[24:27],a[208:211],a[56:59],v[20:23]
v_lshlrev_b32_sdwa v36,1,v36 dst_sel:DWORD dst_unused:UNUSED_PAD src0_sel:DWORD src1_sel:WORD_1
v_fma_mixlo_f16 v8,v53,s99,0
v_mfma_f32_16x16x32_bf16 v[24:27],a[204:207],a[72:75],v[24:27]
ds_read_u16 v36,v36
v_lshlrev_b32_sdwa v38,1,v38 dst_sel:DWORD dst_unused:UNUSED_PAD src0_sel:DWORD src1_sel:WORD_1
v_mfma_f32_16x16x32_bf16 v[24:27],a[212:215],a[88:91],v[24:27]
ds_read_u16_d16_hi v38,v38
v_lshl_add_u64 v[60:61],v[60:61],0,v[62:63]
v_mfma_f32_16x16x32_bf16 v[28:31],a[200:203],a[88:91],v[16:19]
s_waitcnt lgkmcnt(7)
v_lshl_add_u64 v[62:63],v[32:33],0,v[34:35]
v_fma_mixhi_f16 v8,v55,s99,0
v_mfma_f32_16x16x32_bf16 v[28:31],a[196:199],a[72:75],v[28:31]
v_lshl_add_u64 v[32:33],v[48:49],0,v[50:51]
v_lshlrev_b32_sdwa v37,1,v37 dst_sel:DWORD dst_unused:UNUSED_PAD src0_sel:DWORD src1_sel:WORD_1
v_pk_fma_f16 v9,v8,v8,s54
v_mfma_f32_16x16x32_bf16 v[28:31],a[192:195],a[56:59],v[28:31]
ds_read_u16 v37,v37
v_lshlrev_b32_sdwa v39,1,v39 dst_sel:DWORD dst_unused:UNUSED_PAD src0_sel:DWORD src1_sel:WORD_1
v_mfma_f32_16x16x32_bf16 v[48:51],a[192:195],a[60:63],v[16:19]
ds_read_b128 v[16:19],v4 offset:1024
v_pk_mul_f16 v9,v8,v9
v_mfma_f32_16x16x32_bf16 v[48:51],a[196:199],a[76:79],v[48:51]
s_waitcnt lgkmcnt(5)
ds_read_u16_d16_hi v39,v39
ds_read_b64_tr_b16 a[192:193],v1 offset:22576
v_lshl_add_u64 v[34:35],v[44:45],0,v[46:47]
v_lshlrev_b32_sdwa v40,1,v40 dst_sel:DWORD dst_unused:UNUSED_PAD src0_sel:DWORD src1_sel:WORD_1
v_mfma_f32_16x16x32_bf16 v[48:51],a[200:203],a[92:95],v[48:51]
ds_read_b64_tr_b16 a[194:195],v1 offset:23600
v_exp_f16_e64 v8,v9
v_mfma_f32_16x16x32_bf16 v[44:47],a[212:215],a[92:95],v[20:23]
ds_read_b128 v[20:23],v4 offset:1056
ds_read_b64_tr_b16 a[196:197],v2 offset:22576
v_lshlrev_b32_sdwa v42,1,v42 dst_sel:DWORD dst_unused:UNUSED_PAD src0_sel:DWORD src1_sel:WORD_1
v_mfma_f32_16x16x32_bf16 v[44:47],a[204:207],a[76:79],v[44:47]
ds_read_b64_tr_b16 a[198:199],v2 offset:23600
ds_read_u16 v40,v40
v_lshlrev_b32_sdwa v41,1,v41 dst_sel:DWORD dst_unused:UNUSED_PAD src0_sel:DWORD src1_sel:WORD_1
v_mfma_f32_16x16x32_bf16 v[44:47],a[208:211],a[60:63],v[44:47]
ds_read_u16_d16_hi v42,v42
v_exp_f16_sdwa v8,v9 dst_sel:WORD_1 dst_unused:UNUSED_PRESERVE src0_sel:WORD_1
v_mfma_f32_16x16x32_bf16 v[112:115],a[228:231],v[60:63],v[112:115]
ds_read_u16 v41,v41
ds_read_b64_tr_b16 a[200:201],v2 offset:20528
v_lshlrev_b32_sdwa v43,1,v43 dst_sel:DWORD dst_unused:UNUSED_PAD src0_sel:DWORD src1_sel:WORD_1
v_mfma_f32_16x16x32_bf16 v[116:119],a[228:231],v[32:35],v[116:119]
ds_read_u16_d16_hi v43,v43
v_lshlrev_b32_sdwa v56,1,v56 dst_sel:DWORD dst_unused:UNUSED_PAD src0_sel:DWORD src1_sel:WORD_1
v_mfma_f32_16x16x32_bf16 v[76:79],a[232:235],v[32:35],v[76:79]
ds_read_b64_tr_b16 a[202:203],v2 offset:21552
v_pk_fma_f16 v8,v8,s55,s55
v_mfma_f32_16x16x32_bf16 v[72:75],a[232:235],v[60:63],v[72:75]
v_rcp_f16 v9,v8
v_lshlrev_b32_sdwa v58,1,v58 dst_sel:DWORD dst_unused:UNUSED_PAD src0_sel:DWORD src1_sel:WORD_1
v_mfma_f32_16x16x32_bf16 v[120:123],a[236:239],v[60:63],v[120:123]
ds_read_u16 v56,v56
v_lshlrev_b32_sdwa v57,1,v57 dst_sel:DWORD dst_unused:UNUSED_PAD src0_sel:DWORD src1_sel:WORD_1
v_mfma_f32_16x16x32_bf16 v[124:127],a[236:239],v[32:35],v[124:127]
ds_read_u16_d16_hi v58,v58
v_lshlrev_b32_sdwa v59,1,v59 dst_sel:DWORD dst_unused:UNUSED_PAD src0_sel:DWORD src1_sel:WORD_1
v_mfma_f32_16x16x32_bf16 v[92:95],a[244:247],v[32:35],v[92:95]
ds_read_u16 v57,v57
v_lshlrev_b32_sdwa v52,1,v52 dst_sel:DWORD dst_unused:UNUSED_PAD src0_sel:DWORD src1_sel:WORD_1
v_mfma_f32_16x16x32_bf16 v[88:91],a[244:247],v[60:63],v[88:91]
ds_read_u16_d16_hi v59,v59
v_rcp_f16_sdwa v9,v8 dst_sel:WORD_1 dst_unused:UNUSED_PRESERVE src0_sel:WORD_1
v_mfma_f32_16x16x32_bf16 v[128:131],a[248:251],v[60:63],v[128:131]
ds_read_b64_tr_b16 a[212:213],v2 offset:18480
v_lshlrev_b32_sdwa v54,1,v54 dst_sel:DWORD dst_unused:UNUSED_PAD src0_sel:DWORD src1_sel:WORD_1
v_mfma_f32_16x16x32_bf16 v[132:135],a[248:251],v[32:35],v[132:135]
ds_read_b64_tr_b16 a[214:215],v2 offset:19504
v_fma_mix_f32 v53,v53,v9,0 op_sel_hi:[0,1,0]
v_mfma_f32_16x16x32_bf16 v[108:111],a[252:255],v[32:35],v[108:111]
ds_read_u16 v52,v52
v_fma_mix_f32 v55,v55,v9,0 op_sel:[0,1,0] op_sel_hi:[0,1,0]
v_mfma_f32_16x16x32_bf16 v[104:107],a[252:255],v[60:63],v[104:107]
s_waitcnt lgkmcnt(7)
ds_read_u16_d16_hi v54,v54
v_mfma_f32_16x16x32_bf16 v[32:35],a[220:223],a[0:3],v[16:19]
s_add_u32 m0,s57,3544
v_lshlrev_b32_sdwa v24,1,v24 dst_sel:DWORD dst_unused:UNUSED_PAD src0_sel:DWORD src1_sel:WORD_1
v_lshlrev_b32_sdwa v26,1,v26 dst_sel:DWORD dst_unused:UNUSED_PAD src0_sel:DWORD src1_sel:WORD_1
v_mfma_f32_16x16x32_bf16 v[32:35],a[216:219],a[16:19],v[32:35]
s_add_u32 s89,s56,5013504
ds_read_u16 v24,v24
v_lshlrev_b32_sdwa v25,1,v25 dst_sel:DWORD dst_unused:UNUSED_PAD src0_sel:DWORD src1_sel:WORD_1
v_mfma_f32_16x16x32_bf16 v[32:35],a[192:195],a[32:35],v[32:35]
buffer_load_dwordx4 v6,s[36:39],s89 offen lds
v_lshl_add_u64 v[36:37],v[36:37],0,v[38:39]
v_mfma_f32_16x16x32_bf16 v[60:63],a[196:199],a[32:35],v[20:23]
s_add_u32 m0,s57,7072
ds_read_u16_d16_hi v26,v26
v_lshl_add_u64 v[38:39],v[40:41],0,v[42:43]
v_mfma_f32_16x16x32_bf16 v[60:63],a[200:203],a[16:19],v[60:63]
s_waitcnt lgkmcnt(4)
ds_read_u16 v25,v25
v_lshl_add_u64 v[40:41],v[56:57],0,v[58:59]
v_mfma_f32_16x16x32_bf16 v[60:63],a[212:215],a[0:3],v[60:63]
s_add_u32 s89,s56,5111808
v_lshlrev_b32_sdwa v27,1,v27 dst_sel:DWORD dst_unused:UNUSED_PAD src0_sel:DWORD src1_sel:WORD_1
v_lshlrev_b32_sdwa v28,1,v28 dst_sel:DWORD dst_unused:UNUSED_PAD src0_sel:DWORD src1_sel:WORD_1
v_mfma_f32_16x16x32_bf16 v[56:59],a[212:215],a[4:7],v[20:23]
s_waitcnt lgkmcnt(3)
buffer_load_dwordx4 v6,s[36:39],s89 offen lds
v_or_b32 v42,v52,v54
v_mfma_f32_16x16x32_bf16 v[56:59],a[200:203],a[20:23],v[56:59]
ds_read_u16_d16_hi v27,v27
v_cvt_pk_bf16_f32 v43,v53,v55
v_lshlrev_b32_sdwa v30,1,v30 dst_sel:DWORD dst_unused:UNUSED_PAD src0_sel:DWORD src1_sel:WORD_1
v_mfma_f32_16x16x32_bf16 v[56:59],a[196:199],a[36:39],v[56:59]
ds_read_u16 v28,v28
v_lshlrev_b32_sdwa v29,1,v29 dst_sel:DWORD dst_unused:UNUSED_PAD src0_sel:DWORD src1_sel:WORD_1
v_mfma_f32_16x16x32_bf16 v[52:55],a[192:195],a[36:39],v[16:19]
ds_read_u16_d16_hi v30,v30
v_lshlrev_b32_sdwa v31,1,v31 dst_sel:DWORD dst_unused:UNUSED_PAD src0_sel:DWORD src1_sel:WORD_1
v_mfma_f32_16x16x32_bf16 v[52:55],a[216:219],a[20:23],v[52:55]
ds_read_u16 v29,v29
v_lshlrev_b32_sdwa v44,1,v44 dst_sel:DWORD dst_unused:UNUSED_PAD src0_sel:DWORD src1_sel:WORD_1
v_mfma_f32_16x16x32_bf16 v[52:55],a[220:223],a[4:7],v[52:55]
ds_read_u16_d16_hi v31,v31
v_lshlrev_b32_sdwa v46,1,v46 dst_sel:DWORD dst_unused:UNUSED_PAD src0_sel:DWORD src1_sel:WORD_1
v_mfma_f32_16x16x32_bf16 v[232:235],a[228:231],v[36:39],v[232:235]
ds_read_u16 v44,v44
v_lshlrev_b32_sdwa v45,1,v45 dst_sel:DWORD dst_unused:UNUSED_PAD src0_sel:DWORD src1_sel:WORD_1
v_mfma_f32_16x16x32_bf16 v[236:239],a[228:231],v[40:43],v[236:239]
ds_read_u16_d16_hi v46,v46
v_lshlrev_b32_sdwa v47,1,v47 dst_sel:DWORD dst_unused:UNUSED_PAD src0_sel:DWORD src1_sel:WORD_1
v_mfma_f32_16x16x32_bf16 v[164:167],a[232:235],v[40:43],v[164:167]
ds_read_u16 v45,v45
ds_read_u16_d16_hi v47,v47
v_mfma_f32_16x16x32_bf16 v[160:163],a[232:235],v[36:39],v[160:163]
ds_read_b64_tr_b16 a[240:241],v3 offset:0
v_lshlrev_b32_sdwa v48,1,v48 dst_sel:DWORD dst_unused:UNUSED_PAD src0_sel:DWORD src1_sel:WORD_1
v_mfma_f32_16x16x32_bf16 v[240:243],a[236:239],v[36:39],v[240:243]
ds_read_u16 v48,v48
v_lshlrev_b32_sdwa v50,1,v50 dst_sel:DWORD dst_unused:UNUSED_PAD src0_sel:DWORD src1_sel:WORD_1
v_mfma_f32_16x16x32_bf16 v[244:247],a[236:239],v[40:43],v[244:247]
ds_read_u16_d16_hi v50,v50
v_lshlrev_b32_sdwa v49,1,v49 dst_sel:DWORD dst_unused:UNUSED_PAD src0_sel:DWORD src1_sel:WORD_1
v_mfma_f32_16x16x32_bf16 v[180:183],a[244:247],v[40:43],v[180:183]
ds_read_u16 v49,v49
v_lshlrev_b32_sdwa v51,1,v51 dst_sel:DWORD dst_unused:UNUSED_PAD src0_sel:DWORD src1_sel:WORD_1
v_mfma_f32_16x16x32_bf16 v[176:179],a[244:247],v[36:39],v[176:179]
ds_read_u16_d16_hi v51,v51
v_mfma_f32_16x16x32_bf16 v[248:251],a[248:251],v[36:39],v[248:251]
ds_read_b64_tr_b16 a[242:243],v3 offset:1536
v_mfma_f32_16x16x32_bf16 v[252:255],a[248:251],v[40:43],v[252:255]
ds_read_b64_tr_b16 a[224:225],v3 offset:8
v_mfma_f32_16x16x32_bf16 v[196:199],a[252:255],v[40:43],v[196:199]
ds_read_b64_tr_b16 a[226:227],v3 offset:1544
v_mfma_f32_16x16x32_bf16 v[192:195],a[252:255],v[36:39],v[192:195]
v_mfma_f32_16x16x32_bf16 v[40:43],a[220:223],a[8:11],v[16:19]
ds_read_b64_tr_b16 a[204:205],v1 offset:0
v_lshlrev_b32_sdwa v32,1,v32 dst_sel:DWORD dst_unused:UNUSED_PAD src0_sel:DWORD src1_sel:WORD_1
v_mfma_f32_16x16x32_bf16 v[40:43],a[216:219],a[24:27],v[40:43]
ds_read_u16 v32,v32
v_fma_mixlo_f16 v8,v57,s99,0
v_mfma_f32_16x16x32_bf16 v[40:43],a[192:195],a[40:43],v[40:43]
s_waitcnt lgkmcnt(7)
ds_read_b64_tr_b16 a[206:207],v1 offset:1024
v_lshl_add_u64 v[24:25],v[24:25],0,v[26:27]
v_lshlrev_b32_sdwa v34,1,v34 dst_sel:DWORD dst_unused:UNUSED_PAD src0_sel:DWORD src1_sel:WORD_1
v_mfma_f32_16x16x32_bf16 v[36:39],a[196:199],a[40:43],v[20:23]
ds_read_u16_d16_hi v34,v34
v_lshl_add_u64 v[26:27],v[28:29],0,v[30:31]
v_fma_mixhi_f16 v8,v59,s99,0
v_mfma_f32_16x16x32_bf16 v[36:39],a[200:203],a[24:27],v[36:39]
ds_read_b64_tr_b16 a[208:209],v1 offset:2048
v_lshl_add_u64 v[28:29],v[44:45],0,v[46:47]
v_lshlrev_b32_sdwa v33,1,v33 dst_sel:DWORD dst_unused:UNUSED_PAD src0_sel:DWORD src1_sel:WORD_1
v_pk_fma_f16 v9,v8,v8,s54
v_mfma_f32_16x16x32_bf16 v[36:39],a[212:215],a[8:11],v[36:39]
ds_read_u16 v33,v33
v_lshlrev_b32_sdwa v35,1,v35 dst_sel:DWORD dst_unused:UNUSED_PAD src0_sel:DWORD src1_sel:WORD_1
v_mfma_f32_16x16x32_bf16 v[44:47],a[212:215],a[12:15],v[20:23]
ds_read_u16_d16_hi v35,v35
v_pk_mul_f16 v9,v8,v9
v_mfma_f32_16x16x32_bf16 v[44:47],a[200:203],a[28:31],v[44:47]
s_waitcnt lgkmcnt(7)
ds_read_b64_tr_b16 a[210:211],v1 offset:3072
v_lshl_add_u64 v[30:31],v[48:49],0,v[50:51]
v_lshlrev_b32_sdwa v60,1,v60 dst_sel:DWORD dst_unused:UNUSED_PAD src0_sel:DWORD src1_sel:WORD_1
v_mfma_f32_16x16x32_bf16 v[44:47],a[196:199],a[44:47],v[44:47]
v_exp_f16 v8,v9
v_mfma_f32_16x16x32_bf16 v[48:51],a[192:195],a[44:47],v[16:19]
ds_read_u16 v60,v60
v_lshlrev_b32_sdwa v62,1,v62 dst_sel:DWORD dst_unused:UNUSED_PAD src0_sel:DWORD src1_sel:WORD_1
v_mfma_f32_16x16x32_bf16 v[48:51],a[216:219],a[28:31],v[48:51]
ds_read_u16_d16_hi v62,v62
v_lshlrev_b32_sdwa v61,1,v61 dst_sel:DWORD dst_unused:UNUSED_PAD src0_sel:DWORD src1_sel:WORD_1
v_mfma_f32_16x16x32_bf16 v[48:51],a[220:223],a[12:15],v[48:51]
v_exp_f16_sdwa v8,v9 dst_sel:WORD_1 dst_unused:UNUSED_PRESERVE src0_sel:WORD_1
v_mfma_f32_16x16x32_bf16 v[208:211],a[228:231],v[24:27],v[208:211]
ds_read_u16 v61,v61
v_lshlrev_b32_sdwa v63,1,v63 dst_sel:DWORD dst_unused:UNUSED_PAD src0_sel:DWORD src1_sel:WORD_1
v_mfma_f32_16x16x32_bf16 v[212:215],a[228:231],v[28:31],v[212:215]
ds_read_u16_d16_hi v63,v63
v_lshlrev_b32_sdwa v52,1,v52 dst_sel:DWORD dst_unused:UNUSED_PAD src0_sel:DWORD src1_sel:WORD_1
v_mfma_f32_16x16x32_bf16 v[172:175],a[232:235],v[28:31],v[172:175]
ds_read_u16 v52,v52
v_pk_fma_f16 v8,v8,s55,s55
v_mfma_f32_16x16x32_bf16 v[168:171],a[232:235],v[24:27],v[168:171]
ds_read_b64_tr_b16 a[228:229],v3 offset:64
v_rcp_f16_e64 v9,v8
v_lshlrev_b32_sdwa v54,1,v54 dst_sel:DWORD dst_unused:UNUSED_PAD src0_sel:DWORD src1_sel:WORD_1
v_mfma_f32_16x16x32_bf16 v[216:219],a[236:239],v[24:27],v[216:219]
ds_read_u16_d16_hi v54,v54
v_lshlrev_b32_sdwa v53,1,v53 dst_sel:DWORD dst_unused:UNUSED_PAD src0_sel:DWORD src1_sel:WORD_1
v_mfma_f32_16x16x32_bf16 v[220:223],a[236:239],v[28:31],v[220:223]
ds_read_u16 v53,v53
ds_read_b64_tr_b16 a[230:231],v3 offset:1600
v_mfma_f32_16x16x32_bf16 v[188:191],a[244:247],v[28:31],v[188:191]
ds_read_b64_tr_b16 a[232:233],v3 offset:72
v_lshlrev_b32_sdwa v55,1,v55 dst_sel:DWORD dst_unused:UNUSED_PAD src0_sel:DWORD src1_sel:WORD_1
v_mfma_f32_16x16x32_bf16 v[184:187],a[244:247],v[24:27],v[184:187]
ds_read_u16_d16_hi v55,v55
v_rcp_f16_sdwa v9,v8 dst_sel:WORD_1 dst_unused:UNUSED_PRESERVE src0_sel:WORD_1
v_mfma_f32_16x16x32_bf16 v[224:227],a[248:251],v[24:27],v[224:227]
ds_read_b64_tr_b16 a[234:235],v3 offset:1608
ds_read_b64_tr_b16 a[236:237],v3 offset:128
v_lshlrev_b32_sdwa v56,1,v56 dst_sel:DWORD dst_unused:UNUSED_PAD src0_sel:DWORD src1_sel:WORD_1
v_mfma_f32_16x16x32_bf16 v[228:231],a[248:251],v[28:31],v[228:231]
ds_read_u16 v56,v56
v_lshlrev_b32_sdwa v58,1,v58 dst_sel:DWORD dst_unused:UNUSED_PAD src0_sel:DWORD src1_sel:WORD_1
v_mfma_f32_16x16x32_bf16 v[204:207],a[252:255],v[28:31],v[204:207]
ds_read_u16_d16_hi v58,v58
v_fma_mix_f32 v57,v57,v9,0 op_sel_hi:[0,1,0]
v_mfma_f32_16x16x32_bf16 v[200:203],a[252:255],v[24:27],v[200:203]
ds_read_b64_tr_b16 a[238:239],v3 offset:1664
v_fma_mix_f32 v59,v59,v9,0 op_sel:[0,1,0] op_sel_hi:[0,1,0]
v_mfma_f32_16x16x32_bf16 v[28:31],a[220:223],a[48:51],v[16:19]
s_add_u32 m0,s57,10608
ds_read_b64 v[14:15],v7 offset:0
v_lshlrev_b32_sdwa v40,1,v40 dst_sel:DWORD dst_unused:UNUSED_PAD src0_sel:DWORD src1_sel:WORD_1
v_mfma_f32_16x16x32_bf16 v[28:31],a[216:219],a[64:67],v[28:31]
s_add_u32 s89,s56,5210112
ds_read_b64_tr_b16 a[244:245],v3 offset:136
v_lshlrev_b32_sdwa v42,1,v42 dst_sel:DWORD dst_unused:UNUSED_PAD src0_sel:DWORD src1_sel:WORD_1
v_mfma_f32_16x16x32_bf16 v[28:31],a[192:195],a[80:83],v[28:31]
s_waitcnt lgkmcnt(9)
buffer_load_dwordx4 v6,s[36:39],s89 offen lds
v_lshl_add_u64 v[32:33],v[32:33],0,v[34:35]
v_mfma_f32_16x16x32_bf16 v[24:27],a[196:199],a[80:83],v[20:23]
s_add_u32 m0,s57,14152
ds_read_b64_tr_b16 a[246:247],v3 offset:1672
v_lshl_add_u64 v[34:35],v[60:61],0,v[62:63]
v_lshlrev_b32_sdwa v41,1,v41 dst_sel:DWORD dst_unused:UNUSED_PAD src0_sel:DWORD src1_sel:WORD_1
v_mfma_f32_16x16x32_bf16 v[24:27],a[200:203],a[64:67],v[24:27]
s_waitcnt lgkmcnt(8)
ds_read_u16 v40,v40
v_lshl_add_u64 v[60:61],v[52:53],0,v[54:55]
v_mfma_f32_16x16x32_bf16 v[24:27],a[212:215],a[48:51],v[24:27]
s_waitcnt lgkmcnt(3)
ds_read_u16_d16_hi v42,v42
v_or3_b32 v13,v14,v15,v13
v_lshlrev_b32_sdwa v43,1,v43 dst_sel:DWORD dst_unused:UNUSED_PAD src0_sel:DWORD src1_sel:WORD_1
v_mfma_f32_16x16x32_bf16 v[52:55],a[212:215],a[52:55],v[20:23]
s_add_u32 s89,s56,5308416
ds_read_b64 v[14:15],v7 offset:512
v_or_b32 v62,v56,v58
v_lshlrev_b32_sdwa v36,1,v36 dst_sel:DWORD dst_unused:UNUSED_PAD src0_sel:DWORD src1_sel:WORD_1
v_mfma_f32_16x16x32_bf16 v[52:55],a[200:203],a[68:71],v[52:55]
buffer_load_dwordx4 v6,s[36:39],s89 offen lds
v_cvt_pk_bf16_f32 v63,v57,v59
v_lshlrev_b32_sdwa v38,1,v38 dst_sel:DWORD dst_unused:UNUSED_PAD src0_sel:DWORD src1_sel:WORD_1
v_mfma_f32_16x16x32_bf16 v[52:55],a[196:199],a[84:87],v[52:55]
ds_read_u16 v41,v41
v_lshlrev_b32_sdwa v37,1,v37 dst_sel:DWORD dst_unused:UNUSED_PAD src0_sel:DWORD src1_sel:WORD_1
v_mfma_f32_16x16x32_bf16 v[56:59],a[192:195],a[84:87],v[16:19]
ds_read_u16_d16_hi v43,v43
v_lshlrev_b32_sdwa v39,1,v39 dst_sel:DWORD dst_unused:UNUSED_PAD src0_sel:DWORD src1_sel:WORD_1
v_mfma_f32_16x16x32_bf16 v[56:59],a[216:219],a[68:71],v[56:59]
ds_read_u16 v36,v36
v_lshlrev_b32_sdwa v48,1,v48 dst_sel:DWORD dst_unused:UNUSED_PAD src0_sel:DWORD src1_sel:WORD_1
v_mfma_f32_16x16x32_bf16 v[56:59],a[220:223],a[52:55],v[56:59]
s_waitcnt lgkmcnt(3)
ds_read_u16_d16_hi v38,v38
v_or3_b32 v13,v14,v15,v13
v_mfma_f32_16x16x32_bf16 v[136:139],a[240:243],v[32:35],v[136:139]
ds_read_b64 v[14:15],v7 offset:1024
v_lshlrev_b32_sdwa v50,1,v50 dst_sel:DWORD dst_unused:UNUSED_PAD src0_sel:DWORD src1_sel:WORD_1
v_mfma_f32_16x16x32_bf16 v[140:143],a[240:243],v[60:63],v[140:143]
ds_read_u16 v37,v37
v_lshlrev_b32_sdwa v49,1,v49 dst_sel:DWORD dst_unused:UNUSED_PAD src0_sel:DWORD src1_sel:WORD_1
v_mfma_f32_16x16x32_bf16 v[68:71],a[224:227],v[60:63],v[68:71]
ds_read_u16_d16_hi v39,v39
v_lshlrev_b32_sdwa v51,1,v51 dst_sel:DWORD dst_unused:UNUSED_PAD src0_sel:DWORD src1_sel:WORD_1
v_mfma_f32_16x16x32_bf16 v[64:67],a[224:227],v[32:35],v[64:67]
ds_read_u16 v48,v48
v_lshlrev_b32_sdwa v44,1,v44 dst_sel:DWORD dst_unused:UNUSED_PAD src0_sel:DWORD src1_sel:WORD_1
v_mfma_f32_16x16x32_bf16 v[144:147],a[228:231],v[32:35],v[144:147]
ds_read_u16_d16_hi v50,v50
v_lshlrev_b32_sdwa v46,1,v46 dst_sel:DWORD dst_unused:UNUSED_PAD src0_sel:DWORD src1_sel:WORD_1
v_mfma_f32_16x16x32_bf16 v[148:151],a[228:231],v[60:63],v[148:151]
TP3810_%=:
s_cbranch_execz TP5954_%=
s_waitcnt lgkmcnt(4)
ds_read_u16 v49,v49
v_or3_b32 v13,v14,v15,v13
v_mfma_f32_16x16x32_bf16 v[84:87],a[232:235],v[60:63],v[84:87]
s_add_u32 m0,s57,17688
ds_read_u16_d16_hi v51,v51
v_lshlrev_b32_sdwa v45,1,v45 dst_sel:DWORD dst_unused:UNUSED_PAD src0_sel:DWORD src1_sel:WORD_1
v_mfma_f32_16x16x32_bf16 v[80:83],a[232:235],v[32:35],v[80:83]
s_add_u32 s89,s56,5406720
ds_read_u16 v44,v44
v_lshlrev_b32_sdwa v47,1,v47 dst_sel:DWORD dst_unused:UNUSED_PAD src0_sel:DWORD src1_sel:WORD_1
v_mfma_f32_16x16x32_bf16 v[152:155],a[236:239],v[32:35],v[152:155]
s_nop 0
buffer_load_dwordx4 v6,s[36:39],s89 offen lds
v_mfma_f32_16x16x32_bf16 v[156:159],a[236:239],v[60:63],v[156:159]
ds_read_u16_d16_hi v46,v46
v_mfma_f32_16x16x32_bf16 v[100:103],a[244:247],v[60:63],v[100:103]
ds_read_u16 v45,v45
v_mfma_f32_16x16x32_bf16 v[96:99],a[244:247],v[32:35],v[96:99]
ds_read_u16_d16_hi v47,v47
v_mfma_f32_16x16x32_bf16 v[60:63],a[220:223],a[56:59],v[16:19]
v_lshlrev_b32_sdwa v28,1,v28 dst_sel:DWORD dst_unused:UNUSED_PAD src0_sel:DWORD src1_sel:WORD_1
v_fma_mixlo_f16 v8,v53,s99,0
v_mfma_f32_16x16x32_bf16 v[60:63],a[216:219],a[72:75],v[60:63]
ds_read_u16 v28,v28
v_lshlrev_b32_sdwa v30,1,v30 dst_sel:DWORD dst_unused:UNUSED_PAD src0_sel:DWORD src1_sel:WORD_1
v_mfma_f32_16x16x32_bf16 v[60:63],a[192:195],a[88:91],v[60:63]
ds_read_u16_d16_hi v30,v30
v_lshl_add_u64 v[40:41],v[40:41],0,v[42:43]
v_mfma_f32_16x16x32_bf16 v[32:35],a[196:199],a[88:91],v[20:23]
s_waitcnt lgkmcnt(6)
v_lshl_add_u64 v[42:43],v[36:37],0,v[38:39]
v_fma_mixhi_f16 v8,v55,s99,0
v_mfma_f32_16x16x32_bf16 v[32:35],a[200:203],a[72:75],v[32:35]
v_lshl_add_u64 v[36:37],v[48:49],0,v[50:51]
v_lshlrev_b32_sdwa v29,1,v29 dst_sel:DWORD dst_unused:UNUSED_PAD src0_sel:DWORD src1_sel:WORD_1
v_pk_fma_f16 v9,v8,v8,s54
v_mfma_f32_16x16x32_bf16 v[32:35],a[212:215],a[56:59],v[32:35]
ds_read_u16 v29,v29
v_lshlrev_b32_sdwa v31,1,v31 dst_sel:DWORD dst_unused:UNUSED_PAD src0_sel:DWORD src1_sel:WORD_1
v_mfma_f32_16x16x32_bf16 v[48:51],a[212:215],a[60:63],v[20:23]
ds_read_b128 v[20:23],v4 offset:1152
v_pk_mul_f16 v9,v8,v9
v_mfma_f32_16x16x32_bf16 v[48:51],a[200:203],a[76:79],v[48:51]
s_waitcnt lgkmcnt(4)
ds_read_u16_d16_hi v31,v31
ds_read_b64_tr_b16 a[212:213],v1 offset:4096
v_lshl_add_u64 v[38:39],v[44:45],0,v[46:47]
v_lshlrev_b32_sdwa v24,1,v24 dst_sel:DWORD dst_unused:UNUSED_PAD src0_sel:DWORD src1_sel:WORD_1
v_mfma_f32_16x16x32_bf16 v[48:51],a[196:199],a[92:95],v[48:51]
ds_read_b64_tr_b16 a[214:215],v1 offset:5120
v_exp_f16 v8,v9
v_mfma_f32_16x16x32_bf16 v[44:47],a[192:195],a[92:95],v[16:19]
ds_read_b128 v[16:19],v4 offset:1184
ds_read_b64_tr_b16 a[200:201],v2 offset:4096
v_lshlrev_b32_sdwa v26,1,v26 dst_sel:DWORD dst_unused:UNUSED_PAD src0_sel:DWORD src1_sel:WORD_1
v_mfma_f32_16x16x32_bf16 v[44:47],a[216:219],a[76:79],v[44:47]
ds_read_b64_tr_b16 a[202:203],v2 offset:5120
ds_read_u16 v24,v24
v_lshlrev_b32_sdwa v25,1,v25 dst_sel:DWORD dst_unused:UNUSED_PAD src0_sel:DWORD src1_sel:WORD_1
v_mfma_f32_16x16x32_bf16 v[44:47],a[220:223],a[60:63],v[44:47]
ds_read_u16_d16_hi v26,v26
v_exp_f16_sdwa v8,v9 dst_sel:WORD_1 dst_unused:UNUSED_PRESERVE src0_sel:WORD_1
v_mfma_f32_16x16x32_bf16 v[112:115],a[240:243],v[40:43],v[112:115]
ds_read_u16 v25,v25
ds_read_b64_tr_b16 a[196:197],v2 offset:2048
v_lshlrev_b32_sdwa v27,1,v27 dst_sel:DWORD dst_unused:UNUSED_PAD src0_sel:DWORD src1_sel:WORD_1
v_mfma_f32_16x16x32_bf16 v[116:119],a[240:243],v[36:39],v[116:119]
ds_read_u16_d16_hi v27,v27
v_lshlrev_b32_sdwa v56,1,v56 dst_sel:DWORD dst_unused:UNUSED_PAD src0_sel:DWORD src1_sel:WORD_1
v_mfma_f32_16x16x32_bf16 v[76:79],a[224:227],v[36:39],v[76:79]
ds_read_b64_tr_b16 a[198:199],v2 offset:3072
v_pk_fma_f16 v8,v8,s55,s55
v_mfma_f32_16x16x32_bf16 v[72:75],a[224:227],v[40:43],v[72:75]
v_rcp_f16 v9,v8
v_lshlrev_b32_sdwa v58,1,v58 dst_sel:DWORD dst_unused:UNUSED_PAD src0_sel:DWORD src1_sel:WORD_1
v_mfma_f32_16x16x32_bf16 v[120:123],a[228:231],v[40:43],v[120:123]
ds_read_u16 v56,v56
v_lshlrev_b32_sdwa v57,1,v57 dst_sel:DWORD dst_unused:UNUSED_PAD src0_sel:DWORD src1_sel:WORD_1
v_mfma_f32_16x16x32_bf16 v[124:127],a[228:231],v[36:39],v[124:127]
ds_read_u16_d16_hi v58,v58
v_lshlrev_b32_sdwa v59,1,v59 dst_sel:DWORD dst_unused:UNUSED_PAD src0_sel:DWORD src1_sel:WORD_1
v_mfma_f32_16x16x32_bf16 v[92:95],a[232:235],v[36:39],v[92:95]
ds_read_u16 v57,v57
v_lshlrev_b32_sdwa v52,1,v52 dst_sel:DWORD dst_unused:UNUSED_PAD src0_sel:DWORD src1_sel:WORD_1
v_mfma_f32_16x16x32_bf16 v[88:91],a[232:235],v[40:43],v[88:91]
ds_read_u16_d16_hi v59,v59
v_rcp_f16_sdwa v9,v8 dst_sel:WORD_1 dst_unused:UNUSED_PRESERVE src0_sel:WORD_1
v_mfma_f32_16x16x32_bf16 v[128:131],a[236:239],v[40:43],v[128:131]
ds_read_b64_tr_b16 a[192:193],v2 offset:0
v_lshlrev_b32_sdwa v54,1,v54 dst_sel:DWORD dst_unused:UNUSED_PAD src0_sel:DWORD src1_sel:WORD_1
v_mfma_f32_16x16x32_bf16 v[132:135],a[236:239],v[36:39],v[132:135]
ds_read_b64_tr_b16 a[194:195],v2 offset:1024
v_fma_mix_f32 v53,v53,v9,0 op_sel_hi:[0,1,0]
v_mfma_f32_16x16x32_bf16 v[108:111],a[244:247],v[36:39],v[108:111]
ds_read_u16 v52,v52
v_fma_mix_f32 v55,v55,v9,0 op_sel:[0,1,0] op_sel_hi:[0,1,0]
v_mfma_f32_16x16x32_bf16 v[104:107],a[244:247],v[40:43],v[104:107]
s_waitcnt lgkmcnt(7)
ds_read_u16_d16_hi v54,v54
v_mfma_f32_16x16x32_bf16 v[36:39],a[204:207],a[0:3],v[20:23]
s_add_u32 m0,s57,21216
v_lshlrev_b32_sdwa v60,1,v60 dst_sel:DWORD dst_unused:UNUSED_PAD src0_sel:DWORD src1_sel:WORD_1
v_lshlrev_b32_sdwa v62,1,v62 dst_sel:DWORD dst_unused:UNUSED_PAD src0_sel:DWORD src1_sel:WORD_1
v_mfma_f32_16x16x32_bf16 v[36:39],a[208:211],a[16:19],v[36:39]
s_add_u32 s89,s56,5505024
ds_read_u16 v60,v60
v_lshlrev_b32_sdwa v61,1,v61 dst_sel:DWORD dst_unused:UNUSED_PAD src0_sel:DWORD src1_sel:WORD_1
v_mfma_f32_16x16x32_bf16 v[36:39],a[212:215],a[32:35],v[36:39]
buffer_load_dwordx4 v6,s[36:39],s89 offen lds
v_lshl_add_u64 v[28:29],v[28:29],0,v[30:31]
v_mfma_f32_16x16x32_bf16 v[40:43],a[200:203],a[32:35],v[16:19]
ds_read_u16_d16_hi v62,v62
v_lshl_add_u64 v[30:31],v[24:25],0,v[26:27]
v_mfma_f32_16x16x32_bf16 v[40:43],a[196:199],a[16:19],v[40:43]
s_waitcnt lgkmcnt(4)
ds_read_u16 v61,v61
v_lshl_add_u64 v[24:25],v[56:57],0,v[58:59]
v_mfma_f32_16x16x32_bf16 v[40:43],a[192:195],a[0:3],v[40:43]
v_lshlrev_b32_sdwa v63,1,v63 dst_sel:DWORD dst_unused:UNUSED_PAD src0_sel:DWORD src1_sel:WORD_1
v_lshlrev_b32_sdwa v32,1,v32 dst_sel:DWORD dst_unused:UNUSED_PAD src0_sel:DWORD src1_sel:WORD_1
v_mfma_f32_16x16x32_bf16 v[56:59],a[192:195],a[4:7],v[16:19]
s_waitcnt lgkmcnt(3)
ds_read_u16_d16_hi v63,v63
v_or_b32 v26,v52,v54
v_mfma_f32_16x16x32_bf16 v[56:59],a[196:199],a[20:23],v[56:59]
s_waitcnt vmcnt(7)
ds_read_u16 v32,v32
v_cvt_pk_bf16_f32 v27,v53,v55
v_lshlrev_b32_sdwa v34,1,v34 dst_sel:DWORD dst_unused:UNUSED_PAD src0_sel:DWORD src1_sel:WORD_1
v_mfma_f32_16x16x32_bf16 v[56:59],a[200:203],a[36:39],v[56:59]
s_barrier
ds_read_u16_d16_hi v34,v34
v_lshlrev_b32_sdwa v33,1,v33 dst_sel:DWORD dst_unused:UNUSED_PAD src0_sel:DWORD src1_sel:WORD_1
v_mfma_f32_16x16x32_bf16 v[52:55],a[212:215],a[36:39],v[20:23]
ds_read_u16 v33,v33
v_lshlrev_b32_sdwa v35,1,v35 dst_sel:DWORD dst_unused:UNUSED_PAD src0_sel:DWORD src1_sel:WORD_1
v_mfma_f32_16x16x32_bf16 v[52:55],a[208:211],a[20:23],v[52:55]
s_add_u32 m0,s58,18480
ds_read_u16_d16_hi v35,v35
v_lshlrev_b32_sdwa v44,1,v44 dst_sel:DWORD dst_unused:UNUSED_PAD src0_sel:DWORD src1_sel:WORD_1
v_mfma_f32_16x16x32_bf16 v[52:55],a[204:207],a[4:7],v[52:55]
s_mov_b32 s69,s88
ds_read_u16 v44,v44
v_lshlrev_b32_sdwa v46,1,v46 dst_sel:DWORD dst_unused:UNUSED_PAD src0_sel:DWORD src1_sel:WORD_1
v_mfma_f32_16x16x32_bf16 v[232:235],a[240:243],v[28:31],v[232:235]
buffer_load_dwordx4 v5,s[44:47],s69 offen offset:0 lds
v_lshlrev_b32_sdwa v45,1,v45 dst_sel:DWORD dst_unused:UNUSED_PAD src0_sel:DWORD src1_sel:WORD_1
v_mfma_f32_16x16x32_bf16 v[236:239],a[240:243],v[24:27],v[236:239]
ds_read_u16_d16_hi v46,v46
v_lshlrev_b32_sdwa v47,1,v47 dst_sel:DWORD dst_unused:UNUSED_PAD src0_sel:DWORD src1_sel:WORD_1
v_mfma_f32_16x16x32_bf16 v[164:167],a[224:227],v[24:27],v[164:167]
ds_read_u16 v45,v45
ds_read_u16_d16_hi v47,v47
v_mfma_f32_16x16x32_bf16 v[160:163],a[224:227],v[28:31],v[160:163]
ds_read_b64_tr_b16 a[248:249],v3 offset:6160
v_lshlrev_b32_sdwa v48,1,v48 dst_sel:DWORD dst_unused:UNUSED_PAD src0_sel:DWORD src1_sel:WORD_1
v_mfma_f32_16x16x32_bf16 v[240:243],a[228:231],v[28:31],v[240:243]
s_add_u32 s69,s69,s53
ds_read_u16 v48,v48
v_lshlrev_b32_sdwa v50,1,v50 dst_sel:DWORD dst_unused:UNUSED_PAD src0_sel:DWORD src1_sel:WORD_1
v_mfma_f32_16x16x32_bf16 v[244:247],a[228:231],v[24:27],v[244:247]
buffer_load_dwordx4 v5,s[44:47],s69 offen offset:1024 lds
ds_read_u16_d16_hi v50,v50
v_mfma_f32_16x16x32_bf16 v[180:183],a[232:235],v[24:27],v[180:183]
ds_read_b64_tr_b16 a[250:251],v3 offset:7696
v_lshlrev_b32_sdwa v49,1,v49 dst_sel:DWORD dst_unused:UNUSED_PAD src0_sel:DWORD src1_sel:WORD_1
v_mfma_f32_16x16x32_bf16 v[176:179],a[232:235],v[28:31],v[176:179]
ds_read_u16 v49,v49
v_lshlrev_b32_sdwa v51,1,v51 dst_sel:DWORD dst_unused:UNUSED_PAD src0_sel:DWORD src1_sel:WORD_1
v_mfma_f32_16x16x32_bf16 v[248:251],a[236:239],v[28:31],v[248:251]
ds_read_u16_d16_hi v51,v51
v_mfma_f32_16x16x32_bf16 v[252:255],a[236:239],v[24:27],v[252:255]
s_add_u32 s69,s69,s53
ds_read_b64_tr_b16 a[252:253],v3 offset:6168
v_mfma_f32_16x16x32_bf16 v[196:199],a[244:247],v[24:27],v[196:199]
buffer_load_dwordx4 v5,s[44:47],s69 offen offset:2048 lds
v_mfma_f32_16x16x32_bf16 v[192:195],a[244:247],v[28:31],v[192:195]
ds_read_b64_tr_b16 a[254:255],v3 offset:7704
v_mfma_f32_16x16x32_bf16 v[24:27],a[204:207],a[8:11],v[20:23]
ds_read_b64_tr_b16 a[216:217],v1 offset:6160
v_lshlrev_b32_sdwa v36,1,v36 dst_sel:DWORD dst_unused:UNUSED_PAD src0_sel:DWORD src1_sel:WORD_1
v_mfma_f32_16x16x32_bf16 v[24:27],a[208:211],a[24:27],v[24:27]
ds_read_u16 v36,v36
v_fma_mixlo_f16 v8,v57,s99,0
v_mfma_f32_16x16x32_bf16 v[24:27],a[212:215],a[40:43],v[24:27]
s_waitcnt lgkmcnt(7)
ds_read_b64_tr_b16 a[218:219],v1 offset:7184
v_lshl_add_u64 v[60:61],v[60:61],0,v[62:63]
v_lshlrev_b32_sdwa v38,1,v38 dst_sel:DWORD dst_unused:UNUSED_PAD src0_sel:DWORD src1_sel:WORD_1
v_mfma_f32_16x16x32_bf16 v[28:31],a[200:203],a[40:43],v[16:19]
ds_read_u16_d16_hi v38,v38
v_lshl_add_u64 v[62:63],v[32:33],0,v[34:35]
v_fma_mixhi_f16 v8,v59,s99,0
v_mfma_f32_16x16x32_bf16 v[28:31],a[196:199],a[24:27],v[28:31]
ds_read_b64_tr_b16 a[220:221],v1 offset:8208
v_lshl_add_u64 v[32:33],v[44:45],0,v[46:47]
v_lshlrev_b32_sdwa v37,1,v37 dst_sel:DWORD dst_unused:UNUSED_PAD src0_sel:DWORD src1_sel:WORD_1
v_pk_fma_f16 v9,v8,v8,s54
v_mfma_f32_16x16x32_bf16 v[28:31],a[192:195],a[8:11],v[28:31]
ds_read_u16 v37,v37
v_lshlrev_b32_sdwa v39,1,v39 dst_sel:DWORD dst_unused:UNUSED_PAD src0_sel:DWORD src1_sel:WORD_1
v_mfma_f32_16x16x32_bf16 v[44:47],a[192:195],a[12:15],v[16:19]
ds_read_u16_d16_hi v39,v39
v_pk_mul_f16 v9,v8,v9
v_mfma_f32_16x16x32_bf16 v[44:47],a[196:199],a[28:31],v[44:47]
s_waitcnt lgkmcnt(8)
ds_read_b64_tr_b16 a[222:223],v1 offset:9232
v_lshl_add_u64 v[34:35],v[48:49],0,v[50:51]
v_lshlrev_b32_sdwa v40,1,v40 dst_sel:DWORD dst_unused:UNUSED_PAD src0_sel:DWORD src1_sel:WORD_1
v_mfma_f32_16x16x32_bf16 v[44:47],a[200:203],a[44:47],v[44:47]
v_exp_f16 v8,v9
v_mfma_f32_16x16x32_bf16 v[48:51],a[212:215],a[44:47],v[20:23]
ds_read_u16 v40,v40
v_lshlrev_b32_sdwa v42,1,v42 dst_sel:DWORD dst_unused:UNUSED_PAD src0_sel:DWORD src1_sel:WORD_1
v_mfma_f32_16x16x32_bf16 v[48:51],a[208:211],a[28:31],v[48:51]
ds_read_u16_d16_hi v42,v42
v_lshlrev_b32_sdwa v41,1,v41 dst_sel:DWORD dst_unused:UNUSED_PAD src0_sel:DWORD src1_sel:WORD_1
v_mfma_f32_16x16x32_bf16 v[48:51],a[204:207],a[12:15],v[48:51]
v_exp_f16_sdwa v8,v9 dst_sel:WORD_1 dst_unused:UNUSED_PRESERVE src0_sel:WORD_1
v_mfma_f32_16x16x32_bf16 v[208:211],a[240:243],v[60:63],v[208:211]
ds_read_u16 v41,v41
v_lshlrev_b32_sdwa v43,1,v43 dst_sel:DWORD dst_unused:UNUSED_PAD src0_sel:DWORD src1_sel:WORD_1
v_mfma_f32_16x16x32_bf16 v[212:215],a[240:243],v[32:35],v[212:215]
ds_read_u16_d16_hi v43,v43
v_lshlrev_b32_sdwa v52,1,v52 dst_sel:DWORD dst_unused:UNUSED_PAD src0_sel:DWORD src1_sel:WORD_1
v_mfma_f32_16x16x32_bf16 v[172:175],a[224:227],v[32:35],v[172:175]
ds_read_u16 v52,v52
v_pk_fma_f16 v8,v8,s55,s55
v_mfma_f32_16x16x32_bf16 v[168:171],a[224:227],v[60:63],v[168:171]
ds_read_b64_tr_b16 a[240:241],v3 offset:6224
v_rcp_f16_e64 v9,v8
v_lshlrev_b32_sdwa v54,1,v54 dst_sel:DWORD dst_unused:UNUSED_PAD src0_sel:DWORD src1_sel:WORD_1
v_mfma_f32_16x16x32_bf16 v[216:219],a[228:231],v[60:63],v[216:219]
ds_read_u16_d16_hi v54,v54
v_lshlrev_b32_sdwa v53,1,v53 dst_sel:DWORD dst_unused:UNUSED_PAD src0_sel:DWORD src1_sel:WORD_1
v_mfma_f32_16x16x32_bf16 v[220:223],a[228:231],v[32:35],v[220:223]
ds_read_u16 v53,v53
ds_read_b64_tr_b16 a[242:243],v3 offset:7760
v_mfma_f32_16x16x32_bf16 v[188:191],a[232:235],v[32:35],v[188:191]
ds_read_b64_tr_b16 a[224:225],v3 offset:6232
v_lshlrev_b32_sdwa v55,1,v55 dst_sel:DWORD dst_unused:UNUSED_PAD src0_sel:DWORD src1_sel:WORD_1
v_mfma_f32_16x16x32_bf16 v[184:187],a[232:235],v[60:63],v[184:187]
ds_read_u16_d16_hi v55,v55
v_rcp_f16_sdwa v9,v8 dst_sel:WORD_1 dst_unused:UNUSED_PRESERVE src0_sel:WORD_1
v_mfma_f32_16x16x32_bf16 v[224:227],a[236:239],v[60:63],v[224:227]
ds_read_b64_tr_b16 a[226:227],v3 offset:7768
ds_read_b64_tr_b16 a[228:229],v3 offset:6288
v_lshlrev_b32_sdwa v56,1,v56 dst_sel:DWORD dst_unused:UNUSED_PAD src0_sel:DWORD src1_sel:WORD_1
v_mfma_f32_16x16x32_bf16 v[228:231],a[236:239],v[32:35],v[228:231]
ds_read_u16 v56,v56
v_lshlrev_b32_sdwa v58,1,v58 dst_sel:DWORD dst_unused:UNUSED_PAD src0_sel:DWORD src1_sel:WORD_1
v_mfma_f32_16x16x32_bf16 v[204:207],a[244:247],v[32:35],v[204:207]
ds_read_u16_d16_hi v58,v58
v_fma_mix_f32 v57,v57,v9,0 op_sel_hi:[0,1,0]
v_mfma_f32_16x16x32_bf16 v[200:203],a[244:247],v[60:63],v[200:203]
ds_read_b64_tr_b16 a[230:231],v3 offset:7824
v_fma_mix_f32 v59,v59,v9,0 op_sel:[0,1,0] op_sel_hi:[0,1,0]
v_mfma_f32_16x16x32_bf16 v[32:35],a[204:207],a[48:51],v[20:23]
s_add_u32 m0,s58,0
ds_read_b64 v[14:15],v7 offset:6160
v_lshlrev_b32_sdwa v24,1,v24 dst_sel:DWORD dst_unused:UNUSED_PAD src0_sel:DWORD src1_sel:WORD_1
v_mfma_f32_16x16x32_bf16 v[32:35],a[208:211],a[64:67],v[32:35]
s_mov_b32 s69,s87
ds_read_b64_tr_b16 a[232:233],v3 offset:6296
v_lshlrev_b32_sdwa v26,1,v26 dst_sel:DWORD dst_unused:UNUSED_PAD src0_sel:DWORD src1_sel:WORD_1
v_mfma_f32_16x16x32_bf16 v[32:35],a[212:215],a[80:83],v[32:35]
s_waitcnt lgkmcnt(9)
buffer_load_dwordx4 v5,s[44:47],s69 offen offset:0 lds
v_lshl_add_u64 v[36:37],v[36:37],0,v[38:39]
v_mfma_f32_16x16x32_bf16 v[60:63],a[200:203],a[80:83],v[16:19]
s_add_u32 s69,s69,s53
ds_read_b64_tr_b16 a[234:235],v3 offset:7832
v_lshl_add_u64 v[38:39],v[40:41],0,v[42:43]
v_lshlrev_b32_sdwa v25,1,v25 dst_sel:DWORD dst_unused:UNUSED_PAD src0_sel:DWORD src1_sel:WORD_1
v_mfma_f32_16x16x32_bf16 v[60:63],a[196:199],a[64:67],v[60:63]
s_waitcnt lgkmcnt(8)
buffer_load_dwordx4 v5,s[44:47],s69 offen offset:1024 lds
v_lshl_add_u64 v[40:41],v[52:53],0,v[54:55]
v_mfma_f32_16x16x32_bf16 v[60:63],a[192:195],a[48:51],v[60:63]
s_waitcnt lgkmcnt(2)
ds_read_u16 v24,v24
v_or3_b32 v13,v14,v15,v13
v_lshlrev_b32_sdwa v27,1,v27 dst_sel:DWORD dst_unused:UNUSED_PAD src0_sel:DWORD src1_sel:WORD_1
v_mfma_f32_16x16x32_bf16 v[52:55],a[192:195],a[52:55],v[16:19]
s_add_u32 s69,s69,s53
ds_read_b64 v[14:15],v7 offset:6672
v_or_b32 v42,v56,v58
v_lshlrev_b32_sdwa v28,1,v28 dst_sel:DWORD dst_unused:UNUSED_PAD src0_sel:DWORD src1_sel:WORD_1
v_mfma_f32_16x16x32_bf16 v[52:55],a[196:199],a[68:71],v[52:55]
buffer_load_dwordx4 v5,s[44:47],s69 offen offset:2048 lds
ds_read_u16_d16_hi v26,v26
v_cvt_pk_bf16_f32 v43,v57,v59
v_lshlrev_b32_sdwa v30,1,v30 dst_sel:DWORD dst_unused:UNUSED_PAD src0_sel:DWORD src1_sel:WORD_1
v_mfma_f32_16x16x32_bf16 v[52:55],a[200:203],a[84:87],v[52:55]
ds_read_u16 v25,v25
v_lshlrev_b32_sdwa v29,1,v29 dst_sel:DWORD dst_unused:UNUSED_PAD src0_sel:DWORD src1_sel:WORD_1
v_mfma_f32_16x16x32_bf16 v[56:59],a[212:215],a[84:87],v[20:23]
ds_read_u16_d16_hi v27,v27
v_lshlrev_b32_sdwa v31,1,v31 dst_sel:DWORD dst_unused:UNUSED_PAD src0_sel:DWORD src1_sel:WORD_1
v_mfma_f32_16x16x32_bf16 v[56:59],a[208:211],a[68:71],v[56:59]
ds_read_u16 v28,v28
v_lshlrev_b32_sdwa v48,1,v48 dst_sel:DWORD dst_unused:UNUSED_PAD src0_sel:DWORD src1_sel:WORD_1
v_mfma_f32_16x16x32_bf16 v[56:59],a[204:207],a[52:55],v[56:59]
s_waitcnt lgkmcnt(4)
ds_read_u16_d16_hi v30,v30
v_or3_b32 v13,v14,v15,v13
v_mfma_f32_16x16x32_bf16 v[136:139],a[248:251],v[36:39],v[136:139]
ds_read_b64 v[14:15],v7 offset:7184
v_lshlrev_b32_sdwa v50,1,v50 dst_sel:DWORD dst_unused:UNUSED_PAD src0_sel:DWORD src1_sel:WORD_1
v_mfma_f32_16x16x32_bf16 v[140:143],a[248:251],v[40:43],v[140:143]
ds_read_u16 v29,v29
v_lshlrev_b32_sdwa v49,1,v49 dst_sel:DWORD dst_unused:UNUSED_PAD src0_sel:DWORD src1_sel:WORD_1
v_mfma_f32_16x16x32_bf16 v[68:71],a[252:255],v[40:43],v[68:71]
ds_read_u16_d16_hi v31,v31
v_lshlrev_b32_sdwa v51,1,v51 dst_sel:DWORD dst_unused:UNUSED_PAD src0_sel:DWORD src1_sel:WORD_1
v_mfma_f32_16x16x32_bf16 v[64:67],a[252:255],v[36:39],v[64:67]
ds_read_u16 v48,v48
v_lshlrev_b32_sdwa v44,1,v44 dst_sel:DWORD dst_unused:UNUSED_PAD src0_sel:DWORD src1_sel:WORD_1
v_mfma_f32_16x16x32_bf16 v[144:147],a[240:243],v[36:39],v[144:147]
ds_read_u16_d16_hi v50,v50
v_lshlrev_b32_sdwa v46,1,v46 dst_sel:DWORD dst_unused:UNUSED_PAD src0_sel:DWORD src1_sel:WORD_1
v_mfma_f32_16x16x32_bf16 v[148:151],a[240:243],v[40:43],v[148:151]
s_waitcnt lgkmcnt(4)
ds_read_u16 v49,v49
v_or3_b32 v13,v14,v15,v13
v_mfma_f32_16x16x32_bf16 v[84:87],a[224:227],v[40:43],v[84:87]
ds_read_u16_d16_hi v51,v51
v_lshlrev_b32_sdwa v45,1,v45 dst_sel:DWORD dst_unused:UNUSED_PAD src0_sel:DWORD src1_sel:WORD_1
v_mfma_f32_16x16x32_bf16 v[80:83],a[224:227],v[36:39],v[80:83]
ds_read_u16 v44,v44
v_lshlrev_b32_sdwa v47,1,v47 dst_sel:DWORD dst_unused:UNUSED_PAD src0_sel:DWORD src1_sel:WORD_1
v_mfma_f32_16x16x32_bf16 v[152:155],a[228:231],v[36:39],v[152:155]
ds_read_u16_d16_hi v46,v46
v_mfma_f32_16x16x32_bf16 v[156:159],a[228:231],v[40:43],v[156:159]
ds_read_u16 v45,v45
v_mfma_f32_16x16x32_bf16 v[100:103],a[232:235],v[40:43],v[100:103]
ds_read_u16_d16_hi v47,v47
v_mfma_f32_16x16x32_bf16 v[96:99],a[232:235],v[36:39],v[96:99]
v_mfma_f32_16x16x32_bf16 v[40:43],a[204:207],a[56:59],v[20:23]
v_lshlrev_b32_sdwa v32,1,v32 dst_sel:DWORD dst_unused:UNUSED_PAD src0_sel:DWORD src1_sel:WORD_1
v_fma_mixlo_f16 v8,v53,s99,0
v_mfma_f32_16x16x32_bf16 v[40:43],a[208:211],a[72:75],v[40:43]
ds_read_u16 v32,v32
v_lshlrev_b32_sdwa v34,1,v34 dst_sel:DWORD dst_unused:UNUSED_PAD src0_sel:DWORD src1_sel:WORD_1
v_mfma_f32_16x16x32_bf16 v[40:43],a[212:215],a[88:91],v[40:43]
ds_read_u16_d16_hi v34,v34
v_lshl_add_u64 v[24:25],v[24:25],0,v[26:27]
v_mfma_f32_16x16x32_bf16 v[36:39],a[200:203],a[88:91],v[16:19]
s_waitcnt lgkmcnt(6)
v_lshl_add_u64 v[26:27],v[28:29],0,v[30:31]
v_fma_mixhi_f16 v8,v55,s99,0
v_mfma_f32_16x16x32_bf16 v[36:39],a[196:199],a[72:75],v[36:39]
v_lshl_add_u64 v[28:29],v[48:49],0,v[50:51]
v_lshlrev_b32_sdwa v33,1,v33 dst_sel:DWORD dst_unused:UNUSED_PAD src0_sel:DWORD src1_sel:WORD_1
v_pk_fma_f16 v9,v8,v8,s54
v_mfma_f32_16x16x32_bf16 v[36:39],a[192:195],a[56:59],v[36:39]
ds_read_u16 v33,v33
v_lshlrev_b32_sdwa v35,1,v35 dst_sel:DWORD dst_unused:UNUSED_PAD src0_sel:DWORD src1_sel:WORD_1
v_mfma_f32_16x16x32_bf16 v[48:51],a[192:195],a[60:63],v[16:19]
ds_read_b128 v[16:19],v4 offset:1280
v_pk_mul_f16 v9,v8,v9
v_mfma_f32_16x16x32_bf16 v[48:51],a[196:199],a[76:79],v[48:51]
s_waitcnt lgkmcnt(4)
ds_read_u16_d16_hi v35,v35
ds_read_b64_tr_b16 a[192:193],v1 offset:10256
v_lshl_add_u64 v[30:31],v[44:45],0,v[46:47]
v_lshlrev_b32_sdwa v60,1,v60 dst_sel:DWORD dst_unused:UNUSED_PAD src0_sel:DWORD src1_sel:WORD_1
v_mfma_f32_16x16x32_bf16 v[48:51],a[200:203],a[92:95],v[48:51]
ds_read_b64_tr_b16 a[194:195],v1 offset:11280
v_exp_f16_e64 v8,v9
v_mfma_f32_16x16x32_bf16 v[44:47],a[212:215],a[92:95],v[20:23]
ds_read_b128 v[20:23],v4 offset:1312
ds_read_b64_tr_b16 a[196:197],v2 offset:10256
v_lshlrev_b32_sdwa v62,1,v62 dst_sel:DWORD dst_unused:UNUSED_PAD src0_sel:DWORD src1_sel:WORD_1
v_mfma_f32_16x16x32_bf16 v[44:47],a[208:211],a[76:79],v[44:47]
ds_read_b64_tr_b16 a[198:199],v2 offset:11280
ds_read_u16 v60,v60
v_lshlrev_b32_sdwa v61,1,v61 dst_sel:DWORD dst_unused:UNUSED_PAD src0_sel:DWORD src1_sel:WORD_1
v_mfma_f32_16x16x32_bf16 v[44:47],a[204:207],a[60:63],v[44:47]
ds_read_u16_d16_hi v62,v62
v_exp_f16_sdwa v8,v9 dst_sel:WORD_1 dst_unused:UNUSED_PRESERVE src0_sel:WORD_1
v_mfma_f32_16x16x32_bf16 v[112:115],a[248:251],v[24:27],v[112:115]
ds_read_u16 v61,v61
ds_read_b64_tr_b16 a[200:201],v2 offset:8208
v_lshlrev_b32_sdwa v63,1,v63 dst_sel:DWORD dst_unused:UNUSED_PAD src0_sel:DWORD src1_sel:WORD_1
v_mfma_f32_16x16x32_bf16 v[116:119],a[248:251],v[28:31],v[116:119]
ds_read_u16_d16_hi v63,v63
v_lshlrev_b32_sdwa v56,1,v56 dst_sel:DWORD dst_unused:UNUSED_PAD src0_sel:DWORD src1_sel:WORD_1
v_mfma_f32_16x16x32_bf16 v[76:79],a[252:255],v[28:31],v[76:79]
ds_read_b64_tr_b16 a[202:203],v2 offset:9232
v_pk_fma_f16 v8,v8,s55,s55
v_mfma_f32_16x16x32_bf16 v[72:75],a[252:255],v[24:27],v[72:75]
v_rcp_f16 v9,v8
v_lshlrev_b32_sdwa v58,1,v58 dst_sel:DWORD dst_unused:UNUSED_PAD src0_sel:DWORD src1_sel:WORD_1
v_mfma_f32_16x16x32_bf16 v[120:123],a[240:243],v[24:27],v[120:123]
ds_read_u16 v56,v56
v_lshlrev_b32_sdwa v57,1,v57 dst_sel:DWORD dst_unused:UNUSED_PAD src0_sel:DWORD src1_sel:WORD_1
v_mfma_f32_16x16x32_bf16 v[124:127],a[240:243],v[28:31],v[124:127]
ds_read_u16_d16_hi v58,v58
v_lshlrev_b32_sdwa v59,1,v59 dst_sel:DWORD dst_unused:UNUSED_PAD src0_sel:DWORD src1_sel:WORD_1
v_mfma_f32_16x16x32_bf16 v[92:95],a[224:227],v[28:31],v[92:95]
ds_read_u16 v57,v57
v_lshlrev_b32_sdwa v52,1,v52 dst_sel:DWORD dst_unused:UNUSED_PAD src0_sel:DWORD src1_sel:WORD_1
v_mfma_f32_16x16x32_bf16 v[88:91],a[224:227],v[24:27],v[88:91]
ds_read_u16_d16_hi v59,v59
v_rcp_f16_sdwa v9,v8 dst_sel:WORD_1 dst_unused:UNUSED_PRESERVE src0_sel:WORD_1
v_mfma_f32_16x16x32_bf16 v[128:131],a[228:231],v[24:27],v[128:131]
ds_read_b64_tr_b16 a[212:213],v2 offset:6160
v_lshlrev_b32_sdwa v54,1,v54 dst_sel:DWORD dst_unused:UNUSED_PAD src0_sel:DWORD src1_sel:WORD_1
v_mfma_f32_16x16x32_bf16 v[132:135],a[228:231],v[28:31],v[132:135]
ds_read_b64_tr_b16 a[214:215],v2 offset:7184
v_fma_mix_f32 v53,v53,v9,0 op_sel_hi:[0,1,0]
v_mfma_f32_16x16x32_bf16 v[108:111],a[232:235],v[28:31],v[108:111]
ds_read_u16 v52,v52
v_fma_mix_f32 v55,v55,v9,0 op_sel:[0,1,0] op_sel_hi:[0,1,0]
v_mfma_f32_16x16x32_bf16 v[104:107],a[232:235],v[24:27],v[104:107]
s_waitcnt lgkmcnt(7)
ds_read_u16_d16_hi v54,v54
v_mfma_f32_16x16x32_bf16 v[28:31],a[216:219],a[0:3],v[16:19]
v_lshlrev_b32_sdwa v40,1,v40 dst_sel:DWORD dst_unused:UNUSED_PAD src0_sel:DWORD src1_sel:WORD_1
v_lshlrev_b32_sdwa v42,1,v42 dst_sel:DWORD dst_unused:UNUSED_PAD src0_sel:DWORD src1_sel:WORD_1
v_mfma_f32_16x16x32_bf16 v[28:31],a[220:223],a[16:19],v[28:31]
ds_read_u16 v40,v40
v_lshlrev_b32_sdwa v41,1,v41 dst_sel:DWORD dst_unused:UNUSED_PAD src0_sel:DWORD src1_sel:WORD_1
v_mfma_f32_16x16x32_bf16 v[28:31],a[192:195],a[32:35],v[28:31]
ds_read_u16_d16_hi v42,v42
v_lshl_add_u64 v[32:33],v[32:33],0,v[34:35]
v_mfma_f32_16x16x32_bf16 v[24:27],a[196:199],a[32:35],v[20:23]
ds_read_u16 v41,v41
v_lshl_add_u64 v[34:35],v[60:61],0,v[62:63]
v_mfma_f32_16x16x32_bf16 v[24:27],a[200:203],a[16:19],v[24:27]
s_waitcnt lgkmcnt(5)
v_lshl_add_u64 v[60:61],v[56:57],0,v[58:59]
v_lshlrev_b32_sdwa v43,1,v43 dst_sel:DWORD dst_unused:UNUSED_PAD src0_sel:DWORD src1_sel:WORD_1
v_mfma_f32_16x16x32_bf16 v[24:27],a[212:215],a[0:3],v[24:27]
ds_read_u16_d16_hi v43,v43
v_lshlrev_b32_sdwa v36,1,v36 dst_sel:DWORD dst_unused:UNUSED_PAD src0_sel:DWORD src1_sel:WORD_1
v_mfma_f32_16x16x32_bf16 v[56:59],a[212:215],a[4:7],v[20:23]
s_waitcnt lgkmcnt(4)
ds_read_u16 v36,v36
v_or_b32 v62,v52,v54
v_mfma_f32_16x16x32_bf16 v[56:59],a[200:203],a[20:23],v[56:59]
v_cvt_pk_bf16_f32 v63,v53,v55
v_lshlrev_b32_sdwa v38,1,v38 dst_sel:DWORD dst_unused:UNUSED_PAD src0_sel:DWORD src1_sel:WORD_1
v_mfma_f32_16x16x32_bf16 v[56:59],a[196:199],a[36:39],v[56:59]
ds_read_u16_d16_hi v38,v38
v_lshlrev_b32_sdwa v37,1,v37 dst_sel:DWORD dst_unused:UNUSED_PAD src0_sel:DWORD src1_sel:WORD_1
v_mfma_f32_16x16x32_bf16 v[52:55],a[192:195],a[36:39],v[16:19]
ds_read_u16 v37,v37
v_lshlrev_b32_sdwa v39,1,v39 dst_sel:DWORD dst_unused:UNUSED_PAD src0_sel:DWORD src1_sel:WORD_1
v_mfma_f32_16x16x32_bf16 v[52:55],a[220:223],a[20:23],v[52:55]
ds_read_u16_d16_hi v39,v39
v_lshlrev_b32_sdwa v44,1,v44 dst_sel:DWORD dst_unused:UNUSED_PAD src0_sel:DWORD src1_sel:WORD_1
v_mfma_f32_16x16x32_bf16 v[52:55],a[216:219],a[4:7],v[52:55]
ds_read_u16 v44,v44
v_lshlrev_b32_sdwa v46,1,v46 dst_sel:DWORD dst_unused:UNUSED_PAD src0_sel:DWORD src1_sel:WORD_1
v_mfma_f32_16x16x32_bf16 v[232:235],a[248:251],v[32:35],v[232:235]
ds_read_u16_d16_hi v46,v46
v_lshlrev_b32_sdwa v45,1,v45 dst_sel:DWORD dst_unused:UNUSED_PAD src0_sel:DWORD src1_sel:WORD_1
v_mfma_f32_16x16x32_bf16 v[236:239],a[248:251],v[60:63],v[236:239]
ds_read_u16 v45,v45
v_lshlrev_b32_sdwa v47,1,v47 dst_sel:DWORD dst_unused:UNUSED_PAD src0_sel:DWORD src1_sel:WORD_1
v_mfma_f32_16x16x32_bf16 v[164:167],a[252:255],v[60:63],v[164:167]
ds_read_u16_d16_hi v47,v47
ds_read_b64_tr_b16 a[236:237],v3 offset:12320
v_mfma_f32_16x16x32_bf16 v[160:163],a[252:255],v[32:35],v[160:163]
ds_read_b64_tr_b16 a[238:239],v3 offset:13856
v_lshlrev_b32_sdwa v48,1,v48 dst_sel:DWORD dst_unused:UNUSED_PAD src0_sel:DWORD src1_sel:WORD_1
v_mfma_f32_16x16x32_bf16 v[240:243],a[240:243],v[32:35],v[240:243]
ds_read_u16 v48,v48
v_lshlrev_b32_sdwa v50,1,v50 dst_sel:DWORD dst_unused:UNUSED_PAD src0_sel:DWORD src1_sel:WORD_1
v_mfma_f32_16x16x32_bf16 v[244:247],a[240:243],v[60:63],v[244:247]
ds_read_u16_d16_hi v50,v50
v_lshlrev_b32_sdwa v49,1,v49 dst_sel:DWORD dst_unused:UNUSED_PAD src0_sel:DWORD src1_sel:WORD_1
v_mfma_f32_16x16x32_bf16 v[180:183],a[224:227],v[60:63],v[180:183]
ds_read_u16 v49,v49
v_lshlrev_b32_sdwa v51,1,v51 dst_sel:DWORD dst_unused:UNUSED_PAD src0_sel:DWORD src1_sel:WORD_1
v_mfma_f32_16x16x32_bf16 v[176:179],a[224:227],v[32:35],v[176:179]
ds_read_u16_d16_hi v51,v51
v_mfma_f32_16x16x32_bf16 v[248:251],a[228:231],v[32:35],v[248:251]
ds_read_b64_tr_b16 a[244:245],v3 offset:12328
v_mfma_f32_16x16x32_bf16 v[252:255],a[228:231],v[60:63],v[252:255]
ds_read_b64_tr_b16 a[246:247],v3 offset:13864
v_mfma_f32_16x16x32_bf16 v[196:199],a[232:235],v[60:63],v[196:199]
v_mfma_f32_16x16x32_bf16 v[192:195],a[232:235],v[32:35],v[192:195]
v_mfma_f32_16x16x32_bf16 v[60:63],a[216:219],a[8:11],v[16:19]
ds_read_b64_tr_b16 a[208:209],v1 offset:12320
v_lshlrev_b32_sdwa v28,1,v28 dst_sel:DWORD dst_unused:UNUSED_PAD src0_sel:DWORD src1_sel:WORD_1
v_mfma_f32_16x16x32_bf16 v[60:63],a[220:223],a[24:27],v[60:63]
ds_read_u16 v28,v28
v_fma_mixlo_f16 v8,v57,s99,0
v_mfma_f32_16x16x32_bf16 v[60:63],a[192:195],a[40:43],v[60:63]
s_waitcnt lgkmcnt(6)
ds_read_b64_tr_b16 a[210:211],v1 offset:13344
v_lshl_add_u64 v[40:41],v[40:41],0,v[42:43]
v_lshlrev_b32_sdwa v30,1,v30 dst_sel:DWORD dst_unused:UNUSED_PAD src0_sel:DWORD src1_sel:WORD_1
v_mfma_f32_16x16x32_bf16 v[32:35],a[196:199],a[40:43],v[20:23]
ds_read_u16_d16_hi v30,v30
v_lshl_add_u64 v[42:43],v[36:37],0,v[38:39]
v_fma_mixhi_f16 v8,v59,s99,0
v_mfma_f32_16x16x32_bf16 v[32:35],a[200:203],a[24:27],v[32:35]
ds_read_b64_tr_b16 a[204:205],v1 offset:14368
v_lshl_add_u64 v[36:37],v[44:45],0,v[46:47]
v_lshlrev_b32_sdwa v29,1,v29 dst_sel:DWORD dst_unused:UNUSED_PAD src0_sel:DWORD src1_sel:WORD_1
v_pk_fma_f16 v9,v8,v8,s54
v_mfma_f32_16x16x32_bf16 v[32:35],a[212:215],a[8:11],v[32:35]
ds_read_u16 v29,v29
v_lshlrev_b32_sdwa v31,1,v31 dst_sel:DWORD dst_unused:UNUSED_PAD src0_sel:DWORD src1_sel:WORD_1
v_mfma_f32_16x16x32_bf16 v[44:47],a[212:215],a[12:15],v[20:23]
ds_read_u16_d16_hi v31,v31
v_pk_mul_f16 v9,v8,v9
v_mfma_f32_16x16x32_bf16 v[44:47],a[200:203],a[28:31],v[44:47]
s_waitcnt lgkmcnt(7)
ds_read_b64_tr_b16 a[206:207],v1 offset:15392
v_lshl_add_u64 v[38:39],v[48:49],0,v[50:51]
v_lshlrev_b32_sdwa v24,1,v24 dst_sel:DWORD dst_unused:UNUSED_PAD src0_sel:DWORD src1_sel:WORD_1
v_mfma_f32_16x16x32_bf16 v[44:47],a[196:199],a[44:47],v[44:47]
v_exp_f16 v8,v9
v_mfma_f32_16x16x32_bf16 v[48:51],a[192:195],a[44:47],v[16:19]
ds_read_u16 v24,v24
v_lshlrev_b32_sdwa v26,1,v26 dst_sel:DWORD dst_unused:UNUSED_PAD src0_sel:DWORD src1_sel:WORD_1
v_mfma_f32_16x16x32_bf16 v[48:51],a[220:223],a[28:31],v[48:51]
ds_read_u16_d16_hi v26,v26
v_lshlrev_b32_sdwa v25,1,v25 dst_sel:DWORD dst_unused:UNUSED_PAD src0_sel:DWORD src1_sel:WORD_1
v_mfma_f32_16x16x32_bf16 v[48:51],a[216:219],a[12:15],v[48:51]
v_exp_f16_sdwa v8,v9 dst_sel:WORD_1 dst_unused:UNUSED_PRESERVE src0_sel:WORD_1
v_mfma_f32_16x16x32_bf16 v[208:211],a[248:251],v[40:43],v[208:211]
ds_read_u16 v25,v25
v_lshlrev_b32_sdwa v27,1,v27 dst_sel:DWORD dst_unused:UNUSED_PAD src0_sel:DWORD src1_sel:WORD_1
v_mfma_f32_16x16x32_bf16 v[212:215],a[248:251],v[36:39],v[212:215]
TP4341_%=:
s_cbranch_execz TP6483_%=
ds_read_u16_d16_hi v27,v27
v_lshlrev_b32_sdwa v52,1,v52 dst_sel:DWORD dst_unused:UNUSED_PAD src0_sel:DWORD src1_sel:WORD_1
v_mfma_f32_16x16x32_bf16 v[172:175],a[252:255],v[36:39],v[172:175]
ds_read_u16 v52,v52
v_pk_fma_f16 v8,v8,s55,s55
v_mfma_f32_16x16x32_bf16 v[168:171],a[252:255],v[40:43],v[168:171]
ds_read_b64_tr_b16 a[248:249],v3 offset:12384
v_rcp_f16 v9,v8
v_lshlrev_b32_sdwa v54,1,v54 dst_sel:DWORD dst_unused:UNUSED_PAD src0_sel:DWORD src1_sel:WORD_1
v_mfma_f32_16x16x32_bf16 v[216:219],a[240:243],v[40:43],v[216:219]
ds_read_u16_d16_hi v54,v54
v_lshlrev_b32_sdwa v53,1,v53 dst_sel:DWORD dst_unused:UNUSED_PAD src0_sel:DWORD src1_sel:WORD_1
v_mfma_f32_16x16x32_bf16 v[220:223],a[240:243],v[36:39],v[220:223]
ds_read_u16 v53,v53
ds_read_b64_tr_b16 a[250:251],v3 offset:13920
v_mfma_f32_16x16x32_bf16 v[188:191],a[224:227],v[36:39],v[188:191]
ds_read_b64_tr_b16 a[252:253],v3 offset:12392
v_lshlrev_b32_sdwa v55,1,v55 dst_sel:DWORD dst_unused:UNUSED_PAD src0_sel:DWORD src1_sel:WORD_1
v_mfma_f32_16x16x32_bf16 v[184:187],a[224:227],v[40:43],v[184:187]
ds_read_u16_d16_hi v55,v55
v_rcp_f16_sdwa v9,v8 dst_sel:WORD_1 dst_unused:UNUSED_PRESERVE src0_sel:WORD_1
v_mfma_f32_16x16x32_bf16 v[224:227],a[228:231],v[40:43],v[224:227]
ds_read_b64_tr_b16 a[254:255],v3 offset:13928
ds_read_b64_tr_b16 a[240:241],v3 offset:12448
v_lshlrev_b32_sdwa v56,1,v56 dst_sel:DWORD dst_unused:UNUSED_PAD src0_sel:DWORD src1_sel:WORD_1
v_mfma_f32_16x16x32_bf16 v[228:231],a[228:231],v[36:39],v[228:231]
ds_read_u16 v56,v56
v_lshlrev_b32_sdwa v58,1,v58 dst_sel:DWORD dst_unused:UNUSED_PAD src0_sel:DWORD src1_sel:WORD_1
v_mfma_f32_16x16x32_bf16 v[204:207],a[232:235],v[36:39],v[204:207]
ds_read_u16_d16_hi v58,v58
v_fma_mix_f32 v57,v57,v9,0 op_sel_hi:[0,1,0]
v_mfma_f32_16x16x32_bf16 v[200:203],a[232:235],v[40:43],v[200:203]
ds_read_b64_tr_b16 a[242:243],v3 offset:13984
v_fma_mix_f32 v59,v59,v9,0 op_sel:[0,1,0] op_sel_hi:[0,1,0]
v_mbcnt_lo_u32_b32 v8,-1,0
N_qs10_%=:
s_add_u32 s64,s16,N_qs10_%=-N_pf_%=+0
v_and_b32_e64 v8,3,v8
s_mov_b32 m0,12288
v_lshl_add_u32 v8,v8,7,s64
v_min_u32 v8,N_mainend_%=-N_pf_%=+2048,v8
global_load_lds_dword v8,s[14:15]
v_mfma_f32_16x16x32_bf16 v[36:39],a[216:219],a[48:51],v[16:19]
ds_read_b64 v[14:15],v7 offset:12320
v_lshlrev_b32_sdwa v60,1,v60 dst_sel:DWORD dst_unused:UNUSED_PAD src0_sel:DWORD src1_sel:WORD_1
v_mfma_f32_16x16x32_bf16 v[36:39],a[220:223],a[64:67],v[36:39]
ds_read_b64_tr_b16 a[224:225],v3 offset:12456
v_lshlrev_b32_sdwa v62,1,v62 dst_sel:DWORD dst_unused:UNUSED_PAD src0_sel:DWORD src1_sel:WORD_1
v_mfma_f32_16x16x32_bf16 v[36:39],a[192:195],a[80:83],v[36:39]
s_waitcnt lgkmcnt(9)
ds_read_b64_tr_b16 a[226:227],v3 offset:13992
v_lshl_add_u64 v[28:29],v[28:29],0,v[30:31]
v_mfma_f32_16x16x32_bf16 v[40:43],a[196:199],a[80:83],v[20:23]
ds_read_u16 v60,v60
v_lshl_add_u64 v[30:31],v[24:25],0,v[26:27]
v_lshlrev_b32_sdwa v61,1,v61 dst_sel:DWORD dst_unused:UNUSED_PAD src0_sel:DWORD src1_sel:WORD_1
v_mfma_f32_16x16x32_bf16 v[40:43],a[200:203],a[64:67],v[40:43]
s_waitcnt lgkmcnt(9)
ds_read_u16_d16_hi v62,v62
v_lshl_add_u64 v[24:25],v[52:53],0,v[54:55]
v_mfma_f32_16x16x32_bf16 v[40:43],a[212:215],a[48:51],v[40:43]
s_waitcnt lgkmcnt(4)
ds_read_u16 v61,v61
v_or3_b32 v13,v14,v15,v13
v_lshlrev_b32_sdwa v63,1,v63 dst_sel:DWORD dst_unused:UNUSED_PAD src0_sel:DWORD src1_sel:WORD_1
v_mfma_f32_16x16x32_bf16 v[52:55],a[212:215],a[52:55],v[20:23]
ds_read_b64 v[14:15],v7 offset:12832
v_or_b32 v26,v56,v58
v_lshlrev_b32_sdwa v32,1,v32 dst_sel:DWORD dst_unused:UNUSED_PAD src0_sel:DWORD src1_sel:WORD_1
v_mfma_f32_16x16x32_bf16 v[52:55],a[200:203],a[68:71],v[52:55]
ds_read_u16_d16_hi v63,v63
v_cvt_pk_bf16_f32 v27,v57,v59
v_lshlrev_b32_sdwa v34,1,v34 dst_sel:DWORD dst_unused:UNUSED_PAD src0_sel:DWORD src1_sel:WORD_1
v_mfma_f32_16x16x32_bf16 v[52:55],a[196:199],a[84:87],v[52:55]
ds_read_u16 v32,v32
v_lshlrev_b32_sdwa v33,1,v33 dst_sel:DWORD dst_unused:UNUSED_PAD src0_sel:DWORD src1_sel:WORD_1
v_mfma_f32_16x16x32_bf16 v[56:59],a[192:195],a[84:87],v[16:19]
ds_read_u16_d16_hi v34,v34
v_lshlrev_b32_sdwa v35,1,v35 dst_sel:DWORD dst_unused:UNUSED_PAD src0_sel:DWORD src1_sel:WORD_1
v_mfma_f32_16x16x32_bf16 v[56:59],a[220:223],a[68:71],v[56:59]
ds_read_u16 v33,v33
v_lshlrev_b32_sdwa v48,1,v48 dst_sel:DWORD dst_unused:UNUSED_PAD src0_sel:DWORD src1_sel:WORD_1
v_mfma_f32_16x16x32_bf16 v[56:59],a[216:219],a[52:55],v[56:59]
s_waitcnt lgkmcnt(4)
ds_read_u16_d16_hi v35,v35
v_or3_b32 v13,v14,v15,v13
v_mfma_f32_16x16x32_bf16 v[136:139],a[236:239],v[28:31],v[136:139]
ds_read_b64 v[14:15],v7 offset:13344
v_lshlrev_b32_sdwa v50,1,v50 dst_sel:DWORD dst_unused:UNUSED_PAD src0_sel:DWORD src1_sel:WORD_1
v_mfma_f32_16x16x32_bf16 v[140:143],a[236:239],v[24:27],v[140:143]
ds_read_u16 v48,v48
v_lshlrev_b32_sdwa v49,1,v49 dst_sel:DWORD dst_unused:UNUSED_PAD src0_sel:DWORD src1_sel:WORD_1
v_mfma_f32_16x16x32_bf16 v[68:71],a[244:247],v[24:27],v[68:71]
ds_read_u16_d16_hi v50,v50
v_lshlrev_b32_sdwa v51,1,v51 dst_sel:DWORD dst_unused:UNUSED_PAD src0_sel:DWORD src1_sel:WORD_1
v_mfma_f32_16x16x32_bf16 v[64:67],a[244:247],v[28:31],v[64:67]
ds_read_u16 v49,v49
v_lshlrev_b32_sdwa v44,1,v44 dst_sel:DWORD dst_unused:UNUSED_PAD src0_sel:DWORD src1_sel:WORD_1
v_mfma_f32_16x16x32_bf16 v[144:147],a[248:251],v[28:31],v[144:147]
ds_read_u16_d16_hi v51,v51
v_lshlrev_b32_sdwa v46,1,v46 dst_sel:DWORD dst_unused:UNUSED_PAD src0_sel:DWORD src1_sel:WORD_1
v_mfma_f32_16x16x32_bf16 v[148:151],a[248:251],v[24:27],v[148:151]
s_waitcnt lgkmcnt(4)
ds_read_u16 v44,v44
v_or3_b32 v13,v14,v15,v13
v_mfma_f32_16x16x32_bf16 v[84:87],a[252:255],v[24:27],v[84:87]
ds_read_u16_d16_hi v46,v46
v_and_b32 v13,0x40004000,v13
v_mfma_f32_16x16x32_bf16 v[80:83],a[252:255],v[28:31],v[80:83]
v_cmp_ne_u32_e32 vcc,0,v13
s_nop 0
s_or_b64 s[90:91],s[90:91],vcc
v_lshlrev_b32_sdwa v45,1,v45 dst_sel:DWORD dst_unused:UNUSED_PAD src0_sel:DWORD src1_sel:WORD_1
v_mfma_f32_16x16x32_bf16 v[152:155],a[240:243],v[28:31],v[152:155]
ds_read_u16 v45,v45
v_mbcnt_lo_u32_b32 v15,-1,0
v_mfma_f32_16x16x32_bf16 v[156:159],a[240:243],v[24:27],v[156:159]
v_mbcnt_hi_u32_b32 v15,-1,v15
v_lshlrev_b32_sdwa v47,1,v47 dst_sel:DWORD dst_unused:UNUSED_PAD src0_sel:DWORD src1_sel:WORD_1
v_mfma_f32_16x16x32_bf16 v[100:103],a[224:227],v[24:27],v[100:103]
ds_read_u16_d16_hi v47,v47
v_lshlrev_b32 v15,2,v15
v_mfma_f32_16x16x32_bf16 v[96:99],a[224:227],v[28:31],v[96:99]
ds_read_b32 v7,v15 offset:14848
ds_read_b32 v13,v15 offset:15104
v_mfma_f32_16x16x32_bf16 v[24:27],a[216:219],a[56:59],v[16:19]
ds_read_b32 v14,v15 offset:14336
ds_read_b32 v15,v15 offset:14592
v_lshlrev_b32_sdwa v36,1,v36 dst_sel:DWORD dst_unused:UNUSED_PAD src0_sel:DWORD src1_sel:WORD_1
v_mfma_f32_16x16x32_bf16 v[24:27],a[220:223],a[72:75],v[24:27]
ds_read_u16 v36,v36
v_fma_mixlo_f16 v8,v53,s99,0
v_mfma_f32_16x16x32_bf16 v[24:27],a[192:195],a[88:91],v[24:27]
v_lshl_add_u64 v[60:61],v[60:61],0,v[62:63]
v_lshlrev_b32_sdwa v38,1,v38 dst_sel:DWORD dst_unused:UNUSED_PAD src0_sel:DWORD src1_sel:WORD_1
v_mfma_f32_16x16x32_bf16 v[28:31],a[196:199],a[88:91],v[20:23]
ds_read_u16_d16_hi v38,v38
v_lshl_add_u64 v[62:63],v[32:33],0,v[34:35]
v_fma_mixhi_f16 v8,v55,s99,0
v_mfma_f32_16x16x32_bf16 v[28:31],a[200:203],a[72:75],v[28:31]
s_waitcnt lgkmcnt(8)
v_lshl_add_u64 v[32:33],v[48:49],0,v[50:51]
v_lshlrev_b32_sdwa v37,1,v37 dst_sel:DWORD dst_unused:UNUSED_PAD src0_sel:DWORD src1_sel:WORD_1
v_pk_fma_f16 v9,v8,v8,s54
v_mfma_f32_16x16x32_bf16 v[28:31],a[212:215],a[56:59],v[28:31]
ds_read_u16 v37,v37
v_lshlrev_b32_sdwa v39,1,v39 dst_sel:DWORD dst_unused:UNUSED_PAD src0_sel:DWORD src1_sel:WORD_1
v_mfma_f32_16x16x32_bf16 v[48:51],a[212:215],a[60:63],v[20:23]
ds_read_b128 v[20:23],v4 offset:1408
v_pk_mul_f16 v9,v8,v9
v_mfma_f32_16x16x32_bf16 v[48:51],a[200:203],a[76:79],v[48:51]
s_waitcnt lgkmcnt(8)
ds_read_u16_d16_hi v39,v39
ds_read_b64_tr_b16 a[212:213],v1 offset:16416
v_lshl_add_u64 v[34:35],v[44:45],0,v[46:47]
v_lshlrev_b32_sdwa v40,1,v40 dst_sel:DWORD dst_unused:UNUSED_PAD src0_sel:DWORD src1_sel:WORD_1
v_mfma_f32_16x16x32_bf16 v[48:51],a[196:199],a[92:95],v[48:51]
ds_read_b64_tr_b16 a[214:215],v1 offset:17440
v_exp_f16 v8,v9
v_mfma_f32_16x16x32_bf16 v[44:47],a[192:195],a[92:95],v[16:19]
ds_read_b128 v[16:19],v4 offset:1440
ds_read_b64_tr_b16 a[200:201],v2 offset:16416
v_lshlrev_b32_sdwa v42,1,v42 dst_sel:DWORD dst_unused:UNUSED_PAD src0_sel:DWORD src1_sel:WORD_1
v_mfma_f32_16x16x32_bf16 v[44:47],a[220:223],a[76:79],v[44:47]
ds_read_b64_tr_b16 a[202:203],v2 offset:17440
ds_read_u16 v40,v40
v_lshlrev_b32_sdwa v41,1,v41 dst_sel:DWORD dst_unused:UNUSED_PAD src0_sel:DWORD src1_sel:WORD_1
v_mfma_f32_16x16x32_bf16 v[44:47],a[216:219],a[60:63],v[44:47]
ds_read_u16_d16_hi v42,v42
v_exp_f16_sdwa v8,v9 dst_sel:WORD_1 dst_unused:UNUSED_PRESERVE src0_sel:WORD_1
v_mfma_f32_16x16x32_bf16 v[112:115],a[236:239],v[60:63],v[112:115]
ds_read_u16 v41,v41
ds_read_b64_tr_b16 a[196:197],v2 offset:14368
v_lshlrev_b32_sdwa v43,1,v43 dst_sel:DWORD dst_unused:UNUSED_PAD src0_sel:DWORD src1_sel:WORD_1
v_mfma_f32_16x16x32_bf16 v[116:119],a[236:239],v[32:35],v[116:119]
ds_read_u16_d16_hi v43,v43
v_lshlrev_b32_sdwa v56,1,v56 dst_sel:DWORD dst_unused:UNUSED_PAD src0_sel:DWORD src1_sel:WORD_1
v_mfma_f32_16x16x32_bf16 v[76:79],a[244:247],v[32:35],v[76:79]
ds_read_b64_tr_b16 a[198:199],v2 offset:15392
v_pk_fma_f16 v8,v8,s55,s55
v_mfma_f32_16x16x32_bf16 v[72:75],a[244:247],v[60:63],v[72:75]
v_rcp_f16 v9,v8
v_lshlrev_b32_sdwa v58,1,v58 dst_sel:DWORD dst_unused:UNUSED_PAD src0_sel:DWORD src1_sel:WORD_1
v_mfma_f32_16x16x32_bf16 v[120:123],a[248:251],v[60:63],v[120:123]
ds_read_u16 v56,v56
v_lshlrev_b32_sdwa v57,1,v57 dst_sel:DWORD dst_unused:UNUSED_PAD src0_sel:DWORD src1_sel:WORD_1
v_mfma_f32_16x16x32_bf16 v[124:127],a[248:251],v[32:35],v[124:127]
ds_read_u16_d16_hi v58,v58
v_lshlrev_b32_sdwa v59,1,v59 dst_sel:DWORD dst_unused:UNUSED_PAD src0_sel:DWORD src1_sel:WORD_1
v_mfma_f32_16x16x32_bf16 v[92:95],a[252:255],v[32:35],v[92:95]
ds_read_u16 v57,v57
v_lshlrev_b32_sdwa v52,1,v52 dst_sel:DWORD dst_unused:UNUSED_PAD src0_sel:DWORD src1_sel:WORD_1
v_mfma_f32_16x16x32_bf16 v[88:91],a[252:255],v[60:63],v[88:91]
ds_read_u16_d16_hi v59,v59
v_rcp_f16_sdwa v9,v8 dst_sel:WORD_1 dst_unused:UNUSED_PRESERVE src0_sel:WORD_1
v_mfma_f32_16x16x32_bf16 v[128:131],a[240:243],v[60:63],v[128:131]
ds_read_b64_tr_b16 a[192:193],v2 offset:12320
v_lshlrev_b32_sdwa v54,1,v54 dst_sel:DWORD dst_unused:UNUSED_PAD src0_sel:DWORD src1_sel:WORD_1
v_mfma_f32_16x16x32_bf16 v[132:135],a[240:243],v[32:35],v[132:135]
ds_read_b64_tr_b16 a[194:195],v2 offset:13344
v_fma_mix_f32 v53,v53,v9,0 op_sel_hi:[0,1,0]
v_mfma_f32_16x16x32_bf16 v[108:111],a[224:227],v[32:35],v[108:111]
ds_read_u16 v52,v52
v_fma_mix_f32 v55,v55,v9,0 op_sel:[0,1,0] op_sel_hi:[0,1,0]
v_mfma_f32_16x16x32_bf16 v[104:107],a[224:227],v[60:63],v[104:107]
s_waitcnt lgkmcnt(7)
ds_read_u16_d16_hi v54,v54
v_mfma_f32_16x16x32_bf16 v[32:35],a[208:211],a[0:3],v[20:23]
v_lshlrev_b32_sdwa v24,1,v24 dst_sel:DWORD dst_unused:UNUSED_PAD src0_sel:DWORD src1_sel:WORD_1
v_lshlrev_b32_sdwa v26,1,v26 dst_sel:DWORD dst_unused:UNUSED_PAD src0_sel:DWORD src1_sel:WORD_1
v_mfma_f32_16x16x32_bf16 v[32:35],a[204:207],a[16:19],v[32:35]
ds_read_u16 v24,v24
v_lshlrev_b32_sdwa v25,1,v25 dst_sel:DWORD dst_unused:UNUSED_PAD src0_sel:DWORD src1_sel:WORD_1
v_mfma_f32_16x16x32_bf16 v[32:35],a[212:215],a[32:35],v[32:35]
ds_read_u16_d16_hi v26,v26
v_lshl_add_u64 v[36:37],v[36:37],0,v[38:39]
v_mfma_f32_16x16x32_bf16 v[60:63],a[200:203],a[32:35],v[16:19]
ds_read_u16 v25,v25
v_lshl_add_u64 v[38:39],v[40:41],0,v[42:43]
v_mfma_f32_16x16x32_bf16 v[60:63],a[196:199],a[16:19],v[60:63]
s_waitcnt lgkmcnt(5)
v_lshl_add_u64 v[40:41],v[56:57],0,v[58:59]
v_lshlrev_b32_sdwa v27,1,v27 dst_sel:DWORD dst_unused:UNUSED_PAD src0_sel:DWORD src1_sel:WORD_1
v_mfma_f32_16x16x32_bf16 v[60:63],a[192:195],a[0:3],v[60:63]
ds_read_u16_d16_hi v27,v27
v_lshlrev_b32_sdwa v28,1,v28 dst_sel:DWORD dst_unused:UNUSED_PAD src0_sel:DWORD src1_sel:WORD_1
v_mfma_f32_16x16x32_bf16 v[56:59],a[192:195],a[4:7],v[16:19]
s_waitcnt lgkmcnt(4)
ds_read_u16 v28,v28
v_or_b32 v42,v52,v54
v_mfma_f32_16x16x32_bf16 v[56:59],a[196:199],a[20:23],v[56:59]
s_waitcnt vmcnt(4)
v_cvt_pk_bf16_f32 v43,v53,v55
v_lshlrev_b32_sdwa v30,1,v30 dst_sel:DWORD dst_unused:UNUSED_PAD src0_sel:DWORD src1_sel:WORD_1
v_mfma_f32_16x16x32_bf16 v[56:59],a[200:203],a[36:39],v[56:59]
s_barrier
ds_read_u16_d16_hi v30,v30
v_lshlrev_b32_sdwa v29,1,v29 dst_sel:DWORD dst_unused:UNUSED_PAD src0_sel:DWORD src1_sel:WORD_1
v_mfma_f32_16x16x32_bf16 v[52:55],a[212:215],a[36:39],v[20:23]
ds_read_u16 v29,v29
v_lshlrev_b32_sdwa v31,1,v31 dst_sel:DWORD dst_unused:UNUSED_PAD src0_sel:DWORD src1_sel:WORD_1
v_mfma_f32_16x16x32_bf16 v[52:55],a[204:207],a[20:23],v[52:55]
s_add_u32 m0,s58,6160
ds_read_u16_d16_hi v31,v31
v_lshlrev_b32_sdwa v44,1,v44 dst_sel:DWORD dst_unused:UNUSED_PAD src0_sel:DWORD src1_sel:WORD_1
v_mfma_f32_16x16x32_bf16 v[52:55],a[208:211],a[4:7],v[52:55]
s_add_u32 s69,s69,s59
ds_read_u16 v44,v44
v_lshlrev_b32_sdwa v46,1,v46 dst_sel:DWORD dst_unused:UNUSED_PAD src0_sel:DWORD src1_sel:WORD_1
v_mfma_f32_16x16x32_bf16 v[232:235],a[236:239],v[36:39],v[232:235]
buffer_load_dwordx4 v5,s[44:47],s69 offen offset:0 lds
v_lshlrev_b32_sdwa v45,1,v45 dst_sel:DWORD dst_unused:UNUSED_PAD src0_sel:DWORD src1_sel:WORD_1
v_mfma_f32_16x16x32_bf16 v[236:239],a[236:239],v[40:43],v[236:239]
ds_read_u16_d16_hi v46,v46
v_lshlrev_b32_sdwa v47,1,v47 dst_sel:DWORD dst_unused:UNUSED_PAD src0_sel:DWORD src1_sel:WORD_1
v_mfma_f32_16x16x32_bf16 v[164:167],a[244:247],v[40:43],v[164:167]
ds_read_u16 v45,v45
ds_read_u16_d16_hi v47,v47
v_mfma_f32_16x16x32_bf16 v[160:163],a[244:247],v[36:39],v[160:163]
ds_read_b64_tr_b16 a[228:229],v3 offset:18480
v_lshlrev_b32_sdwa v48,1,v48 dst_sel:DWORD dst_unused:UNUSED_PAD src0_sel:DWORD src1_sel:WORD_1
v_mfma_f32_16x16x32_bf16 v[240:243],a[248:251],v[36:39],v[240:243]
s_add_u32 s69,s69,s53
ds_read_u16 v48,v48
v_lshlrev_b32_sdwa v50,1,v50 dst_sel:DWORD dst_unused:UNUSED_PAD src0_sel:DWORD src1_sel:WORD_1
v_mfma_f32_16x16x32_bf16 v[244:247],a[248:251],v[40:43],v[244:247]
buffer_load_dwordx4 v5,s[44:47],s69 offen offset:1024 lds
ds_read_u16_d16_hi v50,v50
v_mfma_f32_16x16x32_bf16 v[180:183],a[252:255],v[40:43],v[180:183]
ds_read_b64_tr_b16 a[230:231],v3 offset:20016
v_lshlrev_b32_sdwa v49,1,v49 dst_sel:DWORD dst_unused:UNUSED_PAD src0_sel:DWORD src1_sel:WORD_1
v_mfma_f32_16x16x32_bf16 v[176:179],a[252:255],v[36:39],v[176:179]
ds_read_u16 v49,v49
v_lshlrev_b32_sdwa v51,1,v51 dst_sel:DWORD dst_unused:UNUSED_PAD src0_sel:DWORD src1_sel:WORD_1
v_mfma_f32_16x16x32_bf16 v[248:251],a[240:243],v[36:39],v[248:251]
ds_read_u16_d16_hi v51,v51
v_mfma_f32_16x16x32_bf16 v[252:255],a[240:243],v[40:43],v[252:255]
s_add_u32 s69,s69,s53
ds_read_b64_tr_b16 a[232:233],v3 offset:18488
v_mfma_f32_16x16x32_bf16 v[196:199],a[224:227],v[40:43],v[196:199]
buffer_load_dwordx4 v5,s[44:47],s69 offen offset:2048 lds
v_mfma_f32_16x16x32_bf16 v[192:195],a[224:227],v[36:39],v[192:195]
ds_read_b64_tr_b16 a[234:235],v3 offset:20024
v_mfma_f32_16x16x32_bf16 v[40:43],a[208:211],a[8:11],v[20:23]
ds_read_b64_tr_b16 a[220:221],v1 offset:18480
v_lshlrev_b32_sdwa v32,1,v32 dst_sel:DWORD dst_unused:UNUSED_PAD src0_sel:DWORD src1_sel:WORD_1
v_mfma_f32_16x16x32_bf16 v[40:43],a[204:207],a[24:27],v[40:43]
ds_read_u16 v32,v32
v_fma_mixlo_f16 v8,v57,s99,0
v_mfma_f32_16x16x32_bf16 v[40:43],a[212:215],a[40:43],v[40:43]
s_waitcnt lgkmcnt(7)
ds_read_b64_tr_b16 a[222:223],v1 offset:19504
v_lshl_add_u64 v[24:25],v[24:25],0,v[26:27]
v_lshlrev_b32_sdwa v34,1,v34 dst_sel:DWORD dst_unused:UNUSED_PAD src0_sel:DWORD src1_sel:WORD_1
v_mfma_f32_16x16x32_bf16 v[36:39],a[200:203],a[40:43],v[16:19]
ds_read_u16_d16_hi v34,v34
v_lshl_add_u64 v[26:27],v[28:29],0,v[30:31]
v_fma_mixhi_f16 v8,v59,s99,0
v_mfma_f32_16x16x32_bf16 v[36:39],a[196:199],a[24:27],v[36:39]
ds_read_b64_tr_b16 a[216:217],v1 offset:20528
v_lshl_add_u64 v[28:29],v[44:45],0,v[46:47]
v_lshlrev_b32_sdwa v33,1,v33 dst_sel:DWORD dst_unused:UNUSED_PAD src0_sel:DWORD src1_sel:WORD_1
v_pk_fma_f16 v9,v8,v8,s54
v_mfma_f32_16x16x32_bf16 v[36:39],a[192:195],a[8:11],v[36:39]
ds_read_u16 v33,v33
v_lshlrev_b32_sdwa v35,1,v35 dst_sel:DWORD dst_unused:UNUSED_PAD src0_sel:DWORD src1_sel:WORD_1
v_mfma_f32_16x16x32_bf16 v[44:47],a[192:195],a[12:15],v[16:19]
ds_read_u16_d16_hi v35,v35
v_pk_mul_f16 v9,v8,v9
v_mfma_f32_16x16x32_bf16 v[44:47],a[196:199],a[28:31],v[44:47]
s_waitcnt lgkmcnt(8)
ds_read_b64_tr_b16 a[218:219],v1 offset:21552
v_lshl_add_u64 v[30:31],v[48:49],0,v[50:51]
v_lshlrev_b32_sdwa v60,1,v60 dst_sel:DWORD dst_unused:UNUSED_PAD src0_sel:DWORD src1_sel:WORD_1
v_mfma_f32_16x16x32_bf16 v[44:47],a[200:203],a[44:47],v[44:47]
v_exp_f16 v8,v9
v_mfma_f32_16x16x32_bf16 v[48:51],a[212:215],a[44:47],v[20:23]
ds_read_u16 v60,v60
v_lshlrev_b32_sdwa v62,1,v62 dst_sel:DWORD dst_unused:UNUSED_PAD src0_sel:DWORD src1_sel:WORD_1
v_mfma_f32_16x16x32_bf16 v[48:51],a[204:207],a[28:31],v[48:51]
ds_read_u16_d16_hi v62,v62
v_lshlrev_b32_sdwa v61,1,v61 dst_sel:DWORD dst_unused:UNUSED_PAD src0_sel:DWORD src1_sel:WORD_1
v_mfma_f32_16x16x32_bf16 v[48:51],a[208:211],a[12:15],v[48:51]
v_exp_f16_sdwa v8,v9 dst_sel:WORD_1 dst_unused:UNUSED_PRESERVE src0_sel:WORD_1
v_mfma_f32_16x16x32_bf16 v[208:211],a[236:239],v[24:27],v[208:211]
ds_read_u16 v61,v61
v_lshlrev_b32_sdwa v63,1,v63 dst_sel:DWORD dst_unused:UNUSED_PAD src0_sel:DWORD src1_sel:WORD_1
v_mfma_f32_16x16x32_bf16 v[212:215],a[236:239],v[28:31],v[212:215]
ds_read_u16_d16_hi v63,v63
v_lshlrev_b32_sdwa v52,1,v52 dst_sel:DWORD dst_unused:UNUSED_PAD src0_sel:DWORD src1_sel:WORD_1
v_mfma_f32_16x16x32_bf16 v[172:175],a[244:247],v[28:31],v[172:175]
ds_read_u16 v52,v52
v_pk_fma_f16 v8,v8,s55,s55
v_mfma_f32_16x16x32_bf16 v[168:171],a[244:247],v[24:27],v[168:171]
ds_read_b64_tr_b16 a[236:237],v3 offset:18544
v_rcp_f16 v9,v8
v_lshlrev_b32_sdwa v54,1,v54 dst_sel:DWORD dst_unused:UNUSED_PAD src0_sel:DWORD src1_sel:WORD_1
v_mfma_f32_16x16x32_bf16 v[216:219],a[248:251],v[24:27],v[216:219]
ds_read_u16_d16_hi v54,v54
v_lshlrev_b32_sdwa v53,1,v53 dst_sel:DWORD dst_unused:UNUSED_PAD src0_sel:DWORD src1_sel:WORD_1
v_mfma_f32_16x16x32_bf16 v[220:223],a[248:251],v[28:31],v[220:223]
ds_read_u16 v53,v53
ds_read_b64_tr_b16 a[238:239],v3 offset:20080
v_mfma_f32_16x16x32_bf16 v[188:191],a[252:255],v[28:31],v[188:191]
ds_read_b64_tr_b16 a[244:245],v3 offset:18552
v_lshlrev_b32_sdwa v55,1,v55 dst_sel:DWORD dst_unused:UNUSED_PAD src0_sel:DWORD src1_sel:WORD_1
v_mfma_f32_16x16x32_bf16 v[184:187],a[252:255],v[24:27],v[184:187]
ds_read_u16_d16_hi v55,v55
v_rcp_f16_sdwa v9,v8 dst_sel:WORD_1 dst_unused:UNUSED_PRESERVE src0_sel:WORD_1
v_mfma_f32_16x16x32_bf16 v[224:227],a[240:243],v[24:27],v[224:227]
ds_read_b64_tr_b16 a[246:247],v3 offset:20088
ds_read_b64_tr_b16 a[248:249],v3 offset:18608
v_lshlrev_b32_sdwa v56,1,v56 dst_sel:DWORD dst_unused:UNUSED_PAD src0_sel:DWORD src1_sel:WORD_1
v_mfma_f32_16x16x32_bf16 v[228:231],a[240:243],v[28:31],v[228:231]
ds_read_u16 v56,v56
v_lshlrev_b32_sdwa v58,1,v58 dst_sel:DWORD dst_unused:UNUSED_PAD src0_sel:DWORD src1_sel:WORD_1
v_mfma_f32_16x16x32_bf16 v[204:207],a[224:227],v[28:31],v[204:207]
ds_read_u16_d16_hi v58,v58
v_fma_mix_f32 v57,v57,v9,0 op_sel_hi:[0,1,0]
v_mfma_f32_16x16x32_bf16 v[200:203],a[224:227],v[24:27],v[200:203]
ds_read_b64_tr_b16 a[250:251],v3 offset:20144
v_fma_mix_f32 v59,v59,v9,0 op_sel:[0,1,0] op_sel_hi:[0,1,0]
v_mfma_f32_16x16x32_bf16 v[28:31],a[208:211],a[48:51],v[20:23]
s_add_u32 m0,s58,12320
ds_read_b64_tr_b16 a[252:253],v3 offset:18616
v_lshlrev_b32_sdwa v40,1,v40 dst_sel:DWORD dst_unused:UNUSED_PAD src0_sel:DWORD src1_sel:WORD_1
v_mfma_f32_16x16x32_bf16 v[28:31],a[204:207],a[64:67],v[28:31]
s_add_u32 s69,s69,s59
ds_read_b64_tr_b16 a[254:255],v3 offset:20152
v_lshlrev_b32_sdwa v42,1,v42 dst_sel:DWORD dst_unused:UNUSED_PAD src0_sel:DWORD src1_sel:WORD_1
v_mfma_f32_16x16x32_bf16 v[28:31],a[212:215],a[80:83],v[28:31]
s_waitcnt lgkmcnt(9)
buffer_load_dwordx4 v5,s[44:47],s69 offen offset:0 lds
v_lshl_add_u64 v[32:33],v[32:33],0,v[34:35]
v_mfma_f32_16x16x32_bf16 v[24:27],a[200:203],a[80:83],v[16:19]
s_add_u32 s69,s69,s53
ds_read_u16 v40,v40
v_lshl_add_u64 v[34:35],v[60:61],0,v[62:63]
v_lshlrev_b32_sdwa v41,1,v41 dst_sel:DWORD dst_unused:UNUSED_PAD src0_sel:DWORD src1_sel:WORD_1
v_mfma_f32_16x16x32_bf16 v[24:27],a[196:199],a[64:67],v[24:27]
s_waitcnt lgkmcnt(8)
buffer_load_dwordx4 v5,s[44:47],s69 offen offset:1024 lds
v_lshl_add_u64 v[60:61],v[52:53],0,v[54:55]
v_mfma_f32_16x16x32_bf16 v[24:27],a[192:195],a[48:51],v[24:27]
s_add_u32 s69,s69,s53
ds_read_u16_d16_hi v42,v42
v_lshlrev_b32_sdwa v43,1,v43 dst_sel:DWORD dst_unused:UNUSED_PAD src0_sel:DWORD src1_sel:WORD_1
v_mfma_f32_16x16x32_bf16 v[52:55],a[192:195],a[52:55],v[16:19]
s_waitcnt lgkmcnt(5)
buffer_load_dwordx4 v5,s[44:47],s69 offen offset:2048 lds
v_or_b32 v62,v56,v58
v_lshlrev_b32_sdwa v36,1,v36 dst_sel:DWORD dst_unused:UNUSED_PAD src0_sel:DWORD src1_sel:WORD_1
v_mfma_f32_16x16x32_bf16 v[52:55],a[196:199],a[68:71],v[52:55]
ds_read_u16 v41,v41
v_cvt_pk_bf16_f32 v63,v57,v59
v_lshlrev_b32_sdwa v38,1,v38 dst_sel:DWORD dst_unused:UNUSED_PAD src0_sel:DWORD src1_sel:WORD_1
v_mfma_f32_16x16x32_bf16 v[52:55],a[200:203],a[84:87],v[52:55]
ds_read_u16_d16_hi v43,v43
v_lshlrev_b32_sdwa v37,1,v37 dst_sel:DWORD dst_unused:UNUSED_PAD src0_sel:DWORD src1_sel:WORD_1
v_mfma_f32_16x16x32_bf16 v[56:59],a[212:215],a[84:87],v[20:23]
ds_read_u16 v36,v36
v_lshlrev_b32_sdwa v39,1,v39 dst_sel:DWORD dst_unused:UNUSED_PAD src0_sel:DWORD src1_sel:WORD_1
v_mfma_f32_16x16x32_bf16 v[56:59],a[204:207],a[68:71],v[56:59]
ds_read_u16_d16_hi v38,v38
v_lshlrev_b32_sdwa v48,1,v48 dst_sel:DWORD dst_unused:UNUSED_PAD src0_sel:DWORD src1_sel:WORD_1
v_mfma_f32_16x16x32_bf16 v[56:59],a[208:211],a[52:55],v[56:59]
ds_read_u16 v37,v37
v_lshlrev_b32_sdwa v50,1,v50 dst_sel:DWORD dst_unused:UNUSED_PAD src0_sel:DWORD src1_sel:WORD_1
v_mfma_f32_16x16x32_bf16 v[136:139],a[228:231],v[32:35],v[136:139]
ds_read_u16_d16_hi v39,v39
v_lshlrev_b32_sdwa v49,1,v49 dst_sel:DWORD dst_unused:UNUSED_PAD src0_sel:DWORD src1_sel:WORD_1
v_mfma_f32_16x16x32_bf16 v[140:143],a[228:231],v[60:63],v[140:143]
ds_read_u16 v48,v48
v_lshlrev_b32_sdwa v51,1,v51 dst_sel:DWORD dst_unused:UNUSED_PAD src0_sel:DWORD src1_sel:WORD_1
v_mfma_f32_16x16x32_bf16 v[68:71],a[232:235],v[60:63],v[68:71]
ds_read_u16_d16_hi v50,v50
v_lshlrev_b32_sdwa v44,1,v44 dst_sel:DWORD dst_unused:UNUSED_PAD src0_sel:DWORD src1_sel:WORD_1
v_mfma_f32_16x16x32_bf16 v[64:67],a[232:235],v[32:35],v[64:67]
ds_read_u16 v49,v49
v_lshlrev_b32_sdwa v46,1,v46 dst_sel:DWORD dst_unused:UNUSED_PAD src0_sel:DWORD src1_sel:WORD_1
v_mfma_f32_16x16x32_bf16 v[144:147],a[236:239],v[32:35],v[144:147]
ds_read_u16_d16_hi v51,v51
v_lshlrev_b32_sdwa v45,1,v45 dst_sel:DWORD dst_unused:UNUSED_PAD src0_sel:DWORD src1_sel:WORD_1
v_mfma_f32_16x16x32_bf16 v[148:151],a[236:239],v[60:63],v[148:151]
ds_read_u16 v44,v44
v_lshlrev_b32_sdwa v47,1,v47 dst_sel:DWORD dst_unused:UNUSED_PAD src0_sel:DWORD src1_sel:WORD_1
v_mfma_f32_16x16x32_bf16 v[84:87],a[244:247],v[60:63],v[84:87]
ds_read_u16_d16_hi v46,v46
v_mfma_f32_16x16x32_bf16 v[80:83],a[244:247],v[32:35],v[80:83]
s_waitcnt lgkmcnt(8)
ds_read_u16 v45,v45
v_mfma_f32_16x16x32_bf16 v[152:155],a[248:251],v[32:35],v[152:155]
ds_read_u16_d16_hi v47,v47
v_mfma_f32_16x16x32_bf16 v[156:159],a[248:251],v[60:63],v[156:159]
v_mfma_f32_16x16x32_bf16 v[100:103],a[252:255],v[60:63],v[100:103]
v_mfma_f32_16x16x32_bf16 v[96:99],a[252:255],v[32:35],v[96:99]
v_mfma_f32_16x16x32_bf16 v[60:63],a[208:211],a[56:59],v[20:23]
v_mul_f32_dpp v136,v14,v136 row_newbcast:0 row_mask:0xf bank_mask:0xf
s_add_u32 s8,s61,0
v_mul_f32_dpp v137,v14,v137 row_newbcast:1 row_mask:0xf bank_mask:0xf
v_mul_f32_dpp v138,v14,v138 row_newbcast:2 row_mask:0xf bank_mask:0xf
v_lshlrev_b32_sdwa v28,1,v28 dst_sel:DWORD dst_unused:UNUSED_PAD src0_sel:DWORD src1_sel:WORD_1
v_mfma_f32_16x16x32_bf16 v[60:63],a[204:207],a[72:75],v[60:63]
ds_read_u16 v28,v28
v_mul_f32_dpp v139,v14,v139 row_newbcast:3 row_mask:0xf bank_mask:0xf
v_mul_f32_dpp v64,v14,v64 row_newbcast:4 row_mask:0xf bank_mask:0xf
v_mul_f32_dpp v65,v14,v65 row_newbcast:5 row_mask:0xf bank_mask:0xf
v_mfma_f32_16x16x32_bf16 v[60:63],a[212:215],a[88:91],v[60:63]
s_nop 0
v_lshl_add_u64 v[40:41],v[40:41],0,v[42:43]
v_mul_f32_dpp v66,v14,v66 row_newbcast:6 row_mask:0xf bank_mask:0xf
v_mul_f32_dpp v67,v14,v67 row_newbcast:7 row_mask:0xf bank_mask:0xf
v_lshlrev_b32_sdwa v30,1,v30 dst_sel:DWORD dst_unused:UNUSED_PAD src0_sel:DWORD src1_sel:WORD_1
v_cvt_pk_bf16_f32 v136,v136,v137
v_mfma_f32_16x16x32_bf16 v[32:35],a[200:203],a[88:91],v[16:19]
s_waitcnt lgkmcnt(3)
ds_read_u16_d16_hi v30,v30
v_lshl_add_u64 v[42:43],v[36:37],0,v[38:39]
v_cvt_pk_bf16_f32 v137,v138,v139
v_cvt_pk_bf16_f32 v138,v64,v65
v_lshlrev_b32_sdwa v29,1,v29 dst_sel:DWORD dst_unused:UNUSED_PAD src0_sel:DWORD src1_sel:WORD_1
v_cvt_pk_bf16_f32 v139,v66,v67
v_mfma_f32_16x16x32_bf16 v[32:35],a[196:199],a[72:75],v[32:35]
buffer_store_dwordx4 v[136:139],v7,s[40:43],s8 offen offset:0 sc1
v_lshl_add_u64 v[36:37],v[48:49],0,v[50:51]
v_mul_f32_dpp v144,v14,v144 row_newbcast:8 row_mask:0xf bank_mask:0xf
v_mul_f32_dpp v145,v14,v145 row_newbcast:9 row_mask:0xf bank_mask:0xf
v_mul_f32_dpp v146,v14,v146 row_newbcast:10 row_mask:0xf bank_mask:0xf
v_mfma_f32_16x16x32_bf16 v[32:35],a[192:195],a[56:59],v[32:35]
ds_read_u16 v29,v29
v_mul_f32_dpp v147,v14,v147 row_newbcast:11 row_mask:0xf bank_mask:0xf
v_mul_f32_dpp v80,v14,v80 row_newbcast:12 row_mask:0xf bank_mask:0xf
v_lshlrev_b32_sdwa v31,1,v31 dst_sel:DWORD dst_unused:UNUSED_PAD src0_sel:DWORD src1_sel:WORD_1
v_mul_f32_dpp v81,v14,v81 row_newbcast:13 row_mask:0xf bank_mask:0xf
v_mfma_f32_16x16x32_bf16 v[48:51],a[192:195],a[60:63],v[16:19]
s_nop 0
ds_read_b128 v[16:19],v4 offset:0
v_mul_f32_dpp v82,v14,v82 row_newbcast:14 row_mask:0xf bank_mask:0xf
v_mul_f32_dpp v83,v14,v83 row_newbcast:15 row_mask:0xf bank_mask:0xf
v_lshlrev_b32_sdwa v24,1,v24 dst_sel:DWORD dst_unused:UNUSED_PAD src0_sel:DWORD src1_sel:WORD_1
v_cvt_pk_bf16_f32 v144,v144,v145
v_mfma_f32_16x16x32_bf16 v[48:51],a[196:199],a[76:79],v[48:51]
s_waitcnt lgkmcnt(4)
ds_read_u16_d16_hi v31,v31
ds_read_b64_tr_b16 a[192:193],v1 offset:22576
v_lshl_add_u64 v[38:39],v[44:45],0,v[46:47]
v_cvt_pk_bf16_f32 v145,v146,v147
v_cvt_pk_bf16_f32 v146,v80,v81
v_cvt_pk_bf16_f32 v147,v82,v83
v_lshlrev_b32_sdwa v26,1,v26 dst_sel:DWORD dst_unused:UNUSED_PAD src0_sel:DWORD src1_sel:WORD_1
v_mfma_f32_16x16x32_bf16 v[48:51],a[200:203],a[92:95],v[48:51]
ds_read_b64_tr_b16 a[194:195],v1 offset:23600
ds_read_u16 v24,v24
v_mul_f32_dpp v152,v15,v152 row_newbcast:0 row_mask:0xf bank_mask:0xf
v_mul_f32_dpp v153,v15,v153 row_newbcast:1 row_mask:0xf bank_mask:0xf
v_mul_f32_dpp v154,v15,v154 row_newbcast:2 row_mask:0xf bank_mask:0xf
v_mfma_f32_16x16x32_bf16 v[44:47],a[212:215],a[92:95],v[20:23]
ds_read_b128 v[20:23],v4 offset:32
ds_read_b64_tr_b16 a[196:197],v2 offset:22576
v_mul_f32_dpp v155,v15,v155 row_newbcast:3 row_mask:0xf bank_mask:0xf
v_mul_f32_dpp v96,v15,v96 row_newbcast:4 row_mask:0xf bank_mask:0xf
v_lshlrev_b32_sdwa v25,1,v25 dst_sel:DWORD dst_unused:UNUSED_PAD src0_sel:DWORD src1_sel:WORD_1
v_mul_f32_dpp v97,v15,v97 row_newbcast:5 row_mask:0xf bank_mask:0xf
v_mfma_f32_16x16x32_bf16 v[44:47],a[204:207],a[76:79],v[44:47]
ds_read_b64_tr_b16 a[198:199],v2 offset:23600
ds_read_u16_d16_hi v26,v26
v_mul_f32_dpp v98,v15,v98 row_newbcast:6 row_mask:0xf bank_mask:0xf
v_mul_f32_dpp v99,v15,v99 row_newbcast:7 row_mask:0xf bank_mask:0xf
v_lshlrev_b32_sdwa v27,1,v27 dst_sel:DWORD dst_unused:UNUSED_PAD src0_sel:DWORD src1_sel:WORD_1
v_cvt_pk_bf16_f32 v152,v152,v153
v_mfma_f32_16x16x32_bf16 v[44:47],a[208:211],a[60:63],v[44:47]
ds_read_u16 v25,v25
ds_read_u16_d16_hi v27,v27
v_cvt_pk_bf16_f32 v153,v154,v155
v_cvt_pk_bf16_f32 v154,v96,v97
v_cvt_pk_bf16_f32 v155,v98,v99
v_mfma_f32_16x16x32_bf16 v[112:115],a[228:231],v[40:43],v[112:115]
ds_read_b64_tr_b16 a[200:201],v2 offset:20528
v_mul_f32_dpp v140,v14,v140 row_newbcast:0 row_mask:0xf bank_mask:0xf
v_mul_f32_dpp v141,v14,v141 row_newbcast:1 row_mask:0xf bank_mask:0xf
v_lshlrev_b32_sdwa v56,1,v56 dst_sel:DWORD dst_unused:UNUSED_PAD src0_sel:DWORD src1_sel:WORD_1
v_mul_f32_dpp v142,v14,v142 row_newbcast:2 row_mask:0xf bank_mask:0xf
v_mfma_f32_16x16x32_bf16 v[116:119],a[228:231],v[36:39],v[116:119]
ds_read_b64_tr_b16 a[202:203],v2 offset:21552
v_mul_f32_dpp v143,v14,v143 row_newbcast:3 row_mask:0xf bank_mask:0xf
v_mul_f32_dpp v68,v14,v68 row_newbcast:4 row_mask:0xf bank_mask:0xf
v_lshlrev_b32_sdwa v58,1,v58 dst_sel:DWORD dst_unused:UNUSED_PAD src0_sel:DWORD src1_sel:WORD_1
v_mul_f32_dpp v69,v14,v69 row_newbcast:5 row_mask:0xf bank_mask:0xf
v_mfma_f32_16x16x32_bf16 v[76:79],a[232:235],v[36:39],v[76:79]
ds_read_u16 v56,v56
v_mul_f32_dpp v70,v14,v70 row_newbcast:6 row_mask:0xf bank_mask:0xf
v_mul_f32_dpp v71,v14,v71 row_newbcast:7 row_mask:0xf bank_mask:0xf
v_cvt_pk_bf16_f32 v140,v140,v141
v_lshlrev_b32_sdwa v57,1,v57 dst_sel:DWORD dst_unused:UNUSED_PAD src0_sel:DWORD src1_sel:WORD_1
v_mfma_f32_16x16x32_bf16 v[72:75],a[232:235],v[40:43],v[72:75]
TP4876_%=:
s_cbranch_execz TP7012_%=
buffer_store_dwordx4 v[144:147],v7,s[40:43],s8 offen offset:64 sc1
ds_read_u16_d16_hi v58,v58
v_cvt_pk_bf16_f32 v141,v142,v143
v_cvt_pk_bf16_f32 v142,v68,v69
v_cvt_pk_bf16_f32 v143,v70,v71
v_mfma_f32_16x16x32_bf16 v[120:123],a[236:239],v[40:43],v[120:123]
s_nop 0
ds_read_u16 v57,v57
v_mul_f32_dpp v148,v14,v148 row_newbcast:8 row_mask:0xf bank_mask:0xf
v_mul_f32_dpp v149,v14,v149 row_newbcast:9 row_mask:0xf bank_mask:0xf
v_lshlrev_b32_sdwa v59,1,v59 dst_sel:DWORD dst_unused:UNUSED_PAD src0_sel:DWORD src1_sel:WORD_1
v_mul_f32_dpp v150,v14,v150 row_newbcast:10 row_mask:0xf bank_mask:0xf
v_mfma_f32_16x16x32_bf16 v[124:127],a[236:239],v[36:39],v[124:127]
ds_read_u16_d16_hi v59,v59
v_mul_f32_dpp v151,v14,v151 row_newbcast:11 row_mask:0xf bank_mask:0xf
v_mul_f32_dpp v84,v14,v84 row_newbcast:12 row_mask:0xf bank_mask:0xf
v_lshlrev_b32_sdwa v52,1,v52 dst_sel:DWORD dst_unused:UNUSED_PAD src0_sel:DWORD src1_sel:WORD_1
v_mul_f32_dpp v85,v14,v85 row_newbcast:13 row_mask:0xf bank_mask:0xf
v_mfma_f32_16x16x32_bf16 v[92:95],a[244:247],v[36:39],v[92:95]
ds_read_b64_tr_b16 a[212:213],v2 offset:18480
v_mul_f32_dpp v86,v14,v86 row_newbcast:14 row_mask:0xf bank_mask:0xf
v_mul_f32_dpp v87,v14,v87 row_newbcast:15 row_mask:0xf bank_mask:0xf
v_cvt_pk_bf16_f32 v148,v148,v149
v_mfma_f32_16x16x32_bf16 v[88:91],a[244:247],v[40:43],v[88:91]
ds_read_b64_tr_b16 a[214:215],v2 offset:19504
v_cvt_pk_bf16_f32 v149,v150,v151
v_cvt_pk_bf16_f32 v150,v84,v85
v_lshlrev_b32_sdwa v54,1,v54 dst_sel:DWORD dst_unused:UNUSED_PAD src0_sel:DWORD src1_sel:WORD_1
v_cvt_pk_bf16_f32 v151,v86,v87
v_mfma_f32_16x16x32_bf16 v[128:131],a[248:251],v[40:43],v[128:131]
ds_read_u16 v52,v52
v_mul_f32_dpp v156,v15,v156 row_newbcast:0 row_mask:0xf bank_mask:0xf
v_mul_f32_dpp v157,v15,v157 row_newbcast:1 row_mask:0xf bank_mask:0xf
v_lshlrev_b32_sdwa v53,1,v53 dst_sel:DWORD dst_unused:UNUSED_PAD src0_sel:DWORD src1_sel:WORD_1
v_mul_f32_dpp v158,v15,v158 row_newbcast:2 row_mask:0xf bank_mask:0xf
v_mfma_f32_16x16x32_bf16 v[132:135],a[248:251],v[36:39],v[132:135]
ds_read_u16_d16_hi v54,v54
v_mul_f32_dpp v159,v15,v159 row_newbcast:3 row_mask:0xf bank_mask:0xf
v_mul_f32_dpp v100,v15,v100 row_newbcast:4 row_mask:0xf bank_mask:0xf
v_mul_f32_dpp v101,v15,v101 row_newbcast:5 row_mask:0xf bank_mask:0xf
v_lshlrev_b32_sdwa v55,1,v55 dst_sel:DWORD dst_unused:UNUSED_PAD src0_sel:DWORD src1_sel:WORD_1
v_mfma_f32_16x16x32_bf16 v[108:111],a[252:255],v[36:39],v[108:111]
ds_read_u16 v53,v53
v_mul_f32_dpp v102,v15,v102 row_newbcast:6 row_mask:0xf bank_mask:0xf
v_mul_f32_dpp v103,v15,v103 row_newbcast:7 row_mask:0xf bank_mask:0xf
v_cvt_pk_bf16_f32 v156,v156,v157
v_mfma_f32_16x16x32_bf16 v[104:107],a[252:255],v[40:43],v[104:107]
s_waitcnt lgkmcnt(8)
ds_read_u16_d16_hi v55,v55
v_cvt_pk_bf16_f32 v157,v158,v159
v_cvt_pk_bf16_f32 v158,v100,v101
v_cvt_pk_bf16_f32 v159,v102,v103
v_mfma_f32_16x16x32_bf16 v[36:39],a[220:223],a[48:51],v[16:19]
s_nop 0
v_mul_f32_dpp v112,v14,v112 row_newbcast:0 row_mask:0xf bank_mask:0xf
s_add_u32 s9,s61,98304
v_accvgpr_write_b32 a16,0
v_accvgpr_write_b32 a17,0
v_lshlrev_b32_sdwa v60,1,v60 dst_sel:DWORD dst_unused:UNUSED_PAD src0_sel:DWORD src1_sel:WORD_1
v_mfma_f32_16x16x32_bf16 v[36:39],a[216:219],a[64:67],v[36:39]
ds_read_u16 v60,v60
v_accvgpr_write_b32 a18,0
v_accvgpr_write_b32 a19,0
v_mul_f32_dpp v113,v14,v113 row_newbcast:1 row_mask:0xf bank_mask:0xf
v_mfma_f32_16x16x32_bf16 v[36:39],a[192:195],a[80:83],v[36:39]
v_lshl_add_u64 v[28:29],v[28:29],0,v[30:31]
v_mul_f32_dpp v114,v14,v114 row_newbcast:2 row_mask:0xf bank_mask:0xf
v_mul_f32_dpp v115,v14,v115 row_newbcast:3 row_mask:0xf bank_mask:0xf
v_lshlrev_b32_sdwa v62,1,v62 dst_sel:DWORD dst_unused:UNUSED_PAD src0_sel:DWORD src1_sel:WORD_1
v_mul_f32_dpp v72,v14,v72 row_newbcast:4 row_mask:0xf bank_mask:0xf
v_mfma_f32_16x16x32_bf16 v[40:43],a[196:199],a[80:83],v[20:23]
buffer_store_dwordx4 v[152:155],v7,s[40:43],s8 offen offset:128 sc1
v_lshl_add_u64 v[30:31],v[24:25],0,v[26:27]
v_mul_f32_dpp v73,v14,v73 row_newbcast:5 row_mask:0xf bank_mask:0xf
v_mul_f32_dpp v74,v14,v74 row_newbcast:6 row_mask:0xf bank_mask:0xf
v_lshlrev_b32_sdwa v61,1,v61 dst_sel:DWORD dst_unused:UNUSED_PAD src0_sel:DWORD src1_sel:WORD_1
v_mul_f32_dpp v75,v14,v75 row_newbcast:7 row_mask:0xf bank_mask:0xf
v_mfma_f32_16x16x32_bf16 v[40:43],a[200:203],a[64:67],v[40:43]
s_waitcnt lgkmcnt(5)
ds_read_u16_d16_hi v62,v62
v_lshl_add_u64 v[24:25],v[56:57],0,v[58:59]
v_cvt_pk_bf16_f32 v112,v112,v113
v_cvt_pk_bf16_f32 v113,v114,v115
v_cvt_pk_bf16_f32 v114,v72,v73
v_mfma_f32_16x16x32_bf16 v[40:43],a[212:215],a[48:51],v[40:43]
s_nop 0
ds_read_u16 v61,v61
v_cvt_pk_bf16_f32 v115,v74,v75
v_mul_f32_dpp v120,v14,v120 row_newbcast:8 row_mask:0xf bank_mask:0xf
v_lshlrev_b32_sdwa v63,1,v63 dst_sel:DWORD dst_unused:UNUSED_PAD src0_sel:DWORD src1_sel:WORD_1
v_accvgpr_write_b32 a24,v112
v_mfma_f32_16x16x32_bf16 v[56:59],a[212:215],a[52:55],v[20:23]
s_nop 0
ds_read_u16_d16_hi v63,v63
v_mul_f32_dpp v121,v14,v121 row_newbcast:9 row_mask:0xf bank_mask:0xf
v_mul_f32_dpp v122,v14,v122 row_newbcast:10 row_mask:0xf bank_mask:0xf
v_lshlrev_b32_sdwa v32,1,v32 dst_sel:DWORD dst_unused:UNUSED_PAD src0_sel:DWORD src1_sel:WORD_1
v_accvgpr_write_b32 a25,v113
v_mfma_f32_16x16x32_bf16 v[56:59],a[200:203],a[68:71],v[56:59]
s_waitcnt lgkmcnt(4)
ds_read_u16 v32,v32
v_lshl_add_u64 v[26:27],v[52:53],0,v[54:55]
v_mul_f32_dpp v123,v14,v123 row_newbcast:11 row_mask:0xf bank_mask:0xf
v_mul_f32_dpp v88,v14,v88 row_newbcast:12 row_mask:0xf bank_mask:0xf
v_accvgpr_write_b32 a26,v114
v_lshlrev_b32_sdwa v34,1,v34 dst_sel:DWORD dst_unused:UNUSED_PAD src0_sel:DWORD src1_sel:WORD_1
v_mfma_f32_16x16x32_bf16 v[56:59],a[196:199],a[84:87],v[56:59]
s_waitcnt vmcnt(10)
s_barrier
ds_read_u16_d16_hi v34,v34
v_mul_f32_dpp v89,v14,v89 row_newbcast:13 row_mask:0xf bank_mask:0xf
v_mul_f32_dpp v90,v14,v90 row_newbcast:14 row_mask:0xf bank_mask:0xf
v_accvgpr_write_b32 a27,v115
v_mfma_f32_16x16x32_bf16 v[52:55],a[192:195],a[84:87],v[16:19]
ds_read_b64_tr_b16 a[240:241],v3 offset:0
v_mul_f32_dpp v91,v14,v91 row_newbcast:15 row_mask:0xf bank_mask:0xf
v_cvt_pk_bf16_f32 v120,v120,v121
v_lshlrev_b32_sdwa v33,1,v33 dst_sel:DWORD dst_unused:UNUSED_PAD src0_sel:DWORD src1_sel:WORD_1
v_cvt_pk_bf16_f32 v121,v122,v123
v_mfma_f32_16x16x32_bf16 v[52:55],a[216:219],a[68:71],v[52:55]
s_add_u32 m0,s58,18480
ds_read_u16 v33,v33
v_cvt_pk_bf16_f32 v122,v88,v89
v_cvt_pk_bf16_f32 v123,v90,v91
v_lshlrev_b32_sdwa v35,1,v35 dst_sel:DWORD dst_unused:UNUSED_PAD src0_sel:DWORD src1_sel:WORD_1
v_mul_f32_dpp v128,v15,v128 row_newbcast:0 row_mask:0xf bank_mask:0xf
v_mfma_f32_16x16x32_bf16 v[52:55],a[220:223],a[52:55],v[52:55]
s_add_u32 s69,s69,s59
ds_read_u16_d16_hi v35,v35
v_accvgpr_write_b32 a28,v120
v_mul_f32_dpp v129,v15,v129 row_newbcast:1 row_mask:0xf bank_mask:0xf
v_mul_f32_dpp v130,v15,v130 row_newbcast:2 row_mask:0xf bank_mask:0xf
v_mfma_f32_16x16x32_bf16 v[232:235],a[228:231],v[28:31],v[232:235]
buffer_load_dwordx4 v5,s[44:47],s69 offen offset:0 lds
v_accvgpr_write_b32 a29,v121
v_mul_f32_dpp v131,v15,v131 row_newbcast:3 row_mask:0xf bank_mask:0xf
v_lshlrev_b32_sdwa v44,1,v44 dst_sel:DWORD dst_unused:UNUSED_PAD src0_sel:DWORD src1_sel:WORD_1
v_mul_f32_dpp v104,v15,v104 row_newbcast:4 row_mask:0xf bank_mask:0xf
v_mfma_f32_16x16x32_bf16 v[236:239],a[228:231],v[24:27],v[236:239]
s_add_u32 s69,s69,s53
ds_read_u16 v44,v44
v_accvgpr_write_b32 a30,v122
v_mul_f32_dpp v105,v15,v105 row_newbcast:5 row_mask:0xf bank_mask:0xf
v_lshlrev_b32_sdwa v46,1,v46 dst_sel:DWORD dst_unused:UNUSED_PAD src0_sel:DWORD src1_sel:WORD_1
v_mul_f32_dpp v106,v15,v106 row_newbcast:6 row_mask:0xf bank_mask:0xf
v_mfma_f32_16x16x32_bf16 v[164:167],a[232:235],v[24:27],v[164:167]
buffer_load_dwordx4 v5,s[44:47],s69 offen offset:1024 lds
ds_read_u16_d16_hi v46,v46
v_accvgpr_write_b32 a31,v123
v_mul_f32_dpp v107,v15,v107 row_newbcast:7 row_mask:0xf bank_mask:0xf
v_cvt_pk_bf16_f32 v128,v128,v129
v_lshlrev_b32_sdwa v45,1,v45 dst_sel:DWORD dst_unused:UNUSED_PAD src0_sel:DWORD src1_sel:WORD_1
v_mfma_f32_16x16x32_bf16 v[160:163],a[232:235],v[28:31],v[160:163]
s_nop 0
buffer_store_dwordx4 v[140:143],v7,s[40:43],s8 offen offset:192 sc1
ds_read_u16 v45,v45
v_cvt_pk_bf16_f32 v129,v130,v131
v_cvt_pk_bf16_f32 v130,v104,v105
v_cvt_pk_bf16_f32 v131,v106,v107
v_mfma_f32_16x16x32_bf16 v[240:243],a[236:239],v[28:31],v[240:243]
s_add_u32 s69,s69,s53
ds_read_b64_tr_b16 a[242:243],v3 offset:1536
v_mul_f32_dpp v116,v14,v116 row_newbcast:0 row_mask:0xf bank_mask:0xf
v_accvgpr_write_b32 a32,v128
v_lshlrev_b32_sdwa v47,1,v47 dst_sel:DWORD dst_unused:UNUSED_PAD src0_sel:DWORD src1_sel:WORD_1
v_mul_f32_dpp v117,v14,v117 row_newbcast:1 row_mask:0xf bank_mask:0xf
v_mfma_f32_16x16x32_bf16 v[244:247],a[236:239],v[24:27],v[244:247]
buffer_load_dwordx4 v5,s[44:47],s69 offen offset:2048 lds
v_mul_f32_dpp v118,v14,v118 row_newbcast:2 row_mask:0xf bank_mask:0xf
v_accvgpr_write_b32 a33,v129
v_lshlrev_b32_sdwa v48,1,v48 dst_sel:DWORD dst_unused:UNUSED_PAD src0_sel:DWORD src1_sel:WORD_1
v_mul_f32_dpp v119,v14,v119 row_newbcast:3 row_mask:0xf bank_mask:0xf
v_mfma_f32_16x16x32_bf16 v[180:183],a[244:247],v[24:27],v[180:183]
ds_read_u16_d16_hi v47,v47
v_mul_f32_dpp v76,v14,v76 row_newbcast:4 row_mask:0xf bank_mask:0xf
v_accvgpr_write_b32 a34,v130
v_mul_f32_dpp v77,v14,v77 row_newbcast:5 row_mask:0xf bank_mask:0xf
v_mfma_f32_16x16x32_bf16 v[176:179],a[244:247],v[28:31],v[176:179]
ds_read_u16 v48,v48
v_mul_f32_dpp v78,v14,v78 row_newbcast:6 row_mask:0xf bank_mask:0xf
v_accvgpr_write_b32 a35,v131
v_lshlrev_b32_sdwa v50,1,v50 dst_sel:DWORD dst_unused:UNUSED_PAD src0_sel:DWORD src1_sel:WORD_1
v_mul_f32_dpp v79,v14,v79 row_newbcast:7 row_mask:0xf bank_mask:0xf
v_mfma_f32_16x16x32_bf16 v[248:251],a[248:251],v[28:31],v[248:251]
ds_read_u16_d16_hi v50,v50
v_cvt_pk_bf16_f32 v116,v116,v117
v_cvt_pk_bf16_f32 v117,v118,v119
v_lshlrev_b32_sdwa v49,1,v49 dst_sel:DWORD dst_unused:UNUSED_PAD src0_sel:DWORD src1_sel:WORD_1
v_cvt_pk_bf16_f32 v118,v76,v77
v_mfma_f32_16x16x32_bf16 v[252:255],a[248:251],v[24:27],v[252:255]
ds_read_u16 v49,v49
v_cvt_pk_bf16_f32 v119,v78,v79
v_mfma_f32_32x32x16_bf16 v[64:79],a[16:19],a[16:19],0
v_mul_f32_dpp v124,v14,v124 row_newbcast:8 row_mask:0xf bank_mask:0xf
v_lshlrev_b32_sdwa v51,1,v51 dst_sel:DWORD dst_unused:UNUSED_PAD src0_sel:DWORD src1_sel:WORD_1
v_accvgpr_write_b32 a36,v116
v_mul_f32_dpp v125,v14,v125 row_newbcast:9 row_mask:0xf bank_mask:0xf
v_mul_f32_dpp v126,v14,v126 row_newbcast:10 row_mask:0xf bank_mask:0xf
v_mfma_f32_16x16x32_bf16 v[196:199],a[252:255],v[24:27],v[196:199]
ds_read_u16_d16_hi v51,v51
v_accvgpr_write_b32 a37,v117
v_mfma_f32_16x16x32_bf16 v[192:195],a[252:255],v[28:31],v[192:195]
s_nop 0
ds_read_b64_tr_b16 a[224:225],v3 offset:8
v_mul_f32_dpp v127,v14,v127 row_newbcast:11 row_mask:0xf bank_mask:0xf
v_mul_f32_dpp v92,v14,v92 row_newbcast:12 row_mask:0xf bank_mask:0xf
ds_read_b64_tr_b16 a[226:227],v3 offset:1544
s_mov_b64 exec,0
s_cmp_eq_u32 s52,0
s_cbranch_scc1 TE10_%=
s_cmp_eq_u32 s52,1
s_cbranch_scc1 TE11_%=
s_cmp_eq_u32 s52,2
s_cbranch_scc1 TE12_%=
s_branch TE13_%=
TR1_%=:
s_mov_b64 exec,-1
v_add_u32_sdwa v28,s60,v10 dst_sel:DWORD dst_unused:UNUSED_PAD src0_sel:DWORD src1_sel:WORD_1
v_subrev_u32 v8,88400,v28
v_min_u32_e64 v28,v28,v8
v_add_u32 v29,7072,v28
v_subrev_u32 v8,88400,v29
v_min_u32_e64 v29,v29,v8
s_waitcnt lgkmcnt(15)
v_lshl_add_u64 v[60:61],v[60:61],0,v[62:63]
s_waitcnt lgkmcnt(11)
v_lshl_add_u64 v[62:63],v[32:33],0,v[34:35]
s_waitcnt lgkmcnt(6)
v_lshl_add_u64 v[32:33],v[44:45],0,v[46:47]
s_waitcnt lgkmcnt(2)
v_lshl_add_u64 v[34:35],v[48:49],0,v[50:51]
v_mfma_f32_16x16x32_bf16 v[208:211],a[228:231],v[60:63],v[208:211]
v_xad_u32 v24,v11,0,v28
v_xad_u32 v25,v11,8,v29
v_xad_u32 v26,v11,16,v28
v_xad_u32 v27,v11,24,v29
v_mfma_f32_16x16x32_bf16 v[212:215],a[228:231],v[32:35],v[212:215]
ds_read_b64 a[0:1],v24 offset:0
v_accvgpr_write_b32 a38,v118
v_mul_f32_dpp v93,v14,v93 row_newbcast:13 row_mask:0xf bank_mask:0xf
v_mul_f32_dpp v94,v14,v94 row_newbcast:14 row_mask:0xf bank_mask:0xf
v_accvgpr_write_b32 a39,v119
v_mfma_f32_16x16x32_bf16 v[172:175],a[232:235],v[32:35],v[172:175]
buffer_store_dwordx4 v[148:151],v7,s[40:43],s8 offen offset:256 sc1
ds_read_b64 a[2:3],v24 offset:192
v_mul_f32_dpp v95,v14,v95 row_newbcast:15 row_mask:0xf bank_mask:0xf
v_cvt_pk_bf16_f32 v124,v124,v125
v_cvt_pk_bf16_f32 v125,v126,v127
v_mfma_f32_16x16x32_bf16 v[168:171],a[232:235],v[60:63],v[168:171]
ds_read_b64 a[4:5],v25 offset:0
v_cvt_pk_bf16_f32 v126,v92,v93
v_cvt_pk_bf16_f32 v127,v94,v95
v_mfma_f32_32x32x16_bf16 v[80:95],a[16:19],a[16:19],0
v_mfma_f32_16x16x32_bf16 v[216:219],a[236:239],v[60:63],v[216:219]
ds_read_b64 a[6:7],v25 offset:192
v_mul_f32_dpp v132,v15,v132 row_newbcast:0 row_mask:0xf bank_mask:0xf
v_accvgpr_write_b32 a40,v124
v_mul_f32_dpp v133,v15,v133 row_newbcast:1 row_mask:0xf bank_mask:0xf
v_mfma_f32_16x16x32_bf16 v[220:223],a[236:239],v[32:35],v[220:223]
ds_read_b64 a[8:9],v24 offset:8
v_mul_f32_dpp v134,v15,v134 row_newbcast:2 row_mask:0xf bank_mask:0xf
v_accvgpr_write_b32 a41,v125
v_mul_f32_dpp v135,v15,v135 row_newbcast:3 row_mask:0xf bank_mask:0xf
v_mfma_f32_16x16x32_bf16 v[188:191],a[244:247],v[32:35],v[188:191]
ds_read_b64 a[10:11],v24 offset:200
v_mul_f32_dpp v108,v15,v108 row_newbcast:4 row_mask:0xf bank_mask:0xf
v_accvgpr_write_b32 a42,v126
v_mul_f32_dpp v109,v15,v109 row_newbcast:5 row_mask:0xf bank_mask:0xf
v_mfma_f32_16x16x32_bf16 v[184:187],a[244:247],v[60:63],v[184:187]
ds_read_b64 a[12:13],v25 offset:8
v_mul_f32_dpp v110,v15,v110 row_newbcast:6 row_mask:0xf bank_mask:0xf
v_accvgpr_write_b32 a43,v127
v_mul_f32_dpp v111,v15,v111 row_newbcast:7 row_mask:0xf bank_mask:0xf
v_mfma_f32_16x16x32_bf16 v[224:227],a[248:251],v[60:63],v[224:227]
ds_read_b64 a[14:15],v25 offset:200
v_cvt_pk_bf16_f32 v132,v132,v133
v_cvt_pk_bf16_f32 v133,v134,v135
v_cvt_pk_bf16_f32 v134,v108,v109
v_mfma_f32_16x16x32_bf16 v[228:231],a[248:251],v[32:35],v[228:231]
v_cvt_pk_bf16_f32 v135,v110,v111
v_mfma_f32_32x32x16_bf16 v[96:111],a[16:19],a[16:19],0
v_accvgpr_write_b32 a44,v132
v_mfma_f32_16x16x32_bf16 v[204:207],a[252:255],v[32:35],v[204:207]
v_accvgpr_write_b32 a45,v133
v_accvgpr_write_b32 a46,v134
v_accvgpr_write_b32 a47,v135
v_mfma_f32_16x16x32_bf16 v[200:203],a[252:255],v[60:63],v[200:203]
v_mfma_f32_32x32x16_bf16 v[112:127],a[16:19],a[16:19],0
v_mov_b64 v[44:45],0
v_mov_b64 v[46:47],0
ds_read_b64 a[16:17],v26 offset:0
ds_read_b64 a[18:19],v26 offset:192
ds_read_b64 a[20:21],v27 offset:0
ds_read_b64 a[22:23],v27 offset:192
s_waitcnt lgkmcnt(8)
v_smfmac_f32_16x16x64_bf16 v[64:67],a[96:99],a[0:7],v10
s_waitcnt lgkmcnt(4)
ds_read_b64 a[0:1],v26 offset:8
ds_read_b64 a[2:3],v26 offset:200
ds_read_b64 a[4:5],v27 offset:8
ds_read_b64 a[6:7],v27 offset:200
v_smfmac_f32_16x16x64_bf16 v[68:71],a[100:103],a[8:15],v10
s_waitcnt lgkmcnt(4)
ds_read_b64 a[8:9],v24 offset:64
ds_read_b64 a[10:11],v24 offset:256
ds_read_b64 a[12:13],v25 offset:64
ds_read_b64 a[14:15],v25 offset:256
v_smfmac_f32_16x16x64_bf16 v[72:75],a[104:107],a[16:23],v10
s_waitcnt lgkmcnt(4)
buffer_store_dwordx4 v[156:159],v7,s[40:43],s8 offen offset:320 sc1
ds_read_b64 a[16:17],v24 offset:72
ds_read_b64 a[18:19],v24 offset:264
ds_read_b64 a[20:21],v25 offset:72
ds_read_b64 a[22:23],v25 offset:264
v_and_b32 v60,8,v11
v_pk_mul_f32 v[32:33],v[64:65],v[64:65]
v_pk_mul_f32 v[34:35],v[66:67],v[66:67]
v_smfmac_f32_16x16x64_bf16 v[76:79],a[108:111],a[0:7],v10
s_waitcnt lgkmcnt(4)
ds_read_b64 a[0:1],v26 offset:64
ds_read_b64 a[2:3],v26 offset:256
ds_read_b64 a[4:5],v27 offset:64
ds_read_b64 a[6:7],v27 offset:256
v_mul_u32_u24 v60,884,v60
s_add_u32 s64,s60,3536
v_pk_add_f32 v[48:49],v[64:65],v[68:69]
v_pk_fma_f32 v[32:33],v[68:69],v[68:69],v[32:33]
v_pk_add_f32 v[50:51],v[66:67],v[70:71]
v_pk_fma_f32 v[34:35],v[70:71],v[70:71],v[34:35]
v_smfmac_f32_16x16x64_bf16 v[80:83],a[128:131],a[8:15],v10
s_waitcnt lgkmcnt(4)
ds_read_b64 a[8:9],v26 offset:72
ds_read_b64 a[10:11],v26 offset:264
ds_read_b64 a[12:13],v27 offset:72
ds_read_b64 a[14:15],v27 offset:264
v_add_u32 v63,s64,v60
v_pk_add_f32 v[48:49],v[48:49],v[72:73]
v_pk_fma_f32 v[32:33],v[72:73],v[72:73],v[32:33]
v_pk_add_f32 v[50:51],v[50:51],v[74:75]
v_pk_fma_f32 v[34:35],v[74:75],v[74:75],v[34:35]
v_smfmac_f32_16x16x64_bf16 v[84:87],a[132:135],a[16:23],v10
s_waitcnt lgkmcnt(4)
ds_read_b64 a[16:17],v24 offset:128
ds_read_b64 a[18:19],v24 offset:320
ds_read_b64 a[20:21],v25 offset:128
ds_read_b64 a[22:23],v25 offset:320
v_subrev_u32 v8,88400,v63
v_pk_add_f32 v[48:49],v[48:49],v[76:77]
v_pk_fma_f32 v[32:33],v[76:77],v[76:77],v[32:33]
v_pk_add_f32 v[50:51],v[50:51],v[78:79]
v_pk_fma_f32 v[34:35],v[78:79],v[78:79],v[34:35]
v_smfmac_f32_16x16x64_bf16 v[88:91],a[136:139],a[0:7],v10
s_waitcnt lgkmcnt(4)
ds_read_b64 a[0:1],v24 offset:136
ds_read_b64 a[2:3],v24 offset:328
ds_read_b64 a[4:5],v25 offset:136
ds_read_b64 a[6:7],v25 offset:328
v_min_u32 v63,v63,v8
v_pk_add_f32 v[48:49],v[48:49],v[80:81]
v_pk_fma_f32 v[32:33],v[80:81],v[80:81],v[32:33]
v_pk_add_f32 v[50:51],v[50:51],v[82:83]
v_pk_fma_f32 v[34:35],v[82:83],v[82:83],v[34:35]
v_smfmac_f32_16x16x64_bf16 v[92:95],a[140:143],a[8:15],v10
s_waitcnt lgkmcnt(4)
ds_read_b64 a[8:9],v26 offset:128
ds_read_b64 a[10:11],v26 offset:320
ds_read_b64 a[12:13],v27 offset:128
ds_read_b64 a[14:15],v27 offset:320
v_xad_u32 v24,v11,32,v28
v_xad_u32 v25,v11,40,v29
v_pk_add_f32 v[48:49],v[48:49],v[84:85]
v_pk_fma_f32 v[32:33],v[84:85],v[84:85],v[32:33]
v_pk_add_f32 v[50:51],v[50:51],v[86:87]
v_pk_fma_f32 v[34:35],v[86:87],v[86:87],v[34:35]
v_smfmac_f32_16x16x64_bf16 v[96:99],a[160:163],a[16:23],v10
s_waitcnt lgkmcnt(4)
ds_read_b64 a[16:17],v26 offset:136
ds_read_b64 a[18:19],v26 offset:328
ds_read_b64 a[20:21],v27 offset:136
ds_read_b64 a[22:23],v27 offset:328
v_add_u32 v62,3536,v63
v_pk_add_f32 v[48:49],v[48:49],v[88:89]
v_pk_fma_f32 v[32:33],v[88:89],v[88:89],v[32:33]
v_pk_add_f32 v[50:51],v[50:51],v[90:91]
v_pk_fma_f32 v[34:35],v[90:91],v[90:91],v[34:35]
v_smfmac_f32_16x16x64_bf16 v[100:103],a[164:167],a[0:7],v10
s_waitcnt lgkmcnt(4)
buffer_store_dwordx4 a[24:27],v7,s[40:43],s9 offen offset:0 sc1
ds_read_b64 a[0:1],v24 offset:0
ds_read_b64 a[2:3],v24 offset:192
ds_read_b64 a[4:5],v25 offset:0
ds_read_b64 a[6:7],v25 offset:192
v_xad_u32 v26,v11,48,v28
v_xad_u32 v27,v11,56,v29
v_pk_add_f32 v[48:49],v[48:49],v[92:93]
v_pk_fma_f32 v[32:33],v[92:93],v[92:93],v[32:33]
v_pk_add_f32 v[50:51],v[50:51],v[94:95]
v_pk_fma_f32 v[34:35],v[94:95],v[94:95],v[34:35]
v_smfmac_f32_16x16x64_bf16 v[104:107],a[168:171],a[8:15],v10
s_waitcnt lgkmcnt(4)
ds_read_b64 a[8:9],v24 offset:8
ds_read_b64 a[10:11],v24 offset:200
ds_read_b64 a[12:13],v25 offset:8
ds_read_b64 a[14:15],v25 offset:200
v_subrev_u32 v8,88400,v62
v_pk_add_f32 v[48:49],v[48:49],v[96:97]
v_pk_fma_f32 v[32:33],v[96:97],v[96:97],v[32:33]
v_pk_add_f32 v[50:51],v[50:51],v[98:99]
v_pk_fma_f32 v[34:35],v[98:99],v[98:99],v[34:35]
v_smfmac_f32_16x16x64_bf16 v[108:111],a[172:175],a[16:23],v10
s_waitcnt lgkmcnt(4)
ds_read_b64 a[16:17],v24 offset:64
ds_read_b64 a[18:19],v24 offset:256
ds_read_b64 a[20:21],v25 offset:64
ds_read_b64 a[22:23],v25 offset:256
v_min_u32 v62,v62,v8
v_pk_add_f32 v[48:49],v[48:49],v[100:101]
v_pk_fma_f32 v[32:33],v[100:101],v[100:101],v[32:33]
v_pk_add_f32 v[50:51],v[50:51],v[102:103]
v_pk_fma_f32 v[34:35],v[102:103],v[102:103],v[34:35]
v_smfmac_f32_16x16x64_bf16 v[112:115],a[112:115],a[0:7],v10
s_waitcnt lgkmcnt(4)
ds_read_b64 a[0:1],v24 offset:72
ds_read_b64 a[2:3],v24 offset:264
ds_read_b64 a[4:5],v25 offset:72
ds_read_b64 a[6:7],v25 offset:264
v_xor_b32 v9,0x80008,v12
v_pk_add_f32 v[48:49],v[48:49],v[104:105]
v_pk_fma_f32 v[32:33],v[104:105],v[104:105],v[32:33]
v_pk_add_f32 v[50:51],v[50:51],v[106:107]
v_pk_fma_f32 v[34:35],v[106:107],v[106:107],v[34:35]
v_mfma_f32_32x32x16_bf16 v[128:143],v[44:47],v[44:47],0
v_smfmac_f32_16x16x64_bf16 v[116:119],a[116:119],a[8:15],v10
s_waitcnt lgkmcnt(4)
ds_read_b64 a[8:9],v24 offset:128
ds_read_b64 a[10:11],v24 offset:320
ds_read_b64 a[12:13],v25 offset:128
ds_read_b64 a[14:15],v25 offset:320
v_add_u32_sdwa v60,v12,v63 dst_sel:DWORD dst_unused:UNUSED_PAD src0_sel:WORD_0 src1_sel:DWORD
v_pk_add_f32 v[48:49],v[48:49],v[108:109]
v_pk_fma_f32 v[32:33],v[108:109],v[108:109],v[32:33]
v_pk_add_f32 v[50:51],v[50:51],v[110:111]
v_pk_fma_f32 v[34:35],v[110:111],v[110:111],v[34:35]
v_smfmac_f32_16x16x64_bf16 v[120:123],a[144:147],a[16:23],v10
s_waitcnt lgkmcnt(4)
ds_read_b64 a[16:17],v24 offset:136
ds_read_b64 a[18:19],v24 offset:328
ds_read_b64 a[20:21],v25 offset:136
ds_read_b64 a[22:23],v25 offset:328
v_add_u32_sdwa v61,v12,v63 dst_sel:DWORD dst_unused:UNUSED_PAD src0_sel:WORD_1 src1_sel:DWORD
v_pk_add_f32 v[48:49],v[48:49],v[112:113]
v_pk_fma_f32 v[32:33],v[112:113],v[112:113],v[32:33]
v_pk_add_f32 v[50:51],v[50:51],v[114:115]
v_pk_fma_f32 v[34:35],v[114:115],v[114:115],v[34:35]
v_smfmac_f32_16x16x64_bf16 v[124:127],a[148:151],a[0:7],v10
s_waitcnt lgkmcnt(4)
buffer_store_dwordx4 a[28:31],v7,s[40:43],s9 offen offset:64 sc1
ds_read_b64 a[0:1],v26 offset:0
ds_read_b64 a[2:3],v26 offset:192
ds_read_b64 a[4:5],v27 offset:0
ds_read_b64 a[6:7],v27 offset:192
v_add_u32_sdwa v63,v9,v62 dst_sel:DWORD dst_unused:UNUSED_PAD src0_sel:WORD_1 src1_sel:DWORD
v_pk_add_f32 v[48:49],v[48:49],v[116:117]
v_pk_fma_f32 v[32:33],v[116:117],v[116:117],v[32:33]
v_pk_add_f32 v[50:51],v[50:51],v[118:119]
v_pk_fma_f32 v[34:35],v[118:119],v[118:119],v[34:35]
v_smfmac_f32_16x16x64_bf16 v[128:131],a[176:179],a[8:15],v10
s_waitcnt lgkmcnt(4)
ds_read_b64 a[8:9],v26 offset:8
ds_read_b64 a[10:11],v26 offset:200
ds_read_b64 a[12:13],v27 offset:8
ds_read_b64 a[14:15],v27 offset:200
v_add_u32_sdwa v62,v9,v62 dst_sel:DWORD dst_unused:UNUSED_PAD src0_sel:WORD_0 src1_sel:DWORD
v_pk_add_f32 v[48:49],v[48:49],v[120:121]
v_pk_fma_f32 v[32:33],v[120:121],v[120:121],v[32:33]
v_pk_add_f32 v[50:51],v[50:51],v[122:123]
v_pk_fma_f32 v[34:35],v[122:123],v[122:123],v[34:35]
v_mfma_f32_32x32x16_bf16 v[144:159],v[44:47],v[44:47],0
v_smfmac_f32_16x16x64_bf16 v[132:135],a[180:183],a[16:23],v10
s_waitcnt lgkmcnt(4)
ds_read_b64 a[16:17],v26 offset:64
ds_read_b64 a[18:19],v26 offset:256
ds_read_b64 a[20:21],v27 offset:64
ds_read_b64 a[22:23],v27 offset:256
v_pk_add_f32 v[48:49],v[48:49],v[124:125]
v_pk_fma_f32 v[32:33],v[124:125],v[124:125],v[32:33]
v_pk_add_f32 v[50:51],v[50:51],v[126:127]
v_pk_fma_f32 v[34:35],v[126:127],v[126:127],v[34:35]
v_smfmac_f32_16x16x64_bf16 v[136:139],a[120:123],a[0:7],v10
s_waitcnt lgkmcnt(4)
ds_read_b64 a[0:1],v26 offset:72
ds_read_b64 a[2:3],v26 offset:264
ds_read_b64 a[4:5],v27 offset:72
ds_read_b64 a[6:7],v27 offset:264
v_pk_add_f32 v[48:49],v[48:49],v[128:129]
v_pk_fma_f32 v[32:33],v[128:129],v[128:129],v[32:33]
v_pk_add_f32 v[50:51],v[50:51],v[130:131]
v_pk_fma_f32 v[34:35],v[130:131],v[130:131],v[34:35]
v_smfmac_f32_16x16x64_bf16 v[140:143],a[124:127],a[8:15],v10
s_waitcnt lgkmcnt(4)
ds_read_b64 a[8:9],v26 offset:128
ds_read_b64 a[10:11],v26 offset:320
ds_read_b64 a[12:13],v27 offset:128
ds_read_b64 a[14:15],v27 offset:320
v_pk_add_f32 v[48:49],v[48:49],v[132:133]
v_pk_fma_f32 v[32:33],v[132:133],v[132:133],v[32:33]
v_pk_add_f32 v[50:51],v[50:51],v[134:135]
v_pk_fma_f32 v[34:35],v[134:135],v[134:135],v[34:35]
v_smfmac_f32_16x16x64_bf16 v[144:147],a[152:155],a[16:23],v10
s_waitcnt lgkmcnt(4)
ds_read_b64 a[16:17],v26 offset:136
ds_read_b64 a[18:19],v26 offset:328
ds_read_b64 a[20:21],v27 offset:136
ds_read_b64 a[22:23],v27 offset:328
v_pk_add_f32 v[48:49],v[48:49],v[136:137]
v_pk_fma_f32 v[32:33],v[136:137],v[136:137],v[32:33]
v_pk_add_f32 v[50:51],v[50:51],v[138:139]
v_pk_fma_f32 v[34:35],v[138:139],v[138:139],v[34:35]
v_smfmac_f32_16x16x64_bf16 v[148:151],a[156:159],a[0:7],v10
s_waitcnt lgkmcnt(4)
ds_read_b64 a[228:229],v60 offset:57344
ds_read_b64 a[230:231],v60 offset:57352
ds_read_b64 a[232:233],v61 offset:57344
ds_read_b64 a[234:235],v61 offset:57352
ds_read_b128 a[248:251],v13 offset:0
v_pk_add_f32 v[48:49],v[48:49],v[140:141]
v_pk_fma_f32 v[32:33],v[140:141],v[140:141],v[32:33]
v_pk_add_f32 v[50:51],v[50:51],v[142:143]
v_pk_fma_f32 v[34:35],v[142:143],v[142:143],v[34:35]
v_smfmac_f32_16x16x64_bf16 v[152:155],a[184:187],a[8:15],v10
s_waitcnt lgkmcnt(5)
buffer_store_dwordx4 a[32:35],v7,s[40:43],s9 offen offset:128 sc1
ds_read_b64 a[236:237],v62 offset:57344
ds_read_b64 a[238:239],v62 offset:57352
ds_read_b64 a[244:245],v63 offset:57344
ds_read_b64 a[246:247],v63 offset:57352
ds_read_b128 a[252:255],v13 offset:256
v_pk_add_f32 v[48:49],v[48:49],v[144:145]
TP5414_%=:
s_cbranch_execz TR0_%=
s_nop 0
v_pk_fma_f32 v[32:33],v[144:145],v[144:145],v[32:33]
v_pk_add_f32 v[50:51],v[50:51],v[146:147]
v_pk_fma_f32 v[34:35],v[146:147],v[146:147],v[34:35]
v_smfmac_f32_16x16x64_bf16 v[156:159],a[188:191],a[16:23],v10
ds_read_b128 v[24:27],v4 offset:3584
ds_read_b128 v[28:31],v4 offset:3616
v_pk_add_f32 v[48:49],v[48:49],v[148:149]
v_pk_fma_f32 v[32:33],v[148:149],v[148:149],v[32:33]
v_pk_add_f32 v[50:51],v[50:51],v[150:151]
v_pk_fma_f32 v[34:35],v[150:151],v[150:151],v[34:35]
v_pk_add_f32 v[48:49],v[48:49],v[152:153]
v_pk_fma_f32 v[32:33],v[152:153],v[152:153],v[32:33]
v_pk_add_f32 v[50:51],v[50:51],v[154:155]
v_pk_fma_f32 v[34:35],v[154:155],v[154:155],v[34:35]
v_pk_add_f32 v[48:49],v[48:49],v[156:157]
v_pk_fma_f32 v[32:33],v[156:157],v[156:157],v[32:33]
v_pk_add_f32 v[50:51],v[50:51],v[158:159]
v_pk_fma_f32 v[34:35],v[158:159],v[158:159],v[34:35]
s_nop 0
v_permlane32_swap_b32 v48,v50
v_permlane32_swap_b32 v49,v51
v_permlane32_swap_b32 v32,v34
v_permlane32_swap_b32_e64 v33,v35
v_pk_add_f32 v[48:49],v[48:49],v[50:51]
v_pk_add_f32 v[32:33],v[32:33],v[34:35]
s_add_u32 s60,s60,56576
s_sub_u32 s64,s60,88400
s_min_u32 s60,s60,s64
v_permlane16_swap_b32 v48,v49
v_permlane16_swap_b32 v32,v33
v_add_f32 v8,v48,v49
v_add_f32_e64 v9,v32,v33
v_mul_f32 v8,0x3c2aaaab,v8
v_mul_f32 v9,0x3c2aaaab,v9
v_cmp_ngt_f32 vcc,0x5d800000,v9
v_fma_f32 v9,-v8,v8,v9
v_max_f32 v9,0,v9
s_or_b64 s[90:91],s[90:91],vcc
v_add_f32 v9,0x3727c5ac,v9
v_rsq_f32 v9,v9
s_nop 0
v_mul_f32 v48,s96,v9
v_mul_f32 v49,s96,v9
v_mul_f32 v32,-v8,v48
v_mul_f32 v33,-v8,v48
v_permlane16_swap_b32 v48,v49
s_nop 0
v_permlane16_swap_b32 v32,v33
v_mov_b64 v[50:51],v[48:49]
v_mov_b64 v[34:35],v[32:33]
s_nop 0
v_permlane32_swap_b32 v48,v50
v_permlane32_swap_b32 v49,v51
v_permlane32_swap_b32 v32,v34
v_permlane32_swap_b32 v33,v35
v_pk_fma_f32 v[64:65],v[64:65],v[48:49],v[32:33]
v_pk_fma_f32 v[66:67],v[66:67],v[50:51],v[34:35]
v_pk_fma_f32 v[68:69],v[68:69],v[48:49],v[32:33]
v_pk_fma_f32 v[70:71],v[70:71],v[50:51],v[34:35]
v_pk_fma_f32 v[72:73],v[72:73],v[48:49],v[32:33]
v_pk_fma_f32 v[74:75],v[74:75],v[50:51],v[34:35]
v_pk_fma_f32 v[76:77],v[76:77],v[48:49],v[32:33]
v_pk_fma_f32 v[78:79],v[78:79],v[50:51],v[34:35]
v_pk_fma_f32 v[112:113],v[112:113],v[48:49],v[32:33]
v_pk_fma_f32 v[114:115],v[114:115],v[50:51],v[34:35]
v_pk_fma_f32 v[116:117],v[116:117],v[48:49],v[32:33]
v_pk_fma_f32 v[118:119],v[118:119],v[50:51],v[34:35]
v_pk_fma_f32 v[136:137],v[136:137],v[48:49],v[32:33]
v_pk_fma_f32 v[138:139],v[138:139],v[50:51],v[34:35]
v_pk_fma_f32 v[140:141],v[140:141],v[48:49],v[32:33]
v_pk_fma_f32 v[142:143],v[142:143],v[50:51],v[34:35]
v_pk_fma_f32 v[80:81],v[80:81],v[48:49],v[32:33]
v_pk_fma_f32 v[82:83],v[82:83],v[50:51],v[34:35]
v_pk_fma_f32 v[84:85],v[84:85],v[48:49],v[32:33]
v_pk_fma_f32 v[86:87],v[86:87],v[50:51],v[34:35]
v_pk_fma_f32 v[88:89],v[88:89],v[48:49],v[32:33]
v_pk_fma_f32 v[90:91],v[90:91],v[50:51],v[34:35]
v_pk_fma_f32 v[92:93],v[92:93],v[48:49],v[32:33]
v_pk_fma_f32 v[94:95],v[94:95],v[50:51],v[34:35]
v_pk_fma_f32 v[120:121],v[120:121],v[48:49],v[32:33]
v_pk_fma_f32 v[122:123],v[122:123],v[50:51],v[34:35]
v_pk_fma_f32 v[124:125],v[124:125],v[48:49],v[32:33]
v_pk_fma_f32 v[126:127],v[126:127],v[50:51],v[34:35]
v_pk_fma_f32 v[144:145],v[144:145],v[48:49],v[32:33]
v_pk_fma_f32 v[146:147],v[146:147],v[50:51],v[34:35]
v_pk_fma_f32 v[148:149],v[148:149],v[48:49],v[32:33]
v_pk_fma_f32 v[150:151],v[150:151],v[50:51],v[34:35]
v_pk_fma_f32 v[96:97],v[96:97],v[48:49],v[32:33]
v_pk_fma_f32 v[98:99],v[98:99],v[50:51],v[34:35]
v_pk_fma_f32 v[100:101],v[100:101],v[48:49],v[32:33]
v_pk_fma_f32 v[102:103],v[102:103],v[50:51],v[34:35]
v_pk_fma_f32 v[104:105],v[104:105],v[48:49],v[32:33]
v_pk_fma_f32 v[106:107],v[106:107],v[50:51],v[34:35]
v_pk_fma_f32 v[108:109],v[108:109],v[48:49],v[32:33]
v_pk_fma_f32 v[110:111],v[110:111],v[50:51],v[34:35]
v_pk_fma_f32 v[128:129],v[128:129],v[48:49],v[32:33]
v_pk_fma_f32 v[130:131],v[130:131],v[50:51],v[34:35]
v_pk_fma_f32 v[132:133],v[132:133],v[48:49],v[32:33]
v_pk_fma_f32 v[134:135],v[134:135],v[50:51],v[34:35]
v_pk_fma_f32 v[152:153],v[152:153],v[48:49],v[32:33]
v_pk_fma_f32 v[154:155],v[154:155],v[50:51],v[34:35]
v_pk_fma_f32 v[156:157],v[156:157],v[48:49],v[32:33]
v_pk_fma_f32 v[158:159],v[158:159],v[50:51],v[34:35]
v_mfma_f32_16x16x32_bf16 v[44:47],a[220:223],a[56:59],v[16:19]
buffer_store_dwordx4 a[36:39],v7,s[40:43],s9 offen offset:192 sc1
v_cvt_pk_bf16_f32 v64,v64,v68
v_cvt_pk_bf16_f32 v72,v72,v76
v_cvt_pk_bf16_f32 v112,v112,v116
v_lshlrev_b32_sdwa v36,1,v36 dst_sel:DWORD dst_unused:UNUSED_PAD src0_sel:DWORD src1_sel:WORD_1
v_mfma_f32_16x16x32_bf16 v[44:47],a[216:219],a[72:75],v[44:47]
ds_read_u16 v36,v36
v_accvgpr_write_b32 a0,v64
v_accvgpr_write_b32 a1,v72
v_cvt_pk_bf16_f32 v136,v136,v140
v_accvgpr_write_b32 a2,v112
v_mfma_f32_16x16x32_bf16 v[44:47],a[192:195],a[88:91],v[44:47]
ds_read_b64_tr_b16 a[204:205],v1 offset:0
v_cvt_pk_bf16_f32 v65,v65,v69
v_accvgpr_write_b32 a3,v136
v_cvt_pk_bf16_f32 v73,v73,v77
v_accvgpr_write_b32 a4,v65
v_mfma_f32_16x16x32_bf16 v[48:51],a[196:199],a[88:91],v[20:23]
ds_read_b64_tr_b16 a[206:207],v1 offset:1024
v_cvt_pk_bf16_f32 v113,v113,v117
v_accvgpr_write_b32 a5,v73
v_cvt_pk_bf16_f32 v137,v137,v141
v_lshlrev_b32_sdwa v38,1,v38 dst_sel:DWORD dst_unused:UNUSED_PAD src0_sel:DWORD src1_sel:WORD_1
v_accvgpr_write_b32 a6,v113
v_mfma_f32_16x16x32_bf16 v[48:51],a[200:203],a[72:75],v[48:51]
ds_read_u16_d16_hi v38,v38
v_cvt_pk_bf16_f32 v66,v66,v70
v_accvgpr_write_b32 a7,v137
v_cvt_pk_bf16_f32 v74,v74,v78
v_accvgpr_write_b32 a8,v66
v_mfma_f32_16x16x32_bf16 v[48:51],a[212:215],a[56:59],v[48:51]
ds_read_b64_tr_b16 a[208:209],v1 offset:2048
v_accvgpr_write_b32 a9,v74
v_cvt_pk_bf16_f32 v114,v114,v118
v_accvgpr_write_b32 a10,v114
v_lshlrev_b32_sdwa v37,1,v37 dst_sel:DWORD dst_unused:UNUSED_PAD src0_sel:DWORD src1_sel:WORD_1
v_cvt_pk_bf16_f32 v138,v138,v142
v_mfma_f32_16x16x32_bf16 v[32:35],a[212:215],a[60:63],v[20:23]
buffer_store_dwordx4 a[40:43],v7,s[40:43],s9 offen offset:256 sc1
v_accvgpr_write_b32 a11,v138
v_cvt_pk_bf16_f32 v67,v67,v71
v_accvgpr_write_b32 a12,v67
v_mfma_f32_16x16x32_bf16 v[32:35],a[200:203],a[76:79],v[32:35]
ds_read_u16 v37,v37
v_cvt_pk_bf16_f32 v75,v75,v79
v_accvgpr_write_b32 a13,v75
v_cvt_pk_bf16_f32 v115,v115,v119
v_accvgpr_write_b32 a14,v115
v_lshlrev_b32_sdwa v39,1,v39 dst_sel:DWORD dst_unused:UNUSED_PAD src0_sel:DWORD src1_sel:WORD_1
v_mfma_f32_16x16x32_bf16 v[32:35],a[196:199],a[92:95],v[32:35]
s_waitcnt lgkmcnt(6)
ds_read_u16_d16_hi v39,v39
v_cvt_pk_bf16_f32 v139,v139,v143
v_accvgpr_write_b32 a15,v139
v_cvt_pk_bf16_f32 v80,v80,v84
v_cvt_pk_bf16_f32 v88,v88,v92
v_mfma_f32_16x16x32_bf16 v[136:139],a[248:251],a[228:231],v[24:27]
s_nop 0
ds_read_b64_tr_b16 a[210:211],v1 offset:3072
v_cvt_pk_bf16_f32 v120,v120,v124
v_accvgpr_write_b32 a16,v80
v_accvgpr_write_b32 a17,v88
v_cvt_pk_bf16_f32 v144,v144,v148
v_lshlrev_b32_sdwa v40,1,v40 dst_sel:DWORD dst_unused:UNUSED_PAD src0_sel:DWORD src1_sel:WORD_1
v_mfma_f32_16x16x32_bf16 v[140:143],a[248:251],a[232:235],v[24:27]
ds_read_u16 v40,v40
v_accvgpr_write_b32 a18,v120
v_cvt_pk_bf16_f32 v81,v81,v85
v_accvgpr_write_b32 a19,v144
v_cvt_pk_bf16_f32 v89,v89,v93
v_mfma_f32_16x16x32_bf16 v[112:115],a[248:251],a[236:239],v[24:27]
v_accvgpr_write_b32 a20,v81
v_cvt_pk_bf16_f32 v121,v121,v125
v_accvgpr_write_b32 a21,v89
v_lshlrev_b32_sdwa v42,1,v42 dst_sel:DWORD dst_unused:UNUSED_PAD src0_sel:DWORD src1_sel:WORD_1
v_cvt_pk_bf16_f32 v145,v145,v149
v_mfma_f32_16x16x32_bf16 v[116:119],a[248:251],a[244:247],v[24:27]
ds_read_b128 a[248:251],v13 offset:512
ds_read_b128 v[24:27],v4 offset:3712
v_accvgpr_write_b32 a22,v121
v_cvt_pk_bf16_f32 v82,v82,v86
v_accvgpr_write_b32 a23,v145
v_mfma_f32_16x16x32_bf16 v[64:67],a[252:255],a[228:231],v[28:31]
ds_read_b64 a[228:229],v60 offset:57408
ds_read_b64 a[230:231],v60 offset:57416
v_cvt_pk_bf16_f32 v90,v90,v94
v_accvgpr_write_b32 a24,v82
v_accvgpr_write_b32 a25,v90
v_cvt_pk_bf16_f32 v122,v122,v126
v_lshlrev_b32_sdwa v41,1,v41 dst_sel:DWORD dst_unused:UNUSED_PAD src0_sel:DWORD src1_sel:WORD_1
v_mfma_f32_16x16x32_bf16 v[68:71],a[252:255],a[232:235],v[28:31]
ds_read_b64 a[232:233],v61 offset:57408
ds_read_b64 a[234:235],v61 offset:57416
v_accvgpr_write_b32 a26,v122
v_cvt_pk_bf16_f32 v146,v146,v150
v_accvgpr_write_b32 a27,v146
v_cvt_pk_bf16_f32 v83,v83,v87
v_mfma_f32_16x16x32_bf16 v[72:75],a[252:255],a[236:239],v[28:31]
ds_read_b64 a[236:237],v62 offset:57408
ds_read_b64 a[238:239],v62 offset:57416
v_accvgpr_write_b32 a28,v83
v_cvt_pk_bf16_f32 v91,v91,v95
v_accvgpr_write_b32 a29,v91
v_cvt_pk_bf16_f32 v123,v123,v127
v_lshlrev_b32_sdwa v43,1,v43 dst_sel:DWORD dst_unused:UNUSED_PAD src0_sel:DWORD src1_sel:WORD_1
v_mfma_f32_16x16x32_bf16 v[76:79],a[252:255],a[244:247],v[28:31]
s_waitcnt lgkmcnt(4)
buffer_store_dwordx4 a[44:47],v7,s[40:43],s9 offen offset:320 sc1
ds_read_b64 a[244:245],v63 offset:57408
v_accvgpr_write_b32 a30,v123
v_cvt_pk_bf16_f32 v147,v147,v151
v_accvgpr_write_b32 a31,v147
v_cvt_pk_bf16_f32 v96,v96,v100
v_mfma_f32_16x16x32_bf16 v[144:147],a[248:251],a[228:231],v[24:27]
s_waitcnt lgkmcnt(3)
ds_read_b64 a[246:247],v63 offset:57416
ds_read_b128 a[252:255],v13 offset:768
v_cvt_pk_bf16_f32 v104,v104,v108
v_cvt_pk_bf16_f32 v128,v128,v132
v_accvgpr_write_b32 a32,v96
v_accvgpr_write_b32 a33,v104
v_lshlrev_b32_sdwa v52,1,v52 dst_sel:DWORD dst_unused:UNUSED_PAD src0_sel:DWORD src1_sel:WORD_1
v_mfma_f32_16x16x32_bf16 v[148:151],a[248:251],a[232:235],v[24:27]
s_waitcnt lgkmcnt(3)
ds_read_b128 v[28:31],v4 offset:3744
ds_read_u16_d16_hi v42,v42
v_cvt_pk_bf16_f32 v152,v152,v156
v_accvgpr_write_b32 a34,v128
v_cvt_pk_bf16_f32 v97,v97,v101
v_mfma_f32_16x16x32_bf16 v[120:123],a[248:251],a[236:239],v[24:27]
s_waitcnt lgkmcnt(3)
ds_read_u16 v41,v41
v_accvgpr_write_b32 a35,v152
v_cvt_pk_bf16_f32 v105,v105,v109
v_accvgpr_write_b32 a36,v97
v_cvt_pk_bf16_f32 v129,v129,v133
v_lshlrev_b32_sdwa v54,1,v54 dst_sel:DWORD dst_unused:UNUSED_PAD src0_sel:DWORD src1_sel:WORD_1
v_mfma_f32_16x16x32_bf16 v[124:127],a[248:251],a[244:247],v[24:27]
s_waitcnt lgkmcnt(2)
ds_read_b128 a[248:251],v13 offset:1024
ds_read_b128 v[24:27],v4 offset:3840
v_accvgpr_write_b32 a37,v105
v_cvt_pk_bf16_f32 v153,v153,v157
v_accvgpr_write_b32 a38,v129
v_cvt_pk_bf16_f32 v98,v98,v102
v_mfma_f32_16x16x32_bf16 v[80:83],a[252:255],a[228:231],v[28:31]
s_nop 0
ds_read_b64 a[228:229],v60 offset:57472
ds_read_b64 a[230:231],v60 offset:57480
v_accvgpr_write_b32 a39,v153
v_cvt_pk_bf16_f32 v106,v106,v110
v_accvgpr_write_b32 a40,v98
v_accvgpr_write_b32 a41,v106
v_lshlrev_b32_sdwa v53,1,v53 dst_sel:DWORD dst_unused:UNUSED_PAD src0_sel:DWORD src1_sel:WORD_1
v_mfma_f32_16x16x32_bf16 v[84:87],a[252:255],a[232:235],v[28:31]
ds_read_b64 a[232:233],v61 offset:57472
ds_read_b64 a[234:235],v61 offset:57480
v_cvt_pk_bf16_f32 v130,v130,v134
v_accvgpr_write_b32 a42,v130
v_cvt_pk_bf16_f32 v154,v154,v158
v_accvgpr_write_b32 a43,v154
v_mfma_f32_16x16x32_bf16 v[88:91],a[252:255],a[236:239],v[28:31]
s_nop 0
ds_read_b64 a[236:237],v62 offset:57472
ds_read_b64 a[238:239],v62 offset:57480
v_cvt_pk_bf16_f32 v99,v99,v103
v_accvgpr_write_b32 a44,v99
v_cvt_pk_bf16_f32 v107,v107,v111
v_accvgpr_write_b32 a45,v107
v_mfma_f32_16x16x32_bf16 v[92:95],a[252:255],a[244:247],v[28:31]
s_waitcnt lgkmcnt(4)
ds_read_b64 a[244:245],v63 offset:57472
ds_read_b64 a[246:247],v63 offset:57480
v_cvt_pk_bf16_f32 v131,v131,v135
v_accvgpr_write_b32 a46,v131
v_cvt_pk_bf16_f32 v155,v155,v159
v_lshlrev_b32_sdwa v55,1,v55 dst_sel:DWORD dst_unused:UNUSED_PAD src0_sel:DWORD src1_sel:WORD_1
v_accvgpr_write_b32 a47,v155
v_mfma_f32_16x16x32_bf16 v[152:155],a[248:251],a[228:231],v[24:27]
s_waitcnt lgkmcnt(4)
ds_read_b128 a[252:255],v13 offset:1280
ds_read_b128 v[28:31],v4 offset:3872
v_mfma_f32_16x16x32_bf16 v[156:159],a[248:251],a[232:235],v[24:27]
s_waitcnt lgkmcnt(4)
ds_read_u16_d16_hi v43,v43
ds_read_u16 v52,v52
v_lshlrev_b32_sdwa v56,1,v56 dst_sel:DWORD dst_unused:UNUSED_PAD src0_sel:DWORD src1_sel:WORD_1
v_mfma_f32_16x16x32_bf16 v[128:131],a[248:251],a[236:239],v[24:27]
s_waitcnt lgkmcnt(4)
ds_read_u16_d16_hi v54,v54
v_lshlrev_b32_sdwa v58,1,v58 dst_sel:DWORD dst_unused:UNUSED_PAD src0_sel:DWORD src1_sel:WORD_1
v_mfma_f32_16x16x32_bf16 v[132:135],a[248:251],a[244:247],v[24:27]
s_waitcnt lgkmcnt(3)
ds_read_b64_tr_b16 a[248:249],v3 offset:64
ds_read_b64_tr_b16 a[250:251],v3 offset:1600
v_mfma_f32_16x16x32_bf16 v[96:99],a[252:255],a[228:231],v[28:31]
ds_read_b64_tr_b16 a[228:229],v3 offset:72
ds_read_b64_tr_b16 a[230:231],v3 offset:1608
v_mfma_f32_16x16x32_bf16 v[100:103],a[252:255],a[232:235],v[28:31]
ds_read_b64_tr_b16 a[232:233],v3 offset:128
ds_read_b64_tr_b16 a[234:235],v3 offset:1664
v_lshlrev_b32_sdwa v57,1,v57 dst_sel:DWORD dst_unused:UNUSED_PAD src0_sel:DWORD src1_sel:WORD_1
v_mfma_f32_16x16x32_bf16 v[104:107],a[252:255],a[236:239],v[28:31]
ds_read_b64_tr_b16 a[236:237],v3 offset:136
ds_read_b64_tr_b16 a[238:239],v3 offset:1672
v_mfma_f32_16x16x32_bf16 v[108:111],a[252:255],a[244:247],v[28:31]
ds_read_u16 v53,v53
ds_read_u16_d16_hi v55,v55
v_lshlrev_b32_sdwa v59,1,v59 dst_sel:DWORD dst_unused:UNUSED_PAD src0_sel:DWORD src1_sel:WORD_1
v_mfma_f32_16x16x32_bf16 v[60:63],a[192:195],a[92:95],v[16:19]
ds_read_u16 v56,v56
v_mfma_f32_16x16x32_bf16 v[60:63],a[216:219],a[76:79],v[60:63]
s_waitcnt vmcnt(18)
ds_read_u16_d16_hi v58,v58
v_mfma_f32_16x16x32_bf16 v[60:63],a[220:223],a[60:63],v[60:63]
s_barrier
ds_read_u16 v57,v57
ds_read_u16_d16_hi v59,v59
v_mfma_f32_16x16x32_bf16 v[24:27],a[220:223],a[0:3],v[16:19]
s_add_u32 m0,s57,24752
v_lshlrev_b32_sdwa v44,1,v44 dst_sel:DWORD dst_unused:UNUSED_PAD src0_sel:DWORD src1_sel:WORD_1
v_lshlrev_b32_sdwa v46,1,v46 dst_sel:DWORD dst_unused:UNUSED_PAD src0_sel:DWORD src1_sel:WORD_1
v_mfma_f32_16x16x32_bf16 v[24:27],a[216:219],a[16:19],v[24:27]
s_add_u32 s89,s56,5603328
ds_read_u16 v44,v44
v_lshlrev_b32_sdwa v45,1,v45 dst_sel:DWORD dst_unused:UNUSED_PAD src0_sel:DWORD src1_sel:WORD_1
v_mfma_f32_16x16x32_bf16 v[24:27],a[192:195],a[32:35],v[24:27]
buffer_load_dwordx4 v6,s[36:39],s89 offen lds
v_lshl_add_u64 v[36:37],v[36:37],0,v[38:39]
v_mfma_f32_16x16x32_bf16 v[28:31],a[196:199],a[32:35],v[20:23]
s_waitcnt lgkmcnt(9)
ds_read_u16_d16_hi v46,v46
v_lshl_add_u64 v[38:39],v[40:41],0,v[42:43]
v_mfma_f32_16x16x32_bf16 v[28:31],a[200:203],a[16:19],v[28:31]
s_waitcnt lgkmcnt(6)
ds_read_u16 v45,v45
v_lshl_add_u64 v[40:41],v[52:53],0,v[54:55]
v_mfma_f32_16x16x32_bf16 v[28:31],a[212:215],a[0:3],v[28:31]
v_lshlrev_b32_sdwa v47,1,v47 dst_sel:DWORD dst_unused:UNUSED_PAD src0_sel:DWORD src1_sel:WORD_1
v_lshlrev_b32_sdwa v48,1,v48 dst_sel:DWORD dst_unused:UNUSED_PAD src0_sel:DWORD src1_sel:WORD_1
v_mfma_f32_16x16x32_bf16 v[52:55],a[212:215],a[4:7],v[20:23]
s_nop 0
ds_read_u16_d16_hi v47,v47
v_lshlrev_b32_sdwa v50,1,v50 dst_sel:DWORD dst_unused:UNUSED_PAD src0_sel:DWORD src1_sel:WORD_1
v_mfma_f32_16x16x32_bf16 v[52:55],a[200:203],a[20:23],v[52:55]
s_waitcnt lgkmcnt(4)
ds_read_u16 v48,v48
v_lshl_add_u64 v[42:43],v[56:57],0,v[58:59]
v_mfma_f32_16x16x32_bf16 v[52:55],a[196:199],a[36:39],v[52:55]
ds_read_u16_d16_hi v50,v50
v_lshlrev_b32_sdwa v49,1,v49 dst_sel:DWORD dst_unused:UNUSED_PAD src0_sel:DWORD src1_sel:WORD_1
v_mfma_f32_16x16x32_bf16 v[56:59],a[192:195],a[36:39],v[16:19]
ds_read_u16 v49,v49
v_lshlrev_b32_sdwa v51,1,v51 dst_sel:DWORD dst_unused:UNUSED_PAD src0_sel:DWORD src1_sel:WORD_1
v_mfma_f32_16x16x32_bf16 v[56:59],a[216:219],a[20:23],v[56:59]
ds_read_u16_d16_hi v51,v51
v_lshlrev_b32_sdwa v60,1,v60 dst_sel:DWORD dst_unused:UNUSED_PAD src0_sel:DWORD src1_sel:WORD_1
v_mfma_f32_16x16x32_bf16 v[56:59],a[220:223],a[4:7],v[56:59]
ds_read_u16 v60,v60
v_lshlrev_b32_sdwa v62,1,v62 dst_sel:DWORD dst_unused:UNUSED_PAD src0_sel:DWORD src1_sel:WORD_1
v_mfma_f32_16x16x32_bf16 v[232:235],a[240:243],v[36:39],v[232:235]
ds_read_u16_d16_hi v62,v62
v_lshlrev_b32_sdwa v61,1,v61 dst_sel:DWORD dst_unused:UNUSED_PAD src0_sel:DWORD src1_sel:WORD_1
v_mfma_f32_16x16x32_bf16 v[236:239],a[240:243],v[40:43],v[236:239]
ds_read_u16 v61,v61
v_lshlrev_b32_sdwa v63,1,v63 dst_sel:DWORD dst_unused:UNUSED_PAD src0_sel:DWORD src1_sel:WORD_1
v_mfma_f32_16x16x32_bf16 v[164:167],a[224:227],v[40:43],v[164:167]
ds_read_u16_d16_hi v63,v63
v_lshlrev_b32_sdwa v32,1,v32 dst_sel:DWORD dst_unused:UNUSED_PAD src0_sel:DWORD src1_sel:WORD_1
v_mfma_f32_16x16x32_bf16 v[160:163],a[224:227],v[36:39],v[160:163]
ds_read_u16 v32,v32
v_lshlrev_b32_sdwa v34,1,v34 dst_sel:DWORD dst_unused:UNUSED_PAD src0_sel:DWORD src1_sel:WORD_1
v_mfma_f32_16x16x32_bf16 v[240:243],a[248:251],v[36:39],v[240:243]
ds_read_u16_d16_hi v34,v34
v_lshlrev_b32_sdwa v33,1,v33 dst_sel:DWORD dst_unused:UNUSED_PAD src0_sel:DWORD src1_sel:WORD_1
v_mfma_f32_16x16x32_bf16 v[244:247],a[248:251],v[40:43],v[244:247]
ds_read_u16 v33,v33
v_lshlrev_b32_sdwa v35,1,v35 dst_sel:DWORD dst_unused:UNUSED_PAD src0_sel:DWORD src1_sel:WORD_1
v_mfma_f32_16x16x32_bf16 v[180:183],a[228:231],v[40:43],v[180:183]
ds_read_u16_d16_hi v35,v35
v_mfma_f32_16x16x32_bf16 v[176:179],a[228:231],v[36:39],v[176:179]
v_mfma_f32_16x16x32_bf16 v[248:251],a[232:235],v[36:39],v[248:251]
v_mfma_f32_16x16x32_bf16 v[252:255],a[232:235],v[40:43],v[252:255]
v_mfma_f32_16x16x32_bf16 v[196:199],a[236:239],v[40:43],v[196:199]
v_mfma_f32_16x16x32_bf16 v[192:195],a[236:239],v[36:39],v[192:195]
v_mfma_f32_16x16x32_bf16 v[40:43],a[220:223],a[8:11],v[16:19]
v_lshlrev_b32_sdwa v24,1,v24 dst_sel:DWORD dst_unused:UNUSED_PAD src0_sel:DWORD src1_sel:WORD_1
v_fma_mixlo_f16 v8,v53,s99,0
v_mfma_f32_16x16x32_bf16 v[40:43],a[216:219],a[24:27],v[40:43]
ds_read_u16 v24,v24
v_lshlrev_b32_sdwa v26,1,v26 dst_sel:DWORD dst_unused:UNUSED_PAD src0_sel:DWORD src1_sel:WORD_1
v_mfma_f32_16x16x32_bf16 v[40:43],a[192:195],a[40:43],v[40:43]
s_waitcnt lgkmcnt(2)
ds_read_u16_d16_hi v26,v26
v_lshl_add_u64 v[44:45],v[44:45],0,v[46:47]
v_mfma_f32_16x16x32_bf16 v[36:39],a[196:199],a[40:43],v[20:23]
v_lshl_add_u64 v[46:47],v[48:49],0,v[50:51]
v_fma_mixhi_f16 v8,v55,s99,0
v_mfma_f32_16x16x32_bf16 v[36:39],a[200:203],a[24:27],v[36:39]
v_lshl_add_u64 v[48:49],v[60:61],0,v[62:63]
v_lshlrev_b32_sdwa v25,1,v25 dst_sel:DWORD dst_unused:UNUSED_PAD src0_sel:DWORD src1_sel:WORD_1
v_pk_fma_f16 v9,v8,v8,s54
v_mfma_f32_16x16x32_bf16 v[36:39],a[212:215],a[8:11],v[36:39]
ds_read_u16 v25,v25
v_lshlrev_b32_sdwa v27,1,v27 dst_sel:DWORD dst_unused:UNUSED_PAD src0_sel:DWORD src1_sel:WORD_1
v_mfma_f32_16x16x32_bf16 v[60:63],a[212:215],a[12:15],v[20:23]
ds_read_b128 v[20:23],v4 offset:128
ds_read_b64_tr_b16 a[212:213],v1 offset:4096
v_pk_mul_f16 v9,v8,v9
v_mfma_f32_16x16x32_bf16 v[60:63],a[200:203],a[28:31],v[60:63]
s_waitcnt lgkmcnt(5)
ds_read_b64_tr_b16 a[214:215],v1 offset:5120
ds_read_b64_tr_b16 a[200:201],v2 offset:4096
v_lshl_add_u64 v[50:51],v[32:33],0,v[34:35]
v_lshlrev_b32_sdwa v28,1,v28 dst_sel:DWORD dst_unused:UNUSED_PAD src0_sel:DWORD src1_sel:WORD_1
v_mfma_f32_16x16x32_bf16 v[60:63],a[196:199],a[44:47],v[60:63]
ds_read_b64_tr_b16 a[202:203],v2 offset:5120
ds_read_b64_tr_b16 a[196:197],v2 offset:2048
v_exp_f16 v8,v9
v_mfma_f32_16x16x32_bf16 v[32:35],a[192:195],a[44:47],v[16:19]
ds_read_b128 v[16:19],v4 offset:160
ds_read_b64_tr_b16 a[198:199],v2 offset:3072
v_lshlrev_b32_sdwa v30,1,v30 dst_sel:DWORD dst_unused:UNUSED_PAD src0_sel:DWORD src1_sel:WORD_1
v_mfma_f32_16x16x32_bf16 v[32:35],a[216:219],a[28:31],v[32:35]
ds_read_b64_tr_b16 a[192:193],v2 offset:0
ds_read_b64_tr_b16 a[194:195],v2 offset:1024
v_lshlrev_b32_sdwa v29,1,v29 dst_sel:DWORD dst_unused:UNUSED_PAD src0_sel:DWORD src1_sel:WORD_1
v_mfma_f32_16x16x32_bf16 v[32:35],a[220:223],a[12:15],v[32:35]
ds_read_u16_d16_hi v27,v27
v_exp_f16_sdwa v8,v9 dst_sel:WORD_1 dst_unused:UNUSED_PRESERVE src0_sel:WORD_1
v_mfma_f32_16x16x32_bf16 v[208:211],a[240:243],v[44:47],v[208:211]
ds_read_u16 v28,v28
v_lshlrev_b32_sdwa v31,1,v31 dst_sel:DWORD dst_unused:UNUSED_PAD src0_sel:DWORD src1_sel:WORD_1
v_mfma_f32_16x16x32_bf16 v[212:215],a[240:243],v[48:51],v[212:215]
ds_read_u16_d16_hi v30,v30
v_lshlrev_b32_sdwa v56,1,v56 dst_sel:DWORD dst_unused:UNUSED_PAD src0_sel:DWORD src1_sel:WORD_1
v_mfma_f32_16x16x32_bf16 v[172:175],a[224:227],v[48:51],v[172:175]
ds_read_u16 v29,v29
v_pk_fma_f16 v8,v8,s55,s55
v_mfma_f32_16x16x32_bf16 v[168:171],a[224:227],v[44:47],v[168:171]
v_rcp_f16 v9,v8
v_lshlrev_b32_sdwa v58,1,v58 dst_sel:DWORD dst_unused:UNUSED_PAD src0_sel:DWORD src1_sel:WORD_1
v_mfma_f32_16x16x32_bf16 v[216:219],a[248:251],v[44:47],v[216:219]
ds_read_u16_d16_hi v31,v31
v_lshlrev_b32_sdwa v57,1,v57 dst_sel:DWORD dst_unused:UNUSED_PAD src0_sel:DWORD src1_sel:WORD_1
v_mfma_f32_16x16x32_bf16 v[220:223],a[248:251],v[48:51],v[220:223]
ds_read_u16 v56,v56
v_lshlrev_b32_sdwa v59,1,v59 dst_sel:DWORD dst_unused:UNUSED_PAD src0_sel:DWORD src1_sel:WORD_1
v_mfma_f32_16x16x32_bf16 v[188:191],a[228:231],v[48:51],v[188:191]
ds_read_u16_d16_hi v58,v58
v_lshlrev_b32_sdwa v52,1,v52 dst_sel:DWORD dst_unused:UNUSED_PAD src0_sel:DWORD src1_sel:WORD_1
v_mfma_f32_16x16x32_bf16 v[184:187],a[228:231],v[44:47],v[184:187]
v_rcp_f16_sdwa v9,v8 dst_sel:WORD_1 dst_unused:UNUSED_PRESERVE src0_sel:WORD_1
v_mfma_f32_16x16x32_bf16 v[224:227],a[232:235],v[44:47],v[224:227]
ds_read_u16 v57,v57
ds_read_u16_d16_hi v59,v59
v_mfma_f32_16x16x32_bf16 v[228:231],a[232:235],v[48:51],v[228:231]
ds_read_u16 v52,v52
v_lshlrev_b32_sdwa v54,1,v54 dst_sel:DWORD dst_unused:UNUSED_PAD src0_sel:DWORD src1_sel:WORD_1
v_mfma_f32_16x16x32_bf16 v[204:207],a[236:239],v[48:51],v[204:207]
ds_read_u16_d16_hi v54,v54
v_fma_mix_f32 v53,v53,v9,0 op_sel_hi:[0,1,0]
v_mfma_f32_16x16x32_bf16 v[200:203],a[236:239],v[44:47],v[200:203]
s_waitcnt lgkmcnt(7)
v_fma_mix_f32 v55,v55,v9,0 op_sel:[0,1,0] op_sel_hi:[0,1,0]
v_mfma_f32_16x16x32_bf16 v[48:51],a[204:207],a[48:51],v[20:23]
s_add_u32 m0,s57,28296
v_lshlrev_b32_sdwa v40,1,v40 dst_sel:DWORD dst_unused:UNUSED_PAD src0_sel:DWORD src1_sel:WORD_1
v_lshlrev_b32_sdwa v42,1,v42 dst_sel:DWORD dst_unused:UNUSED_PAD src0_sel:DWORD src1_sel:WORD_1
v_mfma_f32_16x16x32_bf16 v[48:51],a[208:211],a[64:67],v[48:51]
s_add_u32 s89,s56,5701632
ds_read_u16 v40,v40
v_lshlrev_b32_sdwa v41,1,v41 dst_sel:DWORD dst_unused:UNUSED_PAD src0_sel:DWORD src1_sel:WORD_1
v_mfma_f32_16x16x32_bf16 v[48:51],a[212:215],a[80:83],v[48:51]
buffer_load_dwordx4 v6,s[36:39],s89 offen lds
v_lshl_add_u64 v[24:25],v[24:25],0,v[26:27]
v_mfma_f32_16x16x32_bf16 v[44:47],a[200:203],a[80:83],v[16:19]
s_waitcnt lgkmcnt(5)
ds_read_u16_d16_hi v42,v42
v_lshl_add_u64 v[26:27],v[28:29],0,v[30:31]
v_mfma_f32_16x16x32_bf16 v[44:47],a[196:199],a[64:67],v[44:47]
s_waitcnt lgkmcnt(4)
ds_read_u16 v41,v41
v_lshl_add_u64 v[28:29],v[56:57],0,v[58:59]
v_mfma_f32_16x16x32_bf16 v[44:47],a[192:195],a[48:51],v[44:47]
v_lshlrev_b32_sdwa v43,1,v43 dst_sel:DWORD dst_unused:UNUSED_PAD src0_sel:DWORD src1_sel:WORD_1
v_lshlrev_b32_sdwa v36,1,v36 dst_sel:DWORD dst_unused:UNUSED_PAD src0_sel:DWORD src1_sel:WORD_1
v_mfma_f32_16x16x32_bf16 v[56:59],a[192:195],a[52:55],v[16:19]
s_waitcnt lgkmcnt(3)
ds_read_u16_d16_hi v43,v43
v_or_b32 v30,v52,v54
v_mfma_f32_16x16x32_bf16 v[56:59],a[196:199],a[68:71],v[56:59]
ds_read_u16 v36,v36
v_cvt_pk_bf16_f32 v31,v53,v55
v_lshlrev_b32_sdwa v38,1,v38 dst_sel:DWORD dst_unused:UNUSED_PAD src0_sel:DWORD src1_sel:WORD_1
v_mfma_f32_16x16x32_bf16 v[56:59],a[200:203],a[84:87],v[56:59]
ds_read_u16_d16_hi v38,v38
v_lshlrev_b32_sdwa v37,1,v37 dst_sel:DWORD dst_unused:UNUSED_PAD src0_sel:DWORD src1_sel:WORD_1
v_mfma_f32_16x16x32_bf16 v[52:55],a[212:215],a[84:87],v[20:23]
ds_read_u16 v37,v37
v_lshlrev_b32_sdwa v39,1,v39 dst_sel:DWORD dst_unused:UNUSED_PAD src0_sel:DWORD src1_sel:WORD_1
v_mfma_f32_16x16x32_bf16 v[52:55],a[208:211],a[68:71],v[52:55]
ds_read_u16_d16_hi v39,v39
v_lshlrev_b32_sdwa v32,1,v32 dst_sel:DWORD dst_unused:UNUSED_PAD src0_sel:DWORD src1_sel:WORD_1
v_mfma_f32_16x16x32_bf16 v[52:55],a[204:207],a[52:55],v[52:55]
s_add_u32 m0,s57,31832
ds_read_u16 v32,v32
v_lshlrev_b32_sdwa v34,1,v34 dst_sel:DWORD dst_unused:UNUSED_PAD src0_sel:DWORD src1_sel:WORD_1
v_mfma_f32_16x16x32_bf16 v[136:139],a[240:243],v[24:27],v[136:139]
s_add_u32 s89,s56,5799936
ds_read_u16_d16_hi v34,v34
v_lshlrev_b32_sdwa v33,1,v33 dst_sel:DWORD dst_unused:UNUSED_PAD src0_sel:DWORD src1_sel:WORD_1
v_mfma_f32_16x16x32_bf16 v[140:143],a[240:243],v[28:31],v[140:143]
buffer_load_dwordx4 v6,s[36:39],s89 offen lds
v_lshlrev_b32_sdwa v35,1,v35 dst_sel:DWORD dst_unused:UNUSED_PAD src0_sel:DWORD src1_sel:WORD_1
v_mfma_f32_16x16x32_bf16 v[68:71],a[224:227],v[28:31],v[68:71]
ds_read_u16 v33,v33
ds_read_u16_d16_hi v35,v35
v_mfma_f32_16x16x32_bf16 v[64:67],a[224:227],v[24:27],v[64:67]
ds_read_b64_tr_b16 a[244:245],v3 offset:6160
v_lshlrev_b32_sdwa v60,1,v60 dst_sel:DWORD dst_unused:UNUSED_PAD src0_sel:DWORD src1_sel:WORD_1
v_mfma_f32_16x16x32_bf16 v[144:147],a[248:251],v[24:27],v[144:147]
ds_read_u16 v60,v60
v_lshlrev_b32_sdwa v62,1,v62 dst_sel:DWORD dst_unused:UNUSED_PAD src0_sel:DWORD src1_sel:WORD_1
v_mfma_f32_16x16x32_bf16 v[148:151],a[248:251],v[28:31],v[148:151]
ds_read_u16_d16_hi v62,v62
v_lshlrev_b32_sdwa v61,1,v61 dst_sel:DWORD dst_unused:UNUSED_PAD src0_sel:DWORD src1_sel:WORD_1
v_mfma_f32_16x16x32_bf16 v[84:87],a[228:231],v[28:31],v[84:87]
ds_read_u16 v61,v61
v_lshlrev_b32_sdwa v63,1,v63 dst_sel:DWORD dst_unused:UNUSED_PAD src0_sel:DWORD src1_sel:WORD_1
v_mfma_f32_16x16x32_bf16 v[80:83],a[228:231],v[24:27],v[80:83]
ds_read_u16_d16_hi v63,v63
v_mfma_f32_16x16x32_bf16 v[152:155],a[232:235],v[24:27],v[152:155]
ds_read_b64_tr_b16 a[246:247],v3 offset:7696
v_mfma_f32_16x16x32_bf16 v[156:159],a[232:235],v[28:31],v[156:159]
TP5954_%=:
s_cbranch_execz TR0_%=
ds_read_b64_tr_b16 a[252:253],v3 offset:6168
v_mfma_f32_16x16x32_bf16 v[100:103],a[236:239],v[28:31],v[100:103]
ds_read_b64_tr_b16 a[254:255],v3 offset:7704
v_mfma_f32_16x16x32_bf16 v[96:99],a[236:239],v[24:27],v[96:99]
v_mfma_f32_16x16x32_bf16 v[28:31],a[204:207],a[56:59],v[20:23]
ds_read_b64_tr_b16 a[216:217],v1 offset:6160
v_lshlrev_b32_sdwa v48,1,v48 dst_sel:DWORD dst_unused:UNUSED_PAD src0_sel:DWORD src1_sel:WORD_1
v_mfma_f32_16x16x32_bf16 v[28:31],a[208:211],a[72:75],v[28:31]
s_nop 0
ds_read_u16 v48,v48
v_fma_mixlo_f16 v8,v57,s99,0
v_mfma_f32_16x16x32_bf16 v[28:31],a[212:215],a[88:91],v[28:31]
s_waitcnt lgkmcnt(7)
ds_read_b64_tr_b16 a[218:219],v1 offset:7184
v_lshl_add_u64 v[40:41],v[40:41],0,v[42:43]
v_lshlrev_b32_sdwa v50,1,v50 dst_sel:DWORD dst_unused:UNUSED_PAD src0_sel:DWORD src1_sel:WORD_1
v_mfma_f32_16x16x32_bf16 v[24:27],a[200:203],a[88:91],v[16:19]
ds_read_u16_d16_hi v50,v50
v_lshl_add_u64 v[42:43],v[36:37],0,v[38:39]
v_fma_mixhi_f16 v8,v59,s99,0
v_mfma_f32_16x16x32_bf16 v[24:27],a[196:199],a[72:75],v[24:27]
ds_read_b64_tr_b16 a[220:221],v1 offset:8208
v_lshl_add_u64 v[36:37],v[32:33],0,v[34:35]
v_lshlrev_b32_sdwa v49,1,v49 dst_sel:DWORD dst_unused:UNUSED_PAD src0_sel:DWORD src1_sel:WORD_1
v_pk_fma_f16 v9,v8,v8,s54
v_mfma_f32_16x16x32_bf16 v[24:27],a[192:195],a[56:59],v[24:27]
ds_read_u16 v49,v49
v_lshlrev_b32_sdwa v51,1,v51 dst_sel:DWORD dst_unused:UNUSED_PAD src0_sel:DWORD src1_sel:WORD_1
v_mfma_f32_16x16x32_bf16 v[32:35],a[192:195],a[60:63],v[16:19]
ds_read_u16_d16_hi v51,v51
v_pk_mul_f16 v9,v8,v9
v_mfma_f32_16x16x32_bf16 v[32:35],a[196:199],a[76:79],v[32:35]
s_waitcnt lgkmcnt(7)
ds_read_b64_tr_b16 a[222:223],v1 offset:9232
v_lshl_add_u64 v[38:39],v[60:61],0,v[62:63]
v_lshlrev_b32_sdwa v44,1,v44 dst_sel:DWORD dst_unused:UNUSED_PAD src0_sel:DWORD src1_sel:WORD_1
v_mfma_f32_16x16x32_bf16 v[32:35],a[200:203],a[92:95],v[32:35]
v_exp_f16 v8,v9
v_mfma_f32_16x16x32_bf16 v[60:63],a[212:215],a[92:95],v[20:23]
ds_read_u16 v44,v44
v_lshlrev_b32_sdwa v46,1,v46 dst_sel:DWORD dst_unused:UNUSED_PAD src0_sel:DWORD src1_sel:WORD_1
v_mfma_f32_16x16x32_bf16 v[60:63],a[208:211],a[76:79],v[60:63]
ds_read_u16_d16_hi v46,v46
v_lshlrev_b32_sdwa v45,1,v45 dst_sel:DWORD dst_unused:UNUSED_PAD src0_sel:DWORD src1_sel:WORD_1
v_mfma_f32_16x16x32_bf16 v[60:63],a[204:207],a[60:63],v[60:63]
v_exp_f16_sdwa v8,v9 dst_sel:WORD_1 dst_unused:UNUSED_PRESERVE src0_sel:WORD_1
v_mfma_f32_16x16x32_bf16 v[112:115],a[240:243],v[40:43],v[112:115]
ds_read_u16 v45,v45
v_lshlrev_b32_sdwa v47,1,v47 dst_sel:DWORD dst_unused:UNUSED_PAD src0_sel:DWORD src1_sel:WORD_1
v_mfma_f32_16x16x32_bf16 v[116:119],a[240:243],v[36:39],v[116:119]
ds_read_u16_d16_hi v47,v47
v_lshlrev_b32_sdwa v52,1,v52 dst_sel:DWORD dst_unused:UNUSED_PAD src0_sel:DWORD src1_sel:WORD_1
v_mfma_f32_16x16x32_bf16 v[76:79],a[224:227],v[36:39],v[76:79]
ds_read_u16 v52,v52
v_pk_fma_f16 v8,v8,s55,s55
v_mfma_f32_16x16x32_bf16 v[72:75],a[224:227],v[40:43],v[72:75]
ds_read_b64_tr_b16 a[240:241],v3 offset:6224
v_rcp_f16_e64 v9,v8
v_lshlrev_b32_sdwa v54,1,v54 dst_sel:DWORD dst_unused:UNUSED_PAD src0_sel:DWORD src1_sel:WORD_1
v_mfma_f32_16x16x32_bf16 v[120:123],a[248:251],v[40:43],v[120:123]
ds_read_u16_d16_hi v54,v54
v_lshlrev_b32_sdwa v53,1,v53 dst_sel:DWORD dst_unused:UNUSED_PAD src0_sel:DWORD src1_sel:WORD_1
v_mfma_f32_16x16x32_bf16 v[124:127],a[248:251],v[36:39],v[124:127]
ds_read_u16 v53,v53
ds_read_b64_tr_b16 a[242:243],v3 offset:7760
v_mfma_f32_16x16x32_bf16 v[92:95],a[228:231],v[36:39],v[92:95]
ds_read_b64_tr_b16 a[224:225],v3 offset:6232
v_lshlrev_b32_sdwa v55,1,v55 dst_sel:DWORD dst_unused:UNUSED_PAD src0_sel:DWORD src1_sel:WORD_1
v_mfma_f32_16x16x32_bf16 v[88:91],a[228:231],v[40:43],v[88:91]
ds_read_u16_d16_hi v55,v55
v_rcp_f16_sdwa v9,v8 dst_sel:WORD_1 dst_unused:UNUSED_PRESERVE src0_sel:WORD_1
v_mfma_f32_16x16x32_bf16 v[128:131],a[232:235],v[40:43],v[128:131]
ds_read_b64_tr_b16 a[226:227],v3 offset:7768
ds_read_b64_tr_b16 a[248:249],v3 offset:6288
v_lshlrev_b32_sdwa v56,1,v56 dst_sel:DWORD dst_unused:UNUSED_PAD src0_sel:DWORD src1_sel:WORD_1
v_mfma_f32_16x16x32_bf16 v[132:135],a[232:235],v[36:39],v[132:135]
ds_read_u16 v56,v56
v_lshlrev_b32_sdwa v58,1,v58 dst_sel:DWORD dst_unused:UNUSED_PAD src0_sel:DWORD src1_sel:WORD_1
v_mfma_f32_16x16x32_bf16 v[108:111],a[236:239],v[36:39],v[108:111]
ds_read_u16_d16_hi v58,v58
v_fma_mix_f32 v57,v57,v9,0 op_sel_hi:[0,1,0]
v_mfma_f32_16x16x32_bf16 v[104:107],a[236:239],v[40:43],v[104:107]
ds_read_b64_tr_b16 a[250:251],v3 offset:7824
v_fma_mix_f32 v59,v59,v9,0 op_sel:[0,1,0] op_sel_hi:[0,1,0]
v_mfma_f32_16x16x32_bf16 v[36:39],a[204:207],a[0:3],v[20:23]
s_add_u32 m0,s57,35360
ds_read_b64_tr_b16 a[228:229],v3 offset:6296
v_lshlrev_b32_sdwa v28,1,v28 dst_sel:DWORD dst_unused:UNUSED_PAD src0_sel:DWORD src1_sel:WORD_1
v_mfma_f32_16x16x32_bf16 v[36:39],a[208:211],a[16:19],v[36:39]
s_add_u32 s89,s56,5898240
ds_read_b64_tr_b16 a[230:231],v3 offset:7832
v_lshlrev_b32_sdwa v30,1,v30 dst_sel:DWORD dst_unused:UNUSED_PAD src0_sel:DWORD src1_sel:WORD_1
v_mfma_f32_16x16x32_bf16 v[36:39],a[212:215],a[32:35],v[36:39]
s_waitcnt lgkmcnt(9)
buffer_load_dwordx4 v6,s[36:39],s89 offen lds
v_lshl_add_u64 v[48:49],v[48:49],0,v[50:51]
v_mfma_f32_16x16x32_bf16 v[40:43],a[200:203],a[32:35],v[16:19]
ds_read_u16 v28,v28
v_lshl_add_u64 v[50:51],v[44:45],0,v[46:47]
v_lshlrev_b32_sdwa v29,1,v29 dst_sel:DWORD dst_unused:UNUSED_PAD src0_sel:DWORD src1_sel:WORD_1
v_mfma_f32_16x16x32_bf16 v[40:43],a[196:199],a[16:19],v[40:43]
s_waitcnt lgkmcnt(8)
ds_read_u16_d16_hi v30,v30
v_lshl_add_u64 v[44:45],v[52:53],0,v[54:55]
v_mfma_f32_16x16x32_bf16 v[40:43],a[192:195],a[0:3],v[40:43]
ds_read_u16 v29,v29
v_lshlrev_b32_sdwa v31,1,v31 dst_sel:DWORD dst_unused:UNUSED_PAD src0_sel:DWORD src1_sel:WORD_1
v_mfma_f32_16x16x32_bf16 v[52:55],a[192:195],a[4:7],v[16:19]
s_waitcnt lgkmcnt(6)
ds_read_u16_d16_hi v31,v31
v_or_b32 v46,v56,v58
v_lshlrev_b32_sdwa v24,1,v24 dst_sel:DWORD dst_unused:UNUSED_PAD src0_sel:DWORD src1_sel:WORD_1
v_mfma_f32_16x16x32_bf16 v[52:55],a[196:199],a[20:23],v[52:55]
ds_read_u16 v24,v24
v_cvt_pk_bf16_f32 v47,v57,v59
v_lshlrev_b32_sdwa v26,1,v26 dst_sel:DWORD dst_unused:UNUSED_PAD src0_sel:DWORD src1_sel:WORD_1
v_mfma_f32_16x16x32_bf16 v[52:55],a[200:203],a[36:39],v[52:55]
ds_read_u16_d16_hi v26,v26
v_lshlrev_b32_sdwa v25,1,v25 dst_sel:DWORD dst_unused:UNUSED_PAD src0_sel:DWORD src1_sel:WORD_1
v_mfma_f32_16x16x32_bf16 v[56:59],a[212:215],a[36:39],v[20:23]
ds_read_u16 v25,v25
v_lshlrev_b32_sdwa v27,1,v27 dst_sel:DWORD dst_unused:UNUSED_PAD src0_sel:DWORD src1_sel:WORD_1
v_mfma_f32_16x16x32_bf16 v[56:59],a[208:211],a[20:23],v[56:59]
ds_read_u16_d16_hi v27,v27
v_lshlrev_b32_sdwa v60,1,v60 dst_sel:DWORD dst_unused:UNUSED_PAD src0_sel:DWORD src1_sel:WORD_1
v_mfma_f32_16x16x32_bf16 v[56:59],a[204:207],a[4:7],v[56:59]
ds_read_u16 v60,v60
v_lshlrev_b32_sdwa v62,1,v62 dst_sel:DWORD dst_unused:UNUSED_PAD src0_sel:DWORD src1_sel:WORD_1
v_mfma_f32_16x16x32_bf16 v[232:235],a[244:247],v[48:51],v[232:235]
ds_read_u16_d16_hi v62,v62
v_lshlrev_b32_sdwa v61,1,v61 dst_sel:DWORD dst_unused:UNUSED_PAD src0_sel:DWORD src1_sel:WORD_1
v_mfma_f32_16x16x32_bf16 v[236:239],a[244:247],v[44:47],v[236:239]
ds_read_u16 v61,v61
v_lshlrev_b32_sdwa v63,1,v63 dst_sel:DWORD dst_unused:UNUSED_PAD src0_sel:DWORD src1_sel:WORD_1
v_mfma_f32_16x16x32_bf16 v[164:167],a[252:255],v[44:47],v[164:167]
ds_read_u16_d16_hi v63,v63
v_lshlrev_b32_sdwa v32,1,v32 dst_sel:DWORD dst_unused:UNUSED_PAD src0_sel:DWORD src1_sel:WORD_1
v_mfma_f32_16x16x32_bf16 v[160:163],a[252:255],v[48:51],v[160:163]
ds_read_u16 v32,v32
v_lshlrev_b32_sdwa v34,1,v34 dst_sel:DWORD dst_unused:UNUSED_PAD src0_sel:DWORD src1_sel:WORD_1
v_mfma_f32_16x16x32_bf16 v[240:243],a[240:243],v[48:51],v[240:243]
ds_read_u16_d16_hi v34,v34
v_lshlrev_b32_sdwa v33,1,v33 dst_sel:DWORD dst_unused:UNUSED_PAD src0_sel:DWORD src1_sel:WORD_1
v_mfma_f32_16x16x32_bf16 v[244:247],a[240:243],v[44:47],v[244:247]
ds_read_u16 v33,v33
v_lshlrev_b32_sdwa v35,1,v35 dst_sel:DWORD dst_unused:UNUSED_PAD src0_sel:DWORD src1_sel:WORD_1
v_mfma_f32_16x16x32_bf16 v[180:183],a[224:227],v[44:47],v[180:183]
ds_read_u16_d16_hi v35,v35
v_mfma_f32_16x16x32_bf16 v[176:179],a[224:227],v[48:51],v[176:179]
s_waitcnt lgkmcnt(8)
v_mfma_f32_16x16x32_bf16 v[248:251],a[248:251],v[48:51],v[248:251]
s_add_u32 m0,s57,38896
v_mfma_f32_16x16x32_bf16 v[252:255],a[248:251],v[44:47],v[252:255]
s_add_u32 s89,s56,5996544
v_mfma_f32_16x16x32_bf16 v[196:199],a[228:231],v[44:47],v[196:199]
buffer_load_dwordx4 v6,s[36:39],s89 offen lds
v_mfma_f32_16x16x32_bf16 v[192:195],a[228:231],v[48:51],v[192:195]
v_mfma_f32_16x16x32_bf16 v[44:47],a[204:207],a[8:11],v[20:23]
v_lshlrev_b32_sdwa v36,1,v36 dst_sel:DWORD dst_unused:UNUSED_PAD src0_sel:DWORD src1_sel:WORD_1
v_fma_mixlo_f16 v8,v53,s99,0
v_mfma_f32_16x16x32_bf16 v[44:47],a[208:211],a[24:27],v[44:47]
ds_read_u16 v36,v36
v_lshlrev_b32_sdwa v38,1,v38 dst_sel:DWORD dst_unused:UNUSED_PAD src0_sel:DWORD src1_sel:WORD_1
v_mfma_f32_16x16x32_bf16 v[44:47],a[212:215],a[40:43],v[44:47]
ds_read_u16_d16_hi v38,v38
v_lshl_add_u64 v[28:29],v[28:29],0,v[30:31]
v_mfma_f32_16x16x32_bf16 v[48:51],a[200:203],a[40:43],v[16:19]
v_lshl_add_u64 v[30:31],v[24:25],0,v[26:27]
v_fma_mixhi_f16 v8,v55,s99,0
v_mfma_f32_16x16x32_bf16 v[48:51],a[196:199],a[24:27],v[48:51]
s_waitcnt lgkmcnt(2)
v_lshl_add_u64 v[24:25],v[60:61],0,v[62:63]
v_lshlrev_b32_sdwa v37,1,v37 dst_sel:DWORD dst_unused:UNUSED_PAD src0_sel:DWORD src1_sel:WORD_1
v_pk_fma_f16 v9,v8,v8,s54
v_mfma_f32_16x16x32_bf16 v[48:51],a[192:195],a[8:11],v[48:51]
ds_read_u16 v37,v37
v_lshlrev_b32_sdwa v39,1,v39 dst_sel:DWORD dst_unused:UNUSED_PAD src0_sel:DWORD src1_sel:WORD_1
v_mfma_f32_16x16x32_bf16 v[60:63],a[192:195],a[12:15],v[16:19]
ds_read_b128 v[16:19],v4 offset:256
v_pk_mul_f16 v9,v8,v9
v_mfma_f32_16x16x32_bf16 v[60:63],a[196:199],a[28:31],v[60:63]
ds_read_u16_d16_hi v39,v39
ds_read_b64_tr_b16 a[192:193],v1 offset:10256
v_lshl_add_u64 v[26:27],v[32:33],0,v[34:35]
v_lshlrev_b32_sdwa v40,1,v40 dst_sel:DWORD dst_unused:UNUSED_PAD src0_sel:DWORD src1_sel:WORD_1
v_mfma_f32_16x16x32_bf16 v[60:63],a[200:203],a[44:47],v[60:63]
ds_read_b64_tr_b16 a[194:195],v1 offset:11280
v_exp_f16_e64 v8,v9
v_mfma_f32_16x16x32_bf16 v[32:35],a[212:215],a[44:47],v[20:23]
ds_read_b128 v[20:23],v4 offset:288
ds_read_b64_tr_b16 a[196:197],v2 offset:10256
v_lshlrev_b32_sdwa v42,1,v42 dst_sel:DWORD dst_unused:UNUSED_PAD src0_sel:DWORD src1_sel:WORD_1
v_mfma_f32_16x16x32_bf16 v[32:35],a[208:211],a[28:31],v[32:35]
ds_read_b64_tr_b16 a[198:199],v2 offset:11280
ds_read_u16 v40,v40
v_lshlrev_b32_sdwa v41,1,v41 dst_sel:DWORD dst_unused:UNUSED_PAD src0_sel:DWORD src1_sel:WORD_1
v_mfma_f32_16x16x32_bf16 v[32:35],a[204:207],a[12:15],v[32:35]
ds_read_u16_d16_hi v42,v42
v_exp_f16_sdwa v8,v9 dst_sel:WORD_1 dst_unused:UNUSED_PRESERVE src0_sel:WORD_1
v_mfma_f32_16x16x32_bf16 v[208:211],a[244:247],v[28:31],v[208:211]
ds_read_u16 v41,v41
ds_read_b64_tr_b16 a[200:201],v2 offset:8208
v_lshlrev_b32_sdwa v43,1,v43 dst_sel:DWORD dst_unused:UNUSED_PAD src0_sel:DWORD src1_sel:WORD_1
v_mfma_f32_16x16x32_bf16 v[212:215],a[244:247],v[24:27],v[212:215]
ds_read_u16_d16_hi v43,v43
v_lshlrev_b32_sdwa v56,1,v56 dst_sel:DWORD dst_unused:UNUSED_PAD src0_sel:DWORD src1_sel:WORD_1
v_mfma_f32_16x16x32_bf16 v[172:175],a[252:255],v[24:27],v[172:175]
ds_read_b64_tr_b16 a[202:203],v2 offset:9232
v_pk_fma_f16 v8,v8,s55,s55
v_mfma_f32_16x16x32_bf16 v[168:171],a[252:255],v[28:31],v[168:171]
v_rcp_f16 v9,v8
v_lshlrev_b32_sdwa v58,1,v58 dst_sel:DWORD dst_unused:UNUSED_PAD src0_sel:DWORD src1_sel:WORD_1
v_mfma_f32_16x16x32_bf16 v[216:219],a[240:243],v[28:31],v[216:219]
ds_read_u16 v56,v56
v_lshlrev_b32_sdwa v57,1,v57 dst_sel:DWORD dst_unused:UNUSED_PAD src0_sel:DWORD src1_sel:WORD_1
v_mfma_f32_16x16x32_bf16 v[220:223],a[240:243],v[24:27],v[220:223]
ds_read_u16_d16_hi v58,v58
v_lshlrev_b32_sdwa v59,1,v59 dst_sel:DWORD dst_unused:UNUSED_PAD src0_sel:DWORD src1_sel:WORD_1
v_mfma_f32_16x16x32_bf16 v[188:191],a[224:227],v[24:27],v[188:191]
ds_read_u16 v57,v57
v_lshlrev_b32_sdwa v52,1,v52 dst_sel:DWORD dst_unused:UNUSED_PAD src0_sel:DWORD src1_sel:WORD_1
v_mfma_f32_16x16x32_bf16 v[184:187],a[224:227],v[28:31],v[184:187]
ds_read_u16_d16_hi v59,v59
v_rcp_f16_sdwa v9,v8 dst_sel:WORD_1 dst_unused:UNUSED_PRESERVE src0_sel:WORD_1
v_mfma_f32_16x16x32_bf16 v[224:227],a[248:251],v[28:31],v[224:227]
ds_read_b64_tr_b16 a[212:213],v2 offset:6160
v_lshlrev_b32_sdwa v54,1,v54 dst_sel:DWORD dst_unused:UNUSED_PAD src0_sel:DWORD src1_sel:WORD_1
v_mfma_f32_16x16x32_bf16 v[228:231],a[248:251],v[24:27],v[228:231]
ds_read_b64_tr_b16 a[214:215],v2 offset:7184
v_fma_mix_f32 v53,v53,v9,0 op_sel_hi:[0,1,0]
v_mfma_f32_16x16x32_bf16 v[204:207],a[228:231],v[24:27],v[204:207]
ds_read_u16 v52,v52
v_fma_mix_f32 v55,v55,v9,0 op_sel:[0,1,0] op_sel_hi:[0,1,0]
v_mfma_f32_16x16x32_bf16 v[200:203],a[228:231],v[28:31],v[200:203]
s_waitcnt lgkmcnt(7)
ds_read_u16_d16_hi v54,v54
v_mfma_f32_16x16x32_bf16 v[24:27],a[216:219],a[48:51],v[16:19]
v_lshlrev_b32_sdwa v44,1,v44 dst_sel:DWORD dst_unused:UNUSED_PAD src0_sel:DWORD src1_sel:WORD_1
v_lshlrev_b32_sdwa v46,1,v46 dst_sel:DWORD dst_unused:UNUSED_PAD src0_sel:DWORD src1_sel:WORD_1
v_mfma_f32_16x16x32_bf16 v[24:27],a[220:223],a[64:67],v[24:27]
s_add_u32 m0,s57,42440
ds_read_u16 v44,v44
v_lshlrev_b32_sdwa v45,1,v45 dst_sel:DWORD dst_unused:UNUSED_PAD src0_sel:DWORD src1_sel:WORD_1
v_mfma_f32_16x16x32_bf16 v[24:27],a[192:195],a[80:83],v[24:27]
s_add_u32 s89,s56,6094848
ds_read_u16_d16_hi v46,v46
v_lshl_add_u64 v[36:37],v[36:37],0,v[38:39]
v_mfma_f32_16x16x32_bf16 v[28:31],a[196:199],a[80:83],v[20:23]
buffer_load_dwordx4 v6,s[36:39],s89 offen lds
v_lshl_add_u64 v[38:39],v[40:41],0,v[42:43]
v_mfma_f32_16x16x32_bf16 v[28:31],a[200:203],a[64:67],v[28:31]
s_waitcnt lgkmcnt(4)
ds_read_u16 v45,v45
v_lshl_add_u64 v[40:41],v[56:57],0,v[58:59]
v_mfma_f32_16x16x32_bf16 v[28:31],a[212:215],a[48:51],v[28:31]
v_lshlrev_b32_sdwa v47,1,v47 dst_sel:DWORD dst_unused:UNUSED_PAD src0_sel:DWORD src1_sel:WORD_1
v_lshlrev_b32_sdwa v48,1,v48 dst_sel:DWORD dst_unused:UNUSED_PAD src0_sel:DWORD src1_sel:WORD_1
v_mfma_f32_16x16x32_bf16 v[56:59],a[212:215],a[52:55],v[20:23]
s_waitcnt lgkmcnt(3)
ds_read_u16_d16_hi v47,v47
v_or_b32 v42,v52,v54
v_mfma_f32_16x16x32_bf16 v[56:59],a[200:203],a[68:71],v[56:59]
s_waitcnt vmcnt(14)
ds_read_u16 v48,v48
v_cvt_pk_bf16_f32 v43,v53,v55
v_lshlrev_b32_sdwa v50,1,v50 dst_sel:DWORD dst_unused:UNUSED_PAD src0_sel:DWORD src1_sel:WORD_1
v_mfma_f32_16x16x32_bf16 v[56:59],a[196:199],a[84:87],v[56:59]
s_barrier
ds_read_u16_d16_hi v50,v50
v_lshlrev_b32_sdwa v49,1,v49 dst_sel:DWORD dst_unused:UNUSED_PAD src0_sel:DWORD src1_sel:WORD_1
v_mfma_f32_16x16x32_bf16 v[52:55],a[192:195],a[84:87],v[16:19]
ds_read_u16 v49,v49
v_lshlrev_b32_sdwa v51,1,v51 dst_sel:DWORD dst_unused:UNUSED_PAD src0_sel:DWORD src1_sel:WORD_1
v_mfma_f32_16x16x32_bf16 v[52:55],a[220:223],a[68:71],v[52:55]
s_add_u32 m0,s58,0
ds_read_u16_d16_hi v51,v51
v_lshlrev_b32_sdwa v32,1,v32 dst_sel:DWORD dst_unused:UNUSED_PAD src0_sel:DWORD src1_sel:WORD_1
v_mfma_f32_16x16x32_bf16 v[52:55],a[216:219],a[52:55],v[52:55]
s_add_u32 s69,s69,s59
ds_read_u16 v32,v32
v_lshlrev_b32_sdwa v34,1,v34 dst_sel:DWORD dst_unused:UNUSED_PAD src0_sel:DWORD src1_sel:WORD_1
v_mfma_f32_16x16x32_bf16 v[136:139],a[244:247],v[36:39],v[136:139]
buffer_load_dwordx4 v5,s[44:47],s69 offen offset:0 lds
v_lshlrev_b32_sdwa v33,1,v33 dst_sel:DWORD dst_unused:UNUSED_PAD src0_sel:DWORD src1_sel:WORD_1
v_mfma_f32_16x16x32_bf16 v[140:143],a[244:247],v[40:43],v[140:143]
ds_read_u16_d16_hi v34,v34
v_lshlrev_b32_sdwa v35,1,v35 dst_sel:DWORD dst_unused:UNUSED_PAD src0_sel:DWORD src1_sel:WORD_1
v_mfma_f32_16x16x32_bf16 v[68:71],a[252:255],v[40:43],v[68:71]
ds_read_u16 v33,v33
ds_read_u16_d16_hi v35,v35
v_mfma_f32_16x16x32_bf16 v[64:67],a[252:255],v[36:39],v[64:67]
ds_read_b64_tr_b16 a[232:233],v3 offset:12320
v_lshlrev_b32_sdwa v60,1,v60 dst_sel:DWORD dst_unused:UNUSED_PAD src0_sel:DWORD src1_sel:WORD_1
v_mfma_f32_16x16x32_bf16 v[144:147],a[240:243],v[36:39],v[144:147]
s_add_u32 s69,s69,s53
ds_read_u16 v60,v60
v_lshlrev_b32_sdwa v62,1,v62 dst_sel:DWORD dst_unused:UNUSED_PAD src0_sel:DWORD src1_sel:WORD_1
v_mfma_f32_16x16x32_bf16 v[148:151],a[240:243],v[40:43],v[148:151]
buffer_load_dwordx4 v5,s[44:47],s69 offen offset:1024 lds
ds_read_u16_d16_hi v62,v62
v_mfma_f32_16x16x32_bf16 v[84:87],a[224:227],v[40:43],v[84:87]
ds_read_b64_tr_b16 a[234:235],v3 offset:13856
v_lshlrev_b32_sdwa v61,1,v61 dst_sel:DWORD dst_unused:UNUSED_PAD src0_sel:DWORD src1_sel:WORD_1
v_mfma_f32_16x16x32_bf16 v[80:83],a[224:227],v[36:39],v[80:83]
ds_read_u16 v61,v61
v_lshlrev_b32_sdwa v63,1,v63 dst_sel:DWORD dst_unused:UNUSED_PAD src0_sel:DWORD src1_sel:WORD_1
v_mfma_f32_16x16x32_bf16 v[152:155],a[248:251],v[36:39],v[152:155]
ds_read_u16_d16_hi v63,v63
v_mfma_f32_16x16x32_bf16 v[156:159],a[248:251],v[40:43],v[156:159]
s_add_u32 s69,s69,s53
ds_read_b64_tr_b16 a[236:237],v3 offset:12328
v_mfma_f32_16x16x32_bf16 v[100:103],a[228:231],v[40:43],v[100:103]
buffer_load_dwordx4 v5,s[44:47],s69 offen offset:2048 lds
v_mfma_f32_16x16x32_bf16 v[96:99],a[228:231],v[36:39],v[96:99]
ds_read_b64_tr_b16 a[238:239],v3 offset:13864
v_mfma_f32_16x16x32_bf16 v[40:43],a[216:219],a[56:59],v[16:19]
ds_read_b64_tr_b16 a[208:209],v1 offset:12320
v_lshlrev_b32_sdwa v24,1,v24 dst_sel:DWORD dst_unused:UNUSED_PAD src0_sel:DWORD src1_sel:WORD_1
v_mfma_f32_16x16x32_bf16 v[40:43],a[220:223],a[72:75],v[40:43]
ds_read_u16 v24,v24
v_fma_mixlo_f16 v8,v57,s99,0
v_mfma_f32_16x16x32_bf16 v[40:43],a[192:195],a[88:91],v[40:43]
s_waitcnt lgkmcnt(7)
ds_read_b64_tr_b16 a[210:211],v1 offset:13344
v_lshl_add_u64 v[44:45],v[44:45],0,v[46:47]
v_lshlrev_b32_sdwa v26,1,v26 dst_sel:DWORD dst_unused:UNUSED_PAD src0_sel:DWORD src1_sel:WORD_1
v_mfma_f32_16x16x32_bf16 v[36:39],a[196:199],a[88:91],v[20:23]
ds_read_u16_d16_hi v26,v26
v_lshl_add_u64 v[46:47],v[48:49],0,v[50:51]
v_fma_mixhi_f16 v8,v59,s99,0
v_mfma_f32_16x16x32_bf16 v[36:39],a[200:203],a[72:75],v[36:39]
ds_read_b64_tr_b16 a[204:205],v1 offset:14368
v_lshl_add_u64 v[48:49],v[32:33],0,v[34:35]
v_lshlrev_b32_sdwa v25,1,v25 dst_sel:DWORD dst_unused:UNUSED_PAD src0_sel:DWORD src1_sel:WORD_1
v_pk_fma_f16 v9,v8,v8,s54
v_mfma_f32_16x16x32_bf16 v[36:39],a[212:215],a[56:59],v[36:39]
ds_read_u16 v25,v25
v_lshlrev_b32_sdwa v27,1,v27 dst_sel:DWORD dst_unused:UNUSED_PAD src0_sel:DWORD src1_sel:WORD_1
v_mfma_f32_16x16x32_bf16 v[32:35],a[212:215],a[60:63],v[20:23]
ds_read_u16_d16_hi v27,v27
v_pk_mul_f16 v9,v8,v9
v_mfma_f32_16x16x32_bf16 v[32:35],a[200:203],a[76:79],v[32:35]
s_waitcnt lgkmcnt(8)
ds_read_b64_tr_b16 a[206:207],v1 offset:15392
v_lshl_add_u64 v[50:51],v[60:61],0,v[62:63]
v_lshlrev_b32_sdwa v28,1,v28 dst_sel:DWORD dst_unused:UNUSED_PAD src0_sel:DWORD src1_sel:WORD_1
v_mfma_f32_16x16x32_bf16 v[32:35],a[196:199],a[92:95],v[32:35]
v_exp_f16 v8,v9
v_mfma_f32_16x16x32_bf16 v[60:63],a[192:195],a[92:95],v[16:19]
ds_read_u16 v28,v28
v_lshlrev_b32_sdwa v30,1,v30 dst_sel:DWORD dst_unused:UNUSED_PAD src0_sel:DWORD src1_sel:WORD_1
v_mfma_f32_16x16x32_bf16 v[60:63],a[220:223],a[76:79],v[60:63]
ds_read_u16_d16_hi v30,v30
v_lshlrev_b32_sdwa v29,1,v29 dst_sel:DWORD dst_unused:UNUSED_PAD src0_sel:DWORD src1_sel:WORD_1
v_mfma_f32_16x16x32_bf16 v[60:63],a[216:219],a[60:63],v[60:63]
v_exp_f16_sdwa v8,v9 dst_sel:WORD_1 dst_unused:UNUSED_PRESERVE src0_sel:WORD_1
v_mfma_f32_16x16x32_bf16 v[112:115],a[244:247],v[44:47],v[112:115]
ds_read_u16 v29,v29
v_lshlrev_b32_sdwa v31,1,v31 dst_sel:DWORD dst_unused:UNUSED_PAD src0_sel:DWORD src1_sel:WORD_1
v_mfma_f32_16x16x32_bf16 v[116:119],a[244:247],v[48:51],v[116:119]
ds_read_u16_d16_hi v31,v31
v_lshlrev_b32_sdwa v52,1,v52 dst_sel:DWORD dst_unused:UNUSED_PAD src0_sel:DWORD src1_sel:WORD_1
v_mfma_f32_16x16x32_bf16 v[76:79],a[252:255],v[48:51],v[76:79]
ds_read_u16 v52,v52
v_pk_fma_f16 v8,v8,s55,s55
v_mfma_f32_16x16x32_bf16 v[72:75],a[252:255],v[44:47],v[72:75]
ds_read_b64_tr_b16 a[244:245],v3 offset:12384
v_rcp_f16_e64 v9,v8
v_lshlrev_b32_sdwa v54,1,v54 dst_sel:DWORD dst_unused:UNUSED_PAD src0_sel:DWORD src1_sel:WORD_1
v_mfma_f32_16x16x32_bf16 v[120:123],a[240:243],v[44:47],v[120:123]
ds_read_u16_d16_hi v54,v54
v_lshlrev_b32_sdwa v53,1,v53 dst_sel:DWORD dst_unused:UNUSED_PAD src0_sel:DWORD src1_sel:WORD_1
v_mfma_f32_16x16x32_bf16 v[124:127],a[240:243],v[48:51],v[124:127]
ds_read_u16 v53,v53
ds_read_b64_tr_b16 a[246:247],v3 offset:13920
v_mfma_f32_16x16x32_bf16 v[92:95],a[224:227],v[48:51],v[92:95]
ds_read_b64_tr_b16 a[252:253],v3 offset:12392
v_lshlrev_b32_sdwa v55,1,v55 dst_sel:DWORD dst_unused:UNUSED_PAD src0_sel:DWORD src1_sel:WORD_1
v_mfma_f32_16x16x32_bf16 v[88:91],a[224:227],v[44:47],v[88:91]
ds_read_u16_d16_hi v55,v55
v_rcp_f16_sdwa v9,v8 dst_sel:WORD_1 dst_unused:UNUSED_PRESERVE src0_sel:WORD_1
v_mfma_f32_16x16x32_bf16 v[128:131],a[248:251],v[44:47],v[128:131]
ds_read_b64_tr_b16 a[254:255],v3 offset:13928
ds_read_b64_tr_b16 a[240:241],v3 offset:12448
v_lshlrev_b32_sdwa v56,1,v56 dst_sel:DWORD dst_unused:UNUSED_PAD src0_sel:DWORD src1_sel:WORD_1
v_mfma_f32_16x16x32_bf16 v[132:135],a[248:251],v[48:51],v[132:135]
ds_read_u16 v56,v56
v_lshlrev_b32_sdwa v58,1,v58 dst_sel:DWORD dst_unused:UNUSED_PAD src0_sel:DWORD src1_sel:WORD_1
v_mfma_f32_16x16x32_bf16 v[108:111],a[228:231],v[48:51],v[108:111]
ds_read_u16_d16_hi v58,v58
v_fma_mix_f32 v57,v57,v9,0 op_sel_hi:[0,1,0]
v_mfma_f32_16x16x32_bf16 v[104:107],a[228:231],v[44:47],v[104:107]
ds_read_b64_tr_b16 a[242:243],v3 offset:13984
v_fma_mix_f32 v59,v59,v9,0 op_sel:[0,1,0] op_sel_hi:[0,1,0]
v_mfma_f32_16x16x32_bf16 v[48:51],a[216:219],a[0:3],v[16:19]
s_add_u32 m0,s58,6160
ds_read_b64_tr_b16 a[224:225],v3 offset:12456
v_lshlrev_b32_sdwa v40,1,v40 dst_sel:DWORD dst_unused:UNUSED_PAD src0_sel:DWORD src1_sel:WORD_1
v_mfma_f32_16x16x32_bf16 v[48:51],a[220:223],a[16:19],v[48:51]
s_add_u32 s69,s69,s59
ds_read_b64_tr_b16 a[226:227],v3 offset:13992
v_lshlrev_b32_sdwa v42,1,v42 dst_sel:DWORD dst_unused:UNUSED_PAD src0_sel:DWORD src1_sel:WORD_1
v_mfma_f32_16x16x32_bf16 v[48:51],a[192:195],a[32:35],v[48:51]
s_waitcnt lgkmcnt(9)
buffer_load_dwordx4 v5,s[44:47],s69 offen offset:0 lds
v_lshl_add_u64 v[24:25],v[24:25],0,v[26:27]
v_mfma_f32_16x16x32_bf16 v[44:47],a[196:199],a[32:35],v[20:23]
s_add_u32 m0,s57,45976
ds_read_u16 v40,v40
v_lshl_add_u64 v[26:27],v[28:29],0,v[30:31]
v_lshlrev_b32_sdwa v41,1,v41 dst_sel:DWORD dst_unused:UNUSED_PAD src0_sel:DWORD src1_sel:WORD_1
v_mfma_f32_16x16x32_bf16 v[44:47],a[200:203],a[16:19],v[44:47]
s_waitcnt lgkmcnt(8)
ds_read_u16_d16_hi v42,v42
v_lshl_add_u64 v[28:29],v[52:53],0,v[54:55]
v_mfma_f32_16x16x32_bf16 v[44:47],a[212:215],a[0:3],v[44:47]
s_add_u32 s89,s56,6193152
ds_read_u16 v41,v41
v_lshlrev_b32_sdwa v43,1,v43 dst_sel:DWORD dst_unused:UNUSED_PAD src0_sel:DWORD src1_sel:WORD_1
v_mfma_f32_16x16x32_bf16 v[52:55],a[212:215],a[4:7],v[20:23]
s_waitcnt lgkmcnt(6)
buffer_load_dwordx4 v6,s[36:39],s89 offen lds
v_or_b32 v30,v56,v58
v_lshlrev_b32_sdwa v36,1,v36 dst_sel:DWORD dst_unused:UNUSED_PAD src0_sel:DWORD src1_sel:WORD_1
v_mfma_f32_16x16x32_bf16 v[52:55],a[200:203],a[20:23],v[52:55]
s_add_u32 m0,s58,6160
ds_read_u16_d16_hi v43,v43
v_cvt_pk_bf16_f32 v31,v57,v59
v_lshlrev_b32_sdwa v38,1,v38 dst_sel:DWORD dst_unused:UNUSED_PAD src0_sel:DWORD src1_sel:WORD_1
v_mfma_f32_16x16x32_bf16 v[52:55],a[196:199],a[36:39],v[52:55]
s_add_u32 s69,s69,s53
ds_read_u16 v36,v36
v_lshlrev_b32_sdwa v37,1,v37 dst_sel:DWORD dst_unused:UNUSED_PAD src0_sel:DWORD src1_sel:WORD_1
v_mfma_f32_16x16x32_bf16 v[56:59],a[192:195],a[36:39],v[16:19]
buffer_load_dwordx4 v5,s[44:47],s69 offen offset:1024 lds
v_lshlrev_b32_sdwa v39,1,v39 dst_sel:DWORD dst_unused:UNUSED_PAD src0_sel:DWORD src1_sel:WORD_1
v_mfma_f32_16x16x32_bf16 v[56:59],a[220:223],a[20:23],v[56:59]
s_add_u32 s69,s69,s53
ds_read_u16_d16_hi v38,v38
v_lshlrev_b32_sdwa v60,1,v60 dst_sel:DWORD dst_unused:UNUSED_PAD src0_sel:DWORD src1_sel:WORD_1
v_mfma_f32_16x16x32_bf16 v[56:59],a[216:219],a[4:7],v[56:59]
buffer_load_dwordx4 v5,s[44:47],s69 offen offset:2048 lds
v_lshlrev_b32_sdwa v62,1,v62 dst_sel:DWORD dst_unused:UNUSED_PAD src0_sel:DWORD src1_sel:WORD_1
v_mfma_f32_16x16x32_bf16 v[232:235],a[232:235],v[24:27],v[232:235]
s_add_u32 m0,s57,49504
ds_read_u16 v37,v37
v_lshlrev_b32_sdwa v61,1,v61 dst_sel:DWORD dst_unused:UNUSED_PAD src0_sel:DWORD src1_sel:WORD_1
v_mfma_f32_16x16x32_bf16 v[236:239],a[232:235],v[28:31],v[236:239]
s_add_u32 s89,s56,6291456
ds_read_u16_d16_hi v39,v39
v_lshlrev_b32_sdwa v63,1,v63 dst_sel:DWORD dst_unused:UNUSED_PAD src0_sel:DWORD src1_sel:WORD_1
v_mfma_f32_16x16x32_bf16 v[164:167],a[236:239],v[28:31],v[164:167]
buffer_load_dwordx4 v6,s[36:39],s89 offen lds
ds_read_u16 v60,v60
v_mfma_f32_16x16x32_bf16 v[160:163],a[236:239],v[24:27],v[160:163]
ds_read_u16_d16_hi v62,v62
v_lshlrev_b32_sdwa v32,1,v32 dst_sel:DWORD dst_unused:UNUSED_PAD src0_sel:DWORD src1_sel:WORD_1
v_mfma_f32_16x16x32_bf16 v[240:243],a[244:247],v[24:27],v[240:243]
ds_read_u16 v61,v61
v_lshlrev_b32_sdwa v34,1,v34 dst_sel:DWORD dst_unused:UNUSED_PAD src0_sel:DWORD src1_sel:WORD_1
v_mfma_f32_16x16x32_bf16 v[244:247],a[244:247],v[28:31],v[244:247]
ds_read_u16_d16_hi v63,v63
v_lshlrev_b32_sdwa v33,1,v33 dst_sel:DWORD dst_unused:UNUSED_PAD src0_sel:DWORD src1_sel:WORD_1
v_mfma_f32_16x16x32_bf16 v[180:183],a[252:255],v[28:31],v[180:183]
ds_read_u16 v32,v32
v_lshlrev_b32_sdwa v35,1,v35 dst_sel:DWORD dst_unused:UNUSED_PAD src0_sel:DWORD src1_sel:WORD_1
v_mfma_f32_16x16x32_bf16 v[176:179],a[252:255],v[24:27],v[176:179]
s_waitcnt lgkmcnt(7)
ds_read_u16_d16_hi v34,v34
v_mfma_f32_16x16x32_bf16 v[248:251],a[240:243],v[24:27],v[248:251]
ds_read_u16 v33,v33
v_mfma_f32_16x16x32_bf16 v[252:255],a[240:243],v[28:31],v[252:255]
ds_read_u16_d16_hi v35,v35
v_mfma_f32_16x16x32_bf16 v[196:199],a[224:227],v[28:31],v[196:199]
v_mfma_f32_16x16x32_bf16 v[192:195],a[224:227],v[24:27],v[192:195]
v_mfma_f32_16x16x32_bf16 v[28:31],a[216:219],a[8:11],v[16:19]
v_lshlrev_b32_sdwa v48,1,v48 dst_sel:DWORD dst_unused:UNUSED_PAD src0_sel:DWORD src1_sel:WORD_1
v_fma_mixlo_f16 v8,v53,s99,0
v_mfma_f32_16x16x32_bf16 v[28:31],a[220:223],a[24:27],v[28:31]
ds_read_u16 v48,v48
v_lshlrev_b32_sdwa v50,1,v50 dst_sel:DWORD dst_unused:UNUSED_PAD src0_sel:DWORD src1_sel:WORD_1
v_mfma_f32_16x16x32_bf16 v[28:31],a[192:195],a[40:43],v[28:31]
ds_read_u16_d16_hi v50,v50
v_lshl_add_u64 v[40:41],v[40:41],0,v[42:43]
v_mfma_f32_16x16x32_bf16 v[24:27],a[196:199],a[40:43],v[20:23]
s_waitcnt lgkmcnt(5)
v_lshl_add_u64 v[42:43],v[36:37],0,v[38:39]
v_fma_mixhi_f16 v8,v55,s99,0
v_mfma_f32_16x16x32_bf16 v[24:27],a[200:203],a[24:27],v[24:27]
v_lshl_add_u64 v[36:37],v[60:61],0,v[62:63]
v_lshlrev_b32_sdwa v49,1,v49 dst_sel:DWORD dst_unused:UNUSED_PAD src0_sel:DWORD src1_sel:WORD_1
v_pk_fma_f16 v9,v8,v8,s54
v_mfma_f32_16x16x32_bf16 v[24:27],a[212:215],a[8:11],v[24:27]
ds_read_u16 v49,v49
v_lshlrev_b32_sdwa v51,1,v51 dst_sel:DWORD dst_unused:UNUSED_PAD src0_sel:DWORD src1_sel:WORD_1
v_mfma_f32_16x16x32_bf16 v[60:63],a[212:215],a[12:15],v[20:23]
ds_read_b128 v[20:23],v4 offset:384
v_pk_mul_f16 v9,v8,v9
v_mfma_f32_16x16x32_bf16 v[60:63],a[200:203],a[28:31],v[60:63]
s_waitcnt lgkmcnt(4)
ds_read_u16_d16_hi v51,v51
ds_read_b64_tr_b16 a[212:213],v1 offset:16416
v_lshl_add_u64 v[38:39],v[32:33],0,v[34:35]
v_lshlrev_b32_sdwa v44,1,v44 dst_sel:DWORD dst_unused:UNUSED_PAD src0_sel:DWORD src1_sel:WORD_1
v_mfma_f32_16x16x32_bf16 v[60:63],a[196:199],a[44:47],v[60:63]
ds_read_b64_tr_b16 a[214:215],v1 offset:17440
v_exp_f16_e64 v8,v9
v_mfma_f32_16x16x32_bf16 v[32:35],a[192:195],a[44:47],v[16:19]
ds_read_b128 v[16:19],v4 offset:416
ds_read_b64_tr_b16 a[200:201],v2 offset:16416
v_lshlrev_b32_sdwa v46,1,v46 dst_sel:DWORD dst_unused:UNUSED_PAD src0_sel:DWORD src1_sel:WORD_1
v_mfma_f32_16x16x32_bf16 v[32:35],a[220:223],a[28:31],v[32:35]
ds_read_b64_tr_b16 a[202:203],v2 offset:17440
ds_read_u16 v44,v44
v_lshlrev_b32_sdwa v45,1,v45 dst_sel:DWORD dst_unused:UNUSED_PAD src0_sel:DWORD src1_sel:WORD_1
v_mfma_f32_16x16x32_bf16 v[32:35],a[216:219],a[12:15],v[32:35]
ds_read_u16_d16_hi v46,v46
v_exp_f16_sdwa v8,v9 dst_sel:WORD_1 dst_unused:UNUSED_PRESERVE src0_sel:WORD_1
v_mfma_f32_16x16x32_bf16 v[208:211],a[232:235],v[40:43],v[208:211]
ds_read_u16 v45,v45
ds_read_b64_tr_b16 a[196:197],v2 offset:14368
v_lshlrev_b32_sdwa v47,1,v47 dst_sel:DWORD dst_unused:UNUSED_PAD src0_sel:DWORD src1_sel:WORD_1
v_mfma_f32_16x16x32_bf16 v[212:215],a[232:235],v[36:39],v[212:215]
ds_read_u16_d16_hi v47,v47
v_lshlrev_b32_sdwa v56,1,v56 dst_sel:DWORD dst_unused:UNUSED_PAD src0_sel:DWORD src1_sel:WORD_1
v_mfma_f32_16x16x32_bf16 v[172:175],a[236:239],v[36:39],v[172:175]
ds_read_b64_tr_b16 a[198:199],v2 offset:15392
v_pk_fma_f16 v8,v8,s55,s55
v_mfma_f32_16x16x32_bf16 v[168:171],a[236:239],v[40:43],v[168:171]
TP6483_%=:
s_cbranch_execz TR0_%=
v_rcp_f16 v9,v8
v_lshlrev_b32_sdwa v58,1,v58 dst_sel:DWORD dst_unused:UNUSED_PAD src0_sel:DWORD src1_sel:WORD_1
v_mfma_f32_16x16x32_bf16 v[216:219],a[244:247],v[40:43],v[216:219]
ds_read_u16 v56,v56
v_lshlrev_b32_sdwa v57,1,v57 dst_sel:DWORD dst_unused:UNUSED_PAD src0_sel:DWORD src1_sel:WORD_1
v_mfma_f32_16x16x32_bf16 v[220:223],a[244:247],v[36:39],v[220:223]
ds_read_u16_d16_hi v58,v58
v_lshlrev_b32_sdwa v59,1,v59 dst_sel:DWORD dst_unused:UNUSED_PAD src0_sel:DWORD src1_sel:WORD_1
v_mfma_f32_16x16x32_bf16 v[188:191],a[252:255],v[36:39],v[188:191]
ds_read_u16 v57,v57
v_lshlrev_b32_sdwa v52,1,v52 dst_sel:DWORD dst_unused:UNUSED_PAD src0_sel:DWORD src1_sel:WORD_1
v_mfma_f32_16x16x32_bf16 v[184:187],a[252:255],v[40:43],v[184:187]
ds_read_u16_d16_hi v59,v59
v_rcp_f16_sdwa v9,v8 dst_sel:WORD_1 dst_unused:UNUSED_PRESERVE src0_sel:WORD_1
v_mfma_f32_16x16x32_bf16 v[224:227],a[240:243],v[40:43],v[224:227]
ds_read_b64_tr_b16 a[192:193],v2 offset:12320
v_lshlrev_b32_sdwa v54,1,v54 dst_sel:DWORD dst_unused:UNUSED_PAD src0_sel:DWORD src1_sel:WORD_1
v_mfma_f32_16x16x32_bf16 v[228:231],a[240:243],v[36:39],v[228:231]
ds_read_b64_tr_b16 a[194:195],v2 offset:13344
v_fma_mix_f32 v53,v53,v9,0 op_sel_hi:[0,1,0]
v_mfma_f32_16x16x32_bf16 v[204:207],a[224:227],v[36:39],v[204:207]
ds_read_u16 v52,v52
v_fma_mix_f32 v55,v55,v9,0 op_sel:[0,1,0] op_sel_hi:[0,1,0]
v_mfma_f32_16x16x32_bf16 v[200:203],a[224:227],v[40:43],v[200:203]
s_waitcnt lgkmcnt(7)
ds_read_u16_d16_hi v54,v54
v_mfma_f32_16x16x32_bf16 v[36:39],a[208:211],a[48:51],v[20:23]
s_add_u32 m0,s57,53040
v_lshlrev_b32_sdwa v28,1,v28 dst_sel:DWORD dst_unused:UNUSED_PAD src0_sel:DWORD src1_sel:WORD_1
v_lshlrev_b32_sdwa v30,1,v30 dst_sel:DWORD dst_unused:UNUSED_PAD src0_sel:DWORD src1_sel:WORD_1
v_mfma_f32_16x16x32_bf16 v[36:39],a[204:207],a[64:67],v[36:39]
s_add_u32 s89,s56,6389760
ds_read_u16 v28,v28
v_lshlrev_b32_sdwa v29,1,v29 dst_sel:DWORD dst_unused:UNUSED_PAD src0_sel:DWORD src1_sel:WORD_1
v_mfma_f32_16x16x32_bf16 v[36:39],a[212:215],a[80:83],v[36:39]
buffer_load_dwordx4 v6,s[36:39],s89 offen lds
v_lshl_add_u64 v[48:49],v[48:49],0,v[50:51]
v_mfma_f32_16x16x32_bf16 v[40:43],a[200:203],a[80:83],v[16:19]
ds_read_u16_d16_hi v30,v30
v_lshl_add_u64 v[50:51],v[44:45],0,v[46:47]
v_mfma_f32_16x16x32_bf16 v[40:43],a[196:199],a[64:67],v[40:43]
s_waitcnt lgkmcnt(4)
ds_read_u16 v29,v29
v_lshl_add_u64 v[44:45],v[56:57],0,v[58:59]
v_mfma_f32_16x16x32_bf16 v[40:43],a[192:195],a[48:51],v[40:43]
v_lshlrev_b32_sdwa v31,1,v31 dst_sel:DWORD dst_unused:UNUSED_PAD src0_sel:DWORD src1_sel:WORD_1
v_lshlrev_b32_sdwa v24,1,v24 dst_sel:DWORD dst_unused:UNUSED_PAD src0_sel:DWORD src1_sel:WORD_1
v_mfma_f32_16x16x32_bf16 v[56:59],a[192:195],a[52:55],v[16:19]
s_waitcnt lgkmcnt(3)
ds_read_u16_d16_hi v31,v31
v_or_b32_e64 v46,v52,v54
v_mfma_f32_16x16x32_bf16 v[56:59],a[196:199],a[68:71],v[56:59]
ds_read_u16 v24,v24
v_cvt_pk_bf16_f32 v47,v53,v55
v_lshlrev_b32_sdwa v26,1,v26 dst_sel:DWORD dst_unused:UNUSED_PAD src0_sel:DWORD src1_sel:WORD_1
v_mfma_f32_16x16x32_bf16 v[56:59],a[200:203],a[84:87],v[56:59]
ds_read_u16_d16_hi v26,v26
v_lshlrev_b32_sdwa v25,1,v25 dst_sel:DWORD dst_unused:UNUSED_PAD src0_sel:DWORD src1_sel:WORD_1
v_mfma_f32_16x16x32_bf16 v[52:55],a[212:215],a[84:87],v[20:23]
ds_read_u16 v25,v25
v_lshlrev_b32_sdwa v27,1,v27 dst_sel:DWORD dst_unused:UNUSED_PAD src0_sel:DWORD src1_sel:WORD_1
v_mfma_f32_16x16x32_bf16 v[52:55],a[204:207],a[68:71],v[52:55]
ds_read_u16_d16_hi v27,v27
v_lshlrev_b32_sdwa v32,1,v32 dst_sel:DWORD dst_unused:UNUSED_PAD src0_sel:DWORD src1_sel:WORD_1
v_mfma_f32_16x16x32_bf16 v[52:55],a[208:211],a[52:55],v[52:55]
ds_read_u16 v32,v32
v_lshlrev_b32_sdwa v34,1,v34 dst_sel:DWORD dst_unused:UNUSED_PAD src0_sel:DWORD src1_sel:WORD_1
v_mfma_f32_16x16x32_bf16 v[136:139],a[232:235],v[48:51],v[136:139]
ds_read_u16_d16_hi v34,v34
v_lshlrev_b32_sdwa v33,1,v33 dst_sel:DWORD dst_unused:UNUSED_PAD src0_sel:DWORD src1_sel:WORD_1
v_mfma_f32_16x16x32_bf16 v[140:143],a[232:235],v[44:47],v[140:143]
ds_read_u16 v33,v33
v_lshlrev_b32_sdwa v35,1,v35 dst_sel:DWORD dst_unused:UNUSED_PAD src0_sel:DWORD src1_sel:WORD_1
v_mfma_f32_16x16x32_bf16 v[68:71],a[236:239],v[44:47],v[68:71]
ds_read_u16_d16_hi v35,v35
ds_read_b64_tr_b16 a[248:249],v3 offset:18480
v_mfma_f32_16x16x32_bf16 v[64:67],a[236:239],v[48:51],v[64:67]
ds_read_b64_tr_b16 a[250:251],v3 offset:20016
v_lshlrev_b32_sdwa v60,1,v60 dst_sel:DWORD dst_unused:UNUSED_PAD src0_sel:DWORD src1_sel:WORD_1
v_mfma_f32_16x16x32_bf16 v[144:147],a[244:247],v[48:51],v[144:147]
ds_read_u16 v60,v60
v_lshlrev_b32_sdwa v62,1,v62 dst_sel:DWORD dst_unused:UNUSED_PAD src0_sel:DWORD src1_sel:WORD_1
v_mfma_f32_16x16x32_bf16 v[148:151],a[244:247],v[44:47],v[148:151]
ds_read_u16_d16_hi v62,v62
v_lshlrev_b32_sdwa v61,1,v61 dst_sel:DWORD dst_unused:UNUSED_PAD src0_sel:DWORD src1_sel:WORD_1
v_mfma_f32_16x16x32_bf16 v[84:87],a[252:255],v[44:47],v[84:87]
ds_read_u16 v61,v61
v_lshlrev_b32_sdwa v63,1,v63 dst_sel:DWORD dst_unused:UNUSED_PAD src0_sel:DWORD src1_sel:WORD_1
v_mfma_f32_16x16x32_bf16 v[80:83],a[252:255],v[48:51],v[80:83]
ds_read_u16_d16_hi v63,v63
v_mfma_f32_16x16x32_bf16 v[152:155],a[240:243],v[48:51],v[152:155]
ds_read_b64_tr_b16 a[228:229],v3 offset:18488
v_mfma_f32_16x16x32_bf16 v[156:159],a[240:243],v[44:47],v[156:159]
ds_read_b64_tr_b16 a[230:231],v3 offset:20024
v_mfma_f32_16x16x32_bf16 v[100:103],a[224:227],v[44:47],v[100:103]
v_mfma_f32_16x16x32_bf16 v[96:99],a[224:227],v[48:51],v[96:99]
v_mfma_f32_16x16x32_bf16 v[44:47],a[208:211],a[56:59],v[20:23]
ds_read_b64_tr_b16 a[220:221],v1 offset:18480
v_lshlrev_b32_sdwa v36,1,v36 dst_sel:DWORD dst_unused:UNUSED_PAD src0_sel:DWORD src1_sel:WORD_1
v_mfma_f32_16x16x32_bf16 v[44:47],a[204:207],a[72:75],v[44:47]
ds_read_u16 v36,v36
v_fma_mixlo_f16 v8,v57,s99,0
v_mfma_f32_16x16x32_bf16 v[44:47],a[212:215],a[88:91],v[44:47]
s_waitcnt lgkmcnt(6)
ds_read_b64_tr_b16 a[222:223],v1 offset:19504
v_lshl_add_u64 v[28:29],v[28:29],0,v[30:31]
v_lshlrev_b32_sdwa v38,1,v38 dst_sel:DWORD dst_unused:UNUSED_PAD src0_sel:DWORD src1_sel:WORD_1
v_mfma_f32_16x16x32_bf16 v[48:51],a[200:203],a[88:91],v[16:19]
ds_read_u16_d16_hi v38,v38
v_lshl_add_u64 v[30:31],v[24:25],0,v[26:27]
v_fma_mixhi_f16 v8,v59,s99,0
v_mfma_f32_16x16x32_bf16 v[48:51],a[196:199],a[72:75],v[48:51]
ds_read_b64_tr_b16 a[216:217],v1 offset:20528
v_lshl_add_u64 v[24:25],v[32:33],0,v[34:35]
v_lshlrev_b32_sdwa v37,1,v37 dst_sel:DWORD dst_unused:UNUSED_PAD src0_sel:DWORD src1_sel:WORD_1
v_pk_fma_f16 v9,v8,v8,s54
v_mfma_f32_16x16x32_bf16 v[48:51],a[192:195],a[56:59],v[48:51]
ds_read_u16 v37,v37
v_lshlrev_b32_sdwa v39,1,v39 dst_sel:DWORD dst_unused:UNUSED_PAD src0_sel:DWORD src1_sel:WORD_1
v_mfma_f32_16x16x32_bf16 v[32:35],a[192:195],a[60:63],v[16:19]
ds_read_u16_d16_hi v39,v39
v_pk_mul_f16 v9,v8,v9
v_mfma_f32_16x16x32_bf16 v[32:35],a[196:199],a[76:79],v[32:35]
s_waitcnt lgkmcnt(7)
ds_read_b64_tr_b16 a[218:219],v1 offset:21552
v_lshl_add_u64 v[26:27],v[60:61],0,v[62:63]
v_lshlrev_b32_sdwa v40,1,v40 dst_sel:DWORD dst_unused:UNUSED_PAD src0_sel:DWORD src1_sel:WORD_1
v_mfma_f32_16x16x32_bf16 v[32:35],a[200:203],a[92:95],v[32:35]
v_exp_f16 v8,v9
v_mfma_f32_16x16x32_bf16 v[60:63],a[212:215],a[92:95],v[20:23]
ds_read_u16 v40,v40
v_lshlrev_b32_sdwa v42,1,v42 dst_sel:DWORD dst_unused:UNUSED_PAD src0_sel:DWORD src1_sel:WORD_1
v_mfma_f32_16x16x32_bf16 v[60:63],a[204:207],a[76:79],v[60:63]
ds_read_u16_d16_hi v42,v42
v_lshlrev_b32_sdwa v41,1,v41 dst_sel:DWORD dst_unused:UNUSED_PAD src0_sel:DWORD src1_sel:WORD_1
v_mfma_f32_16x16x32_bf16 v[60:63],a[208:211],a[60:63],v[60:63]
v_exp_f16_sdwa v8,v9 dst_sel:WORD_1 dst_unused:UNUSED_PRESERVE src0_sel:WORD_1
v_mfma_f32_16x16x32_bf16 v[112:115],a[232:235],v[28:31],v[112:115]
ds_read_u16 v41,v41
v_lshlrev_b32_sdwa v43,1,v43 dst_sel:DWORD dst_unused:UNUSED_PAD src0_sel:DWORD src1_sel:WORD_1
v_mfma_f32_16x16x32_bf16 v[116:119],a[232:235],v[24:27],v[116:119]
ds_read_u16_d16_hi v43,v43
v_lshlrev_b32_sdwa v52,1,v52 dst_sel:DWORD dst_unused:UNUSED_PAD src0_sel:DWORD src1_sel:WORD_1
v_mfma_f32_16x16x32_bf16 v[76:79],a[236:239],v[24:27],v[76:79]
ds_read_u16 v52,v52
v_pk_fma_f16 v8,v8,s55,s55
v_mfma_f32_16x16x32_bf16 v[72:75],a[236:239],v[28:31],v[72:75]
ds_read_b64_tr_b16 a[232:233],v3 offset:18544
v_rcp_f16_e64 v9,v8
v_lshlrev_b32_sdwa v54,1,v54 dst_sel:DWORD dst_unused:UNUSED_PAD src0_sel:DWORD src1_sel:WORD_1
v_mfma_f32_16x16x32_bf16 v[120:123],a[244:247],v[28:31],v[120:123]
ds_read_u16_d16_hi v54,v54
v_lshlrev_b32_sdwa v53,1,v53 dst_sel:DWORD dst_unused:UNUSED_PAD src0_sel:DWORD src1_sel:WORD_1
v_mfma_f32_16x16x32_bf16 v[124:127],a[244:247],v[24:27],v[124:127]
ds_read_u16 v53,v53
ds_read_b64_tr_b16 a[234:235],v3 offset:20080
v_mfma_f32_16x16x32_bf16 v[92:95],a[252:255],v[24:27],v[92:95]
ds_read_b64_tr_b16 a[236:237],v3 offset:18552
v_lshlrev_b32_sdwa v55,1,v55 dst_sel:DWORD dst_unused:UNUSED_PAD src0_sel:DWORD src1_sel:WORD_1
v_mfma_f32_16x16x32_bf16 v[88:91],a[252:255],v[28:31],v[88:91]
ds_read_u16_d16_hi v55,v55
v_rcp_f16_sdwa v9,v8 dst_sel:WORD_1 dst_unused:UNUSED_PRESERVE src0_sel:WORD_1
v_mfma_f32_16x16x32_bf16 v[128:131],a[240:243],v[28:31],v[128:131]
ds_read_b64_tr_b16 a[238:239],v3 offset:20088
ds_read_b64_tr_b16 a[244:245],v3 offset:18608
v_lshlrev_b32_sdwa v56,1,v56 dst_sel:DWORD dst_unused:UNUSED_PAD src0_sel:DWORD src1_sel:WORD_1
v_mfma_f32_16x16x32_bf16 v[132:135],a[240:243],v[24:27],v[132:135]
ds_read_u16 v56,v56
v_lshlrev_b32_sdwa v58,1,v58 dst_sel:DWORD dst_unused:UNUSED_PAD src0_sel:DWORD src1_sel:WORD_1
v_mfma_f32_16x16x32_bf16 v[108:111],a[224:227],v[24:27],v[108:111]
ds_read_u16_d16_hi v58,v58
v_fma_mix_f32 v57,v57,v9,0 op_sel_hi:[0,1,0]
v_mfma_f32_16x16x32_bf16 v[104:107],a[224:227],v[28:31],v[104:107]
ds_read_b64_tr_b16 a[246:247],v3 offset:20144
v_fma_mix_f32 v59,v59,v9,0 op_sel:[0,1,0] op_sel_hi:[0,1,0]
v_mfma_f32_16x16x32_bf16 v[24:27],a[208:211],a[0:3],v[20:23]
ds_read_b64_tr_b16 a[252:253],v3 offset:18616
v_lshlrev_b32_sdwa v44,1,v44 dst_sel:DWORD dst_unused:UNUSED_PAD src0_sel:DWORD src1_sel:WORD_1
v_mfma_f32_16x16x32_bf16 v[24:27],a[204:207],a[16:19],v[24:27]
ds_read_b64_tr_b16 a[254:255],v3 offset:20152
v_lshlrev_b32_sdwa v46,1,v46 dst_sel:DWORD dst_unused:UNUSED_PAD src0_sel:DWORD src1_sel:WORD_1
v_mfma_f32_16x16x32_bf16 v[24:27],a[212:215],a[32:35],v[24:27]
s_waitcnt lgkmcnt(9)
ds_read_u16 v44,v44
v_lshl_add_u64 v[36:37],v[36:37],0,v[38:39]
v_mfma_f32_16x16x32_bf16 v[28:31],a[200:203],a[32:35],v[16:19]
ds_read_u16_d16_hi v46,v46
v_lshl_add_u64 v[38:39],v[40:41],0,v[42:43]
v_lshlrev_b32_sdwa v45,1,v45 dst_sel:DWORD dst_unused:UNUSED_PAD src0_sel:DWORD src1_sel:WORD_1
v_mfma_f32_16x16x32_bf16 v[28:31],a[196:199],a[16:19],v[28:31]
s_waitcnt lgkmcnt(9)
ds_read_u16 v45,v45
v_lshl_add_u64 v[40:41],v[52:53],0,v[54:55]
v_mfma_f32_16x16x32_bf16 v[28:31],a[192:195],a[0:3],v[28:31]
v_lshlrev_b32_sdwa v47,1,v47 dst_sel:DWORD dst_unused:UNUSED_PAD src0_sel:DWORD src1_sel:WORD_1
v_lshlrev_b32_sdwa v48,1,v48 dst_sel:DWORD dst_unused:UNUSED_PAD src0_sel:DWORD src1_sel:WORD_1
v_mfma_f32_16x16x32_bf16 v[52:55],a[192:195],a[4:7],v[16:19]
s_waitcnt lgkmcnt(6)
ds_read_u16_d16_hi v47,v47
v_or_b32 v42,v56,v58
v_mfma_f32_16x16x32_bf16 v[52:55],a[196:199],a[20:23],v[52:55]
ds_read_u16 v48,v48
v_cvt_pk_bf16_f32 v43,v57,v59
v_lshlrev_b32_sdwa v50,1,v50 dst_sel:DWORD dst_unused:UNUSED_PAD src0_sel:DWORD src1_sel:WORD_1
v_mfma_f32_16x16x32_bf16 v[52:55],a[200:203],a[36:39],v[52:55]
ds_read_u16_d16_hi v50,v50
v_lshlrev_b32_sdwa v49,1,v49 dst_sel:DWORD dst_unused:UNUSED_PAD src0_sel:DWORD src1_sel:WORD_1
v_mfma_f32_16x16x32_bf16 v[56:59],a[212:215],a[36:39],v[20:23]
ds_read_u16 v49,v49
v_lshlrev_b32_sdwa v51,1,v51 dst_sel:DWORD dst_unused:UNUSED_PAD src0_sel:DWORD src1_sel:WORD_1
v_mfma_f32_16x16x32_bf16 v[56:59],a[204:207],a[20:23],v[56:59]
ds_read_u16_d16_hi v51,v51
v_lshlrev_b32_sdwa v60,1,v60 dst_sel:DWORD dst_unused:UNUSED_PAD src0_sel:DWORD src1_sel:WORD_1
v_mfma_f32_16x16x32_bf16 v[56:59],a[208:211],a[4:7],v[56:59]
ds_read_u16 v60,v60
v_lshlrev_b32_sdwa v62,1,v62 dst_sel:DWORD dst_unused:UNUSED_PAD src0_sel:DWORD src1_sel:WORD_1
v_mfma_f32_16x16x32_bf16 v[232:235],a[248:251],v[36:39],v[232:235]
ds_read_u16_d16_hi v62,v62
v_lshlrev_b32_sdwa v61,1,v61 dst_sel:DWORD dst_unused:UNUSED_PAD src0_sel:DWORD src1_sel:WORD_1
v_mfma_f32_16x16x32_bf16 v[236:239],a[248:251],v[40:43],v[236:239]
ds_read_u16 v61,v61
v_lshlrev_b32_sdwa v63,1,v63 dst_sel:DWORD dst_unused:UNUSED_PAD src0_sel:DWORD src1_sel:WORD_1
v_mfma_f32_16x16x32_bf16 v[164:167],a[228:231],v[40:43],v[164:167]
ds_read_u16_d16_hi v63,v63
v_lshlrev_b32_sdwa v32,1,v32 dst_sel:DWORD dst_unused:UNUSED_PAD src0_sel:DWORD src1_sel:WORD_1
v_mfma_f32_16x16x32_bf16 v[160:163],a[228:231],v[36:39],v[160:163]
ds_read_u16 v32,v32
v_lshlrev_b32_sdwa v34,1,v34 dst_sel:DWORD dst_unused:UNUSED_PAD src0_sel:DWORD src1_sel:WORD_1
v_mfma_f32_16x16x32_bf16 v[240:243],a[232:235],v[36:39],v[240:243]
ds_read_u16_d16_hi v34,v34
v_lshlrev_b32_sdwa v33,1,v33 dst_sel:DWORD dst_unused:UNUSED_PAD src0_sel:DWORD src1_sel:WORD_1
v_mfma_f32_16x16x32_bf16 v[244:247],a[232:235],v[40:43],v[244:247]
ds_read_u16 v33,v33
v_lshlrev_b32_sdwa v35,1,v35 dst_sel:DWORD dst_unused:UNUSED_PAD src0_sel:DWORD src1_sel:WORD_1
v_mfma_f32_16x16x32_bf16 v[180:183],a[236:239],v[40:43],v[180:183]
ds_read_u16_d16_hi v35,v35
v_mfma_f32_16x16x32_bf16 v[176:179],a[236:239],v[36:39],v[176:179]
s_waitcnt lgkmcnt(8)
v_mfma_f32_16x16x32_bf16 v[248:251],a[244:247],v[36:39],v[248:251]
v_mfma_f32_16x16x32_bf16 v[252:255],a[244:247],v[40:43],v[252:255]
v_mfma_f32_16x16x32_bf16 v[196:199],a[252:255],v[40:43],v[196:199]
v_mfma_f32_16x16x32_bf16 v[192:195],a[252:255],v[36:39],v[192:195]
v_mfma_f32_16x16x32_bf16 v[40:43],a[208:211],a[8:11],v[20:23]
v_lshlrev_b32_sdwa v24,1,v24 dst_sel:DWORD dst_unused:UNUSED_PAD src0_sel:DWORD src1_sel:WORD_1
v_fma_mixlo_f16 v8,v53,s99,0
v_mfma_f32_16x16x32_bf16 v[40:43],a[204:207],a[24:27],v[40:43]
ds_read_u16 v24,v24
v_lshlrev_b32_sdwa v26,1,v26 dst_sel:DWORD dst_unused:UNUSED_PAD src0_sel:DWORD src1_sel:WORD_1
v_mfma_f32_16x16x32_bf16 v[40:43],a[212:215],a[40:43],v[40:43]
ds_read_u16_d16_hi v26,v26
v_lshl_add_u64 v[44:45],v[44:45],0,v[46:47]
v_mfma_f32_16x16x32_bf16 v[36:39],a[200:203],a[40:43],v[16:19]
v_lshl_add_u64 v[46:47],v[48:49],0,v[50:51]
v_fma_mixhi_f16 v8,v55,s99,0
v_mfma_f32_16x16x32_bf16 v[36:39],a[196:199],a[24:27],v[36:39]
s_waitcnt lgkmcnt(2)
v_lshl_add_u64 v[48:49],v[60:61],0,v[62:63]
v_lshlrev_b32_sdwa v25,1,v25 dst_sel:DWORD dst_unused:UNUSED_PAD src0_sel:DWORD src1_sel:WORD_1
v_pk_fma_f16 v9,v8,v8,s54
v_mfma_f32_16x16x32_bf16 v[36:39],a[192:195],a[8:11],v[36:39]
ds_read_u16 v25,v25
v_lshlrev_b32_sdwa v27,1,v27 dst_sel:DWORD dst_unused:UNUSED_PAD src0_sel:DWORD src1_sel:WORD_1
v_mfma_f32_16x16x32_bf16 v[60:63],a[192:195],a[12:15],v[16:19]
ds_read_b128 v[16:19],v4 offset:512
v_pk_mul_f16 v9,v8,v9
v_mfma_f32_16x16x32_bf16 v[60:63],a[196:199],a[28:31],v[60:63]
ds_read_u16_d16_hi v27,v27
ds_read_b64_tr_b16 a[192:193],v1 offset:22576
v_lshl_add_u64 v[50:51],v[32:33],0,v[34:35]
v_lshlrev_b32_sdwa v28,1,v28 dst_sel:DWORD dst_unused:UNUSED_PAD src0_sel:DWORD src1_sel:WORD_1
v_mfma_f32_16x16x32_bf16 v[60:63],a[200:203],a[44:47],v[60:63]
ds_read_b64_tr_b16 a[194:195],v1 offset:23600
v_exp_f16 v8,v9
v_mfma_f32_16x16x32_bf16 v[32:35],a[212:215],a[44:47],v[20:23]
ds_read_b128 v[20:23],v4 offset:544
ds_read_b64_tr_b16 a[196:197],v2 offset:22576
v_lshlrev_b32_sdwa v30,1,v30 dst_sel:DWORD dst_unused:UNUSED_PAD src0_sel:DWORD src1_sel:WORD_1
v_mfma_f32_16x16x32_bf16 v[32:35],a[204:207],a[28:31],v[32:35]
ds_read_b64_tr_b16 a[198:199],v2 offset:23600
ds_read_u16 v28,v28
v_lshlrev_b32_sdwa v29,1,v29 dst_sel:DWORD dst_unused:UNUSED_PAD src0_sel:DWORD src1_sel:WORD_1
v_mfma_f32_16x16x32_bf16 v[32:35],a[208:211],a[12:15],v[32:35]
ds_read_u16_d16_hi v30,v30
v_exp_f16_sdwa v8,v9 dst_sel:WORD_1 dst_unused:UNUSED_PRESERVE src0_sel:WORD_1
v_mfma_f32_16x16x32_bf16 v[208:211],a[248:251],v[44:47],v[208:211]
ds_read_u16 v29,v29
ds_read_b64_tr_b16 a[200:201],v2 offset:20528
v_lshlrev_b32_sdwa v31,1,v31 dst_sel:DWORD dst_unused:UNUSED_PAD src0_sel:DWORD src1_sel:WORD_1
v_mfma_f32_16x16x32_bf16 v[212:215],a[248:251],v[48:51],v[212:215]
ds_read_u16_d16_hi v31,v31
v_lshlrev_b32_sdwa v56,1,v56 dst_sel:DWORD dst_unused:UNUSED_PAD src0_sel:DWORD src1_sel:WORD_1
v_mfma_f32_16x16x32_bf16 v[172:175],a[228:231],v[48:51],v[172:175]
ds_read_b64_tr_b16 a[202:203],v2 offset:21552
v_pk_fma_f16 v8,v8,s55,s55
v_mfma_f32_16x16x32_bf16 v[168:171],a[228:231],v[44:47],v[168:171]
v_rcp_f16 v9,v8
v_lshlrev_b32_sdwa v58,1,v58 dst_sel:DWORD dst_unused:UNUSED_PAD src0_sel:DWORD src1_sel:WORD_1
v_mfma_f32_16x16x32_bf16 v[216:219],a[232:235],v[44:47],v[216:219]
ds_read_u16 v56,v56
v_lshlrev_b32_sdwa v57,1,v57 dst_sel:DWORD dst_unused:UNUSED_PAD src0_sel:DWORD src1_sel:WORD_1
v_mfma_f32_16x16x32_bf16 v[220:223],a[232:235],v[48:51],v[220:223]
ds_read_u16_d16_hi v58,v58
v_lshlrev_b32_sdwa v59,1,v59 dst_sel:DWORD dst_unused:UNUSED_PAD src0_sel:DWORD src1_sel:WORD_1
v_mfma_f32_16x16x32_bf16 v[188:191],a[236:239],v[48:51],v[188:191]
ds_read_u16 v57,v57
v_lshlrev_b32_sdwa v52,1,v52 dst_sel:DWORD dst_unused:UNUSED_PAD src0_sel:DWORD src1_sel:WORD_1
v_mfma_f32_16x16x32_bf16 v[184:187],a[236:239],v[44:47],v[184:187]
ds_read_u16_d16_hi v59,v59
v_rcp_f16_sdwa v9,v8 dst_sel:WORD_1 dst_unused:UNUSED_PRESERVE src0_sel:WORD_1
v_mfma_f32_16x16x32_bf16 v[224:227],a[244:247],v[44:47],v[224:227]
ds_read_b64_tr_b16 a[212:213],v2 offset:18480
v_lshlrev_b32_sdwa v54,1,v54 dst_sel:DWORD dst_unused:UNUSED_PAD src0_sel:DWORD src1_sel:WORD_1
v_mfma_f32_16x16x32_bf16 v[228:231],a[244:247],v[48:51],v[228:231]
ds_read_b64_tr_b16 a[214:215],v2 offset:19504
v_fma_mix_f32 v53,v53,v9,0 op_sel_hi:[0,1,0]
v_mfma_f32_16x16x32_bf16 v[204:207],a[252:255],v[48:51],v[204:207]
ds_read_u16 v52,v52
v_fma_mix_f32 v55,v55,v9,0 op_sel:[0,1,0] op_sel_hi:[0,1,0]
v_mfma_f32_16x16x32_bf16 v[200:203],a[252:255],v[44:47],v[200:203]
s_waitcnt lgkmcnt(7)
ds_read_u16_d16_hi v54,v54
v_mfma_f32_16x16x32_bf16 v[48:51],a[220:223],a[48:51],v[16:19]
v_lshlrev_b32_sdwa v40,1,v40 dst_sel:DWORD dst_unused:UNUSED_PAD src0_sel:DWORD src1_sel:WORD_1
v_lshlrev_b32_sdwa v42,1,v42 dst_sel:DWORD dst_unused:UNUSED_PAD src0_sel:DWORD src1_sel:WORD_1
v_mfma_f32_16x16x32_bf16 v[48:51],a[216:219],a[64:67],v[48:51]
ds_read_u16 v40,v40
v_lshlrev_b32_sdwa v41,1,v41 dst_sel:DWORD dst_unused:UNUSED_PAD src0_sel:DWORD src1_sel:WORD_1
v_mfma_f32_16x16x32_bf16 v[48:51],a[192:195],a[80:83],v[48:51]
ds_read_u16_d16_hi v42,v42
v_lshl_add_u64 v[24:25],v[24:25],0,v[26:27]
v_mfma_f32_16x16x32_bf16 v[44:47],a[196:199],a[80:83],v[20:23]
ds_read_u16 v41,v41
v_lshl_add_u64 v[26:27],v[28:29],0,v[30:31]
v_mfma_f32_16x16x32_bf16 v[44:47],a[200:203],a[64:67],v[44:47]
s_waitcnt lgkmcnt(5)
v_lshl_add_u64 v[28:29],v[56:57],0,v[58:59]
v_lshlrev_b32_sdwa v43,1,v43 dst_sel:DWORD dst_unused:UNUSED_PAD src0_sel:DWORD src1_sel:WORD_1
v_mfma_f32_16x16x32_bf16 v[44:47],a[212:215],a[48:51],v[44:47]
ds_read_u16_d16_hi v43,v43
v_lshlrev_b32_sdwa v36,1,v36 dst_sel:DWORD dst_unused:UNUSED_PAD src0_sel:DWORD src1_sel:WORD_1
v_mfma_f32_16x16x32_bf16 v[56:59],a[212:215],a[52:55],v[20:23]
s_waitcnt lgkmcnt(4)
ds_read_u16 v36,v36
v_or_b32 v30,v52,v54
v_mfma_f32_16x16x32_bf16 v[56:59],a[200:203],a[68:71],v[56:59]
s_waitcnt vmcnt(6)
v_cvt_pk_bf16_f32 v31,v53,v55
v_lshlrev_b32_sdwa v38,1,v38 dst_sel:DWORD dst_unused:UNUSED_PAD src0_sel:DWORD src1_sel:WORD_1
v_mfma_f32_16x16x32_bf16 v[56:59],a[196:199],a[84:87],v[56:59]
s_barrier
ds_read_u16_d16_hi v38,v38
v_lshlrev_b32_sdwa v37,1,v37 dst_sel:DWORD dst_unused:UNUSED_PAD src0_sel:DWORD src1_sel:WORD_1
v_mfma_f32_16x16x32_bf16 v[52:55],a[192:195],a[84:87],v[16:19]
ds_read_u16 v37,v37
v_lshlrev_b32_sdwa v39,1,v39 dst_sel:DWORD dst_unused:UNUSED_PAD src0_sel:DWORD src1_sel:WORD_1
v_mfma_f32_16x16x32_bf16 v[52:55],a[216:219],a[68:71],v[52:55]
s_add_u32 m0,s58,12320
ds_read_u16_d16_hi v39,v39
v_lshlrev_b32_sdwa v32,1,v32 dst_sel:DWORD dst_unused:UNUSED_PAD src0_sel:DWORD src1_sel:WORD_1
v_mfma_f32_16x16x32_bf16 v[52:55],a[220:223],a[52:55],v[52:55]
s_add_u32 s69,s69,s59
ds_read_u16 v32,v32
v_lshlrev_b32_sdwa v34,1,v34 dst_sel:DWORD dst_unused:UNUSED_PAD src0_sel:DWORD src1_sel:WORD_1
v_mfma_f32_16x16x32_bf16 v[136:139],a[248:251],v[24:27],v[136:139]
buffer_load_dwordx4 v5,s[44:47],s69 offen offset:0 lds
v_lshlrev_b32_sdwa v33,1,v33 dst_sel:DWORD dst_unused:UNUSED_PAD src0_sel:DWORD src1_sel:WORD_1
v_mfma_f32_16x16x32_bf16 v[140:143],a[248:251],v[28:31],v[140:143]
ds_read_u16_d16_hi v34,v34
v_lshlrev_b32_sdwa v35,1,v35 dst_sel:DWORD dst_unused:UNUSED_PAD src0_sel:DWORD src1_sel:WORD_1
v_mfma_f32_16x16x32_bf16 v[68:71],a[228:231],v[28:31],v[68:71]
ds_read_u16 v33,v33
ds_read_u16_d16_hi v35,v35
v_mfma_f32_16x16x32_bf16 v[64:67],a[228:231],v[24:27],v[64:67]
ds_read_b64_tr_b16 a[240:241],v3 offset:0
v_lshlrev_b32_sdwa v60,1,v60 dst_sel:DWORD dst_unused:UNUSED_PAD src0_sel:DWORD src1_sel:WORD_1
v_mfma_f32_16x16x32_bf16 v[144:147],a[232:235],v[24:27],v[144:147]
s_add_u32 s69,s69,s53
ds_read_u16 v60,v60
v_lshlrev_b32_sdwa v62,1,v62 dst_sel:DWORD dst_unused:UNUSED_PAD src0_sel:DWORD src1_sel:WORD_1
v_mfma_f32_16x16x32_bf16 v[148:151],a[232:235],v[28:31],v[148:151]
buffer_load_dwordx4 v5,s[44:47],s69 offen offset:1024 lds
ds_read_u16_d16_hi v62,v62
v_mfma_f32_16x16x32_bf16 v[84:87],a[236:239],v[28:31],v[84:87]
ds_read_b64_tr_b16 a[242:243],v3 offset:1536
v_lshlrev_b32_sdwa v61,1,v61 dst_sel:DWORD dst_unused:UNUSED_PAD src0_sel:DWORD src1_sel:WORD_1
v_mfma_f32_16x16x32_bf16 v[80:83],a[236:239],v[24:27],v[80:83]
ds_read_u16 v61,v61
v_lshlrev_b32_sdwa v63,1,v63 dst_sel:DWORD dst_unused:UNUSED_PAD src0_sel:DWORD src1_sel:WORD_1
v_mfma_f32_16x16x32_bf16 v[152:155],a[244:247],v[24:27],v[152:155]
ds_read_u16_d16_hi v63,v63
v_mfma_f32_16x16x32_bf16 v[156:159],a[244:247],v[28:31],v[156:159]
s_add_u32 s69,s69,s53
ds_read_b64_tr_b16 a[224:225],v3 offset:8
v_mfma_f32_16x16x32_bf16 v[100:103],a[252:255],v[28:31],v[100:103]
buffer_load_dwordx4 v5,s[44:47],s69 offen offset:2048 lds
v_mfma_f32_16x16x32_bf16 v[96:99],a[252:255],v[24:27],v[96:99]
ds_read_b64_tr_b16 a[226:227],v3 offset:1544
v_mfma_f32_16x16x32_bf16 v[28:31],a[220:223],a[56:59],v[16:19]
ds_read_b64_tr_b16 a[204:205],v1 offset:0
v_lshlrev_b32_sdwa v48,1,v48 dst_sel:DWORD dst_unused:UNUSED_PAD src0_sel:DWORD src1_sel:WORD_1
v_mfma_f32_16x16x32_bf16 v[28:31],a[216:219],a[72:75],v[28:31]
ds_read_u16 v48,v48
v_fma_mixlo_f16 v8,v57,s99,0
v_mfma_f32_16x16x32_bf16 v[28:31],a[192:195],a[88:91],v[28:31]
s_waitcnt lgkmcnt(7)
ds_read_b64_tr_b16 a[206:207],v1 offset:1024
v_lshl_add_u64 v[40:41],v[40:41],0,v[42:43]
v_lshlrev_b32_sdwa v50,1,v50 dst_sel:DWORD dst_unused:UNUSED_PAD src0_sel:DWORD src1_sel:WORD_1
v_mfma_f32_16x16x32_bf16 v[24:27],a[196:199],a[88:91],v[20:23]
ds_read_u16_d16_hi v50,v50
v_lshl_add_u64 v[42:43],v[36:37],0,v[38:39]
v_fma_mixhi_f16 v8,v59,s99,0
v_mfma_f32_16x16x32_bf16 v[24:27],a[200:203],a[72:75],v[24:27]
ds_read_b64_tr_b16 a[208:209],v1 offset:2048
v_lshl_add_u64 v[36:37],v[32:33],0,v[34:35]
v_lshlrev_b32_sdwa v49,1,v49 dst_sel:DWORD dst_unused:UNUSED_PAD src0_sel:DWORD src1_sel:WORD_1
v_pk_fma_f16 v9,v8,v8,s54
v_mfma_f32_16x16x32_bf16 v[24:27],a[212:215],a[56:59],v[24:27]
ds_read_u16 v49,v49
v_lshlrev_b32_sdwa v51,1,v51 dst_sel:DWORD dst_unused:UNUSED_PAD src0_sel:DWORD src1_sel:WORD_1
v_mfma_f32_16x16x32_bf16 v[32:35],a[212:215],a[60:63],v[20:23]
ds_read_u16_d16_hi v51,v51
v_pk_mul_f16 v9,v8,v9
v_mfma_f32_16x16x32_bf16 v[32:35],a[200:203],a[76:79],v[32:35]
s_waitcnt lgkmcnt(8)
ds_read_b64_tr_b16 a[210:211],v1 offset:3072
v_lshl_add_u64 v[38:39],v[60:61],0,v[62:63]
v_lshlrev_b32_sdwa v44,1,v44 dst_sel:DWORD dst_unused:UNUSED_PAD src0_sel:DWORD src1_sel:WORD_1
v_mfma_f32_16x16x32_bf16 v[32:35],a[196:199],a[92:95],v[32:35]
v_exp_f16 v8,v9
v_mfma_f32_16x16x32_bf16 v[60:63],a[192:195],a[92:95],v[16:19]
ds_read_u16 v44,v44
v_lshlrev_b32_sdwa v46,1,v46 dst_sel:DWORD dst_unused:UNUSED_PAD src0_sel:DWORD src1_sel:WORD_1
v_mfma_f32_16x16x32_bf16 v[60:63],a[216:219],a[76:79],v[60:63]
ds_read_u16_d16_hi v46,v46
v_lshlrev_b32_sdwa v45,1,v45 dst_sel:DWORD dst_unused:UNUSED_PAD src0_sel:DWORD src1_sel:WORD_1
v_mfma_f32_16x16x32_bf16 v[60:63],a[220:223],a[60:63],v[60:63]
v_exp_f16_sdwa v8,v9 dst_sel:WORD_1 dst_unused:UNUSED_PRESERVE src0_sel:WORD_1
v_mfma_f32_16x16x32_bf16 v[112:115],a[248:251],v[40:43],v[112:115]
ds_read_u16 v45,v45
v_lshlrev_b32_sdwa v47,1,v47 dst_sel:DWORD dst_unused:UNUSED_PAD src0_sel:DWORD src1_sel:WORD_1
v_mfma_f32_16x16x32_bf16 v[116:119],a[248:251],v[36:39],v[116:119]
ds_read_u16_d16_hi v47,v47
v_lshlrev_b32_sdwa v52,1,v52 dst_sel:DWORD dst_unused:UNUSED_PAD src0_sel:DWORD src1_sel:WORD_1
v_mfma_f32_16x16x32_bf16 v[76:79],a[228:231],v[36:39],v[76:79]
ds_read_u16 v52,v52
v_pk_fma_f16 v8,v8,s55,s55
v_mfma_f32_16x16x32_bf16 v[72:75],a[228:231],v[40:43],v[72:75]
ds_read_b64_tr_b16 a[248:249],v3 offset:64
v_rcp_f16_e64 v9,v8
v_lshlrev_b32_sdwa v54,1,v54 dst_sel:DWORD dst_unused:UNUSED_PAD src0_sel:DWORD src1_sel:WORD_1
v_mfma_f32_16x16x32_bf16 v[120:123],a[232:235],v[40:43],v[120:123]
ds_read_u16_d16_hi v54,v54
v_lshlrev_b32_sdwa v53,1,v53 dst_sel:DWORD dst_unused:UNUSED_PAD src0_sel:DWORD src1_sel:WORD_1
v_mfma_f32_16x16x32_bf16 v[124:127],a[232:235],v[36:39],v[124:127]
ds_read_u16 v53,v53
ds_read_b64_tr_b16 a[250:251],v3 offset:1600
v_mfma_f32_16x16x32_bf16 v[92:95],a[236:239],v[36:39],v[92:95]
ds_read_b64_tr_b16 a[228:229],v3 offset:72
v_lshlrev_b32_sdwa v55,1,v55 dst_sel:DWORD dst_unused:UNUSED_PAD src0_sel:DWORD src1_sel:WORD_1
v_mfma_f32_16x16x32_bf16 v[88:91],a[236:239],v[40:43],v[88:91]
ds_read_u16_d16_hi v55,v55
v_rcp_f16_sdwa v9,v8 dst_sel:WORD_1 dst_unused:UNUSED_PRESERVE src0_sel:WORD_1
v_mfma_f32_16x16x32_bf16 v[128:131],a[244:247],v[40:43],v[128:131]
ds_read_b64_tr_b16 a[230:231],v3 offset:1608
ds_read_b64_tr_b16 a[232:233],v3 offset:128
v_lshlrev_b32_sdwa v56,1,v56 dst_sel:DWORD dst_unused:UNUSED_PAD src0_sel:DWORD src1_sel:WORD_1
v_mfma_f32_16x16x32_bf16 v[132:135],a[244:247],v[36:39],v[132:135]
ds_read_u16 v56,v56
v_lshlrev_b32_sdwa v58,1,v58 dst_sel:DWORD dst_unused:UNUSED_PAD src0_sel:DWORD src1_sel:WORD_1
v_mfma_f32_16x16x32_bf16 v[108:111],a[252:255],v[36:39],v[108:111]
ds_read_u16_d16_hi v58,v58
v_fma_mix_f32 v57,v57,v9,0 op_sel_hi:[0,1,0]
v_mfma_f32_16x16x32_bf16 v[104:107],a[252:255],v[40:43],v[104:107]
ds_read_b64_tr_b16 a[234:235],v3 offset:1664
v_fma_mix_f32 v59,v59,v9,0 op_sel:[0,1,0] op_sel_hi:[0,1,0]
v_mfma_f32_16x16x32_bf16 v[36:39],a[220:223],a[0:3],v[16:19]
s_add_u32 m0,s58,18480
ds_read_b64_tr_b16 a[236:237],v3 offset:136
v_lshlrev_b32_sdwa v28,1,v28 dst_sel:DWORD dst_unused:UNUSED_PAD src0_sel:DWORD src1_sel:WORD_1
v_mfma_f32_16x16x32_bf16 v[36:39],a[216:219],a[16:19],v[36:39]
s_add_u32 s69,s69,s59
ds_read_b64_tr_b16 a[238:239],v3 offset:1672
v_lshlrev_b32_sdwa v30,1,v30 dst_sel:DWORD dst_unused:UNUSED_PAD src0_sel:DWORD src1_sel:WORD_1
v_mfma_f32_16x16x32_bf16 v[36:39],a[192:195],a[32:35],v[36:39]
s_waitcnt lgkmcnt(9)
buffer_load_dwordx4 v5,s[44:47],s69 offen offset:0 lds
v_lshl_add_u64 v[48:49],v[48:49],0,v[50:51]
v_mfma_f32_16x16x32_bf16 v[40:43],a[196:199],a[32:35],v[20:23]
s_add_u32 s69,s69,s53
ds_read_u16 v28,v28
v_lshl_add_u64 v[50:51],v[44:45],0,v[46:47]
v_lshlrev_b32_sdwa v29,1,v29 dst_sel:DWORD dst_unused:UNUSED_PAD src0_sel:DWORD src1_sel:WORD_1
v_mfma_f32_16x16x32_bf16 v[40:43],a[200:203],a[16:19],v[40:43]
s_waitcnt lgkmcnt(8)
buffer_load_dwordx4 v5,s[44:47],s69 offen offset:1024 lds
v_lshl_add_u64 v[44:45],v[52:53],0,v[54:55]
v_mfma_f32_16x16x32_bf16 v[40:43],a[212:215],a[0:3],v[40:43]
s_add_u32 s69,s69,s53
ds_read_u16_d16_hi v30,v30
v_lshlrev_b32_sdwa v31,1,v31 dst_sel:DWORD dst_unused:UNUSED_PAD src0_sel:DWORD src1_sel:WORD_1
v_mfma_f32_16x16x32_bf16 v[52:55],a[212:215],a[4:7],v[20:23]
s_waitcnt lgkmcnt(5)
buffer_load_dwordx4 v5,s[44:47],s69 offen offset:2048 lds
v_or_b32 v46,v56,v58
v_lshlrev_b32_sdwa v24,1,v24 dst_sel:DWORD dst_unused:UNUSED_PAD src0_sel:DWORD src1_sel:WORD_1
v_mfma_f32_16x16x32_bf16 v[52:55],a[200:203],a[20:23],v[52:55]
ds_read_u16 v29,v29
v_cvt_pk_bf16_f32 v47,v57,v59
v_lshlrev_b32_sdwa v26,1,v26 dst_sel:DWORD dst_unused:UNUSED_PAD src0_sel:DWORD src1_sel:WORD_1
v_mfma_f32_16x16x32_bf16 v[52:55],a[196:199],a[36:39],v[52:55]
ds_read_u16_d16_hi v31,v31
v_lshlrev_b32_sdwa v25,1,v25 dst_sel:DWORD dst_unused:UNUSED_PAD src0_sel:DWORD src1_sel:WORD_1
v_mfma_f32_16x16x32_bf16 v[56:59],a[192:195],a[36:39],v[16:19]
ds_read_u16 v24,v24
v_lshlrev_b32_sdwa v27,1,v27 dst_sel:DWORD dst_unused:UNUSED_PAD src0_sel:DWORD src1_sel:WORD_1
v_mfma_f32_16x16x32_bf16 v[56:59],a[216:219],a[20:23],v[56:59]
ds_read_u16_d16_hi v26,v26
v_lshlrev_b32_sdwa v60,1,v60 dst_sel:DWORD dst_unused:UNUSED_PAD src0_sel:DWORD src1_sel:WORD_1
v_mfma_f32_16x16x32_bf16 v[56:59],a[220:223],a[4:7],v[56:59]
ds_read_u16 v25,v25
v_lshlrev_b32_sdwa v62,1,v62 dst_sel:DWORD dst_unused:UNUSED_PAD src0_sel:DWORD src1_sel:WORD_1
v_mfma_f32_16x16x32_bf16 v[232:235],a[240:243],v[48:51],v[232:235]
ds_read_u16_d16_hi v27,v27
v_lshlrev_b32_sdwa v61,1,v61 dst_sel:DWORD dst_unused:UNUSED_PAD src0_sel:DWORD src1_sel:WORD_1
v_mfma_f32_16x16x32_bf16 v[236:239],a[240:243],v[44:47],v[236:239]
ds_read_u16 v60,v60
v_lshlrev_b32_sdwa v63,1,v63 dst_sel:DWORD dst_unused:UNUSED_PAD src0_sel:DWORD src1_sel:WORD_1
v_mfma_f32_16x16x32_bf16 v[164:167],a[224:227],v[44:47],v[164:167]
TP7012_%=:
s_cbranch_execz TR0_%=
ds_read_u16_d16_hi v62,v62
v_lshlrev_b32_sdwa v32,1,v32 dst_sel:DWORD dst_unused:UNUSED_PAD src0_sel:DWORD src1_sel:WORD_1
v_mfma_f32_16x16x32_bf16 v[160:163],a[224:227],v[48:51],v[160:163]
ds_read_u16 v61,v61
v_lshlrev_b32_sdwa v34,1,v34 dst_sel:DWORD dst_unused:UNUSED_PAD src0_sel:DWORD src1_sel:WORD_1
v_mfma_f32_16x16x32_bf16 v[240:243],a[248:251],v[48:51],v[240:243]
ds_read_u16_d16_hi v63,v63
v_lshlrev_b32_sdwa v33,1,v33 dst_sel:DWORD dst_unused:UNUSED_PAD src0_sel:DWORD src1_sel:WORD_1
v_mfma_f32_16x16x32_bf16 v[244:247],a[248:251],v[44:47],v[244:247]
ds_read_u16 v32,v32
v_lshlrev_b32_sdwa v35,1,v35 dst_sel:DWORD dst_unused:UNUSED_PAD src0_sel:DWORD src1_sel:WORD_1
v_mfma_f32_16x16x32_bf16 v[180:183],a[228:231],v[44:47],v[180:183]
s_nop 0
ds_read_u16_d16_hi v34,v34
v_mfma_f32_16x16x32_bf16 v[176:179],a[228:231],v[48:51],v[176:179]
s_waitcnt lgkmcnt(8)
ds_read_u16 v33,v33
v_mfma_f32_16x16x32_bf16 v[248:251],a[232:235],v[48:51],v[248:251]
ds_read_u16_d16_hi v35,v35
v_mfma_f32_16x16x32_bf16 v[252:255],a[232:235],v[44:47],v[252:255]
v_mfma_f32_16x16x32_bf16 v[196:199],a[236:239],v[44:47],v[196:199]
v_mfma_f32_16x16x32_bf16 v[192:195],a[236:239],v[48:51],v[192:195]
v_mfma_f32_16x16x32_bf16 v[44:47],a[220:223],a[8:11],v[16:19]
v_mul_f32_dpp v232,v14,v232 row_newbcast:0 row_mask:0xf bank_mask:0xf
s_add_u32 s10,s61,1572864
v_mul_f32_dpp v233,v14,v233 row_newbcast:1 row_mask:0xf bank_mask:0xf
v_mul_f32_dpp v234,v14,v234 row_newbcast:2 row_mask:0xf bank_mask:0xf
v_lshlrev_b32_sdwa v36,1,v36 dst_sel:DWORD dst_unused:UNUSED_PAD src0_sel:DWORD src1_sel:WORD_1
v_mfma_f32_16x16x32_bf16 v[44:47],a[216:219],a[24:27],v[44:47]
ds_read_u16 v36,v36
v_mul_f32_dpp v235,v14,v235 row_newbcast:3 row_mask:0xf bank_mask:0xf
v_mul_f32_dpp v160,v14,v160 row_newbcast:4 row_mask:0xf bank_mask:0xf
v_mul_f32_dpp v161,v14,v161 row_newbcast:5 row_mask:0xf bank_mask:0xf
v_mfma_f32_16x16x32_bf16 v[44:47],a[192:195],a[40:43],v[44:47]
s_nop 0
v_lshl_add_u64 v[28:29],v[28:29],0,v[30:31]
v_mul_f32_dpp v162,v14,v162 row_newbcast:6 row_mask:0xf bank_mask:0xf
v_mul_f32_dpp v163,v14,v163 row_newbcast:7 row_mask:0xf bank_mask:0xf
v_lshlrev_b32_sdwa v38,1,v38 dst_sel:DWORD dst_unused:UNUSED_PAD src0_sel:DWORD src1_sel:WORD_1
v_cvt_pk_bf16_f32 v232,v232,v233
v_mfma_f32_16x16x32_bf16 v[48:51],a[196:199],a[40:43],v[20:23]
s_waitcnt lgkmcnt(3)
ds_read_u16_d16_hi v38,v38
v_lshl_add_u64 v[30:31],v[24:25],0,v[26:27]
v_cvt_pk_bf16_f32 v233,v234,v235
v_cvt_pk_bf16_f32 v234,v160,v161
v_lshlrev_b32_sdwa v37,1,v37 dst_sel:DWORD dst_unused:UNUSED_PAD src0_sel:DWORD src1_sel:WORD_1
v_cvt_pk_bf16_f32 v235,v162,v163
v_mfma_f32_16x16x32_bf16 v[48:51],a[200:203],a[24:27],v[48:51]
buffer_store_dwordx4 v[232:235],v7,s[40:43],s10 offen offset:0 sc1
v_lshl_add_u64 v[24:25],v[60:61],0,v[62:63]
v_mul_f32_dpp v240,v14,v240 row_newbcast:8 row_mask:0xf bank_mask:0xf
v_mul_f32_dpp v241,v14,v241 row_newbcast:9 row_mask:0xf bank_mask:0xf
v_mul_f32_dpp v242,v14,v242 row_newbcast:10 row_mask:0xf bank_mask:0xf
v_mfma_f32_16x16x32_bf16 v[48:51],a[212:215],a[8:11],v[48:51]
ds_read_u16 v37,v37
v_mul_f32_dpp v243,v14,v243 row_newbcast:11 row_mask:0xf bank_mask:0xf
v_mul_f32_dpp v176,v14,v176 row_newbcast:12 row_mask:0xf bank_mask:0xf
v_lshlrev_b32_sdwa v39,1,v39 dst_sel:DWORD dst_unused:UNUSED_PAD src0_sel:DWORD src1_sel:WORD_1
v_mul_f32_dpp v177,v14,v177 row_newbcast:13 row_mask:0xf bank_mask:0xf
v_mfma_f32_16x16x32_bf16 v[60:63],a[212:215],a[12:15],v[20:23]
s_nop 0
ds_read_b128 v[20:23],v4 offset:640
v_mul_f32_dpp v178,v14,v178 row_newbcast:14 row_mask:0xf bank_mask:0xf
v_mul_f32_dpp v179,v14,v179 row_newbcast:15 row_mask:0xf bank_mask:0xf
v_lshlrev_b32_sdwa v40,1,v40 dst_sel:DWORD dst_unused:UNUSED_PAD src0_sel:DWORD src1_sel:WORD_1
v_cvt_pk_bf16_f32 v240,v240,v241
v_mfma_f32_16x16x32_bf16 v[60:63],a[200:203],a[28:31],v[60:63]
s_waitcnt lgkmcnt(4)
ds_read_u16_d16_hi v39,v39
ds_read_b64_tr_b16 a[212:213],v1 offset:4096
v_lshl_add_u64 v[26:27],v[32:33],0,v[34:35]
v_cvt_pk_bf16_f32 v241,v242,v243
v_cvt_pk_bf16_f32 v242,v176,v177
v_cvt_pk_bf16_f32 v243,v178,v179
v_lshlrev_b32_sdwa v42,1,v42 dst_sel:DWORD dst_unused:UNUSED_PAD src0_sel:DWORD src1_sel:WORD_1
v_mfma_f32_16x16x32_bf16 v[60:63],a[196:199],a[44:47],v[60:63]
ds_read_b64_tr_b16 a[214:215],v1 offset:5120
ds_read_u16 v40,v40
v_mul_f32_dpp v248,v15,v248 row_newbcast:0 row_mask:0xf bank_mask:0xf
v_mul_f32_dpp v249,v15,v249 row_newbcast:1 row_mask:0xf bank_mask:0xf
v_mul_f32_dpp v250,v15,v250 row_newbcast:2 row_mask:0xf bank_mask:0xf
v_mfma_f32_16x16x32_bf16 v[32:35],a[192:195],a[44:47],v[16:19]
ds_read_b128 v[16:19],v4 offset:672
ds_read_b64_tr_b16 a[200:201],v2 offset:4096
v_mul_f32_dpp v251,v15,v251 row_newbcast:3 row_mask:0xf bank_mask:0xf
v_mul_f32_dpp v192,v15,v192 row_newbcast:4 row_mask:0xf bank_mask:0xf
v_lshlrev_b32_sdwa v41,1,v41 dst_sel:DWORD dst_unused:UNUSED_PAD src0_sel:DWORD src1_sel:WORD_1
v_mul_f32_dpp v193,v15,v193 row_newbcast:5 row_mask:0xf bank_mask:0xf
v_mfma_f32_16x16x32_bf16 v[32:35],a[216:219],a[28:31],v[32:35]
ds_read_b64_tr_b16 a[202:203],v2 offset:5120
ds_read_u16_d16_hi v42,v42
v_mul_f32_dpp v194,v15,v194 row_newbcast:6 row_mask:0xf bank_mask:0xf
v_mul_f32_dpp v195,v15,v195 row_newbcast:7 row_mask:0xf bank_mask:0xf
v_lshlrev_b32_sdwa v43,1,v43 dst_sel:DWORD dst_unused:UNUSED_PAD src0_sel:DWORD src1_sel:WORD_1
v_cvt_pk_bf16_f32 v248,v248,v249
v_mfma_f32_16x16x32_bf16 v[32:35],a[220:223],a[12:15],v[32:35]
ds_read_u16 v41,v41
ds_read_u16_d16_hi v43,v43
v_cvt_pk_bf16_f32 v249,v250,v251
v_cvt_pk_bf16_f32 v250,v192,v193
v_cvt_pk_bf16_f32 v251,v194,v195
v_mfma_f32_16x16x32_bf16 v[208:211],a[240:243],v[28:31],v[208:211]
ds_read_b64_tr_b16 a[196:197],v2 offset:2048
v_mul_f32_dpp v236,v14,v236 row_newbcast:0 row_mask:0xf bank_mask:0xf
v_mul_f32_dpp v237,v14,v237 row_newbcast:1 row_mask:0xf bank_mask:0xf
v_lshlrev_b32_sdwa v56,1,v56 dst_sel:DWORD dst_unused:UNUSED_PAD src0_sel:DWORD src1_sel:WORD_1
v_mul_f32_dpp v238,v14,v238 row_newbcast:2 row_mask:0xf bank_mask:0xf
v_mfma_f32_16x16x32_bf16 v[212:215],a[240:243],v[24:27],v[212:215]
ds_read_b64_tr_b16 a[198:199],v2 offset:3072
v_mul_f32_dpp v239,v14,v239 row_newbcast:3 row_mask:0xf bank_mask:0xf
v_mul_f32_dpp v164,v14,v164 row_newbcast:4 row_mask:0xf bank_mask:0xf
v_lshlrev_b32_sdwa v58,1,v58 dst_sel:DWORD dst_unused:UNUSED_PAD src0_sel:DWORD src1_sel:WORD_1
v_mul_f32_dpp v165,v14,v165 row_newbcast:5 row_mask:0xf bank_mask:0xf
v_mfma_f32_16x16x32_bf16 v[172:175],a[224:227],v[24:27],v[172:175]
ds_read_u16 v56,v56
v_mul_f32_dpp v166,v14,v166 row_newbcast:6 row_mask:0xf bank_mask:0xf
v_mul_f32_dpp v167,v14,v167 row_newbcast:7 row_mask:0xf bank_mask:0xf
v_cvt_pk_bf16_f32 v236,v236,v237
v_lshlrev_b32_sdwa v57,1,v57 dst_sel:DWORD dst_unused:UNUSED_PAD src0_sel:DWORD src1_sel:WORD_1
v_mfma_f32_16x16x32_bf16 v[168:171],a[224:227],v[28:31],v[168:171]
buffer_store_dwordx4 v[240:243],v7,s[40:43],s10 offen offset:64 sc1
ds_read_u16_d16_hi v58,v58
v_cvt_pk_bf16_f32 v237,v238,v239
v_cvt_pk_bf16_f32 v238,v164,v165
v_cvt_pk_bf16_f32 v239,v166,v167
v_mfma_f32_16x16x32_bf16 v[216:219],a[248:251],v[28:31],v[216:219]
ds_read_u16 v57,v57
v_mul_f32_dpp v244,v14,v244 row_newbcast:8 row_mask:0xf bank_mask:0xf
v_mul_f32_dpp v245,v14,v245 row_newbcast:9 row_mask:0xf bank_mask:0xf
v_lshlrev_b32_sdwa v59,1,v59 dst_sel:DWORD dst_unused:UNUSED_PAD src0_sel:DWORD src1_sel:WORD_1
v_mul_f32_dpp v246,v14,v246 row_newbcast:10 row_mask:0xf bank_mask:0xf
v_mfma_f32_16x16x32_bf16 v[220:223],a[248:251],v[24:27],v[220:223]
ds_read_u16_d16_hi v59,v59
v_mul_f32_dpp v247,v14,v247 row_newbcast:11 row_mask:0xf bank_mask:0xf
v_mul_f32_dpp v180,v14,v180 row_newbcast:12 row_mask:0xf bank_mask:0xf
v_lshlrev_b32_sdwa v52,1,v52 dst_sel:DWORD dst_unused:UNUSED_PAD src0_sel:DWORD src1_sel:WORD_1
v_mul_f32_dpp v181,v14,v181 row_newbcast:13 row_mask:0xf bank_mask:0xf
v_mfma_f32_16x16x32_bf16 v[188:191],a[228:231],v[24:27],v[188:191]
ds_read_b64_tr_b16 a[192:193],v2 offset:0
v_mul_f32_dpp v182,v14,v182 row_newbcast:14 row_mask:0xf bank_mask:0xf
v_mul_f32_dpp v183,v14,v183 row_newbcast:15 row_mask:0xf bank_mask:0xf
v_cvt_pk_bf16_f32 v244,v244,v245
v_mfma_f32_16x16x32_bf16 v[184:187],a[228:231],v[28:31],v[184:187]
ds_read_b64_tr_b16 a[194:195],v2 offset:1024
v_cvt_pk_bf16_f32 v245,v246,v247
v_cvt_pk_bf16_f32 v246,v180,v181
v_lshlrev_b32_sdwa v54,1,v54 dst_sel:DWORD dst_unused:UNUSED_PAD src0_sel:DWORD src1_sel:WORD_1
v_cvt_pk_bf16_f32 v247,v182,v183
v_mfma_f32_16x16x32_bf16 v[224:227],a[232:235],v[28:31],v[224:227]
ds_read_u16 v52,v52
v_mul_f32_dpp v252,v15,v252 row_newbcast:0 row_mask:0xf bank_mask:0xf
v_mul_f32_dpp v253,v15,v253 row_newbcast:1 row_mask:0xf bank_mask:0xf
v_lshlrev_b32_sdwa v53,1,v53 dst_sel:DWORD dst_unused:UNUSED_PAD src0_sel:DWORD src1_sel:WORD_1
v_mul_f32_dpp v254,v15,v254 row_newbcast:2 row_mask:0xf bank_mask:0xf
v_mfma_f32_16x16x32_bf16 v[228:231],a[232:235],v[24:27],v[228:231]
ds_read_u16_d16_hi v54,v54
v_mul_f32_dpp v255,v15,v255 row_newbcast:3 row_mask:0xf bank_mask:0xf
v_mul_f32_dpp v196,v15,v196 row_newbcast:4 row_mask:0xf bank_mask:0xf
v_mul_f32_dpp v197,v15,v197 row_newbcast:5 row_mask:0xf bank_mask:0xf
v_lshlrev_b32_sdwa v55,1,v55 dst_sel:DWORD dst_unused:UNUSED_PAD src0_sel:DWORD src1_sel:WORD_1
v_mfma_f32_16x16x32_bf16 v[204:207],a[236:239],v[24:27],v[204:207]
s_nop 0
ds_read_u16 v53,v53
v_mul_f32_dpp v198,v15,v198 row_newbcast:6 row_mask:0xf bank_mask:0xf
v_mul_f32_dpp v199,v15,v199 row_newbcast:7 row_mask:0xf bank_mask:0xf
v_cvt_pk_bf16_f32 v252,v252,v253
v_mfma_f32_16x16x32_bf16 v[200:203],a[236:239],v[28:31],v[200:203]
s_waitcnt lgkmcnt(8)
ds_read_u16_d16_hi v55,v55
v_cvt_pk_bf16_f32 v253,v254,v255
v_cvt_pk_bf16_f32 v254,v196,v197
v_cvt_pk_bf16_f32 v255,v198,v199
v_mfma_f32_16x16x32_bf16 v[24:27],a[204:207],a[0:3],v[20:23]
v_mul_f32_dpp v208,v14,v208 row_newbcast:0 row_mask:0xf bank_mask:0xf
s_add_u32 s11,s61,1671168
v_accvgpr_write_b32 a64,0
v_accvgpr_write_b32 a65,0
v_lshlrev_b32_sdwa v44,1,v44 dst_sel:DWORD dst_unused:UNUSED_PAD src0_sel:DWORD src1_sel:WORD_1
v_mfma_f32_16x16x32_bf16 v[24:27],a[208:211],a[16:19],v[24:27]
ds_read_u16 v44,v44
v_accvgpr_write_b32 a66,0
v_accvgpr_write_b32 a67,0
v_mul_f32_dpp v209,v14,v209 row_newbcast:1 row_mask:0xf bank_mask:0xf
v_mfma_f32_16x16x32_bf16 v[24:27],a[212:215],a[32:35],v[24:27]
v_lshl_add_u64 v[36:37],v[36:37],0,v[38:39]
v_mul_f32_dpp v210,v14,v210 row_newbcast:2 row_mask:0xf bank_mask:0xf
v_mul_f32_dpp v211,v14,v211 row_newbcast:3 row_mask:0xf bank_mask:0xf
v_lshlrev_b32_sdwa v46,1,v46 dst_sel:DWORD dst_unused:UNUSED_PAD src0_sel:DWORD src1_sel:WORD_1
v_mul_f32_dpp v168,v14,v168 row_newbcast:4 row_mask:0xf bank_mask:0xf
v_mfma_f32_16x16x32_bf16 v[28:31],a[200:203],a[32:35],v[16:19]
buffer_store_dwordx4 v[248:251],v7,s[40:43],s10 offen offset:128 sc1
v_lshl_add_u64 v[38:39],v[40:41],0,v[42:43]
v_mul_f32_dpp v169,v14,v169 row_newbcast:5 row_mask:0xf bank_mask:0xf
v_mul_f32_dpp v170,v14,v170 row_newbcast:6 row_mask:0xf bank_mask:0xf
v_lshlrev_b32_sdwa v45,1,v45 dst_sel:DWORD dst_unused:UNUSED_PAD src0_sel:DWORD src1_sel:WORD_1
v_mul_f32_dpp v171,v14,v171 row_newbcast:7 row_mask:0xf bank_mask:0xf
v_mfma_f32_16x16x32_bf16 v[28:31],a[196:199],a[16:19],v[28:31]
s_waitcnt lgkmcnt(5)
ds_read_u16_d16_hi v46,v46
v_lshl_add_u64 v[40:41],v[56:57],0,v[58:59]
v_cvt_pk_bf16_f32 v208,v208,v209
v_cvt_pk_bf16_f32 v209,v210,v211
v_cvt_pk_bf16_f32 v210,v168,v169
v_mfma_f32_16x16x32_bf16 v[28:31],a[192:195],a[0:3],v[28:31]
s_nop 0
ds_read_u16 v45,v45
v_cvt_pk_bf16_f32 v211,v170,v171
v_mul_f32_dpp v216,v14,v216 row_newbcast:8 row_mask:0xf bank_mask:0xf
v_lshlrev_b32_sdwa v47,1,v47 dst_sel:DWORD dst_unused:UNUSED_PAD src0_sel:DWORD src1_sel:WORD_1
v_accvgpr_write_b32 a72,v208
v_mfma_f32_16x16x32_bf16 v[56:59],a[192:195],a[4:7],v[16:19]
s_nop 0
ds_read_u16_d16_hi v47,v47
v_mul_f32_dpp v217,v14,v217 row_newbcast:9 row_mask:0xf bank_mask:0xf
v_mul_f32_dpp v218,v14,v218 row_newbcast:10 row_mask:0xf bank_mask:0xf
v_lshlrev_b32_sdwa v48,1,v48 dst_sel:DWORD dst_unused:UNUSED_PAD src0_sel:DWORD src1_sel:WORD_1
v_accvgpr_write_b32 a73,v209
v_mfma_f32_16x16x32_bf16 v[56:59],a[196:199],a[20:23],v[56:59]
s_waitcnt lgkmcnt(4)
ds_read_u16 v48,v48
v_lshl_add_u64 v[42:43],v[52:53],0,v[54:55]
v_mul_f32_dpp v219,v14,v219 row_newbcast:11 row_mask:0xf bank_mask:0xf
v_mul_f32_dpp v184,v14,v184 row_newbcast:12 row_mask:0xf bank_mask:0xf
v_accvgpr_write_b32 a74,v210
v_lshlrev_b32_sdwa v50,1,v50 dst_sel:DWORD dst_unused:UNUSED_PAD src0_sel:DWORD src1_sel:WORD_1
v_mfma_f32_16x16x32_bf16 v[56:59],a[200:203],a[36:39],v[56:59]
s_waitcnt vmcnt(9)
s_barrier
ds_read_u16_d16_hi v50,v50
v_mul_f32_dpp v185,v14,v185 row_newbcast:13 row_mask:0xf bank_mask:0xf
v_mul_f32_dpp v186,v14,v186 row_newbcast:14 row_mask:0xf bank_mask:0xf
v_accvgpr_write_b32 a75,v211
v_mfma_f32_16x16x32_bf16 v[52:55],a[212:215],a[36:39],v[20:23]
ds_read_b64_tr_b16 a[244:245],v3 offset:6160
v_mul_f32_dpp v187,v14,v187 row_newbcast:15 row_mask:0xf bank_mask:0xf
v_cvt_pk_bf16_f32 v216,v216,v217
v_lshlrev_b32_sdwa v49,1,v49 dst_sel:DWORD dst_unused:UNUSED_PAD src0_sel:DWORD src1_sel:WORD_1
v_cvt_pk_bf16_f32 v217,v218,v219
v_mfma_f32_16x16x32_bf16 v[52:55],a[208:211],a[20:23],v[52:55]
s_add_u32 m0,s58,0
ds_read_u16 v49,v49
v_cvt_pk_bf16_f32 v218,v184,v185
v_cvt_pk_bf16_f32 v219,v186,v187
v_lshlrev_b32_sdwa v51,1,v51 dst_sel:DWORD dst_unused:UNUSED_PAD src0_sel:DWORD src1_sel:WORD_1
v_mul_f32_dpp v224,v15,v224 row_newbcast:0 row_mask:0xf bank_mask:0xf
v_mfma_f32_16x16x32_bf16 v[52:55],a[204:207],a[4:7],v[52:55]
s_add_u32 s69,s69,s59
ds_read_u16_d16_hi v51,v51
v_accvgpr_write_b32 a76,v216
v_mul_f32_dpp v225,v15,v225 row_newbcast:1 row_mask:0xf bank_mask:0xf
v_mul_f32_dpp v226,v15,v226 row_newbcast:2 row_mask:0xf bank_mask:0xf
v_mfma_f32_16x16x32_bf16 v[136:139],a[240:243],v[36:39],v[136:139]
s_nop 0
buffer_load_dwordx4 v5,s[44:47],s69 offen offset:0 lds
v_accvgpr_write_b32 a77,v217
v_mul_f32_dpp v227,v15,v227 row_newbcast:3 row_mask:0xf bank_mask:0xf
v_lshlrev_b32_sdwa v32,1,v32 dst_sel:DWORD dst_unused:UNUSED_PAD src0_sel:DWORD src1_sel:WORD_1
v_mul_f32_dpp v200,v15,v200 row_newbcast:4 row_mask:0xf bank_mask:0xf
v_mfma_f32_16x16x32_bf16 v[140:143],a[240:243],v[40:43],v[140:143]
s_add_u32 s69,s69,s53
ds_read_u16 v32,v32
v_accvgpr_write_b32 a78,v218
v_mul_f32_dpp v201,v15,v201 row_newbcast:5 row_mask:0xf bank_mask:0xf
v_lshlrev_b32_sdwa v34,1,v34 dst_sel:DWORD dst_unused:UNUSED_PAD src0_sel:DWORD src1_sel:WORD_1
v_mul_f32_dpp v202,v15,v202 row_newbcast:6 row_mask:0xf bank_mask:0xf
v_mfma_f32_16x16x32_bf16 v[68:71],a[224:227],v[40:43],v[68:71]
buffer_load_dwordx4 v5,s[44:47],s69 offen offset:1024 lds
ds_read_u16_d16_hi v34,v34
v_accvgpr_write_b32 a79,v219
v_mul_f32_dpp v203,v15,v203 row_newbcast:7 row_mask:0xf bank_mask:0xf
v_cvt_pk_bf16_f32 v224,v224,v225
v_lshlrev_b32_sdwa v33,1,v33 dst_sel:DWORD dst_unused:UNUSED_PAD src0_sel:DWORD src1_sel:WORD_1
v_mfma_f32_16x16x32_bf16 v[64:67],a[224:227],v[36:39],v[64:67]
s_nop 0
buffer_store_dwordx4 v[236:239],v7,s[40:43],s10 offen offset:192 sc1
ds_read_u16 v33,v33
v_cvt_pk_bf16_f32 v225,v226,v227
v_cvt_pk_bf16_f32 v226,v200,v201
v_cvt_pk_bf16_f32 v227,v202,v203
v_mfma_f32_16x16x32_bf16 v[144:147],a[248:251],v[36:39],v[144:147]
s_add_u32 s69,s69,s53
ds_read_b64_tr_b16 a[246:247],v3 offset:7696
v_mul_f32_dpp v212,v14,v212 row_newbcast:0 row_mask:0xf bank_mask:0xf
v_accvgpr_write_b32 a80,v224
v_lshlrev_b32_sdwa v35,1,v35 dst_sel:DWORD dst_unused:UNUSED_PAD src0_sel:DWORD src1_sel:WORD_1
v_mul_f32_dpp v213,v14,v213 row_newbcast:1 row_mask:0xf bank_mask:0xf
v_mfma_f32_16x16x32_bf16 v[148:151],a[248:251],v[40:43],v[148:151]
buffer_load_dwordx4 v5,s[44:47],s69 offen offset:2048 lds
v_mul_f32_dpp v214,v14,v214 row_newbcast:2 row_mask:0xf bank_mask:0xf
v_accvgpr_write_b32 a81,v225
v_lshlrev_b32_sdwa v60,1,v60 dst_sel:DWORD dst_unused:UNUSED_PAD src0_sel:DWORD src1_sel:WORD_1
v_mul_f32_dpp v215,v14,v215 row_newbcast:3 row_mask:0xf bank_mask:0xf
v_mfma_f32_16x16x32_bf16 v[84:87],a[228:231],v[40:43],v[84:87]
ds_read_u16_d16_hi v35,v35
v_mul_f32_dpp v172,v14,v172 row_newbcast:4 row_mask:0xf bank_mask:0xf
v_accvgpr_write_b32 a82,v226
v_mul_f32_dpp v173,v14,v173 row_newbcast:5 row_mask:0xf bank_mask:0xf
v_mfma_f32_16x16x32_bf16 v[80:83],a[228:231],v[36:39],v[80:83]
ds_read_u16 v60,v60
v_mul_f32_dpp v174,v14,v174 row_newbcast:6 row_mask:0xf bank_mask:0xf
v_accvgpr_write_b32 a83,v227
v_lshlrev_b32_sdwa v62,1,v62 dst_sel:DWORD dst_unused:UNUSED_PAD src0_sel:DWORD src1_sel:WORD_1
v_mul_f32_dpp v175,v14,v175 row_newbcast:7 row_mask:0xf bank_mask:0xf
v_mfma_f32_16x16x32_bf16 v[152:155],a[232:235],v[36:39],v[152:155]
ds_read_u16_d16_hi v62,v62
v_cvt_pk_bf16_f32 v212,v212,v213
v_cvt_pk_bf16_f32 v213,v214,v215
v_lshlrev_b32_sdwa v61,1,v61 dst_sel:DWORD dst_unused:UNUSED_PAD src0_sel:DWORD src1_sel:WORD_1
v_cvt_pk_bf16_f32 v214,v172,v173
v_mfma_f32_16x16x32_bf16 v[156:159],a[232:235],v[40:43],v[156:159]
ds_read_u16 v61,v61
v_cvt_pk_bf16_f32 v215,v174,v175
v_mfma_f32_32x32x16_bf16 v[160:175],a[64:67],a[64:67],0
v_mul_f32_dpp v220,v14,v220 row_newbcast:8 row_mask:0xf bank_mask:0xf
v_lshlrev_b32_sdwa v63,1,v63 dst_sel:DWORD dst_unused:UNUSED_PAD src0_sel:DWORD src1_sel:WORD_1
v_accvgpr_write_b32 a84,v212
v_mul_f32_dpp v221,v14,v221 row_newbcast:9 row_mask:0xf bank_mask:0xf
v_mul_f32_dpp v222,v14,v222 row_newbcast:10 row_mask:0xf bank_mask:0xf
v_mfma_f32_16x16x32_bf16 v[100:103],a[236:239],v[40:43],v[100:103]
ds_read_u16_d16_hi v63,v63
v_accvgpr_write_b32 a85,v213
v_mfma_f32_16x16x32_bf16 v[96:99],a[236:239],v[36:39],v[96:99]
ds_read_b64_tr_b16 a[252:253],v3 offset:6168
v_mul_f32_dpp v223,v14,v223 row_newbcast:11 row_mask:0xf bank_mask:0xf
v_mul_f32_dpp v188,v14,v188 row_newbcast:12 row_mask:0xf bank_mask:0xf
ds_read_b64_tr_b16 a[254:255],v3 offset:7704
v_add_u32_sdwa v36,s60,v10 dst_sel:DWORD dst_unused:UNUSED_PAD src0_sel:DWORD src1_sel:WORD_1
v_subrev_u32 v8,88400,v36
v_min_u32_e64 v36,v36,v8
v_add_u32 v37,7072,v36
v_subrev_u32 v8,88400,v37
v_min_u32_e64 v37,v37,v8
s_waitcnt lgkmcnt(15)
v_lshl_add_u64 v[44:45],v[44:45],0,v[46:47]
s_waitcnt lgkmcnt(11)
v_lshl_add_u64 v[46:47],v[48:49],0,v[50:51]
s_waitcnt lgkmcnt(6)
v_lshl_add_u64 v[48:49],v[32:33],0,v[34:35]
s_waitcnt lgkmcnt(2)
v_lshl_add_u64 v[50:51],v[60:61],0,v[62:63]
v_mfma_f32_16x16x32_bf16 v[112:115],a[240:243],v[44:47],v[112:115]
v_xad_u32 v40,v11,0,v36
v_xad_u32 v41,v11,8,v37
v_xad_u32 v42,v11,16,v36
v_xad_u32 v43,v11,24,v37
v_mfma_f32_16x16x32_bf16 v[116:119],a[240:243],v[48:51],v[116:119]
ds_read_b64 a[48:49],v40 offset:0
v_accvgpr_write_b32 a86,v214
v_mul_f32_dpp v189,v14,v189 row_newbcast:13 row_mask:0xf bank_mask:0xf
v_mul_f32_dpp v190,v14,v190 row_newbcast:14 row_mask:0xf bank_mask:0xf
v_accvgpr_write_b32 a87,v215
v_mfma_f32_16x16x32_bf16 v[76:79],a[224:227],v[48:51],v[76:79]
buffer_store_dwordx4 v[244:247],v7,s[40:43],s10 offen offset:256 sc1
ds_read_b64 a[50:51],v40 offset:192
v_mul_f32_dpp v191,v14,v191 row_newbcast:15 row_mask:0xf bank_mask:0xf
v_cvt_pk_bf16_f32 v220,v220,v221
v_cvt_pk_bf16_f32 v221,v222,v223
v_mfma_f32_16x16x32_bf16 v[72:75],a[224:227],v[44:47],v[72:75]
ds_read_b64 a[52:53],v41 offset:0
v_cvt_pk_bf16_f32 v222,v188,v189
v_cvt_pk_bf16_f32 v223,v190,v191
v_mfma_f32_32x32x16_bf16 v[176:191],a[64:67],a[64:67],0
v_mfma_f32_16x16x32_bf16 v[120:123],a[248:251],v[44:47],v[120:123]
ds_read_b64 a[54:55],v41 offset:192
v_mul_f32_dpp v228,v15,v228 row_newbcast:0 row_mask:0xf bank_mask:0xf
v_accvgpr_write_b32 a88,v220
v_mul_f32_dpp v229,v15,v229 row_newbcast:1 row_mask:0xf bank_mask:0xf
v_mfma_f32_16x16x32_bf16 v[124:127],a[248:251],v[48:51],v[124:127]
ds_read_b64 a[56:57],v40 offset:8
v_mul_f32_dpp v230,v15,v230 row_newbcast:2 row_mask:0xf bank_mask:0xf
v_accvgpr_write_b32 a89,v221
v_mul_f32_dpp v231,v15,v231 row_newbcast:3 row_mask:0xf bank_mask:0xf
v_mfma_f32_16x16x32_bf16 v[92:95],a[228:231],v[48:51],v[92:95]
ds_read_b64 a[58:59],v40 offset:200
v_mul_f32_dpp v204,v15,v204 row_newbcast:4 row_mask:0xf bank_mask:0xf
v_accvgpr_write_b32 a90,v222
v_mul_f32_dpp v205,v15,v205 row_newbcast:5 row_mask:0xf bank_mask:0xf
v_mfma_f32_16x16x32_bf16 v[88:91],a[228:231],v[44:47],v[88:91]
ds_read_b64 a[60:61],v41 offset:8
v_mul_f32_dpp v206,v15,v206 row_newbcast:6 row_mask:0xf bank_mask:0xf
v_accvgpr_write_b32 a91,v223
v_mul_f32_dpp v207,v15,v207 row_newbcast:7 row_mask:0xf bank_mask:0xf
v_mfma_f32_16x16x32_bf16 v[128:131],a[232:235],v[44:47],v[128:131]
ds_read_b64 a[62:63],v41 offset:200
v_cvt_pk_bf16_f32 v228,v228,v229
v_cvt_pk_bf16_f32 v229,v230,v231
v_cvt_pk_bf16_f32 v230,v204,v205
v_mfma_f32_16x16x32_bf16 v[132:135],a[232:235],v[48:51],v[132:135]
v_cvt_pk_bf16_f32 v231,v206,v207
v_mfma_f32_32x32x16_bf16 v[192:207],a[64:67],a[64:67],0
v_accvgpr_write_b32 a92,v228
v_mfma_f32_16x16x32_bf16 v[108:111],a[236:239],v[48:51],v[108:111]
v_accvgpr_write_b32 a93,v229
v_accvgpr_write_b32 a94,v230
v_accvgpr_write_b32 a95,v231
v_mfma_f32_16x16x32_bf16 v[104:107],a[236:239],v[44:47],v[104:107]
v_mfma_f32_32x32x16_bf16 v[208:223],a[64:67],a[64:67],0
v_mov_b64 v[32:33],0
v_mov_b64 v[34:35],0
ds_read_b64 a[64:65],v42 offset:0
ds_read_b64 a[66:67],v42 offset:192
ds_read_b64 a[68:69],v43 offset:0
ds_read_b64 a[70:71],v43 offset:192
s_waitcnt lgkmcnt(8)
v_smfmac_f32_16x16x64_bf16 v[160:163],a[96:99],a[48:55],v10
s_waitcnt lgkmcnt(4)
ds_read_b64 a[48:49],v42 offset:8
ds_read_b64 a[50:51],v42 offset:200
ds_read_b64 a[52:53],v43 offset:8
ds_read_b64 a[54:55],v43 offset:200
v_smfmac_f32_16x16x64_bf16 v[164:167],a[100:103],a[56:63],v10
s_waitcnt lgkmcnt(4)
ds_read_b64 a[56:57],v40 offset:64
ds_read_b64 a[58:59],v40 offset:256
ds_read_b64 a[60:61],v41 offset:64
ds_read_b64 a[62:63],v41 offset:256
v_smfmac_f32_16x16x64_bf16 v[168:171],a[104:107],a[64:71],v10
s_waitcnt lgkmcnt(4)
buffer_store_dwordx4 v[252:255],v7,s[40:43],s10 offen offset:320 sc1
ds_read_b64 a[64:65],v40 offset:72
ds_read_b64 a[66:67],v40 offset:264
ds_read_b64 a[68:69],v41 offset:72
ds_read_b64 a[70:71],v41 offset:264
v_and_b32 v44,8,v11
v_pk_mul_f32 v[48:49],v[160:161],v[160:161]
v_pk_mul_f32 v[50:51],v[162:163],v[162:163]
v_smfmac_f32_16x16x64_bf16 v[172:175],a[108:111],a[48:55],v10
s_waitcnt lgkmcnt(4)
ds_read_b64 a[48:49],v42 offset:64
ds_read_b64 a[50:51],v42 offset:256
ds_read_b64 a[52:53],v43 offset:64
ds_read_b64 a[54:55],v43 offset:256
v_mul_u32_u24 v44,884,v44
s_add_u32 s64,s60,3536
v_pk_add_f32 v[60:61],v[160:161],v[164:165]
v_pk_fma_f32 v[48:49],v[164:165],v[164:165],v[48:49]
v_pk_add_f32 v[62:63],v[162:163],v[166:167]
v_pk_fma_f32 v[50:51],v[166:167],v[166:167],v[50:51]
v_smfmac_f32_16x16x64_bf16 v[176:179],a[128:131],a[56:63],v10
s_waitcnt lgkmcnt(4)
ds_read_b64 a[56:57],v42 offset:72
ds_read_b64 a[58:59],v42 offset:264
ds_read_b64 a[60:61],v43 offset:72
ds_read_b64 a[62:63],v43 offset:264
v_add_u32 v47,s64,v44
v_pk_add_f32 v[60:61],v[60:61],v[168:169]
v_pk_fma_f32 v[48:49],v[168:169],v[168:169],v[48:49]
v_pk_add_f32 v[62:63],v[62:63],v[170:171]
v_pk_fma_f32 v[50:51],v[170:171],v[170:171],v[50:51]
v_smfmac_f32_16x16x64_bf16 v[180:183],a[132:135],a[64:71],v10
s_waitcnt lgkmcnt(4)
ds_read_b64 a[64:65],v40 offset:128
ds_read_b64 a[66:67],v40 offset:320
ds_read_b64 a[68:69],v41 offset:128
ds_read_b64 a[70:71],v41 offset:320
v_subrev_u32 v8,88400,v47
v_pk_add_f32 v[60:61],v[60:61],v[172:173]
v_pk_fma_f32 v[48:49],v[172:173],v[172:173],v[48:49]
v_pk_add_f32 v[62:63],v[62:63],v[174:175]
v_pk_fma_f32 v[50:51],v[174:175],v[174:175],v[50:51]
v_smfmac_f32_16x16x64_bf16 v[184:187],a[136:139],a[48:55],v10
s_waitcnt lgkmcnt(4)
ds_read_b64 a[48:49],v40 offset:136
ds_read_b64 a[50:51],v40 offset:328
ds_read_b64 a[52:53],v41 offset:136
ds_read_b64 a[54:55],v41 offset:328
v_min_u32 v47,v47,v8
v_pk_add_f32 v[60:61],v[60:61],v[176:177]
v_pk_fma_f32 v[48:49],v[176:177],v[176:177],v[48:49]
v_pk_add_f32 v[62:63],v[62:63],v[178:179]
v_pk_fma_f32 v[50:51],v[178:179],v[178:179],v[50:51]
v_smfmac_f32_16x16x64_bf16 v[188:191],a[140:143],a[56:63],v10
s_waitcnt lgkmcnt(4)
ds_read_b64 a[56:57],v42 offset:128
ds_read_b64 a[58:59],v42 offset:320
ds_read_b64 a[60:61],v43 offset:128
ds_read_b64 a[62:63],v43 offset:320
v_xad_u32 v40,v11,32,v36
v_xad_u32 v41,v11,40,v37
v_pk_add_f32 v[60:61],v[60:61],v[180:181]
v_pk_fma_f32 v[48:49],v[180:181],v[180:181],v[48:49]
v_pk_add_f32 v[62:63],v[62:63],v[182:183]
v_pk_fma_f32 v[50:51],v[182:183],v[182:183],v[50:51]
v_smfmac_f32_16x16x64_bf16 v[192:195],a[160:163],a[64:71],v10
s_waitcnt lgkmcnt(4)
ds_read_b64 a[64:65],v42 offset:136
ds_read_b64 a[66:67],v42 offset:328
ds_read_b64 a[68:69],v43 offset:136
ds_read_b64 a[70:71],v43 offset:328
v_add_u32 v46,3536,v47
v_pk_add_f32 v[60:61],v[60:61],v[184:185]
v_pk_fma_f32 v[48:49],v[184:185],v[184:185],v[48:49]
v_pk_add_f32 v[62:63],v[62:63],v[186:187]
v_pk_fma_f32 v[50:51],v[186:187],v[186:187],v[50:51]
v_smfmac_f32_16x16x64_bf16 v[196:199],a[164:167],a[48:55],v10
s_waitcnt lgkmcnt(4)
buffer_store_dwordx4 a[72:75],v7,s[40:43],s11 offen offset:0 sc1
ds_read_b64 a[48:49],v40 offset:0
ds_read_b64 a[50:51],v40 offset:192
ds_read_b64 a[52:53],v41 offset:0
ds_read_b64 a[54:55],v41 offset:192
v_xad_u32 v42,v11,48,v36
v_xad_u32 v43,v11,56,v37
v_pk_add_f32 v[60:61],v[60:61],v[188:189]
v_pk_fma_f32 v[48:49],v[188:189],v[188:189],v[48:49]
v_pk_add_f32 v[62:63],v[62:63],v[190:191]
v_pk_fma_f32 v[50:51],v[190:191],v[190:191],v[50:51]
v_smfmac_f32_16x16x64_bf16 v[200:203],a[168:171],a[56:63],v10
s_waitcnt lgkmcnt(4)
ds_read_b64 a[56:57],v40 offset:8
ds_read_b64 a[58:59],v40 offset:200
ds_read_b64 a[60:61],v41 offset:8
ds_read_b64 a[62:63],v41 offset:200
v_subrev_u32 v8,88400,v46
v_pk_add_f32 v[60:61],v[60:61],v[192:193]
v_pk_fma_f32 v[48:49],v[192:193],v[192:193],v[48:49]
v_pk_add_f32 v[62:63],v[62:63],v[194:195]
v_pk_fma_f32 v[50:51],v[194:195],v[194:195],v[50:51]
v_smfmac_f32_16x16x64_bf16 v[204:207],a[172:175],a[64:71],v10
s_waitcnt lgkmcnt(4)
ds_read_b64 a[64:65],v40 offset:64
ds_read_b64 a[66:67],v40 offset:256
ds_read_b64 a[68:69],v41 offset:64
ds_read_b64 a[70:71],v41 offset:256
v_min_u32 v46,v46,v8
v_pk_add_f32 v[60:61],v[60:61],v[196:197]
v_pk_fma_f32 v[48:49],v[196:197],v[196:197],v[48:49]
v_pk_add_f32 v[62:63],v[62:63],v[198:199]
v_pk_fma_f32 v[50:51],v[198:199],v[198:199],v[50:51]
v_smfmac_f32_16x16x64_bf16 v[208:211],a[112:115],a[48:55],v10
TE10_%=:
s_cbranch_execz TP9676_%=
s_waitcnt lgkmcnt(4)
s_nop 0
ds_read_b64 a[48:49],v40 offset:72
ds_read_b64 a[50:51],v40 offset:264
ds_read_b64 a[52:53],v41 offset:72
ds_read_b64 a[54:55],v41 offset:264
v_xor_b32 v9,0x80008,v12
v_pk_add_f32 v[60:61],v[60:61],v[200:201]
v_pk_fma_f32 v[48:49],v[200:201],v[200:201],v[48:49]
v_pk_add_f32 v[62:63],v[62:63],v[202:203]
v_pk_fma_f32 v[50:51],v[202:203],v[202:203],v[50:51]
v_mfma_f32_32x32x16_bf16 v[224:239],v[32:35],v[32:35],0
v_smfmac_f32_16x16x64_bf16 v[212:215],a[116:119],a[56:63],v10
s_waitcnt lgkmcnt(4)
ds_read_b64 a[56:57],v40 offset:128
ds_read_b64 a[58:59],v40 offset:320
ds_read_b64 a[60:61],v41 offset:128
ds_read_b64 a[62:63],v41 offset:320
v_add_u32_sdwa v44,v12,v47 dst_sel:DWORD dst_unused:UNUSED_PAD src0_sel:WORD_0 src1_sel:DWORD
v_pk_add_f32 v[60:61],v[60:61],v[204:205]
v_pk_fma_f32 v[48:49],v[204:205],v[204:205],v[48:49]
v_pk_add_f32 v[62:63],v[62:63],v[206:207]
v_pk_fma_f32 v[50:51],v[206:207],v[206:207],v[50:51]
v_smfmac_f32_16x16x64_bf16 v[216:219],a[144:147],a[64:71],v10
s_waitcnt lgkmcnt(4)
ds_read_b64 a[64:65],v40 offset:136
ds_read_b64 a[66:67],v40 offset:328
ds_read_b64 a[68:69],v41 offset:136
ds_read_b64 a[70:71],v41 offset:328
v_add_u32_sdwa v45,v12,v47 dst_sel:DWORD dst_unused:UNUSED_PAD src0_sel:WORD_1 src1_sel:DWORD
v_pk_add_f32 v[60:61],v[60:61],v[208:209]
v_pk_fma_f32 v[48:49],v[208:209],v[208:209],v[48:49]
v_pk_add_f32 v[62:63],v[62:63],v[210:211]
v_pk_fma_f32 v[50:51],v[210:211],v[210:211],v[50:51]
v_smfmac_f32_16x16x64_bf16 v[220:223],a[148:151],a[48:55],v10
s_waitcnt lgkmcnt(4)
buffer_store_dwordx4 a[76:79],v7,s[40:43],s11 offen offset:64 sc1
ds_read_b64 a[48:49],v42 offset:0
ds_read_b64 a[50:51],v42 offset:192
ds_read_b64 a[52:53],v43 offset:0
ds_read_b64 a[54:55],v43 offset:192
v_add_u32_sdwa v47,v9,v46 dst_sel:DWORD dst_unused:UNUSED_PAD src0_sel:WORD_1 src1_sel:DWORD
v_pk_add_f32 v[60:61],v[60:61],v[212:213]
v_pk_fma_f32 v[48:49],v[212:213],v[212:213],v[48:49]
v_pk_add_f32 v[62:63],v[62:63],v[214:215]
v_pk_fma_f32 v[50:51],v[214:215],v[214:215],v[50:51]
v_smfmac_f32_16x16x64_bf16 v[224:227],a[176:179],a[56:63],v10
s_waitcnt lgkmcnt(4)
ds_read_b64 a[56:57],v42 offset:8
ds_read_b64 a[58:59],v42 offset:200
ds_read_b64 a[60:61],v43 offset:8
ds_read_b64 a[62:63],v43 offset:200
v_add_u32_sdwa v46,v9,v46 dst_sel:DWORD dst_unused:UNUSED_PAD src0_sel:WORD_0 src1_sel:DWORD
v_pk_add_f32 v[60:61],v[60:61],v[216:217]
v_pk_fma_f32 v[48:49],v[216:217],v[216:217],v[48:49]
v_pk_add_f32 v[62:63],v[62:63],v[218:219]
v_pk_fma_f32 v[50:51],v[218:219],v[218:219],v[50:51]
v_mfma_f32_32x32x16_bf16 v[240:255],v[32:35],v[32:35],0
v_smfmac_f32_16x16x64_bf16 v[228:231],a[180:183],a[64:71],v10
s_waitcnt lgkmcnt(4)
ds_read_b64 a[64:65],v42 offset:64
ds_read_b64 a[66:67],v42 offset:256
ds_read_b64 a[68:69],v43 offset:64
ds_read_b64 a[70:71],v43 offset:256
v_pk_add_f32 v[60:61],v[60:61],v[220:221]
v_pk_fma_f32 v[48:49],v[220:221],v[220:221],v[48:49]
v_pk_add_f32 v[62:63],v[62:63],v[222:223]
v_pk_fma_f32 v[50:51],v[222:223],v[222:223],v[50:51]
v_smfmac_f32_16x16x64_bf16 v[232:235],a[120:123],a[48:55],v10
s_waitcnt lgkmcnt(4)
ds_read_b64 a[48:49],v42 offset:72
ds_read_b64 a[50:51],v42 offset:264
ds_read_b64 a[52:53],v43 offset:72
ds_read_b64 a[54:55],v43 offset:264
v_pk_add_f32 v[60:61],v[60:61],v[224:225]
v_pk_fma_f32 v[48:49],v[224:225],v[224:225],v[48:49]
v_pk_add_f32 v[62:63],v[62:63],v[226:227]
v_pk_fma_f32 v[50:51],v[226:227],v[226:227],v[50:51]
v_smfmac_f32_16x16x64_bf16 v[236:239],a[124:127],a[56:63],v10
s_waitcnt lgkmcnt(4)
ds_read_b64 a[56:57],v42 offset:128
ds_read_b64 a[58:59],v42 offset:320
ds_read_b64 a[60:61],v43 offset:128
ds_read_b64 a[62:63],v43 offset:320
v_pk_add_f32 v[60:61],v[60:61],v[228:229]
v_pk_fma_f32 v[48:49],v[228:229],v[228:229],v[48:49]
v_pk_add_f32 v[62:63],v[62:63],v[230:231]
v_pk_fma_f32 v[50:51],v[230:231],v[230:231],v[50:51]
v_smfmac_f32_16x16x64_bf16 v[240:243],a[152:155],a[64:71],v10
s_waitcnt lgkmcnt(4)
ds_read_b64 a[64:65],v42 offset:136
ds_read_b64 a[66:67],v42 offset:328
ds_read_b64 a[68:69],v43 offset:136
ds_read_b64 a[70:71],v43 offset:328
v_pk_add_f32 v[60:61],v[60:61],v[232:233]
v_pk_fma_f32 v[48:49],v[232:233],v[232:233],v[48:49]
v_pk_add_f32 v[62:63],v[62:63],v[234:235]
v_pk_fma_f32 v[50:51],v[234:235],v[234:235],v[50:51]
v_smfmac_f32_16x16x64_bf16 v[244:247],a[156:159],a[48:55],v10
s_waitcnt lgkmcnt(4)
ds_read_b64 a[240:241],v44 offset:57344
ds_read_b64 a[242:243],v44 offset:57352
ds_read_b64 a[224:225],v45 offset:57344
ds_read_b64 a[226:227],v45 offset:57352
ds_read_b128 a[232:235],v13 offset:0
v_pk_add_f32 v[60:61],v[60:61],v[236:237]
v_pk_fma_f32 v[48:49],v[236:237],v[236:237],v[48:49]
v_pk_add_f32 v[62:63],v[62:63],v[238:239]
v_pk_fma_f32 v[50:51],v[238:239],v[238:239],v[50:51]
v_smfmac_f32_16x16x64_bf16 v[248:251],a[184:187],a[56:63],v10
s_waitcnt lgkmcnt(5)
buffer_store_dwordx4 a[80:83],v7,s[40:43],s11 offen offset:128 sc1
ds_read_b64 a[248:249],v46 offset:57344
ds_read_b64 a[250:251],v46 offset:57352
ds_read_b64 a[228:229],v47 offset:57344
ds_read_b64 a[230:231],v47 offset:57352
ds_read_b128 a[236:239],v13 offset:256
v_pk_add_f32 v[60:61],v[60:61],v[240:241]
v_pk_fma_f32 v[48:49],v[240:241],v[240:241],v[48:49]
v_pk_add_f32 v[62:63],v[62:63],v[242:243]
v_pk_fma_f32 v[50:51],v[242:243],v[242:243],v[50:51]
v_smfmac_f32_16x16x64_bf16 v[252:255],a[188:191],a[64:71],v10
ds_read_b128 v[40:43],v4 offset:3584
ds_read_b128 v[36:39],v4 offset:3616
v_pk_add_f32 v[60:61],v[60:61],v[244:245]
v_pk_fma_f32 v[48:49],v[244:245],v[244:245],v[48:49]
v_pk_add_f32 v[62:63],v[62:63],v[246:247]
v_pk_fma_f32 v[50:51],v[246:247],v[246:247],v[50:51]
v_pk_add_f32 v[60:61],v[60:61],v[248:249]
v_pk_fma_f32 v[48:49],v[248:249],v[248:249],v[48:49]
v_pk_add_f32 v[62:63],v[62:63],v[250:251]
v_pk_fma_f32 v[50:51],v[250:251],v[250:251],v[50:51]
v_pk_add_f32 v[60:61],v[60:61],v[252:253]
v_pk_fma_f32 v[48:49],v[252:253],v[252:253],v[48:49]
v_pk_add_f32 v[62:63],v[62:63],v[254:255]
v_pk_fma_f32 v[50:51],v[254:255],v[254:255],v[50:51]
s_nop 0
v_permlane32_swap_b32 v60,v62
v_permlane32_swap_b32 v61,v63
v_permlane32_swap_b32 v48,v50
v_permlane32_swap_b32_e64 v49,v51
v_pk_add_f32 v[60:61],v[60:61],v[62:63]
v_pk_add_f32 v[48:49],v[48:49],v[50:51]
s_nop 0
v_permlane16_swap_b32 v60,v61
v_permlane16_swap_b32 v48,v49
v_add_f32 v8,v60,v61
v_add_f32_e64 v9,v48,v49
v_mul_f32 v8,0x3c2aaaab,v8
v_mul_f32 v9,0x3c2aaaab,v9
v_cmp_ngt_f32 vcc,0x5d800000,v9
v_fma_f32 v9,-v8,v8,v9
v_max_f32 v9,0,v9
s_or_b64 s[90:91],s[90:91],vcc
v_add_f32 v9,0x3727c5ac,v9
v_rsq_f32 v9,v9
s_nop 0
v_mul_f32 v60,s96,v9
v_mul_f32 v61,s96,v9
v_mul_f32 v48,-v8,v60
v_mul_f32 v49,-v8,v60
v_permlane16_swap_b32 v60,v61
s_nop 0
v_permlane16_swap_b32 v48,v49
v_mov_b64 v[62:63],v[60:61]
v_mov_b64 v[50:51],v[48:49]
s_nop 0
v_permlane32_swap_b32 v60,v62
v_permlane32_swap_b32 v61,v63
v_permlane32_swap_b32 v48,v50
v_permlane32_swap_b32 v49,v51
s_or_b32 s64,s90,s91
s_lshl_b32 s65,s52,2
v_mov_b32 v8,s64
v_mov_b32 v9,s65
ds_write_b32 v9,v8 offset:11648
v_pk_fma_f32 v[160:161],v[160:161],v[60:61],v[48:49]
v_pk_fma_f32 v[162:163],v[162:163],v[62:63],v[50:51]
v_pk_fma_f32 v[164:165],v[164:165],v[60:61],v[48:49]
v_pk_fma_f32 v[166:167],v[166:167],v[62:63],v[50:51]
v_pk_fma_f32 v[168:169],v[168:169],v[60:61],v[48:49]
v_pk_fma_f32 v[170:171],v[170:171],v[62:63],v[50:51]
v_pk_fma_f32 v[172:173],v[172:173],v[60:61],v[48:49]
v_pk_fma_f32 v[174:175],v[174:175],v[62:63],v[50:51]
v_pk_fma_f32 v[208:209],v[208:209],v[60:61],v[48:49]
v_pk_fma_f32 v[210:211],v[210:211],v[62:63],v[50:51]
v_pk_fma_f32 v[212:213],v[212:213],v[60:61],v[48:49]
v_pk_fma_f32 v[214:215],v[214:215],v[62:63],v[50:51]
v_pk_fma_f32 v[232:233],v[232:233],v[60:61],v[48:49]
v_pk_fma_f32 v[234:235],v[234:235],v[62:63],v[50:51]
v_pk_fma_f32 v[236:237],v[236:237],v[60:61],v[48:49]
v_pk_fma_f32 v[238:239],v[238:239],v[62:63],v[50:51]
v_pk_fma_f32 v[176:177],v[176:177],v[60:61],v[48:49]
v_pk_fma_f32 v[178:179],v[178:179],v[62:63],v[50:51]
v_pk_fma_f32 v[180:181],v[180:181],v[60:61],v[48:49]
v_pk_fma_f32 v[182:183],v[182:183],v[62:63],v[50:51]
v_pk_fma_f32 v[184:185],v[184:185],v[60:61],v[48:49]
v_pk_fma_f32 v[186:187],v[186:187],v[62:63],v[50:51]
v_pk_fma_f32 v[188:189],v[188:189],v[60:61],v[48:49]
v_pk_fma_f32 v[190:191],v[190:191],v[62:63],v[50:51]
v_pk_fma_f32 v[216:217],v[216:217],v[60:61],v[48:49]
v_pk_fma_f32 v[218:219],v[218:219],v[62:63],v[50:51]
v_pk_fma_f32 v[220:221],v[220:221],v[60:61],v[48:49]
v_pk_fma_f32 v[222:223],v[222:223],v[62:63],v[50:51]
v_pk_fma_f32 v[240:241],v[240:241],v[60:61],v[48:49]
v_pk_fma_f32 v[242:243],v[242:243],v[62:63],v[50:51]
v_pk_fma_f32 v[244:245],v[244:245],v[60:61],v[48:49]
v_pk_fma_f32 v[246:247],v[246:247],v[62:63],v[50:51]
v_pk_fma_f32 v[192:193],v[192:193],v[60:61],v[48:49]
v_pk_fma_f32 v[194:195],v[194:195],v[62:63],v[50:51]
v_pk_fma_f32 v[196:197],v[196:197],v[60:61],v[48:49]
v_pk_fma_f32 v[198:199],v[198:199],v[62:63],v[50:51]
v_pk_fma_f32 v[200:201],v[200:201],v[60:61],v[48:49]
v_pk_fma_f32 v[202:203],v[202:203],v[62:63],v[50:51]
v_pk_fma_f32 v[204:205],v[204:205],v[60:61],v[48:49]
v_pk_fma_f32 v[206:207],v[206:207],v[62:63],v[50:51]
v_pk_fma_f32 v[224:225],v[224:225],v[60:61],v[48:49]
v_pk_fma_f32 v[226:227],v[226:227],v[62:63],v[50:51]
v_pk_fma_f32 v[228:229],v[228:229],v[60:61],v[48:49]
v_pk_fma_f32 v[230:231],v[230:231],v[62:63],v[50:51]
v_pk_fma_f32 v[248:249],v[248:249],v[60:61],v[48:49]
v_pk_fma_f32 v[250:251],v[250:251],v[62:63],v[50:51]
v_pk_fma_f32 v[252:253],v[252:253],v[60:61],v[48:49]
v_pk_fma_f32 v[254:255],v[254:255],v[62:63],v[50:51]
v_mfma_f32_16x16x32_bf16 v[32:35],a[204:207],a[8:11],v[20:23]
buffer_store_dwordx4 a[84:87],v7,s[40:43],s11 offen offset:192 sc1
v_cvt_pk_bf16_f32 v160,v160,v164
v_cvt_pk_bf16_f32 v168,v168,v172
v_cvt_pk_bf16_f32 v208,v208,v212
v_lshlrev_b32_sdwa v24,1,v24 dst_sel:DWORD dst_unused:UNUSED_PAD src0_sel:DWORD src1_sel:WORD_1
v_mfma_f32_16x16x32_bf16 v[32:35],a[208:211],a[24:27],v[32:35]
ds_read_u16 v24,v24
v_accvgpr_write_b32 a48,v160
v_accvgpr_write_b32 a49,v168
v_cvt_pk_bf16_f32 v232,v232,v236
v_accvgpr_write_b32 a50,v208
v_mfma_f32_16x16x32_bf16 v[32:35],a[212:215],a[40:43],v[32:35]
ds_read_b64_tr_b16 a[216:217],v1 offset:6160
v_cvt_pk_bf16_f32 v161,v161,v165
v_accvgpr_write_b32 a51,v232
v_cvt_pk_bf16_f32 v169,v169,v173
v_accvgpr_write_b32 a52,v161
v_mfma_f32_16x16x32_bf16 v[60:63],a[200:203],a[40:43],v[16:19]
ds_read_b64_tr_b16 a[218:219],v1 offset:7184
v_cvt_pk_bf16_f32 v209,v209,v213
v_accvgpr_write_b32 a53,v169
v_cvt_pk_bf16_f32 v233,v233,v237
v_lshlrev_b32_sdwa v26,1,v26 dst_sel:DWORD dst_unused:UNUSED_PAD src0_sel:DWORD src1_sel:WORD_1
v_accvgpr_write_b32 a54,v209
v_mfma_f32_16x16x32_bf16 v[60:63],a[196:199],a[24:27],v[60:63]
ds_read_u16_d16_hi v26,v26
v_cvt_pk_bf16_f32 v162,v162,v166
v_accvgpr_write_b32 a55,v233
v_cvt_pk_bf16_f32 v170,v170,v174
v_accvgpr_write_b32 a56,v162
v_mfma_f32_16x16x32_bf16 v[60:63],a[192:195],a[8:11],v[60:63]
buffer_store_dwordx4 a[88:91],v7,s[40:43],s11 offen offset:256 sc1
v_accvgpr_write_b32 a57,v170
v_cvt_pk_bf16_f32 v210,v210,v214
v_accvgpr_write_b32 a58,v210
v_lshlrev_b32_sdwa v25,1,v25 dst_sel:DWORD dst_unused:UNUSED_PAD src0_sel:DWORD src1_sel:WORD_1
v_cvt_pk_bf16_f32 v234,v234,v238
v_mfma_f32_16x16x32_bf16 v[48:51],a[192:195],a[12:15],v[16:19]
ds_read_u16 v25,v25
v_accvgpr_write_b32 a59,v234
v_cvt_pk_bf16_f32 v163,v163,v167
v_accvgpr_write_b32 a60,v163
v_mfma_f32_16x16x32_bf16 v[48:51],a[196:199],a[28:31],v[48:51]
ds_read_b64_tr_b16 a[220:221],v1 offset:8208
v_cvt_pk_bf16_f32 v171,v171,v175
v_accvgpr_write_b32 a61,v171
v_cvt_pk_bf16_f32 v211,v211,v215
v_accvgpr_write_b32 a62,v211
v_lshlrev_b32_sdwa v27,1,v27 dst_sel:DWORD dst_unused:UNUSED_PAD src0_sel:DWORD src1_sel:WORD_1
v_mfma_f32_16x16x32_bf16 v[48:51],a[200:203],a[44:47],v[48:51]
s_waitcnt lgkmcnt(6)
ds_read_u16_d16_hi v27,v27
v_cvt_pk_bf16_f32 v235,v235,v239
v_accvgpr_write_b32 a63,v235
v_cvt_pk_bf16_f32 v176,v176,v180
v_cvt_pk_bf16_f32 v184,v184,v188
v_mfma_f32_16x16x32_bf16 v[232:235],a[232:235],a[240:243],v[40:43]
s_nop 0
ds_read_b64_tr_b16 a[222:223],v1 offset:9232
v_cvt_pk_bf16_f32 v216,v216,v220
v_accvgpr_write_b32 a64,v176
v_accvgpr_write_b32 a65,v184
v_cvt_pk_bf16_f32 v240,v240,v244
v_lshlrev_b32_sdwa v28,1,v28 dst_sel:DWORD dst_unused:UNUSED_PAD src0_sel:DWORD src1_sel:WORD_1
v_mfma_f32_16x16x32_bf16 v[236:239],a[232:235],a[224:227],v[40:43]
ds_read_u16 v28,v28
v_accvgpr_write_b32 a66,v216
v_cvt_pk_bf16_f32 v177,v177,v181
v_accvgpr_write_b32 a67,v240
v_cvt_pk_bf16_f32 v185,v185,v189
v_mfma_f32_16x16x32_bf16 v[208:211],a[232:235],a[248:251],v[40:43]
v_accvgpr_write_b32 a68,v177
v_cvt_pk_bf16_f32 v217,v217,v221
v_accvgpr_write_b32 a69,v185
v_lshlrev_b32_sdwa v30,1,v30 dst_sel:DWORD dst_unused:UNUSED_PAD src0_sel:DWORD src1_sel:WORD_1
v_cvt_pk_bf16_f32 v241,v241,v245
v_mfma_f32_16x16x32_bf16 v[212:215],a[232:235],a[228:231],v[40:43]
ds_read_b128 a[232:235],v13 offset:512
ds_read_b128 v[40:43],v4 offset:3712
v_accvgpr_write_b32 a70,v217
v_cvt_pk_bf16_f32 v178,v178,v182
v_accvgpr_write_b32 a71,v241
v_mfma_f32_16x16x32_bf16 v[160:163],a[236:239],a[240:243],v[36:39]
ds_read_b64 a[240:241],v44 offset:57408
ds_read_b64 a[242:243],v44 offset:57416
v_cvt_pk_bf16_f32 v186,v186,v190
v_accvgpr_write_b32 a72,v178
v_accvgpr_write_b32 a73,v186
v_cvt_pk_bf16_f32 v218,v218,v222
v_lshlrev_b32_sdwa v29,1,v29 dst_sel:DWORD dst_unused:UNUSED_PAD src0_sel:DWORD src1_sel:WORD_1
v_mfma_f32_16x16x32_bf16 v[164:167],a[236:239],a[224:227],v[36:39]
ds_read_b64 a[224:225],v45 offset:57408
ds_read_b64 a[226:227],v45 offset:57416
v_accvgpr_write_b32 a74,v218
v_cvt_pk_bf16_f32 v242,v242,v246
v_accvgpr_write_b32 a75,v242
v_cvt_pk_bf16_f32 v179,v179,v183
v_mfma_f32_16x16x32_bf16 v[168:171],a[236:239],a[248:251],v[36:39]
ds_read_b64 a[248:249],v46 offset:57408
ds_read_b64 a[250:251],v46 offset:57416
v_accvgpr_write_b32 a76,v179
v_cvt_pk_bf16_f32 v187,v187,v191
v_accvgpr_write_b32 a77,v187
v_cvt_pk_bf16_f32 v219,v219,v223
v_lshlrev_b32_sdwa v31,1,v31 dst_sel:DWORD dst_unused:UNUSED_PAD src0_sel:DWORD src1_sel:WORD_1
v_mfma_f32_16x16x32_bf16 v[172:175],a[236:239],a[228:231],v[36:39]
s_waitcnt lgkmcnt(4)
buffer_store_dwordx4 a[92:95],v7,s[40:43],s11 offen offset:320 sc1
ds_read_b64 a[228:229],v47 offset:57408
v_accvgpr_write_b32 a78,v219
v_cvt_pk_bf16_f32 v243,v243,v247
v_accvgpr_write_b32 a79,v243
v_cvt_pk_bf16_f32 v192,v192,v196
v_mfma_f32_16x16x32_bf16 v[240:243],a[232:235],a[240:243],v[40:43]
s_waitcnt lgkmcnt(3)
ds_read_b64 a[230:231],v47 offset:57416
ds_read_b128 a[236:239],v13 offset:768
v_cvt_pk_bf16_f32 v200,v200,v204
v_cvt_pk_bf16_f32 v224,v224,v228
v_accvgpr_write_b32 a80,v192
v_accvgpr_write_b32 a81,v200
v_lshlrev_b32_sdwa v52,1,v52 dst_sel:DWORD dst_unused:UNUSED_PAD src0_sel:DWORD src1_sel:WORD_1
v_mfma_f32_16x16x32_bf16 v[244:247],a[232:235],a[224:227],v[40:43]
s_waitcnt lgkmcnt(3)
ds_read_b128 v[36:39],v4 offset:3744
ds_read_u16_d16_hi v30,v30
v_cvt_pk_bf16_f32 v248,v248,v252
v_accvgpr_write_b32 a82,v224
v_cvt_pk_bf16_f32 v193,v193,v197
v_mfma_f32_16x16x32_bf16 v[216:219],a[232:235],a[248:251],v[40:43]
s_waitcnt lgkmcnt(3)
ds_read_u16 v29,v29
v_accvgpr_write_b32 a83,v248
v_cvt_pk_bf16_f32 v201,v201,v205
v_accvgpr_write_b32 a84,v193
v_cvt_pk_bf16_f32 v225,v225,v229
v_lshlrev_b32_sdwa v54,1,v54 dst_sel:DWORD dst_unused:UNUSED_PAD src0_sel:DWORD src1_sel:WORD_1
v_mfma_f32_16x16x32_bf16 v[220:223],a[232:235],a[228:231],v[40:43]
s_waitcnt lgkmcnt(2)
ds_read_b128 a[232:235],v13 offset:1024
ds_read_b128 v[40:43],v4 offset:3840
v_accvgpr_write_b32 a85,v201
v_cvt_pk_bf16_f32 v249,v249,v253
v_accvgpr_write_b32 a86,v225
v_cvt_pk_bf16_f32 v194,v194,v198
v_mfma_f32_16x16x32_bf16 v[176:179],a[236:239],a[240:243],v[36:39]
s_nop 0
ds_read_b64 a[240:241],v44 offset:57472
ds_read_b64 a[242:243],v44 offset:57480
v_accvgpr_write_b32 a87,v249
v_cvt_pk_bf16_f32 v202,v202,v206
v_accvgpr_write_b32 a88,v194
v_accvgpr_write_b32 a89,v202
v_lshlrev_b32_sdwa v53,1,v53 dst_sel:DWORD dst_unused:UNUSED_PAD src0_sel:DWORD src1_sel:WORD_1
v_mfma_f32_16x16x32_bf16 v[180:183],a[236:239],a[224:227],v[36:39]
ds_read_b64 a[224:225],v45 offset:57472
ds_read_b64 a[226:227],v45 offset:57480
v_cvt_pk_bf16_f32 v226,v226,v230
v_accvgpr_write_b32 a90,v226
v_cvt_pk_bf16_f32 v250,v250,v254
v_accvgpr_write_b32 a91,v250
v_mfma_f32_16x16x32_bf16 v[184:187],a[236:239],a[248:251],v[36:39]
s_nop 0
ds_read_b64 a[248:249],v46 offset:57472
ds_read_b64 a[250:251],v46 offset:57480
v_cvt_pk_bf16_f32 v195,v195,v199
v_accvgpr_write_b32 a92,v195
v_cvt_pk_bf16_f32 v203,v203,v207
v_accvgpr_write_b32 a93,v203
v_mfma_f32_16x16x32_bf16 v[188:191],a[236:239],a[228:231],v[36:39]
s_waitcnt lgkmcnt(4)
ds_read_b64 a[228:229],v47 offset:57472
ds_read_b64 a[230:231],v47 offset:57480
v_cvt_pk_bf16_f32 v227,v227,v231
v_accvgpr_write_b32 a94,v227
v_cvt_pk_bf16_f32 v251,v251,v255
v_lshlrev_b32_sdwa v55,1,v55 dst_sel:DWORD dst_unused:UNUSED_PAD src0_sel:DWORD src1_sel:WORD_1
v_accvgpr_write_b32 a95,v251
v_mfma_f32_16x16x32_bf16 v[248:251],a[232:235],a[240:243],v[40:43]
s_waitcnt lgkmcnt(4)
ds_read_b128 a[236:239],v13 offset:1280
ds_read_b128 v[36:39],v4 offset:3872
v_mfma_f32_16x16x32_bf16 v[252:255],a[232:235],a[224:227],v[40:43]
s_waitcnt lgkmcnt(4)
ds_read_u16_d16_hi v31,v31
ds_read_u16 v52,v52
v_lshlrev_b32_sdwa v56,1,v56 dst_sel:DWORD dst_unused:UNUSED_PAD src0_sel:DWORD src1_sel:WORD_1
v_mfma_f32_16x16x32_bf16 v[224:227],a[232:235],a[248:251],v[40:43]
s_waitcnt lgkmcnt(4)
ds_read_u16_d16_hi v54,v54
v_lshlrev_b32_sdwa v58,1,v58 dst_sel:DWORD dst_unused:UNUSED_PAD src0_sel:DWORD src1_sel:WORD_1
v_mfma_f32_16x16x32_bf16 v[228:231],a[232:235],a[228:231],v[40:43]
s_waitcnt lgkmcnt(3)
ds_read_b64_tr_b16 a[232:233],v3 offset:6224
ds_read_b64_tr_b16 a[234:235],v3 offset:7760
v_mfma_f32_16x16x32_bf16 v[192:195],a[236:239],a[240:243],v[36:39]
ds_read_b64_tr_b16 a[240:241],v3 offset:6232
ds_read_b64_tr_b16 a[242:243],v3 offset:7768
v_mfma_f32_16x16x32_bf16 v[196:199],a[236:239],a[224:227],v[36:39]
ds_read_b64_tr_b16 a[224:225],v3 offset:6288
ds_read_b64_tr_b16 a[226:227],v3 offset:7824
v_lshlrev_b32_sdwa v57,1,v57 dst_sel:DWORD dst_unused:UNUSED_PAD src0_sel:DWORD src1_sel:WORD_1
v_mfma_f32_16x16x32_bf16 v[200:203],a[236:239],a[248:251],v[36:39]
ds_read_b64_tr_b16 a[248:249],v3 offset:6296
ds_read_b64_tr_b16 a[250:251],v3 offset:7832
v_mfma_f32_16x16x32_bf16 v[204:207],a[236:239],a[228:231],v[36:39]
ds_read_u16 v53,v53
ds_read_u16_d16_hi v55,v55
v_lshlrev_b32_sdwa v59,1,v59 dst_sel:DWORD dst_unused:UNUSED_PAD src0_sel:DWORD src1_sel:WORD_1
v_mfma_f32_16x16x32_bf16 v[44:47],a[212:215],a[44:47],v[20:23]
ds_read_u16 v56,v56
v_mfma_f32_16x16x32_bf16 v[44:47],a[208:211],a[28:31],v[44:47]
s_waitcnt vmcnt(18)
ds_read_u16_d16_hi v58,v58
v_mfma_f32_16x16x32_bf16 v[44:47],a[204:207],a[12:15],v[44:47]
s_barrier
ds_read_u16 v57,v57
ds_read_u16_d16_hi v59,v59
v_mfma_f32_16x16x32_bf16 v[40:43],a[204:207],a[48:51],v[20:23]
v_lshlrev_b32_sdwa v32,1,v32 dst_sel:DWORD dst_unused:UNUSED_PAD src0_sel:DWORD src1_sel:WORD_1
v_lshlrev_b32_sdwa v34,1,v34 dst_sel:DWORD dst_unused:UNUSED_PAD src0_sel:DWORD src1_sel:WORD_1
v_mfma_f32_16x16x32_bf16 v[40:43],a[208:211],a[64:67],v[40:43]
ds_read_u16 v32,v32
v_lshlrev_b32_sdwa v33,1,v33 dst_sel:DWORD dst_unused:UNUSED_PAD src0_sel:DWORD src1_sel:WORD_1
v_mfma_f32_16x16x32_bf16 v[40:43],a[212:215],a[80:83],v[40:43]
ds_read_u16_d16_hi v34,v34
v_lshl_add_u64 v[24:25],v[24:25],0,v[26:27]
v_mfma_f32_16x16x32_bf16 v[36:39],a[200:203],a[80:83],v[16:19]
s_waitcnt lgkmcnt(10)
ds_read_u16 v33,v33
v_lshl_add_u64 v[26:27],v[28:29],0,v[30:31]
v_mfma_f32_16x16x32_bf16 v[36:39],a[196:199],a[64:67],v[36:39]
s_waitcnt lgkmcnt(7)
v_lshl_add_u64 v[28:29],v[52:53],0,v[54:55]
v_lshlrev_b32_sdwa v35,1,v35 dst_sel:DWORD dst_unused:UNUSED_PAD src0_sel:DWORD src1_sel:WORD_1
v_mfma_f32_16x16x32_bf16 v[36:39],a[192:195],a[48:51],v[36:39]
ds_read_u16_d16_hi v35,v35
v_lshlrev_b32_sdwa v60,1,v60 dst_sel:DWORD dst_unused:UNUSED_PAD src0_sel:DWORD src1_sel:WORD_1
v_mfma_f32_16x16x32_bf16 v[52:55],a[192:195],a[52:55],v[16:19]
ds_read_u16 v60,v60
v_lshlrev_b32_sdwa v62,1,v62 dst_sel:DWORD dst_unused:UNUSED_PAD src0_sel:DWORD src1_sel:WORD_1
v_mfma_f32_16x16x32_bf16 v[52:55],a[196:199],a[68:71],v[52:55]
s_waitcnt lgkmcnt(5)
ds_read_u16_d16_hi v62,v62
v_lshl_add_u64 v[30:31],v[56:57],0,v[58:59]
v_mfma_f32_16x16x32_bf16 v[52:55],a[200:203],a[84:87],v[52:55]
s_nop 0
v_lshlrev_b32_sdwa v61,1,v61 dst_sel:DWORD dst_unused:UNUSED_PAD src0_sel:DWORD src1_sel:WORD_1
v_lshlrev_b32_sdwa v63,1,v63 dst_sel:DWORD dst_unused:UNUSED_PAD src0_sel:DWORD src1_sel:WORD_1
v_mfma_f32_16x16x32_bf16 v[56:59],a[212:215],a[84:87],v[20:23]
ds_read_u16 v61,v61
v_lshlrev_b32_sdwa v44,1,v44 dst_sel:DWORD dst_unused:UNUSED_PAD src0_sel:DWORD src1_sel:WORD_1
v_mfma_f32_16x16x32_bf16 v[56:59],a[208:211],a[68:71],v[56:59]
ds_read_u16_d16_hi v63,v63
v_lshlrev_b32_sdwa v46,1,v46 dst_sel:DWORD dst_unused:UNUSED_PAD src0_sel:DWORD src1_sel:WORD_1
v_mfma_f32_16x16x32_bf16 v[56:59],a[204:207],a[52:55],v[56:59]
ds_read_u16 v44,v44
v_lshlrev_b32_sdwa v45,1,v45 dst_sel:DWORD dst_unused:UNUSED_PAD src0_sel:DWORD src1_sel:WORD_1
v_mfma_f32_16x16x32_bf16 v[136:139],a[244:247],v[24:27],v[136:139]
ds_read_u16_d16_hi v46,v46
v_lshlrev_b32_sdwa v47,1,v47 dst_sel:DWORD dst_unused:UNUSED_PAD src0_sel:DWORD src1_sel:WORD_1
v_mfma_f32_16x16x32_bf16 v[140:143],a[244:247],v[28:31],v[140:143]
ds_read_u16 v45,v45
v_lshlrev_b32_sdwa v48,1,v48 dst_sel:DWORD dst_unused:UNUSED_PAD src0_sel:DWORD src1_sel:WORD_1
v_mfma_f32_16x16x32_bf16 v[68:71],a[252:255],v[28:31],v[68:71]
ds_read_u16_d16_hi v47,v47
v_lshlrev_b32_sdwa v50,1,v50 dst_sel:DWORD dst_unused:UNUSED_PAD src0_sel:DWORD src1_sel:WORD_1
v_mfma_f32_16x16x32_bf16 v[64:67],a[252:255],v[24:27],v[64:67]
ds_read_u16 v48,v48
v_lshlrev_b32_sdwa v49,1,v49 dst_sel:DWORD dst_unused:UNUSED_PAD src0_sel:DWORD src1_sel:WORD_1
v_mfma_f32_16x16x32_bf16 v[144:147],a[232:235],v[24:27],v[144:147]
ds_read_u16_d16_hi v50,v50
v_lshlrev_b32_sdwa v51,1,v51 dst_sel:DWORD dst_unused:UNUSED_PAD src0_sel:DWORD src1_sel:WORD_1
v_mfma_f32_16x16x32_bf16 v[148:151],a[232:235],v[28:31],v[148:151]
ds_read_u16 v49,v49
v_mfma_f32_16x16x32_bf16 v[84:87],a[240:243],v[28:31],v[84:87]
ds_read_u16_d16_hi v51,v51
v_mfma_f32_16x16x32_bf16 v[80:83],a[240:243],v[24:27],v[80:83]
v_mfma_f32_16x16x32_bf16 v[152:155],a[224:227],v[24:27],v[152:155]
v_mfma_f32_16x16x32_bf16 v[156:159],a[224:227],v[28:31],v[156:159]
v_mfma_f32_16x16x32_bf16 v[100:103],a[248:251],v[28:31],v[100:103]
v_mfma_f32_16x16x32_bf16 v[96:99],a[248:251],v[24:27],v[96:99]
v_mfma_f32_16x16x32_bf16 v[28:31],a[204:207],a[56:59],v[20:23]
v_lshlrev_b32_sdwa v40,1,v40 dst_sel:DWORD dst_unused:UNUSED_PAD src0_sel:DWORD src1_sel:WORD_1
v_fma_mixlo_f16 v8,v53,s99,0
v_mfma_f32_16x16x32_bf16 v[28:31],a[208:211],a[72:75],v[28:31]
ds_read_u16 v40,v40
v_lshlrev_b32_sdwa v42,1,v42 dst_sel:DWORD dst_unused:UNUSED_PAD src0_sel:DWORD src1_sel:WORD_1
v_mfma_f32_16x16x32_bf16 v[28:31],a[212:215],a[88:91],v[28:31]
s_waitcnt lgkmcnt(2)
ds_read_u16_d16_hi v42,v42
v_lshl_add_u64 v[32:33],v[32:33],0,v[34:35]
v_mfma_f32_16x16x32_bf16 v[24:27],a[200:203],a[88:91],v[16:19]
v_lshl_add_u64 v[34:35],v[60:61],0,v[62:63]
v_fma_mixhi_f16 v8,v55,s99,0
v_mfma_f32_16x16x32_bf16 v[24:27],a[196:199],a[72:75],v[24:27]
v_lshl_add_u64 v[60:61],v[44:45],0,v[46:47]
v_lshlrev_b32_sdwa v41,1,v41 dst_sel:DWORD dst_unused:UNUSED_PAD src0_sel:DWORD src1_sel:WORD_1
v_pk_fma_f16 v9,v8,v8,s54
v_mfma_f32_16x16x32_bf16 v[24:27],a[192:195],a[56:59],v[24:27]
ds_read_u16 v41,v41
v_lshlrev_b32_sdwa v43,1,v43 dst_sel:DWORD dst_unused:UNUSED_PAD src0_sel:DWORD src1_sel:WORD_1
v_mfma_f32_16x16x32_bf16 v[44:47],a[192:195],a[60:63],v[16:19]
ds_read_b128 v[16:19],v4 offset:768
ds_read_b64_tr_b16 a[192:193],v1 offset:10256
v_pk_mul_f16 v9,v8,v9
v_mfma_f32_16x16x32_bf16 v[44:47],a[196:199],a[76:79],v[44:47]
s_waitcnt lgkmcnt(5)
ds_read_b64_tr_b16 a[194:195],v1 offset:11280
ds_read_b64_tr_b16 a[196:197],v2 offset:10256
v_lshl_add_u64 v[62:63],v[48:49],0,v[50:51]
v_lshlrev_b32_sdwa v36,1,v36 dst_sel:DWORD dst_unused:UNUSED_PAD src0_sel:DWORD src1_sel:WORD_1
v_mfma_f32_16x16x32_bf16 v[44:47],a[200:203],a[92:95],v[44:47]
ds_read_b64_tr_b16 a[198:199],v2 offset:11280
ds_read_b64_tr_b16 a[200:201],v2 offset:8208
v_exp_f16 v8,v9
v_mfma_f32_16x16x32_bf16 v[48:51],a[212:215],a[92:95],v[20:23]
ds_read_b128 v[20:23],v4 offset:800
ds_read_b64_tr_b16 a[202:203],v2 offset:9232
v_lshlrev_b32_sdwa v38,1,v38 dst_sel:DWORD dst_unused:UNUSED_PAD src0_sel:DWORD src1_sel:WORD_1
v_mfma_f32_16x16x32_bf16 v[48:51],a[208:211],a[76:79],v[48:51]
TE11_%=:
s_cbranch_execz TP10200_%=
ds_read_b64_tr_b16 a[212:213],v2 offset:6160
ds_read_b64_tr_b16 a[214:215],v2 offset:7184
v_lshlrev_b32_sdwa v37,1,v37 dst_sel:DWORD dst_unused:UNUSED_PAD src0_sel:DWORD src1_sel:WORD_1
v_mfma_f32_16x16x32_bf16 v[48:51],a[204:207],a[60:63],v[48:51]
ds_read_u16_d16_hi v43,v43
v_exp_f16_sdwa v8,v9 dst_sel:WORD_1 dst_unused:UNUSED_PRESERVE src0_sel:WORD_1
v_mfma_f32_16x16x32_bf16 v[112:115],a[244:247],v[32:35],v[112:115]
ds_read_u16 v36,v36
v_lshlrev_b32_sdwa v39,1,v39 dst_sel:DWORD dst_unused:UNUSED_PAD src0_sel:DWORD src1_sel:WORD_1
v_mfma_f32_16x16x32_bf16 v[116:119],a[244:247],v[60:63],v[116:119]
ds_read_u16_d16_hi v38,v38
v_lshlrev_b32_sdwa v56,1,v56 dst_sel:DWORD dst_unused:UNUSED_PAD src0_sel:DWORD src1_sel:WORD_1
v_mfma_f32_16x16x32_bf16 v[76:79],a[252:255],v[60:63],v[76:79]
ds_read_u16 v37,v37
v_pk_fma_f16 v8,v8,s55,s55
v_mfma_f32_16x16x32_bf16 v[72:75],a[252:255],v[32:35],v[72:75]
v_rcp_f16_e64 v9,v8
v_lshlrev_b32_sdwa v58,1,v58 dst_sel:DWORD dst_unused:UNUSED_PAD src0_sel:DWORD src1_sel:WORD_1
v_mfma_f32_16x16x32_bf16 v[120:123],a[232:235],v[32:35],v[120:123]
ds_read_u16_d16_hi v39,v39
v_lshlrev_b32_sdwa v57,1,v57 dst_sel:DWORD dst_unused:UNUSED_PAD src0_sel:DWORD src1_sel:WORD_1
v_mfma_f32_16x16x32_bf16 v[124:127],a[232:235],v[60:63],v[124:127]
ds_read_u16 v56,v56
v_lshlrev_b32_sdwa v59,1,v59 dst_sel:DWORD dst_unused:UNUSED_PAD src0_sel:DWORD src1_sel:WORD_1
v_mfma_f32_16x16x32_bf16 v[92:95],a[240:243],v[60:63],v[92:95]
ds_read_u16_d16_hi v58,v58
v_lshlrev_b32_sdwa v52,1,v52 dst_sel:DWORD dst_unused:UNUSED_PAD src0_sel:DWORD src1_sel:WORD_1
v_mfma_f32_16x16x32_bf16 v[88:91],a[240:243],v[32:35],v[88:91]
v_rcp_f16_sdwa v9,v8 dst_sel:WORD_1 dst_unused:UNUSED_PRESERVE src0_sel:WORD_1
v_mfma_f32_16x16x32_bf16 v[128:131],a[224:227],v[32:35],v[128:131]
ds_read_u16 v57,v57
ds_read_u16_d16_hi v59,v59
v_mfma_f32_16x16x32_bf16 v[132:135],a[224:227],v[60:63],v[132:135]
ds_read_u16 v52,v52
v_lshlrev_b32_sdwa v54,1,v54 dst_sel:DWORD dst_unused:UNUSED_PAD src0_sel:DWORD src1_sel:WORD_1
v_mfma_f32_16x16x32_bf16 v[108:111],a[248:251],v[60:63],v[108:111]
ds_read_u16_d16_hi v54,v54
v_fma_mix_f32 v53,v53,v9,0 op_sel_hi:[0,1,0]
v_mfma_f32_16x16x32_bf16 v[104:107],a[248:251],v[32:35],v[104:107]
s_waitcnt lgkmcnt(7)
v_fma_mix_f32 v55,v55,v9,0 op_sel:[0,1,0] op_sel_hi:[0,1,0]
v_mfma_f32_16x16x32_bf16 v[60:63],a[216:219],a[0:3],v[16:19]
s_add_u32 m0,s62,0
v_lshlrev_b32_sdwa v28,1,v28 dst_sel:DWORD dst_unused:UNUSED_PAD src0_sel:DWORD src1_sel:WORD_1
v_lshlrev_b32_sdwa v30,1,v30 dst_sel:DWORD dst_unused:UNUSED_PAD src0_sel:DWORD src1_sel:WORD_1
v_mfma_f32_16x16x32_bf16 v[60:63],a[220:223],a[16:19],v[60:63]
s_mov_b32 s68,s87
ds_read_u16 v28,v28
v_lshlrev_b32_sdwa v29,1,v29 dst_sel:DWORD dst_unused:UNUSED_PAD src0_sel:DWORD src1_sel:WORD_1
v_mfma_f32_16x16x32_bf16 v[60:63],a[192:195],a[32:35],v[60:63]
buffer_load_dwordx4 v5,s[44:47],s68 offen offset:0 lds
v_lshl_add_u64 v[40:41],v[40:41],0,v[42:43]
v_mfma_f32_16x16x32_bf16 v[32:35],a[196:199],a[32:35],v[20:23]
s_waitcnt lgkmcnt(5)
ds_read_u16_d16_hi v30,v30
v_lshl_add_u64 v[42:43],v[36:37],0,v[38:39]
v_mfma_f32_16x16x32_bf16 v[32:35],a[200:203],a[16:19],v[32:35]
s_waitcnt lgkmcnt(4)
ds_read_u16 v29,v29
v_lshl_add_u64 v[36:37],v[56:57],0,v[58:59]
v_mfma_f32_16x16x32_bf16 v[32:35],a[212:215],a[0:3],v[32:35]
s_add_u32 s68,s68,s53
v_lshlrev_b32_sdwa v31,1,v31 dst_sel:DWORD dst_unused:UNUSED_PAD src0_sel:DWORD src1_sel:WORD_1
v_lshlrev_b32_sdwa v24,1,v24 dst_sel:DWORD dst_unused:UNUSED_PAD src0_sel:DWORD src1_sel:WORD_1
v_mfma_f32_16x16x32_bf16 v[56:59],a[212:215],a[4:7],v[20:23]
s_waitcnt lgkmcnt(3)
buffer_load_dwordx4 v5,s[44:47],s68 offen offset:1024 lds
v_or_b32 v38,v52,v54
v_mfma_f32_16x16x32_bf16 v[56:59],a[200:203],a[20:23],v[56:59]
ds_read_u16_d16_hi v31,v31
v_cvt_pk_bf16_f32 v39,v53,v55
v_lshlrev_b32_sdwa v26,1,v26 dst_sel:DWORD dst_unused:UNUSED_PAD src0_sel:DWORD src1_sel:WORD_1
v_mfma_f32_16x16x32_bf16 v[56:59],a[196:199],a[36:39],v[56:59]
ds_read_u16 v24,v24
v_lshlrev_b32_sdwa v25,1,v25 dst_sel:DWORD dst_unused:UNUSED_PAD src0_sel:DWORD src1_sel:WORD_1
v_mfma_f32_16x16x32_bf16 v[52:55],a[192:195],a[36:39],v[16:19]
ds_read_u16_d16_hi v26,v26
v_lshlrev_b32_sdwa v27,1,v27 dst_sel:DWORD dst_unused:UNUSED_PAD src0_sel:DWORD src1_sel:WORD_1
v_mfma_f32_16x16x32_bf16 v[52:55],a[220:223],a[20:23],v[52:55]
ds_read_u16 v25,v25
v_lshlrev_b32_sdwa v48,1,v48 dst_sel:DWORD dst_unused:UNUSED_PAD src0_sel:DWORD src1_sel:WORD_1
v_mfma_f32_16x16x32_bf16 v[52:55],a[216:219],a[4:7],v[52:55]
ds_read_u16_d16_hi v27,v27
v_lshlrev_b32_sdwa v50,1,v50 dst_sel:DWORD dst_unused:UNUSED_PAD src0_sel:DWORD src1_sel:WORD_1
v_mfma_f32_16x16x32_bf16 v[232:235],a[244:247],v[40:43],v[232:235]
ds_read_u16 v48,v48
v_lshlrev_b32_sdwa v49,1,v49 dst_sel:DWORD dst_unused:UNUSED_PAD src0_sel:DWORD src1_sel:WORD_1
v_mfma_f32_16x16x32_bf16 v[236:239],a[244:247],v[36:39],v[236:239]
ds_read_u16_d16_hi v50,v50
v_lshlrev_b32_sdwa v51,1,v51 dst_sel:DWORD dst_unused:UNUSED_PAD src0_sel:DWORD src1_sel:WORD_1
v_mfma_f32_16x16x32_bf16 v[164:167],a[252:255],v[36:39],v[164:167]
ds_read_u16 v49,v49
ds_read_u16_d16_hi v51,v51
v_mfma_f32_16x16x32_bf16 v[160:163],a[252:255],v[40:43],v[160:163]
s_add_u32 s68,s68,s53
ds_read_b64_tr_b16 a[228:229],v3 offset:12320
v_lshlrev_b32_sdwa v44,1,v44 dst_sel:DWORD dst_unused:UNUSED_PAD src0_sel:DWORD src1_sel:WORD_1
v_mfma_f32_16x16x32_bf16 v[240:243],a[232:235],v[40:43],v[240:243]
buffer_load_dwordx4 v5,s[44:47],s68 offen offset:2048 lds
ds_read_u16 v44,v44
v_mfma_f32_16x16x32_bf16 v[244:247],a[232:235],v[36:39],v[244:247]
ds_read_b64_tr_b16 a[230:231],v3 offset:13856
v_lshlrev_b32_sdwa v46,1,v46 dst_sel:DWORD dst_unused:UNUSED_PAD src0_sel:DWORD src1_sel:WORD_1
v_mfma_f32_16x16x32_bf16 v[180:183],a[240:243],v[36:39],v[180:183]
ds_read_u16_d16_hi v46,v46
v_lshlrev_b32_sdwa v45,1,v45 dst_sel:DWORD dst_unused:UNUSED_PAD src0_sel:DWORD src1_sel:WORD_1
v_mfma_f32_16x16x32_bf16 v[176:179],a[240:243],v[40:43],v[176:179]
ds_read_u16 v45,v45
v_lshlrev_b32_sdwa v47,1,v47 dst_sel:DWORD dst_unused:UNUSED_PAD src0_sel:DWORD src1_sel:WORD_1
v_mfma_f32_16x16x32_bf16 v[248:251],a[224:227],v[40:43],v[248:251]
ds_read_u16_d16_hi v47,v47
v_mfma_f32_16x16x32_bf16 v[252:255],a[224:227],v[36:39],v[252:255]
ds_read_b64_tr_b16 a[236:237],v3 offset:12328
v_mfma_f32_16x16x32_bf16 v[196:199],a[248:251],v[36:39],v[196:199]
ds_read_b64_tr_b16 a[238:239],v3 offset:13864
v_mfma_f32_16x16x32_bf16 v[192:195],a[248:251],v[40:43],v[192:195]
v_mfma_f32_16x16x32_bf16 v[36:39],a[216:219],a[8:11],v[16:19]
ds_read_b64_tr_b16 a[208:209],v1 offset:12320
v_lshlrev_b32_sdwa v60,1,v60 dst_sel:DWORD dst_unused:UNUSED_PAD src0_sel:DWORD src1_sel:WORD_1
v_mfma_f32_16x16x32_bf16 v[36:39],a[220:223],a[24:27],v[36:39]
ds_read_u16 v60,v60
v_fma_mixlo_f16 v8,v57,s99,0
v_mfma_f32_16x16x32_bf16 v[36:39],a[192:195],a[40:43],v[36:39]
s_waitcnt lgkmcnt(7)
ds_read_b64_tr_b16 a[210:211],v1 offset:13344
v_lshl_add_u64 v[28:29],v[28:29],0,v[30:31]
v_lshlrev_b32_sdwa v62,1,v62 dst_sel:DWORD dst_unused:UNUSED_PAD src0_sel:DWORD src1_sel:WORD_1
v_mfma_f32_16x16x32_bf16 v[40:43],a[196:199],a[40:43],v[20:23]
ds_read_u16_d16_hi v62,v62
v_lshl_add_u64 v[30:31],v[24:25],0,v[26:27]
v_fma_mixhi_f16 v8,v59,s99,0
v_mfma_f32_16x16x32_bf16 v[40:43],a[200:203],a[24:27],v[40:43]
ds_read_b64_tr_b16 a[204:205],v1 offset:14368
v_lshl_add_u64 v[24:25],v[48:49],0,v[50:51]
v_lshlrev_b32_sdwa v61,1,v61 dst_sel:DWORD dst_unused:UNUSED_PAD src0_sel:DWORD src1_sel:WORD_1
v_pk_fma_f16 v9,v8,v8,s54
v_mfma_f32_16x16x32_bf16 v[40:43],a[212:215],a[8:11],v[40:43]
ds_read_u16 v61,v61
v_lshlrev_b32_sdwa v63,1,v63 dst_sel:DWORD dst_unused:UNUSED_PAD src0_sel:DWORD src1_sel:WORD_1
v_mfma_f32_16x16x32_bf16 v[48:51],a[212:215],a[12:15],v[20:23]
ds_read_u16_d16_hi v63,v63
v_pk_mul_f16 v9,v8,v9
v_mfma_f32_16x16x32_bf16 v[48:51],a[200:203],a[28:31],v[48:51]
s_waitcnt lgkmcnt(7)
ds_read_b64_tr_b16 a[206:207],v1 offset:15392
v_lshl_add_u64 v[26:27],v[44:45],0,v[46:47]
v_lshlrev_b32_sdwa v32,1,v32 dst_sel:DWORD dst_unused:UNUSED_PAD src0_sel:DWORD src1_sel:WORD_1
v_mfma_f32_16x16x32_bf16 v[48:51],a[196:199],a[44:47],v[48:51]
v_exp_f16 v8,v9
v_mfma_f32_16x16x32_bf16 v[44:47],a[192:195],a[44:47],v[16:19]
ds_read_u16 v32,v32
v_lshlrev_b32_sdwa v34,1,v34 dst_sel:DWORD dst_unused:UNUSED_PAD src0_sel:DWORD src1_sel:WORD_1
v_mfma_f32_16x16x32_bf16 v[44:47],a[220:223],a[28:31],v[44:47]
ds_read_u16_d16_hi v34,v34
v_lshlrev_b32_sdwa v33,1,v33 dst_sel:DWORD dst_unused:UNUSED_PAD src0_sel:DWORD src1_sel:WORD_1
v_mfma_f32_16x16x32_bf16 v[44:47],a[216:219],a[12:15],v[44:47]
v_exp_f16_sdwa v8,v9 dst_sel:WORD_1 dst_unused:UNUSED_PRESERVE src0_sel:WORD_1
v_mfma_f32_16x16x32_bf16 v[208:211],a[244:247],v[28:31],v[208:211]
ds_read_u16 v33,v33
v_lshlrev_b32_sdwa v35,1,v35 dst_sel:DWORD dst_unused:UNUSED_PAD src0_sel:DWORD src1_sel:WORD_1
v_mfma_f32_16x16x32_bf16 v[212:215],a[244:247],v[24:27],v[212:215]
ds_read_u16_d16_hi v35,v35
v_lshlrev_b32_sdwa v52,1,v52 dst_sel:DWORD dst_unused:UNUSED_PAD src0_sel:DWORD src1_sel:WORD_1
v_mfma_f32_16x16x32_bf16 v[172:175],a[252:255],v[24:27],v[172:175]
ds_read_u16 v52,v52
v_pk_fma_f16 v8,v8,s55,s55
v_mfma_f32_16x16x32_bf16 v[168:171],a[252:255],v[28:31],v[168:171]
ds_read_b64_tr_b16 a[244:245],v3 offset:12384
v_rcp_f16 v9,v8
v_lshlrev_b32_sdwa v54,1,v54 dst_sel:DWORD dst_unused:UNUSED_PAD src0_sel:DWORD src1_sel:WORD_1
v_mfma_f32_16x16x32_bf16 v[216:219],a[232:235],v[28:31],v[216:219]
ds_read_u16_d16_hi v54,v54
v_lshlrev_b32_sdwa v53,1,v53 dst_sel:DWORD dst_unused:UNUSED_PAD src0_sel:DWORD src1_sel:WORD_1
v_mfma_f32_16x16x32_bf16 v[220:223],a[232:235],v[24:27],v[220:223]
ds_read_u16 v53,v53
ds_read_b64_tr_b16 a[246:247],v3 offset:13920
v_mfma_f32_16x16x32_bf16 v[188:191],a[240:243],v[24:27],v[188:191]
ds_read_b64_tr_b16 a[252:253],v3 offset:12392
v_lshlrev_b32_sdwa v55,1,v55 dst_sel:DWORD dst_unused:UNUSED_PAD src0_sel:DWORD src1_sel:WORD_1
v_mfma_f32_16x16x32_bf16 v[184:187],a[240:243],v[28:31],v[184:187]
ds_read_u16_d16_hi v55,v55
v_rcp_f16_sdwa v9,v8 dst_sel:WORD_1 dst_unused:UNUSED_PRESERVE src0_sel:WORD_1
v_mfma_f32_16x16x32_bf16 v[224:227],a[224:227],v[28:31],v[224:227]
ds_read_b64_tr_b16 a[254:255],v3 offset:13928
ds_read_b64_tr_b16 a[232:233],v3 offset:12448
v_lshlrev_b32_sdwa v56,1,v56 dst_sel:DWORD dst_unused:UNUSED_PAD src0_sel:DWORD src1_sel:WORD_1
v_mfma_f32_16x16x32_bf16 v[228:231],a[224:227],v[24:27],v[228:231]
ds_read_u16 v56,v56
v_lshlrev_b32_sdwa v58,1,v58 dst_sel:DWORD dst_unused:UNUSED_PAD src0_sel:DWORD src1_sel:WORD_1
v_mfma_f32_16x16x32_bf16 v[204:207],a[248:251],v[24:27],v[204:207]
ds_read_u16_d16_hi v58,v58
v_fma_mix_f32 v57,v57,v9,0 op_sel_hi:[0,1,0]
v_mfma_f32_16x16x32_bf16 v[200:203],a[248:251],v[28:31],v[200:203]
ds_read_b64_tr_b16 a[234:235],v3 offset:13984
v_fma_mix_f32 v59,v59,v9,0 op_sel:[0,1,0] op_sel_hi:[0,1,0]
v_mfma_f32_16x16x32_bf16 v[24:27],a[216:219],a[48:51],v[16:19]
s_add_u32 m0,s62,6160
ds_read_b64_tr_b16 a[240:241],v3 offset:12456
v_lshlrev_b32_sdwa v36,1,v36 dst_sel:DWORD dst_unused:UNUSED_PAD src0_sel:DWORD src1_sel:WORD_1
v_mfma_f32_16x16x32_bf16 v[24:27],a[220:223],a[64:67],v[24:27]
s_add_u32 s68,s68,s59
ds_read_b64_tr_b16 a[242:243],v3 offset:13992
v_lshlrev_b32_sdwa v38,1,v38 dst_sel:DWORD dst_unused:UNUSED_PAD src0_sel:DWORD src1_sel:WORD_1
v_mfma_f32_16x16x32_bf16 v[24:27],a[192:195],a[80:83],v[24:27]
s_waitcnt lgkmcnt(9)
buffer_load_dwordx4 v5,s[44:47],s68 offen offset:0 lds
v_lshl_add_u64 v[60:61],v[60:61],0,v[62:63]
v_mfma_f32_16x16x32_bf16 v[28:31],a[196:199],a[80:83],v[20:23]
s_add_u32 s68,s68,s53
ds_read_u16 v36,v36
v_lshl_add_u64 v[62:63],v[32:33],0,v[34:35]
v_lshlrev_b32_sdwa v37,1,v37 dst_sel:DWORD dst_unused:UNUSED_PAD src0_sel:DWORD src1_sel:WORD_1
v_mfma_f32_16x16x32_bf16 v[28:31],a[200:203],a[64:67],v[28:31]
s_waitcnt lgkmcnt(8)
buffer_load_dwordx4 v5,s[44:47],s68 offen offset:1024 lds
v_lshl_add_u64 v[32:33],v[52:53],0,v[54:55]
v_mfma_f32_16x16x32_bf16 v[28:31],a[212:215],a[48:51],v[28:31]
s_add_u32 s68,s68,s53
ds_read_u16_d16_hi v38,v38
v_lshlrev_b32_sdwa v39,1,v39 dst_sel:DWORD dst_unused:UNUSED_PAD src0_sel:DWORD src1_sel:WORD_1
v_mfma_f32_16x16x32_bf16 v[52:55],a[212:215],a[52:55],v[20:23]
s_waitcnt lgkmcnt(5)
buffer_load_dwordx4 v5,s[44:47],s68 offen offset:2048 lds
v_or_b32 v34,v56,v58
v_lshlrev_b32_sdwa v40,1,v40 dst_sel:DWORD dst_unused:UNUSED_PAD src0_sel:DWORD src1_sel:WORD_1
v_mfma_f32_16x16x32_bf16 v[52:55],a[200:203],a[68:71],v[52:55]
ds_read_u16 v37,v37
v_cvt_pk_bf16_f32 v35,v57,v59
v_lshlrev_b32_sdwa v42,1,v42 dst_sel:DWORD dst_unused:UNUSED_PAD src0_sel:DWORD src1_sel:WORD_1
v_mfma_f32_16x16x32_bf16 v[52:55],a[196:199],a[84:87],v[52:55]
ds_read_u16_d16_hi v39,v39
v_lshlrev_b32_sdwa v41,1,v41 dst_sel:DWORD dst_unused:UNUSED_PAD src0_sel:DWORD src1_sel:WORD_1
v_mfma_f32_16x16x32_bf16 v[56:59],a[192:195],a[84:87],v[16:19]
ds_read_u16 v40,v40
v_lshlrev_b32_sdwa v43,1,v43 dst_sel:DWORD dst_unused:UNUSED_PAD src0_sel:DWORD src1_sel:WORD_1
v_mfma_f32_16x16x32_bf16 v[56:59],a[220:223],a[68:71],v[56:59]
ds_read_u16_d16_hi v42,v42
v_lshlrev_b32_sdwa v44,1,v44 dst_sel:DWORD dst_unused:UNUSED_PAD src0_sel:DWORD src1_sel:WORD_1
v_mfma_f32_16x16x32_bf16 v[56:59],a[216:219],a[52:55],v[56:59]
ds_read_u16 v41,v41
v_lshlrev_b32_sdwa v46,1,v46 dst_sel:DWORD dst_unused:UNUSED_PAD src0_sel:DWORD src1_sel:WORD_1
v_mfma_f32_16x16x32_bf16 v[136:139],a[228:231],v[60:63],v[136:139]
ds_read_u16_d16_hi v43,v43
v_lshlrev_b32_sdwa v45,1,v45 dst_sel:DWORD dst_unused:UNUSED_PAD src0_sel:DWORD src1_sel:WORD_1
v_mfma_f32_16x16x32_bf16 v[140:143],a[228:231],v[32:35],v[140:143]
ds_read_u16 v44,v44
v_lshlrev_b32_sdwa v47,1,v47 dst_sel:DWORD dst_unused:UNUSED_PAD src0_sel:DWORD src1_sel:WORD_1
v_mfma_f32_16x16x32_bf16 v[68:71],a[236:239],v[32:35],v[68:71]
ds_read_u16_d16_hi v46,v46
v_lshlrev_b32_sdwa v48,1,v48 dst_sel:DWORD dst_unused:UNUSED_PAD src0_sel:DWORD src1_sel:WORD_1
v_mfma_f32_16x16x32_bf16 v[64:67],a[236:239],v[60:63],v[64:67]
ds_read_u16 v45,v45
v_lshlrev_b32_sdwa v50,1,v50 dst_sel:DWORD dst_unused:UNUSED_PAD src0_sel:DWORD src1_sel:WORD_1
v_mfma_f32_16x16x32_bf16 v[144:147],a[244:247],v[60:63],v[144:147]
s_add_u32 m0,s62,12320
ds_read_u16_d16_hi v47,v47
v_lshlrev_b32_sdwa v49,1,v49 dst_sel:DWORD dst_unused:UNUSED_PAD src0_sel:DWORD src1_sel:WORD_1
v_mfma_f32_16x16x32_bf16 v[148:151],a[244:247],v[32:35],v[148:151]
s_add_u32 s68,s68,s59
ds_read_u16 v48,v48
v_lshlrev_b32_sdwa v51,1,v51 dst_sel:DWORD dst_unused:UNUSED_PAD src0_sel:DWORD src1_sel:WORD_1
v_mfma_f32_16x16x32_bf16 v[84:87],a[252:255],v[32:35],v[84:87]
buffer_load_dwordx4 v5,s[44:47],s68 offen offset:0 lds
v_mfma_f32_16x16x32_bf16 v[80:83],a[252:255],v[60:63],v[80:83]
s_waitcnt lgkmcnt(7)
ds_read_u16_d16_hi v50,v50
v_mfma_f32_16x16x32_bf16 v[152:155],a[232:235],v[60:63],v[152:155]
ds_read_u16 v49,v49
v_mfma_f32_16x16x32_bf16 v[156:159],a[232:235],v[32:35],v[156:159]
ds_read_u16_d16_hi v51,v51
v_mfma_f32_16x16x32_bf16 v[100:103],a[240:243],v[32:35],v[100:103]
v_mfma_f32_16x16x32_bf16 v[96:99],a[240:243],v[60:63],v[96:99]
v_mfma_f32_16x16x32_bf16 v[32:35],a[216:219],a[56:59],v[16:19]
v_lshlrev_b32_sdwa v24,1,v24 dst_sel:DWORD dst_unused:UNUSED_PAD src0_sel:DWORD src1_sel:WORD_1
v_fma_mixlo_f16 v8,v53,s99,0
v_mfma_f32_16x16x32_bf16 v[32:35],a[220:223],a[72:75],v[32:35]
ds_read_u16 v24,v24
v_lshlrev_b32_sdwa v26,1,v26 dst_sel:DWORD dst_unused:UNUSED_PAD src0_sel:DWORD src1_sel:WORD_1
v_mfma_f32_16x16x32_bf16 v[32:35],a[192:195],a[88:91],v[32:35]
ds_read_u16_d16_hi v26,v26
v_lshl_add_u64 v[36:37],v[36:37],0,v[38:39]
v_mfma_f32_16x16x32_bf16 v[60:63],a[196:199],a[88:91],v[20:23]
s_waitcnt lgkmcnt(5)
v_lshl_add_u64 v[38:39],v[40:41],0,v[42:43]
v_fma_mixhi_f16 v8,v55,s99,0
v_mfma_f32_16x16x32_bf16 v[60:63],a[200:203],a[72:75],v[60:63]
v_lshl_add_u64 v[40:41],v[44:45],0,v[46:47]
v_lshlrev_b32_sdwa v25,1,v25 dst_sel:DWORD dst_unused:UNUSED_PAD src0_sel:DWORD src1_sel:WORD_1
v_pk_fma_f16 v9,v8,v8,s54
v_mfma_f32_16x16x32_bf16 v[60:63],a[212:215],a[56:59],v[60:63]
ds_read_u16 v25,v25
v_lshlrev_b32_sdwa v27,1,v27 dst_sel:DWORD dst_unused:UNUSED_PAD src0_sel:DWORD src1_sel:WORD_1
v_mfma_f32_16x16x32_bf16 v[44:47],a[212:215],a[60:63],v[20:23]
ds_read_b128 v[20:23],v4 offset:896
v_pk_mul_f16 v9,v8,v9
v_mfma_f32_16x16x32_bf16 v[44:47],a[200:203],a[76:79],v[44:47]
s_waitcnt lgkmcnt(4)
ds_read_u16_d16_hi v27,v27
ds_read_b64_tr_b16 a[212:213],v1 offset:16416
v_lshl_add_u64 v[42:43],v[48:49],0,v[50:51]
v_lshlrev_b32_sdwa v28,1,v28 dst_sel:DWORD dst_unused:UNUSED_PAD src0_sel:DWORD src1_sel:WORD_1
v_mfma_f32_16x16x32_bf16 v[44:47],a[196:199],a[92:95],v[44:47]
ds_read_b64_tr_b16 a[214:215],v1 offset:17440
v_exp_f16 v8,v9
v_mfma_f32_16x16x32_bf16 v[48:51],a[192:195],a[92:95],v[16:19]
ds_read_b128 v[16:19],v4 offset:928
ds_read_b64_tr_b16 a[200:201],v2 offset:16416
v_lshlrev_b32_sdwa v30,1,v30 dst_sel:DWORD dst_unused:UNUSED_PAD src0_sel:DWORD src1_sel:WORD_1
v_mfma_f32_16x16x32_bf16 v[48:51],a[220:223],a[76:79],v[48:51]
ds_read_b64_tr_b16 a[202:203],v2 offset:17440
ds_read_u16 v28,v28
v_lshlrev_b32_sdwa v29,1,v29 dst_sel:DWORD dst_unused:UNUSED_PAD src0_sel:DWORD src1_sel:WORD_1
v_mfma_f32_16x16x32_bf16 v[48:51],a[216:219],a[60:63],v[48:51]
ds_read_u16_d16_hi v30,v30
v_exp_f16_sdwa v8,v9 dst_sel:WORD_1 dst_unused:UNUSED_PRESERVE src0_sel:WORD_1
v_mfma_f32_16x16x32_bf16 v[112:115],a[228:231],v[36:39],v[112:115]
ds_read_u16 v29,v29
ds_read_b64_tr_b16 a[196:197],v2 offset:14368
v_lshlrev_b32_sdwa v31,1,v31 dst_sel:DWORD dst_unused:UNUSED_PAD src0_sel:DWORD src1_sel:WORD_1
v_mfma_f32_16x16x32_bf16 v[116:119],a[228:231],v[40:43],v[116:119]
ds_read_u16_d16_hi v31,v31
v_lshlrev_b32_sdwa v56,1,v56 dst_sel:DWORD dst_unused:UNUSED_PAD src0_sel:DWORD src1_sel:WORD_1
v_mfma_f32_16x16x32_bf16 v[76:79],a[236:239],v[40:43],v[76:79]
ds_read_b64_tr_b16 a[198:199],v2 offset:15392
v_pk_fma_f16 v8,v8,s55,s55
v_mfma_f32_16x16x32_bf16 v[72:75],a[236:239],v[36:39],v[72:75]
v_rcp_f16 v9,v8
v_lshlrev_b32_sdwa v58,1,v58 dst_sel:DWORD dst_unused:UNUSED_PAD src0_sel:DWORD src1_sel:WORD_1
v_mfma_f32_16x16x32_bf16 v[120:123],a[244:247],v[36:39],v[120:123]
ds_read_u16 v56,v56
v_lshlrev_b32_sdwa v57,1,v57 dst_sel:DWORD dst_unused:UNUSED_PAD src0_sel:DWORD src1_sel:WORD_1
v_mfma_f32_16x16x32_bf16 v[124:127],a[244:247],v[40:43],v[124:127]
ds_read_u16_d16_hi v58,v58
v_lshlrev_b32_sdwa v59,1,v59 dst_sel:DWORD dst_unused:UNUSED_PAD src0_sel:DWORD src1_sel:WORD_1
v_mfma_f32_16x16x32_bf16 v[92:95],a[252:255],v[40:43],v[92:95]
ds_read_u16 v57,v57
v_lshlrev_b32_sdwa v52,1,v52 dst_sel:DWORD dst_unused:UNUSED_PAD src0_sel:DWORD src1_sel:WORD_1
v_mfma_f32_16x16x32_bf16 v[88:91],a[252:255],v[36:39],v[88:91]
ds_read_u16_d16_hi v59,v59
v_rcp_f16_sdwa v9,v8 dst_sel:WORD_1 dst_unused:UNUSED_PRESERVE src0_sel:WORD_1
v_mfma_f32_16x16x32_bf16 v[128:131],a[232:235],v[36:39],v[128:131]
ds_read_b64_tr_b16 a[192:193],v2 offset:12320
v_lshlrev_b32_sdwa v54,1,v54 dst_sel:DWORD dst_unused:UNUSED_PAD src0_sel:DWORD src1_sel:WORD_1
v_mfma_f32_16x16x32_bf16 v[132:135],a[232:235],v[40:43],v[132:135]
ds_read_b64_tr_b16 a[194:195],v2 offset:13344
v_fma_mix_f32 v53,v53,v9,0 op_sel_hi:[0,1,0]
v_mfma_f32_16x16x32_bf16 v[108:111],a[240:243],v[40:43],v[108:111]
ds_read_u16 v52,v52
v_fma_mix_f32 v55,v55,v9,0 op_sel:[0,1,0] op_sel_hi:[0,1,0]
v_mfma_f32_16x16x32_bf16 v[104:107],a[240:243],v[36:39],v[104:107]
s_waitcnt lgkmcnt(7)
ds_read_u16_d16_hi v54,v54
v_mfma_f32_16x16x32_bf16 v[40:43],a[208:211],a[0:3],v[20:23]
s_add_u32 s68,s68,s53
v_lshlrev_b32_sdwa v32,1,v32 dst_sel:DWORD dst_unused:UNUSED_PAD src0_sel:DWORD src1_sel:WORD_1
v_lshlrev_b32_sdwa v34,1,v34 dst_sel:DWORD dst_unused:UNUSED_PAD src0_sel:DWORD src1_sel:WORD_1
v_mfma_f32_16x16x32_bf16 v[40:43],a[204:207],a[16:19],v[40:43]
buffer_load_dwordx4 v5,s[44:47],s68 offen offset:1024 lds
v_lshlrev_b32_sdwa v33,1,v33 dst_sel:DWORD dst_unused:UNUSED_PAD src0_sel:DWORD src1_sel:WORD_1
v_mfma_f32_16x16x32_bf16 v[40:43],a[212:215],a[32:35],v[40:43]
s_add_u32 s68,s68,s53
ds_read_u16 v32,v32
v_lshl_add_u64 v[24:25],v[24:25],0,v[26:27]
v_mfma_f32_16x16x32_bf16 v[36:39],a[200:203],a[32:35],v[16:19]
buffer_load_dwordx4 v5,s[44:47],s68 offen offset:2048 lds
v_lshl_add_u64 v[26:27],v[28:29],0,v[30:31]
v_mfma_f32_16x16x32_bf16 v[36:39],a[196:199],a[16:19],v[36:39]
s_waitcnt lgkmcnt(3)
ds_read_u16_d16_hi v34,v34
v_lshl_add_u64 v[28:29],v[56:57],0,v[58:59]
v_mfma_f32_16x16x32_bf16 v[36:39],a[192:195],a[0:3],v[36:39]
ds_read_u16 v33,v33
v_lshlrev_b32_sdwa v35,1,v35 dst_sel:DWORD dst_unused:UNUSED_PAD src0_sel:DWORD src1_sel:WORD_1
v_mfma_f32_16x16x32_bf16 v[56:59],a[192:195],a[4:7],v[16:19]
s_waitcnt lgkmcnt(3)
ds_read_u16_d16_hi v35,v35
v_or_b32 v30,v52,v54
v_lshlrev_b32_sdwa v60,1,v60 dst_sel:DWORD dst_unused:UNUSED_PAD src0_sel:DWORD src1_sel:WORD_1
v_mfma_f32_16x16x32_bf16 v[56:59],a[196:199],a[20:23],v[56:59]
s_waitcnt vmcnt(17)
ds_read_u16 v60,v60
v_cvt_pk_bf16_f32 v31,v53,v55
v_lshlrev_b32_sdwa v62,1,v62 dst_sel:DWORD dst_unused:UNUSED_PAD src0_sel:DWORD src1_sel:WORD_1
v_mfma_f32_16x16x32_bf16 v[56:59],a[200:203],a[36:39],v[56:59]
s_barrier
ds_read_u16_d16_hi v62,v62
v_lshlrev_b32_sdwa v61,1,v61 dst_sel:DWORD dst_unused:UNUSED_PAD src0_sel:DWORD src1_sel:WORD_1
v_mfma_f32_16x16x32_bf16 v[52:55],a[212:215],a[36:39],v[20:23]
s_add_u32 m0,s62,18480
ds_read_u16 v61,v61
v_lshlrev_b32_sdwa v63,1,v63 dst_sel:DWORD dst_unused:UNUSED_PAD src0_sel:DWORD src1_sel:WORD_1
v_mfma_f32_16x16x32_bf16 v[52:55],a[204:207],a[20:23],v[52:55]
s_add_u32 s68,s68,s59
ds_read_u16_d16_hi v63,v63
v_lshlrev_b32_sdwa v48,1,v48 dst_sel:DWORD dst_unused:UNUSED_PAD src0_sel:DWORD src1_sel:WORD_1
v_mfma_f32_16x16x32_bf16 v[52:55],a[208:211],a[4:7],v[52:55]
buffer_load_dwordx4 v5,s[44:47],s68 offen offset:0 lds
v_lshlrev_b32_sdwa v50,1,v50 dst_sel:DWORD dst_unused:UNUSED_PAD src0_sel:DWORD src1_sel:WORD_1
v_mfma_f32_16x16x32_bf16 v[232:235],a[228:231],v[24:27],v[232:235]
s_add_u32 m0,s58,6160
ds_read_u16 v48,v48
v_lshlrev_b32_sdwa v49,1,v49 dst_sel:DWORD dst_unused:UNUSED_PAD src0_sel:DWORD src1_sel:WORD_1
v_mfma_f32_16x16x32_bf16 v[236:239],a[228:231],v[28:31],v[236:239]
s_add_u32 s69,s69,s59
ds_read_u16_d16_hi v50,v50
v_lshlrev_b32_sdwa v51,1,v51 dst_sel:DWORD dst_unused:UNUSED_PAD src0_sel:DWORD src1_sel:WORD_1
v_mfma_f32_16x16x32_bf16 v[164:167],a[236:239],v[28:31],v[164:167]
buffer_load_dwordx4 v5,s[44:47],s69 offen offset:0 lds
ds_read_u16 v49,v49
v_mfma_f32_16x16x32_bf16 v[160:163],a[236:239],v[24:27],v[160:163]
ds_read_u16_d16_hi v51,v51
ds_read_b64_tr_b16 a[224:225],v3 offset:18480
v_mfma_f32_16x16x32_bf16 v[240:243],a[244:247],v[24:27],v[240:243]
s_add_u32 s69,s69,s53
ds_read_b64_tr_b16 a[226:227],v3 offset:20016
v_lshlrev_b32_sdwa v44,1,v44 dst_sel:DWORD dst_unused:UNUSED_PAD src0_sel:DWORD src1_sel:WORD_1
v_mfma_f32_16x16x32_bf16 v[244:247],a[244:247],v[28:31],v[244:247]
buffer_load_dwordx4 v5,s[44:47],s69 offen offset:1024 lds
v_lshlrev_b32_sdwa v46,1,v46 dst_sel:DWORD dst_unused:UNUSED_PAD src0_sel:DWORD src1_sel:WORD_1
v_mfma_f32_16x16x32_bf16 v[180:183],a[252:255],v[28:31],v[180:183]
ds_read_u16 v44,v44
v_lshlrev_b32_sdwa v45,1,v45 dst_sel:DWORD dst_unused:UNUSED_PAD src0_sel:DWORD src1_sel:WORD_1
v_mfma_f32_16x16x32_bf16 v[176:179],a[252:255],v[24:27],v[176:179]
s_add_u32 m0,s62,18480
ds_read_u16_d16_hi v46,v46
v_lshlrev_b32_sdwa v47,1,v47 dst_sel:DWORD dst_unused:UNUSED_PAD src0_sel:DWORD src1_sel:WORD_1
v_mfma_f32_16x16x32_bf16 v[248:251],a[232:235],v[24:27],v[248:251]
s_add_u32 s68,s68,s53
ds_read_u16 v45,v45
v_mfma_f32_16x16x32_bf16 v[252:255],a[232:235],v[28:31],v[252:255]
buffer_load_dwordx4 v5,s[44:47],s68 offen offset:1024 lds
v_mfma_f32_16x16x32_bf16 v[196:199],a[240:243],v[28:31],v[196:199]
s_add_u32 m0,s58,6160
ds_read_u16_d16_hi v47,v47
v_mfma_f32_16x16x32_bf16 v[192:195],a[240:243],v[24:27],v[192:195]
s_add_u32 s69,s69,s53
ds_read_b64_tr_b16 a[248:249],v3 offset:18488
v_mfma_f32_16x16x32_bf16 v[28:31],a[208:211],a[8:11],v[20:23]
buffer_load_dwordx4 v5,s[44:47],s69 offen offset:2048 lds
v_lshlrev_b32_sdwa v40,1,v40 dst_sel:DWORD dst_unused:UNUSED_PAD src0_sel:DWORD src1_sel:WORD_1
v_mfma_f32_16x16x32_bf16 v[28:31],a[204:207],a[24:27],v[28:31]
ds_read_u16 v40,v40
v_fma_mixlo_f16 v8,v57,s99,0
v_mfma_f32_16x16x32_bf16 v[28:31],a[212:215],a[40:43],v[28:31]
s_waitcnt lgkmcnt(6)
ds_read_b64_tr_b16 a[250:251],v3 offset:20024
v_lshl_add_u64 v[32:33],v[32:33],0,v[34:35]
v_lshlrev_b32_sdwa v42,1,v42 dst_sel:DWORD dst_unused:UNUSED_PAD src0_sel:DWORD src1_sel:WORD_1
v_mfma_f32_16x16x32_bf16 v[24:27],a[200:203],a[40:43],v[16:19]
ds_read_u16_d16_hi v42,v42
v_lshl_add_u64 v[34:35],v[60:61],0,v[62:63]
v_fma_mixhi_f16 v8,v59,s99,0
v_mfma_f32_16x16x32_bf16 v[24:27],a[196:199],a[24:27],v[24:27]
ds_read_b64_tr_b16 a[220:221],v1 offset:18480
v_lshl_add_u64 v[60:61],v[48:49],0,v[50:51]
v_lshlrev_b32_sdwa v41,1,v41 dst_sel:DWORD dst_unused:UNUSED_PAD src0_sel:DWORD src1_sel:WORD_1
v_pk_fma_f16 v9,v8,v8,s54
v_mfma_f32_16x16x32_bf16 v[24:27],a[192:195],a[8:11],v[24:27]
ds_read_u16 v41,v41
v_lshlrev_b32_sdwa v43,1,v43 dst_sel:DWORD dst_unused:UNUSED_PAD src0_sel:DWORD src1_sel:WORD_1
v_mfma_f32_16x16x32_bf16 v[48:51],a[192:195],a[12:15],v[16:19]
ds_read_u16_d16_hi v43,v43
v_pk_mul_f16 v9,v8,v9
v_mfma_f32_16x16x32_bf16 v[48:51],a[196:199],a[28:31],v[48:51]
s_waitcnt lgkmcnt(7)
ds_read_b64_tr_b16 a[222:223],v1 offset:19504
v_lshl_add_u64 v[62:63],v[44:45],0,v[46:47]
v_lshlrev_b32_sdwa v36,1,v36 dst_sel:DWORD dst_unused:UNUSED_PAD src0_sel:DWORD src1_sel:WORD_1
v_mfma_f32_16x16x32_bf16 v[48:51],a[200:203],a[44:47],v[48:51]
v_exp_f16 v8,v9
v_mfma_f32_16x16x32_bf16 v[44:47],a[212:215],a[44:47],v[20:23]
ds_read_u16 v36,v36
v_lshlrev_b32_sdwa v38,1,v38 dst_sel:DWORD dst_unused:UNUSED_PAD src0_sel:DWORD src1_sel:WORD_1
v_mfma_f32_16x16x32_bf16 v[44:47],a[204:207],a[28:31],v[44:47]
ds_read_u16_d16_hi v38,v38
v_lshlrev_b32_sdwa v37,1,v37 dst_sel:DWORD dst_unused:UNUSED_PAD src0_sel:DWORD src1_sel:WORD_1
v_mfma_f32_16x16x32_bf16 v[44:47],a[208:211],a[12:15],v[44:47]
v_exp_f16_sdwa v8,v9 dst_sel:WORD_1 dst_unused:UNUSED_PRESERVE src0_sel:WORD_1
v_mfma_f32_16x16x32_bf16 v[208:211],a[228:231],v[32:35],v[208:211]
ds_read_u16 v37,v37
v_lshlrev_b32_sdwa v39,1,v39 dst_sel:DWORD dst_unused:UNUSED_PAD src0_sel:DWORD src1_sel:WORD_1
v_mfma_f32_16x16x32_bf16 v[212:215],a[228:231],v[60:63],v[212:215]
ds_read_u16_d16_hi v39,v39
v_lshlrev_b32_sdwa v52,1,v52 dst_sel:DWORD dst_unused:UNUSED_PAD src0_sel:DWORD src1_sel:WORD_1
v_mfma_f32_16x16x32_bf16 v[172:175],a[236:239],v[60:63],v[172:175]
ds_read_u16 v52,v52
v_pk_fma_f16 v8,v8,s55,s55
v_mfma_f32_16x16x32_bf16 v[168:171],a[236:239],v[32:35],v[168:171]
ds_read_b64_tr_b16 a[228:229],v3 offset:18544
v_rcp_f16_e64 v9,v8
v_lshlrev_b32_sdwa v54,1,v54 dst_sel:DWORD dst_unused:UNUSED_PAD src0_sel:DWORD src1_sel:WORD_1
v_mfma_f32_16x16x32_bf16 v[216:219],a[244:247],v[32:35],v[216:219]
ds_read_u16_d16_hi v54,v54
v_lshlrev_b32_sdwa v53,1,v53 dst_sel:DWORD dst_unused:UNUSED_PAD src0_sel:DWORD src1_sel:WORD_1
v_mfma_f32_16x16x32_bf16 v[220:223],a[244:247],v[60:63],v[220:223]
ds_read_u16 v53,v53
ds_read_b64_tr_b16 a[230:231],v3 offset:20080
v_mfma_f32_16x16x32_bf16 v[188:191],a[252:255],v[60:63],v[188:191]
ds_read_b64_tr_b16 a[236:237],v3 offset:18552
v_lshlrev_b32_sdwa v55,1,v55 dst_sel:DWORD dst_unused:UNUSED_PAD src0_sel:DWORD src1_sel:WORD_1
v_mfma_f32_16x16x32_bf16 v[184:187],a[252:255],v[32:35],v[184:187]
ds_read_u16_d16_hi v55,v55
v_rcp_f16_sdwa v9,v8 dst_sel:WORD_1 dst_unused:UNUSED_PRESERVE src0_sel:WORD_1
v_mfma_f32_16x16x32_bf16 v[224:227],a[232:235],v[32:35],v[224:227]
ds_read_b64_tr_b16 a[238:239],v3 offset:20088
ds_read_b64_tr_b16 a[244:245],v3 offset:18608
v_lshlrev_b32_sdwa v56,1,v56 dst_sel:DWORD dst_unused:UNUSED_PAD src0_sel:DWORD src1_sel:WORD_1
v_mfma_f32_16x16x32_bf16 v[228:231],a[232:235],v[60:63],v[228:231]
ds_read_u16 v56,v56
v_lshlrev_b32_sdwa v58,1,v58 dst_sel:DWORD dst_unused:UNUSED_PAD src0_sel:DWORD src1_sel:WORD_1
v_mfma_f32_16x16x32_bf16 v[204:207],a[240:243],v[60:63],v[204:207]
ds_read_u16_d16_hi v58,v58
v_fma_mix_f32 v57,v57,v9,0 op_sel_hi:[0,1,0]
v_mfma_f32_16x16x32_bf16 v[200:203],a[240:243],v[32:35],v[200:203]
ds_read_b64_tr_b16 a[246:247],v3 offset:20144
v_fma_mix_f32 v59,v59,v9,0 op_sel:[0,1,0] op_sel_hi:[0,1,0]
v_mfma_f32_16x16x32_bf16 v[60:63],a[208:211],a[48:51],v[20:23]
s_add_u32 m0,s58,12320
ds_read_b64_tr_b16 a[252:253],v3 offset:18616
v_lshlrev_b32_sdwa v28,1,v28 dst_sel:DWORD dst_unused:UNUSED_PAD src0_sel:DWORD src1_sel:WORD_1
v_mfma_f32_16x16x32_bf16 v[60:63],a[204:207],a[64:67],v[60:63]
s_add_u32 s69,s69,s59
ds_read_b64_tr_b16 a[254:255],v3 offset:20152
v_lshlrev_b32_sdwa v30,1,v30 dst_sel:DWORD dst_unused:UNUSED_PAD src0_sel:DWORD src1_sel:WORD_1
v_mfma_f32_16x16x32_bf16 v[60:63],a[212:215],a[80:83],v[60:63]
s_waitcnt lgkmcnt(9)
buffer_load_dwordx4 v5,s[44:47],s69 offen offset:0 lds
ds_read_u16 v28,v28
v_lshl_add_u64 v[40:41],v[40:41],0,v[42:43]
v_mfma_f32_16x16x32_bf16 v[32:35],a[200:203],a[80:83],v[16:19]
TE12_%=:
s_cbranch_execz TP10720_%=
s_add_u32 s69,s69,s53
ds_read_u16_d16_hi v30,v30
v_lshl_add_u64 v[42:43],v[36:37],0,v[38:39]
v_lshlrev_b32_sdwa v29,1,v29 dst_sel:DWORD dst_unused:UNUSED_PAD src0_sel:DWORD src1_sel:WORD_1
v_mfma_f32_16x16x32_bf16 v[32:35],a[196:199],a[64:67],v[32:35]
s_waitcnt lgkmcnt(9)
buffer_load_dwordx4 v5,s[44:47],s69 offen offset:1024 lds
v_lshl_add_u64 v[36:37],v[52:53],0,v[54:55]
v_mfma_f32_16x16x32_bf16 v[32:35],a[192:195],a[48:51],v[32:35]
s_add_u32 m0,s62,18480
ds_read_u16 v29,v29
v_lshlrev_b32_sdwa v31,1,v31 dst_sel:DWORD dst_unused:UNUSED_PAD src0_sel:DWORD src1_sel:WORD_1
v_mfma_f32_16x16x32_bf16 v[52:55],a[192:195],a[52:55],v[16:19]
s_waitcnt lgkmcnt(6)
ds_read_u16_d16_hi v31,v31
v_or_b32_e64 v38,v56,v58
v_lshlrev_b32_sdwa v24,1,v24 dst_sel:DWORD dst_unused:UNUSED_PAD src0_sel:DWORD src1_sel:WORD_1
v_mfma_f32_16x16x32_bf16 v[52:55],a[196:199],a[68:71],v[52:55]
s_add_u32 s68,s68,s53
ds_read_u16 v24,v24
v_cvt_pk_bf16_f32 v39,v57,v59
v_lshlrev_b32_sdwa v26,1,v26 dst_sel:DWORD dst_unused:UNUSED_PAD src0_sel:DWORD src1_sel:WORD_1
v_mfma_f32_16x16x32_bf16 v[52:55],a[200:203],a[84:87],v[52:55]
buffer_load_dwordx4 v5,s[44:47],s68 offen offset:2048 lds
v_lshlrev_b32_sdwa v25,1,v25 dst_sel:DWORD dst_unused:UNUSED_PAD src0_sel:DWORD src1_sel:WORD_1
v_mfma_f32_16x16x32_bf16 v[56:59],a[212:215],a[84:87],v[20:23]
s_add_u32 m0,s58,12320
ds_read_u16_d16_hi v26,v26
v_lshlrev_b32_sdwa v27,1,v27 dst_sel:DWORD dst_unused:UNUSED_PAD src0_sel:DWORD src1_sel:WORD_1
v_mfma_f32_16x16x32_bf16 v[56:59],a[204:207],a[68:71],v[56:59]
s_add_u32 s69,s69,s53
ds_read_u16 v25,v25
v_lshlrev_b32_sdwa v44,1,v44 dst_sel:DWORD dst_unused:UNUSED_PAD src0_sel:DWORD src1_sel:WORD_1
v_mfma_f32_16x16x32_bf16 v[56:59],a[208:211],a[52:55],v[56:59]
buffer_load_dwordx4 v5,s[44:47],s69 offen offset:2048 lds
ds_read_u16_d16_hi v27,v27
v_mfma_f32_16x16x32_bf16 v[136:139],a[224:227],v[40:43],v[136:139]
s_add_u32 m0,s62,24640
ds_read_u16 v44,v44
v_lshlrev_b32_sdwa v46,1,v46 dst_sel:DWORD dst_unused:UNUSED_PAD src0_sel:DWORD src1_sel:WORD_1
v_mfma_f32_16x16x32_bf16 v[140:143],a[224:227],v[36:39],v[140:143]
s_add_u32 s68,s68,s59
ds_read_u16_d16_hi v46,v46
v_lshlrev_b32_sdwa v45,1,v45 dst_sel:DWORD dst_unused:UNUSED_PAD src0_sel:DWORD src1_sel:WORD_1
v_mfma_f32_16x16x32_bf16 v[68:71],a[248:251],v[36:39],v[68:71]
buffer_load_dwordx4 v5,s[44:47],s68 offen offset:0 lds
v_lshlrev_b32_sdwa v47,1,v47 dst_sel:DWORD dst_unused:UNUSED_PAD src0_sel:DWORD src1_sel:WORD_1
v_mfma_f32_16x16x32_bf16 v[64:67],a[248:251],v[40:43],v[64:67]
s_add_u32 s68,s68,s53
ds_read_u16 v45,v45
v_lshlrev_b32_sdwa v48,1,v48 dst_sel:DWORD dst_unused:UNUSED_PAD src0_sel:DWORD src1_sel:WORD_1
v_mfma_f32_16x16x32_bf16 v[144:147],a[228:231],v[40:43],v[144:147]
buffer_load_dwordx4 v5,s[44:47],s68 offen offset:1024 lds
ds_read_u16_d16_hi v47,v47
v_mfma_f32_16x16x32_bf16 v[148:151],a[228:231],v[36:39],v[148:151]
ds_read_u16 v48,v48
v_lshlrev_b32_sdwa v50,1,v50 dst_sel:DWORD dst_unused:UNUSED_PAD src0_sel:DWORD src1_sel:WORD_1
v_mfma_f32_16x16x32_bf16 v[84:87],a[236:239],v[36:39],v[84:87]
ds_read_u16_d16_hi v50,v50
v_lshlrev_b32_sdwa v49,1,v49 dst_sel:DWORD dst_unused:UNUSED_PAD src0_sel:DWORD src1_sel:WORD_1
v_mfma_f32_16x16x32_bf16 v[80:83],a[236:239],v[40:43],v[80:83]
s_waitcnt lgkmcnt(7)
ds_read_u16 v49,v49
v_lshlrev_b32_sdwa v51,1,v51 dst_sel:DWORD dst_unused:UNUSED_PAD src0_sel:DWORD src1_sel:WORD_1
v_mfma_f32_16x16x32_bf16 v[152:155],a[244:247],v[40:43],v[152:155]
ds_read_u16_d16_hi v51,v51
v_mfma_f32_16x16x32_bf16 v[156:159],a[244:247],v[36:39],v[156:159]
s_add_u32 s68,s68,s53
ds_read_b64_tr_b16 a[216:217],v1 offset:20528
v_mfma_f32_16x16x32_bf16 v[100:103],a[252:255],v[36:39],v[100:103]
buffer_load_dwordx4 v5,s[44:47],s68 offen offset:2048 lds
v_mfma_f32_16x16x32_bf16 v[96:99],a[252:255],v[40:43],v[96:99]
ds_read_b64_tr_b16 a[218:219],v1 offset:21552
v_mfma_f32_16x16x32_bf16 v[36:39],a[208:211],a[56:59],v[20:23]
v_lshlrev_b32_sdwa v60,1,v60 dst_sel:DWORD dst_unused:UNUSED_PAD src0_sel:DWORD src1_sel:WORD_1
v_fma_mixlo_f16 v8,v53,s99,0
v_mfma_f32_16x16x32_bf16 v[36:39],a[204:207],a[72:75],v[36:39]
ds_read_u16 v60,v60
v_lshlrev_b32_sdwa v62,1,v62 dst_sel:DWORD dst_unused:UNUSED_PAD src0_sel:DWORD src1_sel:WORD_1
v_mfma_f32_16x16x32_bf16 v[36:39],a[212:215],a[88:91],v[36:39]
ds_read_u16_d16_hi v62,v62
v_lshl_add_u64 v[28:29],v[28:29],0,v[30:31]
v_mfma_f32_16x16x32_bf16 v[40:43],a[200:203],a[88:91],v[16:19]
s_waitcnt lgkmcnt(6)
v_lshl_add_u64 v[30:31],v[24:25],0,v[26:27]
v_fma_mixhi_f16 v8,v55,s99,0
v_mfma_f32_16x16x32_bf16 v[40:43],a[196:199],a[72:75],v[40:43]
v_lshl_add_u64 v[24:25],v[44:45],0,v[46:47]
v_lshlrev_b32_sdwa v61,1,v61 dst_sel:DWORD dst_unused:UNUSED_PAD src0_sel:DWORD src1_sel:WORD_1
v_pk_fma_f16 v9,v8,v8,s54
v_mfma_f32_16x16x32_bf16 v[40:43],a[192:195],a[56:59],v[40:43]
ds_read_u16 v61,v61
v_lshlrev_b32_sdwa v63,1,v63 dst_sel:DWORD dst_unused:UNUSED_PAD src0_sel:DWORD src1_sel:WORD_1
v_mfma_f32_16x16x32_bf16 v[44:47],a[192:195],a[60:63],v[16:19]
ds_read_b128 v[16:19],v4 offset:1024
v_pk_mul_f16 v9,v8,v9
v_mfma_f32_16x16x32_bf16 v[44:47],a[196:199],a[76:79],v[44:47]
s_waitcnt lgkmcnt(5)
ds_read_u16_d16_hi v63,v63
ds_read_b64_tr_b16 a[192:193],v1 offset:22576
v_lshl_add_u64 v[26:27],v[48:49],0,v[50:51]
v_lshlrev_b32_sdwa v32,1,v32 dst_sel:DWORD dst_unused:UNUSED_PAD src0_sel:DWORD src1_sel:WORD_1
v_mfma_f32_16x16x32_bf16 v[44:47],a[200:203],a[92:95],v[44:47]
ds_read_b64_tr_b16 a[194:195],v1 offset:23600
v_exp_f16_e64 v8,v9
v_mfma_f32_16x16x32_bf16 v[48:51],a[212:215],a[92:95],v[20:23]
ds_read_b128 v[20:23],v4 offset:1056
ds_read_b64_tr_b16 a[196:197],v2 offset:22576
v_lshlrev_b32_sdwa v34,1,v34 dst_sel:DWORD dst_unused:UNUSED_PAD src0_sel:DWORD src1_sel:WORD_1
v_mfma_f32_16x16x32_bf16 v[48:51],a[204:207],a[76:79],v[48:51]
ds_read_b64_tr_b16 a[198:199],v2 offset:23600
ds_read_u16 v32,v32
v_lshlrev_b32_sdwa v33,1,v33 dst_sel:DWORD dst_unused:UNUSED_PAD src0_sel:DWORD src1_sel:WORD_1
v_mfma_f32_16x16x32_bf16 v[48:51],a[208:211],a[60:63],v[48:51]
ds_read_u16_d16_hi v34,v34
v_exp_f16_sdwa v8,v9 dst_sel:WORD_1 dst_unused:UNUSED_PRESERVE src0_sel:WORD_1
v_mfma_f32_16x16x32_bf16 v[112:115],a[224:227],v[28:31],v[112:115]
ds_read_u16 v33,v33
ds_read_b64_tr_b16 a[200:201],v2 offset:20528
v_lshlrev_b32_sdwa v35,1,v35 dst_sel:DWORD dst_unused:UNUSED_PAD src0_sel:DWORD src1_sel:WORD_1
v_mfma_f32_16x16x32_bf16 v[116:119],a[224:227],v[24:27],v[116:119]
ds_read_u16_d16_hi v35,v35
v_lshlrev_b32_sdwa v56,1,v56 dst_sel:DWORD dst_unused:UNUSED_PAD src0_sel:DWORD src1_sel:WORD_1
v_mfma_f32_16x16x32_bf16 v[76:79],a[248:251],v[24:27],v[76:79]
ds_read_b64_tr_b16 a[202:203],v2 offset:21552
v_pk_fma_f16 v8,v8,s55,s55
v_mfma_f32_16x16x32_bf16 v[72:75],a[248:251],v[28:31],v[72:75]
v_rcp_f16 v9,v8
v_lshlrev_b32_sdwa v58,1,v58 dst_sel:DWORD dst_unused:UNUSED_PAD src0_sel:DWORD src1_sel:WORD_1
v_mfma_f32_16x16x32_bf16 v[120:123],a[228:231],v[28:31],v[120:123]
ds_read_u16 v56,v56
v_lshlrev_b32_sdwa v57,1,v57 dst_sel:DWORD dst_unused:UNUSED_PAD src0_sel:DWORD src1_sel:WORD_1
v_mfma_f32_16x16x32_bf16 v[124:127],a[228:231],v[24:27],v[124:127]
ds_read_u16_d16_hi v58,v58
v_lshlrev_b32_sdwa v59,1,v59 dst_sel:DWORD dst_unused:UNUSED_PAD src0_sel:DWORD src1_sel:WORD_1
v_mfma_f32_16x16x32_bf16 v[92:95],a[236:239],v[24:27],v[92:95]
ds_read_u16 v57,v57
v_lshlrev_b32_sdwa v52,1,v52 dst_sel:DWORD dst_unused:UNUSED_PAD src0_sel:DWORD src1_sel:WORD_1
v_mfma_f32_16x16x32_bf16 v[88:91],a[236:239],v[28:31],v[88:91]
ds_read_u16_d16_hi v59,v59
v_rcp_f16_sdwa v9,v8 dst_sel:WORD_1 dst_unused:UNUSED_PRESERVE src0_sel:WORD_1
v_mfma_f32_16x16x32_bf16 v[128:131],a[244:247],v[28:31],v[128:131]
ds_read_b64_tr_b16 a[212:213],v2 offset:18480
v_lshlrev_b32_sdwa v54,1,v54 dst_sel:DWORD dst_unused:UNUSED_PAD src0_sel:DWORD src1_sel:WORD_1
v_mfma_f32_16x16x32_bf16 v[132:135],a[244:247],v[24:27],v[132:135]
ds_read_b64_tr_b16 a[214:215],v2 offset:19504
v_fma_mix_f32 v53,v53,v9,0 op_sel_hi:[0,1,0]
v_mfma_f32_16x16x32_bf16 v[108:111],a[252:255],v[24:27],v[108:111]
ds_read_u16 v52,v52
v_fma_mix_f32 v55,v55,v9,0 op_sel:[0,1,0] op_sel_hi:[0,1,0]
v_mfma_f32_16x16x32_bf16 v[104:107],a[252:255],v[28:31],v[104:107]
s_waitcnt lgkmcnt(7)
ds_read_u16_d16_hi v54,v54
v_mfma_f32_16x16x32_bf16 v[24:27],a[220:223],a[0:3],v[16:19]
v_lshlrev_b32_sdwa v36,1,v36 dst_sel:DWORD dst_unused:UNUSED_PAD src0_sel:DWORD src1_sel:WORD_1
v_lshlrev_b32_sdwa v38,1,v38 dst_sel:DWORD dst_unused:UNUSED_PAD src0_sel:DWORD src1_sel:WORD_1
v_mfma_f32_16x16x32_bf16 v[24:27],a[216:219],a[16:19],v[24:27]
ds_read_u16 v36,v36
v_lshlrev_b32_sdwa v37,1,v37 dst_sel:DWORD dst_unused:UNUSED_PAD src0_sel:DWORD src1_sel:WORD_1
v_mfma_f32_16x16x32_bf16 v[24:27],a[192:195],a[32:35],v[24:27]
ds_read_u16_d16_hi v38,v38
v_lshl_add_u64 v[60:61],v[60:61],0,v[62:63]
v_mfma_f32_16x16x32_bf16 v[28:31],a[196:199],a[32:35],v[20:23]
ds_read_u16 v37,v37
v_lshl_add_u64 v[62:63],v[32:33],0,v[34:35]
v_mfma_f32_16x16x32_bf16 v[28:31],a[200:203],a[16:19],v[28:31]
s_waitcnt lgkmcnt(5)
v_lshl_add_u64 v[32:33],v[56:57],0,v[58:59]
v_lshlrev_b32_sdwa v39,1,v39 dst_sel:DWORD dst_unused:UNUSED_PAD src0_sel:DWORD src1_sel:WORD_1
v_mfma_f32_16x16x32_bf16 v[28:31],a[212:215],a[0:3],v[28:31]
ds_read_u16_d16_hi v39,v39
v_lshlrev_b32_sdwa v40,1,v40 dst_sel:DWORD dst_unused:UNUSED_PAD src0_sel:DWORD src1_sel:WORD_1
v_mfma_f32_16x16x32_bf16 v[56:59],a[212:215],a[4:7],v[20:23]
s_waitcnt lgkmcnt(4)
ds_read_u16 v40,v40
v_or_b32 v34,v52,v54
v_mfma_f32_16x16x32_bf16 v[56:59],a[200:203],a[20:23],v[56:59]
v_cvt_pk_bf16_f32 v35,v53,v55
v_lshlrev_b32_sdwa v42,1,v42 dst_sel:DWORD dst_unused:UNUSED_PAD src0_sel:DWORD src1_sel:WORD_1
v_mfma_f32_16x16x32_bf16 v[56:59],a[196:199],a[36:39],v[56:59]
ds_read_u16_d16_hi v42,v42
v_lshlrev_b32_sdwa v41,1,v41 dst_sel:DWORD dst_unused:UNUSED_PAD src0_sel:DWORD src1_sel:WORD_1
v_mfma_f32_16x16x32_bf16 v[52:55],a[192:195],a[36:39],v[16:19]
ds_read_u16 v41,v41
v_lshlrev_b32_sdwa v43,1,v43 dst_sel:DWORD dst_unused:UNUSED_PAD src0_sel:DWORD src1_sel:WORD_1
v_mfma_f32_16x16x32_bf16 v[52:55],a[216:219],a[20:23],v[52:55]
ds_read_u16_d16_hi v43,v43
v_lshlrev_b32_sdwa v48,1,v48 dst_sel:DWORD dst_unused:UNUSED_PAD src0_sel:DWORD src1_sel:WORD_1
v_mfma_f32_16x16x32_bf16 v[52:55],a[220:223],a[4:7],v[52:55]
ds_read_u16 v48,v48
v_lshlrev_b32_sdwa v50,1,v50 dst_sel:DWORD dst_unused:UNUSED_PAD src0_sel:DWORD src1_sel:WORD_1
v_mfma_f32_16x16x32_bf16 v[232:235],a[224:227],v[60:63],v[232:235]
ds_read_u16_d16_hi v50,v50
v_lshlrev_b32_sdwa v49,1,v49 dst_sel:DWORD dst_unused:UNUSED_PAD src0_sel:DWORD src1_sel:WORD_1
v_mfma_f32_16x16x32_bf16 v[236:239],a[224:227],v[32:35],v[236:239]
ds_read_u16 v49,v49
v_lshlrev_b32_sdwa v51,1,v51 dst_sel:DWORD dst_unused:UNUSED_PAD src0_sel:DWORD src1_sel:WORD_1
v_mfma_f32_16x16x32_bf16 v[164:167],a[248:251],v[32:35],v[164:167]
ds_read_u16_d16_hi v51,v51
ds_read_b64_tr_b16 a[232:233],v3 offset:0
v_mfma_f32_16x16x32_bf16 v[160:163],a[248:251],v[60:63],v[160:163]
ds_read_b64_tr_b16 a[234:235],v3 offset:1536
v_lshlrev_b32_sdwa v44,1,v44 dst_sel:DWORD dst_unused:UNUSED_PAD src0_sel:DWORD src1_sel:WORD_1
v_mfma_f32_16x16x32_bf16 v[240:243],a[228:231],v[60:63],v[240:243]
ds_read_u16 v44,v44
v_lshlrev_b32_sdwa v46,1,v46 dst_sel:DWORD dst_unused:UNUSED_PAD src0_sel:DWORD src1_sel:WORD_1
v_mfma_f32_16x16x32_bf16 v[244:247],a[228:231],v[32:35],v[244:247]
ds_read_u16_d16_hi v46,v46
v_lshlrev_b32_sdwa v45,1,v45 dst_sel:DWORD dst_unused:UNUSED_PAD src0_sel:DWORD src1_sel:WORD_1
v_mfma_f32_16x16x32_bf16 v[180:183],a[236:239],v[32:35],v[180:183]
ds_read_u16 v45,v45
v_lshlrev_b32_sdwa v47,1,v47 dst_sel:DWORD dst_unused:UNUSED_PAD src0_sel:DWORD src1_sel:WORD_1
v_mfma_f32_16x16x32_bf16 v[176:179],a[236:239],v[60:63],v[176:179]
ds_read_u16_d16_hi v47,v47
v_mfma_f32_16x16x32_bf16 v[248:251],a[244:247],v[60:63],v[248:251]
ds_read_b64_tr_b16 a[240:241],v3 offset:8
v_mfma_f32_16x16x32_bf16 v[252:255],a[244:247],v[32:35],v[252:255]
ds_read_b64_tr_b16 a[242:243],v3 offset:1544
v_mfma_f32_16x16x32_bf16 v[196:199],a[252:255],v[32:35],v[196:199]
v_mfma_f32_16x16x32_bf16 v[192:195],a[252:255],v[60:63],v[192:195]
v_mfma_f32_16x16x32_bf16 v[32:35],a[220:223],a[8:11],v[16:19]
ds_read_b64_tr_b16 a[204:205],v1 offset:0
v_lshlrev_b32_sdwa v24,1,v24 dst_sel:DWORD dst_unused:UNUSED_PAD src0_sel:DWORD src1_sel:WORD_1
v_mfma_f32_16x16x32_bf16 v[32:35],a[216:219],a[24:27],v[32:35]
ds_read_u16 v24,v24
v_fma_mixlo_f16 v8,v57,s99,0
v_mfma_f32_16x16x32_bf16 v[32:35],a[192:195],a[40:43],v[32:35]
s_waitcnt lgkmcnt(6)
ds_read_b64_tr_b16 a[206:207],v1 offset:1024
v_lshl_add_u64 v[36:37],v[36:37],0,v[38:39]
v_lshlrev_b32_sdwa v26,1,v26 dst_sel:DWORD dst_unused:UNUSED_PAD src0_sel:DWORD src1_sel:WORD_1
v_mfma_f32_16x16x32_bf16 v[60:63],a[196:199],a[40:43],v[20:23]
ds_read_u16_d16_hi v26,v26
v_lshl_add_u64 v[38:39],v[40:41],0,v[42:43]
v_fma_mixhi_f16 v8,v59,s99,0
v_mfma_f32_16x16x32_bf16 v[60:63],a[200:203],a[24:27],v[60:63]
ds_read_b64_tr_b16 a[208:209],v1 offset:2048
v_lshl_add_u64 v[40:41],v[48:49],0,v[50:51]
v_lshlrev_b32_sdwa v25,1,v25 dst_sel:DWORD dst_unused:UNUSED_PAD src0_sel:DWORD src1_sel:WORD_1
v_pk_fma_f16 v9,v8,v8,s54
v_mfma_f32_16x16x32_bf16 v[60:63],a[212:215],a[8:11],v[60:63]
ds_read_u16 v25,v25
v_lshlrev_b32_sdwa v27,1,v27 dst_sel:DWORD dst_unused:UNUSED_PAD src0_sel:DWORD src1_sel:WORD_1
v_mfma_f32_16x16x32_bf16 v[48:51],a[212:215],a[12:15],v[20:23]
ds_read_u16_d16_hi v27,v27
v_pk_mul_f16 v9,v8,v9
v_mfma_f32_16x16x32_bf16 v[48:51],a[200:203],a[28:31],v[48:51]
s_waitcnt lgkmcnt(7)
ds_read_b64_tr_b16 a[210:211],v1 offset:3072
v_lshl_add_u64 v[42:43],v[44:45],0,v[46:47]
v_lshlrev_b32_sdwa v28,1,v28 dst_sel:DWORD dst_unused:UNUSED_PAD src0_sel:DWORD src1_sel:WORD_1
v_mfma_f32_16x16x32_bf16 v[48:51],a[196:199],a[44:47],v[48:51]
v_exp_f16 v8,v9
v_mfma_f32_16x16x32_bf16 v[44:47],a[192:195],a[44:47],v[16:19]
ds_read_u16 v28,v28
v_lshlrev_b32_sdwa v30,1,v30 dst_sel:DWORD dst_unused:UNUSED_PAD src0_sel:DWORD src1_sel:WORD_1
v_mfma_f32_16x16x32_bf16 v[44:47],a[216:219],a[28:31],v[44:47]
ds_read_u16_d16_hi v30,v30
v_lshlrev_b32_sdwa v29,1,v29 dst_sel:DWORD dst_unused:UNUSED_PAD src0_sel:DWORD src1_sel:WORD_1
v_mfma_f32_16x16x32_bf16 v[44:47],a[220:223],a[12:15],v[44:47]
v_exp_f16_sdwa v8,v9 dst_sel:WORD_1 dst_unused:UNUSED_PRESERVE src0_sel:WORD_1
v_mfma_f32_16x16x32_bf16 v[208:211],a[224:227],v[36:39],v[208:211]
ds_read_u16 v29,v29
v_lshlrev_b32_sdwa v31,1,v31 dst_sel:DWORD dst_unused:UNUSED_PAD src0_sel:DWORD src1_sel:WORD_1
v_mfma_f32_16x16x32_bf16 v[212:215],a[224:227],v[40:43],v[212:215]
ds_read_u16_d16_hi v31,v31
v_lshlrev_b32_sdwa v52,1,v52 dst_sel:DWORD dst_unused:UNUSED_PAD src0_sel:DWORD src1_sel:WORD_1
v_mfma_f32_16x16x32_bf16 v[172:175],a[248:251],v[40:43],v[172:175]
ds_read_u16 v52,v52
v_pk_fma_f16 v8,v8,s55,s55
v_mfma_f32_16x16x32_bf16 v[168:171],a[248:251],v[36:39],v[168:171]
ds_read_b64_tr_b16 a[224:225],v3 offset:64
v_rcp_f16 v9,v8
v_lshlrev_b32_sdwa v54,1,v54 dst_sel:DWORD dst_unused:UNUSED_PAD src0_sel:DWORD src1_sel:WORD_1
v_mfma_f32_16x16x32_bf16 v[216:219],a[228:231],v[36:39],v[216:219]
ds_read_u16_d16_hi v54,v54
v_lshlrev_b32_sdwa v53,1,v53 dst_sel:DWORD dst_unused:UNUSED_PAD src0_sel:DWORD src1_sel:WORD_1
v_mfma_f32_16x16x32_bf16 v[220:223],a[228:231],v[40:43],v[220:223]
ds_read_u16 v53,v53
ds_read_b64_tr_b16 a[226:227],v3 offset:1600
v_mfma_f32_16x16x32_bf16 v[188:191],a[236:239],v[40:43],v[188:191]
ds_read_b64_tr_b16 a[248:249],v3 offset:72
v_lshlrev_b32_sdwa v55,1,v55 dst_sel:DWORD dst_unused:UNUSED_PAD src0_sel:DWORD src1_sel:WORD_1
v_mfma_f32_16x16x32_bf16 v[184:187],a[236:239],v[36:39],v[184:187]
ds_read_u16_d16_hi v55,v55
v_rcp_f16_sdwa v9,v8 dst_sel:WORD_1 dst_unused:UNUSED_PRESERVE src0_sel:WORD_1
v_mfma_f32_16x16x32_bf16 v[224:227],a[244:247],v[36:39],v[224:227]
ds_read_b64_tr_b16 a[250:251],v3 offset:1608
ds_read_b64_tr_b16 a[228:229],v3 offset:128
v_lshlrev_b32_sdwa v56,1,v56 dst_sel:DWORD dst_unused:UNUSED_PAD src0_sel:DWORD src1_sel:WORD_1
v_mfma_f32_16x16x32_bf16 v[228:231],a[244:247],v[40:43],v[228:231]
ds_read_u16 v56,v56
v_lshlrev_b32_sdwa v58,1,v58 dst_sel:DWORD dst_unused:UNUSED_PAD src0_sel:DWORD src1_sel:WORD_1
v_mfma_f32_16x16x32_bf16 v[204:207],a[252:255],v[40:43],v[204:207]
ds_read_u16_d16_hi v58,v58
v_fma_mix_f32 v57,v57,v9,0 op_sel_hi:[0,1,0]
v_mfma_f32_16x16x32_bf16 v[200:203],a[252:255],v[36:39],v[200:203]
ds_read_b64_tr_b16 a[230:231],v3 offset:1664
v_fma_mix_f32 v59,v59,v9,0 op_sel:[0,1,0] op_sel_hi:[0,1,0]
v_mbcnt_lo_u32_b32 v8,-1,0
N_qs20_%=:
s_add_u32 s64,s16,N_qs20_%=-N_pf_%=+0
v_and_b32 v8,3,v8
s_mov_b32 m0,12288
v_lshl_add_u32 v8,v8,7,s64
v_min_u32 v8,N_mainend_%=-N_pf_%=+2048,v8
global_load_lds_dword v8,s[14:15]
v_mfma_f32_16x16x32_bf16 v[40:43],a[220:223],a[48:51],v[16:19]
ds_read_b64_tr_b16 a[236:237],v3 offset:136
v_lshlrev_b32_sdwa v32,1,v32 dst_sel:DWORD dst_unused:UNUSED_PAD src0_sel:DWORD src1_sel:WORD_1
v_mfma_f32_16x16x32_bf16 v[40:43],a[216:219],a[64:67],v[40:43]
ds_read_b64_tr_b16 a[238:239],v3 offset:1672
v_lshlrev_b32_sdwa v34,1,v34 dst_sel:DWORD dst_unused:UNUSED_PAD src0_sel:DWORD src1_sel:WORD_1
v_mfma_f32_16x16x32_bf16 v[40:43],a[192:195],a[80:83],v[40:43]
s_waitcnt lgkmcnt(9)
ds_read_u16 v32,v32
v_lshl_add_u64 v[24:25],v[24:25],0,v[26:27]
v_mfma_f32_16x16x32_bf16 v[36:39],a[196:199],a[80:83],v[20:23]
ds_read_u16_d16_hi v34,v34
v_lshl_add_u64 v[26:27],v[28:29],0,v[30:31]
v_lshlrev_b32_sdwa v33,1,v33 dst_sel:DWORD dst_unused:UNUSED_PAD src0_sel:DWORD src1_sel:WORD_1
v_mfma_f32_16x16x32_bf16 v[36:39],a[200:203],a[64:67],v[36:39]
s_waitcnt lgkmcnt(9)
ds_read_u16 v33,v33
v_lshl_add_u64 v[28:29],v[52:53],0,v[54:55]
v_mfma_f32_16x16x32_bf16 v[36:39],a[212:215],a[48:51],v[36:39]
v_lshlrev_b32_sdwa v35,1,v35 dst_sel:DWORD dst_unused:UNUSED_PAD src0_sel:DWORD src1_sel:WORD_1
v_lshlrev_b32_sdwa v60,1,v60 dst_sel:DWORD dst_unused:UNUSED_PAD src0_sel:DWORD src1_sel:WORD_1
v_mfma_f32_16x16x32_bf16 v[52:55],a[212:215],a[52:55],v[20:23]
s_waitcnt lgkmcnt(6)
ds_read_u16_d16_hi v35,v35
v_or_b32 v30,v56,v58
v_mfma_f32_16x16x32_bf16 v[52:55],a[200:203],a[68:71],v[52:55]
ds_read_u16 v60,v60
v_cvt_pk_bf16_f32 v31,v57,v59
v_lshlrev_b32_sdwa v62,1,v62 dst_sel:DWORD dst_unused:UNUSED_PAD src0_sel:DWORD src1_sel:WORD_1
v_mfma_f32_16x16x32_bf16 v[52:55],a[196:199],a[84:87],v[52:55]
ds_read_u16_d16_hi v62,v62
v_lshlrev_b32_sdwa v61,1,v61 dst_sel:DWORD dst_unused:UNUSED_PAD src0_sel:DWORD src1_sel:WORD_1
v_mfma_f32_16x16x32_bf16 v[56:59],a[192:195],a[84:87],v[16:19]
ds_read_u16 v61,v61
v_lshlrev_b32_sdwa v63,1,v63 dst_sel:DWORD dst_unused:UNUSED_PAD src0_sel:DWORD src1_sel:WORD_1
v_mfma_f32_16x16x32_bf16 v[56:59],a[216:219],a[68:71],v[56:59]
ds_read_u16_d16_hi v63,v63
v_lshlrev_b32_sdwa v44,1,v44 dst_sel:DWORD dst_unused:UNUSED_PAD src0_sel:DWORD src1_sel:WORD_1
v_mfma_f32_16x16x32_bf16 v[56:59],a[220:223],a[52:55],v[56:59]
ds_read_u16 v44,v44
v_lshlrev_b32_sdwa v46,1,v46 dst_sel:DWORD dst_unused:UNUSED_PAD src0_sel:DWORD src1_sel:WORD_1
v_mfma_f32_16x16x32_bf16 v[136:139],a[232:235],v[24:27],v[136:139]
ds_read_u16_d16_hi v46,v46
v_lshlrev_b32_sdwa v45,1,v45 dst_sel:DWORD dst_unused:UNUSED_PAD src0_sel:DWORD src1_sel:WORD_1
v_mfma_f32_16x16x32_bf16 v[140:143],a[232:235],v[28:31],v[140:143]
ds_read_u16 v45,v45
v_lshlrev_b32_sdwa v47,1,v47 dst_sel:DWORD dst_unused:UNUSED_PAD src0_sel:DWORD src1_sel:WORD_1
v_mfma_f32_16x16x32_bf16 v[68:71],a[240:243],v[28:31],v[68:71]
ds_read_u16_d16_hi v47,v47
v_lshlrev_b32_sdwa v48,1,v48 dst_sel:DWORD dst_unused:UNUSED_PAD src0_sel:DWORD src1_sel:WORD_1
v_mfma_f32_16x16x32_bf16 v[64:67],a[240:243],v[24:27],v[64:67]
ds_read_u16 v48,v48
v_lshlrev_b32_sdwa v50,1,v50 dst_sel:DWORD dst_unused:UNUSED_PAD src0_sel:DWORD src1_sel:WORD_1
v_mfma_f32_16x16x32_bf16 v[144:147],a[224:227],v[24:27],v[144:147]
ds_read_u16_d16_hi v50,v50
v_lshlrev_b32_sdwa v49,1,v49 dst_sel:DWORD dst_unused:UNUSED_PAD src0_sel:DWORD src1_sel:WORD_1
v_mfma_f32_16x16x32_bf16 v[148:151],a[224:227],v[28:31],v[148:151]
ds_read_u16 v49,v49
v_lshlrev_b32_sdwa v51,1,v51 dst_sel:DWORD dst_unused:UNUSED_PAD src0_sel:DWORD src1_sel:WORD_1
v_mfma_f32_16x16x32_bf16 v[84:87],a[248:251],v[28:31],v[84:87]
ds_read_u16_d16_hi v51,v51
v_mfma_f32_16x16x32_bf16 v[80:83],a[248:251],v[24:27],v[80:83]
s_waitcnt lgkmcnt(8)
v_mfma_f32_16x16x32_bf16 v[152:155],a[228:231],v[24:27],v[152:155]
v_mfma_f32_16x16x32_bf16 v[156:159],a[228:231],v[28:31],v[156:159]
v_mfma_f32_16x16x32_bf16 v[100:103],a[236:239],v[28:31],v[100:103]
v_mfma_f32_16x16x32_bf16 v[96:99],a[236:239],v[24:27],v[96:99]
v_mfma_f32_16x16x32_bf16 v[28:31],a[220:223],a[56:59],v[16:19]
v_lshlrev_b32_sdwa v40,1,v40 dst_sel:DWORD dst_unused:UNUSED_PAD src0_sel:DWORD src1_sel:WORD_1
v_fma_mixlo_f16 v8,v53,s99,0
v_mfma_f32_16x16x32_bf16 v[28:31],a[216:219],a[72:75],v[28:31]
ds_read_u16 v40,v40
v_lshlrev_b32_sdwa v42,1,v42 dst_sel:DWORD dst_unused:UNUSED_PAD src0_sel:DWORD src1_sel:WORD_1
v_mfma_f32_16x16x32_bf16 v[28:31],a[192:195],a[88:91],v[28:31]
ds_read_u16_d16_hi v42,v42
v_lshl_add_u64 v[32:33],v[32:33],0,v[34:35]
v_mfma_f32_16x16x32_bf16 v[24:27],a[196:199],a[88:91],v[20:23]
v_lshl_add_u64 v[34:35],v[60:61],0,v[62:63]
v_fma_mixhi_f16 v8,v55,s99,0
v_mfma_f32_16x16x32_bf16 v[24:27],a[200:203],a[72:75],v[24:27]
s_waitcnt lgkmcnt(2)
v_lshl_add_u64 v[60:61],v[44:45],0,v[46:47]
v_lshlrev_b32_sdwa v41,1,v41 dst_sel:DWORD dst_unused:UNUSED_PAD src0_sel:DWORD src1_sel:WORD_1
v_pk_fma_f16 v9,v8,v8,s54
v_mfma_f32_16x16x32_bf16 v[24:27],a[212:215],a[56:59],v[24:27]
ds_read_u16 v41,v41
v_lshlrev_b32_sdwa v43,1,v43 dst_sel:DWORD dst_unused:UNUSED_PAD src0_sel:DWORD src1_sel:WORD_1
v_mfma_f32_16x16x32_bf16 v[44:47],a[212:215],a[60:63],v[20:23]
ds_read_b128 v[20:23],v4 offset:1152
v_pk_mul_f16 v9,v8,v9
v_mfma_f32_16x16x32_bf16 v[44:47],a[200:203],a[76:79],v[44:47]
ds_read_u16_d16_hi v43,v43
ds_read_b64_tr_b16 a[212:213],v1 offset:4096
v_lshl_add_u64 v[62:63],v[48:49],0,v[50:51]
v_lshlrev_b32_sdwa v36,1,v36 dst_sel:DWORD dst_unused:UNUSED_PAD src0_sel:DWORD src1_sel:WORD_1
v_mfma_f32_16x16x32_bf16 v[44:47],a[196:199],a[92:95],v[44:47]
ds_read_b64_tr_b16 a[214:215],v1 offset:5120
v_exp_f16 v8,v9
v_mfma_f32_16x16x32_bf16 v[48:51],a[192:195],a[92:95],v[16:19]
ds_read_b128 v[16:19],v4 offset:1184
ds_read_b64_tr_b16 a[200:201],v2 offset:4096
v_lshlrev_b32_sdwa v38,1,v38 dst_sel:DWORD dst_unused:UNUSED_PAD src0_sel:DWORD src1_sel:WORD_1
v_mfma_f32_16x16x32_bf16 v[48:51],a[216:219],a[76:79],v[48:51]
ds_read_b64_tr_b16 a[202:203],v2 offset:5120
ds_read_u16 v36,v36
v_lshlrev_b32_sdwa v37,1,v37 dst_sel:DWORD dst_unused:UNUSED_PAD src0_sel:DWORD src1_sel:WORD_1
v_mfma_f32_16x16x32_bf16 v[48:51],a[220:223],a[60:63],v[48:51]
ds_read_u16_d16_hi v38,v38
v_exp_f16_sdwa v8,v9 dst_sel:WORD_1 dst_unused:UNUSED_PRESERVE src0_sel:WORD_1
v_mfma_f32_16x16x32_bf16 v[112:115],a[232:235],v[32:35],v[112:115]
ds_read_u16 v37,v37
ds_read_b64_tr_b16 a[196:197],v2 offset:2048
v_lshlrev_b32_sdwa v39,1,v39 dst_sel:DWORD dst_unused:UNUSED_PAD src0_sel:DWORD src1_sel:WORD_1
v_mfma_f32_16x16x32_bf16 v[116:119],a[232:235],v[60:63],v[116:119]
ds_read_u16_d16_hi v39,v39
v_lshlrev_b32_sdwa v56,1,v56 dst_sel:DWORD dst_unused:UNUSED_PAD src0_sel:DWORD src1_sel:WORD_1
v_mfma_f32_16x16x32_bf16 v[76:79],a[240:243],v[60:63],v[76:79]
ds_read_b64_tr_b16 a[198:199],v2 offset:3072
v_pk_fma_f16 v8,v8,s55,s55
v_mfma_f32_16x16x32_bf16 v[72:75],a[240:243],v[32:35],v[72:75]
v_rcp_f16 v9,v8
v_lshlrev_b32_sdwa v58,1,v58 dst_sel:DWORD dst_unused:UNUSED_PAD src0_sel:DWORD src1_sel:WORD_1
v_mfma_f32_16x16x32_bf16 v[120:123],a[224:227],v[32:35],v[120:123]
ds_read_u16 v56,v56
v_lshlrev_b32_sdwa v57,1,v57 dst_sel:DWORD dst_unused:UNUSED_PAD src0_sel:DWORD src1_sel:WORD_1
v_mfma_f32_16x16x32_bf16 v[124:127],a[224:227],v[60:63],v[124:127]
ds_read_u16_d16_hi v58,v58
v_lshlrev_b32_sdwa v59,1,v59 dst_sel:DWORD dst_unused:UNUSED_PAD src0_sel:DWORD src1_sel:WORD_1
v_mfma_f32_16x16x32_bf16 v[92:95],a[248:251],v[60:63],v[92:95]
ds_read_u16 v57,v57
v_lshlrev_b32_sdwa v52,1,v52 dst_sel:DWORD dst_unused:UNUSED_PAD src0_sel:DWORD src1_sel:WORD_1
v_mfma_f32_16x16x32_bf16 v[88:91],a[248:251],v[32:35],v[88:91]
ds_read_u16_d16_hi v59,v59
v_rcp_f16_sdwa v9,v8 dst_sel:WORD_1 dst_unused:UNUSED_PRESERVE src0_sel:WORD_1
v_mfma_f32_16x16x32_bf16 v[128:131],a[228:231],v[32:35],v[128:131]
ds_read_b64_tr_b16 a[192:193],v2 offset:0
v_lshlrev_b32_sdwa v54,1,v54 dst_sel:DWORD dst_unused:UNUSED_PAD src0_sel:DWORD src1_sel:WORD_1
v_mfma_f32_16x16x32_bf16 v[132:135],a[228:231],v[60:63],v[132:135]
ds_read_b64_tr_b16 a[194:195],v2 offset:1024
v_fma_mix_f32 v53,v53,v9,0 op_sel_hi:[0,1,0]
v_mfma_f32_16x16x32_bf16 v[108:111],a[236:239],v[60:63],v[108:111]
ds_read_u16 v52,v52
v_fma_mix_f32 v55,v55,v9,0 op_sel:[0,1,0] op_sel_hi:[0,1,0]
v_mfma_f32_16x16x32_bf16 v[104:107],a[236:239],v[32:35],v[104:107]
s_waitcnt lgkmcnt(7)
ds_read_u16_d16_hi v54,v54
v_mfma_f32_16x16x32_bf16 v[60:63],a[204:207],a[0:3],v[20:23]
v_lshlrev_b32_sdwa v28,1,v28 dst_sel:DWORD dst_unused:UNUSED_PAD src0_sel:DWORD src1_sel:WORD_1
v_lshlrev_b32_sdwa v30,1,v30 dst_sel:DWORD dst_unused:UNUSED_PAD src0_sel:DWORD src1_sel:WORD_1
v_mfma_f32_16x16x32_bf16 v[60:63],a[208:211],a[16:19],v[60:63]
ds_read_u16 v28,v28
v_lshlrev_b32_sdwa v29,1,v29 dst_sel:DWORD dst_unused:UNUSED_PAD src0_sel:DWORD src1_sel:WORD_1
v_mfma_f32_16x16x32_bf16 v[60:63],a[212:215],a[32:35],v[60:63]
ds_read_u16_d16_hi v30,v30
v_lshl_add_u64 v[40:41],v[40:41],0,v[42:43]
v_mfma_f32_16x16x32_bf16 v[32:35],a[200:203],a[32:35],v[16:19]
ds_read_u16 v29,v29
v_lshl_add_u64 v[42:43],v[36:37],0,v[38:39]
v_mfma_f32_16x16x32_bf16 v[32:35],a[196:199],a[16:19],v[32:35]
s_waitcnt lgkmcnt(5)
v_lshl_add_u64 v[36:37],v[56:57],0,v[58:59]
v_lshlrev_b32_sdwa v31,1,v31 dst_sel:DWORD dst_unused:UNUSED_PAD src0_sel:DWORD src1_sel:WORD_1
v_mfma_f32_16x16x32_bf16 v[32:35],a[192:195],a[0:3],v[32:35]
ds_read_u16_d16_hi v31,v31
v_lshlrev_b32_sdwa v24,1,v24 dst_sel:DWORD dst_unused:UNUSED_PAD src0_sel:DWORD src1_sel:WORD_1
v_mfma_f32_16x16x32_bf16 v[56:59],a[192:195],a[4:7],v[16:19]
s_waitcnt lgkmcnt(4)
ds_read_u16 v24,v24
v_or_b32 v38,v52,v54
v_mfma_f32_16x16x32_bf16 v[56:59],a[196:199],a[20:23],v[56:59]
s_waitcnt vmcnt(4)
v_cvt_pk_bf16_f32 v39,v53,v55
v_lshlrev_b32_sdwa v26,1,v26 dst_sel:DWORD dst_unused:UNUSED_PAD src0_sel:DWORD src1_sel:WORD_1
v_mfma_f32_16x16x32_bf16 v[56:59],a[200:203],a[36:39],v[56:59]
s_barrier
ds_read_u16_d16_hi v26,v26
v_lshlrev_b32_sdwa v25,1,v25 dst_sel:DWORD dst_unused:UNUSED_PAD src0_sel:DWORD src1_sel:WORD_1
v_mfma_f32_16x16x32_bf16 v[52:55],a[212:215],a[36:39],v[20:23]
ds_read_u16 v25,v25
v_lshlrev_b32_sdwa v27,1,v27 dst_sel:DWORD dst_unused:UNUSED_PAD src0_sel:DWORD src1_sel:WORD_1
v_mfma_f32_16x16x32_bf16 v[52:55],a[208:211],a[20:23],v[52:55]
s_add_u32 m0,s58,18480
ds_read_u16_d16_hi v27,v27
v_lshlrev_b32_sdwa v48,1,v48 dst_sel:DWORD dst_unused:UNUSED_PAD src0_sel:DWORD src1_sel:WORD_1
v_mfma_f32_16x16x32_bf16 v[52:55],a[204:207],a[4:7],v[52:55]
s_mov_b32 s69,s88
ds_read_u16 v48,v48
v_lshlrev_b32_sdwa v50,1,v50 dst_sel:DWORD dst_unused:UNUSED_PAD src0_sel:DWORD src1_sel:WORD_1
v_mfma_f32_16x16x32_bf16 v[232:235],a[232:235],v[40:43],v[232:235]
buffer_load_dwordx4 v5,s[44:47],s69 offen offset:0 lds
v_lshlrev_b32_sdwa v49,1,v49 dst_sel:DWORD dst_unused:UNUSED_PAD src0_sel:DWORD src1_sel:WORD_1
v_mfma_f32_16x16x32_bf16 v[236:239],a[232:235],v[36:39],v[236:239]
ds_read_u16_d16_hi v50,v50
v_lshlrev_b32_sdwa v51,1,v51 dst_sel:DWORD dst_unused:UNUSED_PAD src0_sel:DWORD src1_sel:WORD_1
v_mfma_f32_16x16x32_bf16 v[164:167],a[240:243],v[36:39],v[164:167]
ds_read_u16 v49,v49
ds_read_u16_d16_hi v51,v51
v_mfma_f32_16x16x32_bf16 v[160:163],a[240:243],v[40:43],v[160:163]
ds_read_b64_tr_b16 a[244:245],v3 offset:6160
v_lshlrev_b32_sdwa v44,1,v44 dst_sel:DWORD dst_unused:UNUSED_PAD src0_sel:DWORD src1_sel:WORD_1
v_mfma_f32_16x16x32_bf16 v[240:243],a[224:227],v[40:43],v[240:243]
s_add_u32 s69,s69,s53
ds_read_u16 v44,v44
v_lshlrev_b32_sdwa v46,1,v46 dst_sel:DWORD dst_unused:UNUSED_PAD src0_sel:DWORD src1_sel:WORD_1
v_mfma_f32_16x16x32_bf16 v[244:247],a[224:227],v[36:39],v[244:247]
buffer_load_dwordx4 v5,s[44:47],s69 offen offset:1024 lds
ds_read_u16_d16_hi v46,v46
v_mfma_f32_16x16x32_bf16 v[180:183],a[248:251],v[36:39],v[180:183]
ds_read_b64_tr_b16 a[246:247],v3 offset:7696
v_lshlrev_b32_sdwa v45,1,v45 dst_sel:DWORD dst_unused:UNUSED_PAD src0_sel:DWORD src1_sel:WORD_1
v_mfma_f32_16x16x32_bf16 v[176:179],a[248:251],v[40:43],v[176:179]
ds_read_u16 v45,v45
v_lshlrev_b32_sdwa v47,1,v47 dst_sel:DWORD dst_unused:UNUSED_PAD src0_sel:DWORD src1_sel:WORD_1
v_mfma_f32_16x16x32_bf16 v[248:251],a[228:231],v[40:43],v[248:251]
ds_read_u16_d16_hi v47,v47
v_mfma_f32_16x16x32_bf16 v[252:255],a[228:231],v[36:39],v[252:255]
s_add_u32 s69,s69,s53
ds_read_b64_tr_b16 a[252:253],v3 offset:6168
v_mfma_f32_16x16x32_bf16 v[196:199],a[236:239],v[36:39],v[196:199]
buffer_load_dwordx4 v5,s[44:47],s69 offen offset:2048 lds
v_mfma_f32_16x16x32_bf16 v[192:195],a[236:239],v[40:43],v[192:195]
ds_read_b64_tr_b16 a[254:255],v3 offset:7704
v_add_u32 v11,88272,v1
v_add_u32 v12,88272,v2
v_mfma_f32_16x16x32_bf16 v[36:39],a[204:207],a[8:11],v[20:23]
TE13_%=:
s_cbranch_execz TR1_%=
ds_read_b64_tr_b16 a[96:97],v11 offset:0
ds_read_b64_tr_b16 a[98:99],v11 offset:1024
v_lshlrev_b32_sdwa v60,1,v60 dst_sel:DWORD dst_unused:UNUSED_PAD src0_sel:DWORD src1_sel:WORD_1
v_mfma_f32_16x16x32_bf16 v[36:39],a[208:211],a[24:27],v[36:39]
s_nop 0
ds_read_b64_tr_b16 a[100:101],v11 offset:2048
ds_read_b64_tr_b16 a[102:103],v11 offset:3072
v_fma_mixlo_f16 v8,v57,s99,0
v_mfma_f32_16x16x32_bf16 v[36:39],a[212:215],a[40:43],v[36:39]
s_waitcnt lgkmcnt(9)
ds_read_u16 v60,v60
ds_read_b64_tr_b16 a[104:105],v11 offset:4096
v_lshl_add_u64 v[28:29],v[28:29],0,v[30:31]
v_lshlrev_b32_sdwa v62,1,v62 dst_sel:DWORD dst_unused:UNUSED_PAD src0_sel:DWORD src1_sel:WORD_1
v_mfma_f32_16x16x32_bf16 v[40:43],a[200:203],a[40:43],v[16:19]
ds_read_u16_d16_hi v62,v62
ds_read_b64_tr_b16 a[106:107],v11 offset:5120
v_lshl_add_u64 v[30:31],v[24:25],0,v[26:27]
v_fma_mixhi_f16 v8,v59,s99,0
v_mfma_f32_16x16x32_bf16 v[40:43],a[196:199],a[24:27],v[40:43]
ds_read_b64_tr_b16 a[108:109],v12 offset:4096
v_lshl_add_u64 v[24:25],v[48:49],0,v[50:51]
v_lshlrev_b32_sdwa v61,1,v61 dst_sel:DWORD dst_unused:UNUSED_PAD src0_sel:DWORD src1_sel:WORD_1
v_pk_fma_f16 v9,v8,v8,s54
v_mfma_f32_16x16x32_bf16 v[40:43],a[192:195],a[8:11],v[40:43]
ds_read_u16 v61,v61
v_lshlrev_b32_sdwa v63,1,v63 dst_sel:DWORD dst_unused:UNUSED_PAD src0_sel:DWORD src1_sel:WORD_1
v_mfma_f32_16x16x32_bf16 v[48:51],a[192:195],a[12:15],v[16:19]
s_nop 0
ds_read_u16_d16_hi v63,v63
v_pk_mul_f16 v9,v8,v9
v_mfma_f32_16x16x32_bf16 v[48:51],a[196:199],a[28:31],v[48:51]
s_waitcnt lgkmcnt(12)
ds_read_b64_tr_b16 a[110:111],v12 offset:5120
ds_read_b64_tr_b16 a[112:113],v12 offset:2048
v_lshl_add_u64 v[26:27],v[44:45],0,v[46:47]
v_lshlrev_b32_sdwa v32,1,v32 dst_sel:DWORD dst_unused:UNUSED_PAD src0_sel:DWORD src1_sel:WORD_1
v_mfma_f32_16x16x32_bf16 v[48:51],a[200:203],a[44:47],v[48:51]
ds_read_u16 v32,v32
v_exp_f16 v8,v9
v_mfma_f32_16x16x32_bf16 v[44:47],a[212:215],a[44:47],v[20:23]
ds_read_b64_tr_b16 a[114:115],v12 offset:3072
v_lshlrev_b32_sdwa v34,1,v34 dst_sel:DWORD dst_unused:UNUSED_PAD src0_sel:DWORD src1_sel:WORD_1
v_mfma_f32_16x16x32_bf16 v[44:47],a[208:211],a[28:31],v[44:47]
ds_read_u16_d16_hi v34,v34
v_lshlrev_b32_sdwa v33,1,v33 dst_sel:DWORD dst_unused:UNUSED_PAD src0_sel:DWORD src1_sel:WORD_1
v_mfma_f32_16x16x32_bf16 v[44:47],a[204:207],a[12:15],v[44:47]
v_exp_f16_sdwa v8,v9 dst_sel:WORD_1 dst_unused:UNUSED_PRESERVE src0_sel:WORD_1
v_mfma_f32_16x16x32_bf16 v[208:211],a[232:235],v[28:31],v[208:211]
ds_read_u16 v33,v33
v_lshlrev_b32_sdwa v35,1,v35 dst_sel:DWORD dst_unused:UNUSED_PAD src0_sel:DWORD src1_sel:WORD_1
v_mfma_f32_16x16x32_bf16 v[212:215],a[232:235],v[24:27],v[212:215]
ds_read_u16_d16_hi v35,v35
ds_read_b64_tr_b16 a[232:233],v3 offset:6224
v_lshlrev_b32_sdwa v52,1,v52 dst_sel:DWORD dst_unused:UNUSED_PAD src0_sel:DWORD src1_sel:WORD_1
v_mfma_f32_16x16x32_bf16 v[172:175],a[240:243],v[24:27],v[172:175]
ds_read_u16 v52,v52
v_pk_fma_f16 v8,v8,s55,s55
v_mfma_f32_16x16x32_bf16 v[168:171],a[240:243],v[28:31],v[168:171]
ds_read_b64_tr_b16 a[234:235],v3 offset:7760
v_rcp_f16 v9,v8
v_lshlrev_b32_sdwa v54,1,v54 dst_sel:DWORD dst_unused:UNUSED_PAD src0_sel:DWORD src1_sel:WORD_1
v_mfma_f32_16x16x32_bf16 v[216:219],a[224:227],v[28:31],v[216:219]
ds_read_u16_d16_hi v54,v54
ds_read_b64_tr_b16 a[240:241],v3 offset:6232
v_mfma_f32_16x16x32_bf16 v[220:223],a[224:227],v[24:27],v[220:223]
ds_read_b64_tr_b16 a[242:243],v3 offset:7768
ds_read_b64_tr_b16 a[224:225],v3 offset:6288
v_lshlrev_b32_sdwa v53,1,v53 dst_sel:DWORD dst_unused:UNUSED_PAD src0_sel:DWORD src1_sel:WORD_1
v_mfma_f32_16x16x32_bf16 v[188:191],a[248:251],v[24:27],v[188:191]
ds_read_u16 v53,v53
ds_read_b64_tr_b16 a[226:227],v3 offset:7824
v_lshlrev_b32_sdwa v55,1,v55 dst_sel:DWORD dst_unused:UNUSED_PAD src0_sel:DWORD src1_sel:WORD_1
v_mfma_f32_16x16x32_bf16 v[184:187],a[248:251],v[28:31],v[184:187]
ds_read_u16_d16_hi v55,v55
v_rcp_f16_sdwa v9,v8 dst_sel:WORD_1 dst_unused:UNUSED_PRESERVE src0_sel:WORD_1
v_mfma_f32_16x16x32_bf16 v[224:227],a[228:231],v[28:31],v[224:227]
ds_read_b64_tr_b16 a[248:249],v3 offset:6296
ds_read_b64_tr_b16 a[250:251],v3 offset:7832
v_lshlrev_b32_sdwa v56,1,v56 dst_sel:DWORD dst_unused:UNUSED_PAD src0_sel:DWORD src1_sel:WORD_1
v_mfma_f32_16x16x32_bf16 v[228:231],a[228:231],v[24:27],v[228:231]
ds_read_u16 v56,v56
ds_read_b64_tr_b16 a[116:117],v12 offset:0
v_lshlrev_b32_sdwa v58,1,v58 dst_sel:DWORD dst_unused:UNUSED_PAD src0_sel:DWORD src1_sel:WORD_1
v_mfma_f32_16x16x32_bf16 v[204:207],a[236:239],v[24:27],v[204:207]
ds_read_u16_d16_hi v58,v58
v_fma_mix_f32 v57,v57,v9,0 op_sel_hi:[0,1,0]
v_mfma_f32_16x16x32_bf16 v[200:203],a[236:239],v[28:31],v[200:203]
ds_read_b64_tr_b16 a[118:119],v12 offset:1024
ds_read_b64_tr_b16 a[120:121],v12 offset:6160
v_fma_mix_f32 v59,v59,v9,0 op_sel:[0,1,0] op_sel_hi:[0,1,0]
v_mfma_f32_16x16x32_bf16 v[24:27],a[204:207],a[48:51],v[20:23]
ds_read_b64_tr_b16 a[122:123],v12 offset:7184
ds_read_b64_tr_b16 a[216:217],v1 offset:6160
v_lshlrev_b32_sdwa v36,1,v36 dst_sel:DWORD dst_unused:UNUSED_PAD src0_sel:DWORD src1_sel:WORD_1
v_mfma_f32_16x16x32_bf16 v[24:27],a[208:211],a[64:67],v[24:27]
ds_read_u16 v36,v36
ds_read_b64_tr_b16 a[218:219],v1 offset:7184
v_mfma_f32_16x16x32_bf16 v[24:27],a[212:215],a[80:83],v[24:27]
s_waitcnt lgkmcnt(14)
ds_read_b64_tr_b16 a[124:125],v12 offset:8208
v_lshl_add_u64 v[60:61],v[60:61],0,v[62:63]
v_lshlrev_b32_sdwa v38,1,v38 dst_sel:DWORD dst_unused:UNUSED_PAD src0_sel:DWORD src1_sel:WORD_1
v_mfma_f32_16x16x32_bf16 v[28:31],a[200:203],a[80:83],v[16:19]
ds_read_u16_d16_hi v38,v38
v_lshl_add_u64 v[62:63],v[32:33],0,v[34:35]
v_lshlrev_b32_sdwa v37,1,v37 dst_sel:DWORD dst_unused:UNUSED_PAD src0_sel:DWORD src1_sel:WORD_1
v_mfma_f32_16x16x32_bf16 v[28:31],a[196:199],a[64:67],v[28:31]
s_waitcnt lgkmcnt(13)
ds_read_u16 v37,v37
v_lshl_add_u64 v[32:33],v[52:53],0,v[54:55]
v_mfma_f32_16x16x32_bf16 v[28:31],a[192:195],a[48:51],v[28:31]
ds_read_b64_tr_b16 a[126:127],v12 offset:9232
v_lshlrev_b32_sdwa v39,1,v39 dst_sel:DWORD dst_unused:UNUSED_PAD src0_sel:DWORD src1_sel:WORD_1
v_mfma_f32_16x16x32_bf16 v[52:55],a[192:195],a[52:55],v[16:19]
s_waitcnt lgkmcnt(10)
ds_read_u16_d16_hi v39,v39
v_or_b32 v34,v56,v58
v_lshlrev_b32_sdwa v40,1,v40 dst_sel:DWORD dst_unused:UNUSED_PAD src0_sel:DWORD src1_sel:WORD_1
v_mfma_f32_16x16x32_bf16 v[52:55],a[196:199],a[68:71],v[52:55]
ds_read_u16 v40,v40
ds_read_b64_tr_b16 a[220:221],v1 offset:8208
v_cvt_pk_bf16_f32 v35,v57,v59
v_lshlrev_b32_sdwa v42,1,v42 dst_sel:DWORD dst_unused:UNUSED_PAD src0_sel:DWORD src1_sel:WORD_1
v_mfma_f32_16x16x32_bf16 v[52:55],a[200:203],a[84:87],v[52:55]
ds_read_u16_d16_hi v42,v42
v_lshlrev_b32_sdwa v41,1,v41 dst_sel:DWORD dst_unused:UNUSED_PAD src0_sel:DWORD src1_sel:WORD_1
v_mfma_f32_16x16x32_bf16 v[56:59],a[212:215],a[84:87],v[20:23]
ds_read_u16 v41,v41
v_lshlrev_b32_sdwa v43,1,v43 dst_sel:DWORD dst_unused:UNUSED_PAD src0_sel:DWORD src1_sel:WORD_1
v_mfma_f32_16x16x32_bf16 v[56:59],a[208:211],a[68:71],v[56:59]
ds_read_u16_d16_hi v43,v43
v_lshlrev_b32_sdwa v44,1,v44 dst_sel:DWORD dst_unused:UNUSED_PAD src0_sel:DWORD src1_sel:WORD_1
v_mfma_f32_16x16x32_bf16 v[56:59],a[204:207],a[52:55],v[56:59]
ds_read_u16 v44,v44
v_lshlrev_b32_sdwa v46,1,v46 dst_sel:DWORD dst_unused:UNUSED_PAD src0_sel:DWORD src1_sel:WORD_1
v_mfma_f32_16x16x32_bf16 v[136:139],a[244:247],v[60:63],v[136:139]
ds_read_u16_d16_hi v46,v46
v_lshlrev_b32_sdwa v45,1,v45 dst_sel:DWORD dst_unused:UNUSED_PAD src0_sel:DWORD src1_sel:WORD_1
v_mfma_f32_16x16x32_bf16 v[140:143],a[244:247],v[32:35],v[140:143]
ds_read_u16 v45,v45
v_lshlrev_b32_sdwa v47,1,v47 dst_sel:DWORD dst_unused:UNUSED_PAD src0_sel:DWORD src1_sel:WORD_1
v_mfma_f32_16x16x32_bf16 v[68:71],a[252:255],v[32:35],v[68:71]
ds_read_u16_d16_hi v47,v47
v_lshlrev_b32_sdwa v48,1,v48 dst_sel:DWORD dst_unused:UNUSED_PAD src0_sel:DWORD src1_sel:WORD_1
v_mfma_f32_16x16x32_bf16 v[64:67],a[252:255],v[60:63],v[64:67]
ds_read_u16 v48,v48
ds_read_b64_tr_b16 a[222:223],v1 offset:9232
v_mfma_f32_16x16x32_bf16 v[144:147],a[232:235],v[60:63],v[144:147]
ds_read_b64_tr_b16 a[128:129],v12 offset:10256
v_lshlrev_b32_sdwa v50,1,v50 dst_sel:DWORD dst_unused:UNUSED_PAD src0_sel:DWORD src1_sel:WORD_1
v_mfma_f32_16x16x32_bf16 v[148:151],a[232:235],v[32:35],v[148:151]
ds_read_u16_d16_hi v50,v50
v_lshlrev_b32_sdwa v49,1,v49 dst_sel:DWORD dst_unused:UNUSED_PAD src0_sel:DWORD src1_sel:WORD_1
v_mfma_f32_16x16x32_bf16 v[84:87],a[240:243],v[32:35],v[84:87]
ds_read_u16 v49,v49
v_lshlrev_b32_sdwa v51,1,v51 dst_sel:DWORD dst_unused:UNUSED_PAD src0_sel:DWORD src1_sel:WORD_1
v_mfma_f32_16x16x32_bf16 v[80:83],a[240:243],v[60:63],v[80:83]
ds_read_u16_d16_hi v51,v51
v_mfma_f32_16x16x32_bf16 v[152:155],a[224:227],v[60:63],v[152:155]
ds_read_b64_tr_b16 a[130:131],v12 offset:11280
v_mfma_f32_16x16x32_bf16 v[156:159],a[224:227],v[32:35],v[156:159]
ds_read_b64_tr_b16 a[132:133],v11 offset:10256
v_mfma_f32_16x16x32_bf16 v[100:103],a[248:251],v[32:35],v[100:103]
ds_read_b64_tr_b16 a[134:135],v11 offset:11280
v_mfma_f32_16x16x32_bf16 v[96:99],a[248:251],v[60:63],v[96:99]
ds_read_b64_tr_b16 a[136:137],v11 offset:8208
v_mfma_f32_16x16x32_bf16 v[32:35],a[204:207],a[56:59],v[20:23]
ds_read_b64_tr_b16 a[138:139],v11 offset:9232
v_lshlrev_b32_sdwa v24,1,v24 dst_sel:DWORD dst_unused:UNUSED_PAD src0_sel:DWORD src1_sel:WORD_1
v_mfma_f32_16x16x32_bf16 v[32:35],a[208:211],a[72:75],v[32:35]
s_nop 0
ds_read_u16 v24,v24
v_fma_mixlo_f16 v8,v53,s99,0
v_mfma_f32_16x16x32_bf16 v[32:35],a[212:215],a[88:91],v[32:35]
s_waitcnt lgkmcnt(8)
ds_read_b64_tr_b16 a[140:141],v11 offset:6160
v_lshl_add_u64 v[36:37],v[36:37],0,v[38:39]
v_lshlrev_b32_sdwa v26,1,v26 dst_sel:DWORD dst_unused:UNUSED_PAD src0_sel:DWORD src1_sel:WORD_1
v_mfma_f32_16x16x32_bf16 v[60:63],a[200:203],a[88:91],v[16:19]
ds_read_u16_d16_hi v26,v26
v_lshl_add_u64 v[38:39],v[40:41],0,v[42:43]
v_fma_mixhi_f16 v8,v55,s99,0
v_mfma_f32_16x16x32_bf16 v[60:63],a[196:199],a[72:75],v[60:63]
ds_read_b64_tr_b16 a[142:143],v11 offset:7184
v_lshl_add_u64 v[40:41],v[44:45],0,v[46:47]
v_lshlrev_b32_sdwa v25,1,v25 dst_sel:DWORD dst_unused:UNUSED_PAD src0_sel:DWORD src1_sel:WORD_1
v_pk_fma_f16 v9,v8,v8,s54
v_mfma_f32_16x16x32_bf16 v[60:63],a[192:195],a[56:59],v[60:63]
ds_read_u16 v25,v25
v_lshlrev_b32_sdwa v27,1,v27 dst_sel:DWORD dst_unused:UNUSED_PAD src0_sel:DWORD src1_sel:WORD_1
v_mfma_f32_16x16x32_bf16 v[44:47],a[192:195],a[60:63],v[16:19]
s_nop 0
ds_read_b128 v[16:19],v4 offset:1280
v_pk_mul_f16 v9,v8,v9
v_mfma_f32_16x16x32_bf16 v[44:47],a[196:199],a[76:79],v[44:47]
s_waitcnt lgkmcnt(8)
ds_read_u16_d16_hi v27,v27
ds_read_b64_tr_b16 a[192:193],v1 offset:10256
v_lshl_add_u64 v[42:43],v[48:49],0,v[50:51]
v_lshlrev_b32_sdwa v28,1,v28 dst_sel:DWORD dst_unused:UNUSED_PAD src0_sel:DWORD src1_sel:WORD_1
v_mfma_f32_16x16x32_bf16 v[44:47],a[200:203],a[92:95],v[44:47]
ds_read_b64_tr_b16 a[194:195],v1 offset:11280
v_exp_f16_e64 v8,v9
v_mfma_f32_16x16x32_bf16 v[48:51],a[212:215],a[92:95],v[20:23]
ds_read_b128 v[20:23],v4 offset:1312
ds_read_b64_tr_b16 a[196:197],v2 offset:10256
v_lshlrev_b32_sdwa v30,1,v30 dst_sel:DWORD dst_unused:UNUSED_PAD src0_sel:DWORD src1_sel:WORD_1
v_mfma_f32_16x16x32_bf16 v[48:51],a[208:211],a[76:79],v[48:51]
ds_read_b64_tr_b16 a[198:199],v2 offset:11280
ds_read_u16 v28,v28
v_lshlrev_b32_sdwa v29,1,v29 dst_sel:DWORD dst_unused:UNUSED_PAD src0_sel:DWORD src1_sel:WORD_1
v_mfma_f32_16x16x32_bf16 v[48:51],a[204:207],a[60:63],v[48:51]
ds_read_u16_d16_hi v30,v30
v_exp_f16_sdwa v8,v9 dst_sel:WORD_1 dst_unused:UNUSED_PRESERVE src0_sel:WORD_1
v_mfma_f32_16x16x32_bf16 v[112:115],a[244:247],v[36:39],v[112:115]
ds_read_u16 v29,v29
ds_read_b64_tr_b16 a[200:201],v2 offset:8208
v_lshlrev_b32_sdwa v31,1,v31 dst_sel:DWORD dst_unused:UNUSED_PAD src0_sel:DWORD src1_sel:WORD_1
v_mfma_f32_16x16x32_bf16 v[116:119],a[244:247],v[40:43],v[116:119]
ds_read_u16_d16_hi v31,v31
v_lshlrev_b32_sdwa v56,1,v56 dst_sel:DWORD dst_unused:UNUSED_PAD src0_sel:DWORD src1_sel:WORD_1
v_mfma_f32_16x16x32_bf16 v[76:79],a[252:255],v[40:43],v[76:79]
ds_read_b64_tr_b16 a[202:203],v2 offset:9232
v_pk_fma_f16 v8,v8,s55,s55
v_mfma_f32_16x16x32_bf16 v[72:75],a[252:255],v[36:39],v[72:75]
ds_read_u16 v56,v56
v_rcp_f16 v9,v8
v_lshlrev_b32_sdwa v58,1,v58 dst_sel:DWORD dst_unused:UNUSED_PAD src0_sel:DWORD src1_sel:WORD_1
v_mfma_f32_16x16x32_bf16 v[120:123],a[232:235],v[36:39],v[120:123]
ds_read_u16_d16_hi v58,v58
ds_read_b64_tr_b16 a[212:213],v2 offset:6160
v_mfma_f32_16x16x32_bf16 v[124:127],a[232:235],v[40:43],v[124:127]
ds_read_b64_tr_b16 a[214:215],v2 offset:7184
v_lshlrev_b32_sdwa v57,1,v57 dst_sel:DWORD dst_unused:UNUSED_PAD src0_sel:DWORD src1_sel:WORD_1
v_mfma_f32_16x16x32_bf16 v[92:95],a[240:243],v[40:43],v[92:95]
ds_read_u16 v57,v57
v_lshlrev_b32_sdwa v59,1,v59 dst_sel:DWORD dst_unused:UNUSED_PAD src0_sel:DWORD src1_sel:WORD_1
v_mfma_f32_16x16x32_bf16 v[88:91],a[240:243],v[36:39],v[88:91]
v_rcp_f16_sdwa v9,v8 dst_sel:WORD_1 dst_unused:UNUSED_PRESERVE src0_sel:WORD_1
v_mfma_f32_16x16x32_bf16 v[128:131],a[224:227],v[36:39],v[128:131]
ds_read_u16_d16_hi v59,v59
v_lshlrev_b32_sdwa v52,1,v52 dst_sel:DWORD dst_unused:UNUSED_PAD src0_sel:DWORD src1_sel:WORD_1
v_mfma_f32_16x16x32_bf16 v[132:135],a[224:227],v[40:43],v[132:135]
ds_read_u16 v52,v52
v_lshlrev_b32_sdwa v54,1,v54 dst_sel:DWORD dst_unused:UNUSED_PAD src0_sel:DWORD src1_sel:WORD_1
v_mfma_f32_16x16x32_bf16 v[108:111],a[248:251],v[40:43],v[108:111]
ds_read_u16_d16_hi v54,v54
v_fma_mix_f32 v53,v53,v9,0 op_sel_hi:[0,1,0]
v_mfma_f32_16x16x32_bf16 v[104:107],a[248:251],v[36:39],v[104:107]
s_waitcnt lgkmcnt(8)
ds_read_b64_tr_b16 a[144:145],v11 offset:12320
v_fma_mix_f32 v55,v55,v9,0 op_sel:[0,1,0] op_sel_hi:[0,1,0]
v_mfma_f32_16x16x32_bf16 v[40:43],a[216:219],a[0:3],v[16:19]
ds_read_b64_tr_b16 a[146:147],v11 offset:13344
v_lshlrev_b32_sdwa v32,1,v32 dst_sel:DWORD dst_unused:UNUSED_PAD src0_sel:DWORD src1_sel:WORD_1
v_mfma_f32_16x16x32_bf16 v[40:43],a[220:223],a[16:19],v[40:43]
ds_read_u16 v32,v32
v_lshlrev_b32_sdwa v34,1,v34 dst_sel:DWORD dst_unused:UNUSED_PAD src0_sel:DWORD src1_sel:WORD_1
v_mfma_f32_16x16x32_bf16 v[40:43],a[192:195],a[32:35],v[40:43]
ds_read_u16_d16_hi v34,v34
v_lshl_add_u64 v[24:25],v[24:25],0,v[26:27]
v_mfma_f32_16x16x32_bf16 v[36:39],a[196:199],a[32:35],v[20:23]
ds_read_b64_tr_b16 a[148:149],v11 offset:14368
v_lshl_add_u64 v[26:27],v[28:29],0,v[30:31]
v_lshlrev_b32_sdwa v33,1,v33 dst_sel:DWORD dst_unused:UNUSED_PAD src0_sel:DWORD src1_sel:WORD_1
v_mfma_f32_16x16x32_bf16 v[36:39],a[200:203],a[16:19],v[36:39]
s_waitcnt lgkmcnt(7)
ds_read_u16 v33,v33
v_lshl_add_u64 v[28:29],v[56:57],0,v[58:59]
v_mfma_f32_16x16x32_bf16 v[36:39],a[212:215],a[0:3],v[36:39]
ds_read_b64_tr_b16 a[150:151],v11 offset:15392
v_lshlrev_b32_sdwa v35,1,v35 dst_sel:DWORD dst_unused:UNUSED_PAD src0_sel:DWORD src1_sel:WORD_1
v_mfma_f32_16x16x32_bf16 v[56:59],a[212:215],a[4:7],v[20:23]
s_waitcnt lgkmcnt(7)
ds_read_u16_d16_hi v35,v35
v_or_b32 v30,v52,v54
v_lshlrev_b32_sdwa v60,1,v60 dst_sel:DWORD dst_unused:UNUSED_PAD src0_sel:DWORD src1_sel:WORD_1
v_mfma_f32_16x16x32_bf16 v[56:59],a[200:203],a[20:23],v[56:59]
ds_read_u16 v60,v60
v_cvt_pk_bf16_f32 v31,v53,v55
v_lshlrev_b32_sdwa v62,1,v62 dst_sel:DWORD dst_unused:UNUSED_PAD src0_sel:DWORD src1_sel:WORD_1
v_mfma_f32_16x16x32_bf16 v[56:59],a[196:199],a[36:39],v[56:59]
ds_read_u16_d16_hi v62,v62
v_lshlrev_b32_sdwa v61,1,v61 dst_sel:DWORD dst_unused:UNUSED_PAD src0_sel:DWORD src1_sel:WORD_1
v_mfma_f32_16x16x32_bf16 v[52:55],a[192:195],a[36:39],v[16:19]
ds_read_u16 v61,v61
v_lshlrev_b32_sdwa v63,1,v63 dst_sel:DWORD dst_unused:UNUSED_PAD src0_sel:DWORD src1_sel:WORD_1
v_mfma_f32_16x16x32_bf16 v[52:55],a[220:223],a[20:23],v[52:55]
ds_read_u16_d16_hi v63,v63
v_lshlrev_b32_sdwa v48,1,v48 dst_sel:DWORD dst_unused:UNUSED_PAD src0_sel:DWORD src1_sel:WORD_1
v_mfma_f32_16x16x32_bf16 v[52:55],a[216:219],a[4:7],v[52:55]
ds_read_u16 v48,v48
v_lshlrev_b32_sdwa v50,1,v50 dst_sel:DWORD dst_unused:UNUSED_PAD src0_sel:DWORD src1_sel:WORD_1
v_mfma_f32_16x16x32_bf16 v[232:235],a[244:247],v[24:27],v[232:235]
ds_read_u16_d16_hi v50,v50
ds_read_b64_tr_b16 a[152:153],v11 offset:16416
v_mfma_f32_16x16x32_bf16 v[236:239],a[244:247],v[28:31],v[236:239]
ds_read_b64_tr_b16 a[154:155],v11 offset:17440
v_lshlrev_b32_sdwa v49,1,v49 dst_sel:DWORD dst_unused:UNUSED_PAD src0_sel:DWORD src1_sel:WORD_1
v_mfma_f32_16x16x32_bf16 v[164:167],a[252:255],v[28:31],v[164:167]
ds_read_u16 v49,v49
ds_read_b64_tr_b16 a[228:229],v3 offset:12320
v_mfma_f32_16x16x32_bf16 v[160:163],a[252:255],v[24:27],v[160:163]
ds_read_b64_tr_b16 a[230:231],v3 offset:13856
ds_read_b64_tr_b16 a[236:237],v3 offset:12328
v_mfma_f32_16x16x32_bf16 v[240:243],a[232:235],v[24:27],v[240:243]
ds_read_b64_tr_b16 a[238:239],v3 offset:13864
v_lshlrev_b32_sdwa v51,1,v51 dst_sel:DWORD dst_unused:UNUSED_PAD src0_sel:DWORD src1_sel:WORD_1
v_mfma_f32_16x16x32_bf16 v[244:247],a[232:235],v[28:31],v[244:247]
ds_read_u16_d16_hi v51,v51
v_lshlrev_b32_sdwa v44,1,v44 dst_sel:DWORD dst_unused:UNUSED_PAD src0_sel:DWORD src1_sel:WORD_1
v_mfma_f32_16x16x32_bf16 v[180:183],a[240:243],v[28:31],v[180:183]
ds_read_u16 v44,v44
v_lshlrev_b32_sdwa v46,1,v46 dst_sel:DWORD dst_unused:UNUSED_PAD src0_sel:DWORD src1_sel:WORD_1
v_mfma_f32_16x16x32_bf16 v[176:179],a[240:243],v[24:27],v[176:179]
ds_read_u16_d16_hi v46,v46
v_lshlrev_b32_sdwa v45,1,v45 dst_sel:DWORD dst_unused:UNUSED_PAD src0_sel:DWORD src1_sel:WORD_1
v_mfma_f32_16x16x32_bf16 v[248:251],a[224:227],v[24:27],v[248:251]
ds_read_u16 v45,v45
v_lshlrev_b32_sdwa v47,1,v47 dst_sel:DWORD dst_unused:UNUSED_PAD src0_sel:DWORD src1_sel:WORD_1
v_mfma_f32_16x16x32_bf16 v[252:255],a[224:227],v[28:31],v[252:255]
ds_read_u16_d16_hi v47,v47
v_mfma_f32_16x16x32_bf16 v[196:199],a[248:251],v[28:31],v[196:199]
ds_read_b64_tr_b16 a[156:157],v12 offset:16416
v_mfma_f32_16x16x32_bf16 v[192:195],a[248:251],v[24:27],v[192:195]
ds_read_b64_tr_b16 a[158:159],v12 offset:17440
v_mfma_f32_16x16x32_bf16 v[28:31],a[216:219],a[8:11],v[16:19]
ds_read_b64_tr_b16 a[160:161],v12 offset:14368
v_lshlrev_b32_sdwa v40,1,v40 dst_sel:DWORD dst_unused:UNUSED_PAD src0_sel:DWORD src1_sel:WORD_1
v_mfma_f32_16x16x32_bf16 v[28:31],a[220:223],a[24:27],v[28:31]
ds_read_u16 v40,v40
v_fma_mixlo_f16 v8,v57,s99,0
v_mfma_f32_16x16x32_bf16 v[28:31],a[192:195],a[40:43],v[28:31]
s_waitcnt lgkmcnt(8)
ds_read_b64_tr_b16 a[162:163],v12 offset:15392
v_lshl_add_u64 v[32:33],v[32:33],0,v[34:35]
v_lshlrev_b32_sdwa v42,1,v42 dst_sel:DWORD dst_unused:UNUSED_PAD src0_sel:DWORD src1_sel:WORD_1
v_mfma_f32_16x16x32_bf16 v[24:27],a[196:199],a[40:43],v[20:23]
ds_read_u16_d16_hi v42,v42
v_lshl_add_u64 v[34:35],v[60:61],0,v[62:63]
v_fma_mixhi_f16 v8,v59,s99,0
v_mfma_f32_16x16x32_bf16 v[24:27],a[200:203],a[24:27],v[24:27]
ds_read_b64_tr_b16 a[164:165],v12 offset:12320
v_lshl_add_u64 v[60:61],v[48:49],0,v[50:51]
v_lshlrev_b32_sdwa v41,1,v41 dst_sel:DWORD dst_unused:UNUSED_PAD src0_sel:DWORD src1_sel:WORD_1
v_pk_fma_f16 v9,v8,v8,s54
v_mfma_f32_16x16x32_bf16 v[24:27],a[212:215],a[8:11],v[24:27]
ds_read_u16 v41,v41
v_lshlrev_b32_sdwa v43,1,v43 dst_sel:DWORD dst_unused:UNUSED_PAD src0_sel:DWORD src1_sel:WORD_1
v_mfma_f32_16x16x32_bf16 v[48:51],a[212:215],a[12:15],v[20:23]
ds_read_u16_d16_hi v43,v43
v_pk_mul_f16 v9,v8,v9
v_mfma_f32_16x16x32_bf16 v[48:51],a[200:203],a[28:31],v[48:51]
s_waitcnt lgkmcnt(8)
ds_read_b64_tr_b16 a[166:167],v12 offset:13344
v_lshl_add_u64 v[62:63],v[44:45],0,v[46:47]
v_lshlrev_b32_sdwa v36,1,v36 dst_sel:DWORD dst_unused:UNUSED_PAD src0_sel:DWORD src1_sel:WORD_1
v_mfma_f32_16x16x32_bf16 v[48:51],a[196:199],a[44:47],v[48:51]
v_exp_f16 v8,v9
v_mfma_f32_16x16x32_bf16 v[44:47],a[192:195],a[44:47],v[16:19]
ds_read_u16 v36,v36
v_lshlrev_b32_sdwa v38,1,v38 dst_sel:DWORD dst_unused:UNUSED_PAD src0_sel:DWORD src1_sel:WORD_1
v_mfma_f32_16x16x32_bf16 v[44:47],a[220:223],a[28:31],v[44:47]
ds_read_u16_d16_hi v38,v38
v_lshlrev_b32_sdwa v37,1,v37 dst_sel:DWORD dst_unused:UNUSED_PAD src0_sel:DWORD src1_sel:WORD_1
v_mfma_f32_16x16x32_bf16 v[44:47],a[216:219],a[12:15],v[44:47]
v_exp_f16_sdwa v8,v9 dst_sel:WORD_1 dst_unused:UNUSED_PRESERVE src0_sel:WORD_1
v_mfma_f32_16x16x32_bf16 v[208:211],a[244:247],v[32:35],v[208:211]
ds_read_u16 v37,v37
v_lshlrev_b32_sdwa v39,1,v39 dst_sel:DWORD dst_unused:UNUSED_PAD src0_sel:DWORD src1_sel:WORD_1
v_mfma_f32_16x16x32_bf16 v[212:215],a[244:247],v[60:63],v[212:215]
ds_read_u16_d16_hi v39,v39
v_lshlrev_b32_sdwa v52,1,v52 dst_sel:DWORD dst_unused:UNUSED_PAD src0_sel:DWORD src1_sel:WORD_1
v_mfma_f32_16x16x32_bf16 v[172:175],a[252:255],v[60:63],v[172:175]
ds_read_u16 v52,v52
v_pk_fma_f16 v8,v8,s55,s55
v_mfma_f32_16x16x32_bf16 v[168:171],a[252:255],v[32:35],v[168:171]
ds_read_b64_tr_b16 a[244:245],v3 offset:12384
v_rcp_f16_e64 v9,v8
v_lshlrev_b32_sdwa v54,1,v54 dst_sel:DWORD dst_unused:UNUSED_PAD src0_sel:DWORD src1_sel:WORD_1
v_mfma_f32_16x16x32_bf16 v[216:219],a[232:235],v[32:35],v[216:219]
ds_read_u16_d16_hi v54,v54
ds_read_b64_tr_b16 a[246:247],v3 offset:13920
v_mfma_f32_16x16x32_bf16 v[220:223],a[232:235],v[60:63],v[220:223]
ds_read_b64_tr_b16 a[252:253],v3 offset:12392
ds_read_b64_tr_b16 a[254:255],v3 offset:13928
v_lshlrev_b32_sdwa v53,1,v53 dst_sel:DWORD dst_unused:UNUSED_PAD src0_sel:DWORD src1_sel:WORD_1
v_mfma_f32_16x16x32_bf16 v[188:191],a[240:243],v[60:63],v[188:191]
ds_read_u16 v53,v53
ds_read_b64_tr_b16 a[232:233],v3 offset:12448
v_lshlrev_b32_sdwa v55,1,v55 dst_sel:DWORD dst_unused:UNUSED_PAD src0_sel:DWORD src1_sel:WORD_1
v_mfma_f32_16x16x32_bf16 v[184:187],a[240:243],v[32:35],v[184:187]
ds_read_u16_d16_hi v55,v55
v_rcp_f16_sdwa v9,v8 dst_sel:WORD_1 dst_unused:UNUSED_PRESERVE src0_sel:WORD_1
v_mfma_f32_16x16x32_bf16 v[224:227],a[224:227],v[32:35],v[224:227]
ds_read_b64_tr_b16 a[234:235],v3 offset:13984
ds_read_b64_tr_b16 a[240:241],v3 offset:12456
v_lshlrev_b32_sdwa v56,1,v56 dst_sel:DWORD dst_unused:UNUSED_PAD src0_sel:DWORD src1_sel:WORD_1
v_mfma_f32_16x16x32_bf16 v[228:231],a[224:227],v[60:63],v[228:231]
ds_read_u16 v56,v56
v_lshlrev_b32_sdwa v58,1,v58 dst_sel:DWORD dst_unused:UNUSED_PAD src0_sel:DWORD src1_sel:WORD_1
v_mfma_f32_16x16x32_bf16 v[204:207],a[248:251],v[60:63],v[204:207]
ds_read_u16_d16_hi v58,v58
v_fma_mix_f32 v57,v57,v9,0 op_sel_hi:[0,1,0]
v_mfma_f32_16x16x32_bf16 v[200:203],a[248:251],v[32:35],v[200:203]
ds_read_b64_tr_b16 a[242:243],v3 offset:13992
ds_read_b64_tr_b16 a[168:169],v12 offset:18480
v_fma_mix_f32 v59,v59,v9,0 op_sel:[0,1,0] op_sel_hi:[0,1,0]
v_mfma_f32_16x16x32_bf16 v[60:63],a[216:219],a[48:51],v[16:19]
ds_read_b64_tr_b16 a[170:171],v12 offset:19504
ds_read_b64_tr_b16 a[172:173],v12 offset:20528
v_lshlrev_b32_sdwa v28,1,v28 dst_sel:DWORD dst_unused:UNUSED_PAD src0_sel:DWORD src1_sel:WORD_1
v_mfma_f32_16x16x32_bf16 v[60:63],a[220:223],a[64:67],v[60:63]
ds_read_u16 v28,v28
ds_read_b64_tr_b16 a[174:175],v12 offset:21552
v_mfma_f32_16x16x32_bf16 v[60:63],a[192:195],a[80:83],v[60:63]
s_waitcnt lgkmcnt(13)
ds_read_b64_tr_b16 a[208:209],v1 offset:12320
v_lshl_add_u64 v[40:41],v[40:41],0,v[42:43]
v_lshlrev_b32_sdwa v30,1,v30 dst_sel:DWORD dst_unused:UNUSED_PAD src0_sel:DWORD src1_sel:WORD_1
v_mfma_f32_16x16x32_bf16 v[32:35],a[196:199],a[80:83],v[20:23]
ds_read_u16_d16_hi v30,v30
v_lshl_add_u64 v[42:43],v[36:37],0,v[38:39]
v_lshlrev_b32_sdwa v29,1,v29 dst_sel:DWORD dst_unused:UNUSED_PAD src0_sel:DWORD src1_sel:WORD_1
v_mfma_f32_16x16x32_bf16 v[32:35],a[200:203],a[64:67],v[32:35]
s_waitcnt lgkmcnt(12)
ds_read_u16 v29,v29
v_lshl_add_u64 v[36:37],v[52:53],0,v[54:55]
v_mfma_f32_16x16x32_bf16 v[32:35],a[212:215],a[48:51],v[32:35]
ds_read_b64_tr_b16 a[210:211],v1 offset:13344
v_lshlrev_b32_sdwa v31,1,v31 dst_sel:DWORD dst_unused:UNUSED_PAD src0_sel:DWORD src1_sel:WORD_1
v_mfma_f32_16x16x32_bf16 v[52:55],a[212:215],a[52:55],v[20:23]
s_waitcnt lgkmcnt(10)
ds_read_u16_d16_hi v31,v31
v_or_b32 v38,v56,v58
v_lshlrev_b32_sdwa v24,1,v24 dst_sel:DWORD dst_unused:UNUSED_PAD src0_sel:DWORD src1_sel:WORD_1
v_mfma_f32_16x16x32_bf16 v[52:55],a[200:203],a[68:71],v[52:55]
ds_read_u16 v24,v24
ds_read_b64_tr_b16 a[204:205],v1 offset:14368
v_cvt_pk_bf16_f32 v39,v57,v59
v_lshlrev_b32_sdwa v26,1,v26 dst_sel:DWORD dst_unused:UNUSED_PAD src0_sel:DWORD src1_sel:WORD_1
v_mfma_f32_16x16x32_bf16 v[52:55],a[196:199],a[84:87],v[52:55]
ds_read_u16_d16_hi v26,v26
v_lshlrev_b32_sdwa v25,1,v25 dst_sel:DWORD dst_unused:UNUSED_PAD src0_sel:DWORD src1_sel:WORD_1
v_mfma_f32_16x16x32_bf16 v[56:59],a[192:195],a[84:87],v[16:19]
ds_read_u16 v25,v25
v_lshlrev_b32_sdwa v27,1,v27 dst_sel:DWORD dst_unused:UNUSED_PAD src0_sel:DWORD src1_sel:WORD_1
v_mfma_f32_16x16x32_bf16 v[56:59],a[220:223],a[68:71],v[56:59]
ds_read_u16_d16_hi v27,v27
v_lshlrev_b32_sdwa v44,1,v44 dst_sel:DWORD dst_unused:UNUSED_PAD src0_sel:DWORD src1_sel:WORD_1
v_mfma_f32_16x16x32_bf16 v[56:59],a[216:219],a[52:55],v[56:59]
ds_read_u16 v44,v44
v_lshlrev_b32_sdwa v46,1,v46 dst_sel:DWORD dst_unused:UNUSED_PAD src0_sel:DWORD src1_sel:WORD_1
v_mfma_f32_16x16x32_bf16 v[136:139],a[228:231],v[40:43],v[136:139]
ds_read_u16_d16_hi v46,v46
v_lshlrev_b32_sdwa v45,1,v45 dst_sel:DWORD dst_unused:UNUSED_PAD src0_sel:DWORD src1_sel:WORD_1
v_mfma_f32_16x16x32_bf16 v[140:143],a[228:231],v[36:39],v[140:143]
ds_read_u16 v45,v45
v_lshlrev_b32_sdwa v47,1,v47 dst_sel:DWORD dst_unused:UNUSED_PAD src0_sel:DWORD src1_sel:WORD_1
v_mfma_f32_16x16x32_bf16 v[68:71],a[236:239],v[36:39],v[68:71]
ds_read_u16_d16_hi v47,v47
v_lshlrev_b32_sdwa v48,1,v48 dst_sel:DWORD dst_unused:UNUSED_PAD src0_sel:DWORD src1_sel:WORD_1
v_mfma_f32_16x16x32_bf16 v[64:67],a[236:239],v[40:43],v[64:67]
ds_read_u16 v48,v48
ds_read_b64_tr_b16 a[206:207],v1 offset:15392
v_mfma_f32_16x16x32_bf16 v[144:147],a[244:247],v[40:43],v[144:147]
ds_read_b64_tr_b16 a[176:177],v12 offset:22576
v_lshlrev_b32_sdwa v50,1,v50 dst_sel:DWORD dst_unused:UNUSED_PAD src0_sel:DWORD src1_sel:WORD_1
v_mfma_f32_16x16x32_bf16 v[148:151],a[244:247],v[36:39],v[148:151]
ds_read_u16_d16_hi v50,v50
v_lshlrev_b32_sdwa v49,1,v49 dst_sel:DWORD dst_unused:UNUSED_PAD src0_sel:DWORD src1_sel:WORD_1
v_mfma_f32_16x16x32_bf16 v[84:87],a[252:255],v[36:39],v[84:87]
ds_read_u16 v49,v49
v_lshlrev_b32_sdwa v51,1,v51 dst_sel:DWORD dst_unused:UNUSED_PAD src0_sel:DWORD src1_sel:WORD_1
v_mfma_f32_16x16x32_bf16 v[80:83],a[252:255],v[40:43],v[80:83]
ds_read_u16_d16_hi v51,v51
v_mfma_f32_16x16x32_bf16 v[152:155],a[232:235],v[40:43],v[152:155]
ds_read_b64_tr_b16 a[178:179],v12 offset:23600
v_mfma_f32_16x16x32_bf16 v[156:159],a[232:235],v[36:39],v[156:159]
s_waitcnt lgkmcnt(9)
ds_read_b64_tr_b16 a[180:181],v11 offset:22576
v_mfma_f32_16x16x32_bf16 v[100:103],a[240:243],v[36:39],v[100:103]
ds_read_b64_tr_b16 a[182:183],v11 offset:23600
v_mfma_f32_16x16x32_bf16 v[96:99],a[240:243],v[40:43],v[96:99]
ds_read_b64_tr_b16 a[184:185],v11 offset:20528
v_mfma_f32_16x16x32_bf16 v[36:39],a[216:219],a[56:59],v[16:19]
ds_read_b64_tr_b16 a[186:187],v11 offset:21552
v_lshlrev_b32_sdwa v60,1,v60 dst_sel:DWORD dst_unused:UNUSED_PAD src0_sel:DWORD src1_sel:WORD_1
v_mfma_f32_16x16x32_bf16 v[36:39],a[220:223],a[72:75],v[36:39]
ds_read_u16 v60,v60
v_fma_mixlo_f16 v8,v53,s99,0
v_mfma_f32_16x16x32_bf16 v[36:39],a[192:195],a[88:91],v[36:39]
ds_read_b64_tr_b16 a[188:189],v11 offset:18480
v_lshl_add_u64 v[28:29],v[28:29],0,v[30:31]
v_lshlrev_b32_sdwa v62,1,v62 dst_sel:DWORD dst_unused:UNUSED_PAD src0_sel:DWORD src1_sel:WORD_1
v_mfma_f32_16x16x32_bf16 v[40:43],a[196:199],a[88:91],v[20:23]
ds_read_u16_d16_hi v62,v62
v_lshl_add_u64 v[30:31],v[24:25],0,v[26:27]
v_fma_mixhi_f16 v8,v55,s99,0
v_mfma_f32_16x16x32_bf16 v[40:43],a[200:203],a[72:75],v[40:43]
s_waitcnt lgkmcnt(8)
ds_read_b64_tr_b16 a[190:191],v11 offset:19504
v_lshl_add_u64 v[24:25],v[44:45],0,v[46:47]
v_lshlrev_b32_sdwa v61,1,v61 dst_sel:DWORD dst_unused:UNUSED_PAD src0_sel:DWORD src1_sel:WORD_1
v_pk_fma_f16 v9,v8,v8,s54
v_mfma_f32_16x16x32_bf16 v[40:43],a[212:215],a[56:59],v[40:43]
ds_read_u16 v61,v61
v_lshlrev_b32_sdwa v63,1,v63 dst_sel:DWORD dst_unused:UNUSED_PAD src0_sel:DWORD src1_sel:WORD_1
v_mfma_f32_16x16x32_bf16 v[44:47],a[212:215],a[60:63],v[20:23]
ds_read_b128 v[20:23],v4 offset:1408
v_pk_mul_f16 v9,v8,v9
v_mfma_f32_16x16x32_bf16 v[44:47],a[200:203],a[76:79],v[44:47]
ds_read_u16_d16_hi v63,v63
ds_read_b64_tr_b16 a[212:213],v1 offset:16416
v_lshl_add_u64 v[26:27],v[48:49],0,v[50:51]
v_lshlrev_b32_sdwa v32,1,v32 dst_sel:DWORD dst_unused:UNUSED_PAD src0_sel:DWORD src1_sel:WORD_1
v_mfma_f32_16x16x32_bf16 v[44:47],a[196:199],a[92:95],v[44:47]
ds_read_b64_tr_b16 a[214:215],v1 offset:17440
v_exp_f16_e64 v8,v9
v_mfma_f32_16x16x32_bf16 v[48:51],a[192:195],a[92:95],v[16:19]
ds_read_b128 v[16:19],v4 offset:1440
ds_read_b64_tr_b16 a[200:201],v2 offset:16416
v_lshlrev_b32_sdwa v34,1,v34 dst_sel:DWORD dst_unused:UNUSED_PAD src0_sel:DWORD src1_sel:WORD_1
v_mfma_f32_16x16x32_bf16 v[48:51],a[220:223],a[76:79],v[48:51]
TP9676_%=:
s_cbranch_execz TR1_%=
ds_read_b64_tr_b16 a[202:203],v2 offset:17440
ds_read_u16 v32,v32
v_lshlrev_b32_sdwa v33,1,v33 dst_sel:DWORD dst_unused:UNUSED_PAD src0_sel:DWORD src1_sel:WORD_1
v_mfma_f32_16x16x32_bf16 v[48:51],a[216:219],a[60:63],v[48:51]
ds_read_u16_d16_hi v34,v34
v_exp_f16_sdwa v8,v9 dst_sel:WORD_1 dst_unused:UNUSED_PRESERVE src0_sel:WORD_1
v_mfma_f32_16x16x32_bf16 v[112:115],a[228:231],v[28:31],v[112:115]
ds_read_u16 v33,v33
ds_read_b64_tr_b16 a[196:197],v2 offset:14368
v_lshlrev_b32_sdwa v35,1,v35 dst_sel:DWORD dst_unused:UNUSED_PAD src0_sel:DWORD src1_sel:WORD_1
v_mfma_f32_16x16x32_bf16 v[116:119],a[228:231],v[24:27],v[116:119]
ds_read_u16_d16_hi v35,v35
v_lshlrev_b32_sdwa v56,1,v56 dst_sel:DWORD dst_unused:UNUSED_PAD src0_sel:DWORD src1_sel:WORD_1
v_mfma_f32_16x16x32_bf16 v[76:79],a[236:239],v[24:27],v[76:79]
ds_read_b64_tr_b16 a[198:199],v2 offset:15392
v_pk_fma_f16 v8,v8,s55,s55
v_mfma_f32_16x16x32_bf16 v[72:75],a[236:239],v[28:31],v[72:75]
v_rcp_f16 v9,v8
v_lshlrev_b32_sdwa v58,1,v58 dst_sel:DWORD dst_unused:UNUSED_PAD src0_sel:DWORD src1_sel:WORD_1
v_mfma_f32_16x16x32_bf16 v[120:123],a[244:247],v[28:31],v[120:123]
ds_read_u16 v56,v56
v_lshlrev_b32_sdwa v57,1,v57 dst_sel:DWORD dst_unused:UNUSED_PAD src0_sel:DWORD src1_sel:WORD_1
v_mfma_f32_16x16x32_bf16 v[124:127],a[244:247],v[24:27],v[124:127]
ds_read_u16_d16_hi v58,v58
v_lshlrev_b32_sdwa v59,1,v59 dst_sel:DWORD dst_unused:UNUSED_PAD src0_sel:DWORD src1_sel:WORD_1
v_mfma_f32_16x16x32_bf16 v[92:95],a[252:255],v[24:27],v[92:95]
ds_read_u16 v57,v57
v_lshlrev_b32_sdwa v52,1,v52 dst_sel:DWORD dst_unused:UNUSED_PAD src0_sel:DWORD src1_sel:WORD_1
v_mfma_f32_16x16x32_bf16 v[88:91],a[252:255],v[28:31],v[88:91]
ds_read_u16_d16_hi v59,v59
v_rcp_f16_sdwa v9,v8 dst_sel:WORD_1 dst_unused:UNUSED_PRESERVE src0_sel:WORD_1
v_mfma_f32_16x16x32_bf16 v[128:131],a[232:235],v[28:31],v[128:131]
ds_read_b64_tr_b16 a[192:193],v2 offset:12320
v_lshlrev_b32_sdwa v54,1,v54 dst_sel:DWORD dst_unused:UNUSED_PAD src0_sel:DWORD src1_sel:WORD_1
v_mfma_f32_16x16x32_bf16 v[132:135],a[232:235],v[24:27],v[132:135]
ds_read_b64_tr_b16 a[194:195],v2 offset:13344
v_fma_mix_f32 v53,v53,v9,0 op_sel_hi:[0,1,0]
v_mfma_f32_16x16x32_bf16 v[108:111],a[240:243],v[24:27],v[108:111]
ds_read_u16 v52,v52
v_fma_mix_f32 v55,v55,v9,0 op_sel:[0,1,0] op_sel_hi:[0,1,0]
v_mfma_f32_16x16x32_bf16 v[104:107],a[240:243],v[28:31],v[104:107]
s_waitcnt lgkmcnt(7)
ds_read_u16_d16_hi v54,v54
v_mfma_f32_16x16x32_bf16 v[24:27],a[208:211],a[0:3],v[20:23]
v_lshlrev_b32_sdwa v36,1,v36 dst_sel:DWORD dst_unused:UNUSED_PAD src0_sel:DWORD src1_sel:WORD_1
v_lshlrev_b32_sdwa v38,1,v38 dst_sel:DWORD dst_unused:UNUSED_PAD src0_sel:DWORD src1_sel:WORD_1
v_mfma_f32_16x16x32_bf16 v[24:27],a[204:207],a[16:19],v[24:27]
ds_read_u16 v36,v36
v_lshlrev_b32_sdwa v37,1,v37 dst_sel:DWORD dst_unused:UNUSED_PAD src0_sel:DWORD src1_sel:WORD_1
v_mfma_f32_16x16x32_bf16 v[24:27],a[212:215],a[32:35],v[24:27]
ds_read_u16_d16_hi v38,v38
v_lshl_add_u64 v[60:61],v[60:61],0,v[62:63]
v_mfma_f32_16x16x32_bf16 v[28:31],a[200:203],a[32:35],v[16:19]
ds_read_u16 v37,v37
v_lshl_add_u64 v[62:63],v[32:33],0,v[34:35]
v_mfma_f32_16x16x32_bf16 v[28:31],a[196:199],a[16:19],v[28:31]
s_waitcnt lgkmcnt(5)
v_lshl_add_u64 v[32:33],v[56:57],0,v[58:59]
v_lshlrev_b32_sdwa v39,1,v39 dst_sel:DWORD dst_unused:UNUSED_PAD src0_sel:DWORD src1_sel:WORD_1
v_mfma_f32_16x16x32_bf16 v[28:31],a[192:195],a[0:3],v[28:31]
ds_read_u16_d16_hi v39,v39
v_lshlrev_b32_sdwa v40,1,v40 dst_sel:DWORD dst_unused:UNUSED_PAD src0_sel:DWORD src1_sel:WORD_1
v_mfma_f32_16x16x32_bf16 v[56:59],a[192:195],a[4:7],v[16:19]
s_waitcnt lgkmcnt(4)
ds_read_u16 v40,v40
v_or_b32_e64 v34,v52,v54
v_mfma_f32_16x16x32_bf16 v[56:59],a[196:199],a[20:23],v[56:59]
s_waitcnt vmcnt(0)
v_cvt_pk_bf16_f32 v35,v53,v55
v_lshlrev_b32_sdwa v42,1,v42 dst_sel:DWORD dst_unused:UNUSED_PAD src0_sel:DWORD src1_sel:WORD_1
v_mfma_f32_16x16x32_bf16 v[56:59],a[200:203],a[36:39],v[56:59]
s_barrier
ds_read_u16_d16_hi v42,v42
v_lshlrev_b32_sdwa v41,1,v41 dst_sel:DWORD dst_unused:UNUSED_PAD src0_sel:DWORD src1_sel:WORD_1
v_mfma_f32_16x16x32_bf16 v[52:55],a[212:215],a[36:39],v[20:23]
ds_read_u16 v41,v41
v_lshlrev_b32_sdwa v43,1,v43 dst_sel:DWORD dst_unused:UNUSED_PAD src0_sel:DWORD src1_sel:WORD_1
v_mfma_f32_16x16x32_bf16 v[52:55],a[204:207],a[20:23],v[52:55]
ds_read_u16_d16_hi v43,v43
v_lshlrev_b32_sdwa v48,1,v48 dst_sel:DWORD dst_unused:UNUSED_PAD src0_sel:DWORD src1_sel:WORD_1
v_mfma_f32_16x16x32_bf16 v[52:55],a[208:211],a[4:7],v[52:55]
ds_read_u16 v48,v48
v_lshlrev_b32_sdwa v50,1,v50 dst_sel:DWORD dst_unused:UNUSED_PAD src0_sel:DWORD src1_sel:WORD_1
v_mfma_f32_16x16x32_bf16 v[232:235],a[228:231],v[60:63],v[232:235]
ds_read_u16_d16_hi v50,v50
v_lshlrev_b32_sdwa v49,1,v49 dst_sel:DWORD dst_unused:UNUSED_PAD src0_sel:DWORD src1_sel:WORD_1
v_mfma_f32_16x16x32_bf16 v[236:239],a[228:231],v[32:35],v[236:239]
ds_read_u16 v49,v49
v_lshlrev_b32_sdwa v51,1,v51 dst_sel:DWORD dst_unused:UNUSED_PAD src0_sel:DWORD src1_sel:WORD_1
v_mfma_f32_16x16x32_bf16 v[164:167],a[236:239],v[32:35],v[164:167]
ds_read_u16_d16_hi v51,v51
ds_read_b64_tr_b16 a[224:225],v3 offset:18480
v_mfma_f32_16x16x32_bf16 v[160:163],a[236:239],v[60:63],v[160:163]
ds_read_b64_tr_b16 a[226:227],v3 offset:20016
v_lshlrev_b32_sdwa v44,1,v44 dst_sel:DWORD dst_unused:UNUSED_PAD src0_sel:DWORD src1_sel:WORD_1
v_mfma_f32_16x16x32_bf16 v[240:243],a[244:247],v[60:63],v[240:243]
ds_read_u16 v44,v44
v_lshlrev_b32_sdwa v46,1,v46 dst_sel:DWORD dst_unused:UNUSED_PAD src0_sel:DWORD src1_sel:WORD_1
v_mfma_f32_16x16x32_bf16 v[244:247],a[244:247],v[32:35],v[244:247]
ds_read_u16_d16_hi v46,v46
v_lshlrev_b32_sdwa v45,1,v45 dst_sel:DWORD dst_unused:UNUSED_PAD src0_sel:DWORD src1_sel:WORD_1
v_mfma_f32_16x16x32_bf16 v[180:183],a[252:255],v[32:35],v[180:183]
ds_read_u16 v45,v45
v_lshlrev_b32_sdwa v47,1,v47 dst_sel:DWORD dst_unused:UNUSED_PAD src0_sel:DWORD src1_sel:WORD_1
v_mfma_f32_16x16x32_bf16 v[176:179],a[252:255],v[60:63],v[176:179]
ds_read_u16_d16_hi v47,v47
v_mfma_f32_16x16x32_bf16 v[248:251],a[232:235],v[60:63],v[248:251]
ds_read_b64_tr_b16 a[248:249],v3 offset:18488
v_mfma_f32_16x16x32_bf16 v[252:255],a[232:235],v[32:35],v[252:255]
ds_read_b64_tr_b16 a[250:251],v3 offset:20024
v_mfma_f32_16x16x32_bf16 v[196:199],a[240:243],v[32:35],v[196:199]
v_mfma_f32_16x16x32_bf16 v[192:195],a[240:243],v[60:63],v[192:195]
v_mfma_f32_16x16x32_bf16 v[32:35],a[208:211],a[8:11],v[20:23]
ds_read_b64_tr_b16 a[220:221],v1 offset:18480
v_lshlrev_b32_sdwa v24,1,v24 dst_sel:DWORD dst_unused:UNUSED_PAD src0_sel:DWORD src1_sel:WORD_1
v_mfma_f32_16x16x32_bf16 v[32:35],a[204:207],a[24:27],v[32:35]
ds_read_u16 v24,v24
v_fma_mixlo_f16 v8,v57,s99,0
v_mfma_f32_16x16x32_bf16 v[32:35],a[212:215],a[40:43],v[32:35]
s_waitcnt lgkmcnt(6)
ds_read_b64_tr_b16 a[222:223],v1 offset:19504
v_lshl_add_u64 v[36:37],v[36:37],0,v[38:39]
v_lshlrev_b32_sdwa v26,1,v26 dst_sel:DWORD dst_unused:UNUSED_PAD src0_sel:DWORD src1_sel:WORD_1
v_mfma_f32_16x16x32_bf16 v[60:63],a[200:203],a[40:43],v[16:19]
ds_read_u16_d16_hi v26,v26
v_lshl_add_u64 v[38:39],v[40:41],0,v[42:43]
v_fma_mixhi_f16 v8,v59,s99,0
v_mfma_f32_16x16x32_bf16 v[60:63],a[196:199],a[24:27],v[60:63]
ds_read_b64_tr_b16 a[216:217],v1 offset:20528
v_lshl_add_u64 v[40:41],v[48:49],0,v[50:51]
v_lshlrev_b32_sdwa v25,1,v25 dst_sel:DWORD dst_unused:UNUSED_PAD src0_sel:DWORD src1_sel:WORD_1
v_pk_fma_f16 v9,v8,v8,s54
v_mfma_f32_16x16x32_bf16 v[60:63],a[192:195],a[8:11],v[60:63]
ds_read_u16 v25,v25
v_lshlrev_b32_sdwa v27,1,v27 dst_sel:DWORD dst_unused:UNUSED_PAD src0_sel:DWORD src1_sel:WORD_1
v_mfma_f32_16x16x32_bf16 v[48:51],a[192:195],a[12:15],v[16:19]
ds_read_u16_d16_hi v27,v27
v_pk_mul_f16 v9,v8,v9
v_mfma_f32_16x16x32_bf16 v[48:51],a[196:199],a[28:31],v[48:51]
s_waitcnt lgkmcnt(7)
ds_read_b64_tr_b16 a[218:219],v1 offset:21552
v_lshl_add_u64 v[42:43],v[44:45],0,v[46:47]
v_lshlrev_b32_sdwa v28,1,v28 dst_sel:DWORD dst_unused:UNUSED_PAD src0_sel:DWORD src1_sel:WORD_1
v_mfma_f32_16x16x32_bf16 v[48:51],a[200:203],a[44:47],v[48:51]
v_exp_f16 v8,v9
v_mfma_f32_16x16x32_bf16 v[44:47],a[212:215],a[44:47],v[20:23]
ds_read_u16 v28,v28
v_lshlrev_b32_sdwa v30,1,v30 dst_sel:DWORD dst_unused:UNUSED_PAD src0_sel:DWORD src1_sel:WORD_1
v_mfma_f32_16x16x32_bf16 v[44:47],a[204:207],a[28:31],v[44:47]
ds_read_u16_d16_hi v30,v30
v_lshlrev_b32_sdwa v29,1,v29 dst_sel:DWORD dst_unused:UNUSED_PAD src0_sel:DWORD src1_sel:WORD_1
v_mfma_f32_16x16x32_bf16 v[44:47],a[208:211],a[12:15],v[44:47]
ds_read_b64_tr_b16 a[0:1],v3 offset:38992
ds_read_b64_tr_b16 a[2:3],v3 offset:40528
v_exp_f16_sdwa v8,v9 dst_sel:WORD_1 dst_unused:UNUSED_PRESERVE src0_sel:WORD_1
v_mfma_f32_16x16x32_bf16 v[208:211],a[228:231],v[36:39],v[208:211]
ds_read_b64_tr_b16 a[4:5],v3 offset:45152
ds_read_b64_tr_b16 a[6:7],v3 offset:46688
v_lshlrev_b32_sdwa v31,1,v31 dst_sel:DWORD dst_unused:UNUSED_PAD src0_sel:DWORD src1_sel:WORD_1
v_mfma_f32_16x16x32_bf16 v[212:215],a[228:231],v[40:43],v[212:215]
ds_read_u16 v29,v29
ds_read_u16_d16_hi v31,v31
v_lshlrev_b32_sdwa v52,1,v52 dst_sel:DWORD dst_unused:UNUSED_PAD src0_sel:DWORD src1_sel:WORD_1
v_mfma_f32_16x16x32_bf16 v[172:175],a[236:239],v[40:43],v[172:175]
ds_read_u16 v52,v52
ds_read_b64_tr_b16 a[8:9],v3 offset:45160
v_pk_fma_f16 v8,v8,s55,s55
v_mfma_f32_16x16x32_bf16 v[168:171],a[236:239],v[36:39],v[168:171]
ds_read_b64_tr_b16 a[10:11],v3 offset:46696
v_rcp_f16 v9,v8
v_lshlrev_b32_sdwa v54,1,v54 dst_sel:DWORD dst_unused:UNUSED_PAD src0_sel:DWORD src1_sel:WORD_1
v_mfma_f32_16x16x32_bf16 v[216:219],a[244:247],v[36:39],v[216:219]
ds_read_u16_d16_hi v54,v54
ds_read_b64_tr_b16 a[12:13],v3 offset:39000
v_mfma_f32_16x16x32_bf16 v[220:223],a[244:247],v[40:43],v[220:223]
ds_read_b64_tr_b16 a[14:15],v3 offset:40536
ds_read_b64_tr_b16 a[228:229],v3 offset:18544
v_lshlrev_b32_sdwa v53,1,v53 dst_sel:DWORD dst_unused:UNUSED_PAD src0_sel:DWORD src1_sel:WORD_1
v_mfma_f32_16x16x32_bf16 v[188:191],a[252:255],v[40:43],v[188:191]
ds_read_u16 v53,v53
ds_read_b64_tr_b16 a[230:231],v3 offset:20080
v_lshlrev_b32_sdwa v55,1,v55 dst_sel:DWORD dst_unused:UNUSED_PAD src0_sel:DWORD src1_sel:WORD_1
v_mfma_f32_16x16x32_bf16 v[184:187],a[252:255],v[36:39],v[184:187]
ds_read_u16_d16_hi v55,v55
v_rcp_f16_sdwa v9,v8 dst_sel:WORD_1 dst_unused:UNUSED_PRESERVE src0_sel:WORD_1
v_mfma_f32_16x16x32_bf16 v[224:227],a[232:235],v[36:39],v[224:227]
ds_read_b64_tr_b16 a[236:237],v3 offset:18552
ds_read_b64_tr_b16 a[238:239],v3 offset:20088
v_lshlrev_b32_sdwa v56,1,v56 dst_sel:DWORD dst_unused:UNUSED_PAD src0_sel:DWORD src1_sel:WORD_1
v_mfma_f32_16x16x32_bf16 v[228:231],a[232:235],v[40:43],v[228:231]
ds_read_u16 v56,v56
ds_read_b64_tr_b16 a[244:245],v3 offset:18608
v_lshlrev_b32_sdwa v58,1,v58 dst_sel:DWORD dst_unused:UNUSED_PAD src0_sel:DWORD src1_sel:WORD_1
v_mfma_f32_16x16x32_bf16 v[204:207],a[240:243],v[40:43],v[204:207]
ds_read_u16_d16_hi v58,v58
ds_read_b64_tr_b16 a[246:247],v3 offset:20144
v_fma_mix_f32 v57,v57,v9,0 op_sel_hi:[0,1,0]
v_mfma_f32_16x16x32_bf16 v[200:203],a[240:243],v[36:39],v[200:203]
ds_read_b64_tr_b16 a[252:253],v3 offset:18616
ds_read_b64_tr_b16 a[254:255],v3 offset:20152
v_fma_mix_f32 v59,v59,v9,0 op_sel:[0,1,0] op_sel_hi:[0,1,0]
v_mfma_f32_16x16x32_bf16 v[40:43],a[208:211],a[48:51],v[20:23]
ds_read_b64_tr_b16 a[16:17],v3 offset:39056
ds_read_b64_tr_b16 a[18:19],v3 offset:40592
v_lshlrev_b32_sdwa v32,1,v32 dst_sel:DWORD dst_unused:UNUSED_PAD src0_sel:DWORD src1_sel:WORD_1
v_mfma_f32_16x16x32_bf16 v[40:43],a[204:207],a[64:67],v[40:43]
ds_read_u16 v32,v32
ds_read_b64_tr_b16 a[20:21],v3 offset:45216
v_mfma_f32_16x16x32_bf16 v[40:43],a[212:215],a[80:83],v[40:43]
s_waitcnt lgkmcnt(15)
ds_read_b64_tr_b16 a[22:23],v3 offset:46752
ds_read_b64_tr_b16 a[24:25],v3 offset:45224
v_lshl_add_u64 v[24:25],v[24:25],0,v[26:27]
v_lshlrev_b32_sdwa v34,1,v34 dst_sel:DWORD dst_unused:UNUSED_PAD src0_sel:DWORD src1_sel:WORD_1
v_mfma_f32_16x16x32_bf16 v[36:39],a[200:203],a[80:83],v[16:19]
ds_read_u16_d16_hi v34,v34
v_lshl_add_u64 v[26:27],v[28:29],0,v[30:31]
v_lshlrev_b32_sdwa v33,1,v33 dst_sel:DWORD dst_unused:UNUSED_PAD src0_sel:DWORD src1_sel:WORD_1
v_mfma_f32_16x16x32_bf16 v[36:39],a[196:199],a[64:67],v[36:39]
s_waitcnt lgkmcnt(15)
ds_read_u16 v33,v33
v_lshl_add_u64 v[28:29],v[52:53],0,v[54:55]
v_mfma_f32_16x16x32_bf16 v[36:39],a[192:195],a[48:51],v[36:39]
ds_read_b64_tr_b16 a[26:27],v3 offset:46760
v_lshlrev_b32_sdwa v35,1,v35 dst_sel:DWORD dst_unused:UNUSED_PAD src0_sel:DWORD src1_sel:WORD_1
v_mfma_f32_16x16x32_bf16 v[52:55],a[192:195],a[52:55],v[16:19]
s_waitcnt lgkmcnt(12)
ds_read_u16_d16_hi v35,v35
v_or_b32_e64 v30,v56,v58
v_lshlrev_b32_sdwa v60,1,v60 dst_sel:DWORD dst_unused:UNUSED_PAD src0_sel:DWORD src1_sel:WORD_1
v_mfma_f32_16x16x32_bf16 v[52:55],a[196:199],a[68:71],v[52:55]
ds_read_u16 v60,v60
ds_read_b64_tr_b16 a[28:29],v3 offset:39064
v_cvt_pk_bf16_f32 v31,v57,v59
v_lshlrev_b32_sdwa v62,1,v62 dst_sel:DWORD dst_unused:UNUSED_PAD src0_sel:DWORD src1_sel:WORD_1
v_mfma_f32_16x16x32_bf16 v[52:55],a[200:203],a[84:87],v[52:55]
ds_read_u16_d16_hi v62,v62
v_lshlrev_b32_sdwa v61,1,v61 dst_sel:DWORD dst_unused:UNUSED_PAD src0_sel:DWORD src1_sel:WORD_1
v_mfma_f32_16x16x32_bf16 v[56:59],a[212:215],a[84:87],v[20:23]
ds_read_u16 v61,v61
v_lshlrev_b32_sdwa v63,1,v63 dst_sel:DWORD dst_unused:UNUSED_PAD src0_sel:DWORD src1_sel:WORD_1
v_mfma_f32_16x16x32_bf16 v[56:59],a[204:207],a[68:71],v[56:59]
ds_read_u16_d16_hi v63,v63
v_lshlrev_b32_sdwa v44,1,v44 dst_sel:DWORD dst_unused:UNUSED_PAD src0_sel:DWORD src1_sel:WORD_1
v_mfma_f32_16x16x32_bf16 v[56:59],a[208:211],a[52:55],v[56:59]
ds_read_u16 v44,v44
v_lshlrev_b32_sdwa v46,1,v46 dst_sel:DWORD dst_unused:UNUSED_PAD src0_sel:DWORD src1_sel:WORD_1
v_mfma_f32_16x16x32_bf16 v[136:139],a[224:227],v[24:27],v[136:139]
ds_read_u16_d16_hi v46,v46
v_lshlrev_b32_sdwa v45,1,v45 dst_sel:DWORD dst_unused:UNUSED_PAD src0_sel:DWORD src1_sel:WORD_1
v_mfma_f32_16x16x32_bf16 v[140:143],a[224:227],v[28:31],v[140:143]
ds_read_u16 v45,v45
v_lshlrev_b32_sdwa v47,1,v47 dst_sel:DWORD dst_unused:UNUSED_PAD src0_sel:DWORD src1_sel:WORD_1
v_mfma_f32_16x16x32_bf16 v[68:71],a[248:251],v[28:31],v[68:71]
ds_read_u16_d16_hi v47,v47
v_lshlrev_b32_sdwa v48,1,v48 dst_sel:DWORD dst_unused:UNUSED_PAD src0_sel:DWORD src1_sel:WORD_1
v_mfma_f32_16x16x32_bf16 v[64:67],a[248:251],v[24:27],v[64:67]
ds_read_u16 v48,v48
ds_read_b64_tr_b16 a[30:31],v3 offset:40600
v_mfma_f32_16x16x32_bf16 v[144:147],a[228:231],v[24:27],v[144:147]
ds_read_b64_tr_b16 a[32:33],v3 offset:39120
v_lshlrev_b32_sdwa v50,1,v50 dst_sel:DWORD dst_unused:UNUSED_PAD src0_sel:DWORD src1_sel:WORD_1
v_mfma_f32_16x16x32_bf16 v[148:151],a[228:231],v[28:31],v[148:151]
ds_read_u16_d16_hi v50,v50
v_lshlrev_b32_sdwa v49,1,v49 dst_sel:DWORD dst_unused:UNUSED_PAD src0_sel:DWORD src1_sel:WORD_1
v_mfma_f32_16x16x32_bf16 v[84:87],a[236:239],v[28:31],v[84:87]
ds_read_u16 v49,v49
v_lshlrev_b32_sdwa v51,1,v51 dst_sel:DWORD dst_unused:UNUSED_PAD src0_sel:DWORD src1_sel:WORD_1
v_mfma_f32_16x16x32_bf16 v[80:83],a[236:239],v[24:27],v[80:83]
s_waitcnt lgkmcnt(9)
ds_read_u16_d16_hi v51,v51
v_mfma_f32_16x16x32_bf16 v[152:155],a[244:247],v[24:27],v[152:155]
s_nop 0
ds_read_b64_tr_b16 a[34:35],v3 offset:40656
v_mfma_f32_16x16x32_bf16 v[156:159],a[244:247],v[28:31],v[156:159]
ds_read_b64_tr_b16 a[36:37],v3 offset:45280
v_mfma_f32_16x16x32_bf16 v[100:103],a[252:255],v[28:31],v[100:103]
ds_read_b64_tr_b16 a[38:39],v3 offset:46816
v_mfma_f32_16x16x32_bf16 v[96:99],a[252:255],v[24:27],v[96:99]
ds_read_b64_tr_b16 a[40:41],v3 offset:45288
v_mfma_f32_16x16x32_bf16 v[28:31],a[208:211],a[56:59],v[20:23]
ds_read_b64_tr_b16 a[42:43],v3 offset:46824
v_mul_f32_dpp v136,v14,v136 row_newbcast:0 row_mask:0xf bank_mask:0xf
s_add_u32 s8,s61,3145728
v_lshlrev_b32_sdwa v40,1,v40 dst_sel:DWORD dst_unused:UNUSED_PAD src0_sel:DWORD src1_sel:WORD_1
v_mfma_f32_16x16x32_bf16 v[28:31],a[204:207],a[72:75],v[28:31]
ds_read_u16 v40,v40
v_mul_f32_dpp v137,v14,v137 row_newbcast:1 row_mask:0xf bank_mask:0xf
v_mul_f32_dpp v138,v14,v138 row_newbcast:2 row_mask:0xf bank_mask:0xf
v_mfma_f32_16x16x32_bf16 v[28:31],a[212:215],a[88:91],v[28:31]
ds_read_b64_tr_b16 a[44:45],v3 offset:39128
v_lshl_add_u64 v[32:33],v[32:33],0,v[34:35]
v_mul_f32_dpp v139,v14,v139 row_newbcast:3 row_mask:0xf bank_mask:0xf
v_lshlrev_b32_sdwa v42,1,v42 dst_sel:DWORD dst_unused:UNUSED_PAD src0_sel:DWORD src1_sel:WORD_1
v_mfma_f32_16x16x32_bf16 v[24:27],a[200:203],a[88:91],v[16:19]
ds_read_u16_d16_hi v42,v42
v_lshl_add_u64 v[34:35],v[60:61],0,v[62:63]
v_mul_f32_dpp v64,v14,v64 row_newbcast:4 row_mask:0xf bank_mask:0xf
v_lshlrev_b32_sdwa v41,1,v41 dst_sel:DWORD dst_unused:UNUSED_PAD src0_sel:DWORD src1_sel:WORD_1
v_mul_f32_dpp v65,v14,v65 row_newbcast:5 row_mask:0xf bank_mask:0xf
v_mfma_f32_16x16x32_bf16 v[24:27],a[196:199],a[72:75],v[24:27]
s_waitcnt lgkmcnt(8)
ds_read_u16 v41,v41
v_lshl_add_u64 v[60:61],v[44:45],0,v[46:47]
v_mul_f32_dpp v66,v14,v66 row_newbcast:6 row_mask:0xf bank_mask:0xf
v_mfma_f32_16x16x32_bf16 v[24:27],a[192:195],a[56:59],v[24:27]
s_nop 0
ds_read_b64_tr_b16 a[46:47],v3 offset:40664
v_mul_f32_dpp v67,v14,v67 row_newbcast:7 row_mask:0xf bank_mask:0xf
v_lshlrev_b32_sdwa v43,1,v43 dst_sel:DWORD dst_unused:UNUSED_PAD src0_sel:DWORD src1_sel:WORD_1
v_cvt_pk_bf16_f32 v136,v136,v137
v_mfma_f32_16x16x32_bf16 v[44:47],a[192:195],a[60:63],v[16:19]
ds_read_b128 v[16:19],v4 offset:0
v_cvt_pk_bf16_f32 v137,v138,v139
v_lshlrev_b32_sdwa v36,1,v36 dst_sel:DWORD dst_unused:UNUSED_PAD src0_sel:DWORD src1_sel:WORD_1
v_mfma_f32_16x16x32_bf16 v[44:47],a[196:199],a[76:79],v[44:47]
ds_read_u16_d16_hi v43,v43
ds_read_b64_tr_b16 a[192:193],v1 offset:22576
v_lshl_add_u64 v[62:63],v[48:49],0,v[50:51]
v_cvt_pk_bf16_f32 v138,v64,v65
v_cvt_pk_bf16_f32 v139,v66,v67
v_lshlrev_b32_sdwa v38,1,v38 dst_sel:DWORD dst_unused:UNUSED_PAD src0_sel:DWORD src1_sel:WORD_1
v_mfma_f32_16x16x32_bf16 v[44:47],a[200:203],a[92:95],v[44:47]
buffer_store_dwordx4 v[136:139],v7,s[40:43],s8 offen offset:0 sc1
ds_read_b64_tr_b16 a[194:195],v1 offset:23600
v_mul_f32_dpp v144,v14,v144 row_newbcast:8 row_mask:0xf bank_mask:0xf
v_mfma_f32_16x16x32_bf16 v[48:51],a[212:215],a[92:95],v[20:23]
ds_read_b128 v[20:23],v4 offset:32
ds_read_u16 v36,v36
v_mul_f32_dpp v145,v14,v145 row_newbcast:9 row_mask:0xf bank_mask:0xf
v_lshlrev_b32_sdwa v37,1,v37 dst_sel:DWORD dst_unused:UNUSED_PAD src0_sel:DWORD src1_sel:WORD_1
v_mul_f32_dpp v146,v14,v146 row_newbcast:10 row_mask:0xf bank_mask:0xf
v_mfma_f32_16x16x32_bf16 v[48:51],a[204:207],a[76:79],v[48:51]
ds_read_b64_tr_b16 a[196:197],v2 offset:22576
ds_read_b64_tr_b16 a[198:199],v2 offset:23600
v_mul_f32_dpp v147,v14,v147 row_newbcast:11 row_mask:0xf bank_mask:0xf
v_lshlrev_b32_sdwa v39,1,v39 dst_sel:DWORD dst_unused:UNUSED_PAD src0_sel:DWORD src1_sel:WORD_1
v_mfma_f32_16x16x32_bf16 v[48:51],a[208:211],a[60:63],v[48:51]
ds_read_u16_d16_hi v38,v38
ds_read_u16 v37,v37
v_mul_f32_dpp v80,v14,v80 row_newbcast:12 row_mask:0xf bank_mask:0xf
v_mul_f32_dpp v81,v14,v81 row_newbcast:13 row_mask:0xf bank_mask:0xf
v_mfma_f32_16x16x32_bf16 v[112:115],a[224:227],v[32:35],v[112:115]
ds_read_u16_d16_hi v39,v39
ds_read_b64_tr_b16 a[200:201],v2 offset:20528
v_lshlrev_b32_sdwa v56,1,v56 dst_sel:DWORD dst_unused:UNUSED_PAD src0_sel:DWORD src1_sel:WORD_1
v_mul_f32_dpp v82,v14,v82 row_newbcast:14 row_mask:0xf bank_mask:0xf
v_mfma_f32_16x16x32_bf16 v[116:119],a[224:227],v[60:63],v[116:119]
ds_read_b64_tr_b16 a[202:203],v2 offset:21552
v_mul_f32_dpp v83,v14,v83 row_newbcast:15 row_mask:0xf bank_mask:0xf
v_lshlrev_b32_sdwa v58,1,v58 dst_sel:DWORD dst_unused:UNUSED_PAD src0_sel:DWORD src1_sel:WORD_1
v_cvt_pk_bf16_f32 v144,v144,v145
v_mfma_f32_16x16x32_bf16 v[76:79],a[248:251],v[60:63],v[76:79]
ds_read_u16 v56,v56
v_cvt_pk_bf16_f32 v145,v146,v147
v_lshlrev_b32_sdwa v57,1,v57 dst_sel:DWORD dst_unused:UNUSED_PAD src0_sel:DWORD src1_sel:WORD_1
v_mfma_f32_16x16x32_bf16 v[72:75],a[248:251],v[32:35],v[72:75]
ds_read_u16_d16_hi v58,v58
v_cvt_pk_bf16_f32 v146,v80,v81
v_cvt_pk_bf16_f32 v147,v82,v83
v_mfma_f32_16x16x32_bf16 v[120:123],a[228:231],v[32:35],v[120:123]
ds_read_u16 v57,v57
v_mul_f32_dpp v152,v15,v152 row_newbcast:0 row_mask:0xf bank_mask:0xf
v_lshlrev_b32_sdwa v59,1,v59 dst_sel:DWORD dst_unused:UNUSED_PAD src0_sel:DWORD src1_sel:WORD_1
v_mfma_f32_16x16x32_bf16 v[124:127],a[228:231],v[60:63],v[124:127]
ds_read_u16_d16_hi v59,v59
v_mul_f32_dpp v153,v15,v153 row_newbcast:1 row_mask:0xf bank_mask:0xf
v_lshlrev_b32_sdwa v52,1,v52 dst_sel:DWORD dst_unused:UNUSED_PAD src0_sel:DWORD src1_sel:WORD_1
v_mul_f32_dpp v154,v15,v154 row_newbcast:2 row_mask:0xf bank_mask:0xf
v_mfma_f32_16x16x32_bf16 v[92:95],a[236:239],v[60:63],v[92:95]
ds_read_b64_tr_b16 a[212:213],v2 offset:18480
v_mul_f32_dpp v155,v15,v155 row_newbcast:3 row_mask:0xf bank_mask:0xf
v_mfma_f32_16x16x32_bf16 v[88:91],a[236:239],v[32:35],v[88:91]
ds_read_b64_tr_b16 a[214:215],v2 offset:19504
v_mul_f32_dpp v96,v15,v96 row_newbcast:4 row_mask:0xf bank_mask:0xf
v_lshlrev_b32_sdwa v54,1,v54 dst_sel:DWORD dst_unused:UNUSED_PAD src0_sel:DWORD src1_sel:WORD_1
v_mul_f32_dpp v97,v15,v97 row_newbcast:5 row_mask:0xf bank_mask:0xf
v_mfma_f32_16x16x32_bf16 v[128:131],a[244:247],v[32:35],v[128:131]
buffer_store_dwordx4 v[144:147],v7,s[40:43],s8 offen offset:64 sc1
v_mul_f32_dpp v98,v15,v98 row_newbcast:6 row_mask:0xf bank_mask:0xf
v_lshlrev_b32_sdwa v53,1,v53 dst_sel:DWORD dst_unused:UNUSED_PAD src0_sel:DWORD src1_sel:WORD_1
v_mfma_f32_16x16x32_bf16 v[132:135],a[244:247],v[60:63],v[132:135]
ds_read_u16 v52,v52
v_mul_f32_dpp v99,v15,v99 row_newbcast:7 row_mask:0xf bank_mask:0xf
v_cvt_pk_bf16_f32 v152,v152,v153
v_lshlrev_b32_sdwa v55,1,v55 dst_sel:DWORD dst_unused:UNUSED_PAD src0_sel:DWORD src1_sel:WORD_1
v_mfma_f32_16x16x32_bf16 v[108:111],a[252:255],v[60:63],v[108:111]
s_nop 0
ds_read_u16_d16_hi v54,v54
v_cvt_pk_bf16_f32 v153,v154,v155
v_mfma_f32_16x16x32_bf16 v[104:107],a[252:255],v[32:35],v[104:107]
s_waitcnt lgkmcnt(7)
ds_read_u16 v53,v53
ds_read_u16_d16_hi v55,v55
v_cvt_pk_bf16_f32 v154,v96,v97
v_cvt_pk_bf16_f32 v155,v98,v99
v_mfma_f32_16x16x32_bf16 v[60:63],a[220:223],a[48:51],v[16:19]
v_mul_f32_dpp v140,v14,v140 row_newbcast:0 row_mask:0xf bank_mask:0xf
v_lshlrev_b32_sdwa v28,1,v28 dst_sel:DWORD dst_unused:UNUSED_PAD src0_sel:DWORD src1_sel:WORD_1
v_mfma_f32_16x16x32_bf16 v[60:63],a[216:219],a[64:67],v[60:63]
ds_read_u16 v28,v28
v_mul_f32_dpp v141,v14,v141 row_newbcast:1 row_mask:0xf bank_mask:0xf
v_mul_f32_dpp v142,v14,v142 row_newbcast:2 row_mask:0xf bank_mask:0xf
v_mfma_f32_16x16x32_bf16 v[60:63],a[192:195],a[80:83],v[60:63]
v_lshl_add_u64 v[40:41],v[40:41],0,v[42:43]
v_mul_f32_dpp v143,v14,v143 row_newbcast:3 row_mask:0xf bank_mask:0xf
v_lshlrev_b32_sdwa v30,1,v30 dst_sel:DWORD dst_unused:UNUSED_PAD src0_sel:DWORD src1_sel:WORD_1
v_mfma_f32_16x16x32_bf16 v[32:35],a[196:199],a[80:83],v[20:23]
ds_read_u16_d16_hi v30,v30
v_lshl_add_u64 v[42:43],v[36:37],0,v[38:39]
v_mul_f32_dpp v68,v14,v68 row_newbcast:4 row_mask:0xf bank_mask:0xf
v_lshlrev_b32_sdwa v29,1,v29 dst_sel:DWORD dst_unused:UNUSED_PAD src0_sel:DWORD src1_sel:WORD_1
v_mul_f32_dpp v69,v14,v69 row_newbcast:5 row_mask:0xf bank_mask:0xf
v_mfma_f32_16x16x32_bf16 v[32:35],a[200:203],a[64:67],v[32:35]
s_waitcnt lgkmcnt(6)
ds_read_u16 v29,v29
v_lshl_add_u64 v[36:37],v[56:57],0,v[58:59]
v_mul_f32_dpp v70,v14,v70 row_newbcast:6 row_mask:0xf bank_mask:0xf
v_mfma_f32_16x16x32_bf16 v[32:35],a[212:215],a[48:51],v[32:35]
v_mul_f32_dpp v71,v14,v71 row_newbcast:7 row_mask:0xf bank_mask:0xf
v_lshlrev_b32_sdwa v31,1,v31 dst_sel:DWORD dst_unused:UNUSED_PAD src0_sel:DWORD src1_sel:WORD_1
v_cvt_pk_bf16_f32 v140,v140,v141
v_mfma_f32_16x16x32_bf16 v[56:59],a[212:215],a[52:55],v[20:23]
ds_read_u16_d16_hi v31,v31
v_cvt_pk_bf16_f32 v141,v142,v143
v_lshlrev_b32_sdwa v24,1,v24 dst_sel:DWORD dst_unused:UNUSED_PAD src0_sel:DWORD src1_sel:WORD_1
v_mfma_f32_16x16x32_bf16 v[56:59],a[200:203],a[68:71],v[56:59]
s_waitcnt lgkmcnt(4)
ds_read_u16 v24,v24
v_lshl_add_u64 v[38:39],v[52:53],0,v[54:55]
v_cvt_pk_bf16_f32 v142,v68,v69
v_cvt_pk_bf16_f32 v143,v70,v71
v_lshlrev_b32_sdwa v26,1,v26 dst_sel:DWORD dst_unused:UNUSED_PAD src0_sel:DWORD src1_sel:WORD_1
v_mfma_f32_16x16x32_bf16 v[56:59],a[196:199],a[84:87],v[56:59]
ds_read_u16_d16_hi v26,v26
v_mul_f32_dpp v148,v14,v148 row_newbcast:8 row_mask:0xf bank_mask:0xf
v_mfma_f32_16x16x32_bf16 v[52:55],a[192:195],a[84:87],v[16:19]
buffer_store_dwordx4 v[152:155],v7,s[40:43],s8 offen offset:128 sc1
v_mul_f32_dpp v149,v14,v149 row_newbcast:9 row_mask:0xf bank_mask:0xf
v_lshlrev_b32_sdwa v25,1,v25 dst_sel:DWORD dst_unused:UNUSED_PAD src0_sel:DWORD src1_sel:WORD_1
v_mul_f32_dpp v150,v14,v150 row_newbcast:10 row_mask:0xf bank_mask:0xf
v_mfma_f32_16x16x32_bf16 v[52:55],a[216:219],a[68:71],v[52:55]
ds_read_u16 v25,v25
v_mul_f32_dpp v151,v14,v151 row_newbcast:11 row_mask:0xf bank_mask:0xf
v_lshlrev_b32_sdwa v27,1,v27 dst_sel:DWORD dst_unused:UNUSED_PAD src0_sel:DWORD src1_sel:WORD_1
v_mfma_f32_16x16x32_bf16 v[52:55],a[220:223],a[52:55],v[52:55]
ds_read_u16_d16_hi v27,v27
v_mul_f32_dpp v84,v14,v84 row_newbcast:12 row_mask:0xf bank_mask:0xf
v_mul_f32_dpp v85,v14,v85 row_newbcast:13 row_mask:0xf bank_mask:0xf
v_mfma_f32_16x16x32_bf16 v[232:235],a[224:227],v[40:43],v[232:235]
ds_read_b64_tr_b16 a[232:233],v3 offset:32832
v_lshlrev_b32_sdwa v48,1,v48 dst_sel:DWORD dst_unused:UNUSED_PAD src0_sel:DWORD src1_sel:WORD_1
v_mul_f32_dpp v86,v14,v86 row_newbcast:14 row_mask:0xf bank_mask:0xf
v_mfma_f32_16x16x32_bf16 v[236:239],a[224:227],v[36:39],v[236:239]
ds_read_u16 v48,v48
v_mul_f32_dpp v87,v14,v87 row_newbcast:15 row_mask:0xf bank_mask:0xf
v_lshlrev_b32_sdwa v50,1,v50 dst_sel:DWORD dst_unused:UNUSED_PAD src0_sel:DWORD src1_sel:WORD_1
v_cvt_pk_bf16_f32 v148,v148,v149
v_mfma_f32_16x16x32_bf16 v[164:167],a[248:251],v[36:39],v[164:167]
ds_read_u16_d16_hi v50,v50
v_cvt_pk_bf16_f32 v149,v150,v151
v_lshlrev_b32_sdwa v49,1,v49 dst_sel:DWORD dst_unused:UNUSED_PAD src0_sel:DWORD src1_sel:WORD_1
v_mfma_f32_16x16x32_bf16 v[160:163],a[248:251],v[40:43],v[160:163]
ds_read_u16 v49,v49
v_cvt_pk_bf16_f32 v150,v84,v85
v_cvt_pk_bf16_f32 v151,v86,v87
v_mfma_f32_16x16x32_bf16 v[240:243],a[228:231],v[40:43],v[240:243]
ds_read_b64_tr_b16 a[234:235],v3 offset:34368
v_mul_f32_dpp v156,v15,v156 row_newbcast:0 row_mask:0xf bank_mask:0xf
v_lshlrev_b32_sdwa v51,1,v51 dst_sel:DWORD dst_unused:UNUSED_PAD src0_sel:DWORD src1_sel:WORD_1
v_mfma_f32_16x16x32_bf16 v[244:247],a[228:231],v[36:39],v[244:247]
ds_read_u16_d16_hi v51,v51
v_mul_f32_dpp v157,v15,v157 row_newbcast:1 row_mask:0xf bank_mask:0xf
v_lshlrev_b32_sdwa v44,1,v44 dst_sel:DWORD dst_unused:UNUSED_PAD src0_sel:DWORD src1_sel:WORD_1
v_mul_f32_dpp v158,v15,v158 row_newbcast:2 row_mask:0xf bank_mask:0xf
v_mfma_f32_16x16x32_bf16 v[180:183],a[236:239],v[36:39],v[180:183]
ds_read_u16 v44,v44
v_mul_f32_dpp v159,v15,v159 row_newbcast:3 row_mask:0xf bank_mask:0xf
v_mfma_f32_16x16x32_bf16 v[176:179],a[236:239],v[40:43],v[176:179]
ds_read_b64_tr_b16 a[240:241],v3 offset:32840
v_mul_f32_dpp v100,v15,v100 row_newbcast:4 row_mask:0xf bank_mask:0xf
v_lshlrev_b32_sdwa v46,1,v46 dst_sel:DWORD dst_unused:UNUSED_PAD src0_sel:DWORD src1_sel:WORD_1
v_mul_f32_dpp v101,v15,v101 row_newbcast:5 row_mask:0xf bank_mask:0xf
v_mfma_f32_16x16x32_bf16 v[248:251],a[244:247],v[40:43],v[248:251]
ds_read_u16_d16_hi v46,v46
v_mul_f32_dpp v102,v15,v102 row_newbcast:6 row_mask:0xf bank_mask:0xf
v_lshlrev_b32_sdwa v45,1,v45 dst_sel:DWORD dst_unused:UNUSED_PAD src0_sel:DWORD src1_sel:WORD_1
v_mfma_f32_16x16x32_bf16 v[252:255],a[244:247],v[36:39],v[252:255]
ds_read_u16 v45,v45
v_mul_f32_dpp v103,v15,v103 row_newbcast:7 row_mask:0xf bank_mask:0xf
v_cvt_pk_bf16_f32 v156,v156,v157
v_lshlrev_b32_sdwa v47,1,v47 dst_sel:DWORD dst_unused:UNUSED_PAD src0_sel:DWORD src1_sel:WORD_1
v_mfma_f32_16x16x32_bf16 v[196:199],a[252:255],v[36:39],v[196:199]
buffer_store_dwordx4 v[140:143],v7,s[40:43],s8 offen offset:192 sc1
v_cvt_pk_bf16_f32 v157,v158,v159
v_mfma_f32_16x16x32_bf16 v[192:195],a[252:255],v[40:43],v[192:195]
ds_read_u16_d16_hi v47,v47
v_cvt_pk_bf16_f32 v158,v100,v101
v_cvt_pk_bf16_f32 v159,v102,v103
v_mfma_f32_16x16x32_bf16 v[36:39],a[220:223],a[56:59],v[16:19]
ds_read_b128 v[64:67],v4 offset:128
ds_read_b128 v[68:71],v4 offset:160
v_mul_f32_dpp v112,v14,v112 row_newbcast:0 row_mask:0xf bank_mask:0xf
s_add_u32 s9,s61,3244032
v_lshlrev_b32_sdwa v60,1,v60 dst_sel:DWORD dst_unused:UNUSED_PAD src0_sel:DWORD src1_sel:WORD_1
v_mfma_f32_16x16x32_bf16 v[36:39],a[216:219],a[72:75],v[36:39]
ds_read_u16 v60,v60
ds_read_b128 v[80:83],v4 offset:256
v_mul_f32_dpp v113,v14,v113 row_newbcast:1 row_mask:0xf bank_mask:0xf
v_mul_f32_dpp v114,v14,v114 row_newbcast:2 row_mask:0xf bank_mask:0xf
v_mfma_f32_16x16x32_bf16 v[36:39],a[192:195],a[88:91],v[36:39]
s_waitcnt lgkmcnt(9)
ds_read_b128 v[84:87],v4 offset:288
v_lshl_add_u64 v[28:29],v[28:29],0,v[30:31]
v_mul_f32_dpp v115,v14,v115 row_newbcast:3 row_mask:0xf bank_mask:0xf
v_lshlrev_b32_sdwa v62,1,v62 dst_sel:DWORD dst_unused:UNUSED_PAD src0_sel:DWORD src1_sel:WORD_1
v_mfma_f32_16x16x32_bf16 v[40:43],a[196:199],a[88:91],v[20:23]
s_nop 0
ds_read_u16_d16_hi v62,v62
v_lshl_add_u64 v[30:31],v[24:25],0,v[26:27]
v_mul_f32_dpp v72,v14,v72 row_newbcast:4 row_mask:0xf bank_mask:0xf
v_lshlrev_b32_sdwa v61,1,v61 dst_sel:DWORD dst_unused:UNUSED_PAD src0_sel:DWORD src1_sel:WORD_1
v_mul_f32_dpp v73,v14,v73 row_newbcast:5 row_mask:0xf bank_mask:0xf
v_mfma_f32_16x16x32_bf16 v[40:43],a[200:203],a[72:75],v[40:43]
TP10200_%=:
s_cbranch_execz TR1_%=
ds_read_u16 v61,v61
v_lshl_add_u64 v[24:25],v[48:49],0,v[50:51]
v_mul_f32_dpp v74,v14,v74 row_newbcast:6 row_mask:0xf bank_mask:0xf
v_mfma_f32_16x16x32_bf16 v[40:43],a[212:215],a[56:59],v[40:43]
s_nop 0
ds_read_b64_tr_b16 a[242:243],v3 offset:34376
v_mul_f32_dpp v75,v14,v75 row_newbcast:7 row_mask:0xf bank_mask:0xf
v_lshlrev_b32_sdwa v63,1,v63 dst_sel:DWORD dst_unused:UNUSED_PAD src0_sel:DWORD src1_sel:WORD_1
v_cvt_pk_bf16_f32 v112,v112,v113
v_mfma_f32_16x16x32_bf16 v[48:51],a[212:215],a[60:63],v[20:23]
s_nop 0
ds_read_b64_tr_b16 a[212:213],v3 offset:51312
ds_read_b64_tr_b16 a[214:215],v3 offset:52848
v_cvt_pk_bf16_f32 v113,v114,v115
v_lshlrev_b32_sdwa v32,1,v32 dst_sel:DWORD dst_unused:UNUSED_PAD src0_sel:DWORD src1_sel:WORD_1
v_mfma_f32_16x16x32_bf16 v[48:51],a[200:203],a[76:79],v[48:51]
s_waitcnt lgkmcnt(10)
ds_read_b64_tr_b16 a[200:201],v3 offset:57472
ds_read_b64_tr_b16 a[202:203],v3 offset:59008
v_lshl_add_u64 v[26:27],v[44:45],0,v[46:47]
v_cvt_pk_bf16_f32 v114,v72,v73
v_cvt_pk_bf16_f32 v115,v74,v75
v_lshlrev_b32_sdwa v34,1,v34 dst_sel:DWORD dst_unused:UNUSED_PAD src0_sel:DWORD src1_sel:WORD_1
v_mfma_f32_16x16x32_bf16 v[48:51],a[196:199],a[92:95],v[48:51]
ds_read_u16_d16_hi v63,v63
ds_read_u16 v32,v32
v_mul_f32_dpp v120,v14,v120 row_newbcast:8 row_mask:0xf bank_mask:0xf
v_mfma_f32_16x16x32_bf16 v[44:47],a[192:195],a[92:95],v[16:19]
ds_read_u16_d16_hi v34,v34
ds_read_b64_tr_b16 a[196:197],v3 offset:57480
v_mul_f32_dpp v121,v14,v121 row_newbcast:9 row_mask:0xf bank_mask:0xf
v_lshlrev_b32_sdwa v33,1,v33 dst_sel:DWORD dst_unused:UNUSED_PAD src0_sel:DWORD src1_sel:WORD_1
v_mul_f32_dpp v122,v14,v122 row_newbcast:10 row_mask:0xf bank_mask:0xf
v_mfma_f32_16x16x32_bf16 v[44:47],a[216:219],a[76:79],v[44:47]
buffer_store_dwordx4 v[148:151],v7,s[40:43],s8 offen offset:256 sc1
ds_read_u16 v33,v33
v_mul_f32_dpp v123,v14,v123 row_newbcast:11 row_mask:0xf bank_mask:0xf
v_lshlrev_b32_sdwa v35,1,v35 dst_sel:DWORD dst_unused:UNUSED_PAD src0_sel:DWORD src1_sel:WORD_1
v_mfma_f32_16x16x32_bf16 v[44:47],a[220:223],a[60:63],v[44:47]
ds_read_u16_d16_hi v35,v35
ds_read_b64_tr_b16 a[198:199],v3 offset:59016
v_mul_f32_dpp v88,v14,v88 row_newbcast:12 row_mask:0xf bank_mask:0xf
v_mul_f32_dpp v89,v14,v89 row_newbcast:13 row_mask:0xf bank_mask:0xf
v_mfma_f32_16x16x32_bf16 v[208:211],a[224:227],v[28:31],v[208:211]
ds_read_b64_tr_b16 a[192:193],v3 offset:51320
ds_read_b64_tr_b16 a[194:195],v3 offset:52856
v_lshlrev_b32_sdwa v52,1,v52 dst_sel:DWORD dst_unused:UNUSED_PAD src0_sel:DWORD src1_sel:WORD_1
v_mul_f32_dpp v90,v14,v90 row_newbcast:14 row_mask:0xf bank_mask:0xf
v_mfma_f32_16x16x32_bf16 v[212:215],a[224:227],v[24:27],v[212:215]
ds_read_u16 v52,v52
ds_read_b64_tr_b16 a[224:225],v3 offset:32896
v_mul_f32_dpp v91,v14,v91 row_newbcast:15 row_mask:0xf bank_mask:0xf
v_lshlrev_b32_sdwa v54,1,v54 dst_sel:DWORD dst_unused:UNUSED_PAD src0_sel:DWORD src1_sel:WORD_1
v_cvt_pk_bf16_f32 v120,v120,v121
v_mfma_f32_16x16x32_bf16 v[172:175],a[248:251],v[24:27],v[172:175]
ds_read_u16_d16_hi v54,v54
ds_read_b64_tr_b16 a[226:227],v3 offset:34432
v_cvt_pk_bf16_f32 v121,v122,v123
v_lshlrev_b32_sdwa v53,1,v53 dst_sel:DWORD dst_unused:UNUSED_PAD src0_sel:DWORD src1_sel:WORD_1
v_mfma_f32_16x16x32_bf16 v[168:171],a[248:251],v[28:31],v[168:171]
ds_read_u16 v53,v53
ds_read_b64_tr_b16 a[248:249],v3 offset:32904
v_cvt_pk_bf16_f32 v122,v88,v89
v_cvt_pk_bf16_f32 v123,v90,v91
v_mfma_f32_16x16x32_bf16 v[216:219],a[228:231],v[28:31],v[216:219]
ds_read_b64_tr_b16 a[250:251],v3 offset:34440
ds_read_b128 v[96:99],v4 offset:384
v_mul_f32_dpp v128,v15,v128 row_newbcast:0 row_mask:0xf bank_mask:0xf
v_lshlrev_b32_sdwa v55,1,v55 dst_sel:DWORD dst_unused:UNUSED_PAD src0_sel:DWORD src1_sel:WORD_1
v_mfma_f32_16x16x32_bf16 v[220:223],a[228:231],v[24:27],v[220:223]
ds_read_u16_d16_hi v55,v55
ds_read_b128 v[100:103],v4 offset:416
v_mul_f32_dpp v129,v15,v129 row_newbcast:1 row_mask:0xf bank_mask:0xf
v_lshlrev_b32_sdwa v56,1,v56 dst_sel:DWORD dst_unused:UNUSED_PAD src0_sel:DWORD src1_sel:WORD_1
v_mul_f32_dpp v130,v15,v130 row_newbcast:2 row_mask:0xf bank_mask:0xf
v_mfma_f32_16x16x32_bf16 v[188:191],a[236:239],v[24:27],v[188:191]
ds_read_u16 v56,v56
ds_read_b64_tr_b16 a[228:229],v3 offset:32960
v_mul_f32_dpp v131,v15,v131 row_newbcast:3 row_mask:0xf bank_mask:0xf
v_mfma_f32_16x16x32_bf16 v[184:187],a[236:239],v[28:31],v[184:187]
ds_read_b64_tr_b16 a[230:231],v3 offset:34496
ds_read_b64_tr_b16 a[236:237],v3 offset:32968
v_mul_f32_dpp v104,v15,v104 row_newbcast:4 row_mask:0xf bank_mask:0xf
v_lshlrev_b32_sdwa v58,1,v58 dst_sel:DWORD dst_unused:UNUSED_PAD src0_sel:DWORD src1_sel:WORD_1
v_mul_f32_dpp v105,v15,v105 row_newbcast:5 row_mask:0xf bank_mask:0xf
v_mfma_f32_16x16x32_bf16 v[224:227],a[244:247],v[28:31],v[224:227]
ds_read_u16_d16_hi v58,v58
ds_read_b64_tr_b16 a[238:239],v3 offset:34504
v_mul_f32_dpp v106,v15,v106 row_newbcast:6 row_mask:0xf bank_mask:0xf
v_lshlrev_b32_sdwa v57,1,v57 dst_sel:DWORD dst_unused:UNUSED_PAD src0_sel:DWORD src1_sel:WORD_1
v_mfma_f32_16x16x32_bf16 v[228:231],a[244:247],v[24:27],v[228:231]
buffer_store_dwordx4 v[156:159],v7,s[40:43],s8 offen offset:320 sc1
ds_read_u16 v57,v57
v_mul_f32_dpp v107,v15,v107 row_newbcast:7 row_mask:0xf bank_mask:0xf
v_cvt_pk_bf16_f32 v128,v128,v129
v_lshlrev_b32_sdwa v59,1,v59 dst_sel:DWORD dst_unused:UNUSED_PAD src0_sel:DWORD src1_sel:WORD_1
v_mfma_f32_16x16x32_bf16 v[204:207],a[252:255],v[24:27],v[204:207]
s_nop 0
ds_read_u16_d16_hi v59,v59
ds_read_b64_tr_b16 a[216:217],v3 offset:51376
v_cvt_pk_bf16_f32 v129,v130,v131
v_mfma_f32_16x16x32_bf16 v[200:203],a[252:255],v[28:31],v[200:203]
s_waitcnt lgkmcnt(15)
ds_read_b64_tr_b16 a[218:219],v3 offset:52912
ds_read_b128 v[20:23],v4 offset:512
v_cvt_pk_bf16_f32 v130,v104,v105
v_cvt_pk_bf16_f32 v131,v106,v107
v_mfma_f32_16x16x32_bf16 v[24:27],a[96:99],a[48:51],v[64:67]
ds_read_b128 v[16:19],v4 offset:544
ds_read_b64_tr_b16 a[220:221],v3 offset:57536
v_mul_f32_dpp v116,v14,v116 row_newbcast:0 row_mask:0xf bank_mask:0xf
v_lshlrev_b32_sdwa v36,1,v36 dst_sel:DWORD dst_unused:UNUSED_PAD src0_sel:DWORD src1_sel:WORD_1
v_mfma_f32_16x16x32_bf16 v[24:27],a[100:103],a[64:67],v[24:27]
ds_read_u16 v36,v36
ds_read_b64_tr_b16 a[222:223],v3 offset:59072
v_mul_f32_dpp v117,v14,v117 row_newbcast:1 row_mask:0xf bank_mask:0xf
v_mul_f32_dpp v118,v14,v118 row_newbcast:2 row_mask:0xf bank_mask:0xf
v_mfma_f32_16x16x32_bf16 v[24:27],a[104:107],a[80:83],v[24:27]
ds_read_b64_tr_b16 a[204:205],v3 offset:57544
v_lshl_add_u64 v[60:61],v[60:61],0,v[62:63]
v_mul_f32_dpp v119,v14,v119 row_newbcast:3 row_mask:0xf bank_mask:0xf
v_lshlrev_b32_sdwa v38,1,v38 dst_sel:DWORD dst_unused:UNUSED_PAD src0_sel:DWORD src1_sel:WORD_1
v_mfma_f32_16x16x32_bf16 v[28:31],a[108:111],a[80:83],v[68:71]
ds_read_u16_d16_hi v38,v38
v_lshl_add_u64 v[62:63],v[32:33],0,v[34:35]
v_mul_f32_dpp v76,v14,v76 row_newbcast:4 row_mask:0xf bank_mask:0xf
v_lshlrev_b32_sdwa v37,1,v37 dst_sel:DWORD dst_unused:UNUSED_PAD src0_sel:DWORD src1_sel:WORD_1
v_mul_f32_dpp v77,v14,v77 row_newbcast:5 row_mask:0xf bank_mask:0xf
v_mfma_f32_16x16x32_bf16 v[28:31],a[112:115],a[64:67],v[28:31]
s_waitcnt lgkmcnt(13)
ds_read_u16 v37,v37
v_lshl_add_u64 v[32:33],v[52:53],0,v[54:55]
v_mul_f32_dpp v78,v14,v78 row_newbcast:6 row_mask:0xf bank_mask:0xf
v_mfma_f32_16x16x32_bf16 v[28:31],a[116:119],a[48:51],v[28:31]
s_nop 0
ds_read_b64_tr_b16 a[206:207],v3 offset:59080
v_mul_f32_dpp v79,v14,v79 row_newbcast:7 row_mask:0xf bank_mask:0xf
v_lshlrev_b32_sdwa v39,1,v39 dst_sel:DWORD dst_unused:UNUSED_PAD src0_sel:DWORD src1_sel:WORD_1
v_cvt_pk_bf16_f32 v116,v116,v117
v_mfma_f32_16x16x32_bf16 v[52:55],a[120:123],a[48:51],v[84:87]
s_nop 0
ds_read_u16_d16_hi v39,v39
v_cvt_pk_bf16_f32 v117,v118,v119
v_lshlrev_b32_sdwa v40,1,v40 dst_sel:DWORD dst_unused:UNUSED_PAD src0_sel:DWORD src1_sel:WORD_1
v_mfma_f32_16x16x32_bf16 v[52:55],a[124:127],a[64:67],v[52:55]
s_waitcnt lgkmcnt(11)
ds_read_u16 v40,v40
ds_read_b64_tr_b16 a[208:209],v3 offset:51384
v_lshl_add_u64 v[34:35],v[56:57],0,v[58:59]
v_cvt_pk_bf16_f32 v118,v76,v77
v_cvt_pk_bf16_f32 v119,v78,v79
v_lshlrev_b32_sdwa v42,1,v42 dst_sel:DWORD dst_unused:UNUSED_PAD src0_sel:DWORD src1_sel:WORD_1
v_mfma_f32_16x16x32_bf16 v[52:55],a[128:131],a[80:83],v[52:55]
buffer_store_dwordx4 v[112:115],v7,s[40:43],s9 offen offset:0 sc1
ds_read_u16_d16_hi v42,v42
v_mul_f32_dpp v124,v14,v124 row_newbcast:8 row_mask:0xf bank_mask:0xf
v_mfma_f32_16x16x32_bf16 v[56:59],a[132:135],a[80:83],v[80:83]
ds_read_b64_tr_b16 a[210:211],v3 offset:52920
v_mul_f32_dpp v125,v14,v125 row_newbcast:9 row_mask:0xf bank_mask:0xf
v_lshlrev_b32_sdwa v41,1,v41 dst_sel:DWORD dst_unused:UNUSED_PAD src0_sel:DWORD src1_sel:WORD_1
v_mul_f32_dpp v126,v14,v126 row_newbcast:10 row_mask:0xf bank_mask:0xf
v_mfma_f32_16x16x32_bf16 v[56:59],a[136:139],a[64:67],v[56:59]
ds_read_u16 v41,v41
v_mul_f32_dpp v127,v14,v127 row_newbcast:11 row_mask:0xf bank_mask:0xf
v_lshlrev_b32_sdwa v43,1,v43 dst_sel:DWORD dst_unused:UNUSED_PAD src0_sel:DWORD src1_sel:WORD_1
v_mfma_f32_16x16x32_bf16 v[56:59],a[140:143],a[48:51],v[56:59]
ds_read_u16_d16_hi v43,v43
v_mul_f32_dpp v92,v14,v92 row_newbcast:12 row_mask:0xf bank_mask:0xf
v_mul_f32_dpp v93,v14,v93 row_newbcast:13 row_mask:0xf bank_mask:0xf
v_mfma_f32_16x16x32_bf16 v[232:235],a[232:235],v[60:63],v[232:235]
ds_read_b64_tr_b16 a[244:245],v3 offset:51440
v_lshlrev_b32_sdwa v44,1,v44 dst_sel:DWORD dst_unused:UNUSED_PAD src0_sel:DWORD src1_sel:WORD_1
v_mul_f32_dpp v94,v14,v94 row_newbcast:14 row_mask:0xf bank_mask:0xf
v_mfma_f32_16x16x32_bf16 v[236:239],a[232:235],v[32:35],v[236:239]
ds_read_u16 v44,v44
v_mul_f32_dpp v95,v14,v95 row_newbcast:15 row_mask:0xf bank_mask:0xf
v_lshlrev_b32_sdwa v46,1,v46 dst_sel:DWORD dst_unused:UNUSED_PAD src0_sel:DWORD src1_sel:WORD_1
v_cvt_pk_bf16_f32 v124,v124,v125
v_mfma_f32_16x16x32_bf16 v[164:167],a[240:243],v[32:35],v[164:167]
ds_read_u16_d16_hi v46,v46
v_cvt_pk_bf16_f32 v125,v126,v127
v_lshlrev_b32_sdwa v45,1,v45 dst_sel:DWORD dst_unused:UNUSED_PAD src0_sel:DWORD src1_sel:WORD_1
v_mfma_f32_16x16x32_bf16 v[160:163],a[240:243],v[60:63],v[160:163]
ds_read_u16 v45,v45
v_cvt_pk_bf16_f32 v126,v92,v93
v_cvt_pk_bf16_f32 v127,v94,v95
v_mfma_f32_16x16x32_bf16 v[240:243],a[224:227],v[60:63],v[240:243]
ds_read_b64_tr_b16 a[246:247],v3 offset:52976
v_mul_f32_dpp v132,v15,v132 row_newbcast:0 row_mask:0xf bank_mask:0xf
v_lshlrev_b32_sdwa v47,1,v47 dst_sel:DWORD dst_unused:UNUSED_PAD src0_sel:DWORD src1_sel:WORD_1
v_mfma_f32_16x16x32_bf16 v[244:247],a[224:227],v[32:35],v[244:247]
ds_read_u16_d16_hi v47,v47
v_mul_f32_dpp v133,v15,v133 row_newbcast:1 row_mask:0xf bank_mask:0xf
v_lshlrev_b32_sdwa v48,1,v48 dst_sel:DWORD dst_unused:UNUSED_PAD src0_sel:DWORD src1_sel:WORD_1
v_mul_f32_dpp v134,v15,v134 row_newbcast:2 row_mask:0xf bank_mask:0xf
v_mfma_f32_16x16x32_bf16 v[180:183],a[248:251],v[32:35],v[180:183]
ds_read_u16 v48,v48
v_mul_f32_dpp v135,v15,v135 row_newbcast:3 row_mask:0xf bank_mask:0xf
v_mfma_f32_16x16x32_bf16 v[176:179],a[248:251],v[60:63],v[176:179]
ds_read_b64_tr_b16 a[252:253],v3 offset:57600
v_mul_f32_dpp v108,v15,v108 row_newbcast:4 row_mask:0xf bank_mask:0xf
v_lshlrev_b32_sdwa v50,1,v50 dst_sel:DWORD dst_unused:UNUSED_PAD src0_sel:DWORD src1_sel:WORD_1
v_mul_f32_dpp v109,v15,v109 row_newbcast:5 row_mask:0xf bank_mask:0xf
v_mfma_f32_16x16x32_bf16 v[248:251],a[228:231],v[60:63],v[248:251]
ds_read_u16_d16_hi v50,v50
v_mul_f32_dpp v110,v15,v110 row_newbcast:6 row_mask:0xf bank_mask:0xf
v_lshlrev_b32_sdwa v49,1,v49 dst_sel:DWORD dst_unused:UNUSED_PAD src0_sel:DWORD src1_sel:WORD_1
v_mfma_f32_16x16x32_bf16 v[252:255],a[228:231],v[32:35],v[252:255]
buffer_store_dwordx4 v[120:123],v7,s[40:43],s9 offen offset:64 sc1
v_mul_f32_dpp v111,v15,v111 row_newbcast:7 row_mask:0xf bank_mask:0xf
v_cvt_pk_bf16_f32 v132,v132,v133
v_lshlrev_b32_sdwa v51,1,v51 dst_sel:DWORD dst_unused:UNUSED_PAD src0_sel:DWORD src1_sel:WORD_1
v_mfma_f32_16x16x32_bf16 v[196:199],a[236:239],v[32:35],v[196:199]
ds_read_u16 v49,v49
v_cvt_pk_bf16_f32 v133,v134,v135
v_mfma_f32_16x16x32_bf16 v[192:195],a[236:239],v[60:63],v[192:195]
ds_read_u16_d16_hi v51,v51
v_cvt_pk_bf16_f32 v134,v108,v109
v_cvt_pk_bf16_f32 v135,v110,v111
v_mfma_f32_16x16x32_bf16 v[32:35],a[144:147],a[48:51],v[96:99]
ds_read_b64_tr_b16 a[254:255],v3 offset:59136
v_lshlrev_b32_sdwa v24,1,v24 dst_sel:DWORD dst_unused:UNUSED_PAD src0_sel:DWORD src1_sel:WORD_1
v_mfma_f32_16x16x32_bf16 v[32:35],a[148:151],a[64:67],v[32:35]
ds_read_u16 v24,v24
v_lshlrev_b32_sdwa v26,1,v26 dst_sel:DWORD dst_unused:UNUSED_PAD src0_sel:DWORD src1_sel:WORD_1
v_mfma_f32_16x16x32_bf16 v[32:35],a[152:155],a[80:83],v[32:35]
s_waitcnt lgkmcnt(7)
ds_read_u16_d16_hi v26,v26
v_lshl_add_u64 v[36:37],v[36:37],0,v[38:39]
v_mfma_f32_16x16x32_bf16 v[60:63],a[156:159],a[80:83],v[100:103]
v_lshl_add_u64 v[38:39],v[40:41],0,v[42:43]
v_lshlrev_b32_sdwa v25,1,v25 dst_sel:DWORD dst_unused:UNUSED_PAD src0_sel:DWORD src1_sel:WORD_1
v_mfma_f32_16x16x32_bf16 v[60:63],a[160:163],a[64:67],v[60:63]
ds_read_u16 v25,v25
v_lshl_add_u64 v[40:41],v[44:45],0,v[46:47]
v_mfma_f32_16x16x32_bf16 v[60:63],a[164:167],a[48:51],v[60:63]
v_lshlrev_b32_sdwa v27,1,v27 dst_sel:DWORD dst_unused:UNUSED_PAD src0_sel:DWORD src1_sel:WORD_1
v_lshlrev_b32_sdwa v28,1,v28 dst_sel:DWORD dst_unused:UNUSED_PAD src0_sel:DWORD src1_sel:WORD_1
v_mfma_f32_16x16x32_bf16 v[44:47],a[168:171],a[48:51],v[16:19]
ds_read_u16_d16_hi v27,v27
v_lshlrev_b32_sdwa v30,1,v30 dst_sel:DWORD dst_unused:UNUSED_PAD src0_sel:DWORD src1_sel:WORD_1
v_mfma_f32_16x16x32_bf16 v[44:47],a[172:175],a[64:67],v[44:47]
s_waitcnt lgkmcnt(5)
ds_read_u16 v28,v28
v_lshl_add_u64 v[42:43],v[48:49],0,v[50:51]
v_mfma_f32_16x16x32_bf16 v[44:47],a[176:179],a[80:83],v[44:47]
ds_read_u16_d16_hi v30,v30
v_lshlrev_b32_sdwa v29,1,v29 dst_sel:DWORD dst_unused:UNUSED_PAD src0_sel:DWORD src1_sel:WORD_1
v_mfma_f32_16x16x32_bf16 v[48:51],a[180:183],a[80:83],v[20:23]
ds_read_u16 v29,v29
v_lshlrev_b32_sdwa v31,1,v31 dst_sel:DWORD dst_unused:UNUSED_PAD src0_sel:DWORD src1_sel:WORD_1
v_mfma_f32_16x16x32_bf16 v[48:51],a[184:187],a[64:67],v[48:51]
ds_read_u16_d16_hi v31,v31
v_lshlrev_b32_sdwa v56,1,v56 dst_sel:DWORD dst_unused:UNUSED_PAD src0_sel:DWORD src1_sel:WORD_1
v_mfma_f32_16x16x32_bf16 v[48:51],a[188:191],a[48:51],v[48:51]
ds_read_u16 v56,v56
v_lshlrev_b32_sdwa v58,1,v58 dst_sel:DWORD dst_unused:UNUSED_PAD src0_sel:DWORD src1_sel:WORD_1
v_mfma_f32_16x16x32_bf16 v[208:211],a[232:235],v[36:39],v[208:211]
ds_read_u16_d16_hi v58,v58
v_lshlrev_b32_sdwa v57,1,v57 dst_sel:DWORD dst_unused:UNUSED_PAD src0_sel:DWORD src1_sel:WORD_1
v_mfma_f32_16x16x32_bf16 v[212:215],a[232:235],v[40:43],v[212:215]
buffer_store_dwordx4 v[128:131],v7,s[40:43],s9 offen offset:128 sc1
v_lshlrev_b32_sdwa v59,1,v59 dst_sel:DWORD dst_unused:UNUSED_PAD src0_sel:DWORD src1_sel:WORD_1
v_mfma_f32_16x16x32_bf16 v[172:175],a[240:243],v[40:43],v[172:175]
ds_read_u16 v57,v57
v_lshlrev_b32_sdwa v52,1,v52 dst_sel:DWORD dst_unused:UNUSED_PAD src0_sel:DWORD src1_sel:WORD_1
v_mfma_f32_16x16x32_bf16 v[168:171],a[240:243],v[36:39],v[168:171]
ds_read_u16_d16_hi v59,v59
ds_read_u16 v52,v52
v_mfma_f32_16x16x32_bf16 v[216:219],a[224:227],v[36:39],v[216:219]
ds_read_b64_tr_b16 a[232:233],v3 offset:57608
v_lshlrev_b32_sdwa v54,1,v54 dst_sel:DWORD dst_unused:UNUSED_PAD src0_sel:DWORD src1_sel:WORD_1
v_mfma_f32_16x16x32_bf16 v[220:223],a[224:227],v[40:43],v[220:223]
ds_read_u16_d16_hi v54,v54
v_lshlrev_b32_sdwa v53,1,v53 dst_sel:DWORD dst_unused:UNUSED_PAD src0_sel:DWORD src1_sel:WORD_1
v_mfma_f32_16x16x32_bf16 v[188:191],a[248:251],v[40:43],v[188:191]
ds_read_u16 v53,v53
v_lshlrev_b32_sdwa v55,1,v55 dst_sel:DWORD dst_unused:UNUSED_PAD src0_sel:DWORD src1_sel:WORD_1
v_mfma_f32_16x16x32_bf16 v[184:187],a[248:251],v[36:39],v[184:187]
ds_read_u16_d16_hi v55,v55
v_mfma_f32_16x16x32_bf16 v[224:227],a[228:231],v[36:39],v[224:227]
ds_read_b64_tr_b16 a[234:235],v3 offset:59144
v_mfma_f32_16x16x32_bf16 v[228:231],a[228:231],v[40:43],v[228:231]
ds_read_b64_tr_b16 a[240:241],v3 offset:51448
v_mfma_f32_16x16x32_bf16 v[204:207],a[236:239],v[40:43],v[204:207]
ds_read_b64_tr_b16 a[242:243],v3 offset:52984
v_mfma_f32_16x16x32_bf16 v[200:203],a[236:239],v[36:39],v[200:203]
v_mfma_f32_16x16x32_bf16 v[40:43],a[96:99],a[52:55],v[64:67]
v_lshlrev_b32_sdwa v32,1,v32 dst_sel:DWORD dst_unused:UNUSED_PAD src0_sel:DWORD src1_sel:WORD_1
v_lshlrev_b32_sdwa v34,1,v34 dst_sel:DWORD dst_unused:UNUSED_PAD src0_sel:DWORD src1_sel:WORD_1
v_mfma_f32_16x16x32_bf16 v[40:43],a[100:103],a[68:71],v[40:43]
ds_read_u16 v32,v32
v_lshlrev_b32_sdwa v33,1,v33 dst_sel:DWORD dst_unused:UNUSED_PAD src0_sel:DWORD src1_sel:WORD_1
v_mfma_f32_16x16x32_bf16 v[40:43],a[104:107],a[84:87],v[40:43]
s_waitcnt lgkmcnt(6)
ds_read_u16_d16_hi v34,v34
v_lshl_add_u64 v[24:25],v[24:25],0,v[26:27]
v_mfma_f32_16x16x32_bf16 v[36:39],a[108:111],a[84:87],v[68:71]
ds_read_u16 v33,v33
v_lshl_add_u64 v[26:27],v[28:29],0,v[30:31]
v_mfma_f32_16x16x32_bf16 v[36:39],a[112:115],a[68:71],v[36:39]
v_lshl_add_u64 v[28:29],v[56:57],0,v[58:59]
v_lshlrev_b32_sdwa v35,1,v35 dst_sel:DWORD dst_unused:UNUSED_PAD src0_sel:DWORD src1_sel:WORD_1
v_mfma_f32_16x16x32_bf16 v[36:39],a[116:119],a[52:55],v[36:39]
ds_read_u16_d16_hi v35,v35
v_lshlrev_b32_sdwa v60,1,v60 dst_sel:DWORD dst_unused:UNUSED_PAD src0_sel:DWORD src1_sel:WORD_1
v_mfma_f32_16x16x32_bf16 v[56:59],a[120:123],a[52:55],v[84:87]
ds_read_u16 v60,v60
v_lshlrev_b32_sdwa v62,1,v62 dst_sel:DWORD dst_unused:UNUSED_PAD src0_sel:DWORD src1_sel:WORD_1
v_mfma_f32_16x16x32_bf16 v[56:59],a[124:127],a[68:71],v[56:59]
s_waitcnt lgkmcnt(5)
ds_read_u16_d16_hi v62,v62
v_lshl_add_u64 v[30:31],v[52:53],0,v[54:55]
v_mfma_f32_16x16x32_bf16 v[56:59],a[128:131],a[84:87],v[56:59]
buffer_store_dwordx4 v[116:119],v7,s[40:43],s9 offen offset:192 sc1
v_lshlrev_b32_sdwa v61,1,v61 dst_sel:DWORD dst_unused:UNUSED_PAD src0_sel:DWORD src1_sel:WORD_1
v_mfma_f32_16x16x32_bf16 v[52:55],a[132:135],a[84:87],v[80:83]
ds_read_u16 v61,v61
v_lshlrev_b32_sdwa v63,1,v63 dst_sel:DWORD dst_unused:UNUSED_PAD src0_sel:DWORD src1_sel:WORD_1
v_mfma_f32_16x16x32_bf16 v[52:55],a[136:139],a[68:71],v[52:55]
ds_read_u16_d16_hi v63,v63
v_lshlrev_b32_sdwa v48,1,v48 dst_sel:DWORD dst_unused:UNUSED_PAD src0_sel:DWORD src1_sel:WORD_1
v_mfma_f32_16x16x32_bf16 v[52:55],a[140:143],a[52:55],v[52:55]
ds_read_u16 v48,v48
v_lshlrev_b32_sdwa v50,1,v50 dst_sel:DWORD dst_unused:UNUSED_PAD src0_sel:DWORD src1_sel:WORD_1
v_mfma_f32_16x16x32_bf16 v[232:235],a[0:3],v[24:27],v[232:235]
ds_read_u16_d16_hi v50,v50
v_lshlrev_b32_sdwa v49,1,v49 dst_sel:DWORD dst_unused:UNUSED_PAD src0_sel:DWORD src1_sel:WORD_1
v_mfma_f32_16x16x32_bf16 v[232:235],a[4:7],v[28:31],v[232:235]
ds_read_u16 v49,v49
v_lshlrev_b32_sdwa v51,1,v51 dst_sel:DWORD dst_unused:UNUSED_PAD src0_sel:DWORD src1_sel:WORD_1
v_mfma_f32_16x16x32_bf16 v[160:163],a[8:11],v[28:31],v[160:163]
ds_read_u16_d16_hi v51,v51
v_lshlrev_b32_sdwa v44,1,v44 dst_sel:DWORD dst_unused:UNUSED_PAD src0_sel:DWORD src1_sel:WORD_1
v_mfma_f32_16x16x32_bf16 v[160:163],a[12:15],v[24:27],v[160:163]
ds_read_u16 v44,v44
v_lshlrev_b32_sdwa v46,1,v46 dst_sel:DWORD dst_unused:UNUSED_PAD src0_sel:DWORD src1_sel:WORD_1
v_mfma_f32_16x16x32_bf16 v[240:243],a[16:19],v[24:27],v[240:243]
ds_read_u16_d16_hi v46,v46
v_lshlrev_b32_sdwa v45,1,v45 dst_sel:DWORD dst_unused:UNUSED_PAD src0_sel:DWORD src1_sel:WORD_1
v_mfma_f32_16x16x32_bf16 v[240:243],a[20:23],v[28:31],v[240:243]
ds_read_u16 v45,v45
v_lshlrev_b32_sdwa v47,1,v47 dst_sel:DWORD dst_unused:UNUSED_PAD src0_sel:DWORD src1_sel:WORD_1
v_mfma_f32_16x16x32_bf16 v[176:179],a[24:27],v[28:31],v[176:179]
ds_read_u16_d16_hi v47,v47
v_mfma_f32_16x16x32_bf16 v[176:179],a[28:31],v[24:27],v[176:179]
v_mfma_f32_16x16x32_bf16 v[248:251],a[32:35],v[24:27],v[248:251]
v_mfma_f32_16x16x32_bf16 v[248:251],a[36:39],v[28:31],v[248:251]
v_mfma_f32_16x16x32_bf16 v[192:195],a[40:43],v[28:31],v[192:195]
v_mfma_f32_16x16x32_bf16 v[192:195],a[44:47],v[24:27],v[192:195]
v_mfma_f32_16x16x32_bf16 v[28:31],a[144:147],a[52:55],v[96:99]
v_lshlrev_b32_sdwa v40,1,v40 dst_sel:DWORD dst_unused:UNUSED_PAD src0_sel:DWORD src1_sel:WORD_1
v_lshlrev_b32_sdwa v42,1,v42 dst_sel:DWORD dst_unused:UNUSED_PAD src0_sel:DWORD src1_sel:WORD_1
v_mfma_f32_16x16x32_bf16 v[28:31],a[148:151],a[68:71],v[28:31]
ds_read_u16 v40,v40
v_lshlrev_b32_sdwa v41,1,v41 dst_sel:DWORD dst_unused:UNUSED_PAD src0_sel:DWORD src1_sel:WORD_1
v_mfma_f32_16x16x32_bf16 v[28:31],a[152:155],a[84:87],v[28:31]
s_waitcnt lgkmcnt(2)
ds_read_u16_d16_hi v42,v42
v_lshl_add_u64 v[32:33],v[32:33],0,v[34:35]
v_mfma_f32_16x16x32_bf16 v[24:27],a[156:159],a[84:87],v[100:103]
ds_read_u16 v41,v41
v_lshl_add_u64 v[34:35],v[60:61],0,v[62:63]
v_mfma_f32_16x16x32_bf16 v[24:27],a[160:163],a[68:71],v[24:27]
buffer_store_dwordx4 v[124:127],v7,s[40:43],s9 offen offset:256 sc1
v_lshl_add_u64 v[60:61],v[48:49],0,v[50:51]
v_mfma_f32_16x16x32_bf16 v[24:27],a[164:167],a[52:55],v[24:27]
v_lshlrev_b32_sdwa v43,1,v43 dst_sel:DWORD dst_unused:UNUSED_PAD src0_sel:DWORD src1_sel:WORD_1
v_lshlrev_b32_sdwa v36,1,v36 dst_sel:DWORD dst_unused:UNUSED_PAD src0_sel:DWORD src1_sel:WORD_1
v_mfma_f32_16x16x32_bf16 v[48:51],a[168:171],a[52:55],v[16:19]
ds_read_u16_d16_hi v43,v43
v_lshlrev_b32_sdwa v38,1,v38 dst_sel:DWORD dst_unused:UNUSED_PAD src0_sel:DWORD src1_sel:WORD_1
v_mfma_f32_16x16x32_bf16 v[48:51],a[172:175],a[68:71],v[48:51]
s_waitcnt lgkmcnt(4)
ds_read_u16 v36,v36
v_lshl_add_u64 v[62:63],v[44:45],0,v[46:47]
v_mfma_f32_16x16x32_bf16 v[48:51],a[176:179],a[84:87],v[48:51]
ds_read_u16_d16_hi v38,v38
v_lshlrev_b32_sdwa v37,1,v37 dst_sel:DWORD dst_unused:UNUSED_PAD src0_sel:DWORD src1_sel:WORD_1
v_mfma_f32_16x16x32_bf16 v[44:47],a[180:183],a[84:87],v[20:23]
ds_read_u16 v37,v37
v_lshlrev_b32_sdwa v39,1,v39 dst_sel:DWORD dst_unused:UNUSED_PAD src0_sel:DWORD src1_sel:WORD_1
v_mfma_f32_16x16x32_bf16 v[44:47],a[184:187],a[68:71],v[44:47]
ds_read_u16_d16_hi v39,v39
v_lshlrev_b32_sdwa v52,1,v52 dst_sel:DWORD dst_unused:UNUSED_PAD src0_sel:DWORD src1_sel:WORD_1
v_mfma_f32_16x16x32_bf16 v[44:47],a[188:191],a[52:55],v[44:47]
ds_read_u16 v52,v52
v_lshlrev_b32_sdwa v54,1,v54 dst_sel:DWORD dst_unused:UNUSED_PAD src0_sel:DWORD src1_sel:WORD_1
v_mfma_f32_16x16x32_bf16 v[232:235],a[212:215],v[32:35],v[232:235]
ds_read_u16_d16_hi v54,v54
v_lshlrev_b32_sdwa v53,1,v53 dst_sel:DWORD dst_unused:UNUSED_PAD src0_sel:DWORD src1_sel:WORD_1
v_mfma_f32_16x16x32_bf16 v[232:235],a[200:203],v[60:63],v[232:235]
ds_read_u16 v53,v53
v_lshlrev_b32_sdwa v55,1,v55 dst_sel:DWORD dst_unused:UNUSED_PAD src0_sel:DWORD src1_sel:WORD_1
v_mfma_f32_16x16x32_bf16 v[160:163],a[196:199],v[60:63],v[160:163]
ds_read_u16_d16_hi v55,v55
v_lshlrev_b32_sdwa v56,1,v56 dst_sel:DWORD dst_unused:UNUSED_PAD src0_sel:DWORD src1_sel:WORD_1
v_mfma_f32_16x16x32_bf16 v[160:163],a[192:195],v[32:35],v[160:163]
ds_read_u16 v56,v56
v_lshlrev_b32_sdwa v58,1,v58 dst_sel:DWORD dst_unused:UNUSED_PAD src0_sel:DWORD src1_sel:WORD_1
v_mfma_f32_16x16x32_bf16 v[240:243],a[216:219],v[32:35],v[240:243]
ds_read_u16_d16_hi v58,v58
v_lshlrev_b32_sdwa v57,1,v57 dst_sel:DWORD dst_unused:UNUSED_PAD src0_sel:DWORD src1_sel:WORD_1
v_mfma_f32_16x16x32_bf16 v[240:243],a[220:223],v[60:63],v[240:243]
ds_read_u16 v57,v57
v_lshlrev_b32_sdwa v59,1,v59 dst_sel:DWORD dst_unused:UNUSED_PAD src0_sel:DWORD src1_sel:WORD_1
v_mfma_f32_16x16x32_bf16 v[176:179],a[204:207],v[60:63],v[176:179]
ds_read_u16_d16_hi v59,v59
v_mfma_f32_16x16x32_bf16 v[176:179],a[208:211],v[32:35],v[176:179]
v_mfma_f32_16x16x32_bf16 v[248:251],a[244:247],v[32:35],v[248:251]
v_mfma_f32_16x16x32_bf16 v[248:251],a[252:255],v[60:63],v[248:251]
v_mfma_f32_16x16x32_bf16 v[192:195],a[232:235],v[60:63],v[192:195]
v_mfma_f32_16x16x32_bf16 v[192:195],a[240:243],v[32:35],v[192:195]
buffer_store_dwordx4 v[132:135],v7,s[40:43],s9 offen offset:320 sc1
v_mfma_f32_16x16x32_bf16 v[60:63],a[96:99],a[56:59],v[64:67]
v_lshlrev_b32_sdwa v28,1,v28 dst_sel:DWORD dst_unused:UNUSED_PAD src0_sel:DWORD src1_sel:WORD_1
v_mul_f32_dpp v232,v14,v232 row_newbcast:0 row_mask:0xf bank_mask:0xf
s_add_u32 s10,s61,4718592
v_mfma_f32_16x16x32_bf16 v[60:63],a[100:103],a[72:75],v[60:63]
ds_read_u16 v28,v28
v_lshlrev_b32_sdwa v30,1,v30 dst_sel:DWORD dst_unused:UNUSED_PAD src0_sel:DWORD src1_sel:WORD_1
v_mfma_f32_16x16x32_bf16 v[60:63],a[104:107],a[88:91],v[60:63]
s_waitcnt lgkmcnt(2)
ds_read_u16_d16_hi v30,v30
v_lshl_add_u64 v[40:41],v[40:41],0,v[42:43]
v_mul_f32_dpp v233,v14,v233 row_newbcast:1 row_mask:0xf bank_mask:0xf
v_mfma_f32_16x16x32_bf16 v[32:35],a[108:111],a[88:91],v[68:71]
v_lshl_add_u64 v[42:43],v[36:37],0,v[38:39]
v_lshlrev_b32_sdwa v29,1,v29 dst_sel:DWORD dst_unused:UNUSED_PAD src0_sel:DWORD src1_sel:WORD_1
v_mul_f32_dpp v234,v14,v234 row_newbcast:2 row_mask:0xf bank_mask:0xf
v_mfma_f32_16x16x32_bf16 v[32:35],a[112:115],a[72:75],v[32:35]
ds_read_u16 v29,v29
v_lshl_add_u64 v[36:37],v[52:53],0,v[54:55]
v_mfma_f32_16x16x32_bf16 v[32:35],a[116:119],a[56:59],v[32:35]
v_lshlrev_b32_sdwa v31,1,v31 dst_sel:DWORD dst_unused:UNUSED_PAD src0_sel:DWORD src1_sel:WORD_1
v_mul_f32_dpp v235,v14,v235 row_newbcast:3 row_mask:0xf bank_mask:0xf
v_mfma_f32_16x16x32_bf16 v[52:55],a[120:123],a[56:59],v[84:87]
ds_read_u16_d16_hi v31,v31
v_lshlrev_b32_sdwa v24,1,v24 dst_sel:DWORD dst_unused:UNUSED_PAD src0_sel:DWORD src1_sel:WORD_1
v_mul_f32_dpp v160,v14,v160 row_newbcast:4 row_mask:0xf bank_mask:0xf
v_mfma_f32_16x16x32_bf16 v[52:55],a[124:127],a[72:75],v[52:55]
s_waitcnt lgkmcnt(4)
ds_read_u16 v24,v24
v_lshl_add_u64 v[38:39],v[56:57],0,v[58:59]
v_mul_f32_dpp v161,v14,v161 row_newbcast:5 row_mask:0xf bank_mask:0xf
v_lshlrev_b32_sdwa v26,1,v26 dst_sel:DWORD dst_unused:UNUSED_PAD src0_sel:DWORD src1_sel:WORD_1
v_mfma_f32_16x16x32_bf16 v[52:55],a[128:131],a[88:91],v[52:55]
ds_read_u16_d16_hi v26,v26
v_mul_f32_dpp v162,v14,v162 row_newbcast:6 row_mask:0xf bank_mask:0xf
v_mfma_f32_16x16x32_bf16 v[56:59],a[132:135],a[88:91],v[80:83]
v_lshlrev_b32_sdwa v25,1,v25 dst_sel:DWORD dst_unused:UNUSED_PAD src0_sel:DWORD src1_sel:WORD_1
v_mul_f32_dpp v163,v14,v163 row_newbcast:7 row_mask:0xf bank_mask:0xf
v_mfma_f32_16x16x32_bf16 v[56:59],a[136:139],a[72:75],v[56:59]
ds_read_u16 v25,v25
v_lshlrev_b32_sdwa v27,1,v27 dst_sel:DWORD dst_unused:UNUSED_PAD src0_sel:DWORD src1_sel:WORD_1
v_mfma_f32_16x16x32_bf16 v[56:59],a[140:143],a[56:59],v[56:59]
ds_read_u16_d16_hi v27,v27
v_cvt_pk_bf16_f32 v232,v232,v233
v_mfma_f32_16x16x32_bf16 v[236:239],a[0:3],v[40:43],v[236:239]
v_lshlrev_b32_sdwa v44,1,v44 dst_sel:DWORD dst_unused:UNUSED_PAD src0_sel:DWORD src1_sel:WORD_1
v_cvt_pk_bf16_f32 v233,v234,v235
v_mfma_f32_16x16x32_bf16 v[236:239],a[4:7],v[36:39],v[236:239]
ds_read_u16 v44,v44
v_lshlrev_b32_sdwa v46,1,v46 dst_sel:DWORD dst_unused:UNUSED_PAD src0_sel:DWORD src1_sel:WORD_1
v_mfma_f32_16x16x32_bf16 v[164:167],a[8:11],v[36:39],v[164:167]
ds_read_u16_d16_hi v46,v46
v_cvt_pk_bf16_f32 v234,v160,v161
v_lshlrev_b32_sdwa v45,1,v45 dst_sel:DWORD dst_unused:UNUSED_PAD src0_sel:DWORD src1_sel:WORD_1
v_mfma_f32_16x16x32_bf16 v[164:167],a[12:15],v[40:43],v[164:167]
ds_read_u16 v45,v45
v_cvt_pk_bf16_f32 v235,v162,v163
v_mfma_f32_16x16x32_bf16 v[244:247],a[16:19],v[40:43],v[244:247]
buffer_store_dwordx4 v[232:235],v7,s[40:43],s10 offen offset:0 sc1
v_lshlrev_b32_sdwa v47,1,v47 dst_sel:DWORD dst_unused:UNUSED_PAD src0_sel:DWORD src1_sel:WORD_1
v_mfma_f32_16x16x32_bf16 v[244:247],a[20:23],v[36:39],v[244:247]
ds_read_u16_d16_hi v47,v47
v_mul_f32_dpp v240,v14,v240 row_newbcast:8 row_mask:0xf bank_mask:0xf
v_lshlrev_b32_sdwa v48,1,v48 dst_sel:DWORD dst_unused:UNUSED_PAD src0_sel:DWORD src1_sel:WORD_1
v_mfma_f32_16x16x32_bf16 v[180:183],a[24:27],v[36:39],v[180:183]
ds_read_u16 v48,v48
v_mul_f32_dpp v241,v14,v241 row_newbcast:9 row_mask:0xf bank_mask:0xf
v_mfma_f32_16x16x32_bf16 v[180:183],a[28:31],v[40:43],v[180:183]
v_lshlrev_b32_sdwa v50,1,v50 dst_sel:DWORD dst_unused:UNUSED_PAD src0_sel:DWORD src1_sel:WORD_1
v_mul_f32_dpp v242,v14,v242 row_newbcast:10 row_mask:0xf bank_mask:0xf
v_mfma_f32_16x16x32_bf16 v[252:255],a[32:35],v[40:43],v[252:255]
ds_read_u16_d16_hi v50,v50
v_lshlrev_b32_sdwa v49,1,v49 dst_sel:DWORD dst_unused:UNUSED_PAD src0_sel:DWORD src1_sel:WORD_1
v_mfma_f32_16x16x32_bf16 v[252:255],a[36:39],v[36:39],v[252:255]
ds_read_u16 v49,v49
v_mul_f32_dpp v243,v14,v243 row_newbcast:11 row_mask:0xf bank_mask:0xf
v_lshlrev_b32_sdwa v51,1,v51 dst_sel:DWORD dst_unused:UNUSED_PAD src0_sel:DWORD src1_sel:WORD_1
v_mfma_f32_16x16x32_bf16 v[196:199],a[40:43],v[36:39],v[196:199]
ds_read_u16_d16_hi v51,v51
v_mul_f32_dpp v176,v14,v176 row_newbcast:12 row_mask:0xf bank_mask:0xf
v_mfma_f32_16x16x32_bf16 v[196:199],a[44:47],v[40:43],v[196:199]
v_mul_f32_dpp v177,v14,v177 row_newbcast:13 row_mask:0xf bank_mask:0xf
v_mfma_f32_16x16x32_bf16 v[36:39],a[144:147],a[56:59],v[96:99]
v_lshlrev_b32_sdwa v60,1,v60 dst_sel:DWORD dst_unused:UNUSED_PAD src0_sel:DWORD src1_sel:WORD_1
v_mul_f32_dpp v178,v14,v178 row_newbcast:14 row_mask:0xf bank_mask:0xf
v_mfma_f32_16x16x32_bf16 v[36:39],a[148:151],a[72:75],v[36:39]
ds_read_u16 v60,v60
v_lshlrev_b32_sdwa v62,1,v62 dst_sel:DWORD dst_unused:UNUSED_PAD src0_sel:DWORD src1_sel:WORD_1
v_mfma_f32_16x16x32_bf16 v[36:39],a[152:155],a[88:91],v[36:39]
s_waitcnt lgkmcnt(5)
ds_read_u16_d16_hi v62,v62
v_lshl_add_u64 v[28:29],v[28:29],0,v[30:31]
v_mul_f32_dpp v179,v14,v179 row_newbcast:15 row_mask:0xf bank_mask:0xf
v_mfma_f32_16x16x32_bf16 v[40:43],a[156:159],a[88:91],v[100:103]
v_lshl_add_u64 v[30:31],v[24:25],0,v[26:27]
v_lshlrev_b32_sdwa v61,1,v61 dst_sel:DWORD dst_unused:UNUSED_PAD src0_sel:DWORD src1_sel:WORD_1
v_cvt_pk_bf16_f32 v240,v240,v241
v_mfma_f32_16x16x32_bf16 v[40:43],a[160:163],a[72:75],v[40:43]
ds_read_u16 v61,v61
v_lshl_add_u64 v[24:25],v[44:45],0,v[46:47]
v_mfma_f32_16x16x32_bf16 v[40:43],a[164:167],a[56:59],v[40:43]
v_lshlrev_b32_sdwa v63,1,v63 dst_sel:DWORD dst_unused:UNUSED_PAD src0_sel:DWORD src1_sel:WORD_1
v_cvt_pk_bf16_f32 v241,v242,v243
v_mfma_f32_16x16x32_bf16 v[44:47],a[168:171],a[56:59],v[16:19]
ds_read_u16_d16_hi v63,v63
v_lshlrev_b32_sdwa v32,1,v32 dst_sel:DWORD dst_unused:UNUSED_PAD src0_sel:DWORD src1_sel:WORD_1
TP10720_%=:
s_cbranch_execz TR1_%=
v_cvt_pk_bf16_f32 v242,v176,v177
v_mfma_f32_16x16x32_bf16 v[44:47],a[172:175],a[72:75],v[44:47]
s_waitcnt lgkmcnt(4)
ds_read_u16 v32,v32
v_lshl_add_u64 v[26:27],v[48:49],0,v[50:51]
v_cvt_pk_bf16_f32 v243,v178,v179
v_lshlrev_b32_sdwa v34,1,v34 dst_sel:DWORD dst_unused:UNUSED_PAD src0_sel:DWORD src1_sel:WORD_1
v_mfma_f32_16x16x32_bf16 v[44:47],a[176:179],a[88:91],v[44:47]
buffer_store_dwordx4 v[240:243],v7,s[40:43],s10 offen offset:64 sc1
v_mul_f32_dpp v248,v15,v248 row_newbcast:0 row_mask:0xf bank_mask:0xf
v_mfma_f32_16x16x32_bf16 v[48:51],a[180:183],a[88:91],v[20:23]
ds_read_u16_d16_hi v34,v34
v_lshlrev_b32_sdwa v33,1,v33 dst_sel:DWORD dst_unused:UNUSED_PAD src0_sel:DWORD src1_sel:WORD_1
v_mfma_f32_16x16x32_bf16 v[48:51],a[184:187],a[72:75],v[48:51]
ds_read_u16 v33,v33
v_mul_f32_dpp v249,v15,v249 row_newbcast:1 row_mask:0xf bank_mask:0xf
v_lshlrev_b32_sdwa v35,1,v35 dst_sel:DWORD dst_unused:UNUSED_PAD src0_sel:DWORD src1_sel:WORD_1
v_mfma_f32_16x16x32_bf16 v[48:51],a[188:191],a[56:59],v[48:51]
ds_read_u16_d16_hi v35,v35
v_mul_f32_dpp v250,v15,v250 row_newbcast:2 row_mask:0xf bank_mask:0xf
v_mfma_f32_16x16x32_bf16 v[236:239],a[212:215],v[28:31],v[236:239]
v_lshlrev_b32_sdwa v56,1,v56 dst_sel:DWORD dst_unused:UNUSED_PAD src0_sel:DWORD src1_sel:WORD_1
v_mul_f32_dpp v251,v15,v251 row_newbcast:3 row_mask:0xf bank_mask:0xf
v_mfma_f32_16x16x32_bf16 v[236:239],a[200:203],v[24:27],v[236:239]
ds_read_u16 v56,v56
v_lshlrev_b32_sdwa v58,1,v58 dst_sel:DWORD dst_unused:UNUSED_PAD src0_sel:DWORD src1_sel:WORD_1
v_mfma_f32_16x16x32_bf16 v[164:167],a[196:199],v[24:27],v[164:167]
ds_read_u16_d16_hi v58,v58
v_mul_f32_dpp v192,v15,v192 row_newbcast:4 row_mask:0xf bank_mask:0xf
v_lshlrev_b32_sdwa v57,1,v57 dst_sel:DWORD dst_unused:UNUSED_PAD src0_sel:DWORD src1_sel:WORD_1
v_mfma_f32_16x16x32_bf16 v[164:167],a[192:195],v[28:31],v[164:167]
ds_read_u16 v57,v57
v_mul_f32_dpp v193,v15,v193 row_newbcast:5 row_mask:0xf bank_mask:0xf
v_mfma_f32_16x16x32_bf16 v[244:247],a[216:219],v[28:31],v[244:247]
v_lshlrev_b32_sdwa v59,1,v59 dst_sel:DWORD dst_unused:UNUSED_PAD src0_sel:DWORD src1_sel:WORD_1
v_mul_f32_dpp v194,v15,v194 row_newbcast:6 row_mask:0xf bank_mask:0xf
v_mfma_f32_16x16x32_bf16 v[244:247],a[220:223],v[24:27],v[244:247]
ds_read_u16_d16_hi v59,v59
v_lshlrev_b32_sdwa v52,1,v52 dst_sel:DWORD dst_unused:UNUSED_PAD src0_sel:DWORD src1_sel:WORD_1
v_mfma_f32_16x16x32_bf16 v[180:183],a[204:207],v[24:27],v[180:183]
ds_read_u16 v52,v52
v_mul_f32_dpp v195,v15,v195 row_newbcast:7 row_mask:0xf bank_mask:0xf
v_mfma_f32_16x16x32_bf16 v[180:183],a[208:211],v[28:31],v[180:183]
v_lshlrev_b32_sdwa v54,1,v54 dst_sel:DWORD dst_unused:UNUSED_PAD src0_sel:DWORD src1_sel:WORD_1
v_cvt_pk_bf16_f32 v248,v248,v249
v_mfma_f32_16x16x32_bf16 v[252:255],a[244:247],v[28:31],v[252:255]
ds_read_u16_d16_hi v54,v54
v_lshlrev_b32_sdwa v53,1,v53 dst_sel:DWORD dst_unused:UNUSED_PAD src0_sel:DWORD src1_sel:WORD_1
v_mfma_f32_16x16x32_bf16 v[252:255],a[252:255],v[24:27],v[252:255]
ds_read_u16 v53,v53
v_cvt_pk_bf16_f32 v249,v250,v251
v_lshlrev_b32_sdwa v55,1,v55 dst_sel:DWORD dst_unused:UNUSED_PAD src0_sel:DWORD src1_sel:WORD_1
v_mfma_f32_16x16x32_bf16 v[196:199],a[232:235],v[24:27],v[196:199]
ds_read_u16_d16_hi v55,v55
v_cvt_pk_bf16_f32 v250,v192,v193
v_mfma_f32_16x16x32_bf16 v[196:199],a[240:243],v[28:31],v[196:199]
v_cvt_pk_bf16_f32 v251,v194,v195
v_mfma_f32_16x16x32_bf16 v[24:27],a[96:99],a[60:63],v[64:67]
buffer_store_dwordx4 v[248:251],v7,s[40:43],s10 offen offset:128 sc1
v_lshlrev_b32_sdwa v36,1,v36 dst_sel:DWORD dst_unused:UNUSED_PAD src0_sel:DWORD src1_sel:WORD_1
v_mfma_f32_16x16x32_bf16 v[24:27],a[100:103],a[76:79],v[24:27]
ds_read_u16 v36,v36
v_mul_f32_dpp v236,v14,v236 row_newbcast:0 row_mask:0xf bank_mask:0xf
v_mfma_f32_16x16x32_bf16 v[24:27],a[104:107],a[92:95],v[24:27]
s_waitcnt lgkmcnt(5)
v_lshl_add_u64 v[60:61],v[60:61],0,v[62:63]
v_lshlrev_b32_sdwa v38,1,v38 dst_sel:DWORD dst_unused:UNUSED_PAD src0_sel:DWORD src1_sel:WORD_1
v_mul_f32_dpp v237,v14,v237 row_newbcast:1 row_mask:0xf bank_mask:0xf
v_mfma_f32_16x16x32_bf16 v[28:31],a[108:111],a[92:95],v[68:71]
ds_read_u16_d16_hi v38,v38
v_lshl_add_u64 v[62:63],v[32:33],0,v[34:35]
v_lshlrev_b32_sdwa v37,1,v37 dst_sel:DWORD dst_unused:UNUSED_PAD src0_sel:DWORD src1_sel:WORD_1
v_mul_f32_dpp v238,v14,v238 row_newbcast:2 row_mask:0xf bank_mask:0xf
v_mfma_f32_16x16x32_bf16 v[28:31],a[112:115],a[76:79],v[28:31]
ds_read_u16 v37,v37
v_lshl_add_u64 v[32:33],v[56:57],0,v[58:59]
v_mfma_f32_16x16x32_bf16 v[28:31],a[116:119],a[60:63],v[28:31]
v_lshlrev_b32_sdwa v39,1,v39 dst_sel:DWORD dst_unused:UNUSED_PAD src0_sel:DWORD src1_sel:WORD_1
v_mul_f32_dpp v239,v14,v239 row_newbcast:3 row_mask:0xf bank_mask:0xf
v_mfma_f32_16x16x32_bf16 v[56:59],a[120:123],a[60:63],v[84:87]
s_nop 0
ds_read_u16_d16_hi v39,v39
v_lshlrev_b32_sdwa v40,1,v40 dst_sel:DWORD dst_unused:UNUSED_PAD src0_sel:DWORD src1_sel:WORD_1
v_mul_f32_dpp v164,v14,v164 row_newbcast:4 row_mask:0xf bank_mask:0xf
v_mfma_f32_16x16x32_bf16 v[56:59],a[124:127],a[76:79],v[56:59]
s_waitcnt lgkmcnt(4)
ds_read_u16 v40,v40
v_lshl_add_u64 v[34:35],v[52:53],0,v[54:55]
v_mul_f32_dpp v165,v14,v165 row_newbcast:5 row_mask:0xf bank_mask:0xf
v_lshlrev_b32_sdwa v42,1,v42 dst_sel:DWORD dst_unused:UNUSED_PAD src0_sel:DWORD src1_sel:WORD_1
v_mfma_f32_16x16x32_bf16 v[56:59],a[128:131],a[92:95],v[56:59]
ds_read_u16_d16_hi v42,v42
v_mul_f32_dpp v166,v14,v166 row_newbcast:6 row_mask:0xf bank_mask:0xf
v_mfma_f32_16x16x32_bf16 v[52:55],a[132:135],a[92:95],v[80:83]
v_lshlrev_b32_sdwa v41,1,v41 dst_sel:DWORD dst_unused:UNUSED_PAD src0_sel:DWORD src1_sel:WORD_1
v_mul_f32_dpp v167,v14,v167 row_newbcast:7 row_mask:0xf bank_mask:0xf
v_mfma_f32_16x16x32_bf16 v[52:55],a[136:139],a[76:79],v[52:55]
ds_read_u16 v41,v41
v_lshlrev_b32_sdwa v43,1,v43 dst_sel:DWORD dst_unused:UNUSED_PAD src0_sel:DWORD src1_sel:WORD_1
v_mfma_f32_16x16x32_bf16 v[52:55],a[140:143],a[60:63],v[52:55]
ds_read_u16_d16_hi v43,v43
v_cvt_pk_bf16_f32 v236,v236,v237
v_mfma_f32_16x16x32_bf16 v[208:211],a[0:3],v[60:63],v[208:211]
v_lshlrev_b32_sdwa v48,1,v48 dst_sel:DWORD dst_unused:UNUSED_PAD src0_sel:DWORD src1_sel:WORD_1
v_cvt_pk_bf16_f32 v237,v238,v239
v_mfma_f32_16x16x32_bf16 v[208:211],a[4:7],v[32:35],v[208:211]
ds_read_u16 v48,v48
v_lshlrev_b32_sdwa v50,1,v50 dst_sel:DWORD dst_unused:UNUSED_PAD src0_sel:DWORD src1_sel:WORD_1
v_mfma_f32_16x16x32_bf16 v[168:171],a[8:11],v[32:35],v[168:171]
ds_read_u16_d16_hi v50,v50
v_cvt_pk_bf16_f32 v238,v164,v165
v_lshlrev_b32_sdwa v49,1,v49 dst_sel:DWORD dst_unused:UNUSED_PAD src0_sel:DWORD src1_sel:WORD_1
v_mfma_f32_16x16x32_bf16 v[168:171],a[12:15],v[60:63],v[168:171]
ds_read_u16 v49,v49
v_cvt_pk_bf16_f32 v239,v166,v167
v_mfma_f32_16x16x32_bf16 v[216:219],a[16:19],v[60:63],v[216:219]
buffer_store_dwordx4 v[236:239],v7,s[40:43],s10 offen offset:192 sc1
v_lshlrev_b32_sdwa v51,1,v51 dst_sel:DWORD dst_unused:UNUSED_PAD src0_sel:DWORD src1_sel:WORD_1
v_mfma_f32_16x16x32_bf16 v[216:219],a[20:23],v[32:35],v[216:219]
ds_read_u16_d16_hi v51,v51
v_mul_f32_dpp v244,v14,v244 row_newbcast:8 row_mask:0xf bank_mask:0xf
v_lshlrev_b32_sdwa v44,1,v44 dst_sel:DWORD dst_unused:UNUSED_PAD src0_sel:DWORD src1_sel:WORD_1
v_mfma_f32_16x16x32_bf16 v[184:187],a[24:27],v[32:35],v[184:187]
ds_read_u16 v44,v44
v_mul_f32_dpp v245,v14,v245 row_newbcast:9 row_mask:0xf bank_mask:0xf
v_mfma_f32_16x16x32_bf16 v[184:187],a[28:31],v[60:63],v[184:187]
v_lshlrev_b32_sdwa v46,1,v46 dst_sel:DWORD dst_unused:UNUSED_PAD src0_sel:DWORD src1_sel:WORD_1
v_mul_f32_dpp v246,v14,v246 row_newbcast:10 row_mask:0xf bank_mask:0xf
v_mfma_f32_16x16x32_bf16 v[224:227],a[32:35],v[60:63],v[224:227]
ds_read_u16_d16_hi v46,v46
v_lshlrev_b32_sdwa v45,1,v45 dst_sel:DWORD dst_unused:UNUSED_PAD src0_sel:DWORD src1_sel:WORD_1
v_mfma_f32_16x16x32_bf16 v[224:227],a[36:39],v[32:35],v[224:227]
ds_read_u16 v45,v45
v_mul_f32_dpp v247,v14,v247 row_newbcast:11 row_mask:0xf bank_mask:0xf
v_lshlrev_b32_sdwa v47,1,v47 dst_sel:DWORD dst_unused:UNUSED_PAD src0_sel:DWORD src1_sel:WORD_1
v_mfma_f32_16x16x32_bf16 v[200:203],a[40:43],v[32:35],v[200:203]
ds_read_u16_d16_hi v47,v47
v_mul_f32_dpp v180,v14,v180 row_newbcast:12 row_mask:0xf bank_mask:0xf
v_mfma_f32_16x16x32_bf16 v[200:203],a[44:47],v[60:63],v[200:203]
v_mul_f32_dpp v181,v14,v181 row_newbcast:13 row_mask:0xf bank_mask:0xf
v_mov_b32 v10,0
v_mfma_f32_16x16x32_bf16 v[32:35],a[144:147],a[60:63],v[96:99]
ds_read_b128 v[72:75],v10 offset:11648
v_lshlrev_b32_sdwa v24,1,v24 dst_sel:DWORD dst_unused:UNUSED_PAD src0_sel:DWORD src1_sel:WORD_1
v_mfma_f32_16x16x32_bf16 v[32:35],a[148:151],a[76:79],v[32:35]
ds_read_u16 v24,v24
v_mul_f32_dpp v182,v14,v182 row_newbcast:14 row_mask:0xf bank_mask:0xf
v_mfma_f32_16x16x32_bf16 v[32:35],a[152:155],a[92:95],v[32:35]
s_waitcnt lgkmcnt(6)
v_lshl_add_u64 v[36:37],v[36:37],0,v[38:39]
v_lshlrev_b32_sdwa v26,1,v26 dst_sel:DWORD dst_unused:UNUSED_PAD src0_sel:DWORD src1_sel:WORD_1
v_mul_f32_dpp v183,v14,v183 row_newbcast:15 row_mask:0xf bank_mask:0xf
v_mfma_f32_16x16x32_bf16 v[60:63],a[156:159],a[92:95],v[100:103]
ds_read_u16_d16_hi v26,v26
v_lshl_add_u64 v[38:39],v[40:41],0,v[42:43]
v_lshlrev_b32_sdwa v25,1,v25 dst_sel:DWORD dst_unused:UNUSED_PAD src0_sel:DWORD src1_sel:WORD_1
v_cvt_pk_bf16_f32 v244,v244,v245
v_mfma_f32_16x16x32_bf16 v[60:63],a[160:163],a[76:79],v[60:63]
ds_read_u16 v25,v25
v_lshl_add_u64 v[40:41],v[48:49],0,v[50:51]
v_mfma_f32_16x16x32_bf16 v[60:63],a[164:167],a[60:63],v[60:63]
v_lshlrev_b32_sdwa v27,1,v27 dst_sel:DWORD dst_unused:UNUSED_PAD src0_sel:DWORD src1_sel:WORD_1
v_cvt_pk_bf16_f32 v245,v246,v247
v_mfma_f32_16x16x32_bf16 v[48:51],a[168:171],a[60:63],v[16:19]
s_nop 0
ds_read_u16_d16_hi v27,v27
v_lshlrev_b32_sdwa v28,1,v28 dst_sel:DWORD dst_unused:UNUSED_PAD src0_sel:DWORD src1_sel:WORD_1
v_cvt_pk_bf16_f32 v246,v180,v181
v_mfma_f32_16x16x32_bf16 v[48:51],a[172:175],a[76:79],v[48:51]
s_waitcnt lgkmcnt(5)
ds_read_u16 v28,v28
v_lshl_add_u64 v[42:43],v[44:45],0,v[46:47]
v_cvt_pk_bf16_f32 v247,v182,v183
v_lshlrev_b32_sdwa v30,1,v30 dst_sel:DWORD dst_unused:UNUSED_PAD src0_sel:DWORD src1_sel:WORD_1
v_mfma_f32_16x16x32_bf16 v[48:51],a[176:179],a[92:95],v[48:51]
buffer_store_dwordx4 v[244:247],v7,s[40:43],s10 offen offset:256 sc1
v_mul_f32_dpp v252,v15,v252 row_newbcast:0 row_mask:0xf bank_mask:0xf
v_mfma_f32_16x16x32_bf16 v[44:47],a[180:183],a[92:95],v[20:23]
ds_read_u16_d16_hi v30,v30
v_lshlrev_b32_sdwa v29,1,v29 dst_sel:DWORD dst_unused:UNUSED_PAD src0_sel:DWORD src1_sel:WORD_1
v_mfma_f32_16x16x32_bf16 v[44:47],a[184:187],a[76:79],v[44:47]
ds_read_u16 v29,v29
v_mul_f32_dpp v253,v15,v253 row_newbcast:1 row_mask:0xf bank_mask:0xf
v_lshlrev_b32_sdwa v31,1,v31 dst_sel:DWORD dst_unused:UNUSED_PAD src0_sel:DWORD src1_sel:WORD_1
v_mfma_f32_16x16x32_bf16 v[44:47],a[188:191],a[60:63],v[44:47]
ds_read_u16_d16_hi v31,v31
v_mul_f32_dpp v254,v15,v254 row_newbcast:2 row_mask:0xf bank_mask:0xf
v_mfma_f32_16x16x32_bf16 v[208:211],a[212:215],v[36:39],v[208:211]
v_lshlrev_b32_sdwa v52,1,v52 dst_sel:DWORD dst_unused:UNUSED_PAD src0_sel:DWORD src1_sel:WORD_1
v_mul_f32_dpp v255,v15,v255 row_newbcast:3 row_mask:0xf bank_mask:0xf
v_mfma_f32_16x16x32_bf16 v[208:211],a[200:203],v[40:43],v[208:211]
ds_read_u16 v52,v52
v_lshlrev_b32_sdwa v54,1,v54 dst_sel:DWORD dst_unused:UNUSED_PAD src0_sel:DWORD src1_sel:WORD_1
v_mfma_f32_16x16x32_bf16 v[168:171],a[196:199],v[40:43],v[168:171]
ds_read_u16_d16_hi v54,v54
v_mul_f32_dpp v196,v15,v196 row_newbcast:4 row_mask:0xf bank_mask:0xf
v_lshlrev_b32_sdwa v53,1,v53 dst_sel:DWORD dst_unused:UNUSED_PAD src0_sel:DWORD src1_sel:WORD_1
v_mfma_f32_16x16x32_bf16 v[168:171],a[192:195],v[36:39],v[168:171]
ds_read_u16 v53,v53
v_mul_f32_dpp v197,v15,v197 row_newbcast:5 row_mask:0xf bank_mask:0xf
v_mfma_f32_16x16x32_bf16 v[216:219],a[216:219],v[36:39],v[216:219]
v_lshlrev_b32_sdwa v55,1,v55 dst_sel:DWORD dst_unused:UNUSED_PAD src0_sel:DWORD src1_sel:WORD_1
v_mul_f32_dpp v198,v15,v198 row_newbcast:6 row_mask:0xf bank_mask:0xf
v_mfma_f32_16x16x32_bf16 v[216:219],a[220:223],v[40:43],v[216:219]
ds_read_u16_d16_hi v55,v55
v_lshlrev_b32_sdwa v56,1,v56 dst_sel:DWORD dst_unused:UNUSED_PAD src0_sel:DWORD src1_sel:WORD_1
v_mfma_f32_16x16x32_bf16 v[184:187],a[204:207],v[40:43],v[184:187]
ds_read_u16 v56,v56
v_mul_f32_dpp v199,v15,v199 row_newbcast:7 row_mask:0xf bank_mask:0xf
v_mfma_f32_16x16x32_bf16 v[184:187],a[208:211],v[36:39],v[184:187]
v_lshlrev_b32_sdwa v58,1,v58 dst_sel:DWORD dst_unused:UNUSED_PAD src0_sel:DWORD src1_sel:WORD_1
v_cvt_pk_bf16_f32 v252,v252,v253
v_mfma_f32_16x16x32_bf16 v[224:227],a[244:247],v[36:39],v[224:227]
ds_read_u16_d16_hi v58,v58
v_lshlrev_b32_sdwa v57,1,v57 dst_sel:DWORD dst_unused:UNUSED_PAD src0_sel:DWORD src1_sel:WORD_1
v_mfma_f32_16x16x32_bf16 v[224:227],a[252:255],v[40:43],v[224:227]
ds_read_u16 v57,v57
v_cvt_pk_bf16_f32 v253,v254,v255
v_lshlrev_b32_sdwa v59,1,v59 dst_sel:DWORD dst_unused:UNUSED_PAD src0_sel:DWORD src1_sel:WORD_1
v_mfma_f32_16x16x32_bf16 v[200:203],a[232:235],v[40:43],v[200:203]
ds_read_u16_d16_hi v59,v59
v_cvt_pk_bf16_f32 v254,v196,v197
v_mfma_f32_16x16x32_bf16 v[200:203],a[240:243],v[36:39],v[200:203]
v_cvt_pk_bf16_f32 v255,v198,v199
s_waitcnt lgkmcnt(12)
v_lshl_add_u64 v[24:25],v[24:25],0,v[26:27]
s_waitcnt lgkmcnt(8)
v_lshl_add_u64 v[26:27],v[28:29],0,v[30:31]
s_waitcnt lgkmcnt(4)
v_lshl_add_u64 v[28:29],v[52:53],0,v[54:55]
s_waitcnt lgkmcnt(0)
v_lshl_add_u64 v[30:31],v[56:57],0,v[58:59]
v_mfma_f32_16x16x32_bf16 v[212:215],a[0:3],v[24:27],v[212:215]
buffer_store_dwordx4 v[252:255],v7,s[40:43],s10 offen offset:320 sc1
v_mul_f32_dpp v208,v14,v208 row_newbcast:0 row_mask:0xf bank_mask:0xf
s_add_u32 s11,s61,4816896
v_mul_f32_dpp v209,v14,v209 row_newbcast:1 row_mask:0xf bank_mask:0xf
v_mul_f32_dpp v210,v14,v210 row_newbcast:2 row_mask:0xf bank_mask:0xf
v_lshlrev_b32_sdwa v32,1,v32 dst_sel:DWORD dst_unused:UNUSED_PAD src0_sel:DWORD src1_sel:WORD_1
v_mfma_f32_16x16x32_bf16 v[212:215],a[4:7],v[28:31],v[212:215]
ds_read_u16 v32,v32
v_mul_f32_dpp v211,v14,v211 row_newbcast:3 row_mask:0xf bank_mask:0xf
v_mul_f32_dpp v168,v14,v168 row_newbcast:4 row_mask:0xf bank_mask:0xf
v_lshlrev_b32_sdwa v34,1,v34 dst_sel:DWORD dst_unused:UNUSED_PAD src0_sel:DWORD src1_sel:WORD_1
v_mul_f32_dpp v169,v14,v169 row_newbcast:5 row_mask:0xf bank_mask:0xf
v_mfma_f32_16x16x32_bf16 v[172:175],a[8:11],v[28:31],v[172:175]
ds_read_u16_d16_hi v34,v34
v_mul_f32_dpp v170,v14,v170 row_newbcast:6 row_mask:0xf bank_mask:0xf
v_mul_f32_dpp v171,v14,v171 row_newbcast:7 row_mask:0xf bank_mask:0xf
v_lshlrev_b32_sdwa v33,1,v33 dst_sel:DWORD dst_unused:UNUSED_PAD src0_sel:DWORD src1_sel:WORD_1
v_cvt_pk_bf16_f32 v208,v208,v209
v_mfma_f32_16x16x32_bf16 v[172:175],a[12:15],v[24:27],v[172:175]
ds_read_u16 v33,v33
v_cvt_pk_bf16_f32 v209,v210,v211
v_lshlrev_b32_sdwa v35,1,v35 dst_sel:DWORD dst_unused:UNUSED_PAD src0_sel:DWORD src1_sel:WORD_1
v_cvt_pk_bf16_f32 v210,v168,v169
v_lshlrev_b32_sdwa v60,1,v60 dst_sel:DWORD dst_unused:UNUSED_PAD src0_sel:DWORD src1_sel:WORD_1
v_cvt_pk_bf16_f32 v211,v170,v171
v_mfma_f32_16x16x32_bf16 v[220:223],a[16:19],v[24:27],v[220:223]
buffer_store_dwordx4 v[208:211],v7,s[40:43],s11 offen offset:0 sc1
v_mul_f32_dpp v216,v14,v216 row_newbcast:8 row_mask:0xf bank_mask:0xf
v_mul_f32_dpp v217,v14,v217 row_newbcast:9 row_mask:0xf bank_mask:0xf
v_lshlrev_b32_sdwa v62,1,v62 dst_sel:DWORD dst_unused:UNUSED_PAD src0_sel:DWORD src1_sel:WORD_1
v_mul_f32_dpp v218,v14,v218 row_newbcast:10 row_mask:0xf bank_mask:0xf
v_mfma_f32_16x16x32_bf16 v[220:223],a[20:23],v[28:31],v[220:223]
ds_read_u16_d16_hi v35,v35
v_mul_f32_dpp v219,v14,v219 row_newbcast:11 row_mask:0xf bank_mask:0xf
v_lshlrev_b32_sdwa v61,1,v61 dst_sel:DWORD dst_unused:UNUSED_PAD src0_sel:DWORD src1_sel:WORD_1
v_mul_f32_dpp v184,v14,v184 row_newbcast:12 row_mask:0xf bank_mask:0xf
v_lshlrev_b32_sdwa v63,1,v63 dst_sel:DWORD dst_unused:UNUSED_PAD src0_sel:DWORD src1_sel:WORD_1
v_mul_f32_dpp v185,v14,v185 row_newbcast:13 row_mask:0xf bank_mask:0xf
v_mfma_f32_16x16x32_bf16 v[188:191],a[24:27],v[28:31],v[188:191]
ds_read_u16 v60,v60
v_mul_f32_dpp v186,v14,v186 row_newbcast:14 row_mask:0xf bank_mask:0xf
v_mul_f32_dpp v187,v14,v187 row_newbcast:15 row_mask:0xf bank_mask:0xf
v_lshlrev_b32_sdwa v44,1,v44 dst_sel:DWORD dst_unused:UNUSED_PAD src0_sel:DWORD src1_sel:WORD_1
v_cvt_pk_bf16_f32 v216,v216,v217
v_mfma_f32_16x16x32_bf16 v[188:191],a[28:31],v[24:27],v[188:191]
ds_read_u16_d16_hi v62,v62
ds_read_u16 v61,v61
v_cvt_pk_bf16_f32 v217,v218,v219
v_cvt_pk_bf16_f32 v218,v184,v185
v_lshlrev_b32_sdwa v46,1,v46 dst_sel:DWORD dst_unused:UNUSED_PAD src0_sel:DWORD src1_sel:WORD_1
v_cvt_pk_bf16_f32 v219,v186,v187
v_lshlrev_b32_sdwa v45,1,v45 dst_sel:DWORD dst_unused:UNUSED_PAD src0_sel:DWORD src1_sel:WORD_1
v_mfma_f32_16x16x32_bf16 v[228:231],a[32:35],v[24:27],v[228:231]
buffer_store_dwordx4 v[216:219],v7,s[40:43],s11 offen offset:64 sc1
ds_read_u16_d16_hi v63,v63
v_mul_f32_dpp v224,v15,v224 row_newbcast:0 row_mask:0xf bank_mask:0xf
v_mul_f32_dpp v225,v15,v225 row_newbcast:1 row_mask:0xf bank_mask:0xf
v_lshlrev_b32_sdwa v47,1,v47 dst_sel:DWORD dst_unused:UNUSED_PAD src0_sel:DWORD src1_sel:WORD_1
v_mul_f32_dpp v226,v15,v226 row_newbcast:2 row_mask:0xf bank_mask:0xf
v_mfma_f32_16x16x32_bf16 v[228:231],a[36:39],v[28:31],v[228:231]
ds_read_u16 v44,v44
ds_read_u16_d16_hi v46,v46
v_mul_f32_dpp v227,v15,v227 row_newbcast:3 row_mask:0xf bank_mask:0xf
v_mul_f32_dpp v200,v15,v200 row_newbcast:4 row_mask:0xf bank_mask:0xf
v_lshlrev_b32_sdwa v48,1,v48 dst_sel:DWORD dst_unused:UNUSED_PAD src0_sel:DWORD src1_sel:WORD_1
v_mul_f32_dpp v201,v15,v201 row_newbcast:5 row_mask:0xf bank_mask:0xf
v_mfma_f32_16x16x32_bf16 v[204:207],a[40:43],v[28:31],v[204:207]
ds_read_u16 v45,v45
ds_read_u16_d16_hi v47,v47
v_mul_f32_dpp v202,v15,v202 row_newbcast:6 row_mask:0xf bank_mask:0xf
v_lshlrev_b32_sdwa v50,1,v50 dst_sel:DWORD dst_unused:UNUSED_PAD src0_sel:DWORD src1_sel:WORD_1
v_mul_f32_dpp v203,v15,v203 row_newbcast:7 row_mask:0xf bank_mask:0xf
v_lshlrev_b32_sdwa v49,1,v49 dst_sel:DWORD dst_unused:UNUSED_PAD src0_sel:DWORD src1_sel:WORD_1
v_cvt_pk_bf16_f32 v224,v224,v225
v_mfma_f32_16x16x32_bf16 v[204:207],a[44:47],v[24:27],v[204:207]
ds_read_u16 v48,v48
ds_read_u16_d16_hi v50,v50
v_cvt_pk_bf16_f32 v225,v226,v227
v_cvt_pk_bf16_f32 v226,v200,v201
v_cvt_pk_bf16_f32 v227,v202,v203
v_lshlrev_b32_sdwa v51,1,v51 dst_sel:DWORD dst_unused:UNUSED_PAD src0_sel:DWORD src1_sel:WORD_1
ds_read_u16 v49,v49
ds_read_u16_d16_hi v51,v51
s_waitcnt lgkmcnt(12)
v_lshl_add_u64 v[32:33],v[32:33],0,v[34:35]
s_waitcnt lgkmcnt(8)
v_lshl_add_u64 v[34:35],v[60:61],0,v[62:63]
s_waitcnt lgkmcnt(4)
v_lshl_add_u64 v[60:61],v[44:45],0,v[46:47]
s_waitcnt lgkmcnt(0)
v_lshl_add_u64 v[62:63],v[48:49],0,v[50:51]
buffer_store_dwordx4 v[224:227],v7,s[40:43],s11 offen offset:128 sc1
v_mfma_f32_16x16x32_bf16 v[212:215],a[212:215],v[32:35],v[212:215]
v_mfma_f32_16x16x32_bf16 v[212:215],a[200:203],v[60:63],v[212:215]
v_mfma_f32_16x16x32_bf16 v[172:175],a[196:199],v[60:63],v[172:175]
v_mfma_f32_16x16x32_bf16 v[172:175],a[192:195],v[32:35],v[172:175]
v_mfma_f32_16x16x32_bf16 v[220:223],a[216:219],v[32:35],v[220:223]
v_mfma_f32_16x16x32_bf16 v[220:223],a[220:223],v[60:63],v[220:223]
s_nop 0
v_mfma_f32_16x16x32_bf16 v[188:191],a[204:207],v[60:63],v[188:191]
s_nop 2
v_mul_f32_dpp v212,v14,v212 row_newbcast:0 row_mask:0xf bank_mask:0xf
v_mul_f32_dpp v213,v14,v213 row_newbcast:1 row_mask:0xf bank_mask:0xf
v_mul_f32_dpp v214,v14,v214 row_newbcast:2 row_mask:0xf bank_mask:0xf
v_mfma_f32_16x16x32_bf16 v[188:191],a[208:211],v[32:35],v[188:191]
v_mul_f32_dpp v215,v14,v215 row_newbcast:3 row_mask:0xf bank_mask:0xf
v_mul_f32_dpp v172,v14,v172 row_newbcast:4 row_mask:0xf bank_mask:0xf
v_mul_f32_dpp v173,v14,v173 row_newbcast:5 row_mask:0xf bank_mask:0xf
v_mfma_f32_16x16x32_bf16 v[228:231],a[244:247],v[32:35],v[228:231]
v_mul_f32_dpp v174,v14,v174 row_newbcast:6 row_mask:0xf bank_mask:0xf
v_mul_f32_dpp v175,v14,v175 row_newbcast:7 row_mask:0xf bank_mask:0xf
v_cvt_pk_bf16_f32 v212,v212,v213
v_mfma_f32_16x16x32_bf16 v[228:231],a[252:255],v[60:63],v[228:231]
v_cvt_pk_bf16_f32 v213,v214,v215
v_cvt_pk_bf16_f32 v214,v172,v173
v_cvt_pk_bf16_f32 v215,v174,v175
buffer_store_dwordx4 v[212:215],v7,s[40:43],s11 offen offset:192 sc1
v_mfma_f32_16x16x32_bf16 v[204:207],a[232:235],v[60:63],v[204:207]
v_mul_f32_dpp v220,v14,v220 row_newbcast:8 row_mask:0xf bank_mask:0xf
v_mul_f32_dpp v221,v14,v221 row_newbcast:9 row_mask:0xf bank_mask:0xf
v_mul_f32_dpp v222,v14,v222 row_newbcast:10 row_mask:0xf bank_mask:0xf
v_mfma_f32_16x16x32_bf16 v[204:207],a[240:243],v[32:35],v[204:207]
v_mul_f32_dpp v223,v14,v223 row_newbcast:11 row_mask:0xf bank_mask:0xf
v_mul_f32_dpp v188,v14,v188 row_newbcast:12 row_mask:0xf bank_mask:0xf
v_mul_f32_dpp v189,v14,v189 row_newbcast:13 row_mask:0xf bank_mask:0xf
v_mul_f32_dpp v190,v14,v190 row_newbcast:14 row_mask:0xf bank_mask:0xf
v_mul_f32_dpp v191,v14,v191 row_newbcast:15 row_mask:0xf bank_mask:0xf
v_cvt_pk_bf16_f32 v220,v220,v221
v_cvt_pk_bf16_f32 v221,v222,v223
v_cvt_pk_bf16_f32 v222,v188,v189
v_cvt_pk_bf16_f32 v223,v190,v191
buffer_store_dwordx4 v[220:223],v7,s[40:43],s11 offen offset:256 sc1
v_mul_f32_dpp v228,v15,v228 row_newbcast:0 row_mask:0xf bank_mask:0xf
v_mul_f32_dpp v229,v15,v229 row_newbcast:1 row_mask:0xf bank_mask:0xf
v_mul_f32_dpp v230,v15,v230 row_newbcast:2 row_mask:0xf bank_mask:0xf
v_mul_f32_dpp v231,v15,v231 row_newbcast:3 row_mask:0xf bank_mask:0xf
v_mul_f32_dpp v204,v15,v204 row_newbcast:4 row_mask:0xf bank_mask:0xf
v_mul_f32_dpp v205,v15,v205 row_newbcast:5 row_mask:0xf bank_mask:0xf
v_mul_f32_dpp v206,v15,v206 row_newbcast:6 row_mask:0xf bank_mask:0xf
v_mul_f32_dpp v207,v15,v207 row_newbcast:7 row_mask:0xf bank_mask:0xf
v_cvt_pk_bf16_f32 v228,v228,v229
v_cvt_pk_bf16_f32 v229,v230,v231
v_cvt_pk_bf16_f32 v230,v204,v205
v_cvt_pk_bf16_f32 v231,v206,v207
buffer_store_dwordx4 v[228:231],v7,s[40:43],s11 offen offset:320 sc1
v_or3_b32 v72,v72,v73,v74
v_or_b32 v72,v72,v75
s_nop 0
v_readfirstlane_b32 s64,v72
s_cmp_lg_u32 s64,0
s_cbranch_scc1 N_fallback_%=
s_endpgm
N_mainend_%=:
.fill 512,4,0xbf800000
N_fallback_%=:
s_waitcnt vmcnt(0) lgkmcnt(0)
s_barrier
)ASM"
    :: "s"(blockIdx.x), "v"(tid), "s"(ka)
    : "v1", "v2", "v3", "v4", "v5", "v6", "v7", "v8", "v9", "v10", "v11", "v12", "v13", "v14", "v15", "v16", "v17", "v18", "v19", "v20", "v21", "v22", "v23", "v24", "v25", "v26", "v27", "v28", "v29", "v30", "v31", "v32", "v33", "v34", "v35", "v36", "v37", "v38", "v39", "v40", "v41", "v42", "v43", "v44", "v45", "v46", "v47", "v48", "v49", "v50", "v51", "v52", "v53", "v54", "v55", "v56", "v57", "v58", "v59", "v60", "v61", "v62", "v63", "v64", "v65", "v66", "v67", "v68", "v69", "v70", "v71", "v72", "v73", "v74", "v75", "v76", "v77", "v78", "v79", "v80", "v81", "v82", "v83", "v84", "v85", "v86", "v87", "v88", "v89", "v90", "v91", "v92", "v93", "v94", "v95", "v96", "v97", "v98", "v99", "v100", "v101", "v102", "v103", "v104", "v105", "v106", "v107", "v108", "v109", "v110", "v111", "v112", "v113", "v114", "v115", "v116", "v117", "v118", "v119", "v120", "v121", "v122", "v123", "v124", "v125", "v126", "v127", "v128", "v129", "v130", "v131", "v132", "v133", "v134", "v135", "v136", "v137", "v138", "v139", "v140", "v141", "v142", "v143", "v144", "v145", "v146", "v147", "v148", "v149", "v150", "v151", "v152", "v153", "v154", "v155", "v156", "v157", "v158", "v159", "v160", "v161", "v162", "v163", "v164", "v165", "v166", "v167", "v168", "v169", "v170", "v171", "v172", "v173", "v174", "v175", "v176", "v177", "v178", "v179", "v180", "v181", "v182", "v183", "v184", "v185", "v186", "v187", "v188", "v189", "v190", "v191", "v192", "v193", "v194", "v195", "v196", "v197", "v198", "v199", "v200", "v201", "v202", "v203", "v204", "v205", "v206", "v207", "v208", "v209", "v210", "v211", "v212", "v213", "v214", "v215", "v216", "v217", "v218", "v219", "v220", "v221", "v222", "v223", "v224", "v225", "v226", "v227", "v228", "v229", "v230", "v231", "v232", "v233", "v234", "v235", "v236", "v237", "v238", "v239", "v240", "v241", "v242", "v243", "v244", "v245", "v246", "v247", "v248", "v249", "v250", "v251", "v252", "v253", "v254", "v255", "a0", "a1", "a2", "a3", "a4", "a5", "a6", "a7", "a8", "a9", "a10", "a11", "a12", "a13", "a14", "a15", "a16", "a17", "a18", "a19", "a20", "a21", "a22", "a23", "a24", "a25", "a26", "a27", "a28", "a29", "a30", "a31", "a32", "a33", "a34", "a35", "a36", "a37", "a38", "a39", "a40", "a41", "a42", "a43", "a44", "a45", "a46", "a47", "a48", "a49", "a50", "a51", "a52", "a53", "a54", "a55", "a56", "a57", "a58", "a59", "a60", "a61", "a62", "a63", "a64", "a65", "a66", "a67", "a68", "a69", "a70", "a71", "a72", "a73", "a74", "a75", "a76", "a77", "a78", "a79", "a80", "a81", "a82", "a83", "a84", "a85", "a86", "a87", "a88", "a89", "a90", "a91", "a92", "a93", "a94", "a95", "a96", "a97", "a98", "a99", "a100", "a101", "a102", "a103", "a104", "a105", "a106", "a107", "a108", "a109", "a110", "a111", "a112", "a113", "a114", "a115", "a116", "a117", "a118", "a119", "a120", "a121", "a122", "a123", "a124", "a125", "a126", "a127", "a128", "a129", "a130", "a131", "a132", "a133", "a134", "a135", "a136", "a137", "a138", "a139", "a140", "a141", "a142", "a143", "a144", "a145", "a146", "a147", "a148", "a149", "a150", "a151", "a152", "a153", "a154", "a155", "a156", "a157", "a158", "a159", "a160", "a161", "a162", "a163", "a164", "a165", "a166", "a167", "a168", "a169", "a170", "a171", "a172", "a173", "a174", "a175", "a176", "a177", "a178", "a179", "a180", "a181", "a182", "a183", "a184", "a185", "a186", "a187", "a188", "a189", "a190", "a191", "a192", "a193", "a194", "a195", "a196", "a197", "a198", "a199", "a200", "a201", "a202", "a203", "a204", "a205", "a206", "a207", "a208", "a209", "a210", "a211", "a212", "a213", "a214", "a215", "a216", "a217", "a218", "a219", "a220", "a221", "a222", "a223", "a224", "a225", "a226", "a227", "a228", "a229", "a230", "a231", "a232", "a233", "a234", "a235", "a236", "a237", "a238", "a239", "a240", "a241", "a242", "a243", "a244", "a245", "a246", "a247", "a248", "a249", "a250", "a251", "a252", "a253", "a254", "a255", "s4", "s5", "s6", "s7", "s8", "s9", "s10", "s11", "s12", "s13", "s14", "s15", "s16", "s17", "s18", "s19", "s20", "s21", "s22", "s23", "s24", "s25", "s26", "s27", "s28", "s29", "s30", "s31", "s32", "s33", "s34", "s35", "s36", "s37", "s38", "s39", "s40", "s41", "s42", "s43", "s44", "s45", "s46", "s47", "s48", "s49", "s50", "s51", "s52", "s53", "s54", "s55", "s56", "s57", "s58", "s59", "s60", "s61", "s62", "s63", "s64", "s65", "s66", "s67", "s68", "s69", "s70", "s71", "s72", "s73", "s74", "s75", "s76", "s77", "s78", "s79", "s80", "s81", "s82", "s83", "s84", "s85", "s86", "s87", "s88", "s89", "s90", "s91", "s92", "s93", "s94", "s95", "s96", "s97", "s98", "s99", "vcc", "scc", "memory");
    asm volatile("" : "+s"(ka));
    typedef const unsigned short* __attribute__((address_space(1))) gp;
    unsigned long long const __attribute__((address_space(4)))* kp = (unsigned long long const __attribute__((address_space(4)))*)ka;
    const unsigned short* x = (const unsigned short*)kp[0]; const unsigned short* dw = (const unsigned short*)kp[1];
    const unsigned short* w1 = (const unsigned short*)kp[2]; const unsigned short* b1 = (const unsigned short*)kp[3];
    const unsigned short* w2 = (const unsigned short*)kp[4]; const unsigned short* b2 = (const unsigned short*)kp[5];
    const unsigned short* gamma = (const unsigned short*)kp[6]; unsigned short* out = (unsigned short*)kp[7];
    extern __shared__ float sm[];
    float* cv = sm; float* hid = sm + 16 * 96; float* stt = hid + 16 * 384;
    const int x0 = (blockIdx.x >> 3) * 16, y0 = (blockIdx.x & 7) * 64;
    for (int y = y0; y < y0 + 64; ++y) {
        for (int i = tid; i < 16 * 96; i += 256) {
            int p = i / 96, c = i - p * 96; float s = 0.f;
            for (int ky = 0; ky < 3; ++ky) for (int kx = 0; kx < 3; ++kx) {
                int yy = y + ky - 1, xx = x0 + p + kx - 1;
                float v = (yy >= 0 && yy < 512 && xx >= 0 && xx < 512) ? bf2f(x[((size_t)yy * 512 + xx) * 96 + c]) : 0.f;
                s = fmaf(v, bf2f(dw[(ky * 3 + kx) * 96 + c]), s);
            }
            cv[i] = s;
        }
        __syncthreads();
        if (tid < 16) {
            float m = 0.f; for (int c = 0; c < 96; ++c) m += cv[tid * 96 + c];
            m *= (1.f / 96); float q = 0.f;
            for (int c = 0; c < 96; ++c) { float d = cv[tid * 96 + c] - m; q = fmaf(d, d, q); }
            stt[tid] = m; stt[16 + tid] = 1.f / sqrtf(q * (1.f / 96) + 1e-5f);
        }
        __syncthreads();
        for (int i = tid; i < 16 * 96; i += 256) { int p = i / 96; cv[i] = rbf((cv[i] - stt[p]) * stt[16 + p]); }
        __syncthreads();
        for (int e = tid; e < 384; e += 256) {
            float a[16];
#pragma unroll
            for (int p = 0; p < 16; ++p) a[p] = 0.f;
            for (int c = 0; c < 96; ++c) {
                float w = bf2f(w1[c * 384 + e]);
#pragma unroll
                for (int p = 0; p < 16; ++p) a[p] = fmaf(cv[p * 96 + c], w, a[p]);
            }
            float bb = bf2f(b1[e]);
#pragma unroll
            for (int p = 0; p < 16; ++p) {
                float h = a[p] + bb;
                hid[p * 384 + e] = rbf(0.5f * h * (1.f + tanhf(0.7978845608028654f * (h + 0.044715f * h * h * h))));
            }
        }
        __syncthreads();
        for (int i = tid; i < 16 * 96; i += 256) {
            int p = i / 96, c = i - p * 96; float s = 0.f;
            for (int e = 0; e < 384; ++e) s = fmaf(hid[p * 384 + e], bf2f(w2[e * 96 + c]), s);
            size_t o = ((size_t)y * 512 + x0 + p) * 96 + c;
            out[o] = f2bf(bf2f(x[o]) + bf2f(gamma[c]) * (s + bf2f(b2[c])));
        }
        __syncthreads();
    }

}

extern "C" void kernel_launch(void* const* d_in, const int* in_sizes, int n_in,
                              void* d_out, int out_size, void* d_ws, size_t ws_size,
                              hipStream_t stream) {
    (void)hipFuncSetAttribute((const void*)convnext_kernel, hipFuncAttributeMaxDynamicSharedMemorySize, LDS_BYTES);
    convnext_kernel<<<256, 256, LDS_BYTES, stream>>>(
        (const unsigned short*)d_in[0], (const unsigned short*)d_in[1], (const unsigned short*)d_in[2], (const unsigned short*)d_in[3],
        (const unsigned short*)d_in[4], (const unsigned short*)d_in[5], (const unsigned short*)d_in[6], (unsigned short*)d_out);
}
```

```cpp
#include <hip/hip_runtime.h>
#include <stdint.h>

constexpr int LDS_BYTES = 163584;

__device__ __forceinline__ float bf2f(unsigned short b) { return __uint_as_float((unsigned)b << 16); }
__device__ __forceinline__ unsigned short f2bf(float f) { unsigned r; asm("v_cvt_pk_bf16_f32 %0, %1, %1" : "=v"(r) : "v"(f)); return (unsigned short)r; }
__device__ __forceinline__ float rbf(float f) { return bf2f(f2bf(f)); }

__global__ __launch_bounds__(256, 1) __attribute__((aligned(4096))) void convnext_kernel(
    const unsigned short* __restrict__ x_, const unsigned short* __restrict__ dw_, const unsigned short* __restrict__ w1_,
    const unsigned short* __restrict__ b1_, const unsigned short* __restrict__ w2_, const unsigned short* __restrict__ b2_,
    const unsigned short* __restrict__ gamma_, unsigned short* __restrict__ out_) {
    const unsigned tid = threadIdx.x;
    unsigned long long ka = (unsigned long long)__builtin_amdgcn_kernarg_segment_ptr();
    asm volatile(
R"ASM(
s_load_dwordx16 s[20:35],%2,0
v_lshrrev_b32 v27,6,%1
v_and_b32 v24,63,%1
v_readfirstlane_b32 s52,v27
s_lshr_b32 s70,%0,3
v_and_b32_e64 v61,8,v24
v_lshl_add_u32 v62,s70,2,v27
v_mul_u32_u24 v61,15,v61
s_getpc_b64 s[14:15]
N_pf_%=:
v_or_b32 v61,7,v61
v_and_b32 v62,v61,v62
v_and_b32_e64 v61,7,v24
v_lshl_add_u32 v62,v62,3,v61
v_lshlrev_b32_e64 v62,7,v62
v_min_u32 v62,N_mainend_%=-N_pf_%=+2048,v62
global_load_dword v255,v62,s[14:15]
v_mul_u32_u24 v30,52,v27
v_add_u32 v30,v30,v24
v_subrev_u32_e64 v29,4,v30
v_max_i32 v29,0xc0,v29
v_min_u32_e64 v30,v30,v29
v_mul_u32_u24 v28,0xaaab,v30
s_and_b32 s64,%0,7
s_lshr_b32 s70,%0,3
v_lshrrev_b32 v28,19,v28
s_lshl_b32 s71,s64,6
s_mul_i32 s68,s70,3072
v_bfe_u32 v27,v28,2,2
s_sub_u32 s65,s71,1
s_mul_i32 s56,s65,98304
v_xor_b32 v27,v30,v27
s_add_u32 s56,s56,s68
s_cmp_lt_i32 s65,0
s_cselect_b32 s89,0x70000000,s56
v_lshlrev_b32 v27,4,v27
s_cmp_eq_u32 s70,0
s_cselect_b32 s67,0,99
s_cmp_eq_u32 s70,31
s_cselect_b32 s67,17,s67
v_mov_b32 v29,0x7fff0000
v_cmp_eq_u32_e32 vcc,s67,v28
s_mul_i32 s57,s52,832
s_add_u32 s57,s57,73728
v_cndmask_b32_e32 v6,v27,v29,vcc
s_mov_b32 s38,50331840
s_mov_b32 s39,0x00020000
s_mov_b32 s50,1728
s_mov_b32 s51,0x00020000
v_lshlrev_b32_e64 v8,4,v24
v_mul_u32_u24 v44,79,v24
v_add_u32 v45,5056,v44
v_lshrrev_b32 v44,10,v44
v_lshrrev_b32 v45,10,v45
v_sub_u32 v44,v24,v44
v_sub_u32 v45,v24,v45
v_lshlrev_b32_e64 v44,4,v44
v_add_lshl_u32 v45,v45,64,4
v_min_u32 v28,191,%1
v_lshlrev_b32_e64 v32,2,v28
v_min_u32 v34,95,%1
v_lshlrev_b32_e64 v33,1,v34
s_waitcnt lgkmcnt(0)
s_sub_u32 s36,s20,192
s_subb_u32 s37,s21,0
s_and_b32 s37,s37,0xffff
s_add_u32 m0,s57,0
s_nop 0
buffer_load_dwordx4 v6,s[36:39],s89 offen lds
s_add_u32 m0,s57,3536
s_add_u32 s89,s56,98304
buffer_load_dwordx4 v6,s[36:39],s89 offen lds
s_add_u32 m0,s57,7080
s_add_u32 s89,s56,196608
s_nop 0
buffer_load_dwordx4 v6,s[36:39],s89 offen lds
s_mov_b32 s48,s22
s_and_b32 s49,s23,0xffff
s_mov_b32 m0,12288
global_load_ushort v37,v33,s[32:33]
buffer_load_dwordx4 v44,s[48:51],0 offen lds
s_mov_b32 m0,13312
global_load_dword v35,v32,s[26:27]
buffer_load_dwordx4 v45,s[48:51],0 offen lds
global_load_ushort v36,v33,s[30:31]
v_and_b32 v27,15,v24
v_lshrrev_b32 v28,4,v24
s_mul_i32 s66,s52,192
v_min_u32_e64 v27,11,v27
v_mul_u32_u24 v28,18432,v28
v_lshl_add_u32 v27,v27,4,s66
s_mul_i32 s66,s52,18432
v_add_u32 v172,v27,v28
v_add_u32 v173,s66,v8
global_load_dwordx4 v[160:163],v172,s[24:25]
global_load_dwordx4 v[164:167],v172,s[24:25] offset:3840
global_load_dwordx4 v[168:171],v173,s[28:29]
s_and_b32 s64,s52,1
s_mul_i32 s65,s64,48
v_bfe_u32 v27,v24,3,2
v_and_b32 v28,4,v24
v_lshrrev_b32 v29,5,v24
v_add3_u32 v27,v27,v28,s65
v_lshl_add_u32 v27,v29,3,v27
v_mul_u32_u24 v27,768,v27
v_and_b32 v28,3,v24
v_xor_b32 v28,v28,v29
v_lshl_add_u32 v27,v28,4,v27
s_mul_i32 s65,s64,3072
v_add_u32_e64 v28,s65,v8
s_mul_i32 s67,s64,3080
s_add_u32 s67,s67,40896
s_add_u32 s65,s65,16256
s_cmp_lt_u32 s52,2
s_cselect_b32 s58,s65,s67
s_cselect_b64 vcc,-1,0
s_movk_i32 s59,6144
s_cselect_b32 s59,-22464,s59
s_cselect_b32 s53,11264,0
s_cselect_b32 s88,0,67584
s_cselect_b32 s87,64,0
s_cselect_b32 s69,0,0x70000000
s_mov_b32 s62,32832
s_cselect_b32 s62,88272,s62
s_cselect_b64 s[44:45],s[24:25],s[28:29]
s_add_u32 s62,s62,s58
s_and_b32 s45,s45,0xffff
v_cndmask_b32_e32 v5,v28,v27,vcc
s_add_u32 m0,s57,10616
s_add_u32 s89,s56,294912
buffer_load_dwordx4 v6,s[36:39],s89 offen lds
s_mov_b32 s46,73728
s_mov_b32 s47,0x00020000
s_mov_b32 s99,0x72800000
s_mov_b32 s98,0xffff0000
s_mov_b64 s[90:91],0
v_mov_b32 v96,0x409d3ef8
v_lshlrev_b32 v102,4,%1
v_lshlrev_b32 v104,19,%1
v_cmp_eq_u32_e64 s[94:95],0,%1
v_add_u32 v103,0x8000,v102
s_mov_b32 s4,0x3f000000
s_mov_b32 s5,0x3fc00000
s_mov_b32 s6,0x40200000
s_mov_b32 s7,0x40600000
s_mov_b32 s8,0x40900000
s_mov_b32 s9,0x40b00000
s_mov_b32 s10,0x40d00000
s_mov_b32 s11,0x40f00000
s_mov_b32 s12,0x3f80222e
s_mov_b32 s13,0x13089abf
s_mov_b32 s96,0xebefe000
v_add_u32 v28,0x10000,v104
s_add_u32 m0,s57,14144
s_add_u32 s89,s56,393216
buffer_load_dwordx4 v6,s[36:39],s89 offen lds
v_mul_f32_e64 v98,s99,v104
v_fma_f32 v100,v28,s99,-v98
v_pk_fma_f32 v[64:65],v[100:101],s[4:5],v[98:99] op_sel_hi:[0,1,0]
v_pk_fma_f32 v[66:67],v[100:101],s[6:7],v[98:99] op_sel_hi:[0,1,0]
v_pk_fma_f32 v[68:69],v[100:101],s[8:9],v[98:99] op_sel_hi:[0,1,0]
v_pk_fma_f32 v[70:71],v[100:101],s[10:11],v[98:99] op_sel_hi:[0,1,0]
v_cndmask_b32_e64 v64,v64,0,s[94:95]
v_pk_mul_f32 v[72:73],v[64:65],v[64:65]
v_pk_mul_f32 v[74:75],v[66:67],v[66:67]
v_pk_mul_f32 v[76:77],v[68:69],v[68:69]
v_pk_mul_f32 v[78:79],v[70:71],v[70:71]
v_pk_fma_f32 v[72:73],v[72:73],s[12:13],v[96:97] op_sel_hi:[1,0,0]
v_pk_fma_f32 v[74:75],v[74:75],s[12:13],v[96:97] op_sel_hi:[1,0,0]
v_pk_fma_f32 v[76:77],v[76:77],s[12:13],v[96:97] op_sel_hi:[1,0,0]
v_pk_fma_f32 v[78:79],v[78:79],s[12:13],v[96:97] op_sel_hi:[1,0,0]
s_add_u32 m0,s57,17680
s_add_u32 s89,s56,491520
buffer_load_dwordx4 v6,s[36:39],s89 offen lds
v_pk_mul_f32 v[72:73],v[72:73],v[64:65]
v_pk_mul_f32 v[74:75],v[74:75],v[66:67]
v_pk_mul_f32 v[76:77],v[76:77],v[68:69]
v_pk_mul_f32 v[78:79],v[78:79],v[70:71]
v_exp_f32 v72,v72
v_exp_f32 v73,v73
v_exp_f32 v74,v74
v_exp_f32 v75,v75
v_exp_f32 v76,v76
v_exp_f32 v77,v77
s_add_u32 m0,s57,21224
s_add_u32 s89,s56,589824
buffer_load_dwordx4 v6,s[36:39],s89 offen lds
v_exp_f32 v78,v78
v_exp_f32 v79,v79
v_pk_fma_f32 v[72:73],v[72:73],s[96:97],s[96:97] op_sel_hi:[1,0,0]
v_pk_fma_f32 v[74:75],v[74:75],s[96:97],s[96:97] op_sel_hi:[1,0,0]
v_pk_fma_f32 v[76:77],v[76:77],s[96:97],s[96:97] op_sel_hi:[1,0,0]
v_pk_fma_f32 v[78:79],v[78:79],s[96:97],s[96:97] op_sel_hi:[1,0,0]
v_rcp_f32 v72,v72
v_rcp_f32 v73,v73
v_rcp_f32 v74,v74
v_rcp_f32 v75,v75
s_add_u32 m0,s57,24760
s_add_u32 s89,s56,688128
buffer_load_dwordx4 v6,s[36:39],s89 offen lds
v_rcp_f32 v76,v76
v_rcp_f32 v77,v77
v_rcp_f32 v78,v78
v_rcp_f32 v79,v79
v_pk_mul_f32 v[72:73],v[72:73],v[64:65]
v_pk_mul_f32 v[74:75],v[74:75],v[66:67]
v_pk_mul_f32 v[76:77],v[76:77],v[68:69]
v_pk_mul_f32 v[78:79],v[78:79],v[70:71]
v_pk_fma_f32 v[64:65],v[64:65],s[12:13],v[72:73] op_sel:[0,1,0] op_sel_hi:[1,1,1]
v_pk_fma_f32 v[66:67],v[66:67],s[12:13],v[74:75] op_sel:[0,1,0] op_sel_hi:[1,1,1]
v_pk_fma_f32 v[68:69],v[68:69],s[12:13],v[76:77] op_sel:[0,1,0] op_sel_hi:[1,1,1]
s_add_u32 m0,s57,28288
s_add_u32 s89,s56,786432
buffer_load_dwordx4 v6,s[36:39],s89 offen lds
v_pk_fma_f32 v[70:71],v[70:71],s[12:13],v[78:79] op_sel:[0,1,0] op_sel_hi:[1,1,1]
v_cvt_pk_bf16_f32 v80,v72,v73
v_cvt_pk_bf16_f32 v81,v74,v75
v_cvt_pk_bf16_f32 v82,v76,v77
v_cvt_pk_bf16_f32 v83,v78,v79
v_cvt_pk_bf16_f32 v84,v64,v65
v_cvt_pk_bf16_f32 v85,v66,v67
v_cvt_pk_bf16_f32 v86,v68,v69
v_cvt_pk_bf16_f32 v87,v70,v71
ds_write_b128 v102,v[80:83] offset:0
ds_write_b128 v103,v[84:87] offset:32768
v_add_u32 v104,0x8000000,v104
v_add_u32 v28,0x10000,v104
v_mul_f32_e64 v98,s99,v104
v_fma_f32 v100,v28,s99,-v98
s_add_u32 m0,s57,31824
s_add_u32 s89,s56,884736
buffer_load_dwordx4 v6,s[36:39],s89 offen lds
v_pk_fma_f32 v[64:65],v[100:101],s[4:5],v[98:99] op_sel_hi:[0,1,0]
v_pk_fma_f32 v[66:67],v[100:101],s[6:7],v[98:99] op_sel_hi:[0,1,0]
v_pk_fma_f32 v[68:69],v[100:101],s[8:9],v[98:99] op_sel_hi:[0,1,0]
v_pk_fma_f32 v[70:71],v[100:101],s[10:11],v[98:99] op_sel_hi:[0,1,0]
v_pk_mul_f32 v[72:73],v[64:65],v[64:65]
v_pk_mul_f32 v[74:75],v[66:67],v[66:67]
v_pk_mul_f32 v[76:77],v[68:69],v[68:69]
v_pk_mul_f32 v[78:79],v[70:71],v[70:71]
v_pk_fma_f32 v[72:73],v[72:73],s[12:13],v[96:97] op_sel_hi:[1,0,0]
v_pk_fma_f32 v[74:75],v[74:75],s[12:13],v[96:97] op_sel_hi:[1,0,0]
v_pk_fma_f32 v[76:77],v[76:77],s[12:13],v[96:97] op_sel_hi:[1,0,0]
v_pk_fma_f32 v[78:79],v[78:79],s[12:13],v[96:97] op_sel_hi:[1,0,0]
v_pk_mul_f32 v[72:73],v[72:73],v[64:65]
v_pk_mul_f32 v[74:75],v[74:75],v[66:67]
v_pk_mul_f32 v[76:77],v[76:77],v[68:69]
s_add_u32 m0,s57,35368
s_add_u32 s89,s56,983040
buffer_load_dwordx4 v6,s[36:39],s89 offen lds
v_pk_mul_f32 v[78:79],v[78:79],v[70:71]
v_exp_f32 v72,v72
v_exp_f32 v73,v73
v_exp_f32 v74,v74
v_exp_f32 v75,v75
v_exp_f32 v76,v76
v_exp_f32 v77,v77
v_exp_f32_e64 v78,v78
s_add_u32 m0,s57,38904
s_add_u32 s89,s56,1081344
buffer_load_dwordx4 v6,s[36:39],s89 offen lds
v_exp_f32_e64 v79,v79
v_pk_fma_f32 v[72:73],v[72:73],s[96:97],s[96:97] op_sel_hi:[1,0,0]
v_pk_fma_f32 v[74:75],v[74:75],s[96:97],s[96:97] op_sel_hi:[1,0,0]
v_pk_fma_f32 v[76:77],v[76:77],s[96:97],s[96:97] op_sel_hi:[1,0,0]
v_pk_fma_f32 v[78:79],v[78:79],s[96:97],s[96:97] op_sel_hi:[1,0,0]
v_rcp_f32 v72,v72
v_rcp_f32 v73,v73
v_rcp_f32 v74,v74
v_rcp_f32 v75,v75
v_rcp_f32 v76,v76
v_rcp_f32 v77,v77
v_rcp_f32 v78,v78
v_rcp_f32 v79,v79
v_pk_mul_f32 v[72:73],v[72:73],v[64:65]
v_pk_mul_f32 v[74:75],v[74:75],v[66:67]
v_pk_mul_f32 v[76:77],v[76:77],v[68:69]
v_pk_mul_f32 v[78:79],v[78:79],v[70:71]
v_pk_fma_f32 v[64:65],v[64:65],s[12:13],v[72:73] op_sel:[0,1,0] op_sel_hi:[1,1,1]
v_pk_fma_f32 v[66:67],v[66:67],s[12:13],v[74:75] op_sel:[0,1,0] op_sel_hi:[1,1,1]
v_pk_fma_f32 v[68:69],v[68:69],s[12:13],v[76:77] op_sel:[0,1,0] op_sel_hi:[1,1,1]
v_pk_fma_f32 v[70:71],v[70:71],s[12:13],v[78:79] op_sel:[0,1,0] op_sel_hi:[1,1,1]
v_cvt_pk_bf16_f32 v88,v72,v73
v_cvt_pk_bf16_f32 v89,v74,v75
v_cvt_pk_bf16_f32 v90,v76,v77
v_cvt_pk_bf16_f32 v91,v78,v79
v_cvt_pk_bf16_f32 v92,v64,v65
v_cvt_pk_bf16_f32 v93,v66,v67
v_cvt_pk_bf16_f32 v94,v68,v69
v_cvt_pk_bf16_f32 v95,v70,v71
ds_write_b128 v102,v[88:91] offset:4096
ds_write_b128 v103,v[92:95] offset:36864
s_mov_b32 s96,0x8befe000
s_lshl_b32 s66,s52,2
s_mul_i32 s60,s66,3536
s_add_u32 s66,s66,s71
s_lshl2_add_u32 s16,s70,s52
s_mul_i32 s16,s16,512
v_and_b32 v25,15,v24
v_lshrrev_b32_e64 v26,4,v24
s_mul_i32 s61,s66,98304
s_add_u32 s61,s61,s68
s_mov_b32 s54,0x44ea44ea
s_mov_b32 s55,0x877f877f
s_mov_b64 s[40:41],s[34:35]
s_and_b32 s41,s41,0xffff
s_mov_b32 s42,50331648
s_mov_b32 s43,0x00020000
v_mov_b32 v41,0x70000
s_add_u32 m0,s57,42432
s_add_u32 s89,s56,1179648
buffer_load_dwordx4 v6,s[36:39],s89 offen lds
v_and_b32 v39,8,v24
v_and_b32 v40,7,v24
v_bfe_u32 v27,v24,1,1
v_sub_u32_e64 v27,v26,v27
v_cmp_gt_u32_e64 s[92:93],3,v27
v_and_b32 v28,1,v24
v_mul_u32_u24 v27,3,v27
v_sub_u32_e64 v27,v27,v28
v_cmp_eq_u32_e64 s[94:95],0,v28
v_lshrrev_b32_e64 v29,2,v25
v_mul_i32_i24 v27,208,v27
v_lshl_add_u32 v27,v29,1,v27
v_mul_u32_u24 v10,0x5555,v29
v_add_u32 v27,12288,v27
v_cndmask_b32_e64 v45,v41,v27,s[92:93]
v_cndmask_b32_e64 v44,v41,v45,s[94:95]
v_cndmask_b32_e64 v47,v45,v41,s[94:95]
s_waitcnt vmcnt(13)
s_nop 0
ds_read_u16 a96,v44 offset:0
ds_read_u16 a97,v45 offset:208
ds_read_u16 a98,v45 offset:416
ds_read_u16 a99,v47 offset:624
ds_read_u16 a100,v44 offset:8
ds_read_u16 a101,v45 offset:216
ds_read_u16 a102,v45 offset:424
ds_read_u16 a103,v47 offset:632
v_mov_b64 v[20:21],0
v_mov_b64 v[22:23],0
v_cmp_eq_u32_e64 s[92:93],v26,v29
v_lshlrev_b32_e64 v27,4,v25
v_add_u32 v27,10112,v27
v_cndmask_b32_e64 v13,v41,v27,s[92:93]
v_lshrrev_b32 v27,2,v39
v_lshrrev_b32 v28,5,v24
v_add_u32_e64 v27,v27,v28
v_mul_u32_u24 v27,3536,v27
v_lshl_or_b32 v10,v27,16,v10
v_and_b32 v27,1,v26
v_add_u32 v27,v27,v40
v_mul_u32_u24 v28,384,v27
v_lshrrev_b32_e64 v27,1,v27
v_lshl_add_u32 v28,v27,4,v28
v_mfma_f32_32x32x16_bf16 v[64:79],v[20:23],v[20:23],0
ds_read_u16 a104,v44 offset:16
ds_read_u16 a105,v45 offset:224
ds_read_u16 a106,v45 offset:432
ds_read_u16 a107,v47 offset:640
ds_read_u16 a108,v44 offset:24
ds_read_u16 a109,v45 offset:232
ds_read_u16 a110,v45 offset:440
ds_read_u16 a111,v47 offset:648
v_add_u32 v30,73728,v39
s_add_u32 m0,s57,45968
s_add_u32 s89,s56,1277952
buffer_load_dwordx4 v6,s[36:39],s89 offen lds
v_add_u32 v11,v28,v30
s_movk_i32 s66,16384
v_lshl_add_u32 v27,v40,1,1
v_lshrrev_b32 v28,2,v27
v_xor_b32 v28,v28,v26
v_mul_u32_u24 v27,192,v27
v_lshl_add_u32 v27,v28,4,v27
v_add3_u32 v12,v27,v39,s66
v_lshl_add_u32 v27,v40,1,2
v_lshrrev_b32 v28,2,v27
v_xor_b32 v28,v28,v26
v_mul_u32_u24 v27,192,v27
v_lshl_add_u32 v27,v28,4,v27
v_add3_u32 v27,v27,v39,s66
v_lshl_or_b32 v12,v27,16,v12
v_mfma_f32_32x32x16_bf16 v[80:95],v[20:23],v[20:23],0
ds_read_u16 a112,v44 offset:32
ds_read_u16 a113,v45 offset:240
ds_read_u16 a114,v45 offset:448
ds_read_u16 a115,v47 offset:656
ds_read_u16 a116,v44 offset:40
ds_read_u16 a117,v45 offset:248
ds_read_u16 a118,v45 offset:456
ds_read_u16 a119,v47 offset:664
v_mul_u32_u24 v27,24576,v39
v_mul_u32_u24 v28,384,v40
v_lshl_add_u32 v27,v26,4,v27
v_add_u32 v7,v27,v28
v_lshrrev_b32 v27,3,v25
v_bfe_u32 v28,v25,2,1
v_lshl_add_u32 v28,v27,3,v28
v_lshl_add_u32 v28,v26,1,v28
v_lshlrev_b32_e64 v28,6,v28
v_lshl_add_u32 v28,v27,4,v28
v_and_b32_e64 v29,1,v25
v_lshl_add_u32 v28,v29,5,v28
v_bfe_u32 v29,v25,1,1
v_lshl_add_u32 v28,v29,3,v28
v_xor_b32_e64 v29,16,v28
v_add_u32 v1,16256,v28
v_mfma_f32_32x32x16_bf16 v[96:111],v[20:23],v[20:23],0
ds_read_u16 a120,v44 offset:48
ds_read_u16 a121,v45 offset:256
ds_read_u16 a122,v45 offset:464
ds_read_u16 a123,v47 offset:672
ds_read_u16 a124,v44 offset:56
ds_read_u16 a125,v45 offset:264
ds_read_u16 a126,v45 offset:472
ds_read_u16 a127,v47 offset:680
v_add_u32 v2,16256,v29
s_add_u32 m0,s57,49512
s_add_u32 s89,s56,1376256
buffer_load_dwordx4 v6,s[36:39],s89 offen lds
v_lshrrev_b32_e64 v27,3,v25
v_bfe_u32 v28,v25,2,1
v_lshl_add_u32 v27,v28,1,v27
v_lshrrev_b32_e64 v28,1,v26
v_lshl_add_u32 v27,v28,2,v27
v_mul_u32_u24 v27,192,v27
v_and_b32_e64 v29,1,v26
v_mul_u32_u24 v30,3080,v29
v_add_u32 v27,v27,v30
v_and_b32 v30,3,v25
v_lshl_add_u32 v27,v30,4,v27
v_add_u32 v3,40896,v27
v_lshlrev_b32_e64 v29,6,v29
v_lshl_add_u32 v29,v28,4,v29
v_add_u32 v4,8192,v29
v_mfma_f32_32x32x16_bf16 v[112:127],v[20:23],v[20:23],0
ds_read_u16 a128,v44 offset:64
ds_read_u16 a129,v45 offset:272
ds_read_u16 a130,v45 offset:480
ds_read_u16 a131,v47 offset:688
ds_read_u16 a132,v44 offset:72
ds_read_u16 a133,v45 offset:280
ds_read_u16 a134,v45 offset:488
ds_read_u16 a135,v47 offset:696
v_lshrrev_b32_e64 v27,5,v34
v_bfe_u32 v28,v34,2,1
v_and_b32 v29,3,v34
v_lshlrev_b32 v49,7,v27
v_lshl_add_u32 v49,v28,5,v49
v_lshl_add_u32 v49,v29,2,v49
v_bfe_u32 v30,v34,3,1
v_lshl_add_u32 v49,v30,6,v49
v_bfe_u32 v30,v34,4,1
v_lshl_add_u32 v49,v30,4,v49
v_add_u32 v49,11776,v49
v_lshrrev_b32_e64 v55,6,v34
v_bfe_u32 v30,v34,3,2
v_lshl_add_u32 v55,v55,2,v30
v_and_b32_e64 v30,1,v27
v_lshl_add_u32 v55,v55,1,v30
v_mfma_f32_32x32x16_bf16 v[128:143],v[20:23],v[20:23],0
ds_read_u16 a136,v44 offset:80
ds_read_u16 a137,v45 offset:288
ds_read_u16 a138,v45 offset:496
ds_read_u16 a139,v47 offset:704
ds_read_u16 a140,v44 offset:88
ds_read_u16 a141,v45 offset:296
ds_read_u16 a142,v45 offset:504
ds_read_u16 a143,v47 offset:712
v_and_b32_e64 v30,7,v34
s_add_u32 m0,s57,53048
s_add_u32 s89,s56,1474560
buffer_load_dwordx4 v6,s[36:39],s89 offen lds
v_lshl_add_u32 v55,v55,3,v30
v_lshlrev_b32 v55,2,v55
v_lshlrev_b32 v15,2,v24
v_lshl_add_u32 v50,v27,1,v28
v_lshlrev_b32_e64 v50,4,v50
v_bfe_u32 v30,v34,3,2
v_lshl_add_u32 v50,v30,2,v50
v_add_u32 v50,v50,v29
v_lshlrev_b32 v50,4,v50
v_add_u32 v50,10112,v50
v_lshlrev_b32_e64 v51,4,v29
v_cmp_eq_u32_e64 s[94:95],1,v28
v_lshlrev_b32_e64 v48,1,v32
v_add_u32 v48,8192,v48
v_mov_b32_e64 v53,0
v_mfma_f32_32x32x16_bf16 v[144:159],v[20:23],v[20:23],0
ds_read_u16 a144,v44 offset:96
ds_read_u16 a145,v45 offset:304
ds_read_u16 a146,v45 offset:512
ds_read_u16 a147,v47 offset:720
ds_read_u16 a148,v44 offset:104
ds_read_u16 a149,v45 offset:312
ds_read_u16 a150,v45 offset:520
ds_read_u16 a151,v47 offset:728
v_lshlrev_b32 v37,16,v37
v_lshlrev_b32 v36,16,v36
v_mov_b32 v27,0x45800000
v_mov_b32 v28,0x53800000
v_cmp_nlt_f32_e64 s[90:91],|v37|,v27
v_cmp_nlt_f32_e64 vcc,|v36|,v28
v_mov_b32 v27,0x42800000
v_mul_f32 v37,0x6c800000,v37
v_mul_f32 v36,0x12800000,v36
v_rcp_f32 v56,v37
v_lshlrev_b32 v42,16,v35
v_and_b32 v43,s98,v35
s_or_b64 s[90:91],s[90:91],vcc
v_cmp_nlt_f32_e64 vcc,|v42|,v27
v_cmp_nlt_f32_e64 s[92:93],|v43|,v27
v_med3_f32 v56,v56,1.0,-1.0
ds_read_u16 a152,v44 offset:112
ds_read_u16 a153,v45 offset:320
ds_read_u16 a154,v45 offset:528
ds_read_u16 a155,v47 offset:736
ds_read_u16 a156,v44 offset:120
ds_read_u16 a157,v45 offset:328
ds_read_u16 a158,v45 offset:536
ds_read_u16 a159,v47 offset:744
v_pk_mul_f32 v[42:43],v[42:43],s[96:97] op_sel_hi:[1,0]
s_add_u32 m0,s57,56576
s_add_u32 s89,s56,1572864
buffer_load_dwordx4 v6,s[36:39],s89 offen lds
v_cvt_pk_bf16_f32 v52,v56,v53
ds_write_b64 v48,v[42:43]
ds_write_b32 v49,v36
v_lshlrev_b32 v54,16,v52
v_rcp_f32 v54,v54
v_lshlrev_b64 v[52:53],v51,v[52:53]
s_or_b64 s[90:91],s[90:91],vcc
s_or_b64 s[90:91],s[90:91],s[92:93]
v_cndmask_b32_e64 v56,v52,0,s[94:95]
v_cndmask_b32_e64 v58,0,v52,s[94:95]
v_cndmask_b32_e64 v57,v53,0,s[94:95]
v_cndmask_b32_e64 v59,0,v53,s[94:95]
ds_write_b32 v15,v7 offset:14848
ds_write_b32 v15,v13 offset:15104
s_mul_i32 s66,s52,1536
ds_read_u16 a160,v44 offset:128
ds_read_u16 a161,v45 offset:336
ds_read_u16 a162,v45 offset:544
ds_read_u16 a163,v47 offset:752
ds_read_u16 a164,v44 offset:136
ds_read_u16 a165,v45 offset:344
ds_read_u16 a166,v45 offset:552
ds_read_u16 a167,v47 offset:760
s_add_u32 m0,s57,60112
s_add_u32 s89,s56,1671168
buffer_load_dwordx4 v6,s[36:39],s89 offen lds
s_add_u32 m0,s58,18480
s_nop 0
buffer_load_dwordx4 v5,s[44:47],s69 offen offset:0 lds
s_mov_b64 exec,0
s_cmp_eq_u32 s52,0
s_cbranch_scc1 TE00_%=
s_cmp_eq_u32 s52,1
s_cbranch_scc1 TE01_%=
s_cmp_eq_u32 s52,2
s_cbranch_scc1 TE02_%=
s_branch TE03_%=
TR0_%=:
s_mov_b64 exec,-1
s_add_u32 s66,s66,16256
v_lshl_add_u32 v7,v24,3,s66
ds_write_b128 v50,v[56:59]
ds_write_b32 v55,v54 offset:14336
s_waitcnt vmcnt(16)
s_nop 0
v_and_b32 v160,0x7fff7fff,v160
v_and_b32 v161,0x7fff7fff,v161
v_and_b32 v162,0x7fff7fff,v162
v_and_b32 v163,0x7fff7fff,v163
v_and_b32 v164,0x7fff7fff,v164
v_and_b32 v165,0x7fff7fff,v165
v_and_b32 v166,0x7fff7fff,v166
v_and_b32 v167,0x7fff7fff,v167
v_and_b32 v168,0x7fff7fff,v168
v_and_b32 v169,0x7fff7fff,v169
v_and_b32 v170,0x7fff7fff,v170
ds_read_u16 a168,v44 offset:144
ds_read_u16 a169,v45 offset:352
ds_read_u16 a170,v45 offset:560
TE00_%=:
s_cbranch_execz TP2753_%=
ds_read_u16 a171,v47 offset:768
ds_read_u16 a172,v44 offset:152
ds_read_u16 a173,v45 offset:360
ds_read_u16 a174,v45 offset:568
ds_read_u16 a175,v47 offset:776
v_and_b32 v171,0x7fff7fff,v171
s_add_u32 s69,s69,s53
buffer_load_dwordx4 v5,s[44:47],s69 offen offset:1024 lds
v_pk_max_u16 v160,v160,v164
v_pk_max_u16 v161,v161,v165
v_pk_max_u16 v162,v162,v166
v_pk_max_u16 v163,v163,v167
v_mov_b64 v[164:165],v[160:161]
v_mov_b64 v[166:167],v[162:163]
s_nop 0
v_permlane32_swap_b32 v160,v164
v_permlane32_swap_b32 v161,v165
v_permlane32_swap_b32 v162,v166
v_permlane32_swap_b32_e64 v163,v167
v_pk_max_u16 v160,v160,v164
v_pk_max_u16 v161,v161,v165
v_pk_max_u16 v162,v162,v166
v_pk_max_u16 v163,v163,v167
v_mov_b64_e64 v[164:165],v[160:161]
ds_read_u16 a176,v44 offset:160
ds_read_u16 a177,v45 offset:368
ds_read_u16 a178,v45 offset:576
ds_read_u16 a179,v47 offset:784
ds_read_u16 a180,v44 offset:168
ds_read_u16 a181,v45 offset:376
ds_read_u16 a182,v45 offset:584
ds_read_u16 a183,v47 offset:792
v_mov_b64 v[166:167],v[162:163]
v_permlane16_swap_b32 v160,v164
v_permlane16_swap_b32 v161,v165
v_permlane16_swap_b32 v162,v166
v_permlane16_swap_b32_e64 v163,v167
v_pk_max_u16 v160,v160,v164
v_pk_max_u16 v161,v161,v165
v_pk_max_u16 v162,v162,v166
v_pk_max_u16 v163,v163,v167
v_pk_min_u16 v160,v160,v161
v_pk_min_u16 v162,v162,v163
v_pk_min_u16 v160,v160,v162
v_pk_min_u16 v160,v160,v160 op_sel:[0,1] op_sel_hi:[1,0]
v_pk_max_u16 v168,v168,v169
v_pk_max_u16 v170,v170,v171
s_add_u32 s69,s69,s53
s_nop 0
buffer_load_dwordx4 v5,s[44:47],s69 offen offset:2048 lds
v_pk_max_u16 v168,v168,v170
ds_read_u16 a184,v44 offset:176
ds_read_u16 a185,v45 offset:384
ds_read_u16 a186,v45 offset:592
ds_read_u16 a187,v47 offset:800
ds_read_u16 a188,v44 offset:184
ds_read_u16 a189,v45 offset:392
ds_read_u16 a190,v45 offset:600
ds_read_u16 a191,v47 offset:808
v_pk_max_u16 v168,v168,v168 op_sel:[0,1] op_sel_hi:[1,0]
v_cmp_gt_u16_e32 vcc,0x3880,v160
s_nop 0
s_or_b64 s[90:91],s[90:91],vcc
v_cmp_le_u16_e32 vcc,0x5380,v168
s_nop 0
s_or_b64 s[90:91],s[90:91],vcc
v_accvgpr_write_b32 a88,0
v_accvgpr_write_b32 a89,0
v_accvgpr_write_b32 a90,0
v_accvgpr_write_b32 a91,0
s_add_u32 m0,s58,0
s_mov_b32 s69,s87
buffer_load_dwordx4 v5,s[44:47],s69 offen offset:0 lds
s_add_u32 s69,s69,s53
buffer_load_dwordx4 v5,s[44:47],s69 offen offset:1024 lds
s_add_u32 s69,s69,s53
buffer_load_dwordx4 v5,s[44:47],s69 offen offset:2048 lds
s_waitcnt lgkmcnt(0)
s_waitcnt vmcnt(6)
s_barrier
v_add_u32_sdwa v28,s60,v10 dst_sel:DWORD dst_unused:UNUSED_PAD src0_sel:DWORD src1_sel:WORD_1
v_subrev_u32 v8,88400,v28
v_min_u32 v28,v28,v8
v_add_u32 v29,7072,v28
v_subrev_u32 v8,88400,v29
v_min_u32_e64 v29,v29,v8
v_xad_u32 v24,v11,0,v28
v_xad_u32 v25,v11,8,v29
v_xad_u32 v26,v11,16,v28
v_xad_u32 v27,v11,24,v29
ds_read_b64 a[0:1],v24 offset:0
ds_read_b64 a[2:3],v24 offset:192
ds_read_b64 a[4:5],v25 offset:0
ds_read_b64 a[6:7],v25 offset:192
ds_read_b64 a[8:9],v24 offset:8
ds_read_b64 a[10:11],v24 offset:200
ds_read_b64 a[12:13],v25 offset:8
ds_read_b64 a[14:15],v25 offset:200
ds_read_b64 a[16:17],v26 offset:0
ds_read_b64 a[18:19],v26 offset:192
ds_read_b64 a[20:21],v27 offset:0
ds_read_b64 a[22:23],v27 offset:192
s_waitcnt lgkmcnt(8)
v_smfmac_f32_16x16x64_bf16 v[64:67],a[96:99],a[0:7],v10
s_waitcnt lgkmcnt(4)
s_add_u32 m0,s58,6160
s_add_u32 s69,s69,s59
buffer_load_dwordx4 v5,s[44:47],s69 offen offset:0 lds
ds_read_b64 a[0:1],v26 offset:8
ds_read_b64 a[2:3],v26 offset:200
ds_read_b64 a[4:5],v27 offset:8
ds_read_b64 a[6:7],v27 offset:200
v_smfmac_f32_16x16x64_bf16 v[68:71],a[100:103],a[8:15],v10
s_waitcnt lgkmcnt(4)
s_add_u32 m0,s57,63656
s_add_u32 s89,s56,1769472
buffer_load_dwordx4 v6,s[36:39],s89 offen lds
ds_read_b64 a[8:9],v24 offset:64
ds_read_b64 a[10:11],v24 offset:256
ds_read_b64 a[12:13],v25 offset:64
ds_read_b64 a[14:15],v25 offset:256
v_smfmac_f32_16x16x64_bf16 v[72:75],a[104:107],a[16:23],v10
s_waitcnt lgkmcnt(4)
ds_read_b64 a[16:17],v24 offset:72
ds_read_b64 a[18:19],v24 offset:264
ds_read_b64 a[20:21],v25 offset:72
ds_read_b64 a[22:23],v25 offset:264
v_and_b32_e64 v248,8,v11
v_pk_mul_f32 v[40:41],v[64:65],v[64:65]
v_pk_mul_f32 v[42:43],v[66:67],v[66:67]
v_smfmac_f32_16x16x64_bf16 v[76:79],a[108:111],a[0:7],v10
s_waitcnt lgkmcnt(4)
ds_read_b64 a[0:1],v26 offset:64
ds_read_b64 a[2:3],v26 offset:256
ds_read_b64 a[4:5],v27 offset:64
ds_read_b64 a[6:7],v27 offset:256
v_mul_u32_u24 v248,884,v248
s_add_u32 s64,s60,3536
v_pk_add_f32 v[36:37],v[64:65],v[68:69]
v_pk_fma_f32 v[40:41],v[68:69],v[68:69],v[40:41]
v_pk_add_f32 v[38:39],v[66:67],v[70:71]
v_pk_fma_f32 v[42:43],v[70:71],v[70:71],v[42:43]
v_smfmac_f32_16x16x64_bf16 v[80:83],a[128:131],a[8:15],v10
s_waitcnt lgkmcnt(4)
s_add_u32 m0,s57,67192
s_add_u32 s89,s56,1867776
buffer_load_dwordx4 v6,s[36:39],s89 offen lds
ds_read_b64 a[8:9],v26 offset:72
ds_read_b64 a[10:11],v26 offset:264
ds_read_b64 a[12:13],v27 offset:72
ds_read_b64 a[14:15],v27 offset:264
v_add_u32 v251,s64,v248
v_pk_add_f32 v[36:37],v[36:37],v[72:73]
v_pk_fma_f32 v[40:41],v[72:73],v[72:73],v[40:41]
v_pk_add_f32 v[38:39],v[38:39],v[74:75]
v_pk_fma_f32 v[42:43],v[74:75],v[74:75],v[42:43]
v_smfmac_f32_16x16x64_bf16 v[84:87],a[132:135],a[16:23],v10
s_waitcnt lgkmcnt(4)
s_nop 0
ds_read_b64 a[16:17],v24 offset:128
ds_read_b64 a[18:19],v24 offset:320
ds_read_b64 a[20:21],v25 offset:128
ds_read_b64 a[22:23],v25 offset:320
v_subrev_u32 v8,88400,v251
v_pk_add_f32 v[36:37],v[36:37],v[76:77]
v_pk_fma_f32 v[40:41],v[76:77],v[76:77],v[40:41]
v_pk_add_f32 v[38:39],v[38:39],v[78:79]
v_pk_fma_f32 v[42:43],v[78:79],v[78:79],v[42:43]
v_smfmac_f32_16x16x64_bf16 v[88:91],a[136:139],a[0:7],v10
s_waitcnt lgkmcnt(4)
ds_read_b64 a[0:1],v24 offset:136
ds_read_b64 a[2:3],v24 offset:328
ds_read_b64 a[4:5],v25 offset:136
ds_read_b64 a[6:7],v25 offset:328
v_min_u32 v251,v251,v8
v_pk_add_f32 v[36:37],v[36:37],v[80:81]
v_pk_fma_f32 v[40:41],v[80:81],v[80:81],v[40:41]
v_pk_add_f32 v[38:39],v[38:39],v[82:83]
v_pk_fma_f32 v[42:43],v[82:83],v[82:83],v[42:43]
v_smfmac_f32_16x16x64_bf16 v[92:95],a[140:143],a[8:15],v10
s_waitcnt lgkmcnt(4)
s_add_u32 m0,s58,6160
s_add_u32 s69,s69,s53
buffer_load_dwordx4 v5,s[44:47],s69 offen offset:1024 lds
ds_read_b64 a[8:9],v26 offset:128
ds_read_b64 a[10:11],v26 offset:320
ds_read_b64 a[12:13],v27 offset:128
ds_read_b64 a[14:15],v27 offset:320
v_xad_u32 v24,v11,32,v28
v_xad_u32 v25,v11,40,v29
v_pk_add_f32 v[36:37],v[36:37],v[84:85]
v_pk_fma_f32 v[40:41],v[84:85],v[84:85],v[40:41]
v_pk_add_f32 v[38:39],v[38:39],v[86:87]
v_pk_fma_f32 v[42:43],v[86:87],v[86:87],v[42:43]
v_smfmac_f32_16x16x64_bf16 v[96:99],a[160:163],a[16:23],v10
s_waitcnt lgkmcnt(4)
ds_read_b64 a[16:17],v26 offset:136
ds_read_b64 a[18:19],v26 offset:328
ds_read_b64 a[20:21],v27 offset:136
ds_read_b64 a[22:23],v27 offset:328
v_add_u32 v250,3536,v251
v_pk_add_f32 v[36:37],v[36:37],v[88:89]
v_pk_fma_f32 v[40:41],v[88:89],v[88:89],v[40:41]
v_pk_add_f32 v[38:39],v[38:39],v[90:91]
v_pk_fma_f32 v[42:43],v[90:91],v[90:91],v[42:43]
v_smfmac_f32_16x16x64_bf16 v[100:103],a[164:167],a[0:7],v10
s_waitcnt lgkmcnt(4)
s_add_u32 m0,s57,70720
s_add_u32 s89,s56,1966080
buffer_load_dwordx4 v6,s[36:39],s89 offen lds
ds_read_b64 a[0:1],v24 offset:0
ds_read_b64 a[2:3],v24 offset:192
ds_read_b64 a[4:5],v25 offset:0
ds_read_b64 a[6:7],v25 offset:192
v_xad_u32 v26,v11,48,v28
v_xad_u32 v27,v11,56,v29
v_pk_add_f32 v[36:37],v[36:37],v[92:93]
v_pk_fma_f32 v[40:41],v[92:93],v[92:93],v[40:41]
v_pk_add_f32 v[38:39],v[38:39],v[94:95]
v_pk_fma_f32 v[42:43],v[94:95],v[94:95],v[42:43]
v_smfmac_f32_16x16x64_bf16 v[104:107],a[168:171],a[8:15],v10
s_waitcnt lgkmcnt(4)
s_nop 0
ds_read_b64 a[8:9],v24 offset:8
ds_read_b64 a[10:11],v24 offset:200
ds_read_b64 a[12:13],v25 offset:8
ds_read_b64 a[14:15],v25 offset:200
v_subrev_u32 v8,88400,v250
v_pk_add_f32 v[36:37],v[36:37],v[96:97]
v_pk_fma_f32 v[40:41],v[96:97],v[96:97],v[40:41]
v_pk_add_f32 v[38:39],v[38:39],v[98:99]
v_pk_fma_f32 v[42:43],v[98:99],v[98:99],v[42:43]
v_smfmac_f32_16x16x64_bf16 v[108:111],a[172:175],a[16:23],v10
s_waitcnt lgkmcnt(4)
ds_read_b64 a[16:17],v24 offset:64
ds_read_b64 a[18:19],v24 offset:256
ds_read_b64 a[20:21],v25 offset:64
ds_read_b64 a[22:23],v25 offset:256
v_min_u32 v250,v250,v8
v_pk_add_f32 v[36:37],v[36:37],v[100:101]
v_pk_fma_f32 v[40:41],v[100:101],v[100:101],v[40:41]
v_pk_add_f32 v[38:39],v[38:39],v[102:103]
v_pk_fma_f32 v[42:43],v[102:103],v[102:103],v[42:43]
v_smfmac_f32_16x16x64_bf16 v[112:115],a[112:115],a[0:7],v10
s_waitcnt lgkmcnt(4)
s_add_u32 m0,s58,6160
s_add_u32 s69,s69,s53
buffer_load_dwordx4 v5,s[44:47],s69 offen offset:2048 lds
ds_read_b64 a[0:1],v24 offset:72
ds_read_b64 a[2:3],v24 offset:264
ds_read_b64 a[4:5],v25 offset:72
ds_read_b64 a[6:7],v25 offset:264
v_xor_b32 v9,0x80008,v12
v_pk_add_f32 v[36:37],v[36:37],v[104:105]
v_pk_fma_f32 v[40:41],v[104:105],v[104:105],v[40:41]
v_pk_add_f32 v[38:39],v[38:39],v[106:107]
v_pk_fma_f32 v[42:43],v[106:107],v[106:107],v[42:43]
v_smfmac_f32_16x16x64_bf16 v[116:119],a[116:119],a[8:15],v10
s_waitcnt lgkmcnt(4)
ds_read_b64 a[8:9],v24 offset:128
ds_read_b64 a[10:11],v24 offset:320
ds_read_b64 a[12:13],v25 offset:128
ds_read_b64 a[14:15],v25 offset:320
v_add_u32_sdwa v248,v12,v251 dst_sel:DWORD dst_unused:UNUSED_PAD src0_sel:WORD_0 src1_sel:DWORD
v_pk_add_f32 v[36:37],v[36:37],v[108:109]
v_pk_fma_f32 v[40:41],v[108:109],v[108:109],v[40:41]
v_pk_add_f32 v[38:39],v[38:39],v[110:111]
v_pk_fma_f32 v[42:43],v[110:111],v[110:111],v[42:43]
v_smfmac_f32_16x16x64_bf16 v[120:123],a[144:147],a[16:23],v10
s_waitcnt lgkmcnt(4)
s_add_u32 m0,s57,74256
s_add_u32 s89,s56,2064384
buffer_load_dwordx4 v6,s[36:39],s89 offen lds
ds_read_b64 a[16:17],v24 offset:136
ds_read_b64 a[18:19],v24 offset:328
ds_read_b64 a[20:21],v25 offset:136
ds_read_b64 a[22:23],v25 offset:328
v_add_u32_sdwa v249,v12,v251 dst_sel:DWORD dst_unused:UNUSED_PAD src0_sel:WORD_1 src1_sel:DWORD
v_pk_add_f32 v[36:37],v[36:37],v[112:113]
v_pk_fma_f32 v[40:41],v[112:113],v[112:113],v[40:41]
v_pk_add_f32 v[38:39],v[38:39],v[114:115]
v_pk_fma_f32 v[42:43],v[114:115],v[114:115],v[42:43]
v_smfmac_f32_16x16x64_bf16 v[124:127],a[148:151],a[0:7],v10
s_waitcnt lgkmcnt(4)
ds_read_b64 a[0:1],v26 offset:0
ds_read_b64 a[2:3],v26 offset:192
ds_read_b64 a[4:5],v27 offset:0
ds_read_b64 a[6:7],v27 offset:192
v_add_u32_sdwa v251,v9,v250 dst_sel:DWORD dst_unused:UNUSED_PAD src0_sel:WORD_1 src1_sel:DWORD
v_pk_add_f32 v[36:37],v[36:37],v[116:117]
v_pk_fma_f32 v[40:41],v[116:117],v[116:117],v[40:41]
v_pk_add_f32 v[38:39],v[38:39],v[118:119]
v_pk_fma_f32 v[42:43],v[118:119],v[118:119],v[42:43]
v_smfmac_f32_16x16x64_bf16 v[128:131],a[176:179],a[8:15],v10
s_waitcnt lgkmcnt(4)
ds_read_b64 a[8:9],v26 offset:8
ds_read_b64 a[10:11],v26 offset:200
ds_read_b64 a[12:13],v27 offset:8
ds_read_b64 a[14:15],v27 offset:200
v_add_u32_sdwa v250,v9,v250 dst_sel:DWORD dst_unused:UNUSED_PAD src0_sel:WORD_0 src1_sel:DWORD
v_pk_add_f32 v[36:37],v[36:37],v[120:121]
v_pk_fma_f32 v[40:41],v[120:121],v[120:121],v[40:41]
v_pk_add_f32 v[38:39],v[38:39],v[122:123]
v_pk_fma_f32 v[42:43],v[122:123],v[122:123],v[42:43]
v_smfmac_f32_16x16x64_bf16 v[132:135],a[180:183],a[16:23],v10
s_waitcnt lgkmcnt(4)
s_add_u32 m0,s58,12320
s_add_u32 s69,s69,s59
buffer_load_dwordx4 v5,s[44:47],s69 offen offset:0 lds
ds_read_b64 a[16:17],v26 offset:64
ds_read_b64 a[18:19],v26 offset:256
ds_read_b64 a[20:21],v27 offset:64
ds_read_b64 a[22:23],v27 offset:256
v_pk_add_f32 v[36:37],v[36:37],v[124:125]
v_pk_fma_f32 v[40:41],v[124:125],v[124:125],v[40:41]
v_pk_add_f32 v[38:39],v[38:39],v[126:127]
v_pk_fma_f32 v[42:43],v[126:127],v[126:127],v[42:43]
v_smfmac_f32_16x16x64_bf16 v[136:139],a[120:123],a[0:7],v10
s_waitcnt lgkmcnt(4)
ds_read_b64 a[0:1],v26 offset:72
ds_read_b64 a[2:3],v26 offset:264
ds_read_b64 a[4:5],v27 offset:72
ds_read_b64 a[6:7],v27 offset:264
v_pk_add_f32 v[36:37],v[36:37],v[128:129]
v_pk_fma_f32 v[40:41],v[128:129],v[128:129],v[40:41]
v_pk_add_f32 v[38:39],v[38:39],v[130:131]
v_pk_fma_f32 v[42:43],v[130:131],v[130:131],v[42:43]
v_smfmac_f32_16x16x64_bf16 v[140:143],a[124:127],a[8:15],v10
s_waitcnt lgkmcnt(4)
s_add_u32 m0,s57,77800
s_add_u32 s89,s56,2162688
buffer_load_dwordx4 v6,s[36:39],s89 offen lds
ds_read_b64 a[8:9],v26 offset:128
ds_read_b64 a[10:11],v26 offset:320
ds_read_b64 a[12:13],v27 offset:128
ds_read_b64 a[14:15],v27 offset:320
v_pk_add_f32 v[36:37],v[36:37],v[132:133]
v_pk_fma_f32 v[40:41],v[132:133],v[132:133],v[40:41]
v_pk_add_f32 v[38:39],v[38:39],v[134:135]
v_pk_fma_f32 v[42:43],v[134:135],v[134:135],v[42:43]
v_smfmac_f32_16x16x64_bf16 v[144:147],a[152:155],a[16:23],v10
s_waitcnt lgkmcnt(4)
ds_read_b64 a[16:17],v26 offset:136
ds_read_b64 a[18:19],v26 offset:328
ds_read_b64 a[20:21],v27 offset:136
ds_read_b64 a[22:23],v27 offset:328
v_pk_add_f32 v[36:37],v[36:37],v[136:137]
v_pk_fma_f32 v[40:41],v[136:137],v[136:137],v[40:41]
v_pk_add_f32 v[38:39],v[38:39],v[138:139]
v_pk_fma_f32 v[42:43],v[138:139],v[138:139],v[42:43]
v_smfmac_f32_16x16x64_bf16 v[148:151],a[156:159],a[0:7],v10
s_waitcnt lgkmcnt(4)
ds_read_b64 a[24:25],v248 offset:57344
ds_read_b64 a[26:27],v248 offset:57352
ds_read_b64 a[36:37],v249 offset:57344
ds_read_b64 a[38:39],v249 offset:57352
v_pk_add_f32 v[36:37],v[36:37],v[140:141]
v_pk_fma_f32 v[40:41],v[140:141],v[140:141],v[40:41]
v_pk_add_f32 v[38:39],v[38:39],v[142:143]
v_pk_fma_f32 v[42:43],v[142:143],v[142:143],v[42:43]
v_smfmac_f32_16x16x64_bf16 v[152:155],a[184:187],a[8:15],v10
s_waitcnt lgkmcnt(4)
s_add_u32 m0,s58,12320
s_add_u32 s69,s69,s53
buffer_load_dwordx4 v5,s[44:47],s69 offen offset:1024 lds
ds_read_b64 a[28:29],v248 offset:57408
ds_read_b64 a[30:31],v248 offset:57416
ds_read_b64 a[40:41],v249 offset:57408
ds_read_b64 a[42:43],v249 offset:57416
v_pk_add_f32 v[36:37],v[36:37],v[144:145]
v_pk_fma_f32 v[40:41],v[144:145],v[144:145],v[40:41]
v_pk_add_f32 v[38:39],v[38:39],v[146:147]
v_pk_fma_f32 v[42:43],v[146:147],v[146:147],v[42:43]
v_smfmac_f32_16x16x64_bf16 v[156:159],a[188:191],a[16:23],v10
ds_read_b64 a[32:33],v248 offset:57472
ds_read_b64 a[34:35],v248 offset:57480
ds_read_b64 a[44:45],v249 offset:57472
ds_read_b64 a[46:47],v249 offset:57480
v_pk_add_f32 v[36:37],v[36:37],v[148:149]
v_pk_fma_f32 v[40:41],v[148:149],v[148:149],v[40:41]
v_pk_add_f32 v[38:39],v[38:39],v[150:151]
v_pk_fma_f32 v[42:43],v[150:151],v[150:151],v[42:43]
v_pk_add_f32 v[36:37],v[36:37],v[152:153]
v_pk_fma_f32 v[40:41],v[152:153],v[152:153],v[40:41]
v_pk_add_f32 v[38:39],v[38:39],v[154:155]
v_pk_fma_f32 v[42:43],v[154:155],v[154:155],v[42:43]
v_pk_add_f32 v[36:37],v[36:37],v[156:157]
v_pk_fma_f32 v[40:41],v[156:157],v[156:157],v[40:41]
v_pk_add_f32 v[38:39],v[38:39],v[158:159]
v_pk_fma_f32 v[42:43],v[158:159],v[158:159],v[42:43]
s_nop 0
v_permlane32_swap_b32 v36,v38
v_permlane32_swap_b32 v37,v39
v_permlane32_swap_b32 v40,v42
v_permlane32_swap_b32_e64 v41,v43
v_pk_add_f32 v[36:37],v[36:37],v[38:39]
v_pk_add_f32 v[40:41],v[40:41],v[42:43]
s_add_u32 s60,s60,56576
s_sub_u32 s64,s60,88400
s_min_u32 s60,s60,s64
v_permlane16_swap_b32 v36,v37
v_permlane16_swap_b32 v40,v41
v_add_f32 v8,v36,v37
v_add_f32_e64 v9,v40,v41
v_mul_f32 v8,0x3c2aaaab,v8
v_mul_f32 v9,0x3c2aaaab,v9
v_cmp_ngt_f32 vcc,0x5d800000,v9
v_fma_f32 v9,-v8,v8,v9
v_max_f32 v9,0,v9
s_or_b64 s[90:91],s[90:91],vcc
v_add_f32 v9,0x3727c5ac,v9
v_rsq_f32 v9,v9
s_nop 0
v_mul_f32 v36,s96,v9
v_mul_f32 v37,s96,v9
v_mul_f32 v40,-v8,v36
v_mul_f32 v41,-v8,v36
v_permlane16_swap_b32 v36,v37
s_nop 0
v_permlane16_swap_b32 v40,v41
v_mov_b64 v[38:39],v[36:37]
v_mov_b64 v[42:43],v[40:41]
s_nop 0
v_permlane32_swap_b32 v36,v38
v_permlane32_swap_b32 v37,v39
v_permlane32_swap_b32 v40,v42
v_permlane32_swap_b32 v41,v43
s_waitcnt vmcnt(10)
s_barrier
ds_read_b64_tr_b16 a[192:193],v1 offset:18480
ds_read_b64_tr_b16 a[194:195],v1 offset:19504
ds_read_b64_tr_b16 a[196:197],v1 offset:20528
ds_read_b64_tr_b16 a[198:199],v1 offset:21552
v_pk_fma_f32 v[64:65],v[64:65],v[36:37],v[40:41]
v_pk_fma_f32 v[66:67],v[66:67],v[38:39],v[42:43]
v_pk_fma_f32 v[68:69],v[68:69],v[36:37],v[40:41]
v_pk_fma_f32 v[70:71],v[70:71],v[38:39],v[42:43]
v_pk_fma_f32 v[72:73],v[72:73],v[36:37],v[40:41]
v_pk_fma_f32 v[74:75],v[74:75],v[38:39],v[42:43]
v_pk_fma_f32 v[76:77],v[76:77],v[36:37],v[40:41]
v_pk_fma_f32 v[78:79],v[78:79],v[38:39],v[42:43]
v_pk_fma_f32 v[112:113],v[112:113],v[36:37],v[40:41]
v_pk_fma_f32 v[114:115],v[114:115],v[38:39],v[42:43]
ds_read_b64_tr_b16 a[200:201],v1 offset:22576
ds_read_b64_tr_b16 a[202:203],v1 offset:23600
ds_read_b64_tr_b16 a[204:205],v2 offset:22576
ds_read_b64_tr_b16 a[206:207],v2 offset:23600
v_pk_fma_f32 v[116:117],v[116:117],v[36:37],v[40:41]
v_pk_fma_f32 v[118:119],v[118:119],v[38:39],v[42:43]
s_add_u32 m0,s57,81336
s_add_u32 s89,s56,2260992
buffer_load_dwordx4 v6,s[36:39],s89 offen lds
v_pk_fma_f32 v[136:137],v[136:137],v[36:37],v[40:41]
v_pk_fma_f32 v[138:139],v[138:139],v[38:39],v[42:43]
v_pk_fma_f32 v[140:141],v[140:141],v[36:37],v[40:41]
v_pk_fma_f32 v[142:143],v[142:143],v[38:39],v[42:43]
v_pk_fma_f32 v[80:81],v[80:81],v[36:37],v[40:41]
v_pk_fma_f32 v[82:83],v[82:83],v[38:39],v[42:43]
v_pk_fma_f32 v[84:85],v[84:85],v[36:37],v[40:41]
v_pk_fma_f32 v[86:87],v[86:87],v[38:39],v[42:43]
ds_read_b64_tr_b16 a[208:209],v2 offset:20528
ds_read_b64_tr_b16 a[210:211],v2 offset:21552
ds_read_b64_tr_b16 a[212:213],v2 offset:18480
ds_read_b64_tr_b16 a[214:215],v2 offset:19504
v_pk_fma_f32 v[88:89],v[88:89],v[36:37],v[40:41]
v_pk_fma_f32 v[90:91],v[90:91],v[38:39],v[42:43]
v_pk_fma_f32 v[92:93],v[92:93],v[36:37],v[40:41]
v_pk_fma_f32 v[94:95],v[94:95],v[38:39],v[42:43]
s_add_u32 m0,s58,12320
s_add_u32 s69,s69,s53
s_nop 0
buffer_load_dwordx4 v5,s[44:47],s69 offen offset:2048 lds
v_pk_fma_f32 v[120:121],v[120:121],v[36:37],v[40:41]
v_pk_fma_f32 v[122:123],v[122:123],v[38:39],v[42:43]
v_pk_fma_f32 v[124:125],v[124:125],v[36:37],v[40:41]
v_pk_fma_f32 v[126:127],v[126:127],v[38:39],v[42:43]
v_pk_fma_f32 v[144:145],v[144:145],v[36:37],v[40:41]
v_pk_fma_f32 v[146:147],v[146:147],v[38:39],v[42:43]
ds_read_b128 v[16:19],v4 offset:0
ds_read_b128 v[20:23],v4 offset:32
ds_read_b64 v[232:233],v7 offset:18480
ds_read_b64 v[234:235],v7 offset:18992
v_pk_fma_f32 v[148:149],v[148:149],v[36:37],v[40:41]
v_pk_fma_f32 v[150:151],v[150:151],v[38:39],v[42:43]
v_pk_fma_f32 v[96:97],v[96:97],v[36:37],v[40:41]
v_pk_fma_f32 v[98:99],v[98:99],v[38:39],v[42:43]
v_pk_fma_f32 v[100:101],v[100:101],v[36:37],v[40:41]
v_pk_fma_f32 v[102:103],v[102:103],v[38:39],v[42:43]
s_add_u32 m0,s57,84864
s_add_u32 s89,s56,2359296
buffer_load_dwordx4 v6,s[36:39],s89 offen lds
v_pk_fma_f32 v[104:105],v[104:105],v[36:37],v[40:41]
v_pk_fma_f32 v[106:107],v[106:107],v[38:39],v[42:43]
v_pk_fma_f32 v[108:109],v[108:109],v[36:37],v[40:41]
v_pk_fma_f32 v[110:111],v[110:111],v[38:39],v[42:43]
ds_read_b64 v[236:237],v7 offset:19504
v_pk_fma_f32 v[128:129],v[128:129],v[36:37],v[40:41]
v_pk_fma_f32 v[130:131],v[130:131],v[38:39],v[42:43]
v_pk_fma_f32 v[132:133],v[132:133],v[36:37],v[40:41]
v_pk_fma_f32 v[134:135],v[134:135],v[38:39],v[42:43]
v_pk_fma_f32 v[152:153],v[152:153],v[36:37],v[40:41]
v_pk_fma_f32 v[154:155],v[154:155],v[38:39],v[42:43]
v_pk_fma_f32 v[156:157],v[156:157],v[36:37],v[40:41]
v_pk_fma_f32 v[158:159],v[158:159],v[38:39],v[42:43]
v_cvt_pk_bf16_f32 v160,v64,v68
v_cvt_pk_bf16_f32 v161,v72,v76
v_cvt_pk_bf16_f32 v162,v112,v116
v_cvt_pk_bf16_f32 v163,v136,v140
v_cvt_pk_bf16_f32 v164,v65,v69
v_cvt_pk_bf16_f32 v165,v73,v77
v_cvt_pk_bf16_f32 v166,v113,v117
v_cvt_pk_bf16_f32 v167,v137,v141
v_cvt_pk_bf16_f32 v176,v80,v84
v_cvt_pk_bf16_f32 v177,v88,v92
v_cvt_pk_bf16_f32 v178,v120,v124
v_cvt_pk_bf16_f32 v179,v144,v148
v_cvt_pk_bf16_f32 v180,v81,v85
v_cvt_pk_bf16_f32 v181,v89,v93
v_cvt_pk_bf16_f32 v182,v121,v125
v_cvt_pk_bf16_f32 v183,v145,v149
v_cvt_pk_bf16_f32 v192,v96,v100
v_cvt_pk_bf16_f32 v193,v104,v108
v_cvt_pk_bf16_f32 v194,v128,v132
v_cvt_pk_bf16_f32 v195,v152,v156
v_cvt_pk_bf16_f32 v196,v97,v101
v_cvt_pk_bf16_f32 v197,v105,v109
v_cvt_pk_bf16_f32 v198,v129,v133
v_cvt_pk_bf16_f32 v199,v153,v157
s_waitcnt lgkmcnt(0)
v_or3_b32 v252,v232,v233,0
v_or3_b32 v252,v234,v235,v252
v_or3_b32 v252,v236,v237,v252
v_mfma_f32_16x16x32_bf16 v[44:47],a[192:195],v[160:163],v[16:19]
ds_read_b128 a[48:51],v13 offset:0
v_cvt_pk_bf16_f32 v168,v66,v70
v_cvt_pk_bf16_f32 v169,v74,v78
v_mfma_f32_16x16x32_bf16 v[44:47],a[196:199],v[176:179],v[44:47]
ds_read_b128 v[208:211],v4 offset:3584
v_cvt_pk_bf16_f32 v170,v114,v118
v_cvt_pk_bf16_f32 v171,v138,v142
v_mfma_f32_16x16x32_bf16 v[44:47],a[200:203],v[192:195],v[44:47]
ds_read_b128 a[52:55],v13 offset:256
v_cvt_pk_bf16_f32 v172,v67,v71
v_cvt_pk_bf16_f32 v173,v75,v79
v_mfma_f32_16x16x32_bf16 v[48:51],a[204:207],v[192:195],v[20:23]
ds_read_b128 v[212:215],v4 offset:3616
v_cvt_pk_bf16_f32 v174,v115,v119
v_cvt_pk_bf16_f32 v175,v139,v143
v_mfma_f32_16x16x32_bf16 v[48:51],a[208:211],v[176:179],v[48:51]
ds_read_b128 a[56:59],v13 offset:512
v_cvt_pk_bf16_f32 v184,v82,v86
v_cvt_pk_bf16_f32 v185,v90,v94
v_mfma_f32_16x16x32_bf16 v[48:51],a[212:215],v[160:163],v[48:51]
ds_read_b128 v[216:219],v4 offset:3712
v_cvt_pk_bf16_f32 v186,v122,v126
v_cvt_pk_bf16_f32 v187,v146,v150
v_mfma_f32_16x16x32_bf16 v[52:55],a[212:215],v[164:167],v[20:23]
TE01_%=:
s_cbranch_execz TP3285_%=
ds_read_b128 a[60:63],v13 offset:768
v_cvt_pk_bf16_f32 v188,v83,v87
v_cvt_pk_bf16_f32 v189,v91,v95
v_mfma_f32_16x16x32_bf16 v[52:55],a[208:211],v[180:183],v[52:55]
ds_read_b128 v[220:223],v4 offset:3744
v_cvt_pk_bf16_f32 v190,v123,v127
v_cvt_pk_bf16_f32 v191,v147,v151
v_mfma_f32_16x16x32_bf16 v[52:55],a[204:207],v[196:199],v[52:55]
ds_read_b128 a[64:67],v13 offset:1024
v_cvt_pk_bf16_f32 v200,v98,v102
v_cvt_pk_bf16_f32 v201,v106,v110
v_mfma_f32_16x16x32_bf16 v[56:59],a[200:203],v[196:199],v[16:19]
ds_read_b128 v[224:227],v4 offset:3840
v_cvt_pk_bf16_f32 v202,v130,v134
v_cvt_pk_bf16_f32 v203,v154,v158
v_mfma_f32_16x16x32_bf16 v[56:59],a[196:199],v[180:183],v[56:59]
ds_read_b128 a[68:71],v13 offset:1280
v_cvt_pk_bf16_f32 v204,v99,v103
v_cvt_pk_bf16_f32 v205,v107,v111
v_mfma_f32_16x16x32_bf16 v[56:59],a[192:195],v[164:167],v[56:59]
ds_read_b128 v[228:231],v4 offset:3872
v_cvt_pk_bf16_f32 v206,v131,v135
v_cvt_pk_bf16_f32 v207,v155,v159
v_mfma_f32_16x16x32_bf16 v[60:63],a[192:195],v[168:171],v[16:19]
ds_read_b64 v[14:15],v7 offset:0
ds_read_b64 a[0:1],v250 offset:57344
v_lshlrev_b32_sdwa v44,1,v44 dst_sel:DWORD dst_unused:UNUSED_PAD src0_sel:DWORD src1_sel:WORD_1
v_mfma_f32_16x16x32_bf16 v[60:63],a[196:199],v[184:187],v[60:63]
ds_read_b64 a[2:3],v250 offset:57352
ds_read_b64 a[12:13],v251 offset:57344
v_fma_mixlo_f16 v8,v53,s99,0
v_mfma_f32_16x16x32_bf16 v[60:63],a[200:203],v[200:203],v[60:63]
ds_read_b64 a[14:15],v251 offset:57352
ds_read_b64 a[4:5],v250 offset:57408
v_lshlrev_b32_sdwa v46,1,v46 dst_sel:DWORD dst_unused:UNUSED_PAD src0_sel:DWORD src1_sel:WORD_1
v_mfma_f32_16x16x32_bf16 v[32:35],a[204:207],v[200:203],v[20:23]
ds_read_b64 a[6:7],v250 offset:57416
ds_read_b64 a[16:17],v251 offset:57408
v_fma_mixhi_f16 v8,v55,s99,0
v_mfma_f32_16x16x32_bf16 v[32:35],a[208:211],v[184:187],v[32:35]
ds_read_b64 a[18:19],v251 offset:57416
ds_read_b64 a[8:9],v250 offset:57472
v_lshlrev_b32_sdwa v45,1,v45 dst_sel:DWORD dst_unused:UNUSED_PAD src0_sel:DWORD src1_sel:WORD_1
v_pk_fma_f16 v9,v8,v8,s54
v_mfma_f32_16x16x32_bf16 v[32:35],a[212:215],v[168:171],v[32:35]
s_waitcnt lgkmcnt(9)
ds_read_b64 a[10:11],v250 offset:57480
ds_read_b64 a[20:21],v251 offset:57472
v_or3_b32 v252,v14,v15,v252
v_lshlrev_b32_sdwa v47,1,v47 dst_sel:DWORD dst_unused:UNUSED_PAD src0_sel:DWORD src1_sel:WORD_1
v_mfma_f32_16x16x32_bf16 v[36:39],a[212:215],v[172:175],v[20:23]
ds_read_b64 v[14:15],v7 offset:512
ds_read_b128 v[20:23],v4 offset:128
v_pk_mul_f16 v9,v8,v9
v_mfma_f32_16x16x32_bf16 v[36:39],a[208:211],v[188:191],v[36:39]
ds_read_b64 a[22:23],v251 offset:57480
v_exp_f16_e64 v8,v9
v_lshlrev_b32_sdwa v48,1,v48 dst_sel:DWORD dst_unused:UNUSED_PAD src0_sel:DWORD src1_sel:WORD_1
v_mfma_f32_16x16x32_bf16 v[36:39],a[204:207],v[204:207],v[36:39]
ds_read_b64_tr_b16 a[216:217],v1 offset:0
ds_read_b64_tr_b16 a[218:219],v1 offset:1024
v_mfma_f32_16x16x32_bf16 v[40:43],a[200:203],v[204:207],v[16:19]
ds_read_b128 v[16:19],v4 offset:160
ds_read_b64_tr_b16 a[220:221],v1 offset:2048
v_lshlrev_b32_sdwa v50,1,v50 dst_sel:DWORD dst_unused:UNUSED_PAD src0_sel:DWORD src1_sel:WORD_1
v_mfma_f32_16x16x32_bf16 v[40:43],a[196:199],v[188:191],v[40:43]
ds_read_b64_tr_b16 a[222:223],v1 offset:3072
ds_read_u16 v44,v44
v_lshlrev_b32_sdwa v49,1,v49 dst_sel:DWORD dst_unused:UNUSED_PAD src0_sel:DWORD src1_sel:WORD_1
v_mfma_f32_16x16x32_bf16 v[40:43],a[192:195],v[172:175],v[40:43]
s_waitcnt lgkmcnt(8)
ds_read_u16_d16_hi v46,v46
ds_read_u16 v45,v45
v_or3_b32 v252,v14,v15,v252
v_mfma_f32_16x16x32_bf16 v[136:139],a[48:51],a[24:27],v[208:211]
ds_read_b64 v[14:15],v7 offset:1024
ds_read_u16_d16_hi v47,v47
v_exp_f16_sdwa v8,v9 dst_sel:WORD_1 dst_unused:UNUSED_PRESERVE src0_sel:WORD_1
v_lshlrev_b32_sdwa v51,1,v51 dst_sel:DWORD dst_unused:UNUSED_PAD src0_sel:DWORD src1_sel:WORD_1
v_mfma_f32_16x16x32_bf16 v[140:143],a[48:51],a[36:39],v[208:211]
ds_read_b64_tr_b16 a[72:73],v1 offset:4096
ds_read_b64_tr_b16 a[74:75],v1 offset:5120
v_lshlrev_b32_sdwa v56,1,v56 dst_sel:DWORD dst_unused:UNUSED_PAD src0_sel:DWORD src1_sel:WORD_1
v_mfma_f32_16x16x32_bf16 v[68:71],a[52:55],a[36:39],v[212:215]
ds_read_u16 v48,v48
ds_read_u16_d16_hi v50,v50
v_pk_fma_f16 v8,v8,s55,s55
v_mfma_f32_16x16x32_bf16 v[64:67],a[52:55],a[24:27],v[212:215]
ds_read_u16 v49,v49
v_rcp_f16 v9,v8
v_lshlrev_b32_sdwa v58,1,v58 dst_sel:DWORD dst_unused:UNUSED_PAD src0_sel:DWORD src1_sel:WORD_1
v_mfma_f32_16x16x32_bf16 v[144:147],a[56:59],a[28:31],v[216:219]
ds_read_u16_d16_hi v51,v51
ds_read_b64_tr_b16 a[76:77],v2 offset:4096
v_mfma_f32_16x16x32_bf16 v[148:151],a[56:59],a[40:43],v[216:219]
s_waitcnt lgkmcnt(8)
ds_read_b64_tr_b16 a[78:79],v2 offset:5120
ds_read_b64_tr_b16 a[80:81],v2 offset:2048
v_or3_b32 v252,v14,v15,v252
v_lshlrev_b32_sdwa v57,1,v57 dst_sel:DWORD dst_unused:UNUSED_PAD src0_sel:DWORD src1_sel:WORD_1
v_mfma_f32_16x16x32_bf16 v[84:87],a[60:63],a[40:43],v[220:223]
ds_read_b64_tr_b16 a[82:83],v2 offset:3072
ds_read_u16 v56,v56
v_lshlrev_b32_sdwa v59,1,v59 dst_sel:DWORD dst_unused:UNUSED_PAD src0_sel:DWORD src1_sel:WORD_1
v_mfma_f32_16x16x32_bf16 v[80:83],a[60:63],a[28:31],v[220:223]
ds_read_u16_d16_hi v58,v58
v_rcp_f16_sdwa v9,v8 dst_sel:WORD_1 dst_unused:UNUSED_PRESERVE src0_sel:WORD_1
v_mfma_f32_16x16x32_bf16 v[152:155],a[64:67],a[32:35],v[224:227]
ds_read_u16 v57,v57
ds_read_u16_d16_hi v59,v59
v_lshlrev_b32_sdwa v52,1,v52 dst_sel:DWORD dst_unused:UNUSED_PAD src0_sel:DWORD src1_sel:WORD_1
v_mfma_f32_16x16x32_bf16 v[156:159],a[64:67],a[44:47],v[224:227]
ds_read_b64_tr_b16 a[84:85],v2 offset:0
ds_read_b64_tr_b16 a[86:87],v2 offset:1024
v_lshlrev_b32_sdwa v54,1,v54 dst_sel:DWORD dst_unused:UNUSED_PAD src0_sel:DWORD src1_sel:WORD_1
v_mfma_f32_16x16x32_bf16 v[100:103],a[68:71],a[44:47],v[228:231]
s_waitcnt vmcnt(7)
ds_read_u16 v52,v52
ds_read_u16_d16_hi v54,v54
v_fma_mix_f32 v53,v53,v9,0 op_sel_hi:[0,1,0]
v_mfma_f32_16x16x32_bf16 v[96:99],a[68:71],a[32:35],v[228:231]
s_barrier
ds_read_b64_tr_b16 a[224:225],v3 offset:0
ds_read_b64_tr_b16 a[226:227],v3 offset:1536
v_fma_mix_f32 v55,v55,v9,0 op_sel:[0,1,0] op_sel_hi:[0,1,0]
v_mfma_f32_16x16x32_bf16 v[24:27],a[216:219],v[160:163],v[20:23]
s_add_u32 m0,s57,0
ds_read_b64_tr_b16 a[228:229],v3 offset:8
ds_read_b64_tr_b16 a[230:231],v3 offset:1544
v_lshlrev_b32_sdwa v60,1,v60 dst_sel:DWORD dst_unused:UNUSED_PAD src0_sel:DWORD src1_sel:WORD_1
v_mfma_f32_16x16x32_bf16 v[24:27],a[220:223],v[176:179],v[24:27]
s_waitcnt lgkmcnt(15)
ds_read_b64_tr_b16 a[232:233],v3 offset:64
ds_read_b64_tr_b16 a[234:235],v3 offset:1600
v_lshl_add_u64 v[44:45],v[44:45],0,v[46:47]
v_mfma_f32_16x16x32_bf16 v[24:27],a[72:75],v[192:195],v[24:27]
s_waitcnt lgkmcnt(15)
ds_read_b64_tr_b16 a[236:237],v3 offset:72
ds_read_b64_tr_b16 a[238:239],v3 offset:1608
v_lshl_add_u64 v[46:47],v[48:49],0,v[50:51]
v_mfma_f32_16x16x32_bf16 v[28:31],a[76:79],v[192:195],v[16:19]
s_waitcnt lgkmcnt(15)
ds_read_b64_tr_b16 a[240:241],v3 offset:128
ds_read_b64_tr_b16 a[242:243],v3 offset:1664
v_lshlrev_b32_sdwa v62,1,v62 dst_sel:DWORD dst_unused:UNUSED_PAD src0_sel:DWORD src1_sel:WORD_1
v_mfma_f32_16x16x32_bf16 v[28:31],a[80:83],v[176:179],v[28:31]
s_waitcnt lgkmcnt(12)
ds_read_b64_tr_b16 a[244:245],v3 offset:136
v_lshl_add_u64 v[48:49],v[56:57],0,v[58:59]
v_mfma_f32_16x16x32_bf16 v[28:31],a[84:87],v[160:163],v[28:31]
s_add_u32 s89,s56,2457600
ds_read_b64_tr_b16 a[246:247],v3 offset:1672
v_lshlrev_b32_sdwa v61,1,v61 dst_sel:DWORD dst_unused:UNUSED_PAD src0_sel:DWORD src1_sel:WORD_1
v_mfma_f32_16x16x32_bf16 v[56:59],a[84:87],v[164:167],v[16:19]
s_waitcnt lgkmcnt(12)
buffer_load_dwordx4 v6,s[36:39],s89 offen lds
v_or_b32_e64 v50,v52,v54
v_mfma_f32_16x16x32_bf16 v[56:59],a[80:83],v[180:183],v[56:59]
s_add_u32 m0,s58,18480
ds_read_u16 v60,v60
v_cvt_pk_bf16_f32 v51,v53,v55
v_lshlrev_b32_sdwa v63,1,v63 dst_sel:DWORD dst_unused:UNUSED_PAD src0_sel:DWORD src1_sel:WORD_1
v_mfma_f32_16x16x32_bf16 v[56:59],a[76:79],v[196:199],v[56:59]
s_add_u32 s69,s69,s59
ds_read_u16_d16_hi v62,v62
v_lshlrev_b32_sdwa v32,1,v32 dst_sel:DWORD dst_unused:UNUSED_PAD src0_sel:DWORD src1_sel:WORD_1
v_mfma_f32_16x16x32_bf16 v[52:55],a[72:75],v[196:199],v[20:23]
buffer_load_dwordx4 v5,s[44:47],s69 offen offset:0 lds
v_lshlrev_b32_sdwa v34,1,v34 dst_sel:DWORD dst_unused:UNUSED_PAD src0_sel:DWORD src1_sel:WORD_1
v_mfma_f32_16x16x32_bf16 v[52:55],a[220:223],v[180:183],v[52:55]
s_add_u32 s69,s69,s53
ds_read_u16 v61,v61
v_lshlrev_b32_sdwa v33,1,v33 dst_sel:DWORD dst_unused:UNUSED_PAD src0_sel:DWORD src1_sel:WORD_1
v_mfma_f32_16x16x32_bf16 v[52:55],a[216:219],v[164:167],v[52:55]
s_waitcnt lgkmcnt(7)
buffer_load_dwordx4 v5,s[44:47],s69 offen offset:1024 lds
ds_read_u16_d16_hi v63,v63
v_mfma_f32_16x16x32_bf16 v[136:139],a[224:227],v[44:47],v[136:139]
s_add_u32 s69,s69,s53
ds_read_u16 v32,v32
ds_read_u16_d16_hi v34,v34
v_mfma_f32_16x16x32_bf16 v[140:143],a[224:227],v[48:51],v[140:143]
buffer_load_dwordx4 v5,s[44:47],s69 offen offset:2048 lds
v_lshlrev_b32_sdwa v35,1,v35 dst_sel:DWORD dst_unused:UNUSED_PAD src0_sel:DWORD src1_sel:WORD_1
v_mfma_f32_16x16x32_bf16 v[68:71],a[228:231],v[48:51],v[68:71]
ds_read_u16 v33,v33
ds_read_u16_d16_hi v35,v35
v_mfma_f32_16x16x32_bf16 v[64:67],a[228:231],v[44:47],v[64:67]
ds_read_b64_tr_b16 a[248:249],v3 offset:6160
v_lshlrev_b32_sdwa v40,1,v40 dst_sel:DWORD dst_unused:UNUSED_PAD src0_sel:DWORD src1_sel:WORD_1
v_mfma_f32_16x16x32_bf16 v[144:147],a[232:235],v[44:47],v[144:147]
s_add_u32 m0,s57,3544
ds_read_u16 v40,v40
ds_read_b64_tr_b16 a[250:251],v3 offset:7696
v_mfma_f32_16x16x32_bf16 v[148:151],a[232:235],v[48:51],v[148:151]
s_add_u32 s89,s56,2555904
ds_read_b64_tr_b16 a[252:253],v3 offset:6168
v_lshlrev_b32_sdwa v42,1,v42 dst_sel:DWORD dst_unused:UNUSED_PAD src0_sel:DWORD src1_sel:WORD_1
v_mfma_f32_16x16x32_bf16 v[84:87],a[236:239],v[48:51],v[84:87]
buffer_load_dwordx4 v6,s[36:39],s89 offen lds
ds_read_u16_d16_hi v42,v42
v_mfma_f32_16x16x32_bf16 v[80:83],a[236:239],v[44:47],v[80:83]
s_waitcnt lgkmcnt(10)
ds_read_b64_tr_b16 a[254:255],v3 offset:7704
v_lshlrev_b32_sdwa v41,1,v41 dst_sel:DWORD dst_unused:UNUSED_PAD src0_sel:DWORD src1_sel:WORD_1
v_mfma_f32_16x16x32_bf16 v[152:155],a[240:243],v[44:47],v[152:155]
ds_read_u16 v41,v41
v_lshlrev_b32_sdwa v43,1,v43 dst_sel:DWORD dst_unused:UNUSED_PAD src0_sel:DWORD src1_sel:WORD_1
v_mfma_f32_16x16x32_bf16 v[156:159],a[240:243],v[48:51],v[156:159]
ds_read_u16_d16_hi v43,v43
v_lshlrev_b32_sdwa v36,1,v36 dst_sel:DWORD dst_unused:UNUSED_PAD src0_sel:DWORD src1_sel:WORD_1
v_mfma_f32_16x16x32_bf16 v[100:103],a[244:247],v[48:51],v[100:103]
ds_read_u16 v36,v36
v_lshlrev_b32_sdwa v38,1,v38 dst_sel:DWORD dst_unused:UNUSED_PAD src0_sel:DWORD src1_sel:WORD_1
v_mfma_f32_16x16x32_bf16 v[96:99],a[244:247],v[44:47],v[96:99]
ds_read_u16_d16_hi v38,v38
v_lshlrev_b32_sdwa v37,1,v37 dst_sel:DWORD dst_unused:UNUSED_PAD src0_sel:DWORD src1_sel:WORD_1
v_mfma_f32_16x16x32_bf16 v[112:115],a[48:51],a[0:3],v[208:211]
ds_read_u16 v37,v37
v_lshlrev_b32_sdwa v39,1,v39 dst_sel:DWORD dst_unused:UNUSED_PAD src0_sel:DWORD src1_sel:WORD_1
v_mfma_f32_16x16x32_bf16 v[116:119],a[48:51],a[12:15],v[208:211]
ds_read_u16_d16_hi v39,v39
ds_read_b64_tr_b16 a[92:93],v3 offset:6224
v_mfma_f32_16x16x32_bf16 v[76:79],a[52:55],a[12:15],v[212:215]
ds_read_b64_tr_b16 a[94:95],v3 offset:7760
v_mfma_f32_16x16x32_bf16 v[72:75],a[52:55],a[0:3],v[212:215]
ds_read_b64_tr_b16 a[48:49],v3 offset:6232
v_mfma_f32_16x16x32_bf16 v[120:123],a[56:59],a[4:7],v[216:219]
ds_read_b64_tr_b16 a[50:51],v3 offset:7768
v_mfma_f32_16x16x32_bf16 v[124:127],a[56:59],a[16:19],v[216:219]
ds_read_b64_tr_b16 a[56:57],v3 offset:6288
v_mfma_f32_16x16x32_bf16 v[92:95],a[60:63],a[16:19],v[220:223]
ds_read_b64_tr_b16 a[58:59],v3 offset:7824
v_mfma_f32_16x16x32_bf16 v[88:91],a[60:63],a[4:7],v[220:223]
v_mfma_f32_16x16x32_bf16 v[128:131],a[64:67],a[8:11],v[224:227]
v_mfma_f32_16x16x32_bf16 v[132:135],a[64:67],a[20:23],v[224:227]
ds_read_b64_tr_b16 a[64:65],v3 offset:6296
v_mfma_f32_16x16x32_bf16 v[108:111],a[68:71],a[20:23],v[228:231]
ds_read_b64_tr_b16 a[66:67],v3 offset:7832
v_mfma_f32_16x16x32_bf16 v[104:107],a[68:71],a[8:11],v[228:231]
s_add_u32 m0,s57,7080
v_mfma_f32_16x16x32_bf16 v[48:51],a[216:219],v[168:171],v[20:23]
s_add_u32 s89,s56,2654208
ds_read_b64 v[14:15],v7 offset:6160
ds_read_b64_tr_b16 a[212:213],v1 offset:6160
v_lshlrev_b32_sdwa v24,1,v24 dst_sel:DWORD dst_unused:UNUSED_PAD src0_sel:DWORD src1_sel:WORD_1
v_mfma_f32_16x16x32_bf16 v[48:51],a[220:223],v[184:187],v[48:51]
s_waitcnt lgkmcnt(6)
buffer_load_dwordx4 v6,s[36:39],s89 offen lds
ds_read_b64_tr_b16 a[214:215],v1 offset:7184
v_lshl_add_u64 v[60:61],v[60:61],0,v[62:63]
v_fma_mixlo_f16 v8,v57,s99,0
v_mfma_f32_16x16x32_bf16 v[48:51],a[72:75],v[200:203],v[48:51]
ds_read_b64_tr_b16 a[208:209],v1 offset:8208
ds_read_b64_tr_b16 a[210:211],v1 offset:9232
v_lshl_add_u64 v[62:63],v[32:33],0,v[34:35]
v_lshlrev_b32_sdwa v26,1,v26 dst_sel:DWORD dst_unused:UNUSED_PAD src0_sel:DWORD src1_sel:WORD_1
v_mfma_f32_16x16x32_bf16 v[44:47],a[76:79],v[200:203],v[16:19]
ds_read_u16 v24,v24
ds_read_u16_d16_hi v26,v26
v_fma_mixhi_f16 v8,v59,s99,0
v_mfma_f32_16x16x32_bf16 v[44:47],a[80:83],v[184:187],v[44:47]
ds_read_b64_tr_b16 a[204:205],v1 offset:10256
ds_read_b64_tr_b16 a[206:207],v1 offset:11280
v_lshl_add_u64 v[32:33],v[40:41],0,v[42:43]
v_lshlrev_b32_sdwa v25,1,v25 dst_sel:DWORD dst_unused:UNUSED_PAD src0_sel:DWORD src1_sel:WORD_1
v_pk_fma_f16 v9,v8,v8,s54
v_mfma_f32_16x16x32_bf16 v[44:47],a[84:87],v[168:171],v[44:47]
s_waitcnt lgkmcnt(8)
ds_read_u16 v25,v25
ds_read_b64_tr_b16 a[200:201],v2 offset:10256
v_or3_b32 v252,v14,v15,v252
v_lshlrev_b32_sdwa v27,1,v27 dst_sel:DWORD dst_unused:UNUSED_PAD src0_sel:DWORD src1_sel:WORD_1
v_mfma_f32_16x16x32_bf16 v[40:43],a[84:87],v[172:175],v[16:19]
s_nop 0
ds_read_b64 v[14:15],v7 offset:6672
ds_read_b128 v[16:19],v4 offset:256
v_pk_mul_f16 v9,v8,v9
v_mfma_f32_16x16x32_bf16 v[40:43],a[80:83],v[188:191],v[40:43]
ds_read_u16_d16_hi v27,v27
ds_read_b64_tr_b16 a[202:203],v2 offset:11280
v_lshl_add_u64 v[34:35],v[36:37],0,v[38:39]
v_lshlrev_b32_sdwa v28,1,v28 dst_sel:DWORD dst_unused:UNUSED_PAD src0_sel:DWORD src1_sel:WORD_1
v_mfma_f32_16x16x32_bf16 v[40:43],a[76:79],v[204:207],v[40:43]
ds_read_u16 v28,v28
v_exp_f16 v8,v9
v_mfma_f32_16x16x32_bf16 v[36:39],a[72:75],v[204:207],v[20:23]
ds_read_b128 v[20:23],v4 offset:288
ds_read_b64_tr_b16 a[196:197],v2 offset:8208
v_lshlrev_b32_sdwa v30,1,v30 dst_sel:DWORD dst_unused:UNUSED_PAD src0_sel:DWORD src1_sel:WORD_1
v_mfma_f32_16x16x32_bf16 v[36:39],a[220:223],v[188:191],v[36:39]
ds_read_u16_d16_hi v30,v30
v_lshlrev_b32_sdwa v29,1,v29 dst_sel:DWORD dst_unused:UNUSED_PAD src0_sel:DWORD src1_sel:WORD_1
v_mfma_f32_16x16x32_bf16 v[36:39],a[216:219],v[172:175],v[36:39]
s_waitcnt lgkmcnt(7)
ds_read_u16 v29,v29
v_or3_b32 v252,v14,v15,v252
v_mfma_f32_16x16x32_bf16 v[112:115],a[224:227],v[60:63],v[112:115]
s_add_u32 m0,s57,10608
ds_read_b64 v[14:15],v7 offset:7184
v_exp_f16_sdwa v8,v9 dst_sel:WORD_1 dst_unused:UNUSED_PRESERVE src0_sel:WORD_1
v_lshlrev_b32_sdwa v31,1,v31 dst_sel:DWORD dst_unused:UNUSED_PAD src0_sel:DWORD src1_sel:WORD_1
v_mfma_f32_16x16x32_bf16 v[116:119],a[224:227],v[32:35],v[116:119]
s_add_u32 s89,s56,2752512
ds_read_u16_d16_hi v31,v31
v_lshlrev_b32_sdwa v52,1,v52 dst_sel:DWORD dst_unused:UNUSED_PAD src0_sel:DWORD src1_sel:WORD_1
v_mfma_f32_16x16x32_bf16 v[76:79],a[228:231],v[32:35],v[76:79]
buffer_load_dwordx4 v6,s[36:39],s89 offen lds
ds_read_b64_tr_b16 a[198:199],v2 offset:9232
v_pk_fma_f16 v8,v8,s55,s55
v_mfma_f32_16x16x32_bf16 v[72:75],a[228:231],v[60:63],v[72:75]
v_rcp_f16 v9,v8
v_lshlrev_b32_sdwa v54,1,v54 dst_sel:DWORD dst_unused:UNUSED_PAD src0_sel:DWORD src1_sel:WORD_1
v_mfma_f32_16x16x32_bf16 v[120:123],a[232:235],v[60:63],v[120:123]
ds_read_u16 v52,v52
v_lshlrev_b32_sdwa v53,1,v53 dst_sel:DWORD dst_unused:UNUSED_PAD src0_sel:DWORD src1_sel:WORD_1
v_mfma_f32_16x16x32_bf16 v[124:127],a[232:235],v[32:35],v[124:127]
s_waitcnt lgkmcnt(3)
ds_read_u16_d16_hi v54,v54
v_or3_b32 v252,v14,v15,v252
v_mfma_f32_16x16x32_bf16 v[92:95],a[236:239],v[32:35],v[92:95]
ds_read_u16 v53,v53
v_lshlrev_b32_sdwa v55,1,v55 dst_sel:DWORD dst_unused:UNUSED_PAD src0_sel:DWORD src1_sel:WORD_1
v_mfma_f32_16x16x32_bf16 v[88:91],a[236:239],v[60:63],v[88:91]
v_rcp_f16_sdwa v9,v8 dst_sel:WORD_1 dst_unused:UNUSED_PRESERVE src0_sel:WORD_1
v_mfma_f32_16x16x32_bf16 v[128:131],a[240:243],v[60:63],v[128:131]
ds_read_u16_d16_hi v55,v55
v_lshlrev_b32_sdwa v56,1,v56 dst_sel:DWORD dst_unused:UNUSED_PAD src0_sel:DWORD src1_sel:WORD_1
v_mfma_f32_16x16x32_bf16 v[132:135],a[240:243],v[32:35],v[132:135]
ds_read_b64_tr_b16 a[192:193],v2 offset:6160
ds_read_b64_tr_b16 a[194:195],v2 offset:7184
v_lshlrev_b32_sdwa v58,1,v58 dst_sel:DWORD dst_unused:UNUSED_PAD src0_sel:DWORD src1_sel:WORD_1
v_mfma_f32_16x16x32_bf16 v[108:111],a[244:247],v[32:35],v[108:111]
ds_read_u16 v56,v56
v_fma_mix_f32 v57,v57,v9,0 op_sel_hi:[0,1,0]
v_mfma_f32_16x16x32_bf16 v[104:107],a[244:247],v[60:63],v[104:107]
ds_read_u16_d16_hi v58,v58
v_fma_mix_f32 v59,v59,v9,0 op_sel:[0,1,0] op_sel_hi:[0,1,0]
v_mfma_f32_16x16x32_bf16 v[32:35],a[212:215],v[160:163],v[16:19]
v_lshlrev_b32_sdwa v48,1,v48 dst_sel:DWORD dst_unused:UNUSED_PAD src0_sel:DWORD src1_sel:WORD_1
v_mfma_f32_32x32x16_bf16 v[208:223],a[88:91],a[88:91],0
v_lshlrev_b32_sdwa v50,1,v50 dst_sel:DWORD dst_unused:UNUSED_PAD src0_sel:DWORD src1_sel:WORD_1
v_lshlrev_b32_sdwa v49,1,v49 dst_sel:DWORD dst_unused:UNUSED_PAD src0_sel:DWORD src1_sel:WORD_1
v_lshlrev_b32_sdwa v51,1,v51 dst_sel:DWORD dst_unused:UNUSED_PAD src0_sel:DWORD src1_sel:WORD_1
v_lshlrev_b32_sdwa v44,1,v44 dst_sel:DWORD dst_unused:UNUSED_PAD src0_sel:DWORD src1_sel:WORD_1
v_lshlrev_b32_sdwa v46,1,v46 dst_sel:DWORD dst_unused:UNUSED_PAD src0_sel:DWORD src1_sel:WORD_1
v_mfma_f32_16x16x32_bf16 v[32:35],a[208:211],v[176:179],v[32:35]
s_add_u32 m0,s57,14144
ds_read_u16 v48,v48
ds_read_u16_d16_hi v50,v50
v_lshl_add_u64 v[24:25],v[24:25],0,v[26:27]
v_mfma_f32_16x16x32_bf16 v[32:35],a[204:207],v[192:195],v[32:35]
s_waitcnt lgkmcnt(8)
ds_read_u16 v49,v49
v_lshl_add_u64 v[26:27],v[28:29],0,v[30:31]
v_mfma_f32_16x16x32_bf16 v[60:63],a[200:203],v[192:195],v[20:23]
s_add_u32 s89,s56,2850816
ds_read_u16_d16_hi v51,v51
v_mfma_f32_32x32x16_bf16 v[224:239],a[88:91],a[88:91],0
v_accvgpr_write_b32 a0,v160
v_accvgpr_write_b32 a1,v161
v_accvgpr_write_b32 a2,v162
v_accvgpr_write_b32 a3,v163
v_lshlrev_b32_sdwa v45,1,v45 dst_sel:DWORD dst_unused:UNUSED_PAD src0_sel:DWORD src1_sel:WORD_1
v_mfma_f32_16x16x32_bf16 v[60:63],a[196:199],v[176:179],v[60:63]
s_waitcnt lgkmcnt(6)
buffer_load_dwordx4 v6,s[36:39],s89 offen lds
v_lshl_add_u64 v[28:29],v[52:53],0,v[54:55]
v_mfma_f32_16x16x32_bf16 v[60:63],a[192:195],a[0:3],v[60:63]
ds_read_u16 v44,v44
v_lshlrev_b32_sdwa v47,1,v47 dst_sel:DWORD dst_unused:UNUSED_PAD src0_sel:DWORD src1_sel:WORD_1
v_mfma_f32_16x16x32_bf16 v[52:55],a[192:195],v[164:167],v[20:23]
s_waitcnt lgkmcnt(5)
ds_read_u16_d16_hi v46,v46
v_or_b32_e64 v30,v56,v58
v_mfma_f32_16x16x32_bf16 v[52:55],a[196:199],v[180:183],v[52:55]
s_waitcnt vmcnt(9)
ds_read_u16 v45,v45
v_cvt_pk_bf16_f32 v31,v57,v59
v_mfma_f32_16x16x32_bf16 v[52:55],a[200:203],v[196:199],v[52:55]
s_barrier
ds_read_u16_d16_hi v47,v47
v_accvgpr_write_b32 a4,v164
v_mfma_f32_16x16x32_bf16 v[56:59],a[204:207],v[196:199],v[16:19]
ds_read_b64_tr_b16 a[224:225],v3 offset:12320
v_lshlrev_b32_sdwa v36,1,v36 dst_sel:DWORD dst_unused:UNUSED_PAD src0_sel:DWORD src1_sel:WORD_1
v_mfma_f32_16x16x32_bf16 v[56:59],a[208:211],v[180:183],v[56:59]
ds_read_u16 v36,v36
v_accvgpr_write_b32 a5,v165
v_mfma_f32_16x16x32_bf16 v[56:59],a[212:215],v[164:167],v[56:59]
s_add_u32 m0,s58,0
ds_read_b64_tr_b16 a[226:227],v3 offset:13856
v_lshlrev_b32_sdwa v38,1,v38 dst_sel:DWORD dst_unused:UNUSED_PAD src0_sel:DWORD src1_sel:WORD_1
v_mfma_f32_16x16x32_bf16 v[136:139],a[248:251],v[24:27],v[136:139]
s_add_u32 s69,s69,s59
ds_read_u16_d16_hi v38,v38
v_accvgpr_write_b32 a6,v166
v_mfma_f32_16x16x32_bf16 v[140:143],a[248:251],v[28:31],v[140:143]
buffer_load_dwordx4 v5,s[44:47],s69 offen offset:0 lds
v_lshlrev_b32_sdwa v37,1,v37 dst_sel:DWORD dst_unused:UNUSED_PAD src0_sel:DWORD src1_sel:WORD_1
v_mfma_f32_16x16x32_bf16 v[68:71],a[252:255],v[28:31],v[68:71]
ds_read_u16 v37,v37
v_lshlrev_b32_sdwa v39,1,v39 dst_sel:DWORD dst_unused:UNUSED_PAD src0_sel:DWORD src1_sel:WORD_1
v_mfma_f32_16x16x32_bf16 v[64:67],a[252:255],v[24:27],v[64:67]
ds_read_u16_d16_hi v39,v39
v_accvgpr_write_b32 a7,v167
v_mfma_f32_16x16x32_bf16 v[144:147],a[92:95],v[24:27],v[144:147]
s_add_u32 s69,s69,s53
ds_read_b64_tr_b16 a[228:229],v3 offset:12328
v_lshlrev_b32_sdwa v40,1,v40 dst_sel:DWORD dst_unused:UNUSED_PAD src0_sel:DWORD src1_sel:WORD_1
v_mfma_f32_16x16x32_bf16 v[148:151],a[92:95],v[28:31],v[148:151]
buffer_load_dwordx4 v5,s[44:47],s69 offen offset:1024 lds
ds_read_u16 v40,v40
v_mfma_f32_16x16x32_bf16 v[84:87],a[48:51],v[28:31],v[84:87]
s_add_u32 m0,s57,17688
ds_read_b64_tr_b16 a[230:231],v3 offset:13864
ds_read_b64_tr_b16 a[232:233],v3 offset:12384
v_accvgpr_write_b32 a8,v168
v_mfma_f32_16x16x32_bf16 v[80:83],a[48:51],v[24:27],v[80:83]
s_add_u32 s89,s56,2949120
ds_read_b64_tr_b16 a[234:235],v3 offset:13920
v_lshlrev_b32_sdwa v42,1,v42 dst_sel:DWORD dst_unused:UNUSED_PAD src0_sel:DWORD src1_sel:WORD_1
v_mfma_f32_16x16x32_bf16 v[152:155],a[56:59],v[24:27],v[152:155]
buffer_load_dwordx4 v6,s[36:39],s89 offen lds
ds_read_u16_d16_hi v42,v42
v_lshlrev_b32_sdwa v41,1,v41 dst_sel:DWORD dst_unused:UNUSED_PAD src0_sel:DWORD src1_sel:WORD_1
v_accvgpr_write_b32 a9,v169
v_mfma_f32_16x16x32_bf16 v[156:159],a[56:59],v[28:31],v[156:159]
s_add_u32 m0,s58,0
ds_read_u16 v41,v41
ds_read_b64_tr_b16 a[236:237],v3 offset:12392
v_lshlrev_b32_sdwa v43,1,v43 dst_sel:DWORD dst_unused:UNUSED_PAD src0_sel:DWORD src1_sel:WORD_1
v_mfma_f32_16x16x32_bf16 v[100:103],a[64:67],v[28:31],v[100:103]
s_add_u32 s69,s69,s53
ds_read_u16_d16_hi v43,v43
ds_read_b64_tr_b16 a[238:239],v3 offset:13928
v_accvgpr_write_b32 a10,v170
v_mfma_f32_16x16x32_bf16 v[96:99],a[64:67],v[24:27],v[96:99]
buffer_load_dwordx4 v5,s[44:47],s69 offen offset:2048 lds
ds_read_b64_tr_b16 a[240:241],v3 offset:12448
v_accvgpr_write_b32 a11,v171
s_nop 1
v_mfma_f32_16x16x32_bf16 v[28:31],a[212:215],a[8:11],v[16:19]
ds_read_b64 v[14:15],v7 offset:12320
ds_read_b64_tr_b16 a[52:53],v1 offset:12320
v_lshlrev_b32_sdwa v32,1,v32 dst_sel:DWORD dst_unused:UNUSED_PAD src0_sel:DWORD src1_sel:WORD_1
v_mfma_f32_16x16x32_bf16 v[28:31],a[208:211],v[184:187],v[28:31]
s_waitcnt lgkmcnt(12)
ds_read_b64_tr_b16 a[54:55],v1 offset:13344
ds_read_b64_tr_b16 a[60:61],v1 offset:14368
v_lshl_add_u64 v[48:49],v[48:49],0,v[50:51]
v_fma_mixlo_f16 v8,v53,s99,0
v_mfma_f32_16x16x32_bf16 v[28:31],a[204:207],v[200:203],v[28:31]
s_add_u32 m0,s58,6160
ds_read_b64_tr_b16 a[62:63],v1 offset:15392
ds_read_u16 v32,v32
v_lshl_add_u64 v[50:51],v[44:45],0,v[46:47]
v_lshlrev_b32_sdwa v34,1,v34 dst_sel:DWORD dst_unused:UNUSED_PAD src0_sel:DWORD src1_sel:WORD_1
v_mfma_f32_16x16x32_bf16 v[24:27],a[200:203],v[200:203],v[20:23]
s_add_u32 s69,s69,s59
ds_read_u16_d16_hi v34,v34
ds_read_b64_tr_b16 a[68:69],v1 offset:16416
v_fma_mixhi_f16 v8,v55,s99,0
v_mfma_f32_16x16x32_bf16 v[24:27],a[196:199],v[184:187],v[24:27]
buffer_load_dwordx4 v5,s[44:47],s69 offen offset:0 lds
ds_read_b64_tr_b16 a[70:71],v1 offset:17440
v_lshl_add_u64 v[44:45],v[36:37],0,v[38:39]
v_lshlrev_b32_sdwa v33,1,v33 dst_sel:DWORD dst_unused:UNUSED_PAD src0_sel:DWORD src1_sel:WORD_1
v_pk_fma_f16 v9,v8,v8,s54
v_mfma_f32_16x16x32_bf16 v[24:27],a[192:195],a[8:11],v[24:27]
s_waitcnt lgkmcnt(8)
ds_read_u16 v33,v33
ds_read_b64_tr_b16 a[84:85],v2 offset:16416
v_or3_b32 v252,v14,v15,v252
v_lshlrev_b32_sdwa v35,1,v35 dst_sel:DWORD dst_unused:UNUSED_PAD src0_sel:DWORD src1_sel:WORD_1
v_accvgpr_write_b32 a12,v172
v_mfma_f32_16x16x32_bf16 v[36:39],a[192:195],v[172:175],v[20:23]
s_add_u32 s69,s69,s53
ds_read_b64 v[14:15],v7 offset:12832
ds_read_b128 v[20:23],v4 offset:384
v_pk_mul_f16 v9,v8,v9
v_mfma_f32_16x16x32_bf16 v[36:39],a[196:199],v[188:191],v[36:39]
buffer_load_dwordx4 v5,s[44:47],s69 offen offset:1024 lds
ds_read_u16_d16_hi v35,v35
v_lshl_add_u64 v[46:47],v[40:41],0,v[42:43]
v_lshlrev_b32_sdwa v60,1,v60 dst_sel:DWORD dst_unused:UNUSED_PAD src0_sel:DWORD src1_sel:WORD_1
v_mfma_f32_16x16x32_bf16 v[36:39],a[200:203],v[204:207],v[36:39]
s_add_u32 m0,s57,21224
ds_read_u16 v60,v60
v_exp_f16_e64 v8,v9
v_mfma_f32_16x16x32_bf16 v[40:43],a[204:207],v[204:207],v[16:19]
s_add_u32 s89,s56,3047424
ds_read_b128 v[16:19],v4 offset:416
ds_read_b64_tr_b16 a[86:87],v2 offset:17440
v_lshlrev_b32_sdwa v62,1,v62 dst_sel:DWORD dst_unused:UNUSED_PAD src0_sel:DWORD src1_sel:WORD_1
v_mfma_f32_16x16x32_bf16 v[40:43],a[208:211],v[188:191],v[40:43]
buffer_load_dwordx4 v6,s[36:39],s89 offen lds
ds_read_u16_d16_hi v62,v62
v_lshlrev_b32_sdwa v61,1,v61 dst_sel:DWORD dst_unused:UNUSED_PAD src0_sel:DWORD src1_sel:WORD_1
v_mfma_f32_16x16x32_bf16 v[40:43],a[212:215],v[172:175],v[40:43]
s_waitcnt lgkmcnt(6)
ds_read_u16 v61,v61
ds_read_b64_tr_b16 a[80:81],v2 offset:14368
v_or3_b32 v252,v14,v15,v252
v_accvgpr_write_b32 a13,v173
v_mfma_f32_16x16x32_bf16 v[112:115],a[248:251],v[48:51],v[112:115]
s_add_u32 m0,s58,6160
ds_read_b64 v[14:15],v7 offset:13344
ds_read_b64_tr_b16 a[82:83],v2 offset:15392
v_exp_f16_sdwa v8,v9 dst_sel:WORD_1 dst_unused:UNUSED_PRESERVE src0_sel:WORD_1
v_lshlrev_b32_sdwa v63,1,v63 dst_sel:DWORD dst_unused:UNUSED_PAD src0_sel:DWORD src1_sel:WORD_1
v_mfma_f32_16x16x32_bf16 v[116:119],a[248:251],v[44:47],v[116:119]
s_add_u32 s69,s69,s53
ds_read_u16_d16_hi v63,v63
ds_read_b64_tr_b16 a[76:77],v2 offset:12320
v_lshlrev_b32_sdwa v56,1,v56 dst_sel:DWORD dst_unused:UNUSED_PAD src0_sel:DWORD src1_sel:WORD_1
v_mfma_f32_16x16x32_bf16 v[76:79],a[252:255],v[44:47],v[76:79]
buffer_load_dwordx4 v5,s[44:47],s69 offen offset:2048 lds
ds_read_u16 v56,v56
v_pk_fma_f16 v8,v8,s55,s55
v_mfma_f32_16x16x32_bf16 v[72:75],a[252:255],v[48:51],v[72:75]
ds_read_b64_tr_b16 a[78:79],v2 offset:13344
v_rcp_f16_e64 v9,v8
v_lshlrev_b32_sdwa v58,1,v58 dst_sel:DWORD dst_unused:UNUSED_PAD src0_sel:DWORD src1_sel:WORD_1
v_mfma_f32_16x16x32_bf16 v[120:123],a[92:95],v[48:51],v[120:123]
ds_read_u16_d16_hi v58,v58
v_lshlrev_b32_sdwa v57,1,v57 dst_sel:DWORD dst_unused:UNUSED_PAD src0_sel:DWORD src1_sel:WORD_1
v_mfma_f32_16x16x32_bf16 v[124:127],a[92:95],v[44:47],v[124:127]
s_waitcnt lgkmcnt(6)
ds_read_u16 v57,v57
v_or3_b32 v252,v14,v15,v252
TE02_%=:
s_cbranch_execz TP3814_%=
v_accvgpr_write_b32 a14,v174
v_mfma_f32_16x16x32_bf16 v[92:95],a[48:51],v[44:47],v[92:95]
s_add_u32 m0,s57,24752
ds_read_b64_tr_b16 a[242:243],v3 offset:13984
v_lshlrev_b32_sdwa v59,1,v59 dst_sel:DWORD dst_unused:UNUSED_PAD src0_sel:DWORD src1_sel:WORD_1
v_mfma_f32_16x16x32_bf16 v[88:91],a[48:51],v[48:51],v[88:91]
s_add_u32 s89,s56,3145728
v_rcp_f16_sdwa v9,v8 dst_sel:WORD_1 dst_unused:UNUSED_PRESERVE src0_sel:WORD_1
v_mfma_f32_16x16x32_bf16 v[128:131],a[56:59],v[48:51],v[128:131]
s_nop 0
buffer_load_dwordx4 v6,s[36:39],s89 offen lds
ds_read_u16_d16_hi v59,v59
v_lshlrev_b32_sdwa v52,1,v52 dst_sel:DWORD dst_unused:UNUSED_PAD src0_sel:DWORD src1_sel:WORD_1
v_mfma_f32_16x16x32_bf16 v[132:135],a[56:59],v[44:47],v[132:135]
ds_read_u16 v52,v52
v_lshlrev_b32_sdwa v54,1,v54 dst_sel:DWORD dst_unused:UNUSED_PAD src0_sel:DWORD src1_sel:WORD_1
v_mfma_f32_16x16x32_bf16 v[108:111],a[64:67],v[44:47],v[108:111]
ds_read_u16_d16_hi v54,v54
v_fma_mix_f32 v53,v53,v9,0 op_sel_hi:[0,1,0]
v_mfma_f32_16x16x32_bf16 v[104:107],a[64:67],v[48:51],v[104:107]
ds_read_b64_tr_b16 a[244:245],v3 offset:12456
v_accvgpr_write_b32 a15,v175
v_fma_mix_f32 v55,v55,v9,0 op_sel:[0,1,0] op_sel_hi:[0,1,0]
v_mfma_f32_16x16x32_bf16 v[44:47],a[52:55],a[0:3],v[20:23]
ds_read_b64_tr_b16 a[246:247],v3 offset:13992
v_lshlrev_b32_sdwa v28,1,v28 dst_sel:DWORD dst_unused:UNUSED_PAD src0_sel:DWORD src1_sel:WORD_1
v_mfma_f32_16x16x32_bf16 v[44:47],a[60:63],v[176:179],v[44:47]
ds_read_u16 v28,v28
v_lshl_add_u64 v[32:33],v[32:33],0,v[34:35]
v_mfma_f32_32x32x16_bf16 v[160:175],a[88:91],a[88:91],0
v_lshlrev_b32_sdwa v30,1,v30 dst_sel:DWORD dst_unused:UNUSED_PAD src0_sel:DWORD src1_sel:WORD_1
v_accvgpr_write_b32 a16,v176
v_lshlrev_b32_sdwa v29,1,v29 dst_sel:DWORD dst_unused:UNUSED_PAD src0_sel:DWORD src1_sel:WORD_1
v_accvgpr_write_b32 a17,v177
v_lshlrev_b32_sdwa v31,1,v31 dst_sel:DWORD dst_unused:UNUSED_PAD src0_sel:DWORD src1_sel:WORD_1
v_mfma_f32_16x16x32_bf16 v[44:47],a[68:71],v[192:195],v[44:47]
s_waitcnt lgkmcnt(7)
ds_read_u16_d16_hi v30,v30
v_lshl_add_u64 v[34:35],v[60:61],0,v[62:63]
v_mfma_f32_16x16x32_bf16 v[48:51],a[84:87],v[192:195],v[16:19]
ds_read_u16 v29,v29
v_accvgpr_write_b32 a18,v178
v_mfma_f32_16x16x32_bf16 v[48:51],a[80:83],v[176:179],v[48:51]
s_waitcnt lgkmcnt(7)
ds_read_u16_d16_hi v31,v31
v_lshl_add_u64 v[60:61],v[56:57],0,v[58:59]
v_mfma_f32_16x16x32_bf16 v[48:51],a[76:79],a[0:3],v[48:51]
v_lshlrev_b32_sdwa v24,1,v24 dst_sel:DWORD dst_unused:UNUSED_PAD src0_sel:DWORD src1_sel:WORD_1
v_lshlrev_b32_sdwa v26,1,v26 dst_sel:DWORD dst_unused:UNUSED_PAD src0_sel:DWORD src1_sel:WORD_1
v_mfma_f32_16x16x32_bf16 v[56:59],a[76:79],a[4:7],v[16:19]
s_waitcnt lgkmcnt(6)
ds_read_u16 v24,v24
v_or_b32 v62,v52,v54
v_mfma_f32_16x16x32_bf16 v[56:59],a[80:83],v[180:183],v[56:59]
s_waitcnt vmcnt(13)
ds_read_u16_d16_hi v26,v26
v_cvt_pk_bf16_f32 v63,v53,v55
v_mfma_f32_16x16x32_bf16 v[56:59],a[84:87],v[196:199],v[56:59]
s_barrier
v_accvgpr_write_b32 a19,v179
v_lshlrev_b32_sdwa v25,1,v25 dst_sel:DWORD dst_unused:UNUSED_PAD src0_sel:DWORD src1_sel:WORD_1
v_mfma_f32_16x16x32_bf16 v[52:55],a[68:71],v[196:199],v[20:23]
s_add_u32 m0,s57,28288
ds_read_u16 v25,v25
v_accvgpr_write_b32 a20,v180
v_mfma_f32_16x16x32_bf16 v[52:55],a[60:63],v[180:183],v[52:55]
s_add_u32 s89,s56,3244032
ds_read_b64_tr_b16 a[92:93],v3 offset:18480
v_lshlrev_b32_sdwa v27,1,v27 dst_sel:DWORD dst_unused:UNUSED_PAD src0_sel:DWORD src1_sel:WORD_1
v_mfma_f32_16x16x32_bf16 v[52:55],a[52:55],a[4:7],v[52:55]
buffer_load_dwordx4 v6,s[36:39],s89 offen lds
v_accvgpr_write_b32 a21,v181
v_mfma_f32_16x16x32_bf16 v[136:139],a[224:227],v[32:35],v[136:139]
ds_read_u16_d16_hi v27,v27
v_lshlrev_b32_sdwa v40,1,v40 dst_sel:DWORD dst_unused:UNUSED_PAD src0_sel:DWORD src1_sel:WORD_1
v_mfma_f32_16x16x32_bf16 v[140:143],a[224:227],v[60:63],v[140:143]
ds_read_u16 v40,v40
v_lshlrev_b32_sdwa v42,1,v42 dst_sel:DWORD dst_unused:UNUSED_PAD src0_sel:DWORD src1_sel:WORD_1
v_accvgpr_write_b32 a22,v182
v_mfma_f32_16x16x32_bf16 v[68:71],a[228:231],v[60:63],v[68:71]
ds_read_u16_d16_hi v42,v42
v_lshlrev_b32_sdwa v41,1,v41 dst_sel:DWORD dst_unused:UNUSED_PAD src0_sel:DWORD src1_sel:WORD_1
v_mfma_f32_16x16x32_bf16 v[64:67],a[228:231],v[32:35],v[64:67]
ds_read_u16 v41,v41
v_accvgpr_write_b32 a23,v183
v_mfma_f32_16x16x32_bf16 v[144:147],a[232:235],v[32:35],v[144:147]
ds_read_b64_tr_b16 a[94:95],v3 offset:20016
ds_read_b64_tr_b16 a[48:49],v3 offset:18488
v_lshlrev_b32_sdwa v43,1,v43 dst_sel:DWORD dst_unused:UNUSED_PAD src0_sel:DWORD src1_sel:WORD_1
v_mfma_f32_16x16x32_bf16 v[148:151],a[232:235],v[60:63],v[148:151]
ds_read_u16_d16_hi v43,v43
v_accvgpr_write_b32 a24,v184
v_lshlrev_b32_sdwa v36,1,v36 dst_sel:DWORD dst_unused:UNUSED_PAD src0_sel:DWORD src1_sel:WORD_1
v_mfma_f32_16x16x32_bf16 v[84:87],a[236:239],v[60:63],v[84:87]
ds_read_u16 v36,v36
ds_read_b64_tr_b16 a[50:51],v3 offset:20024
v_accvgpr_write_b32 a25,v185
v_mfma_f32_16x16x32_bf16 v[80:83],a[236:239],v[32:35],v[80:83]
ds_read_b64_tr_b16 a[56:57],v3 offset:18544
v_lshlrev_b32_sdwa v38,1,v38 dst_sel:DWORD dst_unused:UNUSED_PAD src0_sel:DWORD src1_sel:WORD_1
v_mfma_f32_16x16x32_bf16 v[152:155],a[240:243],v[32:35],v[152:155]
ds_read_u16_d16_hi v38,v38
ds_read_b64_tr_b16 a[58:59],v3 offset:20080
v_lshlrev_b32_sdwa v37,1,v37 dst_sel:DWORD dst_unused:UNUSED_PAD src0_sel:DWORD src1_sel:WORD_1
v_accvgpr_write_b32 a26,v186
v_mfma_f32_16x16x32_bf16 v[156:159],a[240:243],v[60:63],v[156:159]
s_waitcnt lgkmcnt(11)
ds_read_u16 v37,v37
ds_read_b64_tr_b16 a[64:65],v3 offset:18552
v_lshlrev_b32_sdwa v39,1,v39 dst_sel:DWORD dst_unused:UNUSED_PAD src0_sel:DWORD src1_sel:WORD_1
v_mfma_f32_16x16x32_bf16 v[100:103],a[244:247],v[60:63],v[100:103]
s_nop 0
ds_read_u16_d16_hi v39,v39
ds_read_b64_tr_b16 a[66:67],v3 offset:20088
v_accvgpr_write_b32 a27,v187
v_mfma_f32_16x16x32_bf16 v[96:99],a[244:247],v[32:35],v[96:99]
s_add_u32 m0,s57,31832
ds_read_b64_tr_b16 a[248:249],v3 offset:18608
v_accvgpr_write_b32 a28,v188
v_mfma_f32_16x16x32_bf16 v[60:63],a[52:55],a[8:11],v[20:23]
s_add_u32 s89,s56,3342336
ds_read_b64 v[14:15],v7 offset:18480
ds_read_b64_tr_b16 a[72:73],v1 offset:18480
v_lshlrev_b32_sdwa v44,1,v44 dst_sel:DWORD dst_unused:UNUSED_PAD src0_sel:DWORD src1_sel:WORD_1
v_mfma_f32_16x16x32_bf16 v[60:63],a[60:63],a[24:27],v[60:63]
buffer_load_dwordx4 v6,s[36:39],s89 offen lds
ds_read_b64_tr_b16 a[74:75],v1 offset:19504
v_lshl_add_u64 v[28:29],v[28:29],0,v[30:31]
v_fma_mixlo_f16 v8,v57,s99,0
v_mfma_f32_16x16x32_bf16 v[60:63],a[68:71],v[200:203],v[60:63]
ds_read_b64_tr_b16 a[220:221],v1 offset:20528
ds_read_b64_tr_b16 a[222:223],v1 offset:21552
v_lshl_add_u64 v[30:31],v[24:25],0,v[26:27]
v_lshlrev_b32_sdwa v46,1,v46 dst_sel:DWORD dst_unused:UNUSED_PAD src0_sel:DWORD src1_sel:WORD_1
v_mfma_f32_16x16x32_bf16 v[32:35],a[84:87],v[200:203],v[16:19]
ds_read_u16 v44,v44
ds_read_u16_d16_hi v46,v46
v_fma_mixhi_f16 v8,v59,s99,0
v_mfma_f32_16x16x32_bf16 v[32:35],a[80:83],a[24:27],v[32:35]
s_waitcnt lgkmcnt(14)
ds_read_b64_tr_b16 a[216:217],v1 offset:22576
ds_read_b64_tr_b16 a[218:219],v1 offset:23600
v_lshl_add_u64 v[24:25],v[40:41],0,v[42:43]
v_lshlrev_b32_sdwa v45,1,v45 dst_sel:DWORD dst_unused:UNUSED_PAD src0_sel:DWORD src1_sel:WORD_1
v_pk_fma_f16 v9,v8,v8,s54
v_mfma_f32_16x16x32_bf16 v[32:35],a[76:79],a[8:11],v[32:35]
s_waitcnt lgkmcnt(8)
ds_read_u16 v45,v45
ds_read_b64_tr_b16 a[192:193],v2 offset:22576
v_or3_b32 v252,v14,v15,v252
v_lshlrev_b32_sdwa v47,1,v47 dst_sel:DWORD dst_unused:UNUSED_PAD src0_sel:DWORD src1_sel:WORD_1
v_accvgpr_write_b32 a29,v189
v_mfma_f32_16x16x32_bf16 v[40:43],a[76:79],a[12:15],v[16:19]
ds_read_b64 v[14:15],v7 offset:18992
ds_read_b128 v[16:19],v4 offset:512
v_pk_mul_f16 v9,v8,v9
v_mfma_f32_16x16x32_bf16 v[40:43],a[80:83],v[188:191],v[40:43]
ds_read_u16_d16_hi v47,v47
ds_read_b64_tr_b16 a[194:195],v2 offset:23600
v_lshl_add_u64 v[26:27],v[36:37],0,v[38:39]
v_lshlrev_b32_sdwa v48,1,v48 dst_sel:DWORD dst_unused:UNUSED_PAD src0_sel:DWORD src1_sel:WORD_1
v_mfma_f32_16x16x32_bf16 v[40:43],a[84:87],v[204:207],v[40:43]
ds_read_u16 v48,v48
v_exp_f16 v8,v9
v_mfma_f32_16x16x32_bf16 v[36:39],a[68:71],v[204:207],v[20:23]
ds_read_b128 v[20:23],v4 offset:544
ds_read_b64_tr_b16 a[196:197],v2 offset:20528
v_lshlrev_b32_sdwa v50,1,v50 dst_sel:DWORD dst_unused:UNUSED_PAD src0_sel:DWORD src1_sel:WORD_1
v_mfma_f32_16x16x32_bf16 v[36:39],a[60:63],v[188:191],v[36:39]
ds_read_u16_d16_hi v50,v50
ds_read_b64_tr_b16 a[198:199],v2 offset:21552
v_lshlrev_b32_sdwa v49,1,v49 dst_sel:DWORD dst_unused:UNUSED_PAD src0_sel:DWORD src1_sel:WORD_1
v_mfma_f32_16x16x32_bf16 v[36:39],a[52:55],a[12:15],v[36:39]
s_waitcnt lgkmcnt(8)
ds_read_u16 v49,v49
ds_read_b64_tr_b16 a[200:201],v2 offset:18480
v_or3_b32 v252,v14,v15,v252
v_accvgpr_write_b32 a30,v190
v_mfma_f32_16x16x32_bf16 v[112:115],a[224:227],v[28:31],v[112:115]
s_add_u32 m0,s57,35368
ds_read_b64 v[14:15],v7 offset:19504
ds_read_b64_tr_b16 a[202:203],v2 offset:19504
v_exp_f16_sdwa v8,v9 dst_sel:WORD_1 dst_unused:UNUSED_PRESERVE src0_sel:WORD_1
v_lshlrev_b32_sdwa v51,1,v51 dst_sel:DWORD dst_unused:UNUSED_PAD src0_sel:DWORD src1_sel:WORD_1
v_mfma_f32_16x16x32_bf16 v[116:119],a[224:227],v[24:27],v[116:119]
s_add_u32 s89,s56,3440640
ds_read_u16_d16_hi v51,v51
v_lshlrev_b32_sdwa v52,1,v52 dst_sel:DWORD dst_unused:UNUSED_PAD src0_sel:DWORD src1_sel:WORD_1
v_mfma_f32_16x16x32_bf16 v[76:79],a[228:231],v[24:27],v[76:79]
buffer_load_dwordx4 v6,s[36:39],s89 offen lds
ds_read_u16 v52,v52
v_pk_fma_f16 v8,v8,s55,s55
v_mfma_f32_16x16x32_bf16 v[72:75],a[228:231],v[28:31],v[72:75]
v_rcp_f16 v9,v8
v_lshlrev_b32_sdwa v54,1,v54 dst_sel:DWORD dst_unused:UNUSED_PAD src0_sel:DWORD src1_sel:WORD_1
v_mfma_f32_16x16x32_bf16 v[120:123],a[232:235],v[28:31],v[120:123]
ds_read_u16_d16_hi v54,v54
v_lshlrev_b32_sdwa v53,1,v53 dst_sel:DWORD dst_unused:UNUSED_PAD src0_sel:DWORD src1_sel:WORD_1
v_mfma_f32_16x16x32_bf16 v[124:127],a[232:235],v[24:27],v[124:127]
s_waitcnt lgkmcnt(4)
ds_read_u16 v53,v53
v_or3_b32 v252,v14,v15,v252
v_accvgpr_write_b32 a31,v191
v_mfma_f32_16x16x32_bf16 v[92:95],a[236:239],v[24:27],v[92:95]
ds_read_b64_tr_b16 a[250:251],v3 offset:20144
v_lshlrev_b32_sdwa v55,1,v55 dst_sel:DWORD dst_unused:UNUSED_PAD src0_sel:DWORD src1_sel:WORD_1
v_mfma_f32_16x16x32_bf16 v[88:91],a[236:239],v[28:31],v[88:91]
v_rcp_f16_sdwa v9,v8 dst_sel:WORD_1 dst_unused:UNUSED_PRESERVE src0_sel:WORD_1
v_mfma_f32_16x16x32_bf16 v[128:131],a[240:243],v[28:31],v[128:131]
ds_read_u16_d16_hi v55,v55
v_lshlrev_b32_sdwa v56,1,v56 dst_sel:DWORD dst_unused:UNUSED_PAD src0_sel:DWORD src1_sel:WORD_1
v_mfma_f32_16x16x32_bf16 v[132:135],a[240:243],v[24:27],v[132:135]
ds_read_u16 v56,v56
v_lshlrev_b32_sdwa v58,1,v58 dst_sel:DWORD dst_unused:UNUSED_PAD src0_sel:DWORD src1_sel:WORD_1
v_mfma_f32_16x16x32_bf16 v[108:111],a[244:247],v[24:27],v[108:111]
ds_read_u16_d16_hi v58,v58
v_fma_mix_f32 v57,v57,v9,0 op_sel_hi:[0,1,0]
v_mfma_f32_16x16x32_bf16 v[104:107],a[244:247],v[28:31],v[104:107]
ds_read_b64_tr_b16 a[252:253],v3 offset:18616
v_mfma_f32_32x32x16_bf16 v[176:191],a[88:91],a[88:91],0
v_fma_mix_f32 v59,v59,v9,0 op_sel:[0,1,0] op_sel_hi:[0,1,0]
v_mfma_f32_16x16x32_bf16 v[24:27],a[72:75],a[0:3],v[16:19]
s_add_u32 m0,s57,38896
ds_read_b64_tr_b16 a[254:255],v3 offset:20152
v_lshlrev_b32_sdwa v60,1,v60 dst_sel:DWORD dst_unused:UNUSED_PAD src0_sel:DWORD src1_sel:WORD_1
v_mfma_f32_16x16x32_bf16 v[24:27],a[220:223],a[16:19],v[24:27]
s_add_u32 s89,s56,3538944
ds_read_u16 v60,v60
v_lshl_add_u64 v[44:45],v[44:45],0,v[46:47]
v_accvgpr_write_b32 a32,v192
v_mfma_f32_16x16x32_bf16 v[24:27],a[216:219],v[192:195],v[24:27]
s_waitcnt lgkmcnt(7)
buffer_load_dwordx4 v6,s[36:39],s89 offen lds
v_lshl_add_u64 v[46:47],v[48:49],0,v[50:51]
v_lshlrev_b32_sdwa v62,1,v62 dst_sel:DWORD dst_unused:UNUSED_PAD src0_sel:DWORD src1_sel:WORD_1
v_mfma_f32_16x16x32_bf16 v[28:31],a[192:195],v[192:195],v[20:23]
ds_read_u16_d16_hi v62,v62
v_accvgpr_write_b32 a33,v193
v_lshlrev_b32_sdwa v61,1,v61 dst_sel:DWORD dst_unused:UNUSED_PAD src0_sel:DWORD src1_sel:WORD_1
v_mfma_f32_16x16x32_bf16 v[28:31],a[196:199],a[16:19],v[28:31]
s_waitcnt lgkmcnt(6)
ds_read_u16 v61,v61
v_lshl_add_u64 v[48:49],v[52:53],0,v[54:55]
v_mfma_f32_16x16x32_bf16 v[28:31],a[200:203],a[0:3],v[28:31]
v_lshlrev_b32_sdwa v63,1,v63 dst_sel:DWORD dst_unused:UNUSED_PAD src0_sel:DWORD src1_sel:WORD_1
v_accvgpr_write_b32 a34,v194
v_mfma_f32_16x16x32_bf16 v[52:55],a[200:203],a[4:7],v[20:23]
s_waitcnt lgkmcnt(5)
ds_read_u16_d16_hi v63,v63
v_or_b32 v50,v56,v58
v_lshlrev_b32_sdwa v32,1,v32 dst_sel:DWORD dst_unused:UNUSED_PAD src0_sel:DWORD src1_sel:WORD_1
v_accvgpr_write_b32 a35,v195
v_mfma_f32_16x16x32_bf16 v[52:55],a[196:199],a[20:23],v[52:55]
s_waitcnt vmcnt(9)
ds_read_u16 v32,v32
v_cvt_pk_bf16_f32 v51,v57,v59
v_lshlrev_b32_sdwa v34,1,v34 dst_sel:DWORD dst_unused:UNUSED_PAD src0_sel:DWORD src1_sel:WORD_1
v_mfma_f32_16x16x32_bf16 v[52:55],a[192:195],v[196:199],v[52:55]
s_barrier
ds_read_u16_d16_hi v34,v34
v_accvgpr_write_b32 a36,v196
v_mfma_f32_16x16x32_bf16 v[56:59],a[216:219],v[196:199],v[16:19]
ds_read_b64_tr_b16 a[224:225],v3 offset:0
v_lshlrev_b32_sdwa v33,1,v33 dst_sel:DWORD dst_unused:UNUSED_PAD src0_sel:DWORD src1_sel:WORD_1
v_mfma_f32_16x16x32_bf16 v[56:59],a[220:223],a[20:23],v[56:59]
s_add_u32 m0,s58,12320
ds_read_u16 v33,v33
v_accvgpr_write_b32 a37,v197
v_lshlrev_b32_sdwa v35,1,v35 dst_sel:DWORD dst_unused:UNUSED_PAD src0_sel:DWORD src1_sel:WORD_1
v_mfma_f32_16x16x32_bf16 v[56:59],a[72:75],a[4:7],v[56:59]
s_add_u32 s69,s69,s59
ds_read_u16_d16_hi v35,v35
v_accvgpr_write_b32 a38,v198
v_mfma_f32_16x16x32_bf16 v[136:139],a[92:95],v[44:47],v[136:139]
buffer_load_dwordx4 v5,s[44:47],s69 offen offset:0 lds
v_lshlrev_b32_sdwa v36,1,v36 dst_sel:DWORD dst_unused:UNUSED_PAD src0_sel:DWORD src1_sel:WORD_1
v_mfma_f32_16x16x32_bf16 v[140:143],a[92:95],v[48:51],v[140:143]
ds_read_u16 v36,v36
v_lshlrev_b32_sdwa v38,1,v38 dst_sel:DWORD dst_unused:UNUSED_PAD src0_sel:DWORD src1_sel:WORD_1
v_accvgpr_write_b32 a39,v199
v_mfma_f32_16x16x32_bf16 v[68:71],a[48:51],v[48:51],v[68:71]
ds_read_u16_d16_hi v38,v38
v_lshlrev_b32_sdwa v37,1,v37 dst_sel:DWORD dst_unused:UNUSED_PAD src0_sel:DWORD src1_sel:WORD_1
v_mfma_f32_16x16x32_bf16 v[64:67],a[48:51],v[44:47],v[64:67]
s_add_u32 m0,s57,42432
ds_read_u16 v37,v37
v_accvgpr_write_b32 a40,v200
v_mfma_f32_16x16x32_bf16 v[144:147],a[56:59],v[44:47],v[144:147]
s_add_u32 s89,s56,3637248
ds_read_b64_tr_b16 a[226:227],v3 offset:1536
ds_read_b64_tr_b16 a[228:229],v3 offset:8
v_lshlrev_b32_sdwa v39,1,v39 dst_sel:DWORD dst_unused:UNUSED_PAD src0_sel:DWORD src1_sel:WORD_1
v_mfma_f32_16x16x32_bf16 v[148:151],a[56:59],v[48:51],v[148:151]
buffer_load_dwordx4 v6,s[36:39],s89 offen lds
ds_read_u16_d16_hi v39,v39
v_accvgpr_write_b32 a41,v201
v_lshlrev_b32_sdwa v40,1,v40 dst_sel:DWORD dst_unused:UNUSED_PAD src0_sel:DWORD src1_sel:WORD_1
v_mfma_f32_16x16x32_bf16 v[84:87],a[64:67],v[48:51],v[84:87]
s_add_u32 m0,s58,12320
ds_read_u16 v40,v40
ds_read_b64_tr_b16 a[230:231],v3 offset:1544
v_accvgpr_write_b32 a42,v202
v_mfma_f32_16x16x32_bf16 v[80:83],a[64:67],v[44:47],v[80:83]
s_add_u32 s69,s69,s53
ds_read_b64_tr_b16 a[232:233],v3 offset:64
v_lshlrev_b32_sdwa v42,1,v42 dst_sel:DWORD dst_unused:UNUSED_PAD src0_sel:DWORD src1_sel:WORD_1
v_mfma_f32_16x16x32_bf16 v[152:155],a[248:251],v[44:47],v[152:155]
buffer_load_dwordx4 v5,s[44:47],s69 offen offset:1024 lds
ds_read_u16_d16_hi v42,v42
v_lshlrev_b32_sdwa v41,1,v41 dst_sel:DWORD dst_unused:UNUSED_PAD src0_sel:DWORD src1_sel:WORD_1
v_accvgpr_write_b32 a43,v203
v_mfma_f32_16x16x32_bf16 v[156:159],a[248:251],v[48:51],v[156:159]
s_waitcnt lgkmcnt(10)
ds_read_u16 v41,v41
ds_read_b64_tr_b16 a[234:235],v3 offset:1600
v_lshlrev_b32_sdwa v43,1,v43 dst_sel:DWORD dst_unused:UNUSED_PAD src0_sel:DWORD src1_sel:WORD_1
v_mfma_f32_16x16x32_bf16 v[100:103],a[252:255],v[48:51],v[100:103]
s_add_u32 s69,s69,s53
ds_read_u16_d16_hi v43,v43
ds_read_b64_tr_b16 a[236:237],v3 offset:72
v_accvgpr_write_b32 a44,v204
v_mfma_f32_16x16x32_bf16 v[96:99],a[252:255],v[44:47],v[96:99]
buffer_load_dwordx4 v5,s[44:47],s69 offen offset:2048 lds
ds_read_b64_tr_b16 a[238:239],v3 offset:1608
v_accvgpr_write_b32 a45,v205
v_mfma_f32_16x16x32_bf16 v[48:51],a[72:75],a[8:11],v[16:19]
ds_read_b64 v[14:15],v7 offset:0
ds_read_b64_tr_b16 a[204:205],v1 offset:0
v_lshlrev_b32_sdwa v24,1,v24 dst_sel:DWORD dst_unused:UNUSED_PAD src0_sel:DWORD src1_sel:WORD_1
v_mfma_f32_16x16x32_bf16 v[48:51],a[220:223],a[24:27],v[48:51]
ds_read_b64_tr_b16 a[206:207],v1 offset:1024
ds_read_b64_tr_b16 a[208:209],v1 offset:2048
v_lshl_add_u64 v[60:61],v[60:61],0,v[62:63]
v_fma_mixlo_f16 v8,v53,s99,0
v_mfma_f32_16x16x32_bf16 v[48:51],a[216:219],a[40:43],v[48:51]
ds_read_b64_tr_b16 a[210:211],v1 offset:3072
ds_read_u16 v24,v24
v_lshl_add_u64 v[62:63],v[32:33],0,v[34:35]
v_lshlrev_b32_sdwa v26,1,v26 dst_sel:DWORD dst_unused:UNUSED_PAD src0_sel:DWORD src1_sel:WORD_1
v_mfma_f32_16x16x32_bf16 v[44:47],a[192:195],a[40:43],v[20:23]
ds_read_u16_d16_hi v26,v26
ds_read_b64_tr_b16 a[212:213],v1 offset:4096
v_fma_mixhi_f16 v8,v55,s99,0
v_mfma_f32_16x16x32_bf16 v[44:47],a[196:199],a[24:27],v[44:47]
s_waitcnt lgkmcnt(14)
ds_read_b64_tr_b16 a[214:215],v1 offset:5120
ds_read_b64_tr_b16 a[240:241],v3 offset:128
v_lshl_add_u64 v[32:33],v[36:37],0,v[38:39]
v_lshlrev_b32_sdwa v25,1,v25 dst_sel:DWORD dst_unused:UNUSED_PAD src0_sel:DWORD src1_sel:WORD_1
v_pk_fma_f16 v9,v8,v8,s54
v_mfma_f32_16x16x32_bf16 v[44:47],a[200:203],a[8:11],v[44:47]
s_waitcnt lgkmcnt(9)
ds_read_u16 v25,v25
v_or3_b32 v252,v14,v15,v252
v_lshlrev_b32_sdwa v27,1,v27 dst_sel:DWORD dst_unused:UNUSED_PAD src0_sel:DWORD src1_sel:WORD_1
v_mfma_f32_16x16x32_bf16 v[36:39],a[200:203],a[12:15],v[20:23]
s_add_u32 m0,s57,45976
ds_read_b64 v[14:15],v7 offset:512
ds_read_b128 v[20:23],v4 offset:640
v_pk_mul_f16 v9,v8,v9
v_mfma_f32_16x16x32_bf16 v[36:39],a[196:199],a[28:31],v[36:39]
s_add_u32 s89,s56,3735552
ds_read_u16_d16_hi v27,v27
ds_read_b64_tr_b16 a[200:201],v2 offset:4096
v_lshl_add_u64 v[34:35],v[40:41],0,v[42:43]
v_lshlrev_b32_sdwa v28,1,v28 dst_sel:DWORD dst_unused:UNUSED_PAD src0_sel:DWORD src1_sel:WORD_1
v_accvgpr_write_b32 a46,v206
v_mfma_f32_16x16x32_bf16 v[36:39],a[192:195],v[204:207],v[36:39]
ds_read_u16 v28,v28
v_exp_f16 v8,v9
v_mfma_f32_16x16x32_bf16 v[40:43],a[216:219],v[204:207],v[16:19]
buffer_load_dwordx4 v6,s[36:39],s89 offen lds
ds_read_b128 v[16:19],v4 offset:672
v_lshlrev_b32_sdwa v30,1,v30 dst_sel:DWORD dst_unused:UNUSED_PAD src0_sel:DWORD src1_sel:WORD_1
v_mfma_f32_16x16x32_bf16 v[40:43],a[220:223],a[28:31],v[40:43]
ds_read_u16_d16_hi v30,v30
ds_read_b64_tr_b16 a[202:203],v2 offset:5120
v_lshlrev_b32_sdwa v29,1,v29 dst_sel:DWORD dst_unused:UNUSED_PAD src0_sel:DWORD src1_sel:WORD_1
v_mfma_f32_16x16x32_bf16 v[40:43],a[72:75],a[12:15],v[40:43]
s_waitcnt lgkmcnt(7)
ds_read_u16 v29,v29
ds_read_b64_tr_b16 a[196:197],v2 offset:2048
v_or3_b32 v252,v14,v15,v252
v_mfma_f32_16x16x32_bf16 v[112:115],a[92:95],v[60:63],v[112:115]
ds_read_b64 v[14:15],v7 offset:1024
ds_read_b64_tr_b16 a[198:199],v2 offset:3072
v_exp_f16_sdwa v8,v9 dst_sel:WORD_1 dst_unused:UNUSED_PRESERVE src0_sel:WORD_1
v_lshlrev_b32_sdwa v31,1,v31 dst_sel:DWORD dst_unused:UNUSED_PAD src0_sel:DWORD src1_sel:WORD_1
v_mfma_f32_16x16x32_bf16 v[116:119],a[92:95],v[32:35],v[116:119]
ds_read_u16_d16_hi v31,v31
ds_read_b64_tr_b16 a[192:193],v2 offset:0
v_lshlrev_b32_sdwa v56,1,v56 dst_sel:DWORD dst_unused:UNUSED_PAD src0_sel:DWORD src1_sel:WORD_1
v_mfma_f32_16x16x32_bf16 v[76:79],a[48:51],v[32:35],v[76:79]
ds_read_u16 v56,v56
ds_read_b64_tr_b16 a[194:195],v2 offset:1024
v_pk_fma_f16 v8,v8,s55,s55
v_mfma_f32_16x16x32_bf16 v[72:75],a[48:51],v[60:63],v[72:75]
v_rcp_f16_e64 v9,v8
v_lshlrev_b32_sdwa v58,1,v58 dst_sel:DWORD dst_unused:UNUSED_PAD src0_sel:DWORD src1_sel:WORD_1
v_accvgpr_write_b32 a47,v207
v_mfma_f32_16x16x32_bf16 v[120:123],a[56:59],v[60:63],v[120:123]
s_add_u32 m0,s57,49512
ds_read_u16_d16_hi v58,v58
v_lshlrev_b32_sdwa v57,1,v57 dst_sel:DWORD dst_unused:UNUSED_PAD src0_sel:DWORD src1_sel:WORD_1
v_mfma_f32_16x16x32_bf16 v[124:127],a[56:59],v[32:35],v[124:127]
s_waitcnt lgkmcnt(6)
ds_read_u16 v57,v57
v_or3_b32 v252,v14,v15,v252
v_mfma_f32_16x16x32_bf16 v[92:95],a[64:67],v[32:35],v[92:95]
s_add_u32 s89,s56,3833856
ds_read_b64_tr_b16 a[242:243],v3 offset:1664
v_and_b32 v252,0x40004000,v252
v_lshlrev_b32_sdwa v59,1,v59 dst_sel:DWORD dst_unused:UNUSED_PAD src0_sel:DWORD src1_sel:WORD_1
v_mfma_f32_16x16x32_bf16 v[88:91],a[64:67],v[60:63],v[88:91]
buffer_load_dwordx4 v6,s[36:39],s89 offen lds
v_cmp_ne_u32_e32 vcc,0,v252
s_nop 0
s_or_b64 s[90:91],s[90:91],vcc
v_mfma_f32_16x16x32_bf16 v[128:131],a[248:251],v[60:63],v[128:131]
ds_read_u16_d16_hi v59,v59
v_rcp_f16_sdwa v9,v8 dst_sel:WORD_1 dst_unused:UNUSED_PRESERVE src0_sel:WORD_1
v_lshlrev_b32_sdwa v52,1,v52 dst_sel:DWORD dst_unused:UNUSED_PAD src0_sel:DWORD src1_sel:WORD_1
v_mfma_f32_16x16x32_bf16 v[132:135],a[248:251],v[32:35],v[132:135]
ds_read_u16 v52,v52
v_lshlrev_b32_sdwa v54,1,v54 dst_sel:DWORD dst_unused:UNUSED_PAD src0_sel:DWORD src1_sel:WORD_1
v_mfma_f32_16x16x32_bf16 v[108:111],a[252:255],v[32:35],v[108:111]
ds_read_u16_d16_hi v54,v54
v_fma_mix_f32 v53,v53,v9,0 op_sel_hi:[0,1,0]
v_mfma_f32_16x16x32_bf16 v[104:107],a[252:255],v[60:63],v[104:107]
ds_read_b64_tr_b16 a[244:245],v3 offset:136
v_mfma_f32_32x32x16_bf16 v[192:207],a[88:91],a[88:91],0
v_fma_mix_f32 v55,v55,v9,0 op_sel:[0,1,0] op_sel_hi:[0,1,0]
v_mfma_f32_16x16x32_bf16 v[32:35],a[204:207],a[0:3],v[20:23]
ds_read_b64_tr_b16 a[246:247],v3 offset:1672
v_lshlrev_b32_sdwa v48,1,v48 dst_sel:DWORD dst_unused:UNUSED_PAD src0_sel:DWORD src1_sel:WORD_1
v_mfma_f32_16x16x32_bf16 v[32:35],a[208:211],a[16:19],v[32:35]
ds_read_u16 v48,v48
v_lshl_add_u64 v[24:25],v[24:25],0,v[26:27]
v_mfma_f32_16x16x32_bf16 v[32:35],a[212:215],a[32:35],v[32:35]
s_waitcnt lgkmcnt(7)
v_lshl_add_u64 v[26:27],v[28:29],0,v[30:31]
v_lshlrev_b32_sdwa v50,1,v50 dst_sel:DWORD dst_unused:UNUSED_PAD src0_sel:DWORD src1_sel:WORD_1
v_mfma_f32_16x16x32_bf16 v[60:63],a[200:203],a[32:35],v[16:19]
ds_read_u16_d16_hi v50,v50
v_lshlrev_b32_sdwa v49,1,v49 dst_sel:DWORD dst_unused:UNUSED_PAD src0_sel:DWORD src1_sel:WORD_1
v_mfma_f32_16x16x32_bf16 v[60:63],a[196:199],a[16:19],v[60:63]
s_waitcnt lgkmcnt(6)
ds_read_u16 v49,v49
v_lshl_add_u64 v[28:29],v[56:57],0,v[58:59]
v_mfma_f32_16x16x32_bf16 v[60:63],a[192:195],a[0:3],v[60:63]
v_lshlrev_b32_sdwa v51,1,v51 dst_sel:DWORD dst_unused:UNUSED_PAD src0_sel:DWORD src1_sel:WORD_1
v_lshlrev_b32_sdwa v44,1,v44 dst_sel:DWORD dst_unused:UNUSED_PAD src0_sel:DWORD src1_sel:WORD_1
v_mfma_f32_16x16x32_bf16 v[56:59],a[192:195],a[4:7],v[16:19]
s_waitcnt lgkmcnt(5)
ds_read_u16_d16_hi v51,v51
v_or_b32 v30,v52,v54
v_mfma_f32_16x16x32_bf16 v[56:59],a[196:199],a[20:23],v[56:59]
s_waitcnt vmcnt(11)
ds_read_u16 v44,v44
v_cvt_pk_bf16_f32 v31,v53,v55
v_lshlrev_b32_sdwa v46,1,v46 dst_sel:DWORD dst_unused:UNUSED_PAD src0_sel:DWORD src1_sel:WORD_1
v_mfma_f32_16x16x32_bf16 v[56:59],a[200:203],a[36:39],v[56:59]
s_barrier
ds_read_u16_d16_hi v46,v46
v_lshlrev_b32_sdwa v45,1,v45 dst_sel:DWORD dst_unused:UNUSED_PAD src0_sel:DWORD src1_sel:WORD_1
v_mfma_f32_16x16x32_bf16 v[52:55],a[212:215],a[36:39],v[20:23]
s_add_u32 m0,s57,53040
ds_read_u16 v45,v45
v_lshlrev_b32_sdwa v47,1,v47 dst_sel:DWORD dst_unused:UNUSED_PAD src0_sel:DWORD src1_sel:WORD_1
v_mfma_f32_16x16x32_bf16 v[52:55],a[208:211],a[20:23],v[52:55]
s_add_u32 s89,s56,3932160
ds_read_u16_d16_hi v47,v47
v_mfma_f32_32x32x16_bf16 v[240:255],a[88:91],a[88:91],0
v_lshlrev_b32_sdwa v40,1,v40 dst_sel:DWORD dst_unused:UNUSED_PAD src0_sel:DWORD src1_sel:WORD_1
v_lshlrev_b32_sdwa v42,1,v42 dst_sel:DWORD dst_unused:UNUSED_PAD src0_sel:DWORD src1_sel:WORD_1
v_lshlrev_b32_sdwa v41,1,v41 dst_sel:DWORD dst_unused:UNUSED_PAD src0_sel:DWORD src1_sel:WORD_1
v_lshlrev_b32_sdwa v43,1,v43 dst_sel:DWORD dst_unused:UNUSED_PAD src0_sel:DWORD src1_sel:WORD_1
v_lshlrev_b32_sdwa v36,1,v36 dst_sel:DWORD dst_unused:UNUSED_PAD src0_sel:DWORD src1_sel:WORD_1
v_mfma_f32_16x16x32_bf16 v[52:55],a[204:207],a[4:7],v[52:55]
buffer_load_dwordx4 v6,s[36:39],s89 offen lds
ds_read_u16 v40,v40
v_mfma_f32_16x16x32_bf16 v[136:139],a[224:227],v[24:27],v[136:139]
s_add_u32 m0,s58,18480
ds_read_u16_d16_hi v42,v42
ds_read_u16 v41,v41
v_mfma_f32_16x16x32_bf16 v[140:143],a[224:227],v[28:31],v[140:143]
s_add_u32 s69,s69,s59
ds_read_u16_d16_hi v43,v43
v_lshlrev_b32_sdwa v38,1,v38 dst_sel:DWORD dst_unused:UNUSED_PAD src0_sel:DWORD src1_sel:WORD_1
v_mfma_f32_16x16x32_bf16 v[68:71],a[228:231],v[28:31],v[68:71]
buffer_load_dwordx4 v5,s[44:47],s69 offen offset:0 lds
ds_read_u16 v36,v36
v_mfma_f32_16x16x32_bf16 v[64:67],a[228:231],v[24:27],v[64:67]
s_add_u32 s69,s69,s53
ds_read_u16_d16_hi v38,v38
ds_read_b64_tr_b16 a[248:249],v3 offset:6160
v_mfma_f32_16x16x32_bf16 v[144:147],a[232:235],v[24:27],v[144:147]
buffer_load_dwordx4 v5,s[44:47],s69 offen offset:1024 lds
v_lshlrev_b32_sdwa v37,1,v37 dst_sel:DWORD dst_unused:UNUSED_PAD src0_sel:DWORD src1_sel:WORD_1
v_mfma_f32_16x16x32_bf16 v[148:151],a[232:235],v[28:31],v[148:151]
ds_read_u16 v37,v37
v_lshlrev_b32_sdwa v39,1,v39 dst_sel:DWORD dst_unused:UNUSED_PAD src0_sel:DWORD src1_sel:WORD_1
v_mfma_f32_16x16x32_bf16 v[84:87],a[236:239],v[28:31],v[84:87]
ds_read_u16_d16_hi v39,v39
v_mfma_f32_16x16x32_bf16 v[80:83],a[236:239],v[24:27],v[80:83]
ds_read_b64_tr_b16 a[250:251],v3 offset:7696
v_mfma_f32_16x16x32_bf16 v[152:155],a[240:243],v[24:27],v[152:155]
s_add_u32 s69,s69,s53
ds_read_b64_tr_b16 a[252:253],v3 offset:6168
v_mfma_f32_16x16x32_bf16 v[156:159],a[240:243],v[28:31],v[156:159]
s_waitcnt lgkmcnt(8)
buffer_load_dwordx4 v5,s[44:47],s69 offen offset:2048 lds
v_mfma_f32_16x16x32_bf16 v[100:103],a[244:247],v[28:31],v[100:103]
ds_read_b64_tr_b16 a[254:255],v3 offset:7704
v_mfma_f32_16x16x32_bf16 v[96:99],a[244:247],v[24:27],v[96:99]
s_waitcnt vmcnt(13)
s_barrier
v_add_u32_sdwa v24,s60,v10 dst_sel:DWORD dst_unused:UNUSED_PAD src0_sel:DWORD src1_sel:WORD_1
v_subrev_u32 v8,88400,v24
v_min_u32_e64 v24,v24,v8
v_add_u32 v25,7072,v24
v_subrev_u32 v8,88400,v25
v_min_u32_e64 v25,v25,v8
v_lshl_add_u64 v[48:49],v[48:49],0,v[50:51]
v_lshl_add_u64 v[50:51],v[44:45],0,v[46:47]
s_waitcnt lgkmcnt(8)
v_lshl_add_u64 v[44:45],v[40:41],0,v[42:43]
s_waitcnt lgkmcnt(3)
v_lshl_add_u64 v[46:47],v[36:37],0,v[38:39]
v_mfma_f32_16x16x32_bf16 v[112:115],a[224:227],v[48:51],v[112:115]
s_add_u32 m0,s58,0
s_add_u32 s69,s69,s59
buffer_load_dwordx4 v5,s[44:47],s69 offen offset:0 lds
v_xad_u32 v28,v11,0,v24
v_xad_u32 v29,v11,8,v25
v_mfma_f32_16x16x32_bf16 v[116:119],a[224:227],v[44:47],v[116:119]
TE03_%=:
s_cbranch_execz TP4345_%=
v_xad_u32 v30,v11,16,v24
v_xad_u32 v31,v11,24,v25
v_mfma_f32_16x16x32_bf16 v[76:79],a[228:231],v[44:47],v[76:79]
ds_read_b64 a[48:49],v28 offset:0
ds_read_b64 a[50:51],v28 offset:192
v_mfma_f32_16x16x32_bf16 v[72:75],a[228:231],v[48:51],v[72:75]
ds_read_b64 a[52:53],v29 offset:0
ds_read_b64 a[54:55],v29 offset:192
v_mfma_f32_16x16x32_bf16 v[120:123],a[232:235],v[48:51],v[120:123]
ds_read_b64 a[56:57],v28 offset:8
ds_read_b64 a[58:59],v28 offset:200
v_mfma_f32_16x16x32_bf16 v[124:127],a[232:235],v[44:47],v[124:127]
s_add_u32 s69,s69,s53
buffer_load_dwordx4 v5,s[44:47],s69 offen offset:1024 lds
ds_read_b64 a[60:61],v29 offset:8
ds_read_b64 a[62:63],v29 offset:200
v_mfma_f32_16x16x32_bf16 v[92:95],a[236:239],v[44:47],v[92:95]
v_mfma_f32_16x16x32_bf16 v[88:91],a[236:239],v[48:51],v[88:91]
v_mfma_f32_16x16x32_bf16 v[128:131],a[240:243],v[48:51],v[128:131]
v_mfma_f32_16x16x32_bf16 v[132:135],a[240:243],v[44:47],v[132:135]
v_mfma_f32_16x16x32_bf16 v[108:111],a[244:247],v[44:47],v[108:111]
v_mfma_f32_16x16x32_bf16 v[104:107],a[244:247],v[48:51],v[104:107]
s_nop 0
ds_read_b64 a[64:65],v30 offset:0
ds_read_b64 a[66:67],v30 offset:192
ds_read_b64 a[68:69],v31 offset:0
ds_read_b64 a[70:71],v31 offset:192
s_waitcnt lgkmcnt(8)
v_smfmac_f32_16x16x64_bf16 v[160:163],a[96:99],a[48:55],v10
s_waitcnt lgkmcnt(4)
s_add_u32 s69,s69,s53
buffer_load_dwordx4 v5,s[44:47],s69 offen offset:2048 lds
ds_read_b64 a[48:49],v30 offset:8
ds_read_b64 a[50:51],v30 offset:200
ds_read_b64 a[52:53],v31 offset:8
ds_read_b64 a[54:55],v31 offset:200
v_smfmac_f32_16x16x64_bf16 v[164:167],a[100:103],a[56:63],v10
s_waitcnt lgkmcnt(4)
ds_read_b64 a[56:57],v28 offset:64
ds_read_b64 a[58:59],v28 offset:256
ds_read_b64 a[60:61],v29 offset:64
ds_read_b64 a[62:63],v29 offset:256
v_smfmac_f32_16x16x64_bf16 v[168:171],a[104:107],a[64:71],v10
s_waitcnt lgkmcnt(4)
ds_read_b64 a[64:65],v28 offset:72
ds_read_b64 a[66:67],v28 offset:264
ds_read_b64 a[68:69],v29 offset:72
ds_read_b64 a[70:71],v29 offset:264
v_and_b32 v48,8,v11
v_pk_mul_f32 v[44:45],v[160:161],v[160:161]
v_pk_mul_f32 v[46:47],v[162:163],v[162:163]
v_smfmac_f32_16x16x64_bf16 v[172:175],a[108:111],a[48:55],v10
s_waitcnt lgkmcnt(4)
ds_read_b64 a[48:49],v30 offset:64
ds_read_b64 a[50:51],v30 offset:256
ds_read_b64 a[52:53],v31 offset:64
ds_read_b64 a[54:55],v31 offset:256
v_mul_u32_u24 v48,884,v48
s_add_u32 s64,s60,3536
v_pk_add_f32 v[36:37],v[160:161],v[164:165]
v_pk_fma_f32 v[44:45],v[164:165],v[164:165],v[44:45]
v_pk_add_f32 v[38:39],v[162:163],v[166:167]
v_pk_fma_f32 v[46:47],v[166:167],v[166:167],v[46:47]
v_smfmac_f32_16x16x64_bf16 v[176:179],a[128:131],a[56:63],v10
s_waitcnt lgkmcnt(4)
ds_read_b64 a[56:57],v30 offset:72
ds_read_b64 a[58:59],v30 offset:264
ds_read_b64 a[60:61],v31 offset:72
ds_read_b64 a[62:63],v31 offset:264
v_add_u32 v51,s64,v48
v_pk_add_f32 v[36:37],v[36:37],v[168:169]
v_pk_fma_f32 v[44:45],v[168:169],v[168:169],v[44:45]
v_pk_add_f32 v[38:39],v[38:39],v[170:171]
v_pk_fma_f32 v[46:47],v[170:171],v[170:171],v[46:47]
v_smfmac_f32_16x16x64_bf16 v[180:183],a[132:135],a[64:71],v10
s_waitcnt lgkmcnt(4)
ds_read_b64 a[64:65],v28 offset:128
ds_read_b64 a[66:67],v28 offset:320
ds_read_b64 a[68:69],v29 offset:128
ds_read_b64 a[70:71],v29 offset:320
v_subrev_u32 v8,88400,v51
v_pk_add_f32 v[36:37],v[36:37],v[172:173]
v_pk_fma_f32 v[44:45],v[172:173],v[172:173],v[44:45]
v_pk_add_f32 v[38:39],v[38:39],v[174:175]
v_pk_fma_f32 v[46:47],v[174:175],v[174:175],v[46:47]
v_smfmac_f32_16x16x64_bf16 v[184:187],a[136:139],a[48:55],v10
s_waitcnt lgkmcnt(4)
ds_read_b64 a[48:49],v28 offset:136
ds_read_b64 a[50:51],v28 offset:328
ds_read_b64 a[52:53],v29 offset:136
ds_read_b64 a[54:55],v29 offset:328
v_min_u32 v51,v51,v8
v_pk_add_f32 v[36:37],v[36:37],v[176:177]
v_pk_fma_f32 v[44:45],v[176:177],v[176:177],v[44:45]
v_pk_add_f32 v[38:39],v[38:39],v[178:179]
v_pk_fma_f32 v[46:47],v[178:179],v[178:179],v[46:47]
v_smfmac_f32_16x16x64_bf16 v[188:191],a[140:143],a[56:63],v10
s_waitcnt lgkmcnt(4)
ds_read_b64 a[56:57],v30 offset:128
ds_read_b64 a[58:59],v30 offset:320
ds_read_b64 a[60:61],v31 offset:128
ds_read_b64 a[62:63],v31 offset:320
v_xad_u32 v28,v11,32,v24
v_xad_u32 v29,v11,40,v25
v_pk_add_f32 v[36:37],v[36:37],v[180:181]
v_pk_fma_f32 v[44:45],v[180:181],v[180:181],v[44:45]
v_pk_add_f32 v[38:39],v[38:39],v[182:183]
v_pk_fma_f32 v[46:47],v[182:183],v[182:183],v[46:47]
v_smfmac_f32_16x16x64_bf16 v[192:195],a[160:163],a[64:71],v10
s_waitcnt lgkmcnt(4)
ds_read_b64 a[64:65],v30 offset:136
ds_read_b64 a[66:67],v30 offset:328
ds_read_b64 a[68:69],v31 offset:136
ds_read_b64 a[70:71],v31 offset:328
v_add_u32 v50,3536,v51
v_pk_add_f32 v[36:37],v[36:37],v[184:185]
v_pk_fma_f32 v[44:45],v[184:185],v[184:185],v[44:45]
v_pk_add_f32 v[38:39],v[38:39],v[186:187]
v_pk_fma_f32 v[46:47],v[186:187],v[186:187],v[46:47]
v_smfmac_f32_16x16x64_bf16 v[196:199],a[164:167],a[48:55],v10
s_waitcnt lgkmcnt(4)
ds_read_b64 a[48:49],v28 offset:0
ds_read_b64 a[50:51],v28 offset:192
ds_read_b64 a[52:53],v29 offset:0
ds_read_b64 a[54:55],v29 offset:192
v_xad_u32 v30,v11,48,v24
v_xad_u32 v31,v11,56,v25
v_pk_add_f32 v[36:37],v[36:37],v[188:189]
v_pk_fma_f32 v[44:45],v[188:189],v[188:189],v[44:45]
v_pk_add_f32 v[38:39],v[38:39],v[190:191]
v_pk_fma_f32 v[46:47],v[190:191],v[190:191],v[46:47]
v_smfmac_f32_16x16x64_bf16 v[200:203],a[168:171],a[56:63],v10
s_waitcnt lgkmcnt(4)
ds_read_b64 a[56:57],v28 offset:8
ds_read_b64 a[58:59],v28 offset:200
ds_read_b64 a[60:61],v29 offset:8
ds_read_b64 a[62:63],v29 offset:200
v_subrev_u32 v8,88400,v50
v_pk_add_f32 v[36:37],v[36:37],v[192:193]
v_pk_fma_f32 v[44:45],v[192:193],v[192:193],v[44:45]
v_pk_add_f32 v[38:39],v[38:39],v[194:195]
v_pk_fma_f32 v[46:47],v[194:195],v[194:195],v[46:47]
v_smfmac_f32_16x16x64_bf16 v[204:207],a[172:175],a[64:71],v10
s_waitcnt lgkmcnt(4)
ds_read_b64 a[64:65],v28 offset:64
ds_read_b64 a[66:67],v28 offset:256
ds_read_b64 a[68:69],v29 offset:64
ds_read_b64 a[70:71],v29 offset:256
v_min_u32 v50,v50,v8
v_pk_add_f32 v[36:37],v[36:37],v[196:197]
v_pk_fma_f32 v[44:45],v[196:197],v[196:197],v[44:45]
v_pk_add_f32 v[38:39],v[38:39],v[198:199]
v_pk_fma_f32 v[46:47],v[198:199],v[198:199],v[46:47]
v_smfmac_f32_16x16x64_bf16 v[208:211],a[112:115],a[48:55],v10
s_waitcnt lgkmcnt(4)
ds_read_b64 a[48:49],v28 offset:72
ds_read_b64 a[50:51],v28 offset:264
ds_read_b64 a[52:53],v29 offset:72
ds_read_b64 a[54:55],v29 offset:264
v_xor_b32 v9,0x80008,v12
v_pk_add_f32 v[36:37],v[36:37],v[200:201]
v_pk_fma_f32 v[44:45],v[200:201],v[200:201],v[44:45]
v_pk_add_f32 v[38:39],v[38:39],v[202:203]
v_pk_fma_f32 v[46:47],v[202:203],v[202:203],v[46:47]
v_smfmac_f32_16x16x64_bf16 v[212:215],a[116:119],a[56:63],v10
s_waitcnt lgkmcnt(4)
ds_read_b64 a[56:57],v28 offset:128
ds_read_b64 a[58:59],v28 offset:320
ds_read_b64 a[60:61],v29 offset:128
ds_read_b64 a[62:63],v29 offset:320
v_add_u32_sdwa v48,v12,v51 dst_sel:DWORD dst_unused:UNUSED_PAD src0_sel:WORD_0 src1_sel:DWORD
v_pk_add_f32 v[36:37],v[36:37],v[204:205]
v_pk_fma_f32 v[44:45],v[204:205],v[204:205],v[44:45]
v_pk_add_f32 v[38:39],v[38:39],v[206:207]
v_pk_fma_f32 v[46:47],v[206:207],v[206:207],v[46:47]
v_smfmac_f32_16x16x64_bf16 v[216:219],a[144:147],a[64:71],v10
s_waitcnt lgkmcnt(4)
ds_read_b64 a[64:65],v28 offset:136
ds_read_b64 a[66:67],v28 offset:328
ds_read_b64 a[68:69],v29 offset:136
ds_read_b64 a[70:71],v29 offset:328
v_add_u32_sdwa v49,v12,v51 dst_sel:DWORD dst_unused:UNUSED_PAD src0_sel:WORD_1 src1_sel:DWORD
v_pk_add_f32 v[36:37],v[36:37],v[208:209]
v_pk_fma_f32 v[44:45],v[208:209],v[208:209],v[44:45]
v_pk_add_f32 v[38:39],v[38:39],v[210:211]
v_pk_fma_f32 v[46:47],v[210:211],v[210:211],v[46:47]
v_smfmac_f32_16x16x64_bf16 v[220:223],a[148:151],a[48:55],v10
s_waitcnt lgkmcnt(4)
ds_read_b64 a[48:49],v30 offset:0
ds_read_b64 a[50:51],v30 offset:192
ds_read_b64 a[52:53],v31 offset:0
ds_read_b64 a[54:55],v31 offset:192
v_add_u32_sdwa v51,v9,v50 dst_sel:DWORD dst_unused:UNUSED_PAD src0_sel:WORD_1 src1_sel:DWORD
v_pk_add_f32 v[36:37],v[36:37],v[212:213]
v_pk_fma_f32 v[44:45],v[212:213],v[212:213],v[44:45]
v_pk_add_f32 v[38:39],v[38:39],v[214:215]
v_pk_fma_f32 v[46:47],v[214:215],v[214:215],v[46:47]
v_smfmac_f32_16x16x64_bf16 v[224:227],a[176:179],a[56:63],v10
s_waitcnt lgkmcnt(4)
ds_read_b64 a[56:57],v30 offset:8
ds_read_b64 a[58:59],v30 offset:200
ds_read_b64 a[60:61],v31 offset:8
ds_read_b64 a[62:63],v31 offset:200
v_add_u32_sdwa v50,v9,v50 dst_sel:DWORD dst_unused:UNUSED_PAD src0_sel:WORD_0 src1_sel:DWORD
v_pk_add_f32 v[36:37],v[36:37],v[216:217]
v_pk_fma_f32 v[44:45],v[216:217],v[216:217],v[44:45]
v_pk_add_f32 v[38:39],v[38:39],v[218:219]
v_pk_fma_f32 v[46:47],v[218:219],v[218:219],v[46:47]
v_smfmac_f32_16x16x64_bf16 v[228:231],a[180:183],a[64:71],v10
s_waitcnt lgkmcnt(4)
ds_read_b64 a[64:65],v30 offset:64
ds_read_b64 a[66:67],v30 offset:256
ds_read_b64 a[68:69],v31 offset:64
ds_read_b64 a[70:71],v31 offset:256
v_pk_add_f32 v[36:37],v[36:37],v[220:221]
v_pk_fma_f32 v[44:45],v[220:221],v[220:221],v[44:45]
v_pk_add_f32 v[38:39],v[38:39],v[222:223]
v_pk_fma_f32 v[46:47],v[222:223],v[222:223],v[46:47]
v_smfmac_f32_16x16x64_bf16 v[232:235],a[120:123],a[48:55],v10
s_waitcnt lgkmcnt(4)
ds_read_b64 a[48:49],v30 offset:72
ds_read_b64 a[50:51],v30 offset:264
ds_read_b64 a[52:53],v31 offset:72
ds_read_b64 a[54:55],v31 offset:264
v_pk_add_f32 v[36:37],v[36:37],v[224:225]
v_pk_fma_f32 v[44:45],v[224:225],v[224:225],v[44:45]
v_pk_add_f32 v[38:39],v[38:39],v[226:227]
v_pk_fma_f32 v[46:47],v[226:227],v[226:227],v[46:47]
v_smfmac_f32_16x16x64_bf16 v[236:239],a[124:127],a[56:63],v10
s_waitcnt lgkmcnt(4)
ds_read_b64 a[56:57],v30 offset:128
ds_read_b64 a[58:59],v30 offset:320
ds_read_b64 a[60:61],v31 offset:128
ds_read_b64 a[62:63],v31 offset:320
v_pk_add_f32 v[36:37],v[36:37],v[228:229]
v_pk_fma_f32 v[44:45],v[228:229],v[228:229],v[44:45]
v_pk_add_f32 v[38:39],v[38:39],v[230:231]
v_pk_fma_f32 v[46:47],v[230:231],v[230:231],v[46:47]
v_smfmac_f32_16x16x64_bf16 v[240:243],a[152:155],a[64:71],v10
s_waitcnt lgkmcnt(4)
ds_read_b64 a[64:65],v30 offset:136
ds_read_b64 a[66:67],v30 offset:328
ds_read_b64 a[68:69],v31 offset:136
ds_read_b64 a[70:71],v31 offset:328
v_pk_add_f32 v[36:37],v[36:37],v[232:233]
v_pk_fma_f32 v[44:45],v[232:233],v[232:233],v[44:45]
v_pk_add_f32 v[38:39],v[38:39],v[234:235]
v_pk_fma_f32 v[46:47],v[234:235],v[234:235],v[46:47]
v_smfmac_f32_16x16x64_bf16 v[244:247],a[156:159],a[48:55],v10
s_waitcnt lgkmcnt(4)
ds_read_b64 a[224:225],v48 offset:57344
ds_read_b64 a[226:227],v48 offset:57352
ds_read_b64 a[228:229],v49 offset:57344
ds_read_b64 a[230:231],v49 offset:57352
ds_read_b128 a[240:243],v13 offset:0
v_pk_add_f32 v[36:37],v[36:37],v[236:237]
v_pk_fma_f32 v[44:45],v[236:237],v[236:237],v[44:45]
v_pk_add_f32 v[38:39],v[38:39],v[238:239]
v_pk_fma_f32 v[46:47],v[238:239],v[238:239],v[46:47]
v_smfmac_f32_16x16x64_bf16 v[248:251],a[184:187],a[56:63],v10
s_waitcnt lgkmcnt(5)
ds_read_b64 a[232:233],v50 offset:57344
ds_read_b64 a[234:235],v50 offset:57352
ds_read_b64 a[236:237],v51 offset:57344
ds_read_b64 a[238:239],v51 offset:57352
ds_read_b128 a[244:247],v13 offset:256
v_pk_add_f32 v[36:37],v[36:37],v[240:241]
v_pk_fma_f32 v[44:45],v[240:241],v[240:241],v[44:45]
v_pk_add_f32 v[38:39],v[38:39],v[242:243]
v_pk_fma_f32 v[46:47],v[242:243],v[242:243],v[46:47]
v_smfmac_f32_16x16x64_bf16 v[252:255],a[188:191],a[64:71],v10
ds_read_b128 v[28:31],v4 offset:3584
ds_read_b128 v[24:27],v4 offset:3616
v_pk_add_f32 v[36:37],v[36:37],v[244:245]
v_pk_fma_f32 v[44:45],v[244:245],v[244:245],v[44:45]
v_pk_add_f32 v[38:39],v[38:39],v[246:247]
v_pk_fma_f32 v[46:47],v[246:247],v[246:247],v[46:47]
v_pk_add_f32 v[36:37],v[36:37],v[248:249]
v_pk_fma_f32 v[44:45],v[248:249],v[248:249],v[44:45]
v_pk_add_f32 v[38:39],v[38:39],v[250:251]
v_pk_fma_f32 v[46:47],v[250:251],v[250:251],v[46:47]
v_pk_add_f32 v[36:37],v[36:37],v[252:253]
v_pk_fma_f32 v[44:45],v[252:253],v[252:253],v[44:45]
v_pk_add_f32 v[38:39],v[38:39],v[254:255]
v_pk_fma_f32 v[46:47],v[254:255],v[254:255],v[46:47]
s_nop 0
v_permlane32_swap_b32 v36,v38
v_permlane32_swap_b32 v37,v39
v_permlane32_swap_b32 v44,v46
v_permlane32_swap_b32_e64 v45,v47
v_pk_add_f32 v[36:37],v[36:37],v[38:39]
v_pk_add_f32 v[44:45],v[44:45],v[46:47]
s_add_u32 s60,s60,56576
s_sub_u32 s64,s60,88400
s_min_u32 s60,s60,s64
v_permlane16_swap_b32 v36,v37
v_permlane16_swap_b32 v44,v45
v_add_f32 v8,v36,v37
v_add_f32_e64 v9,v44,v45
v_mul_f32 v8,0x3c2aaaab,v8
v_mul_f32 v9,0x3c2aaaab,v9
v_cmp_ngt_f32 vcc,0x5d800000,v9
v_fma_f32 v9,-v8,v8,v9
v_max_f32 v9,0,v9
s_or_b64 s[90:91],s[90:91],vcc
v_add_f32 v9,0x3727c5ac,v9
v_rsq_f32 v9,v9
s_nop 0
v_mul_f32 v36,s96,v9
v_mul_f32 v37,s96,v9
v_mul_f32 v44,-v8,v36
v_mul_f32 v45,-v8,v36
v_permlane16_swap_b32 v36,v37
s_nop 0
v_permlane16_swap_b32 v44,v45
v_mov_b64 v[38:39],v[36:37]
v_mov_b64 v[46:47],v[44:45]
s_nop 0
v_permlane32_swap_b32 v36,v38
v_permlane32_swap_b32 v37,v39
v_permlane32_swap_b32 v44,v46
v_permlane32_swap_b32 v45,v47
v_pk_fma_f32 v[160:161],v[160:161],v[36:37],v[44:45]
v_pk_fma_f32 v[162:163],v[162:163],v[38:39],v[46:47]
v_pk_fma_f32 v[164:165],v[164:165],v[36:37],v[44:45]
v_pk_fma_f32 v[166:167],v[166:167],v[38:39],v[46:47]
v_pk_fma_f32 v[168:169],v[168:169],v[36:37],v[44:45]
v_pk_fma_f32 v[170:171],v[170:171],v[38:39],v[46:47]
v_pk_fma_f32 v[172:173],v[172:173],v[36:37],v[44:45]
v_pk_fma_f32 v[174:175],v[174:175],v[38:39],v[46:47]
v_pk_fma_f32 v[208:209],v[208:209],v[36:37],v[44:45]
v_pk_fma_f32 v[210:211],v[210:211],v[38:39],v[46:47]
v_pk_fma_f32 v[212:213],v[212:213],v[36:37],v[44:45]
v_pk_fma_f32 v[214:215],v[214:215],v[38:39],v[46:47]
v_pk_fma_f32 v[232:233],v[232:233],v[36:37],v[44:45]
v_pk_fma_f32 v[234:235],v[234:235],v[38:39],v[46:47]
v_pk_fma_f32 v[236:237],v[236:237],v[36:37],v[44:45]
v_pk_fma_f32 v[238:239],v[238:239],v[38:39],v[46:47]
v_pk_fma_f32 v[176:177],v[176:177],v[36:37],v[44:45]
v_pk_fma_f32 v[178:179],v[178:179],v[38:39],v[46:47]
v_pk_fma_f32 v[180:181],v[180:181],v[36:37],v[44:45]
v_pk_fma_f32 v[182:183],v[182:183],v[38:39],v[46:47]
v_pk_fma_f32 v[184:185],v[184:185],v[36:37],v[44:45]
v_pk_fma_f32 v[186:187],v[186:187],v[38:39],v[46:47]
v_pk_fma_f32 v[188:189],v[188:189],v[36:37],v[44:45]
v_pk_fma_f32 v[190:191],v[190:191],v[38:39],v[46:47]
v_pk_fma_f32 v[216:217],v[216:217],v[36:37],v[44:45]
v_pk_fma_f32 v[218:219],v[218:219],v[38:39],v[46:47]
v_pk_fma_f32 v[220:221],v[220:221],v[36:37],v[44:45]
v_pk_fma_f32 v[222:223],v[222:223],v[38:39],v[46:47]
v_pk_fma_f32 v[240:241],v[240:241],v[36:37],v[44:45]
v_pk_fma_f32 v[242:243],v[242:243],v[38:39],v[46:47]
v_pk_fma_f32 v[244:245],v[244:245],v[36:37],v[44:45]
v_pk_fma_f32 v[246:247],v[246:247],v[38:39],v[46:47]
v_pk_fma_f32 v[192:193],v[192:193],v[36:37],v[44:45]
v_pk_fma_f32 v[194:195],v[194:195],v[38:39],v[46:47]
v_pk_fma_f32 v[196:197],v[196:197],v[36:37],v[44:45]
v_pk_fma_f32 v[198:199],v[198:199],v[38:39],v[46:47]
v_pk_fma_f32 v[200:201],v[200:201],v[36:37],v[44:45]
v_pk_fma_f32 v[202:203],v[202:203],v[38:39],v[46:47]
v_pk_fma_f32 v[204:205],v[204:205],v[36:37],v[44:45]
v_pk_fma_f32 v[206:207],v[206:207],v[38:39],v[46:47]
v_pk_fma_f32 v[224:225],v[224:225],v[36:37],v[44:45]
v_pk_fma_f32 v[226:227],v[226:227],v[38:39],v[46:47]
v_pk_fma_f32 v[228:229],v[228:229],v[36:37],v[44:45]
v_pk_fma_f32 v[230:231],v[230:231],v[38:39],v[46:47]
v_pk_fma_f32 v[248:249],v[248:249],v[36:37],v[44:45]
v_pk_fma_f32 v[250:251],v[250:251],v[38:39],v[46:47]
v_pk_fma_f32 v[252:253],v[252:253],v[36:37],v[44:45]
v_pk_fma_f32 v[254:255],v[254:255],v[38:39],v[46:47]
v_mfma_f32_16x16x32_bf16 v[40:43],a[204:207],a[8:11],v[20:23]
ds_read_b64_tr_b16 a[216:217],v1 offset:6160
v_cvt_pk_bf16_f32 v160,v160,v164
v_cvt_pk_bf16_f32 v168,v168,v172
v_cvt_pk_bf16_f32 v208,v208,v212
v_lshlrev_b32_sdwa v32,1,v32 dst_sel:DWORD dst_unused:UNUSED_PAD src0_sel:DWORD src1_sel:WORD_1
v_mfma_f32_16x16x32_bf16 v[40:43],a[208:211],a[24:27],v[40:43]
ds_read_u16 v32,v32
v_accvgpr_write_b32 a48,v160
v_accvgpr_write_b32 a49,v168
v_cvt_pk_bf16_f32 v232,v232,v236
v_accvgpr_write_b32 a50,v208
v_mfma_f32_16x16x32_bf16 v[40:43],a[212:215],a[40:43],v[40:43]
ds_read_b64_tr_b16 a[218:219],v1 offset:7184
v_cvt_pk_bf16_f32 v161,v161,v165
v_accvgpr_write_b32 a51,v232
v_cvt_pk_bf16_f32 v169,v169,v173
v_accvgpr_write_b32 a52,v161
v_mfma_f32_16x16x32_bf16 v[36:39],a[200:203],a[40:43],v[16:19]
ds_read_b64_tr_b16 a[220:221],v1 offset:8208
v_cvt_pk_bf16_f32 v209,v209,v213
v_accvgpr_write_b32 a53,v169
v_cvt_pk_bf16_f32 v233,v233,v237
v_lshlrev_b32_sdwa v34,1,v34 dst_sel:DWORD dst_unused:UNUSED_PAD src0_sel:DWORD src1_sel:WORD_1
v_accvgpr_write_b32 a54,v209
v_mfma_f32_16x16x32_bf16 v[36:39],a[196:199],a[24:27],v[36:39]
ds_read_u16_d16_hi v34,v34
v_cvt_pk_bf16_f32 v162,v162,v166
v_accvgpr_write_b32 a55,v233
v_cvt_pk_bf16_f32 v170,v170,v174
v_accvgpr_write_b32 a56,v162
v_mfma_f32_16x16x32_bf16 v[36:39],a[192:195],a[8:11],v[36:39]
ds_read_b64_tr_b16 a[222:223],v1 offset:9232
v_accvgpr_write_b32 a57,v170
v_cvt_pk_bf16_f32 v210,v210,v214
v_accvgpr_write_b32 a58,v210
v_lshlrev_b32_sdwa v33,1,v33 dst_sel:DWORD dst_unused:UNUSED_PAD src0_sel:DWORD src1_sel:WORD_1
v_cvt_pk_bf16_f32 v234,v234,v238
v_mfma_f32_16x16x32_bf16 v[44:47],a[192:195],a[12:15],v[16:19]
ds_read_u16 v33,v33
v_accvgpr_write_b32 a59,v234
v_cvt_pk_bf16_f32 v163,v163,v167
v_accvgpr_write_b32 a60,v163
v_mfma_f32_16x16x32_bf16 v[44:47],a[196:199],a[28:31],v[44:47]
v_cvt_pk_bf16_f32 v171,v171,v175
v_accvgpr_write_b32 a61,v171
v_cvt_pk_bf16_f32 v211,v211,v215
v_accvgpr_write_b32 a62,v211
v_lshlrev_b32_sdwa v35,1,v35 dst_sel:DWORD dst_unused:UNUSED_PAD src0_sel:DWORD src1_sel:WORD_1
v_mfma_f32_16x16x32_bf16 v[44:47],a[200:203],a[44:47],v[44:47]
s_waitcnt lgkmcnt(7)
ds_read_u16_d16_hi v35,v35
v_cvt_pk_bf16_f32 v235,v235,v239
v_accvgpr_write_b32 a63,v235
v_cvt_pk_bf16_f32 v176,v176,v180
v_cvt_pk_bf16_f32 v184,v184,v188
v_mfma_f32_16x16x32_bf16 v[232:235],a[240:243],a[224:227],v[28:31]
s_nop 0
v_cvt_pk_bf16_f32 v216,v216,v220
v_accvgpr_write_b32 a64,v176
v_accvgpr_write_b32 a65,v184
v_cvt_pk_bf16_f32 v240,v240,v244
v_lshlrev_b32_sdwa v60,1,v60 dst_sel:DWORD dst_unused:UNUSED_PAD src0_sel:DWORD src1_sel:WORD_1
v_mfma_f32_16x16x32_bf16 v[236:239],a[240:243],a[228:231],v[28:31]
ds_read_u16 v60,v60
v_accvgpr_write_b32 a66,v216
v_cvt_pk_bf16_f32 v177,v177,v181
v_accvgpr_write_b32 a67,v240
v_cvt_pk_bf16_f32 v185,v185,v189
v_mfma_f32_16x16x32_bf16 v[208:211],a[240:243],a[232:235],v[28:31]
v_accvgpr_write_b32 a68,v177
v_cvt_pk_bf16_f32 v217,v217,v221
v_accvgpr_write_b32 a69,v185
v_lshlrev_b32_sdwa v62,1,v62 dst_sel:DWORD dst_unused:UNUSED_PAD src0_sel:DWORD src1_sel:WORD_1
v_cvt_pk_bf16_f32 v241,v241,v245
v_mfma_f32_16x16x32_bf16 v[212:215],a[240:243],a[236:239],v[28:31]
ds_read_b128 a[240:243],v13 offset:512
ds_read_b128 v[28:31],v4 offset:3712
v_accvgpr_write_b32 a70,v217
v_cvt_pk_bf16_f32 v178,v178,v182
v_accvgpr_write_b32 a71,v241
v_mfma_f32_16x16x32_bf16 v[160:163],a[244:247],a[224:227],v[24:27]
ds_read_b64 a[224:225],v48 offset:57408
ds_read_b64 a[226:227],v48 offset:57416
v_cvt_pk_bf16_f32 v186,v186,v190
v_accvgpr_write_b32 a72,v178
v_accvgpr_write_b32 a73,v186
v_cvt_pk_bf16_f32 v218,v218,v222
v_lshlrev_b32_sdwa v61,1,v61 dst_sel:DWORD dst_unused:UNUSED_PAD src0_sel:DWORD src1_sel:WORD_1
v_mfma_f32_16x16x32_bf16 v[164:167],a[244:247],a[228:231],v[24:27]
ds_read_b64 a[228:229],v49 offset:57408
ds_read_b64 a[230:231],v49 offset:57416
v_accvgpr_write_b32 a74,v218
v_cvt_pk_bf16_f32 v242,v242,v246
v_accvgpr_write_b32 a75,v242
v_cvt_pk_bf16_f32 v179,v179,v183
v_mfma_f32_16x16x32_bf16 v[168:171],a[244:247],a[232:235],v[24:27]
ds_read_b64 a[232:233],v50 offset:57408
ds_read_b64 a[234:235],v50 offset:57416
v_accvgpr_write_b32 a76,v179
v_cvt_pk_bf16_f32 v187,v187,v191
v_accvgpr_write_b32 a77,v187
v_cvt_pk_bf16_f32 v219,v219,v223
v_lshlrev_b32_sdwa v63,1,v63 dst_sel:DWORD dst_unused:UNUSED_PAD src0_sel:DWORD src1_sel:WORD_1
v_mfma_f32_16x16x32_bf16 v[172:175],a[244:247],a[236:239],v[24:27]
s_waitcnt lgkmcnt(4)
ds_read_b64 a[236:237],v51 offset:57408
ds_read_b64 a[238:239],v51 offset:57416
v_accvgpr_write_b32 a78,v219
v_cvt_pk_bf16_f32 v243,v243,v247
v_accvgpr_write_b32 a79,v243
v_cvt_pk_bf16_f32 v192,v192,v196
v_mfma_f32_16x16x32_bf16 v[240:243],a[240:243],a[224:227],v[28:31]
s_waitcnt lgkmcnt(4)
ds_read_b128 a[244:247],v13 offset:768
ds_read_b128 v[24:27],v4 offset:3744
v_cvt_pk_bf16_f32 v200,v200,v204
v_cvt_pk_bf16_f32 v224,v224,v228
v_accvgpr_write_b32 a80,v192
v_accvgpr_write_b32 a81,v200
v_lshlrev_b32_sdwa v52,1,v52 dst_sel:DWORD dst_unused:UNUSED_PAD src0_sel:DWORD src1_sel:WORD_1
v_mfma_f32_16x16x32_bf16 v[244:247],a[240:243],a[228:231],v[28:31]
s_waitcnt lgkmcnt(4)
ds_read_u16_d16_hi v62,v62
v_cvt_pk_bf16_f32 v248,v248,v252
v_accvgpr_write_b32 a82,v224
v_cvt_pk_bf16_f32 v193,v193,v197
v_mfma_f32_16x16x32_bf16 v[216:219],a[240:243],a[232:235],v[28:31]
s_waitcnt lgkmcnt(3)
ds_read_u16 v61,v61
v_accvgpr_write_b32 a83,v248
v_cvt_pk_bf16_f32 v201,v201,v205
v_accvgpr_write_b32 a84,v193
v_cvt_pk_bf16_f32 v225,v225,v229
v_lshlrev_b32_sdwa v54,1,v54 dst_sel:DWORD dst_unused:UNUSED_PAD src0_sel:DWORD src1_sel:WORD_1
v_mfma_f32_16x16x32_bf16 v[220:223],a[240:243],a[236:239],v[28:31]
s_waitcnt lgkmcnt(2)
ds_read_b128 a[240:243],v13 offset:1024
ds_read_b128 v[28:31],v4 offset:3840
v_accvgpr_write_b32 a85,v201
v_cvt_pk_bf16_f32 v249,v249,v253
v_accvgpr_write_b32 a86,v225
v_cvt_pk_bf16_f32 v194,v194,v198
v_mfma_f32_16x16x32_bf16 v[176:179],a[244:247],a[224:227],v[24:27]
s_nop 0
ds_read_b64 a[224:225],v48 offset:57472
ds_read_b64 a[226:227],v48 offset:57480
v_accvgpr_write_b32 a87,v249
v_cvt_pk_bf16_f32 v202,v202,v206
v_accvgpr_write_b32 a88,v194
v_accvgpr_write_b32 a89,v202
v_lshlrev_b32_sdwa v53,1,v53 dst_sel:DWORD dst_unused:UNUSED_PAD src0_sel:DWORD src1_sel:WORD_1
v_mfma_f32_16x16x32_bf16 v[180:183],a[244:247],a[228:231],v[24:27]
ds_read_b64 a[228:229],v49 offset:57472
ds_read_b64 a[230:231],v49 offset:57480
v_cvt_pk_bf16_f32 v226,v226,v230
v_accvgpr_write_b32 a90,v226
v_cvt_pk_bf16_f32 v250,v250,v254
v_accvgpr_write_b32 a91,v250
v_mfma_f32_16x16x32_bf16 v[184:187],a[244:247],a[232:235],v[24:27]
s_nop 0
ds_read_b64 a[232:233],v50 offset:57472
ds_read_b64 a[234:235],v50 offset:57480
v_cvt_pk_bf16_f32 v195,v195,v199
v_accvgpr_write_b32 a92,v195
v_cvt_pk_bf16_f32 v203,v203,v207
v_accvgpr_write_b32 a93,v203
v_mfma_f32_16x16x32_bf16 v[188:191],a[244:247],a[236:239],v[24:27]
s_waitcnt lgkmcnt(4)
ds_read_b64 a[236:237],v51 offset:57472
ds_read_b64 a[238:239],v51 offset:57480
v_cvt_pk_bf16_f32 v227,v227,v231
v_accvgpr_write_b32 a94,v227
v_cvt_pk_bf16_f32 v251,v251,v255
TP2753_%=:
s_cbranch_execz TP4880_%=
v_lshlrev_b32_sdwa v55,1,v55 dst_sel:DWORD dst_unused:UNUSED_PAD src0_sel:DWORD src1_sel:WORD_1
v_accvgpr_write_b32 a95,v251
v_mfma_f32_16x16x32_bf16 v[248:251],a[240:243],a[224:227],v[28:31]
s_waitcnt lgkmcnt(4)
ds_read_b128 a[244:247],v13 offset:1280
ds_read_b128 v[24:27],v4 offset:3872
v_mfma_f32_16x16x32_bf16 v[252:255],a[240:243],a[228:231],v[28:31]
s_waitcnt lgkmcnt(4)
ds_read_u16_d16_hi v63,v63
ds_read_u16 v52,v52
v_lshlrev_b32_sdwa v56,1,v56 dst_sel:DWORD dst_unused:UNUSED_PAD src0_sel:DWORD src1_sel:WORD_1
v_mfma_f32_16x16x32_bf16 v[224:227],a[240:243],a[232:235],v[28:31]
s_waitcnt lgkmcnt(4)
ds_read_u16_d16_hi v54,v54
v_lshlrev_b32_sdwa v58,1,v58 dst_sel:DWORD dst_unused:UNUSED_PAD src0_sel:DWORD src1_sel:WORD_1
v_mfma_f32_16x16x32_bf16 v[228:231],a[240:243],a[236:239],v[28:31]
s_waitcnt lgkmcnt(3)
ds_read_b64_tr_b16 a[240:241],v3 offset:6224
ds_read_b64_tr_b16 a[242:243],v3 offset:7760
v_mfma_f32_16x16x32_bf16 v[192:195],a[244:247],a[224:227],v[24:27]
ds_read_b64_tr_b16 a[224:225],v3 offset:6232
ds_read_b64_tr_b16 a[226:227],v3 offset:7768
v_mfma_f32_16x16x32_bf16 v[196:199],a[244:247],a[228:231],v[24:27]
ds_read_b64_tr_b16 a[228:229],v3 offset:6288
ds_read_b64_tr_b16 a[230:231],v3 offset:7824
v_lshlrev_b32_sdwa v57,1,v57 dst_sel:DWORD dst_unused:UNUSED_PAD src0_sel:DWORD src1_sel:WORD_1
v_mfma_f32_16x16x32_bf16 v[200:203],a[244:247],a[232:235],v[24:27]
ds_read_b64_tr_b16 a[232:233],v3 offset:6296
ds_read_b64_tr_b16 a[234:235],v3 offset:7832
v_mfma_f32_16x16x32_bf16 v[204:207],a[244:247],a[236:239],v[24:27]
ds_read_u16 v53,v53
ds_read_u16_d16_hi v55,v55
v_lshlrev_b32_sdwa v59,1,v59 dst_sel:DWORD dst_unused:UNUSED_PAD src0_sel:DWORD src1_sel:WORD_1
v_mfma_f32_16x16x32_bf16 v[48:51],a[212:215],a[44:47],v[20:23]
ds_read_u16 v56,v56
v_mfma_f32_16x16x32_bf16 v[48:51],a[208:211],a[28:31],v[48:51]
s_waitcnt vmcnt(9)
ds_read_u16_d16_hi v58,v58
v_mfma_f32_16x16x32_bf16 v[48:51],a[204:207],a[12:15],v[48:51]
s_barrier
ds_read_u16 v57,v57
ds_read_u16_d16_hi v59,v59
v_mfma_f32_16x16x32_bf16 v[28:31],a[204:207],a[48:51],v[20:23]
s_add_u32 m0,s57,56576
ds_read_b64 v[14:15],v7 offset:6160
v_lshlrev_b32_sdwa v40,1,v40 dst_sel:DWORD dst_unused:UNUSED_PAD src0_sel:DWORD src1_sel:WORD_1
v_mfma_f32_16x16x32_bf16 v[28:31],a[208:211],a[64:67],v[28:31]
s_add_u32 s89,s56,4030464
ds_read_u16 v40,v40
v_lshl_add_u64 v[32:33],v[32:33],0,v[34:35]
v_mfma_f32_16x16x32_bf16 v[28:31],a[212:215],a[80:83],v[28:31]
s_waitcnt lgkmcnt(12)
buffer_load_dwordx4 v6,s[36:39],s89 offen lds
v_lshl_add_u64 v[34:35],v[60:61],0,v[62:63]
v_lshlrev_b32_sdwa v42,1,v42 dst_sel:DWORD dst_unused:UNUSED_PAD src0_sel:DWORD src1_sel:WORD_1
v_mfma_f32_16x16x32_bf16 v[24:27],a[200:203],a[80:83],v[16:19]
ds_read_u16_d16_hi v42,v42
v_lshlrev_b32_sdwa v41,1,v41 dst_sel:DWORD dst_unused:UNUSED_PAD src0_sel:DWORD src1_sel:WORD_1
v_mfma_f32_16x16x32_bf16 v[24:27],a[196:199],a[64:67],v[24:27]
s_waitcnt lgkmcnt(7)
ds_read_u16 v41,v41
v_lshl_add_u64 v[60:61],v[52:53],0,v[54:55]
v_mfma_f32_16x16x32_bf16 v[24:27],a[192:195],a[48:51],v[24:27]
s_waitcnt lgkmcnt(3)
v_or3_b32 v13,v14,v15,0
v_lshlrev_b32_sdwa v43,1,v43 dst_sel:DWORD dst_unused:UNUSED_PAD src0_sel:DWORD src1_sel:WORD_1
v_mfma_f32_16x16x32_bf16 v[52:55],a[192:195],a[52:55],v[16:19]
ds_read_b64 v[14:15],v7 offset:6672
v_lshlrev_b32_sdwa v36,1,v36 dst_sel:DWORD dst_unused:UNUSED_PAD src0_sel:DWORD src1_sel:WORD_1
v_mfma_f32_16x16x32_bf16 v[52:55],a[196:199],a[68:71],v[52:55]
ds_read_u16_d16_hi v43,v43
v_lshl_add_u64 v[62:63],v[56:57],0,v[58:59]
v_lshlrev_b32_sdwa v38,1,v38 dst_sel:DWORD dst_unused:UNUSED_PAD src0_sel:DWORD src1_sel:WORD_1
v_mfma_f32_16x16x32_bf16 v[52:55],a[200:203],a[84:87],v[52:55]
ds_read_u16 v36,v36
v_lshlrev_b32_sdwa v37,1,v37 dst_sel:DWORD dst_unused:UNUSED_PAD src0_sel:DWORD src1_sel:WORD_1
v_mfma_f32_16x16x32_bf16 v[56:59],a[212:215],a[84:87],v[20:23]
ds_read_u16_d16_hi v38,v38
v_lshlrev_b32_sdwa v39,1,v39 dst_sel:DWORD dst_unused:UNUSED_PAD src0_sel:DWORD src1_sel:WORD_1
v_mfma_f32_16x16x32_bf16 v[56:59],a[208:211],a[68:71],v[56:59]
ds_read_u16 v37,v37
v_lshlrev_b32_sdwa v48,1,v48 dst_sel:DWORD dst_unused:UNUSED_PAD src0_sel:DWORD src1_sel:WORD_1
v_mfma_f32_16x16x32_bf16 v[56:59],a[204:207],a[52:55],v[56:59]
s_waitcnt lgkmcnt(4)
ds_read_u16_d16_hi v39,v39
v_or3_b32 v13,v14,v15,v13
v_mfma_f32_16x16x32_bf16 v[136:139],a[248:251],v[32:35],v[136:139]
ds_read_b64 v[14:15],v7 offset:7184
v_lshlrev_b32_sdwa v50,1,v50 dst_sel:DWORD dst_unused:UNUSED_PAD src0_sel:DWORD src1_sel:WORD_1
v_mfma_f32_16x16x32_bf16 v[140:143],a[248:251],v[60:63],v[140:143]
ds_read_u16 v48,v48
v_lshlrev_b32_sdwa v49,1,v49 dst_sel:DWORD dst_unused:UNUSED_PAD src0_sel:DWORD src1_sel:WORD_1
v_mfma_f32_16x16x32_bf16 v[68:71],a[252:255],v[60:63],v[68:71]
ds_read_u16_d16_hi v50,v50
v_lshlrev_b32_sdwa v51,1,v51 dst_sel:DWORD dst_unused:UNUSED_PAD src0_sel:DWORD src1_sel:WORD_1
v_mfma_f32_16x16x32_bf16 v[64:67],a[252:255],v[32:35],v[64:67]
ds_read_u16 v49,v49
v_lshlrev_b32_sdwa v44,1,v44 dst_sel:DWORD dst_unused:UNUSED_PAD src0_sel:DWORD src1_sel:WORD_1
v_mfma_f32_16x16x32_bf16 v[144:147],a[240:243],v[32:35],v[144:147]
ds_read_u16_d16_hi v51,v51
v_lshlrev_b32_sdwa v46,1,v46 dst_sel:DWORD dst_unused:UNUSED_PAD src0_sel:DWORD src1_sel:WORD_1
v_mfma_f32_16x16x32_bf16 v[148:151],a[240:243],v[60:63],v[148:151]
s_waitcnt lgkmcnt(4)
ds_read_u16 v44,v44
v_or3_b32 v13,v14,v15,v13
v_mfma_f32_16x16x32_bf16 v[84:87],a[224:227],v[60:63],v[84:87]
ds_read_u16_d16_hi v46,v46
v_lshlrev_b32_sdwa v45,1,v45 dst_sel:DWORD dst_unused:UNUSED_PAD src0_sel:DWORD src1_sel:WORD_1
v_mfma_f32_16x16x32_bf16 v[80:83],a[224:227],v[32:35],v[80:83]
ds_read_u16 v45,v45
v_lshlrev_b32_sdwa v47,1,v47 dst_sel:DWORD dst_unused:UNUSED_PAD src0_sel:DWORD src1_sel:WORD_1
v_mfma_f32_16x16x32_bf16 v[152:155],a[228:231],v[32:35],v[152:155]
ds_read_u16_d16_hi v47,v47
v_mfma_f32_16x16x32_bf16 v[156:159],a[228:231],v[60:63],v[156:159]
v_mfma_f32_16x16x32_bf16 v[100:103],a[232:235],v[60:63],v[100:103]
v_mfma_f32_16x16x32_bf16 v[96:99],a[232:235],v[32:35],v[96:99]
s_add_u32 m0,s57,60120
v_mfma_f32_16x16x32_bf16 v[60:63],a[204:207],a[56:59],v[20:23]
s_add_u32 s89,s56,4128768
v_lshlrev_b32_sdwa v28,1,v28 dst_sel:DWORD dst_unused:UNUSED_PAD src0_sel:DWORD src1_sel:WORD_1
v_fma_mixlo_f16 v8,v53,s99,0
v_mfma_f32_16x16x32_bf16 v[60:63],a[208:211],a[72:75],v[60:63]
buffer_load_dwordx4 v6,s[36:39],s89 offen lds
v_lshl_add_u64 v[40:41],v[40:41],0,v[42:43]
v_mfma_f32_16x16x32_bf16 v[60:63],a[212:215],a[88:91],v[60:63]
ds_read_u16 v28,v28
v_lshl_add_u64 v[42:43],v[36:37],0,v[38:39]
v_lshlrev_b32_sdwa v30,1,v30 dst_sel:DWORD dst_unused:UNUSED_PAD src0_sel:DWORD src1_sel:WORD_1
v_mfma_f32_16x16x32_bf16 v[32:35],a[200:203],a[88:91],v[16:19]
ds_read_u16_d16_hi v30,v30
v_fma_mixhi_f16 v8,v55,s99,0
v_mfma_f32_16x16x32_bf16 v[32:35],a[196:199],a[72:75],v[32:35]
s_waitcnt lgkmcnt(3)
v_lshl_add_u64 v[36:37],v[48:49],0,v[50:51]
v_lshlrev_b32_sdwa v29,1,v29 dst_sel:DWORD dst_unused:UNUSED_PAD src0_sel:DWORD src1_sel:WORD_1
v_pk_fma_f16 v9,v8,v8,s54
v_mfma_f32_16x16x32_bf16 v[32:35],a[192:195],a[56:59],v[32:35]
ds_read_u16 v29,v29
v_lshlrev_b32_sdwa v31,1,v31 dst_sel:DWORD dst_unused:UNUSED_PAD src0_sel:DWORD src1_sel:WORD_1
v_mfma_f32_16x16x32_bf16 v[48:51],a[192:195],a[60:63],v[16:19]
ds_read_b128 v[16:19],v4 offset:768
ds_read_b64_tr_b16 a[192:193],v1 offset:10256
v_pk_mul_f16 v9,v8,v9
v_mfma_f32_16x16x32_bf16 v[48:51],a[196:199],a[76:79],v[48:51]
s_waitcnt lgkmcnt(5)
ds_read_b64_tr_b16 a[194:195],v1 offset:11280
ds_read_b64_tr_b16 a[196:197],v2 offset:10256
v_lshl_add_u64 v[38:39],v[44:45],0,v[46:47]
v_lshlrev_b32_sdwa v24,1,v24 dst_sel:DWORD dst_unused:UNUSED_PAD src0_sel:DWORD src1_sel:WORD_1
v_mfma_f32_16x16x32_bf16 v[48:51],a[200:203],a[92:95],v[48:51]
ds_read_b64_tr_b16 a[198:199],v2 offset:11280
ds_read_b64_tr_b16 a[200:201],v2 offset:8208
v_exp_f16 v8,v9
v_mfma_f32_16x16x32_bf16 v[44:47],a[212:215],a[92:95],v[20:23]
ds_read_b128 v[20:23],v4 offset:800
ds_read_b64_tr_b16 a[202:203],v2 offset:9232
v_lshlrev_b32_sdwa v26,1,v26 dst_sel:DWORD dst_unused:UNUSED_PAD src0_sel:DWORD src1_sel:WORD_1
v_mfma_f32_16x16x32_bf16 v[44:47],a[208:211],a[76:79],v[44:47]
ds_read_b64_tr_b16 a[212:213],v2 offset:6160
ds_read_b64_tr_b16 a[214:215],v2 offset:7184
v_lshlrev_b32_sdwa v25,1,v25 dst_sel:DWORD dst_unused:UNUSED_PAD src0_sel:DWORD src1_sel:WORD_1
v_mfma_f32_16x16x32_bf16 v[44:47],a[204:207],a[60:63],v[44:47]
ds_read_u16_d16_hi v31,v31
v_exp_f16_sdwa v8,v9 dst_sel:WORD_1 dst_unused:UNUSED_PRESERVE src0_sel:WORD_1
v_mfma_f32_16x16x32_bf16 v[112:115],a[248:251],v[40:43],v[112:115]
ds_read_u16 v24,v24
v_lshlrev_b32_sdwa v27,1,v27 dst_sel:DWORD dst_unused:UNUSED_PAD src0_sel:DWORD src1_sel:WORD_1
v_mfma_f32_16x16x32_bf16 v[116:119],a[248:251],v[36:39],v[116:119]
ds_read_u16_d16_hi v26,v26
v_lshlrev_b32_sdwa v56,1,v56 dst_sel:DWORD dst_unused:UNUSED_PAD src0_sel:DWORD src1_sel:WORD_1
v_mfma_f32_16x16x32_bf16 v[76:79],a[252:255],v[36:39],v[76:79]
ds_read_u16 v25,v25
v_pk_fma_f16 v8,v8,s55,s55
v_mfma_f32_16x16x32_bf16 v[72:75],a[252:255],v[40:43],v[72:75]
v_rcp_f16 v9,v8
v_lshlrev_b32_sdwa v58,1,v58 dst_sel:DWORD dst_unused:UNUSED_PAD src0_sel:DWORD src1_sel:WORD_1
v_mfma_f32_16x16x32_bf16 v[120:123],a[240:243],v[40:43],v[120:123]
ds_read_u16_d16_hi v27,v27
v_lshlrev_b32_sdwa v57,1,v57 dst_sel:DWORD dst_unused:UNUSED_PAD src0_sel:DWORD src1_sel:WORD_1
v_mfma_f32_16x16x32_bf16 v[124:127],a[240:243],v[36:39],v[124:127]
ds_read_u16 v56,v56
v_lshlrev_b32_sdwa v59,1,v59 dst_sel:DWORD dst_unused:UNUSED_PAD src0_sel:DWORD src1_sel:WORD_1
v_mfma_f32_16x16x32_bf16 v[92:95],a[224:227],v[36:39],v[92:95]
ds_read_u16_d16_hi v58,v58
v_lshlrev_b32_sdwa v52,1,v52 dst_sel:DWORD dst_unused:UNUSED_PAD src0_sel:DWORD src1_sel:WORD_1
v_mfma_f32_16x16x32_bf16 v[88:91],a[224:227],v[40:43],v[88:91]
v_rcp_f16_sdwa v9,v8 dst_sel:WORD_1 dst_unused:UNUSED_PRESERVE src0_sel:WORD_1
v_mfma_f32_16x16x32_bf16 v[128:131],a[228:231],v[40:43],v[128:131]
ds_read_u16 v57,v57
ds_read_u16_d16_hi v59,v59
v_mfma_f32_16x16x32_bf16 v[132:135],a[228:231],v[36:39],v[132:135]
ds_read_u16 v52,v52
v_lshlrev_b32_sdwa v54,1,v54 dst_sel:DWORD dst_unused:UNUSED_PAD src0_sel:DWORD src1_sel:WORD_1
v_mfma_f32_16x16x32_bf16 v[108:111],a[232:235],v[36:39],v[108:111]
ds_read_u16_d16_hi v54,v54
v_fma_mix_f32 v53,v53,v9,0 op_sel_hi:[0,1,0]
v_mfma_f32_16x16x32_bf16 v[104:107],a[232:235],v[40:43],v[104:107]
s_waitcnt lgkmcnt(7)
v_fma_mix_f32 v55,v55,v9,0 op_sel:[0,1,0] op_sel_hi:[0,1,0]
v_mfma_f32_16x16x32_bf16 v[36:39],a[216:219],a[0:3],v[16:19]
s_add_u32 m0,s57,63656
v_lshlrev_b32_sdwa v60,1,v60 dst_sel:DWORD dst_unused:UNUSED_PAD src0_sel:DWORD src1_sel:WORD_1
v_lshlrev_b32_sdwa v62,1,v62 dst_sel:DWORD dst_unused:UNUSED_PAD src0_sel:DWORD src1_sel:WORD_1
v_mfma_f32_16x16x32_bf16 v[36:39],a[220:223],a[16:19],v[36:39]
s_add_u32 s89,s56,4227072
ds_read_u16 v60,v60
v_lshl_add_u64 v[28:29],v[28:29],0,v[30:31]
v_mfma_f32_16x16x32_bf16 v[36:39],a[192:195],a[32:35],v[36:39]
s_waitcnt lgkmcnt(6)
buffer_load_dwordx4 v6,s[36:39],s89 offen lds
v_lshl_add_u64 v[30:31],v[24:25],0,v[26:27]
v_mfma_f32_16x16x32_bf16 v[40:43],a[196:199],a[32:35],v[20:23]
ds_read_u16_d16_hi v62,v62
v_lshlrev_b32_sdwa v61,1,v61 dst_sel:DWORD dst_unused:UNUSED_PAD src0_sel:DWORD src1_sel:WORD_1
v_mfma_f32_16x16x32_bf16 v[40:43],a[200:203],a[16:19],v[40:43]
s_waitcnt lgkmcnt(4)
ds_read_u16 v61,v61
v_lshl_add_u64 v[24:25],v[56:57],0,v[58:59]
v_mfma_f32_16x16x32_bf16 v[40:43],a[212:215],a[0:3],v[40:43]
v_lshlrev_b32_sdwa v63,1,v63 dst_sel:DWORD dst_unused:UNUSED_PAD src0_sel:DWORD src1_sel:WORD_1
v_lshlrev_b32_sdwa v32,1,v32 dst_sel:DWORD dst_unused:UNUSED_PAD src0_sel:DWORD src1_sel:WORD_1
v_mfma_f32_16x16x32_bf16 v[56:59],a[212:215],a[4:7],v[20:23]
s_waitcnt lgkmcnt(3)
ds_read_u16_d16_hi v63,v63
v_or_b32 v26,v52,v54
v_mfma_f32_16x16x32_bf16 v[56:59],a[200:203],a[20:23],v[56:59]
ds_read_u16 v32,v32
v_cvt_pk_bf16_f32 v27,v53,v55
v_lshlrev_b32_sdwa v34,1,v34 dst_sel:DWORD dst_unused:UNUSED_PAD src0_sel:DWORD src1_sel:WORD_1
v_mfma_f32_16x16x32_bf16 v[56:59],a[196:199],a[36:39],v[56:59]
ds_read_u16_d16_hi v34,v34
v_lshlrev_b32_sdwa v33,1,v33 dst_sel:DWORD dst_unused:UNUSED_PAD src0_sel:DWORD src1_sel:WORD_1
v_mfma_f32_16x16x32_bf16 v[52:55],a[192:195],a[36:39],v[16:19]
ds_read_u16 v33,v33
v_lshlrev_b32_sdwa v35,1,v35 dst_sel:DWORD dst_unused:UNUSED_PAD src0_sel:DWORD src1_sel:WORD_1
v_mfma_f32_16x16x32_bf16 v[52:55],a[220:223],a[20:23],v[52:55]
ds_read_u16_d16_hi v35,v35
v_lshlrev_b32_sdwa v44,1,v44 dst_sel:DWORD dst_unused:UNUSED_PAD src0_sel:DWORD src1_sel:WORD_1
v_mfma_f32_16x16x32_bf16 v[52:55],a[216:219],a[4:7],v[52:55]
ds_read_u16 v44,v44
v_lshlrev_b32_sdwa v46,1,v46 dst_sel:DWORD dst_unused:UNUSED_PAD src0_sel:DWORD src1_sel:WORD_1
v_mfma_f32_16x16x32_bf16 v[232:235],a[248:251],v[28:31],v[232:235]
ds_read_u16_d16_hi v46,v46
v_lshlrev_b32_sdwa v45,1,v45 dst_sel:DWORD dst_unused:UNUSED_PAD src0_sel:DWORD src1_sel:WORD_1
v_mfma_f32_16x16x32_bf16 v[236:239],a[248:251],v[24:27],v[236:239]
ds_read_u16 v45,v45
v_lshlrev_b32_sdwa v47,1,v47 dst_sel:DWORD dst_unused:UNUSED_PAD src0_sel:DWORD src1_sel:WORD_1
v_mfma_f32_16x16x32_bf16 v[164:167],a[252:255],v[24:27],v[164:167]
ds_read_u16_d16_hi v47,v47
ds_read_b64_tr_b16 a[236:237],v3 offset:12320
v_mfma_f32_16x16x32_bf16 v[160:163],a[252:255],v[28:31],v[160:163]
ds_read_b64_tr_b16 a[238:239],v3 offset:13856
v_lshlrev_b32_sdwa v48,1,v48 dst_sel:DWORD dst_unused:UNUSED_PAD src0_sel:DWORD src1_sel:WORD_1
v_mfma_f32_16x16x32_bf16 v[240:243],a[240:243],v[28:31],v[240:243]
ds_read_u16 v48,v48
v_lshlrev_b32_sdwa v50,1,v50 dst_sel:DWORD dst_unused:UNUSED_PAD src0_sel:DWORD src1_sel:WORD_1
v_mfma_f32_16x16x32_bf16 v[244:247],a[240:243],v[24:27],v[244:247]
ds_read_u16_d16_hi v50,v50
v_lshlrev_b32_sdwa v49,1,v49 dst_sel:DWORD dst_unused:UNUSED_PAD src0_sel:DWORD src1_sel:WORD_1
v_mfma_f32_16x16x32_bf16 v[180:183],a[224:227],v[24:27],v[180:183]
s_add_u32 m0,s57,67184
ds_read_u16 v49,v49
v_lshlrev_b32_sdwa v51,1,v51 dst_sel:DWORD dst_unused:UNUSED_PAD src0_sel:DWORD src1_sel:WORD_1
v_mfma_f32_16x16x32_bf16 v[176:179],a[224:227],v[28:31],v[176:179]
s_add_u32 s89,s56,4325376
ds_read_u16_d16_hi v51,v51
v_mfma_f32_16x16x32_bf16 v[248:251],a[228:231],v[28:31],v[248:251]
buffer_load_dwordx4 v6,s[36:39],s89 offen lds
v_mfma_f32_16x16x32_bf16 v[252:255],a[228:231],v[24:27],v[252:255]
ds_read_b64_tr_b16 a[244:245],v3 offset:12328
v_mfma_f32_16x16x32_bf16 v[196:199],a[232:235],v[24:27],v[196:199]
ds_read_b64_tr_b16 a[246:247],v3 offset:13864
v_mfma_f32_16x16x32_bf16 v[192:195],a[232:235],v[28:31],v[192:195]
v_mfma_f32_16x16x32_bf16 v[24:27],a[216:219],a[8:11],v[16:19]
ds_read_b64_tr_b16 a[208:209],v1 offset:12320
v_lshlrev_b32_sdwa v36,1,v36 dst_sel:DWORD dst_unused:UNUSED_PAD src0_sel:DWORD src1_sel:WORD_1
v_mfma_f32_16x16x32_bf16 v[24:27],a[220:223],a[24:27],v[24:27]
s_waitcnt lgkmcnt(6)
ds_read_u16 v36,v36
v_lshl_add_u64 v[60:61],v[60:61],0,v[62:63]
v_fma_mixlo_f16 v8,v57,s99,0
v_mfma_f32_16x16x32_bf16 v[24:27],a[192:195],a[40:43],v[24:27]
ds_read_b64_tr_b16 a[210:211],v1 offset:13344
v_lshl_add_u64 v[62:63],v[32:33],0,v[34:35]
v_lshlrev_b32_sdwa v38,1,v38 dst_sel:DWORD dst_unused:UNUSED_PAD src0_sel:DWORD src1_sel:WORD_1
v_mfma_f32_16x16x32_bf16 v[28:31],a[196:199],a[40:43],v[20:23]
ds_read_u16_d16_hi v38,v38
v_fma_mixhi_f16 v8,v59,s99,0
v_mfma_f32_16x16x32_bf16 v[28:31],a[200:203],a[24:27],v[28:31]
ds_read_b64_tr_b16 a[204:205],v1 offset:14368
v_lshl_add_u64 v[32:33],v[44:45],0,v[46:47]
v_lshlrev_b32_sdwa v37,1,v37 dst_sel:DWORD dst_unused:UNUSED_PAD src0_sel:DWORD src1_sel:WORD_1
v_pk_fma_f16 v9,v8,v8,s54
v_mfma_f32_16x16x32_bf16 v[28:31],a[212:215],a[8:11],v[28:31]
ds_read_u16 v37,v37
v_lshlrev_b32_sdwa v39,1,v39 dst_sel:DWORD dst_unused:UNUSED_PAD src0_sel:DWORD src1_sel:WORD_1
v_mfma_f32_16x16x32_bf16 v[44:47],a[212:215],a[12:15],v[20:23]
ds_read_u16_d16_hi v39,v39
v_pk_mul_f16 v9,v8,v9
v_mfma_f32_16x16x32_bf16 v[44:47],a[200:203],a[28:31],v[44:47]
s_waitcnt lgkmcnt(7)
ds_read_b64_tr_b16 a[206:207],v1 offset:15392
v_lshl_add_u64 v[34:35],v[48:49],0,v[50:51]
v_lshlrev_b32_sdwa v40,1,v40 dst_sel:DWORD dst_unused:UNUSED_PAD src0_sel:DWORD src1_sel:WORD_1
v_mfma_f32_16x16x32_bf16 v[44:47],a[196:199],a[44:47],v[44:47]
v_exp_f16 v8,v9
v_mfma_f32_16x16x32_bf16 v[48:51],a[192:195],a[44:47],v[16:19]
ds_read_u16 v40,v40
v_lshlrev_b32_sdwa v42,1,v42 dst_sel:DWORD dst_unused:UNUSED_PAD src0_sel:DWORD src1_sel:WORD_1
v_mfma_f32_16x16x32_bf16 v[48:51],a[220:223],a[28:31],v[48:51]
ds_read_u16_d16_hi v42,v42
v_lshlrev_b32_sdwa v41,1,v41 dst_sel:DWORD dst_unused:UNUSED_PAD src0_sel:DWORD src1_sel:WORD_1
v_mfma_f32_16x16x32_bf16 v[48:51],a[216:219],a[12:15],v[48:51]
v_exp_f16_sdwa v8,v9 dst_sel:WORD_1 dst_unused:UNUSED_PRESERVE src0_sel:WORD_1
v_mfma_f32_16x16x32_bf16 v[208:211],a[248:251],v[60:63],v[208:211]
ds_read_u16 v41,v41
v_lshlrev_b32_sdwa v43,1,v43 dst_sel:DWORD dst_unused:UNUSED_PAD src0_sel:DWORD src1_sel:WORD_1
v_mfma_f32_16x16x32_bf16 v[212:215],a[248:251],v[32:35],v[212:215]
ds_read_u16_d16_hi v43,v43
v_lshlrev_b32_sdwa v52,1,v52 dst_sel:DWORD dst_unused:UNUSED_PAD src0_sel:DWORD src1_sel:WORD_1
v_mfma_f32_16x16x32_bf16 v[172:175],a[252:255],v[32:35],v[172:175]
ds_read_u16 v52,v52
v_pk_fma_f16 v8,v8,s55,s55
v_mfma_f32_16x16x32_bf16 v[168:171],a[252:255],v[60:63],v[168:171]
ds_read_b64_tr_b16 a[248:249],v3 offset:12384
v_rcp_f16 v9,v8
v_lshlrev_b32_sdwa v54,1,v54 dst_sel:DWORD dst_unused:UNUSED_PAD src0_sel:DWORD src1_sel:WORD_1
v_mfma_f32_16x16x32_bf16 v[216:219],a[240:243],v[60:63],v[216:219]
ds_read_u16_d16_hi v54,v54
v_lshlrev_b32_sdwa v53,1,v53 dst_sel:DWORD dst_unused:UNUSED_PAD src0_sel:DWORD src1_sel:WORD_1
v_mfma_f32_16x16x32_bf16 v[220:223],a[240:243],v[32:35],v[220:223]
ds_read_u16 v53,v53
ds_read_b64_tr_b16 a[250:251],v3 offset:13920
v_mfma_f32_16x16x32_bf16 v[188:191],a[224:227],v[32:35],v[188:191]
ds_read_b64_tr_b16 a[252:253],v3 offset:12392
v_lshlrev_b32_sdwa v55,1,v55 dst_sel:DWORD dst_unused:UNUSED_PAD src0_sel:DWORD src1_sel:WORD_1
v_mfma_f32_16x16x32_bf16 v[184:187],a[224:227],v[60:63],v[184:187]
ds_read_u16_d16_hi v55,v55
v_rcp_f16_sdwa v9,v8 dst_sel:WORD_1 dst_unused:UNUSED_PRESERVE src0_sel:WORD_1
v_mfma_f32_16x16x32_bf16 v[224:227],a[228:231],v[60:63],v[224:227]
ds_read_b64_tr_b16 a[254:255],v3 offset:13928
ds_read_b64_tr_b16 a[240:241],v3 offset:12448
v_lshlrev_b32_sdwa v56,1,v56 dst_sel:DWORD dst_unused:UNUSED_PAD src0_sel:DWORD src1_sel:WORD_1
v_mfma_f32_16x16x32_bf16 v[228:231],a[228:231],v[32:35],v[228:231]
ds_read_u16 v56,v56
v_lshlrev_b32_sdwa v58,1,v58 dst_sel:DWORD dst_unused:UNUSED_PAD src0_sel:DWORD src1_sel:WORD_1
v_mfma_f32_16x16x32_bf16 v[204:207],a[232:235],v[32:35],v[204:207]
ds_read_u16_d16_hi v58,v58
v_fma_mix_f32 v57,v57,v9,0 op_sel_hi:[0,1,0]
v_mfma_f32_16x16x32_bf16 v[200:203],a[232:235],v[60:63],v[200:203]
ds_read_b64_tr_b16 a[242:243],v3 offset:13984
v_fma_mix_f32 v59,v59,v9,0 op_sel:[0,1,0] op_sel_hi:[0,1,0]
v_mfma_f32_16x16x32_bf16 v[32:35],a[216:219],a[48:51],v[16:19]
s_add_u32 m0,s57,70720
ds_read_b64 v[14:15],v7 offset:12320
v_lshlrev_b32_sdwa v24,1,v24 dst_sel:DWORD dst_unused:UNUSED_PAD src0_sel:DWORD src1_sel:WORD_1
v_mfma_f32_16x16x32_bf16 v[32:35],a[220:223],a[64:67],v[32:35]
s_waitcnt lgkmcnt(10)
ds_read_b64_tr_b16 a[224:225],v3 offset:12456
v_lshl_add_u64 v[36:37],v[36:37],0,v[38:39]
v_mfma_f32_16x16x32_bf16 v[32:35],a[192:195],a[80:83],v[32:35]
s_add_u32 s89,s56,4423680
ds_read_b64_tr_b16 a[226:227],v3 offset:13992
v_lshl_add_u64 v[38:39],v[40:41],0,v[42:43]
v_lshlrev_b32_sdwa v26,1,v26 dst_sel:DWORD dst_unused:UNUSED_PAD src0_sel:DWORD src1_sel:WORD_1
v_mfma_f32_16x16x32_bf16 v[60:63],a[196:199],a[80:83],v[20:23]
buffer_load_dwordx4 v6,s[36:39],s89 offen lds
v_lshlrev_b32_sdwa v25,1,v25 dst_sel:DWORD dst_unused:UNUSED_PAD src0_sel:DWORD src1_sel:WORD_1
v_mfma_f32_16x16x32_bf16 v[60:63],a[200:203],a[64:67],v[60:63]
s_waitcnt lgkmcnt(8)
ds_read_u16 v24,v24
v_lshl_add_u64 v[40:41],v[52:53],0,v[54:55]
v_mfma_f32_16x16x32_bf16 v[60:63],a[212:215],a[48:51],v[60:63]
s_waitcnt lgkmcnt(3)
ds_read_u16_d16_hi v26,v26
v_or3_b32 v13,v14,v15,v13
v_lshlrev_b32_sdwa v27,1,v27 dst_sel:DWORD dst_unused:UNUSED_PAD src0_sel:DWORD src1_sel:WORD_1
v_mfma_f32_16x16x32_bf16 v[52:55],a[212:215],a[52:55],v[20:23]
ds_read_b64 v[14:15],v7 offset:12832
v_or_b32 v42,v56,v58
v_lshlrev_b32_sdwa v28,1,v28 dst_sel:DWORD dst_unused:UNUSED_PAD src0_sel:DWORD src1_sel:WORD_1
v_mfma_f32_16x16x32_bf16 v[52:55],a[200:203],a[68:71],v[52:55]
ds_read_u16 v25,v25
v_cvt_pk_bf16_f32 v43,v57,v59
v_lshlrev_b32_sdwa v30,1,v30 dst_sel:DWORD dst_unused:UNUSED_PAD src0_sel:DWORD src1_sel:WORD_1
v_mfma_f32_16x16x32_bf16 v[52:55],a[196:199],a[84:87],v[52:55]
ds_read_u16_d16_hi v27,v27
v_lshlrev_b32_sdwa v29,1,v29 dst_sel:DWORD dst_unused:UNUSED_PAD src0_sel:DWORD src1_sel:WORD_1
v_mfma_f32_16x16x32_bf16 v[56:59],a[192:195],a[84:87],v[16:19]
ds_read_u16 v28,v28
v_lshlrev_b32_sdwa v31,1,v31 dst_sel:DWORD dst_unused:UNUSED_PAD src0_sel:DWORD src1_sel:WORD_1
v_mfma_f32_16x16x32_bf16 v[56:59],a[220:223],a[68:71],v[56:59]
ds_read_u16_d16_hi v30,v30
v_lshlrev_b32_sdwa v48,1,v48 dst_sel:DWORD dst_unused:UNUSED_PAD src0_sel:DWORD src1_sel:WORD_1
v_mfma_f32_16x16x32_bf16 v[56:59],a[216:219],a[52:55],v[56:59]
s_waitcnt lgkmcnt(4)
ds_read_u16 v29,v29
v_or3_b32 v13,v14,v15,v13
v_mfma_f32_16x16x32_bf16 v[136:139],a[236:239],v[36:39],v[136:139]
ds_read_b64 v[14:15],v7 offset:13344
v_lshlrev_b32_sdwa v50,1,v50 dst_sel:DWORD dst_unused:UNUSED_PAD src0_sel:DWORD src1_sel:WORD_1
v_mfma_f32_16x16x32_bf16 v[140:143],a[236:239],v[40:43],v[140:143]
s_add_u32 m0,s57,74264
ds_read_u16_d16_hi v31,v31
v_lshlrev_b32_sdwa v49,1,v49 dst_sel:DWORD dst_unused:UNUSED_PAD src0_sel:DWORD src1_sel:WORD_1
v_mfma_f32_16x16x32_bf16 v[68:71],a[244:247],v[40:43],v[68:71]
s_add_u32 s89,s56,4521984
ds_read_u16 v48,v48
v_lshlrev_b32_sdwa v51,1,v51 dst_sel:DWORD dst_unused:UNUSED_PAD src0_sel:DWORD src1_sel:WORD_1
v_mfma_f32_16x16x32_bf16 v[64:67],a[244:247],v[36:39],v[64:67]
buffer_load_dwordx4 v6,s[36:39],s89 offen lds
v_lshlrev_b32_sdwa v44,1,v44 dst_sel:DWORD dst_unused:UNUSED_PAD src0_sel:DWORD src1_sel:WORD_1
v_mfma_f32_16x16x32_bf16 v[144:147],a[248:251],v[36:39],v[144:147]
ds_read_u16_d16_hi v50,v50
v_lshlrev_b32_sdwa v46,1,v46 dst_sel:DWORD dst_unused:UNUSED_PAD src0_sel:DWORD src1_sel:WORD_1
v_mfma_f32_16x16x32_bf16 v[148:151],a[248:251],v[40:43],v[148:151]
s_waitcnt lgkmcnt(3)
ds_read_u16 v49,v49
v_or3_b32 v13,v14,v15,v13
v_mfma_f32_16x16x32_bf16 v[84:87],a[252:255],v[40:43],v[84:87]
ds_read_u16_d16_hi v51,v51
v_lshlrev_b32_sdwa v45,1,v45 dst_sel:DWORD dst_unused:UNUSED_PAD src0_sel:DWORD src1_sel:WORD_1
v_mfma_f32_16x16x32_bf16 v[80:83],a[252:255],v[36:39],v[80:83]
ds_read_u16 v44,v44
v_lshlrev_b32_sdwa v47,1,v47 dst_sel:DWORD dst_unused:UNUSED_PAD src0_sel:DWORD src1_sel:WORD_1
v_mfma_f32_16x16x32_bf16 v[152:155],a[240:243],v[36:39],v[152:155]
ds_read_u16_d16_hi v46,v46
v_mfma_f32_16x16x32_bf16 v[156:159],a[240:243],v[40:43],v[156:159]
ds_read_u16 v45,v45
v_mfma_f32_16x16x32_bf16 v[100:103],a[224:227],v[40:43],v[100:103]
ds_read_u16_d16_hi v47,v47
v_mfma_f32_16x16x32_bf16 v[96:99],a[224:227],v[36:39],v[96:99]
v_mfma_f32_16x16x32_bf16 v[40:43],a[216:219],a[56:59],v[16:19]
v_lshlrev_b32_sdwa v32,1,v32 dst_sel:DWORD dst_unused:UNUSED_PAD src0_sel:DWORD src1_sel:WORD_1
v_fma_mixlo_f16 v8,v53,s99,0
v_mfma_f32_16x16x32_bf16 v[40:43],a[220:223],a[72:75],v[40:43]
ds_read_u16 v32,v32
v_lshl_add_u64 v[24:25],v[24:25],0,v[26:27]
v_mfma_f32_16x16x32_bf16 v[40:43],a[192:195],a[88:91],v[40:43]
s_waitcnt lgkmcnt(6)
v_lshl_add_u64 v[26:27],v[28:29],0,v[30:31]
v_lshlrev_b32_sdwa v34,1,v34 dst_sel:DWORD dst_unused:UNUSED_PAD src0_sel:DWORD src1_sel:WORD_1
v_mfma_f32_16x16x32_bf16 v[36:39],a[196:199],a[88:91],v[20:23]
ds_read_u16_d16_hi v34,v34
v_fma_mixhi_f16 v8,v55,s99,0
v_mfma_f32_16x16x32_bf16 v[36:39],a[200:203],a[72:75],v[36:39]
s_waitcnt lgkmcnt(5)
v_lshl_add_u64 v[28:29],v[48:49],0,v[50:51]
v_lshlrev_b32_sdwa v33,1,v33 dst_sel:DWORD dst_unused:UNUSED_PAD src0_sel:DWORD src1_sel:WORD_1
v_pk_fma_f16 v9,v8,v8,s54
v_mfma_f32_16x16x32_bf16 v[36:39],a[212:215],a[56:59],v[36:39]
ds_read_u16 v33,v33
v_lshlrev_b32_sdwa v35,1,v35 dst_sel:DWORD dst_unused:UNUSED_PAD src0_sel:DWORD src1_sel:WORD_1
v_mfma_f32_16x16x32_bf16 v[48:51],a[212:215],a[60:63],v[20:23]
ds_read_b128 v[20:23],v4 offset:896
v_pk_mul_f16 v9,v8,v9
v_mfma_f32_16x16x32_bf16 v[48:51],a[200:203],a[76:79],v[48:51]
s_waitcnt lgkmcnt(4)
ds_read_u16_d16_hi v35,v35
ds_read_b64_tr_b16 a[212:213],v1 offset:16416
v_lshl_add_u64 v[30:31],v[44:45],0,v[46:47]
v_lshlrev_b32_sdwa v60,1,v60 dst_sel:DWORD dst_unused:UNUSED_PAD src0_sel:DWORD src1_sel:WORD_1
v_mfma_f32_16x16x32_bf16 v[48:51],a[196:199],a[92:95],v[48:51]
ds_read_b64_tr_b16 a[214:215],v1 offset:17440
v_exp_f16 v8,v9
v_mfma_f32_16x16x32_bf16 v[44:47],a[192:195],a[92:95],v[16:19]
ds_read_b128 v[16:19],v4 offset:928
ds_read_u16 v60,v60
v_lshlrev_b32_sdwa v62,1,v62 dst_sel:DWORD dst_unused:UNUSED_PAD src0_sel:DWORD src1_sel:WORD_1
v_mfma_f32_16x16x32_bf16 v[44:47],a[220:223],a[76:79],v[44:47]
ds_read_u16_d16_hi v62,v62
ds_read_b64_tr_b16 a[200:201],v2 offset:16416
v_lshlrev_b32_sdwa v61,1,v61 dst_sel:DWORD dst_unused:UNUSED_PAD src0_sel:DWORD src1_sel:WORD_1
v_mfma_f32_16x16x32_bf16 v[44:47],a[216:219],a[60:63],v[44:47]
ds_read_u16 v61,v61
v_exp_f16_sdwa v8,v9 dst_sel:WORD_1 dst_unused:UNUSED_PRESERVE src0_sel:WORD_1
v_mfma_f32_16x16x32_bf16 v[112:115],a[236:239],v[24:27],v[112:115]
ds_read_b64_tr_b16 a[202:203],v2 offset:17440
ds_read_b64_tr_b16 a[196:197],v2 offset:14368
v_lshlrev_b32_sdwa v63,1,v63 dst_sel:DWORD dst_unused:UNUSED_PAD src0_sel:DWORD src1_sel:WORD_1
v_mfma_f32_16x16x32_bf16 v[116:119],a[236:239],v[28:31],v[116:119]
ds_read_u16_d16_hi v63,v63
v_lshlrev_b32_sdwa v56,1,v56 dst_sel:DWORD dst_unused:UNUSED_PAD src0_sel:DWORD src1_sel:WORD_1
v_mfma_f32_16x16x32_bf16 v[76:79],a[244:247],v[28:31],v[76:79]
ds_read_b64_tr_b16 a[198:199],v2 offset:15392
v_pk_fma_f16 v8,v8,s55,s55
v_mfma_f32_16x16x32_bf16 v[72:75],a[244:247],v[24:27],v[72:75]
v_rcp_f16 v9,v8
v_lshlrev_b32_sdwa v58,1,v58 dst_sel:DWORD dst_unused:UNUSED_PAD src0_sel:DWORD src1_sel:WORD_1
v_mfma_f32_16x16x32_bf16 v[120:123],a[248:251],v[24:27],v[120:123]
ds_read_u16 v56,v56
v_lshlrev_b32_sdwa v57,1,v57 dst_sel:DWORD dst_unused:UNUSED_PAD src0_sel:DWORD src1_sel:WORD_1
v_mfma_f32_16x16x32_bf16 v[124:127],a[248:251],v[28:31],v[124:127]
ds_read_u16_d16_hi v58,v58
v_lshlrev_b32_sdwa v59,1,v59 dst_sel:DWORD dst_unused:UNUSED_PAD src0_sel:DWORD src1_sel:WORD_1
v_mfma_f32_16x16x32_bf16 v[92:95],a[252:255],v[28:31],v[92:95]
ds_read_u16 v57,v57
v_lshlrev_b32_sdwa v52,1,v52 dst_sel:DWORD dst_unused:UNUSED_PAD src0_sel:DWORD src1_sel:WORD_1
v_mfma_f32_16x16x32_bf16 v[88:91],a[252:255],v[24:27],v[88:91]
ds_read_u16_d16_hi v59,v59
v_rcp_f16_sdwa v9,v8 dst_sel:WORD_1 dst_unused:UNUSED_PRESERVE src0_sel:WORD_1
v_mfma_f32_16x16x32_bf16 v[128:131],a[240:243],v[24:27],v[128:131]
ds_read_b64_tr_b16 a[192:193],v2 offset:12320
v_lshlrev_b32_sdwa v54,1,v54 dst_sel:DWORD dst_unused:UNUSED_PAD src0_sel:DWORD src1_sel:WORD_1
v_mfma_f32_16x16x32_bf16 v[132:135],a[240:243],v[28:31],v[132:135]
ds_read_b64_tr_b16 a[194:195],v2 offset:13344
v_fma_mix_f32 v53,v53,v9,0 op_sel_hi:[0,1,0]
v_mfma_f32_16x16x32_bf16 v[108:111],a[224:227],v[28:31],v[108:111]
ds_read_u16 v52,v52
v_fma_mix_f32 v55,v55,v9,0 op_sel:[0,1,0] op_sel_hi:[0,1,0]
v_mfma_f32_16x16x32_bf16 v[104:107],a[224:227],v[24:27],v[104:107]
s_waitcnt lgkmcnt(7)
ds_read_u16_d16_hi v54,v54
v_mfma_f32_16x16x32_bf16 v[28:31],a[208:211],a[0:3],v[20:23]
s_add_u32 m0,s57,77800
v_lshlrev_b32_sdwa v40,1,v40 dst_sel:DWORD dst_unused:UNUSED_PAD src0_sel:DWORD src1_sel:WORD_1
v_lshlrev_b32_sdwa v42,1,v42 dst_sel:DWORD dst_unused:UNUSED_PAD src0_sel:DWORD src1_sel:WORD_1
v_mfma_f32_16x16x32_bf16 v[28:31],a[204:207],a[16:19],v[28:31]
s_add_u32 s89,s56,4620288
ds_read_u16 v40,v40
v_lshl_add_u64 v[32:33],v[32:33],0,v[34:35]
v_mfma_f32_16x16x32_bf16 v[28:31],a[212:215],a[32:35],v[28:31]
buffer_load_dwordx4 v6,s[36:39],s89 offen lds
v_lshl_add_u64 v[34:35],v[60:61],0,v[62:63]
v_mfma_f32_16x16x32_bf16 v[24:27],a[200:203],a[32:35],v[16:19]
ds_read_u16_d16_hi v42,v42
v_lshlrev_b32_sdwa v41,1,v41 dst_sel:DWORD dst_unused:UNUSED_PAD src0_sel:DWORD src1_sel:WORD_1
v_mfma_f32_16x16x32_bf16 v[24:27],a[196:199],a[16:19],v[24:27]
s_waitcnt lgkmcnt(4)
ds_read_u16 v41,v41
v_lshl_add_u64 v[60:61],v[56:57],0,v[58:59]
v_mfma_f32_16x16x32_bf16 v[24:27],a[192:195],a[0:3],v[24:27]
TP3285_%=:
s_cbranch_execz TP5419_%=
v_lshlrev_b32_sdwa v43,1,v43 dst_sel:DWORD dst_unused:UNUSED_PAD src0_sel:DWORD src1_sel:WORD_1
v_lshlrev_b32_sdwa v36,1,v36 dst_sel:DWORD dst_unused:UNUSED_PAD src0_sel:DWORD src1_sel:WORD_1
v_mfma_f32_16x16x32_bf16 v[56:59],a[192:195],a[4:7],v[16:19]
s_waitcnt lgkmcnt(3)
ds_read_u16_d16_hi v43,v43
v_or_b32_e64 v62,v52,v54
v_mfma_f32_16x16x32_bf16 v[56:59],a[196:199],a[20:23],v[56:59]
s_waitcnt vmcnt(7)
ds_read_u16 v36,v36
v_cvt_pk_bf16_f32 v63,v53,v55
v_lshlrev_b32_sdwa v38,1,v38 dst_sel:DWORD dst_unused:UNUSED_PAD src0_sel:DWORD src1_sel:WORD_1
v_mfma_f32_16x16x32_bf16 v[56:59],a[200:203],a[36:39],v[56:59]
s_barrier
ds_read_u16_d16_hi v38,v38
v_lshlrev_b32_sdwa v37,1,v37 dst_sel:DWORD dst_unused:UNUSED_PAD src0_sel:DWORD src1_sel:WORD_1
v_mfma_f32_16x16x32_bf16 v[52:55],a[212:215],a[36:39],v[20:23]
s_add_u32 m0,s57,81328
ds_read_u16 v37,v37
v_lshlrev_b32_sdwa v39,1,v39 dst_sel:DWORD dst_unused:UNUSED_PAD src0_sel:DWORD src1_sel:WORD_1
v_mfma_f32_16x16x32_bf16 v[52:55],a[204:207],a[20:23],v[52:55]
s_add_u32 s89,s56,4718592
ds_read_u16_d16_hi v39,v39
v_lshlrev_b32_sdwa v44,1,v44 dst_sel:DWORD dst_unused:UNUSED_PAD src0_sel:DWORD src1_sel:WORD_1
v_mfma_f32_16x16x32_bf16 v[52:55],a[208:211],a[4:7],v[52:55]
buffer_load_dwordx4 v6,s[36:39],s89 offen lds
v_lshlrev_b32_sdwa v46,1,v46 dst_sel:DWORD dst_unused:UNUSED_PAD src0_sel:DWORD src1_sel:WORD_1
v_mfma_f32_16x16x32_bf16 v[232:235],a[236:239],v[32:35],v[232:235]
s_add_u32 m0,s58,6160
ds_read_u16 v44,v44
v_lshlrev_b32_sdwa v45,1,v45 dst_sel:DWORD dst_unused:UNUSED_PAD src0_sel:DWORD src1_sel:WORD_1
v_mfma_f32_16x16x32_bf16 v[236:239],a[236:239],v[60:63],v[236:239]
s_add_u32 s69,s69,s59
ds_read_u16_d16_hi v46,v46
v_lshlrev_b32_sdwa v47,1,v47 dst_sel:DWORD dst_unused:UNUSED_PAD src0_sel:DWORD src1_sel:WORD_1
v_mfma_f32_16x16x32_bf16 v[164:167],a[244:247],v[60:63],v[164:167]
buffer_load_dwordx4 v5,s[44:47],s69 offen offset:0 lds
ds_read_u16 v45,v45
v_mfma_f32_16x16x32_bf16 v[160:163],a[244:247],v[32:35],v[160:163]
ds_read_u16_d16_hi v47,v47
ds_read_b64_tr_b16 a[228:229],v3 offset:18480
v_mfma_f32_16x16x32_bf16 v[240:243],a[248:251],v[32:35],v[240:243]
s_add_u32 s69,s69,s53
ds_read_b64_tr_b16 a[230:231],v3 offset:20016
v_lshlrev_b32_sdwa v48,1,v48 dst_sel:DWORD dst_unused:UNUSED_PAD src0_sel:DWORD src1_sel:WORD_1
v_mfma_f32_16x16x32_bf16 v[244:247],a[248:251],v[60:63],v[244:247]
buffer_load_dwordx4 v5,s[44:47],s69 offen offset:1024 lds
v_lshlrev_b32_sdwa v50,1,v50 dst_sel:DWORD dst_unused:UNUSED_PAD src0_sel:DWORD src1_sel:WORD_1
v_mfma_f32_16x16x32_bf16 v[180:183],a[252:255],v[60:63],v[180:183]
ds_read_u16 v48,v48
v_lshlrev_b32_sdwa v49,1,v49 dst_sel:DWORD dst_unused:UNUSED_PAD src0_sel:DWORD src1_sel:WORD_1
v_mfma_f32_16x16x32_bf16 v[176:179],a[252:255],v[32:35],v[176:179]
ds_read_u16_d16_hi v50,v50
v_lshlrev_b32_sdwa v51,1,v51 dst_sel:DWORD dst_unused:UNUSED_PAD src0_sel:DWORD src1_sel:WORD_1
v_mfma_f32_16x16x32_bf16 v[248:251],a[240:243],v[32:35],v[248:251]
ds_read_u16 v49,v49
v_mfma_f32_16x16x32_bf16 v[252:255],a[240:243],v[60:63],v[252:255]
s_add_u32 s69,s69,s53
ds_read_u16_d16_hi v51,v51
v_mfma_f32_16x16x32_bf16 v[196:199],a[224:227],v[60:63],v[196:199]
buffer_load_dwordx4 v5,s[44:47],s69 offen offset:2048 lds
v_mfma_f32_16x16x32_bf16 v[192:195],a[224:227],v[32:35],v[192:195]
ds_read_b64_tr_b16 a[232:233],v3 offset:18488
v_mfma_f32_16x16x32_bf16 v[60:63],a[208:211],a[8:11],v[20:23]
ds_read_b64_tr_b16 a[234:235],v3 offset:20024
v_lshlrev_b32_sdwa v28,1,v28 dst_sel:DWORD dst_unused:UNUSED_PAD src0_sel:DWORD src1_sel:WORD_1
v_mfma_f32_16x16x32_bf16 v[60:63],a[204:207],a[24:27],v[60:63]
s_waitcnt lgkmcnt(6)
ds_read_u16 v28,v28
v_lshl_add_u64 v[40:41],v[40:41],0,v[42:43]
v_fma_mixlo_f16 v8,v57,s99,0
v_mfma_f32_16x16x32_bf16 v[60:63],a[212:215],a[40:43],v[60:63]
ds_read_b64_tr_b16 a[220:221],v1 offset:18480
v_lshl_add_u64 v[42:43],v[36:37],0,v[38:39]
v_lshlrev_b32_sdwa v30,1,v30 dst_sel:DWORD dst_unused:UNUSED_PAD src0_sel:DWORD src1_sel:WORD_1
v_mfma_f32_16x16x32_bf16 v[32:35],a[200:203],a[40:43],v[16:19]
ds_read_u16_d16_hi v30,v30
v_fma_mixhi_f16 v8,v59,s99,0
v_mfma_f32_16x16x32_bf16 v[32:35],a[196:199],a[24:27],v[32:35]
ds_read_b64_tr_b16 a[222:223],v1 offset:19504
v_lshl_add_u64 v[36:37],v[44:45],0,v[46:47]
v_lshlrev_b32_sdwa v29,1,v29 dst_sel:DWORD dst_unused:UNUSED_PAD src0_sel:DWORD src1_sel:WORD_1
v_pk_fma_f16 v9,v8,v8,s54
v_mfma_f32_16x16x32_bf16 v[32:35],a[192:195],a[8:11],v[32:35]
ds_read_u16 v29,v29
v_lshlrev_b32_sdwa v31,1,v31 dst_sel:DWORD dst_unused:UNUSED_PAD src0_sel:DWORD src1_sel:WORD_1
v_mfma_f32_16x16x32_bf16 v[44:47],a[192:195],a[12:15],v[16:19]
ds_read_u16_d16_hi v31,v31
v_pk_mul_f16 v9,v8,v9
v_mfma_f32_16x16x32_bf16 v[44:47],a[196:199],a[28:31],v[44:47]
s_waitcnt lgkmcnt(8)
ds_read_b64_tr_b16 a[216:217],v1 offset:20528
v_lshl_add_u64 v[38:39],v[48:49],0,v[50:51]
v_lshlrev_b32_sdwa v24,1,v24 dst_sel:DWORD dst_unused:UNUSED_PAD src0_sel:DWORD src1_sel:WORD_1
v_mfma_f32_16x16x32_bf16 v[44:47],a[200:203],a[44:47],v[44:47]
v_exp_f16 v8,v9
v_mfma_f32_16x16x32_bf16 v[48:51],a[212:215],a[44:47],v[20:23]
ds_read_u16 v24,v24
v_lshlrev_b32_sdwa v26,1,v26 dst_sel:DWORD dst_unused:UNUSED_PAD src0_sel:DWORD src1_sel:WORD_1
v_mfma_f32_16x16x32_bf16 v[48:51],a[204:207],a[28:31],v[48:51]
ds_read_u16_d16_hi v26,v26
v_lshlrev_b32_sdwa v25,1,v25 dst_sel:DWORD dst_unused:UNUSED_PAD src0_sel:DWORD src1_sel:WORD_1
v_mfma_f32_16x16x32_bf16 v[48:51],a[208:211],a[12:15],v[48:51]
v_exp_f16_sdwa v8,v9 dst_sel:WORD_1 dst_unused:UNUSED_PRESERVE src0_sel:WORD_1
v_mfma_f32_16x16x32_bf16 v[208:211],a[236:239],v[40:43],v[208:211]
ds_read_u16 v25,v25
v_lshlrev_b32_sdwa v27,1,v27 dst_sel:DWORD dst_unused:UNUSED_PAD src0_sel:DWORD src1_sel:WORD_1
v_mfma_f32_16x16x32_bf16 v[212:215],a[236:239],v[36:39],v[212:215]
ds_read_u16_d16_hi v27,v27
v_lshlrev_b32_sdwa v52,1,v52 dst_sel:DWORD dst_unused:UNUSED_PAD src0_sel:DWORD src1_sel:WORD_1
v_mfma_f32_16x16x32_bf16 v[172:175],a[244:247],v[36:39],v[172:175]
ds_read_u16 v52,v52
v_pk_fma_f16 v8,v8,s55,s55
v_mfma_f32_16x16x32_bf16 v[168:171],a[244:247],v[40:43],v[168:171]
ds_read_b64_tr_b16 a[236:237],v3 offset:18544
v_rcp_f16 v9,v8
v_lshlrev_b32_sdwa v54,1,v54 dst_sel:DWORD dst_unused:UNUSED_PAD src0_sel:DWORD src1_sel:WORD_1
v_mfma_f32_16x16x32_bf16 v[216:219],a[248:251],v[40:43],v[216:219]
ds_read_u16_d16_hi v54,v54
v_lshlrev_b32_sdwa v53,1,v53 dst_sel:DWORD dst_unused:UNUSED_PAD src0_sel:DWORD src1_sel:WORD_1
v_mfma_f32_16x16x32_bf16 v[220:223],a[248:251],v[36:39],v[220:223]
ds_read_u16 v53,v53
ds_read_b64_tr_b16 a[238:239],v3 offset:20080
v_mfma_f32_16x16x32_bf16 v[188:191],a[252:255],v[36:39],v[188:191]
ds_read_b64_tr_b16 a[244:245],v3 offset:18552
v_lshlrev_b32_sdwa v55,1,v55 dst_sel:DWORD dst_unused:UNUSED_PAD src0_sel:DWORD src1_sel:WORD_1
v_mfma_f32_16x16x32_bf16 v[184:187],a[252:255],v[40:43],v[184:187]
ds_read_u16_d16_hi v55,v55
v_rcp_f16_sdwa v9,v8 dst_sel:WORD_1 dst_unused:UNUSED_PRESERVE src0_sel:WORD_1
v_mfma_f32_16x16x32_bf16 v[224:227],a[240:243],v[40:43],v[224:227]
ds_read_b64_tr_b16 a[246:247],v3 offset:20088
ds_read_b64_tr_b16 a[248:249],v3 offset:18608
v_lshlrev_b32_sdwa v56,1,v56 dst_sel:DWORD dst_unused:UNUSED_PAD src0_sel:DWORD src1_sel:WORD_1
v_mfma_f32_16x16x32_bf16 v[228:231],a[240:243],v[36:39],v[228:231]
ds_read_u16 v56,v56
v_lshlrev_b32_sdwa v58,1,v58 dst_sel:DWORD dst_unused:UNUSED_PAD src0_sel:DWORD src1_sel:WORD_1
v_mfma_f32_16x16x32_bf16 v[204:207],a[224:227],v[36:39],v[204:207]
ds_read_u16_d16_hi v58,v58
v_fma_mix_f32 v57,v57,v9,0 op_sel_hi:[0,1,0]
v_mfma_f32_16x16x32_bf16 v[200:203],a[224:227],v[40:43],v[200:203]
ds_read_b64_tr_b16 a[250:251],v3 offset:20144
v_fma_mix_f32 v59,v59,v9,0 op_sel:[0,1,0] op_sel_hi:[0,1,0]
v_mfma_f32_16x16x32_bf16 v[36:39],a[208:211],a[48:51],v[20:23]
s_add_u32 m0,s58,12320
ds_read_b64 v[14:15],v7 offset:18480
v_lshlrev_b32_sdwa v60,1,v60 dst_sel:DWORD dst_unused:UNUSED_PAD src0_sel:DWORD src1_sel:WORD_1
v_mfma_f32_16x16x32_bf16 v[36:39],a[204:207],a[64:67],v[36:39]
s_waitcnt lgkmcnt(10)
ds_read_b64_tr_b16 a[252:253],v3 offset:18616
v_lshl_add_u64 v[28:29],v[28:29],0,v[30:31]
v_mfma_f32_16x16x32_bf16 v[36:39],a[212:215],a[80:83],v[36:39]
s_add_u32 s69,s69,s59
ds_read_b64_tr_b16 a[254:255],v3 offset:20152
v_lshl_add_u64 v[30:31],v[24:25],0,v[26:27]
v_lshlrev_b32_sdwa v62,1,v62 dst_sel:DWORD dst_unused:UNUSED_PAD src0_sel:DWORD src1_sel:WORD_1
v_mfma_f32_16x16x32_bf16 v[40:43],a[200:203],a[80:83],v[16:19]
buffer_load_dwordx4 v5,s[44:47],s69 offen offset:0 lds
v_lshlrev_b32_sdwa v61,1,v61 dst_sel:DWORD dst_unused:UNUSED_PAD src0_sel:DWORD src1_sel:WORD_1
v_mfma_f32_16x16x32_bf16 v[40:43],a[196:199],a[64:67],v[40:43]
s_waitcnt lgkmcnt(8)
ds_read_u16 v60,v60
v_lshl_add_u64 v[24:25],v[52:53],0,v[54:55]
v_mfma_f32_16x16x32_bf16 v[40:43],a[192:195],a[48:51],v[40:43]
s_waitcnt lgkmcnt(3)
ds_read_u16_d16_hi v62,v62
v_or3_b32 v13,v14,v15,v13
v_lshlrev_b32_sdwa v63,1,v63 dst_sel:DWORD dst_unused:UNUSED_PAD src0_sel:DWORD src1_sel:WORD_1
v_mfma_f32_16x16x32_bf16 v[52:55],a[192:195],a[52:55],v[16:19]
s_add_u32 s69,s69,s53
ds_read_b64 v[14:15],v7 offset:18992
v_or_b32 v26,v56,v58
v_lshlrev_b32_sdwa v32,1,v32 dst_sel:DWORD dst_unused:UNUSED_PAD src0_sel:DWORD src1_sel:WORD_1
v_mfma_f32_16x16x32_bf16 v[52:55],a[196:199],a[68:71],v[52:55]
buffer_load_dwordx4 v5,s[44:47],s69 offen offset:1024 lds
ds_read_u16 v61,v61
v_cvt_pk_bf16_f32 v27,v57,v59
v_lshlrev_b32_sdwa v34,1,v34 dst_sel:DWORD dst_unused:UNUSED_PAD src0_sel:DWORD src1_sel:WORD_1
v_mfma_f32_16x16x32_bf16 v[52:55],a[200:203],a[84:87],v[52:55]
s_add_u32 m0,s57,84864
ds_read_u16_d16_hi v63,v63
v_lshlrev_b32_sdwa v33,1,v33 dst_sel:DWORD dst_unused:UNUSED_PAD src0_sel:DWORD src1_sel:WORD_1
v_mfma_f32_16x16x32_bf16 v[56:59],a[212:215],a[84:87],v[20:23]
s_add_u32 s89,s56,4816896
ds_read_u16 v32,v32
v_lshlrev_b32_sdwa v35,1,v35 dst_sel:DWORD dst_unused:UNUSED_PAD src0_sel:DWORD src1_sel:WORD_1
v_mfma_f32_16x16x32_bf16 v[56:59],a[204:207],a[68:71],v[56:59]
buffer_load_dwordx4 v6,s[36:39],s89 offen lds
ds_read_u16_d16_hi v34,v34
v_mfma_f32_16x16x32_bf16 v[56:59],a[208:211],a[52:55],v[56:59]
s_waitcnt lgkmcnt(4)
ds_read_u16 v33,v33
v_or3_b32 v13,v14,v15,v13
v_mfma_f32_16x16x32_bf16 v[136:139],a[228:231],v[28:31],v[136:139]
s_add_u32 m0,s58,12320
ds_read_b64 v[14:15],v7 offset:19504
v_lshlrev_b32_sdwa v48,1,v48 dst_sel:DWORD dst_unused:UNUSED_PAD src0_sel:DWORD src1_sel:WORD_1
v_mfma_f32_16x16x32_bf16 v[140:143],a[228:231],v[24:27],v[140:143]
s_add_u32 s69,s69,s53
ds_read_u16_d16_hi v35,v35
v_lshlrev_b32_sdwa v50,1,v50 dst_sel:DWORD dst_unused:UNUSED_PAD src0_sel:DWORD src1_sel:WORD_1
v_mfma_f32_16x16x32_bf16 v[68:71],a[232:235],v[24:27],v[68:71]
buffer_load_dwordx4 v5,s[44:47],s69 offen offset:2048 lds
v_lshlrev_b32_sdwa v49,1,v49 dst_sel:DWORD dst_unused:UNUSED_PAD src0_sel:DWORD src1_sel:WORD_1
v_mfma_f32_16x16x32_bf16 v[64:67],a[232:235],v[28:31],v[64:67]
s_add_u32 m0,s57,8
ds_read_u16 v48,v48
v_lshlrev_b32_sdwa v51,1,v51 dst_sel:DWORD dst_unused:UNUSED_PAD src0_sel:DWORD src1_sel:WORD_1
v_mfma_f32_16x16x32_bf16 v[144:147],a[236:239],v[28:31],v[144:147]
s_add_u32 s89,s56,4915200
ds_read_u16_d16_hi v50,v50
v_lshlrev_b32_sdwa v44,1,v44 dst_sel:DWORD dst_unused:UNUSED_PAD src0_sel:DWORD src1_sel:WORD_1
v_mfma_f32_16x16x32_bf16 v[148:151],a[236:239],v[24:27],v[148:151]
s_waitcnt lgkmcnt(3)
buffer_load_dwordx4 v6,s[36:39],s89 offen lds
ds_read_u16 v49,v49
v_or3_b32 v13,v14,v15,v13
v_mfma_f32_16x16x32_bf16 v[84:87],a[244:247],v[24:27],v[84:87]
ds_read_u16_d16_hi v51,v51
v_lshlrev_b32_sdwa v46,1,v46 dst_sel:DWORD dst_unused:UNUSED_PAD src0_sel:DWORD src1_sel:WORD_1
v_mfma_f32_16x16x32_bf16 v[80:83],a[244:247],v[28:31],v[80:83]
ds_read_u16 v44,v44
v_lshlrev_b32_sdwa v45,1,v45 dst_sel:DWORD dst_unused:UNUSED_PAD src0_sel:DWORD src1_sel:WORD_1
v_mfma_f32_16x16x32_bf16 v[152:155],a[248:251],v[28:31],v[152:155]
ds_read_u16_d16_hi v46,v46
v_lshlrev_b32_sdwa v47,1,v47 dst_sel:DWORD dst_unused:UNUSED_PAD src0_sel:DWORD src1_sel:WORD_1
v_mfma_f32_16x16x32_bf16 v[156:159],a[248:251],v[24:27],v[156:159]
ds_read_u16 v45,v45
v_mfma_f32_16x16x32_bf16 v[100:103],a[252:255],v[24:27],v[100:103]
ds_read_u16_d16_hi v47,v47
v_mfma_f32_16x16x32_bf16 v[96:99],a[252:255],v[28:31],v[96:99]
ds_read_b64_tr_b16 a[218:219],v1 offset:21552
v_mfma_f32_16x16x32_bf16 v[24:27],a[208:211],a[56:59],v[20:23]
v_lshlrev_b32_sdwa v36,1,v36 dst_sel:DWORD dst_unused:UNUSED_PAD src0_sel:DWORD src1_sel:WORD_1
v_fma_mixlo_f16 v8,v53,s99,0
v_mfma_f32_16x16x32_bf16 v[24:27],a[204:207],a[72:75],v[24:27]
ds_read_u16 v36,v36
v_lshl_add_u64 v[60:61],v[60:61],0,v[62:63]
v_mfma_f32_16x16x32_bf16 v[24:27],a[212:215],a[88:91],v[24:27]
s_waitcnt lgkmcnt(7)
v_lshl_add_u64 v[62:63],v[32:33],0,v[34:35]
v_lshlrev_b32_sdwa v38,1,v38 dst_sel:DWORD dst_unused:UNUSED_PAD src0_sel:DWORD src1_sel:WORD_1
v_mfma_f32_16x16x32_bf16 v[28:31],a[200:203],a[88:91],v[16:19]
ds_read_u16_d16_hi v38,v38
v_fma_mixhi_f16 v8,v55,s99,0
v_mfma_f32_16x16x32_bf16 v[28:31],a[196:199],a[72:75],v[28:31]
s_waitcnt lgkmcnt(6)
v_lshl_add_u64 v[32:33],v[48:49],0,v[50:51]
v_lshlrev_b32_sdwa v37,1,v37 dst_sel:DWORD dst_unused:UNUSED_PAD src0_sel:DWORD src1_sel:WORD_1
v_pk_fma_f16 v9,v8,v8,s54
v_mfma_f32_16x16x32_bf16 v[28:31],a[192:195],a[56:59],v[28:31]
ds_read_u16 v37,v37
v_lshlrev_b32_sdwa v39,1,v39 dst_sel:DWORD dst_unused:UNUSED_PAD src0_sel:DWORD src1_sel:WORD_1
v_mfma_f32_16x16x32_bf16 v[48:51],a[192:195],a[60:63],v[16:19]
ds_read_b128 v[16:19],v4 offset:1024
v_pk_mul_f16 v9,v8,v9
v_mfma_f32_16x16x32_bf16 v[48:51],a[196:199],a[76:79],v[48:51]
s_waitcnt lgkmcnt(5)
ds_read_u16_d16_hi v39,v39
ds_read_b64_tr_b16 a[192:193],v1 offset:22576
v_lshl_add_u64 v[34:35],v[44:45],0,v[46:47]
v_lshlrev_b32_sdwa v40,1,v40 dst_sel:DWORD dst_unused:UNUSED_PAD src0_sel:DWORD src1_sel:WORD_1
v_mfma_f32_16x16x32_bf16 v[48:51],a[200:203],a[92:95],v[48:51]
ds_read_b64_tr_b16 a[194:195],v1 offset:23600
v_exp_f16_e64 v8,v9
v_mfma_f32_16x16x32_bf16 v[44:47],a[212:215],a[92:95],v[20:23]
ds_read_b128 v[20:23],v4 offset:1056
ds_read_u16 v40,v40
v_lshlrev_b32_sdwa v42,1,v42 dst_sel:DWORD dst_unused:UNUSED_PAD src0_sel:DWORD src1_sel:WORD_1
v_mfma_f32_16x16x32_bf16 v[44:47],a[204:207],a[76:79],v[44:47]
ds_read_u16_d16_hi v42,v42
ds_read_b64_tr_b16 a[196:197],v2 offset:22576
v_lshlrev_b32_sdwa v41,1,v41 dst_sel:DWORD dst_unused:UNUSED_PAD src0_sel:DWORD src1_sel:WORD_1
v_mfma_f32_16x16x32_bf16 v[44:47],a[208:211],a[60:63],v[44:47]
ds_read_u16 v41,v41
v_exp_f16_sdwa v8,v9 dst_sel:WORD_1 dst_unused:UNUSED_PRESERVE src0_sel:WORD_1
v_mfma_f32_16x16x32_bf16 v[112:115],a[228:231],v[60:63],v[112:115]
ds_read_b64_tr_b16 a[198:199],v2 offset:23600
ds_read_b64_tr_b16 a[200:201],v2 offset:20528
v_lshlrev_b32_sdwa v43,1,v43 dst_sel:DWORD dst_unused:UNUSED_PAD src0_sel:DWORD src1_sel:WORD_1
v_mfma_f32_16x16x32_bf16 v[116:119],a[228:231],v[32:35],v[116:119]
ds_read_u16_d16_hi v43,v43
v_lshlrev_b32_sdwa v56,1,v56 dst_sel:DWORD dst_unused:UNUSED_PAD src0_sel:DWORD src1_sel:WORD_1
v_mfma_f32_16x16x32_bf16 v[76:79],a[232:235],v[32:35],v[76:79]
ds_read_b64_tr_b16 a[202:203],v2 offset:21552
v_pk_fma_f16 v8,v8,s55,s55
v_mfma_f32_16x16x32_bf16 v[72:75],a[232:235],v[60:63],v[72:75]
v_rcp_f16 v9,v8
v_lshlrev_b32_sdwa v58,1,v58 dst_sel:DWORD dst_unused:UNUSED_PAD src0_sel:DWORD src1_sel:WORD_1
v_mfma_f32_16x16x32_bf16 v[120:123],a[236:239],v[60:63],v[120:123]
ds_read_u16 v56,v56
v_lshlrev_b32_sdwa v57,1,v57 dst_sel:DWORD dst_unused:UNUSED_PAD src0_sel:DWORD src1_sel:WORD_1
v_mfma_f32_16x16x32_bf16 v[124:127],a[236:239],v[32:35],v[124:127]
ds_read_u16_d16_hi v58,v58
v_lshlrev_b32_sdwa v59,1,v59 dst_sel:DWORD dst_unused:UNUSED_PAD src0_sel:DWORD src1_sel:WORD_1
v_mfma_f32_16x16x32_bf16 v[92:95],a[244:247],v[32:35],v[92:95]
ds_read_u16 v57,v57
v_lshlrev_b32_sdwa v52,1,v52 dst_sel:DWORD dst_unused:UNUSED_PAD src0_sel:DWORD src1_sel:WORD_1
v_mfma_f32_16x16x32_bf16 v[88:91],a[244:247],v[60:63],v[88:91]
ds_read_u16_d16_hi v59,v59
v_rcp_f16_sdwa v9,v8 dst_sel:WORD_1 dst_unused:UNUSED_PRESERVE src0_sel:WORD_1
v_mfma_f32_16x16x32_bf16 v[128:131],a[248:251],v[60:63],v[128:131]
ds_read_b64_tr_b16 a[212:213],v2 offset:18480
v_lshlrev_b32_sdwa v54,1,v54 dst_sel:DWORD dst_unused:UNUSED_PAD src0_sel:DWORD src1_sel:WORD_1
v_mfma_f32_16x16x32_bf16 v[132:135],a[248:251],v[32:35],v[132:135]
ds_read_b64_tr_b16 a[214:215],v2 offset:19504
v_fma_mix_f32 v53,v53,v9,0 op_sel_hi:[0,1,0]
v_mfma_f32_16x16x32_bf16 v[108:111],a[252:255],v[32:35],v[108:111]
ds_read_u16 v52,v52
v_fma_mix_f32 v55,v55,v9,0 op_sel:[0,1,0] op_sel_hi:[0,1,0]
v_mfma_f32_16x16x32_bf16 v[104:107],a[252:255],v[60:63],v[104:107]
s_waitcnt lgkmcnt(7)
ds_read_u16_d16_hi v54,v54
v_mfma_f32_16x16x32_bf16 v[32:35],a[220:223],a[0:3],v[16:19]
s_add_u32 m0,s57,3544
v_lshlrev_b32_sdwa v24,1,v24 dst_sel:DWORD dst_unused:UNUSED_PAD src0_sel:DWORD src1_sel:WORD_1
v_lshlrev_b32_sdwa v26,1,v26 dst_sel:DWORD dst_unused:UNUSED_PAD src0_sel:DWORD src1_sel:WORD_1
v_mfma_f32_16x16x32_bf16 v[32:35],a[216:219],a[16:19],v[32:35]
s_add_u32 s89,s56,5013504
ds_read_u16 v24,v24
v_lshl_add_u64 v[36:37],v[36:37],0,v[38:39]
v_mfma_f32_16x16x32_bf16 v[32:35],a[192:195],a[32:35],v[32:35]
buffer_load_dwordx4 v6,s[36:39],s89 offen lds
v_lshl_add_u64 v[38:39],v[40:41],0,v[42:43]
v_mfma_f32_16x16x32_bf16 v[60:63],a[196:199],a[32:35],v[20:23]
s_add_u32 m0,s57,7072
ds_read_u16_d16_hi v26,v26
v_lshlrev_b32_sdwa v25,1,v25 dst_sel:DWORD dst_unused:UNUSED_PAD src0_sel:DWORD src1_sel:WORD_1
v_mfma_f32_16x16x32_bf16 v[60:63],a[200:203],a[16:19],v[60:63]
s_waitcnt lgkmcnt(4)
ds_read_u16 v25,v25
v_lshl_add_u64 v[40:41],v[56:57],0,v[58:59]
v_mfma_f32_16x16x32_bf16 v[60:63],a[212:215],a[0:3],v[60:63]
s_add_u32 s89,s56,5111808
v_lshlrev_b32_sdwa v27,1,v27 dst_sel:DWORD dst_unused:UNUSED_PAD src0_sel:DWORD src1_sel:WORD_1
v_lshlrev_b32_sdwa v28,1,v28 dst_sel:DWORD dst_unused:UNUSED_PAD src0_sel:DWORD src1_sel:WORD_1
v_mfma_f32_16x16x32_bf16 v[56:59],a[212:215],a[4:7],v[20:23]
s_waitcnt lgkmcnt(3)
buffer_load_dwordx4 v6,s[36:39],s89 offen lds
v_or_b32 v42,v52,v54
v_mfma_f32_16x16x32_bf16 v[56:59],a[200:203],a[20:23],v[56:59]
ds_read_u16_d16_hi v27,v27
v_cvt_pk_bf16_f32 v43,v53,v55
v_lshlrev_b32_sdwa v30,1,v30 dst_sel:DWORD dst_unused:UNUSED_PAD src0_sel:DWORD src1_sel:WORD_1
v_mfma_f32_16x16x32_bf16 v[56:59],a[196:199],a[36:39],v[56:59]
ds_read_u16 v28,v28
v_lshlrev_b32_sdwa v29,1,v29 dst_sel:DWORD dst_unused:UNUSED_PAD src0_sel:DWORD src1_sel:WORD_1
v_mfma_f32_16x16x32_bf16 v[52:55],a[192:195],a[36:39],v[16:19]
ds_read_u16_d16_hi v30,v30
v_lshlrev_b32_sdwa v31,1,v31 dst_sel:DWORD dst_unused:UNUSED_PAD src0_sel:DWORD src1_sel:WORD_1
v_mfma_f32_16x16x32_bf16 v[52:55],a[216:219],a[20:23],v[52:55]
ds_read_u16 v29,v29
v_lshlrev_b32_sdwa v44,1,v44 dst_sel:DWORD dst_unused:UNUSED_PAD src0_sel:DWORD src1_sel:WORD_1
v_mfma_f32_16x16x32_bf16 v[52:55],a[220:223],a[4:7],v[52:55]
ds_read_u16_d16_hi v31,v31
v_lshlrev_b32_sdwa v46,1,v46 dst_sel:DWORD dst_unused:UNUSED_PAD src0_sel:DWORD src1_sel:WORD_1
v_mfma_f32_16x16x32_bf16 v[232:235],a[228:231],v[36:39],v[232:235]
ds_read_u16 v44,v44
v_lshlrev_b32_sdwa v45,1,v45 dst_sel:DWORD dst_unused:UNUSED_PAD src0_sel:DWORD src1_sel:WORD_1
v_mfma_f32_16x16x32_bf16 v[236:239],a[228:231],v[40:43],v[236:239]
ds_read_u16_d16_hi v46,v46
v_lshlrev_b32_sdwa v47,1,v47 dst_sel:DWORD dst_unused:UNUSED_PAD src0_sel:DWORD src1_sel:WORD_1
v_mfma_f32_16x16x32_bf16 v[164:167],a[232:235],v[40:43],v[164:167]
ds_read_u16 v45,v45
ds_read_u16_d16_hi v47,v47
v_mfma_f32_16x16x32_bf16 v[160:163],a[232:235],v[36:39],v[160:163]
ds_read_b64_tr_b16 a[240:241],v3 offset:0
v_lshlrev_b32_sdwa v48,1,v48 dst_sel:DWORD dst_unused:UNUSED_PAD src0_sel:DWORD src1_sel:WORD_1
v_mfma_f32_16x16x32_bf16 v[240:243],a[236:239],v[36:39],v[240:243]
ds_read_u16 v48,v48
v_lshlrev_b32_sdwa v50,1,v50 dst_sel:DWORD dst_unused:UNUSED_PAD src0_sel:DWORD src1_sel:WORD_1
v_mfma_f32_16x16x32_bf16 v[244:247],a[236:239],v[40:43],v[244:247]
ds_read_u16_d16_hi v50,v50
v_lshlrev_b32_sdwa v49,1,v49 dst_sel:DWORD dst_unused:UNUSED_PAD src0_sel:DWORD src1_sel:WORD_1
v_mfma_f32_16x16x32_bf16 v[180:183],a[244:247],v[40:43],v[180:183]
ds_read_u16 v49,v49
v_lshlrev_b32_sdwa v51,1,v51 dst_sel:DWORD dst_unused:UNUSED_PAD src0_sel:DWORD src1_sel:WORD_1
v_mfma_f32_16x16x32_bf16 v[176:179],a[244:247],v[36:39],v[176:179]
ds_read_u16_d16_hi v51,v51
v_mfma_f32_16x16x32_bf16 v[248:251],a[248:251],v[36:39],v[248:251]
ds_read_b64_tr_b16 a[242:243],v3 offset:1536
v_mfma_f32_16x16x32_bf16 v[252:255],a[248:251],v[40:43],v[252:255]
ds_read_b64_tr_b16 a[224:225],v3 offset:8
v_mfma_f32_16x16x32_bf16 v[196:199],a[252:255],v[40:43],v[196:199]
ds_read_b64_tr_b16 a[226:227],v3 offset:1544
v_mfma_f32_16x16x32_bf16 v[192:195],a[252:255],v[36:39],v[192:195]
v_mfma_f32_16x16x32_bf16 v[40:43],a[220:223],a[8:11],v[16:19]
ds_read_b64_tr_b16 a[204:205],v1 offset:0
v_lshlrev_b32_sdwa v32,1,v32 dst_sel:DWORD dst_unused:UNUSED_PAD src0_sel:DWORD src1_sel:WORD_1
v_mfma_f32_16x16x32_bf16 v[40:43],a[216:219],a[24:27],v[40:43]
s_waitcnt lgkmcnt(7)
ds_read_u16 v32,v32
v_lshl_add_u64 v[24:25],v[24:25],0,v[26:27]
v_fma_mixlo_f16 v8,v57,s99,0
v_mfma_f32_16x16x32_bf16 v[40:43],a[192:195],a[40:43],v[40:43]
ds_read_b64_tr_b16 a[206:207],v1 offset:1024
v_lshl_add_u64 v[26:27],v[28:29],0,v[30:31]
v_lshlrev_b32_sdwa v34,1,v34 dst_sel:DWORD dst_unused:UNUSED_PAD src0_sel:DWORD src1_sel:WORD_1
v_mfma_f32_16x16x32_bf16 v[36:39],a[196:199],a[40:43],v[20:23]
ds_read_u16_d16_hi v34,v34
v_fma_mixhi_f16 v8,v59,s99,0
v_mfma_f32_16x16x32_bf16 v[36:39],a[200:203],a[24:27],v[36:39]
ds_read_b64_tr_b16 a[208:209],v1 offset:2048
v_lshl_add_u64 v[28:29],v[44:45],0,v[46:47]
v_lshlrev_b32_sdwa v33,1,v33 dst_sel:DWORD dst_unused:UNUSED_PAD src0_sel:DWORD src1_sel:WORD_1
v_pk_fma_f16 v9,v8,v8,s54
v_mfma_f32_16x16x32_bf16 v[36:39],a[212:215],a[8:11],v[36:39]
ds_read_u16 v33,v33
v_lshlrev_b32_sdwa v35,1,v35 dst_sel:DWORD dst_unused:UNUSED_PAD src0_sel:DWORD src1_sel:WORD_1
v_mfma_f32_16x16x32_bf16 v[44:47],a[212:215],a[12:15],v[20:23]
ds_read_u16_d16_hi v35,v35
v_pk_mul_f16 v9,v8,v9
v_mfma_f32_16x16x32_bf16 v[44:47],a[200:203],a[28:31],v[44:47]
s_waitcnt lgkmcnt(7)
ds_read_b64_tr_b16 a[210:211],v1 offset:3072
v_lshl_add_u64 v[30:31],v[48:49],0,v[50:51]
v_lshlrev_b32_sdwa v60,1,v60 dst_sel:DWORD dst_unused:UNUSED_PAD src0_sel:DWORD src1_sel:WORD_1
v_mfma_f32_16x16x32_bf16 v[44:47],a[196:199],a[44:47],v[44:47]
v_exp_f16 v8,v9
v_mfma_f32_16x16x32_bf16 v[48:51],a[192:195],a[44:47],v[16:19]
ds_read_u16 v60,v60
v_lshlrev_b32_sdwa v62,1,v62 dst_sel:DWORD dst_unused:UNUSED_PAD src0_sel:DWORD src1_sel:WORD_1
v_mfma_f32_16x16x32_bf16 v[48:51],a[216:219],a[28:31],v[48:51]
ds_read_u16_d16_hi v62,v62
v_lshlrev_b32_sdwa v61,1,v61 dst_sel:DWORD dst_unused:UNUSED_PAD src0_sel:DWORD src1_sel:WORD_1
v_mfma_f32_16x16x32_bf16 v[48:51],a[220:223],a[12:15],v[48:51]
v_exp_f16_sdwa v8,v9 dst_sel:WORD_1 dst_unused:UNUSED_PRESERVE src0_sel:WORD_1
v_mfma_f32_16x16x32_bf16 v[208:211],a[228:231],v[24:27],v[208:211]
ds_read_u16 v61,v61
v_lshlrev_b32_sdwa v63,1,v63 dst_sel:DWORD dst_unused:UNUSED_PAD src0_sel:DWORD src1_sel:WORD_1
v_mfma_f32_16x16x32_bf16 v[212:215],a[228:231],v[28:31],v[212:215]
ds_read_u16_d16_hi v63,v63
v_lshlrev_b32_sdwa v52,1,v52 dst_sel:DWORD dst_unused:UNUSED_PAD src0_sel:DWORD src1_sel:WORD_1
v_mfma_f32_16x16x32_bf16 v[172:175],a[232:235],v[28:31],v[172:175]
ds_read_u16 v52,v52
v_pk_fma_f16 v8,v8,s55,s55
v_mfma_f32_16x16x32_bf16 v[168:171],a[232:235],v[24:27],v[168:171]
ds_read_b64_tr_b16 a[228:229],v3 offset:64
v_rcp_f16 v9,v8
v_lshlrev_b32_sdwa v54,1,v54 dst_sel:DWORD dst_unused:UNUSED_PAD src0_sel:DWORD src1_sel:WORD_1
v_mfma_f32_16x16x32_bf16 v[216:219],a[236:239],v[24:27],v[216:219]
ds_read_u16_d16_hi v54,v54
v_lshlrev_b32_sdwa v53,1,v53 dst_sel:DWORD dst_unused:UNUSED_PAD src0_sel:DWORD src1_sel:WORD_1
v_mfma_f32_16x16x32_bf16 v[220:223],a[236:239],v[28:31],v[220:223]
ds_read_u16 v53,v53
ds_read_b64_tr_b16 a[230:231],v3 offset:1600
v_mfma_f32_16x16x32_bf16 v[188:191],a[244:247],v[28:31],v[188:191]
ds_read_b64_tr_b16 a[232:233],v3 offset:72
v_lshlrev_b32_sdwa v55,1,v55 dst_sel:DWORD dst_unused:UNUSED_PAD src0_sel:DWORD src1_sel:WORD_1
v_mfma_f32_16x16x32_bf16 v[184:187],a[244:247],v[24:27],v[184:187]
ds_read_u16_d16_hi v55,v55
v_rcp_f16_sdwa v9,v8 dst_sel:WORD_1 dst_unused:UNUSED_PRESERVE src0_sel:WORD_1
v_mfma_f32_16x16x32_bf16 v[224:227],a[248:251],v[24:27],v[224:227]
ds_read_b64_tr_b16 a[234:235],v3 offset:1608
ds_read_b64_tr_b16 a[236:237],v3 offset:128
v_lshlrev_b32_sdwa v56,1,v56 dst_sel:DWORD dst_unused:UNUSED_PAD src0_sel:DWORD src1_sel:WORD_1
v_mfma_f32_16x16x32_bf16 v[228:231],a[248:251],v[28:31],v[228:231]
ds_read_u16 v56,v56
v_lshlrev_b32_sdwa v58,1,v58 dst_sel:DWORD dst_unused:UNUSED_PAD src0_sel:DWORD src1_sel:WORD_1
v_mfma_f32_16x16x32_bf16 v[204:207],a[252:255],v[28:31],v[204:207]
ds_read_u16_d16_hi v58,v58
v_fma_mix_f32 v57,v57,v9,0 op_sel_hi:[0,1,0]
v_mfma_f32_16x16x32_bf16 v[200:203],a[252:255],v[24:27],v[200:203]
ds_read_b64_tr_b16 a[238:239],v3 offset:1664
v_fma_mix_f32 v59,v59,v9,0 op_sel:[0,1,0] op_sel_hi:[0,1,0]
v_mfma_f32_16x16x32_bf16 v[28:31],a[220:223],a[48:51],v[16:19]
s_add_u32 m0,s57,10608
ds_read_b64 v[14:15],v7 offset:0
v_lshlrev_b32_sdwa v40,1,v40 dst_sel:DWORD dst_unused:UNUSED_PAD src0_sel:DWORD src1_sel:WORD_1
v_mfma_f32_16x16x32_bf16 v[28:31],a[216:219],a[64:67],v[28:31]
s_waitcnt lgkmcnt(10)
ds_read_b64_tr_b16 a[244:245],v3 offset:136
v_lshl_add_u64 v[32:33],v[32:33],0,v[34:35]
v_mfma_f32_16x16x32_bf16 v[28:31],a[192:195],a[80:83],v[28:31]
s_add_u32 s89,s56,5210112
ds_read_b64_tr_b16 a[246:247],v3 offset:1672
v_lshl_add_u64 v[34:35],v[60:61],0,v[62:63]
v_lshlrev_b32_sdwa v42,1,v42 dst_sel:DWORD dst_unused:UNUSED_PAD src0_sel:DWORD src1_sel:WORD_1
v_mfma_f32_16x16x32_bf16 v[24:27],a[196:199],a[80:83],v[20:23]
buffer_load_dwordx4 v6,s[36:39],s89 offen lds
v_lshlrev_b32_sdwa v41,1,v41 dst_sel:DWORD dst_unused:UNUSED_PAD src0_sel:DWORD src1_sel:WORD_1
v_mfma_f32_16x16x32_bf16 v[24:27],a[200:203],a[64:67],v[24:27]
s_waitcnt lgkmcnt(8)
ds_read_u16 v40,v40
v_lshl_add_u64 v[60:61],v[52:53],0,v[54:55]
v_mfma_f32_16x16x32_bf16 v[24:27],a[212:215],a[48:51],v[24:27]
s_waitcnt lgkmcnt(3)
ds_read_u16_d16_hi v42,v42
v_or3_b32 v13,v14,v15,v13
v_lshlrev_b32_sdwa v43,1,v43 dst_sel:DWORD dst_unused:UNUSED_PAD src0_sel:DWORD src1_sel:WORD_1
v_mfma_f32_16x16x32_bf16 v[52:55],a[212:215],a[52:55],v[20:23]
s_add_u32 m0,s57,14152
ds_read_b64 v[14:15],v7 offset:512
v_or_b32 v62,v56,v58
v_lshlrev_b32_sdwa v36,1,v36 dst_sel:DWORD dst_unused:UNUSED_PAD src0_sel:DWORD src1_sel:WORD_1
v_mfma_f32_16x16x32_bf16 v[52:55],a[200:203],a[68:71],v[52:55]
s_add_u32 s89,s56,5308416
ds_read_u16 v41,v41
v_cvt_pk_bf16_f32 v63,v57,v59
v_lshlrev_b32_sdwa v38,1,v38 dst_sel:DWORD dst_unused:UNUSED_PAD src0_sel:DWORD src1_sel:WORD_1
v_mfma_f32_16x16x32_bf16 v[52:55],a[196:199],a[84:87],v[52:55]
buffer_load_dwordx4 v6,s[36:39],s89 offen lds
v_lshlrev_b32_sdwa v37,1,v37 dst_sel:DWORD dst_unused:UNUSED_PAD src0_sel:DWORD src1_sel:WORD_1
v_mfma_f32_16x16x32_bf16 v[56:59],a[192:195],a[84:87],v[16:19]
ds_read_u16_d16_hi v43,v43
v_lshlrev_b32_sdwa v39,1,v39 dst_sel:DWORD dst_unused:UNUSED_PAD src0_sel:DWORD src1_sel:WORD_1
v_mfma_f32_16x16x32_bf16 v[56:59],a[216:219],a[68:71],v[56:59]
ds_read_u16 v36,v36
v_lshlrev_b32_sdwa v48,1,v48 dst_sel:DWORD dst_unused:UNUSED_PAD src0_sel:DWORD src1_sel:WORD_1
v_mfma_f32_16x16x32_bf16 v[56:59],a[220:223],a[52:55],v[56:59]
s_waitcnt lgkmcnt(3)
ds_read_u16_d16_hi v38,v38
v_or3_b32 v13,v14,v15,v13
v_mfma_f32_16x16x32_bf16 v[136:139],a[240:243],v[32:35],v[136:139]
ds_read_b64 v[14:15],v7 offset:1024
v_lshlrev_b32_sdwa v50,1,v50 dst_sel:DWORD dst_unused:UNUSED_PAD src0_sel:DWORD src1_sel:WORD_1
v_mfma_f32_16x16x32_bf16 v[140:143],a[240:243],v[60:63],v[140:143]
ds_read_u16 v37,v37
v_lshlrev_b32_sdwa v49,1,v49 dst_sel:DWORD dst_unused:UNUSED_PAD src0_sel:DWORD src1_sel:WORD_1
v_mfma_f32_16x16x32_bf16 v[68:71],a[224:227],v[60:63],v[68:71]
ds_read_u16_d16_hi v39,v39
v_lshlrev_b32_sdwa v51,1,v51 dst_sel:DWORD dst_unused:UNUSED_PAD src0_sel:DWORD src1_sel:WORD_1
v_mfma_f32_16x16x32_bf16 v[64:67],a[224:227],v[32:35],v[64:67]
ds_read_u16 v48,v48
v_lshlrev_b32_sdwa v44,1,v44 dst_sel:DWORD dst_unused:UNUSED_PAD src0_sel:DWORD src1_sel:WORD_1
v_mfma_f32_16x16x32_bf16 v[144:147],a[228:231],v[32:35],v[144:147]
ds_read_u16_d16_hi v50,v50
v_lshlrev_b32_sdwa v46,1,v46 dst_sel:DWORD dst_unused:UNUSED_PAD src0_sel:DWORD src1_sel:WORD_1
v_mfma_f32_16x16x32_bf16 v[148:151],a[228:231],v[60:63],v[148:151]
s_waitcnt lgkmcnt(4)
ds_read_u16 v49,v49
TP3814_%=:
s_cbranch_execz TP5958_%=
v_or3_b32 v13,v14,v15,v13
v_mfma_f32_16x16x32_bf16 v[84:87],a[232:235],v[60:63],v[84:87]
s_add_u32 m0,s57,17688
ds_read_u16_d16_hi v51,v51
v_lshlrev_b32_sdwa v45,1,v45 dst_sel:DWORD dst_unused:UNUSED_PAD src0_sel:DWORD src1_sel:WORD_1
v_mfma_f32_16x16x32_bf16 v[80:83],a[232:235],v[32:35],v[80:83]
s_add_u32 s89,s56,5406720
ds_read_u16 v44,v44
v_lshlrev_b32_sdwa v47,1,v47 dst_sel:DWORD dst_unused:UNUSED_PAD src0_sel:DWORD src1_sel:WORD_1
v_mfma_f32_16x16x32_bf16 v[152:155],a[236:239],v[32:35],v[152:155]
buffer_load_dwordx4 v6,s[36:39],s89 offen lds
v_mfma_f32_16x16x32_bf16 v[156:159],a[236:239],v[60:63],v[156:159]
ds_read_u16_d16_hi v46,v46
v_mfma_f32_16x16x32_bf16 v[100:103],a[244:247],v[60:63],v[100:103]
ds_read_u16 v45,v45
v_mfma_f32_16x16x32_bf16 v[96:99],a[244:247],v[32:35],v[96:99]
ds_read_u16_d16_hi v47,v47
v_mfma_f32_16x16x32_bf16 v[60:63],a[220:223],a[56:59],v[16:19]
v_lshlrev_b32_sdwa v28,1,v28 dst_sel:DWORD dst_unused:UNUSED_PAD src0_sel:DWORD src1_sel:WORD_1
v_fma_mixlo_f16 v8,v53,s99,0
v_mfma_f32_16x16x32_bf16 v[60:63],a[216:219],a[72:75],v[60:63]
ds_read_u16 v28,v28
v_lshl_add_u64 v[40:41],v[40:41],0,v[42:43]
v_mfma_f32_16x16x32_bf16 v[60:63],a[192:195],a[88:91],v[60:63]
s_waitcnt lgkmcnt(6)
v_lshl_add_u64 v[42:43],v[36:37],0,v[38:39]
v_lshlrev_b32_sdwa v30,1,v30 dst_sel:DWORD dst_unused:UNUSED_PAD src0_sel:DWORD src1_sel:WORD_1
v_mfma_f32_16x16x32_bf16 v[32:35],a[196:199],a[88:91],v[20:23]
ds_read_u16_d16_hi v30,v30
v_fma_mixhi_f16 v8,v55,s99,0
v_mfma_f32_16x16x32_bf16 v[32:35],a[200:203],a[72:75],v[32:35]
s_waitcnt lgkmcnt(5)
v_lshl_add_u64 v[36:37],v[48:49],0,v[50:51]
v_lshlrev_b32_sdwa v29,1,v29 dst_sel:DWORD dst_unused:UNUSED_PAD src0_sel:DWORD src1_sel:WORD_1
v_pk_fma_f16 v9,v8,v8,s54
v_mfma_f32_16x16x32_bf16 v[32:35],a[212:215],a[56:59],v[32:35]
ds_read_u16 v29,v29
v_lshlrev_b32_sdwa v31,1,v31 dst_sel:DWORD dst_unused:UNUSED_PAD src0_sel:DWORD src1_sel:WORD_1
v_mfma_f32_16x16x32_bf16 v[48:51],a[212:215],a[60:63],v[20:23]
s_nop 0
ds_read_b128 v[20:23],v4 offset:1152
v_pk_mul_f16 v9,v8,v9
v_mfma_f32_16x16x32_bf16 v[48:51],a[200:203],a[76:79],v[48:51]
s_waitcnt lgkmcnt(4)
ds_read_u16_d16_hi v31,v31
ds_read_b64_tr_b16 a[212:213],v1 offset:4096
v_lshl_add_u64 v[38:39],v[44:45],0,v[46:47]
v_lshlrev_b32_sdwa v24,1,v24 dst_sel:DWORD dst_unused:UNUSED_PAD src0_sel:DWORD src1_sel:WORD_1
v_mfma_f32_16x16x32_bf16 v[48:51],a[196:199],a[92:95],v[48:51]
ds_read_b64_tr_b16 a[214:215],v1 offset:5120
v_exp_f16 v8,v9
v_mfma_f32_16x16x32_bf16 v[44:47],a[192:195],a[92:95],v[16:19]
ds_read_b128 v[16:19],v4 offset:1184
ds_read_u16 v24,v24
v_lshlrev_b32_sdwa v26,1,v26 dst_sel:DWORD dst_unused:UNUSED_PAD src0_sel:DWORD src1_sel:WORD_1
v_mfma_f32_16x16x32_bf16 v[44:47],a[216:219],a[76:79],v[44:47]
ds_read_u16_d16_hi v26,v26
ds_read_b64_tr_b16 a[200:201],v2 offset:4096
v_lshlrev_b32_sdwa v25,1,v25 dst_sel:DWORD dst_unused:UNUSED_PAD src0_sel:DWORD src1_sel:WORD_1
v_mfma_f32_16x16x32_bf16 v[44:47],a[220:223],a[60:63],v[44:47]
ds_read_u16 v25,v25
v_exp_f16_sdwa v8,v9 dst_sel:WORD_1 dst_unused:UNUSED_PRESERVE src0_sel:WORD_1
v_mfma_f32_16x16x32_bf16 v[112:115],a[240:243],v[40:43],v[112:115]
ds_read_b64_tr_b16 a[202:203],v2 offset:5120
ds_read_b64_tr_b16 a[196:197],v2 offset:2048
v_lshlrev_b32_sdwa v27,1,v27 dst_sel:DWORD dst_unused:UNUSED_PAD src0_sel:DWORD src1_sel:WORD_1
v_mfma_f32_16x16x32_bf16 v[116:119],a[240:243],v[36:39],v[116:119]
ds_read_u16_d16_hi v27,v27
v_lshlrev_b32_sdwa v56,1,v56 dst_sel:DWORD dst_unused:UNUSED_PAD src0_sel:DWORD src1_sel:WORD_1
v_mfma_f32_16x16x32_bf16 v[76:79],a[224:227],v[36:39],v[76:79]
ds_read_b64_tr_b16 a[198:199],v2 offset:3072
v_pk_fma_f16 v8,v8,s55,s55
v_mfma_f32_16x16x32_bf16 v[72:75],a[224:227],v[40:43],v[72:75]
v_rcp_f16 v9,v8
v_lshlrev_b32_sdwa v58,1,v58 dst_sel:DWORD dst_unused:UNUSED_PAD src0_sel:DWORD src1_sel:WORD_1
v_mfma_f32_16x16x32_bf16 v[120:123],a[228:231],v[40:43],v[120:123]
ds_read_u16 v56,v56
v_lshlrev_b32_sdwa v57,1,v57 dst_sel:DWORD dst_unused:UNUSED_PAD src0_sel:DWORD src1_sel:WORD_1
v_mfma_f32_16x16x32_bf16 v[124:127],a[228:231],v[36:39],v[124:127]
ds_read_u16_d16_hi v58,v58
v_lshlrev_b32_sdwa v59,1,v59 dst_sel:DWORD dst_unused:UNUSED_PAD src0_sel:DWORD src1_sel:WORD_1
v_mfma_f32_16x16x32_bf16 v[92:95],a[232:235],v[36:39],v[92:95]
ds_read_u16 v57,v57
v_lshlrev_b32_sdwa v52,1,v52 dst_sel:DWORD dst_unused:UNUSED_PAD src0_sel:DWORD src1_sel:WORD_1
v_mfma_f32_16x16x32_bf16 v[88:91],a[232:235],v[40:43],v[88:91]
ds_read_u16_d16_hi v59,v59
v_rcp_f16_sdwa v9,v8 dst_sel:WORD_1 dst_unused:UNUSED_PRESERVE src0_sel:WORD_1
v_mfma_f32_16x16x32_bf16 v[128:131],a[236:239],v[40:43],v[128:131]
ds_read_b64_tr_b16 a[192:193],v2 offset:0
v_lshlrev_b32_sdwa v54,1,v54 dst_sel:DWORD dst_unused:UNUSED_PAD src0_sel:DWORD src1_sel:WORD_1
v_mfma_f32_16x16x32_bf16 v[132:135],a[236:239],v[36:39],v[132:135]
ds_read_b64_tr_b16 a[194:195],v2 offset:1024
v_fma_mix_f32 v53,v53,v9,0 op_sel_hi:[0,1,0]
v_mfma_f32_16x16x32_bf16 v[108:111],a[244:247],v[36:39],v[108:111]
ds_read_u16 v52,v52
v_fma_mix_f32 v55,v55,v9,0 op_sel:[0,1,0] op_sel_hi:[0,1,0]
v_mfma_f32_16x16x32_bf16 v[104:107],a[244:247],v[40:43],v[104:107]
s_waitcnt lgkmcnt(7)
ds_read_u16_d16_hi v54,v54
v_mfma_f32_16x16x32_bf16 v[36:39],a[204:207],a[0:3],v[20:23]
s_add_u32 m0,s57,21216
v_lshlrev_b32_sdwa v60,1,v60 dst_sel:DWORD dst_unused:UNUSED_PAD src0_sel:DWORD src1_sel:WORD_1
v_lshlrev_b32_sdwa v62,1,v62 dst_sel:DWORD dst_unused:UNUSED_PAD src0_sel:DWORD src1_sel:WORD_1
v_mfma_f32_16x16x32_bf16 v[36:39],a[208:211],a[16:19],v[36:39]
s_add_u32 s89,s56,5505024
ds_read_u16 v60,v60
v_lshl_add_u64 v[28:29],v[28:29],0,v[30:31]
v_mfma_f32_16x16x32_bf16 v[36:39],a[212:215],a[32:35],v[36:39]
buffer_load_dwordx4 v6,s[36:39],s89 offen lds
v_lshl_add_u64 v[30:31],v[24:25],0,v[26:27]
v_mfma_f32_16x16x32_bf16 v[40:43],a[200:203],a[32:35],v[16:19]
ds_read_u16_d16_hi v62,v62
v_lshlrev_b32_sdwa v61,1,v61 dst_sel:DWORD dst_unused:UNUSED_PAD src0_sel:DWORD src1_sel:WORD_1
v_mfma_f32_16x16x32_bf16 v[40:43],a[196:199],a[16:19],v[40:43]
s_waitcnt lgkmcnt(4)
ds_read_u16 v61,v61
v_lshl_add_u64 v[24:25],v[56:57],0,v[58:59]
v_mfma_f32_16x16x32_bf16 v[40:43],a[192:195],a[0:3],v[40:43]
v_lshlrev_b32_sdwa v63,1,v63 dst_sel:DWORD dst_unused:UNUSED_PAD src0_sel:DWORD src1_sel:WORD_1
v_lshlrev_b32_sdwa v32,1,v32 dst_sel:DWORD dst_unused:UNUSED_PAD src0_sel:DWORD src1_sel:WORD_1
v_mfma_f32_16x16x32_bf16 v[56:59],a[192:195],a[4:7],v[16:19]
s_waitcnt lgkmcnt(3)
ds_read_u16_d16_hi v63,v63
v_or_b32 v26,v52,v54
v_mfma_f32_16x16x32_bf16 v[56:59],a[196:199],a[20:23],v[56:59]
s_waitcnt vmcnt(7)
ds_read_u16 v32,v32
v_cvt_pk_bf16_f32 v27,v53,v55
v_lshlrev_b32_sdwa v34,1,v34 dst_sel:DWORD dst_unused:UNUSED_PAD src0_sel:DWORD src1_sel:WORD_1
v_mfma_f32_16x16x32_bf16 v[56:59],a[200:203],a[36:39],v[56:59]
s_barrier
ds_read_u16_d16_hi v34,v34
v_lshlrev_b32_sdwa v33,1,v33 dst_sel:DWORD dst_unused:UNUSED_PAD src0_sel:DWORD src1_sel:WORD_1
v_mfma_f32_16x16x32_bf16 v[52:55],a[212:215],a[36:39],v[20:23]
ds_read_u16 v33,v33
v_lshlrev_b32_sdwa v35,1,v35 dst_sel:DWORD dst_unused:UNUSED_PAD src0_sel:DWORD src1_sel:WORD_1
v_mfma_f32_16x16x32_bf16 v[52:55],a[208:211],a[20:23],v[52:55]
s_add_u32 m0,s58,18480
ds_read_u16_d16_hi v35,v35
v_lshlrev_b32_sdwa v44,1,v44 dst_sel:DWORD dst_unused:UNUSED_PAD src0_sel:DWORD src1_sel:WORD_1
v_mfma_f32_16x16x32_bf16 v[52:55],a[204:207],a[4:7],v[52:55]
s_mov_b32 s69,s88
ds_read_u16 v44,v44
v_lshlrev_b32_sdwa v46,1,v46 dst_sel:DWORD dst_unused:UNUSED_PAD src0_sel:DWORD src1_sel:WORD_1
v_mfma_f32_16x16x32_bf16 v[232:235],a[240:243],v[28:31],v[232:235]
buffer_load_dwordx4 v5,s[44:47],s69 offen offset:0 lds
v_lshlrev_b32_sdwa v45,1,v45 dst_sel:DWORD dst_unused:UNUSED_PAD src0_sel:DWORD src1_sel:WORD_1
v_mfma_f32_16x16x32_bf16 v[236:239],a[240:243],v[24:27],v[236:239]
ds_read_u16_d16_hi v46,v46
v_lshlrev_b32_sdwa v47,1,v47 dst_sel:DWORD dst_unused:UNUSED_PAD src0_sel:DWORD src1_sel:WORD_1
v_mfma_f32_16x16x32_bf16 v[164:167],a[224:227],v[24:27],v[164:167]
ds_read_u16 v45,v45
ds_read_u16_d16_hi v47,v47
v_mfma_f32_16x16x32_bf16 v[160:163],a[224:227],v[28:31],v[160:163]
ds_read_b64_tr_b16 a[248:249],v3 offset:6160
v_lshlrev_b32_sdwa v48,1,v48 dst_sel:DWORD dst_unused:UNUSED_PAD src0_sel:DWORD src1_sel:WORD_1
v_mfma_f32_16x16x32_bf16 v[240:243],a[228:231],v[28:31],v[240:243]
s_add_u32 s69,s69,s53
ds_read_u16 v48,v48
v_lshlrev_b32_sdwa v50,1,v50 dst_sel:DWORD dst_unused:UNUSED_PAD src0_sel:DWORD src1_sel:WORD_1
v_mfma_f32_16x16x32_bf16 v[244:247],a[228:231],v[24:27],v[244:247]
buffer_load_dwordx4 v5,s[44:47],s69 offen offset:1024 lds
ds_read_u16_d16_hi v50,v50
v_mfma_f32_16x16x32_bf16 v[180:183],a[232:235],v[24:27],v[180:183]
ds_read_b64_tr_b16 a[250:251],v3 offset:7696
v_lshlrev_b32_sdwa v49,1,v49 dst_sel:DWORD dst_unused:UNUSED_PAD src0_sel:DWORD src1_sel:WORD_1
v_mfma_f32_16x16x32_bf16 v[176:179],a[232:235],v[28:31],v[176:179]
ds_read_u16 v49,v49
v_lshlrev_b32_sdwa v51,1,v51 dst_sel:DWORD dst_unused:UNUSED_PAD src0_sel:DWORD src1_sel:WORD_1
v_mfma_f32_16x16x32_bf16 v[248:251],a[236:239],v[28:31],v[248:251]
ds_read_u16_d16_hi v51,v51
v_mfma_f32_16x16x32_bf16 v[252:255],a[236:239],v[24:27],v[252:255]
s_add_u32 s69,s69,s53
ds_read_b64_tr_b16 a[252:253],v3 offset:6168
v_mfma_f32_16x16x32_bf16 v[196:199],a[244:247],v[24:27],v[196:199]
buffer_load_dwordx4 v5,s[44:47],s69 offen offset:2048 lds
v_mfma_f32_16x16x32_bf16 v[192:195],a[244:247],v[28:31],v[192:195]
ds_read_b64_tr_b16 a[254:255],v3 offset:7704
v_mfma_f32_16x16x32_bf16 v[24:27],a[204:207],a[8:11],v[20:23]
ds_read_b64_tr_b16 a[216:217],v1 offset:6160
v_lshlrev_b32_sdwa v36,1,v36 dst_sel:DWORD dst_unused:UNUSED_PAD src0_sel:DWORD src1_sel:WORD_1
v_mfma_f32_16x16x32_bf16 v[24:27],a[208:211],a[24:27],v[24:27]
s_waitcnt lgkmcnt(7)
ds_read_u16 v36,v36
v_lshl_add_u64 v[60:61],v[60:61],0,v[62:63]
v_fma_mixlo_f16 v8,v57,s99,0
v_mfma_f32_16x16x32_bf16 v[24:27],a[212:215],a[40:43],v[24:27]
ds_read_b64_tr_b16 a[218:219],v1 offset:7184
v_lshl_add_u64 v[62:63],v[32:33],0,v[34:35]
v_lshlrev_b32_sdwa v38,1,v38 dst_sel:DWORD dst_unused:UNUSED_PAD src0_sel:DWORD src1_sel:WORD_1
v_mfma_f32_16x16x32_bf16 v[28:31],a[200:203],a[40:43],v[16:19]
ds_read_u16_d16_hi v38,v38
v_fma_mixhi_f16 v8,v59,s99,0
v_mfma_f32_16x16x32_bf16 v[28:31],a[196:199],a[24:27],v[28:31]
ds_read_b64_tr_b16 a[220:221],v1 offset:8208
v_lshl_add_u64 v[32:33],v[44:45],0,v[46:47]
v_lshlrev_b32_sdwa v37,1,v37 dst_sel:DWORD dst_unused:UNUSED_PAD src0_sel:DWORD src1_sel:WORD_1
v_pk_fma_f16 v9,v8,v8,s54
v_mfma_f32_16x16x32_bf16 v[28:31],a[192:195],a[8:11],v[28:31]
ds_read_u16 v37,v37
v_lshlrev_b32_sdwa v39,1,v39 dst_sel:DWORD dst_unused:UNUSED_PAD src0_sel:DWORD src1_sel:WORD_1
v_mfma_f32_16x16x32_bf16 v[44:47],a[192:195],a[12:15],v[16:19]
ds_read_u16_d16_hi v39,v39
v_pk_mul_f16 v9,v8,v9
v_mfma_f32_16x16x32_bf16 v[44:47],a[196:199],a[28:31],v[44:47]
s_waitcnt lgkmcnt(8)
ds_read_b64_tr_b16 a[222:223],v1 offset:9232
v_lshl_add_u64 v[34:35],v[48:49],0,v[50:51]
v_lshlrev_b32_sdwa v40,1,v40 dst_sel:DWORD dst_unused:UNUSED_PAD src0_sel:DWORD src1_sel:WORD_1
v_mfma_f32_16x16x32_bf16 v[44:47],a[200:203],a[44:47],v[44:47]
v_exp_f16 v8,v9
v_mfma_f32_16x16x32_bf16 v[48:51],a[212:215],a[44:47],v[20:23]
ds_read_u16 v40,v40
v_lshlrev_b32_sdwa v42,1,v42 dst_sel:DWORD dst_unused:UNUSED_PAD src0_sel:DWORD src1_sel:WORD_1
v_mfma_f32_16x16x32_bf16 v[48:51],a[208:211],a[28:31],v[48:51]
ds_read_u16_d16_hi v42,v42
v_lshlrev_b32_sdwa v41,1,v41 dst_sel:DWORD dst_unused:UNUSED_PAD src0_sel:DWORD src1_sel:WORD_1
v_mfma_f32_16x16x32_bf16 v[48:51],a[204:207],a[12:15],v[48:51]
v_exp_f16_sdwa v8,v9 dst_sel:WORD_1 dst_unused:UNUSED_PRESERVE src0_sel:WORD_1
v_mfma_f32_16x16x32_bf16 v[208:211],a[240:243],v[60:63],v[208:211]
ds_read_u16 v41,v41
v_lshlrev_b32_sdwa v43,1,v43 dst_sel:DWORD dst_unused:UNUSED_PAD src0_sel:DWORD src1_sel:WORD_1
v_mfma_f32_16x16x32_bf16 v[212:215],a[240:243],v[32:35],v[212:215]
ds_read_u16_d16_hi v43,v43
v_lshlrev_b32_sdwa v52,1,v52 dst_sel:DWORD dst_unused:UNUSED_PAD src0_sel:DWORD src1_sel:WORD_1
v_mfma_f32_16x16x32_bf16 v[172:175],a[224:227],v[32:35],v[172:175]
ds_read_u16 v52,v52
v_pk_fma_f16 v8,v8,s55,s55
v_mfma_f32_16x16x32_bf16 v[168:171],a[224:227],v[60:63],v[168:171]
ds_read_b64_tr_b16 a[240:241],v3 offset:6224
v_rcp_f16 v9,v8
v_lshlrev_b32_sdwa v54,1,v54 dst_sel:DWORD dst_unused:UNUSED_PAD src0_sel:DWORD src1_sel:WORD_1
v_mfma_f32_16x16x32_bf16 v[216:219],a[228:231],v[60:63],v[216:219]
ds_read_u16_d16_hi v54,v54
v_lshlrev_b32_sdwa v53,1,v53 dst_sel:DWORD dst_unused:UNUSED_PAD src0_sel:DWORD src1_sel:WORD_1
v_mfma_f32_16x16x32_bf16 v[220:223],a[228:231],v[32:35],v[220:223]
ds_read_u16 v53,v53
ds_read_b64_tr_b16 a[242:243],v3 offset:7760
v_mfma_f32_16x16x32_bf16 v[188:191],a[232:235],v[32:35],v[188:191]
ds_read_b64_tr_b16 a[224:225],v3 offset:6232
v_lshlrev_b32_sdwa v55,1,v55 dst_sel:DWORD dst_unused:UNUSED_PAD src0_sel:DWORD src1_sel:WORD_1
v_mfma_f32_16x16x32_bf16 v[184:187],a[232:235],v[60:63],v[184:187]
ds_read_u16_d16_hi v55,v55
v_rcp_f16_sdwa v9,v8 dst_sel:WORD_1 dst_unused:UNUSED_PRESERVE src0_sel:WORD_1
v_mfma_f32_16x16x32_bf16 v[224:227],a[236:239],v[60:63],v[224:227]
ds_read_b64_tr_b16 a[226:227],v3 offset:7768
ds_read_b64_tr_b16 a[228:229],v3 offset:6288
v_lshlrev_b32_sdwa v56,1,v56 dst_sel:DWORD dst_unused:UNUSED_PAD src0_sel:DWORD src1_sel:WORD_1
v_mfma_f32_16x16x32_bf16 v[228:231],a[236:239],v[32:35],v[228:231]
ds_read_u16 v56,v56
v_lshlrev_b32_sdwa v58,1,v58 dst_sel:DWORD dst_unused:UNUSED_PAD src0_sel:DWORD src1_sel:WORD_1
v_mfma_f32_16x16x32_bf16 v[204:207],a[244:247],v[32:35],v[204:207]
ds_read_u16_d16_hi v58,v58
v_fma_mix_f32 v57,v57,v9,0 op_sel_hi:[0,1,0]
v_mfma_f32_16x16x32_bf16 v[200:203],a[244:247],v[60:63],v[200:203]
ds_read_b64_tr_b16 a[230:231],v3 offset:7824
v_fma_mix_f32 v59,v59,v9,0 op_sel:[0,1,0] op_sel_hi:[0,1,0]
v_mfma_f32_16x16x32_bf16 v[32:35],a[204:207],a[48:51],v[20:23]
s_add_u32 m0,s58,0
ds_read_b64 v[14:15],v7 offset:6160
v_lshlrev_b32_sdwa v24,1,v24 dst_sel:DWORD dst_unused:UNUSED_PAD src0_sel:DWORD src1_sel:WORD_1
v_mfma_f32_16x16x32_bf16 v[32:35],a[208:211],a[64:67],v[32:35]
s_waitcnt lgkmcnt(10)
ds_read_b64_tr_b16 a[232:233],v3 offset:6296
v_lshl_add_u64 v[36:37],v[36:37],0,v[38:39]
v_mfma_f32_16x16x32_bf16 v[32:35],a[212:215],a[80:83],v[32:35]
s_mov_b32 s69,s87
ds_read_b64_tr_b16 a[234:235],v3 offset:7832
v_lshl_add_u64 v[38:39],v[40:41],0,v[42:43]
v_lshlrev_b32_sdwa v26,1,v26 dst_sel:DWORD dst_unused:UNUSED_PAD src0_sel:DWORD src1_sel:WORD_1
v_mfma_f32_16x16x32_bf16 v[60:63],a[200:203],a[80:83],v[16:19]
buffer_load_dwordx4 v5,s[44:47],s69 offen offset:0 lds
v_lshlrev_b32_sdwa v25,1,v25 dst_sel:DWORD dst_unused:UNUSED_PAD src0_sel:DWORD src1_sel:WORD_1
v_mfma_f32_16x16x32_bf16 v[60:63],a[196:199],a[64:67],v[60:63]
s_waitcnt lgkmcnt(8)
ds_read_u16 v24,v24
v_lshl_add_u64 v[40:41],v[52:53],0,v[54:55]
v_mfma_f32_16x16x32_bf16 v[60:63],a[192:195],a[48:51],v[60:63]
s_waitcnt lgkmcnt(3)
ds_read_u16_d16_hi v26,v26
v_or3_b32 v13,v14,v15,v13
v_lshlrev_b32_sdwa v27,1,v27 dst_sel:DWORD dst_unused:UNUSED_PAD src0_sel:DWORD src1_sel:WORD_1
v_mfma_f32_16x16x32_bf16 v[52:55],a[192:195],a[52:55],v[16:19]
s_add_u32 s69,s69,s53
ds_read_b64 v[14:15],v7 offset:6672
v_or_b32 v42,v56,v58
v_lshlrev_b32_sdwa v28,1,v28 dst_sel:DWORD dst_unused:UNUSED_PAD src0_sel:DWORD src1_sel:WORD_1
v_mfma_f32_16x16x32_bf16 v[52:55],a[196:199],a[68:71],v[52:55]
buffer_load_dwordx4 v5,s[44:47],s69 offen offset:1024 lds
v_cvt_pk_bf16_f32 v43,v57,v59
v_lshlrev_b32_sdwa v30,1,v30 dst_sel:DWORD dst_unused:UNUSED_PAD src0_sel:DWORD src1_sel:WORD_1
v_mfma_f32_16x16x32_bf16 v[52:55],a[200:203],a[84:87],v[52:55]
s_add_u32 s69,s69,s53
ds_read_u16 v25,v25
v_lshlrev_b32_sdwa v29,1,v29 dst_sel:DWORD dst_unused:UNUSED_PAD src0_sel:DWORD src1_sel:WORD_1
v_mfma_f32_16x16x32_bf16 v[56:59],a[212:215],a[84:87],v[20:23]
buffer_load_dwordx4 v5,s[44:47],s69 offen offset:2048 lds
ds_read_u16_d16_hi v27,v27
v_mfma_f32_16x16x32_bf16 v[56:59],a[208:211],a[68:71],v[56:59]
ds_read_u16 v28,v28
v_lshlrev_b32_sdwa v31,1,v31 dst_sel:DWORD dst_unused:UNUSED_PAD src0_sel:DWORD src1_sel:WORD_1
v_mfma_f32_16x16x32_bf16 v[56:59],a[204:207],a[52:55],v[56:59]
s_waitcnt lgkmcnt(3)
ds_read_u16_d16_hi v30,v30
v_or3_b32 v13,v14,v15,v13
v_mfma_f32_16x16x32_bf16 v[136:139],a[248:251],v[36:39],v[136:139]
ds_read_b64 v[14:15],v7 offset:7184
v_lshlrev_b32_sdwa v48,1,v48 dst_sel:DWORD dst_unused:UNUSED_PAD src0_sel:DWORD src1_sel:WORD_1
v_mfma_f32_16x16x32_bf16 v[140:143],a[248:251],v[40:43],v[140:143]
ds_read_u16 v29,v29
v_lshlrev_b32_sdwa v50,1,v50 dst_sel:DWORD dst_unused:UNUSED_PAD src0_sel:DWORD src1_sel:WORD_1
v_mfma_f32_16x16x32_bf16 v[68:71],a[252:255],v[40:43],v[68:71]
ds_read_u16_d16_hi v31,v31
v_lshlrev_b32_sdwa v49,1,v49 dst_sel:DWORD dst_unused:UNUSED_PAD src0_sel:DWORD src1_sel:WORD_1
v_mfma_f32_16x16x32_bf16 v[64:67],a[252:255],v[36:39],v[64:67]
ds_read_u16 v48,v48
v_lshlrev_b32_sdwa v51,1,v51 dst_sel:DWORD dst_unused:UNUSED_PAD src0_sel:DWORD src1_sel:WORD_1
v_mfma_f32_16x16x32_bf16 v[144:147],a[240:243],v[36:39],v[144:147]
ds_read_u16_d16_hi v50,v50
v_lshlrev_b32_sdwa v44,1,v44 dst_sel:DWORD dst_unused:UNUSED_PAD src0_sel:DWORD src1_sel:WORD_1
v_mfma_f32_16x16x32_bf16 v[148:151],a[240:243],v[40:43],v[148:151]
s_waitcnt lgkmcnt(4)
ds_read_u16 v49,v49
v_or3_b32 v13,v14,v15,v13
v_mfma_f32_16x16x32_bf16 v[84:87],a[224:227],v[40:43],v[84:87]
ds_read_u16_d16_hi v51,v51
v_lshlrev_b32_sdwa v46,1,v46 dst_sel:DWORD dst_unused:UNUSED_PAD src0_sel:DWORD src1_sel:WORD_1
v_mfma_f32_16x16x32_bf16 v[80:83],a[224:227],v[36:39],v[80:83]
ds_read_u16 v44,v44
v_lshlrev_b32_sdwa v45,1,v45 dst_sel:DWORD dst_unused:UNUSED_PAD src0_sel:DWORD src1_sel:WORD_1
v_mfma_f32_16x16x32_bf16 v[152:155],a[228:231],v[36:39],v[152:155]
ds_read_u16_d16_hi v46,v46
v_lshlrev_b32_sdwa v47,1,v47 dst_sel:DWORD dst_unused:UNUSED_PAD src0_sel:DWORD src1_sel:WORD_1
v_mfma_f32_16x16x32_bf16 v[156:159],a[228:231],v[40:43],v[156:159]
ds_read_u16 v45,v45
v_mfma_f32_16x16x32_bf16 v[100:103],a[232:235],v[40:43],v[100:103]
ds_read_u16_d16_hi v47,v47
v_mfma_f32_16x16x32_bf16 v[96:99],a[232:235],v[36:39],v[96:99]
v_mfma_f32_16x16x32_bf16 v[40:43],a[204:207],a[56:59],v[20:23]
v_lshlrev_b32_sdwa v32,1,v32 dst_sel:DWORD dst_unused:UNUSED_PAD src0_sel:DWORD src1_sel:WORD_1
v_fma_mixlo_f16 v8,v53,s99,0
v_mfma_f32_16x16x32_bf16 v[40:43],a[208:211],a[72:75],v[40:43]
ds_read_u16 v32,v32
v_lshl_add_u64 v[24:25],v[24:25],0,v[26:27]
v_mfma_f32_16x16x32_bf16 v[40:43],a[212:215],a[88:91],v[40:43]
s_waitcnt lgkmcnt(6)
v_lshl_add_u64 v[26:27],v[28:29],0,v[30:31]
v_lshlrev_b32_sdwa v34,1,v34 dst_sel:DWORD dst_unused:UNUSED_PAD src0_sel:DWORD src1_sel:WORD_1
v_mfma_f32_16x16x32_bf16 v[36:39],a[200:203],a[88:91],v[16:19]
ds_read_u16_d16_hi v34,v34
v_fma_mixhi_f16 v8,v55,s99,0
v_mfma_f32_16x16x32_bf16 v[36:39],a[196:199],a[72:75],v[36:39]
s_waitcnt lgkmcnt(5)
v_lshl_add_u64 v[28:29],v[48:49],0,v[50:51]
v_lshlrev_b32_sdwa v33,1,v33 dst_sel:DWORD dst_unused:UNUSED_PAD src0_sel:DWORD src1_sel:WORD_1
v_pk_fma_f16 v9,v8,v8,s54
v_mfma_f32_16x16x32_bf16 v[36:39],a[192:195],a[56:59],v[36:39]
ds_read_u16 v33,v33
v_lshlrev_b32_sdwa v35,1,v35 dst_sel:DWORD dst_unused:UNUSED_PAD src0_sel:DWORD src1_sel:WORD_1
v_mfma_f32_16x16x32_bf16 v[48:51],a[192:195],a[60:63],v[16:19]
ds_read_b128 v[16:19],v4 offset:1280
v_pk_mul_f16 v9,v8,v9
v_mfma_f32_16x16x32_bf16 v[48:51],a[196:199],a[76:79],v[48:51]
s_waitcnt lgkmcnt(4)
ds_read_u16_d16_hi v35,v35
ds_read_b64_tr_b16 a[192:193],v1 offset:10256
v_lshl_add_u64 v[30:31],v[44:45],0,v[46:47]
v_lshlrev_b32_sdwa v60,1,v60 dst_sel:DWORD dst_unused:UNUSED_PAD src0_sel:DWORD src1_sel:WORD_1
v_mfma_f32_16x16x32_bf16 v[48:51],a[200:203],a[92:95],v[48:51]
ds_read_b64_tr_b16 a[194:195],v1 offset:11280
v_exp_f16_e64 v8,v9
v_mfma_f32_16x16x32_bf16 v[44:47],a[212:215],a[92:95],v[20:23]
ds_read_b128 v[20:23],v4 offset:1312
ds_read_u16 v60,v60
v_lshlrev_b32_sdwa v62,1,v62 dst_sel:DWORD dst_unused:UNUSED_PAD src0_sel:DWORD src1_sel:WORD_1
v_mfma_f32_16x16x32_bf16 v[44:47],a[208:211],a[76:79],v[44:47]
ds_read_u16_d16_hi v62,v62
ds_read_b64_tr_b16 a[196:197],v2 offset:10256
v_lshlrev_b32_sdwa v61,1,v61 dst_sel:DWORD dst_unused:UNUSED_PAD src0_sel:DWORD src1_sel:WORD_1
v_mfma_f32_16x16x32_bf16 v[44:47],a[204:207],a[60:63],v[44:47]
ds_read_u16 v61,v61
v_exp_f16_sdwa v8,v9 dst_sel:WORD_1 dst_unused:UNUSED_PRESERVE src0_sel:WORD_1
v_mfma_f32_16x16x32_bf16 v[112:115],a[248:251],v[24:27],v[112:115]
ds_read_b64_tr_b16 a[198:199],v2 offset:11280
ds_read_b64_tr_b16 a[200:201],v2 offset:8208
v_lshlrev_b32_sdwa v63,1,v63 dst_sel:DWORD dst_unused:UNUSED_PAD src0_sel:DWORD src1_sel:WORD_1
v_mfma_f32_16x16x32_bf16 v[116:119],a[248:251],v[28:31],v[116:119]
ds_read_u16_d16_hi v63,v63
v_lshlrev_b32_sdwa v56,1,v56 dst_sel:DWORD dst_unused:UNUSED_PAD src0_sel:DWORD src1_sel:WORD_1
v_mfma_f32_16x16x32_bf16 v[76:79],a[252:255],v[28:31],v[76:79]
ds_read_b64_tr_b16 a[202:203],v2 offset:9232
v_pk_fma_f16 v8,v8,s55,s55
v_mfma_f32_16x16x32_bf16 v[72:75],a[252:255],v[24:27],v[72:75]
v_rcp_f16 v9,v8
v_lshlrev_b32_sdwa v58,1,v58 dst_sel:DWORD dst_unused:UNUSED_PAD src0_sel:DWORD src1_sel:WORD_1
v_mfma_f32_16x16x32_bf16 v[120:123],a[240:243],v[24:27],v[120:123]
ds_read_u16 v56,v56
v_lshlrev_b32_sdwa v57,1,v57 dst_sel:DWORD dst_unused:UNUSED_PAD src0_sel:DWORD src1_sel:WORD_1
v_mfma_f32_16x16x32_bf16 v[124:127],a[240:243],v[28:31],v[124:127]
ds_read_u16_d16_hi v58,v58
v_lshlrev_b32_sdwa v59,1,v59 dst_sel:DWORD dst_unused:UNUSED_PAD src0_sel:DWORD src1_sel:WORD_1
v_mfma_f32_16x16x32_bf16 v[92:95],a[224:227],v[28:31],v[92:95]
ds_read_u16 v57,v57
v_lshlrev_b32_sdwa v52,1,v52 dst_sel:DWORD dst_unused:UNUSED_PAD src0_sel:DWORD src1_sel:WORD_1
v_mfma_f32_16x16x32_bf16 v[88:91],a[224:227],v[24:27],v[88:91]
ds_read_u16_d16_hi v59,v59
v_rcp_f16_sdwa v9,v8 dst_sel:WORD_1 dst_unused:UNUSED_PRESERVE src0_sel:WORD_1
v_mfma_f32_16x16x32_bf16 v[128:131],a[228:231],v[24:27],v[128:131]
ds_read_b64_tr_b16 a[212:213],v2 offset:6160
v_lshlrev_b32_sdwa v54,1,v54 dst_sel:DWORD dst_unused:UNUSED_PAD src0_sel:DWORD src1_sel:WORD_1
v_mfma_f32_16x16x32_bf16 v[132:135],a[228:231],v[28:31],v[132:135]
ds_read_b64_tr_b16 a[214:215],v2 offset:7184
v_fma_mix_f32 v53,v53,v9,0 op_sel_hi:[0,1,0]
v_mfma_f32_16x16x32_bf16 v[108:111],a[232:235],v[28:31],v[108:111]
ds_read_u16 v52,v52
v_fma_mix_f32 v55,v55,v9,0 op_sel:[0,1,0] op_sel_hi:[0,1,0]
v_mfma_f32_16x16x32_bf16 v[104:107],a[232:235],v[24:27],v[104:107]
s_waitcnt lgkmcnt(7)
ds_read_u16_d16_hi v54,v54
v_mfma_f32_16x16x32_bf16 v[28:31],a[216:219],a[0:3],v[16:19]
v_lshlrev_b32_sdwa v40,1,v40 dst_sel:DWORD dst_unused:UNUSED_PAD src0_sel:DWORD src1_sel:WORD_1
v_lshlrev_b32_sdwa v42,1,v42 dst_sel:DWORD dst_unused:UNUSED_PAD src0_sel:DWORD src1_sel:WORD_1
v_mfma_f32_16x16x32_bf16 v[28:31],a[220:223],a[16:19],v[28:31]
ds_read_u16 v40,v40
v_lshl_add_u64 v[32:33],v[32:33],0,v[34:35]
v_mfma_f32_16x16x32_bf16 v[28:31],a[192:195],a[32:35],v[28:31]
ds_read_u16_d16_hi v42,v42
v_lshl_add_u64 v[34:35],v[60:61],0,v[62:63]
v_mfma_f32_16x16x32_bf16 v[24:27],a[196:199],a[32:35],v[20:23]
v_lshlrev_b32_sdwa v41,1,v41 dst_sel:DWORD dst_unused:UNUSED_PAD src0_sel:DWORD src1_sel:WORD_1
v_lshlrev_b32_sdwa v43,1,v43 dst_sel:DWORD dst_unused:UNUSED_PAD src0_sel:DWORD src1_sel:WORD_1
v_mfma_f32_16x16x32_bf16 v[24:27],a[200:203],a[16:19],v[24:27]
s_waitcnt lgkmcnt(4)
ds_read_u16 v41,v41
v_lshl_add_u64 v[60:61],v[56:57],0,v[58:59]
v_mfma_f32_16x16x32_bf16 v[24:27],a[212:215],a[0:3],v[24:27]
ds_read_u16_d16_hi v43,v43
v_lshlrev_b32_sdwa v36,1,v36 dst_sel:DWORD dst_unused:UNUSED_PAD src0_sel:DWORD src1_sel:WORD_1
v_mfma_f32_16x16x32_bf16 v[56:59],a[212:215],a[4:7],v[20:23]
s_waitcnt lgkmcnt(4)
ds_read_u16 v36,v36
v_or_b32 v62,v52,v54
v_mfma_f32_16x16x32_bf16 v[56:59],a[200:203],a[20:23],v[56:59]
v_cvt_pk_bf16_f32 v63,v53,v55
v_lshlrev_b32_sdwa v38,1,v38 dst_sel:DWORD dst_unused:UNUSED_PAD src0_sel:DWORD src1_sel:WORD_1
v_mfma_f32_16x16x32_bf16 v[56:59],a[196:199],a[36:39],v[56:59]
ds_read_u16_d16_hi v38,v38
v_lshlrev_b32_sdwa v37,1,v37 dst_sel:DWORD dst_unused:UNUSED_PAD src0_sel:DWORD src1_sel:WORD_1
v_mfma_f32_16x16x32_bf16 v[52:55],a[192:195],a[36:39],v[16:19]
ds_read_u16 v37,v37
v_lshlrev_b32_sdwa v39,1,v39 dst_sel:DWORD dst_unused:UNUSED_PAD src0_sel:DWORD src1_sel:WORD_1
v_mfma_f32_16x16x32_bf16 v[52:55],a[220:223],a[20:23],v[52:55]
ds_read_u16_d16_hi v39,v39
v_lshlrev_b32_sdwa v44,1,v44 dst_sel:DWORD dst_unused:UNUSED_PAD src0_sel:DWORD src1_sel:WORD_1
v_mfma_f32_16x16x32_bf16 v[52:55],a[216:219],a[4:7],v[52:55]
ds_read_u16 v44,v44
v_lshlrev_b32_sdwa v46,1,v46 dst_sel:DWORD dst_unused:UNUSED_PAD src0_sel:DWORD src1_sel:WORD_1
v_mfma_f32_16x16x32_bf16 v[232:235],a[248:251],v[32:35],v[232:235]
ds_read_u16_d16_hi v46,v46
v_lshlrev_b32_sdwa v45,1,v45 dst_sel:DWORD dst_unused:UNUSED_PAD src0_sel:DWORD src1_sel:WORD_1
v_mfma_f32_16x16x32_bf16 v[236:239],a[248:251],v[60:63],v[236:239]
ds_read_u16 v45,v45
v_lshlrev_b32_sdwa v47,1,v47 dst_sel:DWORD dst_unused:UNUSED_PAD src0_sel:DWORD src1_sel:WORD_1
v_mfma_f32_16x16x32_bf16 v[164:167],a[252:255],v[60:63],v[164:167]
ds_read_u16_d16_hi v47,v47
ds_read_b64_tr_b16 a[236:237],v3 offset:12320
v_mfma_f32_16x16x32_bf16 v[160:163],a[252:255],v[32:35],v[160:163]
ds_read_b64_tr_b16 a[238:239],v3 offset:13856
v_lshlrev_b32_sdwa v48,1,v48 dst_sel:DWORD dst_unused:UNUSED_PAD src0_sel:DWORD src1_sel:WORD_1
v_mfma_f32_16x16x32_bf16 v[240:243],a[240:243],v[32:35],v[240:243]
ds_read_u16 v48,v48
v_lshlrev_b32_sdwa v50,1,v50 dst_sel:DWORD dst_unused:UNUSED_PAD src0_sel:DWORD src1_sel:WORD_1
v_mfma_f32_16x16x32_bf16 v[244:247],a[240:243],v[60:63],v[244:247]
ds_read_u16_d16_hi v50,v50
v_lshlrev_b32_sdwa v49,1,v49 dst_sel:DWORD dst_unused:UNUSED_PAD src0_sel:DWORD src1_sel:WORD_1
v_mfma_f32_16x16x32_bf16 v[180:183],a[224:227],v[60:63],v[180:183]
ds_read_u16 v49,v49
v_lshlrev_b32_sdwa v51,1,v51 dst_sel:DWORD dst_unused:UNUSED_PAD src0_sel:DWORD src1_sel:WORD_1
v_mfma_f32_16x16x32_bf16 v[176:179],a[224:227],v[32:35],v[176:179]
ds_read_u16_d16_hi v51,v51
v_mfma_f32_16x16x32_bf16 v[248:251],a[228:231],v[32:35],v[248:251]
ds_read_b64_tr_b16 a[244:245],v3 offset:12328
v_mfma_f32_16x16x32_bf16 v[252:255],a[228:231],v[60:63],v[252:255]
ds_read_b64_tr_b16 a[246:247],v3 offset:13864
v_mfma_f32_16x16x32_bf16 v[196:199],a[232:235],v[60:63],v[196:199]
v_mfma_f32_16x16x32_bf16 v[192:195],a[232:235],v[32:35],v[192:195]
v_mfma_f32_16x16x32_bf16 v[60:63],a[216:219],a[8:11],v[16:19]
ds_read_b64_tr_b16 a[208:209],v1 offset:12320
v_lshlrev_b32_sdwa v28,1,v28 dst_sel:DWORD dst_unused:UNUSED_PAD src0_sel:DWORD src1_sel:WORD_1
v_mfma_f32_16x16x32_bf16 v[60:63],a[220:223],a[24:27],v[60:63]
s_waitcnt lgkmcnt(6)
ds_read_u16 v28,v28
v_lshl_add_u64 v[40:41],v[40:41],0,v[42:43]
v_fma_mixlo_f16 v8,v57,s99,0
v_mfma_f32_16x16x32_bf16 v[60:63],a[192:195],a[40:43],v[60:63]
ds_read_b64_tr_b16 a[210:211],v1 offset:13344
v_lshl_add_u64 v[42:43],v[36:37],0,v[38:39]
v_lshlrev_b32_sdwa v30,1,v30 dst_sel:DWORD dst_unused:UNUSED_PAD src0_sel:DWORD src1_sel:WORD_1
v_mfma_f32_16x16x32_bf16 v[32:35],a[196:199],a[40:43],v[20:23]
ds_read_u16_d16_hi v30,v30
v_fma_mixhi_f16 v8,v59,s99,0
v_mfma_f32_16x16x32_bf16 v[32:35],a[200:203],a[24:27],v[32:35]
ds_read_b64_tr_b16 a[204:205],v1 offset:14368
v_lshl_add_u64 v[36:37],v[44:45],0,v[46:47]
v_lshlrev_b32_sdwa v29,1,v29 dst_sel:DWORD dst_unused:UNUSED_PAD src0_sel:DWORD src1_sel:WORD_1
v_pk_fma_f16 v9,v8,v8,s54
v_mfma_f32_16x16x32_bf16 v[32:35],a[212:215],a[8:11],v[32:35]
ds_read_u16 v29,v29
v_lshlrev_b32_sdwa v31,1,v31 dst_sel:DWORD dst_unused:UNUSED_PAD src0_sel:DWORD src1_sel:WORD_1
v_mfma_f32_16x16x32_bf16 v[44:47],a[212:215],a[12:15],v[20:23]
ds_read_u16_d16_hi v31,v31
v_pk_mul_f16 v9,v8,v9
v_mfma_f32_16x16x32_bf16 v[44:47],a[200:203],a[28:31],v[44:47]
s_waitcnt lgkmcnt(7)
ds_read_b64_tr_b16 a[206:207],v1 offset:15392
v_lshl_add_u64 v[38:39],v[48:49],0,v[50:51]
v_lshlrev_b32_sdwa v24,1,v24 dst_sel:DWORD dst_unused:UNUSED_PAD src0_sel:DWORD src1_sel:WORD_1
v_mfma_f32_16x16x32_bf16 v[44:47],a[196:199],a[44:47],v[44:47]
v_exp_f16 v8,v9
v_mfma_f32_16x16x32_bf16 v[48:51],a[192:195],a[44:47],v[16:19]
ds_read_u16 v24,v24
v_lshlrev_b32_sdwa v26,1,v26 dst_sel:DWORD dst_unused:UNUSED_PAD src0_sel:DWORD src1_sel:WORD_1
v_mfma_f32_16x16x32_bf16 v[48:51],a[220:223],a[28:31],v[48:51]
ds_read_u16_d16_hi v26,v26
v_lshlrev_b32_sdwa v25,1,v25 dst_sel:DWORD dst_unused:UNUSED_PAD src0_sel:DWORD src1_sel:WORD_1
v_mfma_f32_16x16x32_bf16 v[48:51],a[216:219],a[12:15],v[48:51]
v_exp_f16_sdwa v8,v9 dst_sel:WORD_1 dst_unused:UNUSED_PRESERVE src0_sel:WORD_1
v_mfma_f32_16x16x32_bf16 v[208:211],a[248:251],v[40:43],v[208:211]
ds_read_u16 v25,v25
v_lshlrev_b32_sdwa v27,1,v27 dst_sel:DWORD dst_unused:UNUSED_PAD src0_sel:DWORD src1_sel:WORD_1
v_mfma_f32_16x16x32_bf16 v[212:215],a[248:251],v[36:39],v[212:215]
TP4345_%=:
s_cbranch_execz TP6487_%=
ds_read_u16_d16_hi v27,v27
v_lshlrev_b32_sdwa v52,1,v52 dst_sel:DWORD dst_unused:UNUSED_PAD src0_sel:DWORD src1_sel:WORD_1
v_mfma_f32_16x16x32_bf16 v[172:175],a[252:255],v[36:39],v[172:175]
ds_read_u16 v52,v52
v_pk_fma_f16 v8,v8,s55,s55
v_mfma_f32_16x16x32_bf16 v[168:171],a[252:255],v[40:43],v[168:171]
ds_read_b64_tr_b16 a[248:249],v3 offset:12384
v_rcp_f16 v9,v8
v_lshlrev_b32_sdwa v54,1,v54 dst_sel:DWORD dst_unused:UNUSED_PAD src0_sel:DWORD src1_sel:WORD_1
v_mfma_f32_16x16x32_bf16 v[216:219],a[240:243],v[40:43],v[216:219]
ds_read_u16_d16_hi v54,v54
v_lshlrev_b32_sdwa v53,1,v53 dst_sel:DWORD dst_unused:UNUSED_PAD src0_sel:DWORD src1_sel:WORD_1
v_mfma_f32_16x16x32_bf16 v[220:223],a[240:243],v[36:39],v[220:223]
ds_read_u16 v53,v53
ds_read_b64_tr_b16 a[250:251],v3 offset:13920
v_mfma_f32_16x16x32_bf16 v[188:191],a[224:227],v[36:39],v[188:191]
ds_read_b64_tr_b16 a[252:253],v3 offset:12392
v_lshlrev_b32_sdwa v55,1,v55 dst_sel:DWORD dst_unused:UNUSED_PAD src0_sel:DWORD src1_sel:WORD_1
v_mfma_f32_16x16x32_bf16 v[184:187],a[224:227],v[40:43],v[184:187]
ds_read_u16_d16_hi v55,v55
v_rcp_f16_sdwa v9,v8 dst_sel:WORD_1 dst_unused:UNUSED_PRESERVE src0_sel:WORD_1
v_mfma_f32_16x16x32_bf16 v[224:227],a[228:231],v[40:43],v[224:227]
ds_read_b64_tr_b16 a[254:255],v3 offset:13928
ds_read_b64_tr_b16 a[240:241],v3 offset:12448
v_lshlrev_b32_sdwa v56,1,v56 dst_sel:DWORD dst_unused:UNUSED_PAD src0_sel:DWORD src1_sel:WORD_1
v_mfma_f32_16x16x32_bf16 v[228:231],a[228:231],v[36:39],v[228:231]
ds_read_u16 v56,v56
v_lshlrev_b32_sdwa v58,1,v58 dst_sel:DWORD dst_unused:UNUSED_PAD src0_sel:DWORD src1_sel:WORD_1
v_mfma_f32_16x16x32_bf16 v[204:207],a[232:235],v[36:39],v[204:207]
ds_read_u16_d16_hi v58,v58
v_fma_mix_f32 v57,v57,v9,0 op_sel_hi:[0,1,0]
v_mfma_f32_16x16x32_bf16 v[200:203],a[232:235],v[40:43],v[200:203]
ds_read_b64_tr_b16 a[242:243],v3 offset:13984
v_fma_mix_f32 v59,v59,v9,0 op_sel:[0,1,0] op_sel_hi:[0,1,0]
v_mbcnt_lo_u32_b32 v8,-1,0
N_qs10_%=:
s_add_u32 s64,s16,N_qs10_%=-N_pf_%=+0
v_and_b32_e64 v8,3,v8
s_mov_b32 m0,12288
v_lshl_add_u32 v8,v8,7,s64
v_min_u32 v8,N_mainend_%=-N_pf_%=+2048,v8
global_load_lds_dword v8,s[14:15]
v_mfma_f32_16x16x32_bf16 v[36:39],a[216:219],a[48:51],v[16:19]
ds_read_b64 v[14:15],v7 offset:12320
v_lshlrev_b32_sdwa v60,1,v60 dst_sel:DWORD dst_unused:UNUSED_PAD src0_sel:DWORD src1_sel:WORD_1
v_mfma_f32_16x16x32_bf16 v[36:39],a[220:223],a[64:67],v[36:39]
s_waitcnt lgkmcnt(10)
ds_read_b64_tr_b16 a[224:225],v3 offset:12456
v_lshl_add_u64 v[28:29],v[28:29],0,v[30:31]
v_mfma_f32_16x16x32_bf16 v[36:39],a[192:195],a[80:83],v[36:39]
ds_read_b64_tr_b16 a[226:227],v3 offset:13992
v_lshl_add_u64 v[30:31],v[24:25],0,v[26:27]
v_lshlrev_b32_sdwa v62,1,v62 dst_sel:DWORD dst_unused:UNUSED_PAD src0_sel:DWORD src1_sel:WORD_1
v_mfma_f32_16x16x32_bf16 v[40:43],a[196:199],a[80:83],v[20:23]
ds_read_u16 v60,v60
v_lshlrev_b32_sdwa v61,1,v61 dst_sel:DWORD dst_unused:UNUSED_PAD src0_sel:DWORD src1_sel:WORD_1
v_mfma_f32_16x16x32_bf16 v[40:43],a[200:203],a[64:67],v[40:43]
s_waitcnt lgkmcnt(9)
ds_read_u16_d16_hi v62,v62
v_lshl_add_u64 v[24:25],v[52:53],0,v[54:55]
v_mfma_f32_16x16x32_bf16 v[40:43],a[212:215],a[48:51],v[40:43]
s_waitcnt lgkmcnt(4)
ds_read_u16 v61,v61
v_or3_b32 v13,v14,v15,v13
v_lshlrev_b32_sdwa v63,1,v63 dst_sel:DWORD dst_unused:UNUSED_PAD src0_sel:DWORD src1_sel:WORD_1
v_mfma_f32_16x16x32_bf16 v[52:55],a[212:215],a[52:55],v[20:23]
ds_read_b64 v[14:15],v7 offset:12832
v_or_b32 v26,v56,v58
v_lshlrev_b32_sdwa v32,1,v32 dst_sel:DWORD dst_unused:UNUSED_PAD src0_sel:DWORD src1_sel:WORD_1
v_mfma_f32_16x16x32_bf16 v[52:55],a[200:203],a[68:71],v[52:55]
ds_read_u16_d16_hi v63,v63
v_cvt_pk_bf16_f32 v27,v57,v59
v_lshlrev_b32_sdwa v34,1,v34 dst_sel:DWORD dst_unused:UNUSED_PAD src0_sel:DWORD src1_sel:WORD_1
v_mfma_f32_16x16x32_bf16 v[52:55],a[196:199],a[84:87],v[52:55]
ds_read_u16 v32,v32
v_lshlrev_b32_sdwa v33,1,v33 dst_sel:DWORD dst_unused:UNUSED_PAD src0_sel:DWORD src1_sel:WORD_1
v_mfma_f32_16x16x32_bf16 v[56:59],a[192:195],a[84:87],v[16:19]
ds_read_u16_d16_hi v34,v34
v_lshlrev_b32_sdwa v35,1,v35 dst_sel:DWORD dst_unused:UNUSED_PAD src0_sel:DWORD src1_sel:WORD_1
v_mfma_f32_16x16x32_bf16 v[56:59],a[220:223],a[68:71],v[56:59]
ds_read_u16 v33,v33
v_lshlrev_b32_sdwa v48,1,v48 dst_sel:DWORD dst_unused:UNUSED_PAD src0_sel:DWORD src1_sel:WORD_1
v_mfma_f32_16x16x32_bf16 v[56:59],a[216:219],a[52:55],v[56:59]
s_waitcnt lgkmcnt(4)
ds_read_u16_d16_hi v35,v35
v_or3_b32 v13,v14,v15,v13
v_mfma_f32_16x16x32_bf16 v[136:139],a[236:239],v[28:31],v[136:139]
ds_read_b64 v[14:15],v7 offset:13344
v_lshlrev_b32_sdwa v50,1,v50 dst_sel:DWORD dst_unused:UNUSED_PAD src0_sel:DWORD src1_sel:WORD_1
v_mfma_f32_16x16x32_bf16 v[140:143],a[236:239],v[24:27],v[140:143]
ds_read_u16 v48,v48
v_lshlrev_b32_sdwa v49,1,v49 dst_sel:DWORD dst_unused:UNUSED_PAD src0_sel:DWORD src1_sel:WORD_1
v_mfma_f32_16x16x32_bf16 v[68:71],a[244:247],v[24:27],v[68:71]
ds_read_u16_d16_hi v50,v50
v_lshlrev_b32_sdwa v51,1,v51 dst_sel:DWORD dst_unused:UNUSED_PAD src0_sel:DWORD src1_sel:WORD_1
v_mfma_f32_16x16x32_bf16 v[64:67],a[244:247],v[28:31],v[64:67]
ds_read_u16 v49,v49
v_lshlrev_b32_sdwa v44,1,v44 dst_sel:DWORD dst_unused:UNUSED_PAD src0_sel:DWORD src1_sel:WORD_1
v_mfma_f32_16x16x32_bf16 v[144:147],a[248:251],v[28:31],v[144:147]
ds_read_u16_d16_hi v51,v51
v_lshlrev_b32_sdwa v46,1,v46 dst_sel:DWORD dst_unused:UNUSED_PAD src0_sel:DWORD src1_sel:WORD_1
v_mfma_f32_16x16x32_bf16 v[148:151],a[248:251],v[24:27],v[148:151]
s_waitcnt lgkmcnt(4)
ds_read_u16 v44,v44
v_or3_b32 v13,v14,v15,v13
v_mfma_f32_16x16x32_bf16 v[84:87],a[252:255],v[24:27],v[84:87]
ds_read_u16_d16_hi v46,v46
v_and_b32 v13,0x40004000,v13
v_mfma_f32_16x16x32_bf16 v[80:83],a[252:255],v[28:31],v[80:83]
v_cmp_ne_u32_e32 vcc,0,v13
s_nop 0
s_or_b64 s[90:91],s[90:91],vcc
v_lshlrev_b32_sdwa v45,1,v45 dst_sel:DWORD dst_unused:UNUSED_PAD src0_sel:DWORD src1_sel:WORD_1
v_mfma_f32_16x16x32_bf16 v[152:155],a[240:243],v[28:31],v[152:155]
ds_read_u16 v45,v45
v_mbcnt_lo_u32_b32 v15,-1,0
v_mfma_f32_16x16x32_bf16 v[156:159],a[240:243],v[24:27],v[156:159]
v_mbcnt_hi_u32_b32 v15,-1,v15
v_lshlrev_b32_sdwa v47,1,v47 dst_sel:DWORD dst_unused:UNUSED_PAD src0_sel:DWORD src1_sel:WORD_1
v_mfma_f32_16x16x32_bf16 v[100:103],a[224:227],v[24:27],v[100:103]
ds_read_u16_d16_hi v47,v47
v_lshlrev_b32 v15,2,v15
v_mfma_f32_16x16x32_bf16 v[96:99],a[224:227],v[28:31],v[96:99]
ds_read_b32 v7,v15 offset:14848
ds_read_b32 v13,v15 offset:15104
v_mfma_f32_16x16x32_bf16 v[24:27],a[216:219],a[56:59],v[16:19]
ds_read_b32 v14,v15 offset:14336
ds_read_b32 v15,v15 offset:14592
v_lshlrev_b32_sdwa v36,1,v36 dst_sel:DWORD dst_unused:UNUSED_PAD src0_sel:DWORD src1_sel:WORD_1
v_mfma_f32_16x16x32_bf16 v[24:27],a[220:223],a[72:75],v[24:27]
ds_read_u16 v36,v36
v_lshl_add_u64 v[60:61],v[60:61],0,v[62:63]
v_fma_mixlo_f16 v8,v53,s99,0
v_mfma_f32_16x16x32_bf16 v[24:27],a[192:195],a[88:91],v[24:27]
v_lshl_add_u64 v[62:63],v[32:33],0,v[34:35]
v_lshlrev_b32_sdwa v38,1,v38 dst_sel:DWORD dst_unused:UNUSED_PAD src0_sel:DWORD src1_sel:WORD_1
v_mfma_f32_16x16x32_bf16 v[28:31],a[196:199],a[88:91],v[20:23]
ds_read_u16_d16_hi v38,v38
v_fma_mixhi_f16 v8,v55,s99,0
v_mfma_f32_16x16x32_bf16 v[28:31],a[200:203],a[72:75],v[28:31]
s_waitcnt lgkmcnt(8)
v_lshl_add_u64 v[32:33],v[48:49],0,v[50:51]
v_lshlrev_b32_sdwa v37,1,v37 dst_sel:DWORD dst_unused:UNUSED_PAD src0_sel:DWORD src1_sel:WORD_1
v_pk_fma_f16 v9,v8,v8,s54
v_mfma_f32_16x16x32_bf16 v[28:31],a[212:215],a[56:59],v[28:31]
ds_read_u16 v37,v37
v_lshlrev_b32_sdwa v39,1,v39 dst_sel:DWORD dst_unused:UNUSED_PAD src0_sel:DWORD src1_sel:WORD_1
v_mfma_f32_16x16x32_bf16 v[48:51],a[212:215],a[60:63],v[20:23]
ds_read_b128 v[20:23],v4 offset:1408
v_pk_mul_f16 v9,v8,v9
v_mfma_f32_16x16x32_bf16 v[48:51],a[200:203],a[76:79],v[48:51]
s_waitcnt lgkmcnt(8)
ds_read_u16_d16_hi v39,v39
ds_read_b64_tr_b16 a[212:213],v1 offset:16416
v_lshl_add_u64 v[34:35],v[44:45],0,v[46:47]
v_lshlrev_b32_sdwa v40,1,v40 dst_sel:DWORD dst_unused:UNUSED_PAD src0_sel:DWORD src1_sel:WORD_1
v_mfma_f32_16x16x32_bf16 v[48:51],a[196:199],a[92:95],v[48:51]
ds_read_b64_tr_b16 a[214:215],v1 offset:17440
v_exp_f16 v8,v9
v_mfma_f32_16x16x32_bf16 v[44:47],a[192:195],a[92:95],v[16:19]
ds_read_b128 v[16:19],v4 offset:1440
ds_read_u16 v40,v40
v_lshlrev_b32_sdwa v42,1,v42 dst_sel:DWORD dst_unused:UNUSED_PAD src0_sel:DWORD src1_sel:WORD_1
v_mfma_f32_16x16x32_bf16 v[44:47],a[220:223],a[76:79],v[44:47]
ds_read_u16_d16_hi v42,v42
ds_read_b64_tr_b16 a[200:201],v2 offset:16416
v_lshlrev_b32_sdwa v41,1,v41 dst_sel:DWORD dst_unused:UNUSED_PAD src0_sel:DWORD src1_sel:WORD_1
v_mfma_f32_16x16x32_bf16 v[44:47],a[216:219],a[60:63],v[44:47]
ds_read_u16 v41,v41
v_exp_f16_sdwa v8,v9 dst_sel:WORD_1 dst_unused:UNUSED_PRESERVE src0_sel:WORD_1
v_mfma_f32_16x16x32_bf16 v[112:115],a[236:239],v[60:63],v[112:115]
ds_read_b64_tr_b16 a[202:203],v2 offset:17440
ds_read_b64_tr_b16 a[196:197],v2 offset:14368
v_lshlrev_b32_sdwa v43,1,v43 dst_sel:DWORD dst_unused:UNUSED_PAD src0_sel:DWORD src1_sel:WORD_1
v_mfma_f32_16x16x32_bf16 v[116:119],a[236:239],v[32:35],v[116:119]
ds_read_u16_d16_hi v43,v43
v_lshlrev_b32_sdwa v56,1,v56 dst_sel:DWORD dst_unused:UNUSED_PAD src0_sel:DWORD src1_sel:WORD_1
v_mfma_f32_16x16x32_bf16 v[76:79],a[244:247],v[32:35],v[76:79]
ds_read_b64_tr_b16 a[198:199],v2 offset:15392
v_pk_fma_f16 v8,v8,s55,s55
v_mfma_f32_16x16x32_bf16 v[72:75],a[244:247],v[60:63],v[72:75]
v_rcp_f16 v9,v8
v_lshlrev_b32_sdwa v58,1,v58 dst_sel:DWORD dst_unused:UNUSED_PAD src0_sel:DWORD src1_sel:WORD_1
v_mfma_f32_16x16x32_bf16 v[120:123],a[248:251],v[60:63],v[120:123]
ds_read_u16 v56,v56
v_lshlrev_b32_sdwa v57,1,v57 dst_sel:DWORD dst_unused:UNUSED_PAD src0_sel:DWORD src1_sel:WORD_1
v_mfma_f32_16x16x32_bf16 v[124:127],a[248:251],v[32:35],v[124:127]
ds_read_u16_d16_hi v58,v58
v_lshlrev_b32_sdwa v59,1,v59 dst_sel:DWORD dst_unused:UNUSED_PAD src0_sel:DWORD src1_sel:WORD_1
v_mfma_f32_16x16x32_bf16 v[92:95],a[252:255],v[32:35],v[92:95]
ds_read_u16 v57,v57
v_lshlrev_b32_sdwa v52,1,v52 dst_sel:DWORD dst_unused:UNUSED_PAD src0_sel:DWORD src1_sel:WORD_1
v_mfma_f32_16x16x32_bf16 v[88:91],a[252:255],v[60:63],v[88:91]
ds_read_u16_d16_hi v59,v59
v_rcp_f16_sdwa v9,v8 dst_sel:WORD_1 dst_unused:UNUSED_PRESERVE src0_sel:WORD_1
v_mfma_f32_16x16x32_bf16 v[128:131],a[240:243],v[60:63],v[128:131]
ds_read_b64_tr_b16 a[192:193],v2 offset:12320
v_lshlrev_b32_sdwa v54,1,v54 dst_sel:DWORD dst_unused:UNUSED_PAD src0_sel:DWORD src1_sel:WORD_1
v_mfma_f32_16x16x32_bf16 v[132:135],a[240:243],v[32:35],v[132:135]
ds_read_b64_tr_b16 a[194:195],v2 offset:13344
v_fma_mix_f32 v53,v53,v9,0 op_sel_hi:[0,1,0]
v_mfma_f32_16x16x32_bf16 v[108:111],a[224:227],v[32:35],v[108:111]
ds_read_u16 v52,v52
v_fma_mix_f32 v55,v55,v9,0 op_sel:[0,1,0] op_sel_hi:[0,1,0]
v_mfma_f32_16x16x32_bf16 v[104:107],a[224:227],v[60:63],v[104:107]
s_waitcnt lgkmcnt(7)
ds_read_u16_d16_hi v54,v54
v_mfma_f32_16x16x32_bf16 v[32:35],a[208:211],a[0:3],v[20:23]
v_lshlrev_b32_sdwa v24,1,v24 dst_sel:DWORD dst_unused:UNUSED_PAD src0_sel:DWORD src1_sel:WORD_1
v_lshlrev_b32_sdwa v26,1,v26 dst_sel:DWORD dst_unused:UNUSED_PAD src0_sel:DWORD src1_sel:WORD_1
v_mfma_f32_16x16x32_bf16 v[32:35],a[204:207],a[16:19],v[32:35]
ds_read_u16 v24,v24
v_lshl_add_u64 v[36:37],v[36:37],0,v[38:39]
v_mfma_f32_16x16x32_bf16 v[32:35],a[212:215],a[32:35],v[32:35]
ds_read_u16_d16_hi v26,v26
v_lshl_add_u64 v[38:39],v[40:41],0,v[42:43]
v_mfma_f32_16x16x32_bf16 v[60:63],a[200:203],a[32:35],v[16:19]
v_lshlrev_b32_sdwa v25,1,v25 dst_sel:DWORD dst_unused:UNUSED_PAD src0_sel:DWORD src1_sel:WORD_1
v_lshlrev_b32_sdwa v27,1,v27 dst_sel:DWORD dst_unused:UNUSED_PAD src0_sel:DWORD src1_sel:WORD_1
v_mfma_f32_16x16x32_bf16 v[60:63],a[196:199],a[16:19],v[60:63]
s_waitcnt lgkmcnt(4)
ds_read_u16 v25,v25
v_lshl_add_u64 v[40:41],v[56:57],0,v[58:59]
v_mfma_f32_16x16x32_bf16 v[60:63],a[192:195],a[0:3],v[60:63]
ds_read_u16_d16_hi v27,v27
v_lshlrev_b32_sdwa v28,1,v28 dst_sel:DWORD dst_unused:UNUSED_PAD src0_sel:DWORD src1_sel:WORD_1
v_mfma_f32_16x16x32_bf16 v[56:59],a[192:195],a[4:7],v[16:19]
s_waitcnt lgkmcnt(4)
ds_read_u16 v28,v28
v_or_b32 v42,v52,v54
v_mfma_f32_16x16x32_bf16 v[56:59],a[196:199],a[20:23],v[56:59]
s_waitcnt vmcnt(4)
v_cvt_pk_bf16_f32 v43,v53,v55
v_lshlrev_b32_sdwa v30,1,v30 dst_sel:DWORD dst_unused:UNUSED_PAD src0_sel:DWORD src1_sel:WORD_1
v_mfma_f32_16x16x32_bf16 v[56:59],a[200:203],a[36:39],v[56:59]
s_barrier
ds_read_u16_d16_hi v30,v30
v_lshlrev_b32_sdwa v29,1,v29 dst_sel:DWORD dst_unused:UNUSED_PAD src0_sel:DWORD src1_sel:WORD_1
v_mfma_f32_16x16x32_bf16 v[52:55],a[212:215],a[36:39],v[20:23]
ds_read_u16 v29,v29
v_lshlrev_b32_sdwa v31,1,v31 dst_sel:DWORD dst_unused:UNUSED_PAD src0_sel:DWORD src1_sel:WORD_1
v_mfma_f32_16x16x32_bf16 v[52:55],a[204:207],a[20:23],v[52:55]
s_add_u32 m0,s58,6160
ds_read_u16_d16_hi v31,v31
v_lshlrev_b32_sdwa v44,1,v44 dst_sel:DWORD dst_unused:UNUSED_PAD src0_sel:DWORD src1_sel:WORD_1
v_mfma_f32_16x16x32_bf16 v[52:55],a[208:211],a[4:7],v[52:55]
s_add_u32 s69,s69,s59
ds_read_u16 v44,v44
v_lshlrev_b32_sdwa v46,1,v46 dst_sel:DWORD dst_unused:UNUSED_PAD src0_sel:DWORD src1_sel:WORD_1
v_mfma_f32_16x16x32_bf16 v[232:235],a[236:239],v[36:39],v[232:235]
buffer_load_dwordx4 v5,s[44:47],s69 offen offset:0 lds
v_lshlrev_b32_sdwa v45,1,v45 dst_sel:DWORD dst_unused:UNUSED_PAD src0_sel:DWORD src1_sel:WORD_1
v_mfma_f32_16x16x32_bf16 v[236:239],a[236:239],v[40:43],v[236:239]
ds_read_u16_d16_hi v46,v46
v_lshlrev_b32_sdwa v47,1,v47 dst_sel:DWORD dst_unused:UNUSED_PAD src0_sel:DWORD src1_sel:WORD_1
v_mfma_f32_16x16x32_bf16 v[164:167],a[244:247],v[40:43],v[164:167]
ds_read_u16 v45,v45
ds_read_u16_d16_hi v47,v47
v_mfma_f32_16x16x32_bf16 v[160:163],a[244:247],v[36:39],v[160:163]
ds_read_b64_tr_b16 a[228:229],v3 offset:18480
v_lshlrev_b32_sdwa v48,1,v48 dst_sel:DWORD dst_unused:UNUSED_PAD src0_sel:DWORD src1_sel:WORD_1
v_mfma_f32_16x16x32_bf16 v[240:243],a[248:251],v[36:39],v[240:243]
s_add_u32 s69,s69,s53
ds_read_u16 v48,v48
v_lshlrev_b32_sdwa v50,1,v50 dst_sel:DWORD dst_unused:UNUSED_PAD src0_sel:DWORD src1_sel:WORD_1
v_mfma_f32_16x16x32_bf16 v[244:247],a[248:251],v[40:43],v[244:247]
buffer_load_dwordx4 v5,s[44:47],s69 offen offset:1024 lds
ds_read_u16_d16_hi v50,v50
v_mfma_f32_16x16x32_bf16 v[180:183],a[252:255],v[40:43],v[180:183]
ds_read_b64_tr_b16 a[230:231],v3 offset:20016
v_lshlrev_b32_sdwa v49,1,v49 dst_sel:DWORD dst_unused:UNUSED_PAD src0_sel:DWORD src1_sel:WORD_1
v_mfma_f32_16x16x32_bf16 v[176:179],a[252:255],v[36:39],v[176:179]
ds_read_u16 v49,v49
v_lshlrev_b32_sdwa v51,1,v51 dst_sel:DWORD dst_unused:UNUSED_PAD src0_sel:DWORD src1_sel:WORD_1
v_mfma_f32_16x16x32_bf16 v[248:251],a[240:243],v[36:39],v[248:251]
ds_read_u16_d16_hi v51,v51
v_mfma_f32_16x16x32_bf16 v[252:255],a[240:243],v[40:43],v[252:255]
s_add_u32 s69,s69,s53
ds_read_b64_tr_b16 a[232:233],v3 offset:18488
v_mfma_f32_16x16x32_bf16 v[196:199],a[224:227],v[40:43],v[196:199]
buffer_load_dwordx4 v5,s[44:47],s69 offen offset:2048 lds
v_mfma_f32_16x16x32_bf16 v[192:195],a[224:227],v[36:39],v[192:195]
ds_read_b64_tr_b16 a[234:235],v3 offset:20024
v_mfma_f32_16x16x32_bf16 v[40:43],a[208:211],a[8:11],v[20:23]
ds_read_b64_tr_b16 a[220:221],v1 offset:18480
v_lshlrev_b32_sdwa v32,1,v32 dst_sel:DWORD dst_unused:UNUSED_PAD src0_sel:DWORD src1_sel:WORD_1
v_mfma_f32_16x16x32_bf16 v[40:43],a[204:207],a[24:27],v[40:43]
s_waitcnt lgkmcnt(7)
ds_read_u16 v32,v32
v_lshl_add_u64 v[24:25],v[24:25],0,v[26:27]
v_fma_mixlo_f16 v8,v57,s99,0
v_mfma_f32_16x16x32_bf16 v[40:43],a[212:215],a[40:43],v[40:43]
ds_read_b64_tr_b16 a[222:223],v1 offset:19504
v_lshl_add_u64 v[26:27],v[28:29],0,v[30:31]
v_lshlrev_b32_sdwa v34,1,v34 dst_sel:DWORD dst_unused:UNUSED_PAD src0_sel:DWORD src1_sel:WORD_1
v_mfma_f32_16x16x32_bf16 v[36:39],a[200:203],a[40:43],v[16:19]
ds_read_u16_d16_hi v34,v34
v_fma_mixhi_f16 v8,v59,s99,0
v_mfma_f32_16x16x32_bf16 v[36:39],a[196:199],a[24:27],v[36:39]
ds_read_b64_tr_b16 a[216:217],v1 offset:20528
v_lshl_add_u64 v[28:29],v[44:45],0,v[46:47]
v_lshlrev_b32_sdwa v33,1,v33 dst_sel:DWORD dst_unused:UNUSED_PAD src0_sel:DWORD src1_sel:WORD_1
v_pk_fma_f16 v9,v8,v8,s54
v_mfma_f32_16x16x32_bf16 v[36:39],a[192:195],a[8:11],v[36:39]
ds_read_u16 v33,v33
v_lshlrev_b32_sdwa v35,1,v35 dst_sel:DWORD dst_unused:UNUSED_PAD src0_sel:DWORD src1_sel:WORD_1
v_mfma_f32_16x16x32_bf16 v[44:47],a[192:195],a[12:15],v[16:19]
ds_read_u16_d16_hi v35,v35
v_pk_mul_f16 v9,v8,v9
v_mfma_f32_16x16x32_bf16 v[44:47],a[196:199],a[28:31],v[44:47]
s_waitcnt lgkmcnt(8)
ds_read_b64_tr_b16 a[218:219],v1 offset:21552
v_lshl_add_u64 v[30:31],v[48:49],0,v[50:51]
v_lshlrev_b32_sdwa v60,1,v60 dst_sel:DWORD dst_unused:UNUSED_PAD src0_sel:DWORD src1_sel:WORD_1
v_mfma_f32_16x16x32_bf16 v[44:47],a[200:203],a[44:47],v[44:47]
v_exp_f16 v8,v9
v_mfma_f32_16x16x32_bf16 v[48:51],a[212:215],a[44:47],v[20:23]
ds_read_u16 v60,v60
v_lshlrev_b32_sdwa v62,1,v62 dst_sel:DWORD dst_unused:UNUSED_PAD src0_sel:DWORD src1_sel:WORD_1
v_mfma_f32_16x16x32_bf16 v[48:51],a[204:207],a[28:31],v[48:51]
ds_read_u16_d16_hi v62,v62
v_lshlrev_b32_sdwa v61,1,v61 dst_sel:DWORD dst_unused:UNUSED_PAD src0_sel:DWORD src1_sel:WORD_1
v_mfma_f32_16x16x32_bf16 v[48:51],a[208:211],a[12:15],v[48:51]
v_exp_f16_sdwa v8,v9 dst_sel:WORD_1 dst_unused:UNUSED_PRESERVE src0_sel:WORD_1
v_mfma_f32_16x16x32_bf16 v[208:211],a[236:239],v[24:27],v[208:211]
ds_read_u16 v61,v61
v_lshlrev_b32_sdwa v63,1,v63 dst_sel:DWORD dst_unused:UNUSED_PAD src0_sel:DWORD src1_sel:WORD_1
v_mfma_f32_16x16x32_bf16 v[212:215],a[236:239],v[28:31],v[212:215]
ds_read_u16_d16_hi v63,v63
v_lshlrev_b32_sdwa v52,1,v52 dst_sel:DWORD dst_unused:UNUSED_PAD src0_sel:DWORD src1_sel:WORD_1
v_mfma_f32_16x16x32_bf16 v[172:175],a[244:247],v[28:31],v[172:175]
ds_read_u16 v52,v52
v_pk_fma_f16 v8,v8,s55,s55
v_mfma_f32_16x16x32_bf16 v[168:171],a[244:247],v[24:27],v[168:171]
ds_read_b64_tr_b16 a[236:237],v3 offset:18544
v_rcp_f16 v9,v8
v_lshlrev_b32_sdwa v54,1,v54 dst_sel:DWORD dst_unused:UNUSED_PAD src0_sel:DWORD src1_sel:WORD_1
v_mfma_f32_16x16x32_bf16 v[216:219],a[248:251],v[24:27],v[216:219]
ds_read_u16_d16_hi v54,v54
v_lshlrev_b32_sdwa v53,1,v53 dst_sel:DWORD dst_unused:UNUSED_PAD src0_sel:DWORD src1_sel:WORD_1
v_mfma_f32_16x16x32_bf16 v[220:223],a[248:251],v[28:31],v[220:223]
ds_read_u16 v53,v53
ds_read_b64_tr_b16 a[238:239],v3 offset:20080
v_mfma_f32_16x16x32_bf16 v[188:191],a[252:255],v[28:31],v[188:191]
ds_read_b64_tr_b16 a[244:245],v3 offset:18552
v_lshlrev_b32_sdwa v55,1,v55 dst_sel:DWORD dst_unused:UNUSED_PAD src0_sel:DWORD src1_sel:WORD_1
v_mfma_f32_16x16x32_bf16 v[184:187],a[252:255],v[24:27],v[184:187]
ds_read_u16_d16_hi v55,v55
v_rcp_f16_sdwa v9,v8 dst_sel:WORD_1 dst_unused:UNUSED_PRESERVE src0_sel:WORD_1
v_mfma_f32_16x16x32_bf16 v[224:227],a[240:243],v[24:27],v[224:227]
ds_read_b64_tr_b16 a[246:247],v3 offset:20088
ds_read_b64_tr_b16 a[248:249],v3 offset:18608
v_lshlrev_b32_sdwa v56,1,v56 dst_sel:DWORD dst_unused:UNUSED_PAD src0_sel:DWORD src1_sel:WORD_1
v_mfma_f32_16x16x32_bf16 v[228:231],a[240:243],v[28:31],v[228:231]
ds_read_u16 v56,v56
v_lshlrev_b32_sdwa v58,1,v58 dst_sel:DWORD dst_unused:UNUSED_PAD src0_sel:DWORD src1_sel:WORD_1
v_mfma_f32_16x16x32_bf16 v[204:207],a[224:227],v[28:31],v[204:207]
ds_read_u16_d16_hi v58,v58
v_fma_mix_f32 v57,v57,v9,0 op_sel_hi:[0,1,0]
v_mfma_f32_16x16x32_bf16 v[200:203],a[224:227],v[24:27],v[200:203]
ds_read_b64_tr_b16 a[250:251],v3 offset:20144
v_fma_mix_f32 v59,v59,v9,0 op_sel:[0,1,0] op_sel_hi:[0,1,0]
v_mfma_f32_16x16x32_bf16 v[28:31],a[208:211],a[48:51],v[20:23]
s_add_u32 m0,s58,12320
ds_read_b64_tr_b16 a[252:253],v3 offset:18616
v_lshlrev_b32_sdwa v40,1,v40 dst_sel:DWORD dst_unused:UNUSED_PAD src0_sel:DWORD src1_sel:WORD_1
v_mfma_f32_16x16x32_bf16 v[28:31],a[204:207],a[64:67],v[28:31]
s_waitcnt lgkmcnt(10)
ds_read_b64_tr_b16 a[254:255],v3 offset:20152
v_lshl_add_u64 v[32:33],v[32:33],0,v[34:35]
v_mfma_f32_16x16x32_bf16 v[28:31],a[212:215],a[80:83],v[28:31]
s_add_u32 s69,s69,s59
ds_read_u16 v40,v40
v_lshl_add_u64 v[34:35],v[60:61],0,v[62:63]
v_lshlrev_b32_sdwa v42,1,v42 dst_sel:DWORD dst_unused:UNUSED_PAD src0_sel:DWORD src1_sel:WORD_1
v_mfma_f32_16x16x32_bf16 v[24:27],a[200:203],a[80:83],v[16:19]
buffer_load_dwordx4 v5,s[44:47],s69 offen offset:0 lds
v_lshlrev_b32_sdwa v41,1,v41 dst_sel:DWORD dst_unused:UNUSED_PAD src0_sel:DWORD src1_sel:WORD_1
v_mfma_f32_16x16x32_bf16 v[24:27],a[196:199],a[64:67],v[24:27]
s_waitcnt lgkmcnt(8)
ds_read_u16_d16_hi v42,v42
v_lshl_add_u64 v[60:61],v[52:53],0,v[54:55]
v_mfma_f32_16x16x32_bf16 v[24:27],a[192:195],a[48:51],v[24:27]
s_add_u32 s69,s69,s53
ds_read_u16 v41,v41
v_lshlrev_b32_sdwa v43,1,v43 dst_sel:DWORD dst_unused:UNUSED_PAD src0_sel:DWORD src1_sel:WORD_1
v_mfma_f32_16x16x32_bf16 v[52:55],a[192:195],a[52:55],v[16:19]
s_waitcnt lgkmcnt(6)
buffer_load_dwordx4 v5,s[44:47],s69 offen offset:1024 lds
v_or_b32 v62,v56,v58
v_lshlrev_b32_sdwa v36,1,v36 dst_sel:DWORD dst_unused:UNUSED_PAD src0_sel:DWORD src1_sel:WORD_1
v_mfma_f32_16x16x32_bf16 v[52:55],a[196:199],a[68:71],v[52:55]
s_add_u32 s69,s69,s53
ds_read_u16_d16_hi v43,v43
v_cvt_pk_bf16_f32 v63,v57,v59
v_lshlrev_b32_sdwa v38,1,v38 dst_sel:DWORD dst_unused:UNUSED_PAD src0_sel:DWORD src1_sel:WORD_1
v_mfma_f32_16x16x32_bf16 v[52:55],a[200:203],a[84:87],v[52:55]
buffer_load_dwordx4 v5,s[44:47],s69 offen offset:2048 lds
v_lshlrev_b32_sdwa v37,1,v37 dst_sel:DWORD dst_unused:UNUSED_PAD src0_sel:DWORD src1_sel:WORD_1
v_mfma_f32_16x16x32_bf16 v[56:59],a[212:215],a[84:87],v[20:23]
ds_read_u16 v36,v36
v_lshlrev_b32_sdwa v39,1,v39 dst_sel:DWORD dst_unused:UNUSED_PAD src0_sel:DWORD src1_sel:WORD_1
v_mfma_f32_16x16x32_bf16 v[56:59],a[204:207],a[68:71],v[56:59]
ds_read_u16_d16_hi v38,v38
v_lshlrev_b32_sdwa v48,1,v48 dst_sel:DWORD dst_unused:UNUSED_PAD src0_sel:DWORD src1_sel:WORD_1
v_mfma_f32_16x16x32_bf16 v[56:59],a[208:211],a[52:55],v[56:59]
ds_read_u16 v37,v37
v_lshlrev_b32_sdwa v50,1,v50 dst_sel:DWORD dst_unused:UNUSED_PAD src0_sel:DWORD src1_sel:WORD_1
v_mfma_f32_16x16x32_bf16 v[136:139],a[228:231],v[32:35],v[136:139]
ds_read_u16_d16_hi v39,v39
v_lshlrev_b32_sdwa v49,1,v49 dst_sel:DWORD dst_unused:UNUSED_PAD src0_sel:DWORD src1_sel:WORD_1
v_mfma_f32_16x16x32_bf16 v[140:143],a[228:231],v[60:63],v[140:143]
ds_read_u16 v48,v48
v_lshlrev_b32_sdwa v51,1,v51 dst_sel:DWORD dst_unused:UNUSED_PAD src0_sel:DWORD src1_sel:WORD_1
v_mfma_f32_16x16x32_bf16 v[68:71],a[232:235],v[60:63],v[68:71]
ds_read_u16_d16_hi v50,v50
v_lshlrev_b32_sdwa v44,1,v44 dst_sel:DWORD dst_unused:UNUSED_PAD src0_sel:DWORD src1_sel:WORD_1
v_mfma_f32_16x16x32_bf16 v[64:67],a[232:235],v[32:35],v[64:67]
ds_read_u16 v49,v49
v_lshlrev_b32_sdwa v46,1,v46 dst_sel:DWORD dst_unused:UNUSED_PAD src0_sel:DWORD src1_sel:WORD_1
v_mfma_f32_16x16x32_bf16 v[144:147],a[236:239],v[32:35],v[144:147]
ds_read_u16_d16_hi v51,v51
v_lshlrev_b32_sdwa v45,1,v45 dst_sel:DWORD dst_unused:UNUSED_PAD src0_sel:DWORD src1_sel:WORD_1
v_mfma_f32_16x16x32_bf16 v[148:151],a[236:239],v[60:63],v[148:151]
ds_read_u16 v44,v44
v_lshlrev_b32_sdwa v47,1,v47 dst_sel:DWORD dst_unused:UNUSED_PAD src0_sel:DWORD src1_sel:WORD_1
v_mfma_f32_16x16x32_bf16 v[84:87],a[244:247],v[60:63],v[84:87]
ds_read_u16_d16_hi v46,v46
v_mfma_f32_16x16x32_bf16 v[80:83],a[244:247],v[32:35],v[80:83]
s_waitcnt lgkmcnt(8)
ds_read_u16 v45,v45
v_mfma_f32_16x16x32_bf16 v[152:155],a[248:251],v[32:35],v[152:155]
ds_read_u16_d16_hi v47,v47
v_mfma_f32_16x16x32_bf16 v[156:159],a[248:251],v[60:63],v[156:159]
v_mfma_f32_16x16x32_bf16 v[100:103],a[252:255],v[60:63],v[100:103]
v_mfma_f32_16x16x32_bf16 v[96:99],a[252:255],v[32:35],v[96:99]
v_mfma_f32_16x16x32_bf16 v[60:63],a[208:211],a[56:59],v[20:23]
v_mul_f32_dpp v136,v14,v136 row_newbcast:0 row_mask:0xf bank_mask:0xf
s_add_u32 s8,s61,0
v_mul_f32_dpp v137,v14,v137 row_newbcast:1 row_mask:0xf bank_mask:0xf
v_mul_f32_dpp v138,v14,v138 row_newbcast:2 row_mask:0xf bank_mask:0xf
v_lshlrev_b32_sdwa v28,1,v28 dst_sel:DWORD dst_unused:UNUSED_PAD src0_sel:DWORD src1_sel:WORD_1
v_mfma_f32_16x16x32_bf16 v[60:63],a[204:207],a[72:75],v[60:63]
s_nop 0
ds_read_u16 v28,v28
v_lshl_add_u64 v[40:41],v[40:41],0,v[42:43]
v_mul_f32_dpp v139,v14,v139 row_newbcast:3 row_mask:0xf bank_mask:0xf
v_mul_f32_dpp v64,v14,v64 row_newbcast:4 row_mask:0xf bank_mask:0xf
v_mul_f32_dpp v65,v14,v65 row_newbcast:5 row_mask:0xf bank_mask:0xf
v_mfma_f32_16x16x32_bf16 v[60:63],a[212:215],a[88:91],v[60:63]
s_waitcnt lgkmcnt(4)
v_lshl_add_u64 v[42:43],v[36:37],0,v[38:39]
v_mul_f32_dpp v66,v14,v66 row_newbcast:6 row_mask:0xf bank_mask:0xf
v_mul_f32_dpp v67,v14,v67 row_newbcast:7 row_mask:0xf bank_mask:0xf
v_lshlrev_b32_sdwa v30,1,v30 dst_sel:DWORD dst_unused:UNUSED_PAD src0_sel:DWORD src1_sel:WORD_1
v_cvt_pk_bf16_f32 v136,v136,v137
v_mfma_f32_16x16x32_bf16 v[32:35],a[200:203],a[88:91],v[16:19]
ds_read_u16_d16_hi v30,v30
v_cvt_pk_bf16_f32 v137,v138,v139
v_cvt_pk_bf16_f32 v138,v64,v65
v_lshlrev_b32_sdwa v29,1,v29 dst_sel:DWORD dst_unused:UNUSED_PAD src0_sel:DWORD src1_sel:WORD_1
v_cvt_pk_bf16_f32 v139,v66,v67
v_mfma_f32_16x16x32_bf16 v[32:35],a[196:199],a[72:75],v[32:35]
buffer_store_dwordx4 v[136:139],v7,s[40:43],s8 offen offset:0 sc1
v_lshl_add_u64 v[36:37],v[48:49],0,v[50:51]
v_mul_f32_dpp v144,v14,v144 row_newbcast:8 row_mask:0xf bank_mask:0xf
v_mul_f32_dpp v145,v14,v145 row_newbcast:9 row_mask:0xf bank_mask:0xf
v_mul_f32_dpp v146,v14,v146 row_newbcast:10 row_mask:0xf bank_mask:0xf
v_mfma_f32_16x16x32_bf16 v[32:35],a[192:195],a[56:59],v[32:35]
ds_read_u16 v29,v29
v_mul_f32_dpp v147,v14,v147 row_newbcast:11 row_mask:0xf bank_mask:0xf
v_mul_f32_dpp v80,v14,v80 row_newbcast:12 row_mask:0xf bank_mask:0xf
v_lshlrev_b32_sdwa v31,1,v31 dst_sel:DWORD dst_unused:UNUSED_PAD src0_sel:DWORD src1_sel:WORD_1
v_mul_f32_dpp v81,v14,v81 row_newbcast:13 row_mask:0xf bank_mask:0xf
v_mfma_f32_16x16x32_bf16 v[48:51],a[192:195],a[60:63],v[16:19]
s_nop 0
ds_read_b128 v[16:19],v4 offset:0
v_mul_f32_dpp v82,v14,v82 row_newbcast:14 row_mask:0xf bank_mask:0xf
v_mul_f32_dpp v83,v14,v83 row_newbcast:15 row_mask:0xf bank_mask:0xf
v_lshlrev_b32_sdwa v24,1,v24 dst_sel:DWORD dst_unused:UNUSED_PAD src0_sel:DWORD src1_sel:WORD_1
v_cvt_pk_bf16_f32 v144,v144,v145
v_mfma_f32_16x16x32_bf16 v[48:51],a[196:199],a[76:79],v[48:51]
s_waitcnt lgkmcnt(4)
ds_read_u16_d16_hi v31,v31
ds_read_b64_tr_b16 a[192:193],v1 offset:22576
v_lshl_add_u64 v[38:39],v[44:45],0,v[46:47]
v_cvt_pk_bf16_f32 v145,v146,v147
v_cvt_pk_bf16_f32 v146,v80,v81
v_cvt_pk_bf16_f32 v147,v82,v83
v_lshlrev_b32_sdwa v26,1,v26 dst_sel:DWORD dst_unused:UNUSED_PAD src0_sel:DWORD src1_sel:WORD_1
v_mfma_f32_16x16x32_bf16 v[48:51],a[200:203],a[92:95],v[48:51]
ds_read_b64_tr_b16 a[194:195],v1 offset:23600
ds_read_u16 v24,v24
v_mul_f32_dpp v152,v15,v152 row_newbcast:0 row_mask:0xf bank_mask:0xf
v_mul_f32_dpp v153,v15,v153 row_newbcast:1 row_mask:0xf bank_mask:0xf
v_mul_f32_dpp v154,v15,v154 row_newbcast:2 row_mask:0xf bank_mask:0xf
v_mfma_f32_16x16x32_bf16 v[44:47],a[212:215],a[92:95],v[20:23]
ds_read_b128 v[20:23],v4 offset:32
ds_read_u16_d16_hi v26,v26
v_mul_f32_dpp v155,v15,v155 row_newbcast:3 row_mask:0xf bank_mask:0xf
v_mul_f32_dpp v96,v15,v96 row_newbcast:4 row_mask:0xf bank_mask:0xf
v_lshlrev_b32_sdwa v25,1,v25 dst_sel:DWORD dst_unused:UNUSED_PAD src0_sel:DWORD src1_sel:WORD_1
v_mul_f32_dpp v97,v15,v97 row_newbcast:5 row_mask:0xf bank_mask:0xf
v_mfma_f32_16x16x32_bf16 v[44:47],a[204:207],a[76:79],v[44:47]
ds_read_u16 v25,v25
ds_read_b64_tr_b16 a[196:197],v2 offset:22576
v_mul_f32_dpp v98,v15,v98 row_newbcast:6 row_mask:0xf bank_mask:0xf
v_mul_f32_dpp v99,v15,v99 row_newbcast:7 row_mask:0xf bank_mask:0xf
v_lshlrev_b32_sdwa v27,1,v27 dst_sel:DWORD dst_unused:UNUSED_PAD src0_sel:DWORD src1_sel:WORD_1
v_cvt_pk_bf16_f32 v152,v152,v153
v_mfma_f32_16x16x32_bf16 v[44:47],a[208:211],a[60:63],v[44:47]
ds_read_u16_d16_hi v27,v27
ds_read_b64_tr_b16 a[198:199],v2 offset:23600
v_cvt_pk_bf16_f32 v153,v154,v155
v_cvt_pk_bf16_f32 v154,v96,v97
v_cvt_pk_bf16_f32 v155,v98,v99
v_mfma_f32_16x16x32_bf16 v[112:115],a[228:231],v[40:43],v[112:115]
ds_read_b64_tr_b16 a[200:201],v2 offset:20528
v_mul_f32_dpp v140,v14,v140 row_newbcast:0 row_mask:0xf bank_mask:0xf
v_mul_f32_dpp v141,v14,v141 row_newbcast:1 row_mask:0xf bank_mask:0xf
v_lshlrev_b32_sdwa v56,1,v56 dst_sel:DWORD dst_unused:UNUSED_PAD src0_sel:DWORD src1_sel:WORD_1
v_mul_f32_dpp v142,v14,v142 row_newbcast:2 row_mask:0xf bank_mask:0xf
v_mfma_f32_16x16x32_bf16 v[116:119],a[228:231],v[36:39],v[116:119]
ds_read_b64_tr_b16 a[202:203],v2 offset:21552
v_mul_f32_dpp v143,v14,v143 row_newbcast:3 row_mask:0xf bank_mask:0xf
v_mul_f32_dpp v68,v14,v68 row_newbcast:4 row_mask:0xf bank_mask:0xf
v_lshlrev_b32_sdwa v58,1,v58 dst_sel:DWORD dst_unused:UNUSED_PAD src0_sel:DWORD src1_sel:WORD_1
v_mul_f32_dpp v69,v14,v69 row_newbcast:5 row_mask:0xf bank_mask:0xf
v_mfma_f32_16x16x32_bf16 v[76:79],a[232:235],v[36:39],v[76:79]
ds_read_u16 v56,v56
v_mul_f32_dpp v70,v14,v70 row_newbcast:6 row_mask:0xf bank_mask:0xf
v_mul_f32_dpp v71,v14,v71 row_newbcast:7 row_mask:0xf bank_mask:0xf
v_cvt_pk_bf16_f32 v140,v140,v141
v_lshlrev_b32_sdwa v57,1,v57 dst_sel:DWORD dst_unused:UNUSED_PAD src0_sel:DWORD src1_sel:WORD_1
v_mfma_f32_16x16x32_bf16 v[72:75],a[232:235],v[40:43],v[72:75]
TP4880_%=:
s_cbranch_execz TP7016_%=
buffer_store_dwordx4 v[144:147],v7,s[40:43],s8 offen offset:64 sc1
ds_read_u16_d16_hi v58,v58
v_cvt_pk_bf16_f32 v141,v142,v143
v_cvt_pk_bf16_f32 v142,v68,v69
v_cvt_pk_bf16_f32 v143,v70,v71
v_mfma_f32_16x16x32_bf16 v[120:123],a[236:239],v[40:43],v[120:123]
s_nop 0
ds_read_u16 v57,v57
v_mul_f32_dpp v148,v14,v148 row_newbcast:8 row_mask:0xf bank_mask:0xf
v_mul_f32_dpp v149,v14,v149 row_newbcast:9 row_mask:0xf bank_mask:0xf
v_lshlrev_b32_sdwa v59,1,v59 dst_sel:DWORD dst_unused:UNUSED_PAD src0_sel:DWORD src1_sel:WORD_1
v_mul_f32_dpp v150,v14,v150 row_newbcast:10 row_mask:0xf bank_mask:0xf
v_mfma_f32_16x16x32_bf16 v[124:127],a[236:239],v[36:39],v[124:127]
ds_read_u16_d16_hi v59,v59
v_mul_f32_dpp v151,v14,v151 row_newbcast:11 row_mask:0xf bank_mask:0xf
v_mul_f32_dpp v84,v14,v84 row_newbcast:12 row_mask:0xf bank_mask:0xf
v_lshlrev_b32_sdwa v52,1,v52 dst_sel:DWORD dst_unused:UNUSED_PAD src0_sel:DWORD src1_sel:WORD_1
v_mul_f32_dpp v85,v14,v85 row_newbcast:13 row_mask:0xf bank_mask:0xf
v_mfma_f32_16x16x32_bf16 v[92:95],a[244:247],v[36:39],v[92:95]
ds_read_b64_tr_b16 a[212:213],v2 offset:18480
v_mul_f32_dpp v86,v14,v86 row_newbcast:14 row_mask:0xf bank_mask:0xf
v_mul_f32_dpp v87,v14,v87 row_newbcast:15 row_mask:0xf bank_mask:0xf
v_cvt_pk_bf16_f32 v148,v148,v149
v_mfma_f32_16x16x32_bf16 v[88:91],a[244:247],v[40:43],v[88:91]
ds_read_b64_tr_b16 a[214:215],v2 offset:19504
v_cvt_pk_bf16_f32 v149,v150,v151
v_cvt_pk_bf16_f32 v150,v84,v85
v_lshlrev_b32_sdwa v54,1,v54 dst_sel:DWORD dst_unused:UNUSED_PAD src0_sel:DWORD src1_sel:WORD_1
v_cvt_pk_bf16_f32 v151,v86,v87
v_mfma_f32_16x16x32_bf16 v[128:131],a[248:251],v[40:43],v[128:131]
ds_read_u16 v52,v52
v_mul_f32_dpp v156,v15,v156 row_newbcast:0 row_mask:0xf bank_mask:0xf
v_mul_f32_dpp v157,v15,v157 row_newbcast:1 row_mask:0xf bank_mask:0xf
v_lshlrev_b32_sdwa v53,1,v53 dst_sel:DWORD dst_unused:UNUSED_PAD src0_sel:DWORD src1_sel:WORD_1
v_mul_f32_dpp v158,v15,v158 row_newbcast:2 row_mask:0xf bank_mask:0xf
v_mfma_f32_16x16x32_bf16 v[132:135],a[248:251],v[36:39],v[132:135]
ds_read_u16_d16_hi v54,v54
v_mul_f32_dpp v159,v15,v159 row_newbcast:3 row_mask:0xf bank_mask:0xf
v_mul_f32_dpp v100,v15,v100 row_newbcast:4 row_mask:0xf bank_mask:0xf
v_mul_f32_dpp v101,v15,v101 row_newbcast:5 row_mask:0xf bank_mask:0xf
v_lshlrev_b32_sdwa v55,1,v55 dst_sel:DWORD dst_unused:UNUSED_PAD src0_sel:DWORD src1_sel:WORD_1
v_mfma_f32_16x16x32_bf16 v[108:111],a[252:255],v[36:39],v[108:111]
ds_read_u16 v53,v53
v_mul_f32_dpp v102,v15,v102 row_newbcast:6 row_mask:0xf bank_mask:0xf
v_mul_f32_dpp v103,v15,v103 row_newbcast:7 row_mask:0xf bank_mask:0xf
v_cvt_pk_bf16_f32 v156,v156,v157
v_mfma_f32_16x16x32_bf16 v[104:107],a[252:255],v[40:43],v[104:107]
s_waitcnt lgkmcnt(8)
ds_read_u16_d16_hi v55,v55
v_cvt_pk_bf16_f32 v157,v158,v159
v_cvt_pk_bf16_f32 v158,v100,v101
v_cvt_pk_bf16_f32 v159,v102,v103
v_mfma_f32_16x16x32_bf16 v[36:39],a[220:223],a[48:51],v[16:19]
s_nop 0
v_mul_f32_dpp v112,v14,v112 row_newbcast:0 row_mask:0xf bank_mask:0xf
s_add_u32 s9,s61,98304
v_accvgpr_write_b32 a16,0
v_accvgpr_write_b32 a17,0
v_lshlrev_b32_sdwa v60,1,v60 dst_sel:DWORD dst_unused:UNUSED_PAD src0_sel:DWORD src1_sel:WORD_1
v_mfma_f32_16x16x32_bf16 v[36:39],a[216:219],a[64:67],v[36:39]
ds_read_u16 v60,v60
v_lshl_add_u64 v[28:29],v[28:29],0,v[30:31]
v_accvgpr_write_b32 a18,0
v_accvgpr_write_b32 a19,0
v_mul_f32_dpp v113,v14,v113 row_newbcast:1 row_mask:0xf bank_mask:0xf
v_mfma_f32_16x16x32_bf16 v[36:39],a[192:195],a[80:83],v[36:39]
v_lshl_add_u64 v[30:31],v[24:25],0,v[26:27]
v_mul_f32_dpp v114,v14,v114 row_newbcast:2 row_mask:0xf bank_mask:0xf
v_mul_f32_dpp v115,v14,v115 row_newbcast:3 row_mask:0xf bank_mask:0xf
v_lshlrev_b32_sdwa v62,1,v62 dst_sel:DWORD dst_unused:UNUSED_PAD src0_sel:DWORD src1_sel:WORD_1
v_mul_f32_dpp v72,v14,v72 row_newbcast:4 row_mask:0xf bank_mask:0xf
v_mfma_f32_16x16x32_bf16 v[40:43],a[196:199],a[80:83],v[20:23]
s_nop 0
buffer_store_dwordx4 v[152:155],v7,s[40:43],s8 offen offset:128 sc1
v_mul_f32_dpp v73,v14,v73 row_newbcast:5 row_mask:0xf bank_mask:0xf
v_mul_f32_dpp v74,v14,v74 row_newbcast:6 row_mask:0xf bank_mask:0xf
v_lshlrev_b32_sdwa v61,1,v61 dst_sel:DWORD dst_unused:UNUSED_PAD src0_sel:DWORD src1_sel:WORD_1
v_mul_f32_dpp v75,v14,v75 row_newbcast:7 row_mask:0xf bank_mask:0xf
v_mfma_f32_16x16x32_bf16 v[40:43],a[200:203],a[64:67],v[40:43]
s_waitcnt lgkmcnt(5)
ds_read_u16_d16_hi v62,v62
v_lshl_add_u64 v[24:25],v[56:57],0,v[58:59]
v_cvt_pk_bf16_f32 v112,v112,v113
v_cvt_pk_bf16_f32 v113,v114,v115
v_cvt_pk_bf16_f32 v114,v72,v73
v_mfma_f32_16x16x32_bf16 v[40:43],a[212:215],a[48:51],v[40:43]
ds_read_u16 v61,v61
v_cvt_pk_bf16_f32 v115,v74,v75
v_mul_f32_dpp v120,v14,v120 row_newbcast:8 row_mask:0xf bank_mask:0xf
v_lshlrev_b32_sdwa v63,1,v63 dst_sel:DWORD dst_unused:UNUSED_PAD src0_sel:DWORD src1_sel:WORD_1
v_accvgpr_write_b32 a24,v112
v_mfma_f32_16x16x32_bf16 v[56:59],a[212:215],a[52:55],v[20:23]
s_nop 0
ds_read_u16_d16_hi v63,v63
v_mul_f32_dpp v121,v14,v121 row_newbcast:9 row_mask:0xf bank_mask:0xf
v_mul_f32_dpp v122,v14,v122 row_newbcast:10 row_mask:0xf bank_mask:0xf
v_lshlrev_b32_sdwa v32,1,v32 dst_sel:DWORD dst_unused:UNUSED_PAD src0_sel:DWORD src1_sel:WORD_1
v_accvgpr_write_b32 a25,v113
v_mfma_f32_16x16x32_bf16 v[56:59],a[200:203],a[68:71],v[56:59]
s_waitcnt lgkmcnt(4)
ds_read_u16 v32,v32
v_lshl_add_u64 v[26:27],v[52:53],0,v[54:55]
v_mul_f32_dpp v123,v14,v123 row_newbcast:11 row_mask:0xf bank_mask:0xf
v_mul_f32_dpp v88,v14,v88 row_newbcast:12 row_mask:0xf bank_mask:0xf
v_accvgpr_write_b32 a26,v114
v_lshlrev_b32_sdwa v34,1,v34 dst_sel:DWORD dst_unused:UNUSED_PAD src0_sel:DWORD src1_sel:WORD_1
v_mfma_f32_16x16x32_bf16 v[56:59],a[196:199],a[84:87],v[56:59]
s_waitcnt vmcnt(10)
s_barrier
ds_read_u16_d16_hi v34,v34
v_mul_f32_dpp v89,v14,v89 row_newbcast:13 row_mask:0xf bank_mask:0xf
v_mul_f32_dpp v90,v14,v90 row_newbcast:14 row_mask:0xf bank_mask:0xf
v_accvgpr_write_b32 a27,v115
v_mfma_f32_16x16x32_bf16 v[52:55],a[192:195],a[84:87],v[16:19]
ds_read_b64_tr_b16 a[240:241],v3 offset:0
v_mul_f32_dpp v91,v14,v91 row_newbcast:15 row_mask:0xf bank_mask:0xf
v_cvt_pk_bf16_f32 v120,v120,v121
v_lshlrev_b32_sdwa v33,1,v33 dst_sel:DWORD dst_unused:UNUSED_PAD src0_sel:DWORD src1_sel:WORD_1
v_cvt_pk_bf16_f32 v121,v122,v123
v_mfma_f32_16x16x32_bf16 v[52:55],a[216:219],a[68:71],v[52:55]
s_add_u32 m0,s58,18480
ds_read_u16 v33,v33
v_cvt_pk_bf16_f32 v122,v88,v89
v_cvt_pk_bf16_f32 v123,v90,v91
v_lshlrev_b32_sdwa v35,1,v35 dst_sel:DWORD dst_unused:UNUSED_PAD src0_sel:DWORD src1_sel:WORD_1
v_mul_f32_dpp v128,v15,v128 row_newbcast:0 row_mask:0xf bank_mask:0xf
v_mfma_f32_16x16x32_bf16 v[52:55],a[220:223],a[52:55],v[52:55]
s_add_u32 s69,s69,s59
ds_read_u16_d16_hi v35,v35
v_accvgpr_write_b32 a28,v120
v_mul_f32_dpp v129,v15,v129 row_newbcast:1 row_mask:0xf bank_mask:0xf
v_mul_f32_dpp v130,v15,v130 row_newbcast:2 row_mask:0xf bank_mask:0xf
v_mfma_f32_16x16x32_bf16 v[232:235],a[228:231],v[28:31],v[232:235]
buffer_load_dwordx4 v5,s[44:47],s69 offen offset:0 lds
v_accvgpr_write_b32 a29,v121
v_mul_f32_dpp v131,v15,v131 row_newbcast:3 row_mask:0xf bank_mask:0xf
v_lshlrev_b32_sdwa v44,1,v44 dst_sel:DWORD dst_unused:UNUSED_PAD src0_sel:DWORD src1_sel:WORD_1
v_mul_f32_dpp v104,v15,v104 row_newbcast:4 row_mask:0xf bank_mask:0xf
v_mfma_f32_16x16x32_bf16 v[236:239],a[228:231],v[24:27],v[236:239]
s_add_u32 s69,s69,s53
ds_read_u16 v44,v44
v_accvgpr_write_b32 a30,v122
v_mul_f32_dpp v105,v15,v105 row_newbcast:5 row_mask:0xf bank_mask:0xf
v_lshlrev_b32_sdwa v46,1,v46 dst_sel:DWORD dst_unused:UNUSED_PAD src0_sel:DWORD src1_sel:WORD_1
v_mul_f32_dpp v106,v15,v106 row_newbcast:6 row_mask:0xf bank_mask:0xf
v_mfma_f32_16x16x32_bf16 v[164:167],a[232:235],v[24:27],v[164:167]
buffer_load_dwordx4 v5,s[44:47],s69 offen offset:1024 lds
ds_read_u16_d16_hi v46,v46
v_accvgpr_write_b32 a31,v123
v_mul_f32_dpp v107,v15,v107 row_newbcast:7 row_mask:0xf bank_mask:0xf
v_cvt_pk_bf16_f32 v128,v128,v129
v_lshlrev_b32_sdwa v45,1,v45 dst_sel:DWORD dst_unused:UNUSED_PAD src0_sel:DWORD src1_sel:WORD_1
v_mfma_f32_16x16x32_bf16 v[160:163],a[232:235],v[28:31],v[160:163]
s_nop 0
buffer_store_dwordx4 v[140:143],v7,s[40:43],s8 offen offset:192 sc1
ds_read_u16 v45,v45
v_cvt_pk_bf16_f32 v129,v130,v131
v_cvt_pk_bf16_f32 v130,v104,v105
v_cvt_pk_bf16_f32 v131,v106,v107
v_mfma_f32_16x16x32_bf16 v[240:243],a[236:239],v[28:31],v[240:243]
s_add_u32 s69,s69,s53
ds_read_b64_tr_b16 a[242:243],v3 offset:1536
v_mul_f32_dpp v116,v14,v116 row_newbcast:0 row_mask:0xf bank_mask:0xf
v_accvgpr_write_b32 a32,v128
v_lshlrev_b32_sdwa v47,1,v47 dst_sel:DWORD dst_unused:UNUSED_PAD src0_sel:DWORD src1_sel:WORD_1
v_mul_f32_dpp v117,v14,v117 row_newbcast:1 row_mask:0xf bank_mask:0xf
v_mfma_f32_16x16x32_bf16 v[244:247],a[236:239],v[24:27],v[244:247]
buffer_load_dwordx4 v5,s[44:47],s69 offen offset:2048 lds
v_mul_f32_dpp v118,v14,v118 row_newbcast:2 row_mask:0xf bank_mask:0xf
v_accvgpr_write_b32 a33,v129
v_lshlrev_b32_sdwa v48,1,v48 dst_sel:DWORD dst_unused:UNUSED_PAD src0_sel:DWORD src1_sel:WORD_1
v_mul_f32_dpp v119,v14,v119 row_newbcast:3 row_mask:0xf bank_mask:0xf
v_mfma_f32_16x16x32_bf16 v[180:183],a[244:247],v[24:27],v[180:183]
ds_read_u16_d16_hi v47,v47
v_mul_f32_dpp v76,v14,v76 row_newbcast:4 row_mask:0xf bank_mask:0xf
v_accvgpr_write_b32 a34,v130
v_mul_f32_dpp v77,v14,v77 row_newbcast:5 row_mask:0xf bank_mask:0xf
v_mfma_f32_16x16x32_bf16 v[176:179],a[244:247],v[28:31],v[176:179]
ds_read_u16 v48,v48
v_mul_f32_dpp v78,v14,v78 row_newbcast:6 row_mask:0xf bank_mask:0xf
v_accvgpr_write_b32 a35,v131
v_lshlrev_b32_sdwa v50,1,v50 dst_sel:DWORD dst_unused:UNUSED_PAD src0_sel:DWORD src1_sel:WORD_1
v_mul_f32_dpp v79,v14,v79 row_newbcast:7 row_mask:0xf bank_mask:0xf
v_mfma_f32_16x16x32_bf16 v[248:251],a[248:251],v[28:31],v[248:251]
ds_read_u16_d16_hi v50,v50
v_cvt_pk_bf16_f32 v116,v116,v117
v_cvt_pk_bf16_f32 v117,v118,v119
v_lshlrev_b32_sdwa v49,1,v49 dst_sel:DWORD dst_unused:UNUSED_PAD src0_sel:DWORD src1_sel:WORD_1
v_cvt_pk_bf16_f32 v118,v76,v77
v_mfma_f32_16x16x32_bf16 v[252:255],a[248:251],v[24:27],v[252:255]
ds_read_u16 v49,v49
v_cvt_pk_bf16_f32 v119,v78,v79
v_mfma_f32_32x32x16_bf16 v[64:79],a[16:19],a[16:19],0
v_mul_f32_dpp v124,v14,v124 row_newbcast:8 row_mask:0xf bank_mask:0xf
v_lshlrev_b32_sdwa v51,1,v51 dst_sel:DWORD dst_unused:UNUSED_PAD src0_sel:DWORD src1_sel:WORD_1
v_accvgpr_write_b32 a36,v116
v_mul_f32_dpp v125,v14,v125 row_newbcast:9 row_mask:0xf bank_mask:0xf
v_mul_f32_dpp v126,v14,v126 row_newbcast:10 row_mask:0xf bank_mask:0xf
v_mfma_f32_16x16x32_bf16 v[196:199],a[252:255],v[24:27],v[196:199]
ds_read_u16_d16_hi v51,v51
v_accvgpr_write_b32 a37,v117
v_mfma_f32_16x16x32_bf16 v[192:195],a[252:255],v[28:31],v[192:195]
s_nop 0
ds_read_b64_tr_b16 a[224:225],v3 offset:8
v_mul_f32_dpp v127,v14,v127 row_newbcast:11 row_mask:0xf bank_mask:0xf
v_mul_f32_dpp v92,v14,v92 row_newbcast:12 row_mask:0xf bank_mask:0xf
ds_read_b64_tr_b16 a[226:227],v3 offset:1544
s_mov_b64 exec,0
s_cmp_eq_u32 s52,0
s_cbranch_scc1 TE10_%=
s_cmp_eq_u32 s52,1
s_cbranch_scc1 TE11_%=
s_cmp_eq_u32 s52,2
s_cbranch_scc1 TE12_%=
s_branch TE13_%=
TR1_%=:
s_mov_b64 exec,-1
v_add_u32_sdwa v28,s60,v10 dst_sel:DWORD dst_unused:UNUSED_PAD src0_sel:DWORD src1_sel:WORD_1
v_subrev_u32 v8,88400,v28
v_min_u32_e64 v28,v28,v8
v_add_u32 v29,7072,v28
v_subrev_u32 v8,88400,v29
v_min_u32_e64 v29,v29,v8
s_waitcnt lgkmcnt(15)
v_lshl_add_u64 v[60:61],v[60:61],0,v[62:63]
s_waitcnt lgkmcnt(11)
v_lshl_add_u64 v[62:63],v[32:33],0,v[34:35]
s_waitcnt lgkmcnt(6)
v_lshl_add_u64 v[32:33],v[44:45],0,v[46:47]
s_waitcnt lgkmcnt(2)
v_lshl_add_u64 v[34:35],v[48:49],0,v[50:51]
v_mfma_f32_16x16x32_bf16 v[208:211],a[228:231],v[60:63],v[208:211]
v_xad_u32 v24,v11,0,v28
v_xad_u32 v25,v11,8,v29
v_xad_u32 v26,v11,16,v28
v_xad_u32 v27,v11,24,v29
v_mfma_f32_16x16x32_bf16 v[212:215],a[228:231],v[32:35],v[212:215]
ds_read_b64 a[0:1],v24 offset:0
v_accvgpr_write_b32 a38,v118
v_mul_f32_dpp v93,v14,v93 row_newbcast:13 row_mask:0xf bank_mask:0xf
v_mul_f32_dpp v94,v14,v94 row_newbcast:14 row_mask:0xf bank_mask:0xf
v_accvgpr_write_b32 a39,v119
v_mfma_f32_16x16x32_bf16 v[172:175],a[232:235],v[32:35],v[172:175]
buffer_store_dwordx4 v[148:151],v7,s[40:43],s8 offen offset:256 sc1
ds_read_b64 a[2:3],v24 offset:192
v_mul_f32_dpp v95,v14,v95 row_newbcast:15 row_mask:0xf bank_mask:0xf
v_cvt_pk_bf16_f32 v124,v124,v125
v_cvt_pk_bf16_f32 v125,v126,v127
v_mfma_f32_16x16x32_bf16 v[168:171],a[232:235],v[60:63],v[168:171]
ds_read_b64 a[4:5],v25 offset:0
v_cvt_pk_bf16_f32 v126,v92,v93
v_cvt_pk_bf16_f32 v127,v94,v95
v_mfma_f32_32x32x16_bf16 v[80:95],a[16:19],a[16:19],0
v_mfma_f32_16x16x32_bf16 v[216:219],a[236:239],v[60:63],v[216:219]
ds_read_b64 a[6:7],v25 offset:192
v_mul_f32_dpp v132,v15,v132 row_newbcast:0 row_mask:0xf bank_mask:0xf
v_accvgpr_write_b32 a40,v124
v_mul_f32_dpp v133,v15,v133 row_newbcast:1 row_mask:0xf bank_mask:0xf
v_mfma_f32_16x16x32_bf16 v[220:223],a[236:239],v[32:35],v[220:223]
ds_read_b64 a[8:9],v24 offset:8
v_mul_f32_dpp v134,v15,v134 row_newbcast:2 row_mask:0xf bank_mask:0xf
v_accvgpr_write_b32 a41,v125
v_mul_f32_dpp v135,v15,v135 row_newbcast:3 row_mask:0xf bank_mask:0xf
v_mfma_f32_16x16x32_bf16 v[188:191],a[244:247],v[32:35],v[188:191]
ds_read_b64 a[10:11],v24 offset:200
v_mul_f32_dpp v108,v15,v108 row_newbcast:4 row_mask:0xf bank_mask:0xf
v_accvgpr_write_b32 a42,v126
v_mul_f32_dpp v109,v15,v109 row_newbcast:5 row_mask:0xf bank_mask:0xf
v_mfma_f32_16x16x32_bf16 v[184:187],a[244:247],v[60:63],v[184:187]
ds_read_b64 a[12:13],v25 offset:8
v_mul_f32_dpp v110,v15,v110 row_newbcast:6 row_mask:0xf bank_mask:0xf
v_accvgpr_write_b32 a43,v127
v_mul_f32_dpp v111,v15,v111 row_newbcast:7 row_mask:0xf bank_mask:0xf
v_mfma_f32_16x16x32_bf16 v[224:227],a[248:251],v[60:63],v[224:227]
ds_read_b64 a[14:15],v25 offset:200
v_cvt_pk_bf16_f32 v132,v132,v133
v_cvt_pk_bf16_f32 v133,v134,v135
v_cvt_pk_bf16_f32 v134,v108,v109
v_mfma_f32_16x16x32_bf16 v[228:231],a[248:251],v[32:35],v[228:231]
v_cvt_pk_bf16_f32 v135,v110,v111
v_mfma_f32_32x32x16_bf16 v[96:111],a[16:19],a[16:19],0
v_accvgpr_write_b32 a44,v132
v_mfma_f32_16x16x32_bf16 v[204:207],a[252:255],v[32:35],v[204:207]
v_accvgpr_write_b32 a45,v133
v_accvgpr_write_b32 a46,v134
v_accvgpr_write_b32 a47,v135
v_mfma_f32_16x16x32_bf16 v[200:203],a[252:255],v[60:63],v[200:203]
v_mfma_f32_32x32x16_bf16 v[112:127],a[16:19],a[16:19],0
v_mov_b64 v[44:45],0
v_mov_b64 v[46:47],0
ds_read_b64 a[16:17],v26 offset:0
ds_read_b64 a[18:19],v26 offset:192
ds_read_b64 a[20:21],v27 offset:0
ds_read_b64 a[22:23],v27 offset:192
s_waitcnt lgkmcnt(8)
v_smfmac_f32_16x16x64_bf16 v[64:67],a[96:99],a[0:7],v10
s_waitcnt lgkmcnt(4)
ds_read_b64 a[0:1],v26 offset:8
ds_read_b64 a[2:3],v26 offset:200
ds_read_b64 a[4:5],v27 offset:8
ds_read_b64 a[6:7],v27 offset:200
v_smfmac_f32_16x16x64_bf16 v[68:71],a[100:103],a[8:15],v10
s_waitcnt lgkmcnt(4)
ds_read_b64 a[8:9],v24 offset:64
ds_read_b64 a[10:11],v24 offset:256
ds_read_b64 a[12:13],v25 offset:64
ds_read_b64 a[14:15],v25 offset:256
v_smfmac_f32_16x16x64_bf16 v[72:75],a[104:107],a[16:23],v10
s_waitcnt lgkmcnt(4)
buffer_store_dwordx4 v[156:159],v7,s[40:43],s8 offen offset:320 sc1
ds_read_b64 a[16:17],v24 offset:72
ds_read_b64 a[18:19],v24 offset:264
ds_read_b64 a[20:21],v25 offset:72
ds_read_b64 a[22:23],v25 offset:264
v_and_b32 v60,8,v11
v_pk_mul_f32 v[32:33],v[64:65],v[64:65]
v_pk_mul_f32 v[34:35],v[66:67],v[66:67]
v_smfmac_f32_16x16x64_bf16 v[76:79],a[108:111],a[0:7],v10
s_waitcnt lgkmcnt(4)
ds_read_b64 a[0:1],v26 offset:64
ds_read_b64 a[2:3],v26 offset:256
ds_read_b64 a[4:5],v27 offset:64
ds_read_b64 a[6:7],v27 offset:256
v_mul_u32_u24 v60,884,v60
s_add_u32 s64,s60,3536
v_pk_add_f32 v[48:49],v[64:65],v[68:69]
v_pk_fma_f32 v[32:33],v[68:69],v[68:69],v[32:33]
v_pk_add_f32 v[50:51],v[66:67],v[70:71]
v_pk_fma_f32 v[34:35],v[70:71],v[70:71],v[34:35]
v_smfmac_f32_16x16x64_bf16 v[80:83],a[128:131],a[8:15],v10
s_waitcnt lgkmcnt(4)
ds_read_b64 a[8:9],v26 offset:72
ds_read_b64 a[10:11],v26 offset:264
ds_read_b64 a[12:13],v27 offset:72
ds_read_b64 a[14:15],v27 offset:264
v_add_u32 v63,s64,v60
v_pk_add_f32 v[48:49],v[48:49],v[72:73]
v_pk_fma_f32 v[32:33],v[72:73],v[72:73],v[32:33]
v_pk_add_f32 v[50:51],v[50:51],v[74:75]
v_pk_fma_f32 v[34:35],v[74:75],v[74:75],v[34:35]
v_smfmac_f32_16x16x64_bf16 v[84:87],a[132:135],a[16:23],v10
s_waitcnt lgkmcnt(4)
ds_read_b64 a[16:17],v24 offset:128
ds_read_b64 a[18:19],v24 offset:320
ds_read_b64 a[20:21],v25 offset:128
ds_read_b64 a[22:23],v25 offset:320
v_subrev_u32 v8,88400,v63
v_pk_add_f32 v[48:49],v[48:49],v[76:77]
v_pk_fma_f32 v[32:33],v[76:77],v[76:77],v[32:33]
v_pk_add_f32 v[50:51],v[50:51],v[78:79]
v_pk_fma_f32 v[34:35],v[78:79],v[78:79],v[34:35]
v_smfmac_f32_16x16x64_bf16 v[88:91],a[136:139],a[0:7],v10
s_waitcnt lgkmcnt(4)
ds_read_b64 a[0:1],v24 offset:136
ds_read_b64 a[2:3],v24 offset:328
ds_read_b64 a[4:5],v25 offset:136
ds_read_b64 a[6:7],v25 offset:328
v_min_u32 v63,v63,v8
v_pk_add_f32 v[48:49],v[48:49],v[80:81]
v_pk_fma_f32 v[32:33],v[80:81],v[80:81],v[32:33]
v_pk_add_f32 v[50:51],v[50:51],v[82:83]
v_pk_fma_f32 v[34:35],v[82:83],v[82:83],v[34:35]
v_smfmac_f32_16x16x64_bf16 v[92:95],a[140:143],a[8:15],v10
s_waitcnt lgkmcnt(4)
ds_read_b64 a[8:9],v26 offset:128
ds_read_b64 a[10:11],v26 offset:320
ds_read_b64 a[12:13],v27 offset:128
ds_read_b64 a[14:15],v27 offset:320
v_xad_u32 v24,v11,32,v28
v_xad_u32 v25,v11,40,v29
v_pk_add_f32 v[48:49],v[48:49],v[84:85]
v_pk_fma_f32 v[32:33],v[84:85],v[84:85],v[32:33]
v_pk_add_f32 v[50:51],v[50:51],v[86:87]
v_pk_fma_f32 v[34:35],v[86:87],v[86:87],v[34:35]
v_smfmac_f32_16x16x64_bf16 v[96:99],a[160:163],a[16:23],v10
s_waitcnt lgkmcnt(4)
ds_read_b64 a[16:17],v26 offset:136
ds_read_b64 a[18:19],v26 offset:328
ds_read_b64 a[20:21],v27 offset:136
ds_read_b64 a[22:23],v27 offset:328
v_add_u32 v62,3536,v63
v_pk_add_f32 v[48:49],v[48:49],v[88:89]
v_pk_fma_f32 v[32:33],v[88:89],v[88:89],v[32:33]
v_pk_add_f32 v[50:51],v[50:51],v[90:91]
v_pk_fma_f32 v[34:35],v[90:91],v[90:91],v[34:35]
v_smfmac_f32_16x16x64_bf16 v[100:103],a[164:167],a[0:7],v10
s_waitcnt lgkmcnt(4)
buffer_store_dwordx4 a[24:27],v7,s[40:43],s9 offen offset:0 sc1
ds_read_b64 a[0:1],v24 offset:0
ds_read_b64 a[2:3],v24 offset:192
ds_read_b64 a[4:5],v25 offset:0
ds_read_b64 a[6:7],v25 offset:192
v_xad_u32 v26,v11,48,v28
v_xad_u32 v27,v11,56,v29
v_pk_add_f32 v[48:49],v[48:49],v[92:93]
v_pk_fma_f32 v[32:33],v[92:93],v[92:93],v[32:33]
v_pk_add_f32 v[50:51],v[50:51],v[94:95]
v_pk_fma_f32 v[34:35],v[94:95],v[94:95],v[34:35]
v_smfmac_f32_16x16x64_bf16 v[104:107],a[168:171],a[8:15],v10
s_waitcnt lgkmcnt(4)
ds_read_b64 a[8:9],v24 offset:8
ds_read_b64 a[10:11],v24 offset:200
ds_read_b64 a[12:13],v25 offset:8
ds_read_b64 a[14:15],v25 offset:200
v_subrev_u32 v8,88400,v62
v_pk_add_f32 v[48:49],v[48:49],v[96:97]
v_pk_fma_f32 v[32:33],v[96:97],v[96:97],v[32:33]
v_pk_add_f32 v[50:51],v[50:51],v[98:99]
v_pk_fma_f32 v[34:35],v[98:99],v[98:99],v[34:35]
v_smfmac_f32_16x16x64_bf16 v[108:111],a[172:175],a[16:23],v10
s_waitcnt lgkmcnt(4)
ds_read_b64 a[16:17],v24 offset:64
ds_read_b64 a[18:19],v24 offset:256
ds_read_b64 a[20:21],v25 offset:64
ds_read_b64 a[22:23],v25 offset:256
v_min_u32 v62,v62,v8
v_pk_add_f32 v[48:49],v[48:49],v[100:101]
v_pk_fma_f32 v[32:33],v[100:101],v[100:101],v[32:33]
v_pk_add_f32 v[50:51],v[50:51],v[102:103]
v_pk_fma_f32 v[34:35],v[102:103],v[102:103],v[34:35]
v_smfmac_f32_16x16x64_bf16 v[112:115],a[112:115],a[0:7],v10
s_waitcnt lgkmcnt(4)
ds_read_b64 a[0:1],v24 offset:72
ds_read_b64 a[2:3],v24 offset:264
ds_read_b64 a[4:5],v25 offset:72
ds_read_b64 a[6:7],v25 offset:264
v_xor_b32 v9,0x80008,v12
v_pk_add_f32 v[48:49],v[48:49],v[104:105]
v_pk_fma_f32 v[32:33],v[104:105],v[104:105],v[32:33]
v_pk_add_f32 v[50:51],v[50:51],v[106:107]
v_pk_fma_f32 v[34:35],v[106:107],v[106:107],v[34:35]
v_mfma_f32_32x32x16_bf16 v[128:143],v[44:47],v[44:47],0
v_smfmac_f32_16x16x64_bf16 v[116:119],a[116:119],a[8:15],v10
s_waitcnt lgkmcnt(4)
ds_read_b64 a[8:9],v24 offset:128
ds_read_b64 a[10:11],v24 offset:320
ds_read_b64 a[12:13],v25 offset:128
ds_read_b64 a[14:15],v25 offset:320
v_add_u32_sdwa v60,v12,v63 dst_sel:DWORD dst_unused:UNUSED_PAD src0_sel:WORD_0 src1_sel:DWORD
v_pk_add_f32 v[48:49],v[48:49],v[108:109]
v_pk_fma_f32 v[32:33],v[108:109],v[108:109],v[32:33]
v_pk_add_f32 v[50:51],v[50:51],v[110:111]
v_pk_fma_f32 v[34:35],v[110:111],v[110:111],v[34:35]
v_smfmac_f32_16x16x64_bf16 v[120:123],a[144:147],a[16:23],v10
s_waitcnt lgkmcnt(4)
ds_read_b64 a[16:17],v24 offset:136
ds_read_b64 a[18:19],v24 offset:328
ds_read_b64 a[20:21],v25 offset:136
ds_read_b64 a[22:23],v25 offset:328
v_add_u32_sdwa v61,v12,v63 dst_sel:DWORD dst_unused:UNUSED_PAD src0_sel:WORD_1 src1_sel:DWORD
v_pk_add_f32 v[48:49],v[48:49],v[112:113]
v_pk_fma_f32 v[32:33],v[112:113],v[112:113],v[32:33]
v_pk_add_f32 v[50:51],v[50:51],v[114:115]
v_pk_fma_f32 v[34:35],v[114:115],v[114:115],v[34:35]
v_smfmac_f32_16x16x64_bf16 v[124:127],a[148:151],a[0:7],v10
s_waitcnt lgkmcnt(4)
buffer_store_dwordx4 a[28:31],v7,s[40:43],s9 offen offset:64 sc1
ds_read_b64 a[0:1],v26 offset:0
ds_read_b64 a[2:3],v26 offset:192
ds_read_b64 a[4:5],v27 offset:0
ds_read_b64 a[6:7],v27 offset:192
v_add_u32_sdwa v63,v9,v62 dst_sel:DWORD dst_unused:UNUSED_PAD src0_sel:WORD_1 src1_sel:DWORD
v_pk_add_f32 v[48:49],v[48:49],v[116:117]
v_pk_fma_f32 v[32:33],v[116:117],v[116:117],v[32:33]
v_pk_add_f32 v[50:51],v[50:51],v[118:119]
v_pk_fma_f32 v[34:35],v[118:119],v[118:119],v[34:35]
v_smfmac_f32_16x16x64_bf16 v[128:131],a[176:179],a[8:15],v10
s_waitcnt lgkmcnt(4)
ds_read_b64 a[8:9],v26 offset:8
ds_read_b64 a[10:11],v26 offset:200
ds_read_b64 a[12:13],v27 offset:8
ds_read_b64 a[14:15],v27 offset:200
v_add_u32_sdwa v62,v9,v62 dst_sel:DWORD dst_unused:UNUSED_PAD src0_sel:WORD_0 src1_sel:DWORD
v_pk_add_f32 v[48:49],v[48:49],v[120:121]
v_pk_fma_f32 v[32:33],v[120:121],v[120:121],v[32:33]
v_pk_add_f32 v[50:51],v[50:51],v[122:123]
v_pk_fma_f32 v[34:35],v[122:123],v[122:123],v[34:35]
v_mfma_f32_32x32x16_bf16 v[144:159],v[44:47],v[44:47],0
v_smfmac_f32_16x16x64_bf16 v[132:135],a[180:183],a[16:23],v10
s_waitcnt lgkmcnt(4)
ds_read_b64 a[16:17],v26 offset:64
ds_read_b64 a[18:19],v26 offset:256
ds_read_b64 a[20:21],v27 offset:64
ds_read_b64 a[22:23],v27 offset:256
v_pk_add_f32 v[48:49],v[48:49],v[124:125]
v_pk_fma_f32 v[32:33],v[124:125],v[124:125],v[32:33]
v_pk_add_f32 v[50:51],v[50:51],v[126:127]
v_pk_fma_f32 v[34:35],v[126:127],v[126:127],v[34:35]
v_smfmac_f32_16x16x64_bf16 v[136:139],a[120:123],a[0:7],v10
s_waitcnt lgkmcnt(4)
ds_read_b64 a[0:1],v26 offset:72
ds_read_b64 a[2:3],v26 offset:264
ds_read_b64 a[4:5],v27 offset:72
ds_read_b64 a[6:7],v27 offset:264
v_pk_add_f32 v[48:49],v[48:49],v[128:129]
v_pk_fma_f32 v[32:33],v[128:129],v[128:129],v[32:33]
v_pk_add_f32 v[50:51],v[50:51],v[130:131]
v_pk_fma_f32 v[34:35],v[130:131],v[130:131],v[34:35]
v_smfmac_f32_16x16x64_bf16 v[140:143],a[124:127],a[8:15],v10
s_waitcnt lgkmcnt(4)
ds_read_b64 a[8:9],v26 offset:128
ds_read_b64 a[10:11],v26 offset:320
ds_read_b64 a[12:13],v27 offset:128
ds_read_b64 a[14:15],v27 offset:320
v_pk_add_f32 v[48:49],v[48:49],v[132:133]
v_pk_fma_f32 v[32:33],v[132:133],v[132:133],v[32:33]
v_pk_add_f32 v[50:51],v[50:51],v[134:135]
v_pk_fma_f32 v[34:35],v[134:135],v[134:135],v[34:35]
v_smfmac_f32_16x16x64_bf16 v[144:147],a[152:155],a[16:23],v10
s_waitcnt lgkmcnt(4)
ds_read_b64 a[16:17],v26 offset:136
ds_read_b64 a[18:19],v26 offset:328
ds_read_b64 a[20:21],v27 offset:136
ds_read_b64 a[22:23],v27 offset:328
v_pk_add_f32 v[48:49],v[48:49],v[136:137]
v_pk_fma_f32 v[32:33],v[136:137],v[136:137],v[32:33]
v_pk_add_f32 v[50:51],v[50:51],v[138:139]
v_pk_fma_f32 v[34:35],v[138:139],v[138:139],v[34:35]
v_smfmac_f32_16x16x64_bf16 v[148:151],a[156:159],a[0:7],v10
s_waitcnt lgkmcnt(4)
ds_read_b64 a[228:229],v60 offset:57344
ds_read_b64 a[230:231],v60 offset:57352
ds_read_b64 a[232:233],v61 offset:57344
ds_read_b64 a[234:235],v61 offset:57352
ds_read_b128 a[248:251],v13 offset:0
v_pk_add_f32 v[48:49],v[48:49],v[140:141]
v_pk_fma_f32 v[32:33],v[140:141],v[140:141],v[32:33]
v_pk_add_f32 v[50:51],v[50:51],v[142:143]
v_pk_fma_f32 v[34:35],v[142:143],v[142:143],v[34:35]
v_smfmac_f32_16x16x64_bf16 v[152:155],a[184:187],a[8:15],v10
s_waitcnt lgkmcnt(5)
buffer_store_dwordx4 a[32:35],v7,s[40:43],s9 offen offset:128 sc1
ds_read_b64 a[236:237],v62 offset:57344
ds_read_b64 a[238:239],v62 offset:57352
ds_read_b64 a[244:245],v63 offset:57344
ds_read_b64 a[246:247],v63 offset:57352
ds_read_b128 a[252:255],v13 offset:256
v_pk_add_f32 v[48:49],v[48:49],v[144:145]
v_pk_fma_f32 v[32:33],v[144:145],v[144:145],v[32:33]
TP5419_%=:
s_cbranch_execz TR0_%=
s_nop 0
v_pk_add_f32 v[50:51],v[50:51],v[146:147]
v_pk_fma_f32 v[34:35],v[146:147],v[146:147],v[34:35]
v_smfmac_f32_16x16x64_bf16 v[156:159],a[188:191],a[16:23],v10
ds_read_b128 v[24:27],v4 offset:3584
ds_read_b128 v[28:31],v4 offset:3616
v_pk_add_f32 v[48:49],v[48:49],v[148:149]
v_pk_fma_f32 v[32:33],v[148:149],v[148:149],v[32:33]
v_pk_add_f32 v[50:51],v[50:51],v[150:151]
v_pk_fma_f32 v[34:35],v[150:151],v[150:151],v[34:35]
v_pk_add_f32 v[48:49],v[48:49],v[152:153]
v_pk_fma_f32 v[32:33],v[152:153],v[152:153],v[32:33]
v_pk_add_f32 v[50:51],v[50:51],v[154:155]
v_pk_fma_f32 v[34:35],v[154:155],v[154:155],v[34:35]
v_pk_add_f32 v[48:49],v[48:49],v[156:157]
v_pk_fma_f32 v[32:33],v[156:157],v[156:157],v[32:33]
v_pk_add_f32 v[50:51],v[50:51],v[158:159]
v_pk_fma_f32 v[34:35],v[158:159],v[158:159],v[34:35]
s_nop 0
v_permlane32_swap_b32 v48,v50
v_permlane32_swap_b32 v49,v51
v_permlane32_swap_b32 v32,v34
v_permlane32_swap_b32_e64 v33,v35
v_pk_add_f32 v[48:49],v[48:49],v[50:51]
v_pk_add_f32 v[32:33],v[32:33],v[34:35]
s_add_u32 s60,s60,56576
s_sub_u32 s64,s60,88400
s_min_u32 s60,s60,s64
v_permlane16_swap_b32 v48,v49
v_permlane16_swap_b32 v32,v33
v_add_f32 v8,v48,v49
v_add_f32_e64 v9,v32,v33
v_mul_f32 v8,0x3c2aaaab,v8
v_mul_f32 v9,0x3c2aaaab,v9
v_cmp_ngt_f32 vcc,0x5d800000,v9
v_fma_f32 v9,-v8,v8,v9
v_max_f32 v9,0,v9
s_or_b64 s[90:91],s[90:91],vcc
v_add_f32 v9,0x3727c5ac,v9
v_rsq_f32 v9,v9
s_nop 0
v_mul_f32 v48,s96,v9
v_mul_f32 v49,s96,v9
v_mul_f32 v32,-v8,v48
v_mul_f32 v33,-v8,v48
v_permlane16_swap_b32 v48,v49
s_nop 0
v_permlane16_swap_b32 v32,v33
v_mov_b64 v[50:51],v[48:49]
v_mov_b64 v[34:35],v[32:33]
s_nop 0
v_permlane32_swap_b32 v48,v50
v_permlane32_swap_b32 v49,v51
v_permlane32_swap_b32 v32,v34
v_permlane32_swap_b32 v33,v35
v_pk_fma_f32 v[64:65],v[64:65],v[48:49],v[32:33]
v_pk_fma_f32 v[66:67],v[66:67],v[50:51],v[34:35]
v_pk_fma_f32 v[68:69],v[68:69],v[48:49],v[32:33]
v_pk_fma_f32 v[70:71],v[70:71],v[50:51],v[34:35]
v_pk_fma_f32 v[72:73],v[72:73],v[48:49],v[32:33]
v_pk_fma_f32 v[74:75],v[74:75],v[50:51],v[34:35]
v_pk_fma_f32 v[76:77],v[76:77],v[48:49],v[32:33]
v_pk_fma_f32 v[78:79],v[78:79],v[50:51],v[34:35]
v_pk_fma_f32 v[112:113],v[112:113],v[48:49],v[32:33]
v_pk_fma_f32 v[114:115],v[114:115],v[50:51],v[34:35]
v_pk_fma_f32 v[116:117],v[116:117],v[48:49],v[32:33]
v_pk_fma_f32 v[118:119],v[118:119],v[50:51],v[34:35]
v_pk_fma_f32 v[136:137],v[136:137],v[48:49],v[32:33]
v_pk_fma_f32 v[138:139],v[138:139],v[50:51],v[34:35]
v_pk_fma_f32 v[140:141],v[140:141],v[48:49],v[32:33]
v_pk_fma_f32 v[142:143],v[142:143],v[50:51],v[34:35]
v_pk_fma_f32 v[80:81],v[80:81],v[48:49],v[32:33]
v_pk_fma_f32 v[82:83],v[82:83],v[50:51],v[34:35]
v_pk_fma_f32 v[84:85],v[84:85],v[48:49],v[32:33]
v_pk_fma_f32 v[86:87],v[86:87],v[50:51],v[34:35]
v_pk_fma_f32 v[88:89],v[88:89],v[48:49],v[32:33]
v_pk_fma_f32 v[90:91],v[90:91],v[50:51],v[34:35]
v_pk_fma_f32 v[92:93],v[92:93],v[48:49],v[32:33]
v_pk_fma_f32 v[94:95],v[94:95],v[50:51],v[34:35]
v_pk_fma_f32 v[120:121],v[120:121],v[48:49],v[32:33]
v_pk_fma_f32 v[122:123],v[122:123],v[50:51],v[34:35]
v_pk_fma_f32 v[124:125],v[124:125],v[48:49],v[32:33]
v_pk_fma_f32 v[126:127],v[126:127],v[50:51],v[34:35]
v_pk_fma_f32 v[144:145],v[144:145],v[48:49],v[32:33]
v_pk_fma_f32 v[146:147],v[146:147],v[50:51],v[34:35]
v_pk_fma_f32 v[148:149],v[148:149],v[48:49],v[32:33]
v_pk_fma_f32 v[150:151],v[150:151],v[50:51],v[34:35]
v_pk_fma_f32 v[96:97],v[96:97],v[48:49],v[32:33]
v_pk_fma_f32 v[98:99],v[98:99],v[50:51],v[34:35]
v_pk_fma_f32 v[100:101],v[100:101],v[48:49],v[32:33]
v_pk_fma_f32 v[102:103],v[102:103],v[50:51],v[34:35]
v_pk_fma_f32 v[104:105],v[104:105],v[48:49],v[32:33]
v_pk_fma_f32 v[106:107],v[106:107],v[50:51],v[34:35]
v_pk_fma_f32 v[108:109],v[108:109],v[48:49],v[32:33]
v_pk_fma_f32 v[110:111],v[110:111],v[50:51],v[34:35]
v_pk_fma_f32 v[128:129],v[128:129],v[48:49],v[32:33]
v_pk_fma_f32 v[130:131],v[130:131],v[50:51],v[34:35]
v_pk_fma_f32 v[132:133],v[132:133],v[48:49],v[32:33]
v_pk_fma_f32 v[134:135],v[134:135],v[50:51],v[34:35]
v_pk_fma_f32 v[152:153],v[152:153],v[48:49],v[32:33]
v_pk_fma_f32 v[154:155],v[154:155],v[50:51],v[34:35]
v_pk_fma_f32 v[156:157],v[156:157],v[48:49],v[32:33]
v_pk_fma_f32 v[158:159],v[158:159],v[50:51],v[34:35]
v_mfma_f32_16x16x32_bf16 v[44:47],a[220:223],a[56:59],v[16:19]
buffer_store_dwordx4 a[36:39],v7,s[40:43],s9 offen offset:192 sc1
v_cvt_pk_bf16_f32 v64,v64,v68
v_cvt_pk_bf16_f32 v72,v72,v76
v_cvt_pk_bf16_f32 v112,v112,v116
v_lshlrev_b32_sdwa v36,1,v36 dst_sel:DWORD dst_unused:UNUSED_PAD src0_sel:DWORD src1_sel:WORD_1
v_mfma_f32_16x16x32_bf16 v[44:47],a[216:219],a[72:75],v[44:47]
ds_read_u16 v36,v36
v_accvgpr_write_b32 a0,v64
v_accvgpr_write_b32 a1,v72
v_cvt_pk_bf16_f32 v136,v136,v140
v_accvgpr_write_b32 a2,v112
v_mfma_f32_16x16x32_bf16 v[44:47],a[192:195],a[88:91],v[44:47]
ds_read_b64_tr_b16 a[204:205],v1 offset:0
v_cvt_pk_bf16_f32 v65,v65,v69
v_accvgpr_write_b32 a3,v136
v_cvt_pk_bf16_f32 v73,v73,v77
v_accvgpr_write_b32 a4,v65
v_mfma_f32_16x16x32_bf16 v[48:51],a[196:199],a[88:91],v[20:23]
ds_read_b64_tr_b16 a[206:207],v1 offset:1024
v_cvt_pk_bf16_f32 v113,v113,v117
v_accvgpr_write_b32 a5,v73
v_cvt_pk_bf16_f32 v137,v137,v141
v_lshlrev_b32_sdwa v38,1,v38 dst_sel:DWORD dst_unused:UNUSED_PAD src0_sel:DWORD src1_sel:WORD_1
v_accvgpr_write_b32 a6,v113
v_mfma_f32_16x16x32_bf16 v[48:51],a[200:203],a[72:75],v[48:51]
ds_read_u16_d16_hi v38,v38
v_cvt_pk_bf16_f32 v66,v66,v70
v_accvgpr_write_b32 a7,v137
v_cvt_pk_bf16_f32 v74,v74,v78
v_accvgpr_write_b32 a8,v66
v_mfma_f32_16x16x32_bf16 v[48:51],a[212:215],a[56:59],v[48:51]
ds_read_b64_tr_b16 a[208:209],v1 offset:2048
v_accvgpr_write_b32 a9,v74
v_cvt_pk_bf16_f32 v114,v114,v118
v_accvgpr_write_b32 a10,v114
v_lshlrev_b32_sdwa v37,1,v37 dst_sel:DWORD dst_unused:UNUSED_PAD src0_sel:DWORD src1_sel:WORD_1
v_cvt_pk_bf16_f32 v138,v138,v142
v_mfma_f32_16x16x32_bf16 v[32:35],a[212:215],a[60:63],v[20:23]
buffer_store_dwordx4 a[40:43],v7,s[40:43],s9 offen offset:256 sc1
v_accvgpr_write_b32 a11,v138
v_cvt_pk_bf16_f32 v67,v67,v71
v_accvgpr_write_b32 a12,v67
v_mfma_f32_16x16x32_bf16 v[32:35],a[200:203],a[76:79],v[32:35]
ds_read_u16 v37,v37
v_cvt_pk_bf16_f32 v75,v75,v79
v_accvgpr_write_b32 a13,v75
v_cvt_pk_bf16_f32 v115,v115,v119
v_accvgpr_write_b32 a14,v115
v_lshlrev_b32_sdwa v39,1,v39 dst_sel:DWORD dst_unused:UNUSED_PAD src0_sel:DWORD src1_sel:WORD_1
v_mfma_f32_16x16x32_bf16 v[32:35],a[196:199],a[92:95],v[32:35]
s_waitcnt lgkmcnt(6)
ds_read_u16_d16_hi v39,v39
v_cvt_pk_bf16_f32 v139,v139,v143
v_accvgpr_write_b32 a15,v139
v_cvt_pk_bf16_f32 v80,v80,v84
v_cvt_pk_bf16_f32 v88,v88,v92
v_mfma_f32_16x16x32_bf16 v[136:139],a[248:251],a[228:231],v[24:27]
s_nop 0
ds_read_b64_tr_b16 a[210:211],v1 offset:3072
v_cvt_pk_bf16_f32 v120,v120,v124
v_accvgpr_write_b32 a16,v80
v_accvgpr_write_b32 a17,v88
v_cvt_pk_bf16_f32 v144,v144,v148
v_lshlrev_b32_sdwa v40,1,v40 dst_sel:DWORD dst_unused:UNUSED_PAD src0_sel:DWORD src1_sel:WORD_1
v_mfma_f32_16x16x32_bf16 v[140:143],a[248:251],a[232:235],v[24:27]
ds_read_u16 v40,v40
v_accvgpr_write_b32 a18,v120
v_cvt_pk_bf16_f32 v81,v81,v85
v_accvgpr_write_b32 a19,v144
v_cvt_pk_bf16_f32 v89,v89,v93
v_mfma_f32_16x16x32_bf16 v[112:115],a[248:251],a[236:239],v[24:27]
v_accvgpr_write_b32 a20,v81
v_cvt_pk_bf16_f32 v121,v121,v125
v_accvgpr_write_b32 a21,v89
v_lshlrev_b32_sdwa v42,1,v42 dst_sel:DWORD dst_unused:UNUSED_PAD src0_sel:DWORD src1_sel:WORD_1
v_cvt_pk_bf16_f32 v145,v145,v149
v_mfma_f32_16x16x32_bf16 v[116:119],a[248:251],a[244:247],v[24:27]
ds_read_b128 a[248:251],v13 offset:512
ds_read_b128 v[24:27],v4 offset:3712
v_accvgpr_write_b32 a22,v121
v_cvt_pk_bf16_f32 v82,v82,v86
v_accvgpr_write_b32 a23,v145
v_mfma_f32_16x16x32_bf16 v[64:67],a[252:255],a[228:231],v[28:31]
ds_read_b64 a[228:229],v60 offset:57408
ds_read_b64 a[230:231],v60 offset:57416
v_cvt_pk_bf16_f32 v90,v90,v94
v_accvgpr_write_b32 a24,v82
v_accvgpr_write_b32 a25,v90
v_cvt_pk_bf16_f32 v122,v122,v126
v_lshlrev_b32_sdwa v41,1,v41 dst_sel:DWORD dst_unused:UNUSED_PAD src0_sel:DWORD src1_sel:WORD_1
v_mfma_f32_16x16x32_bf16 v[68:71],a[252:255],a[232:235],v[28:31]
ds_read_b64 a[232:233],v61 offset:57408
ds_read_b64 a[234:235],v61 offset:57416
v_accvgpr_write_b32 a26,v122
v_cvt_pk_bf16_f32 v146,v146,v150
v_accvgpr_write_b32 a27,v146
v_cvt_pk_bf16_f32 v83,v83,v87
v_mfma_f32_16x16x32_bf16 v[72:75],a[252:255],a[236:239],v[28:31]
ds_read_b64 a[236:237],v62 offset:57408
ds_read_b64 a[238:239],v62 offset:57416
v_accvgpr_write_b32 a28,v83
v_cvt_pk_bf16_f32 v91,v91,v95
v_accvgpr_write_b32 a29,v91
v_cvt_pk_bf16_f32 v123,v123,v127
v_lshlrev_b32_sdwa v43,1,v43 dst_sel:DWORD dst_unused:UNUSED_PAD src0_sel:DWORD src1_sel:WORD_1
v_mfma_f32_16x16x32_bf16 v[76:79],a[252:255],a[244:247],v[28:31]
s_waitcnt lgkmcnt(4)
buffer_store_dwordx4 a[44:47],v7,s[40:43],s9 offen offset:320 sc1
ds_read_b64 a[244:245],v63 offset:57408
v_accvgpr_write_b32 a30,v123
v_cvt_pk_bf16_f32 v147,v147,v151
v_accvgpr_write_b32 a31,v147
v_cvt_pk_bf16_f32 v96,v96,v100
v_mfma_f32_16x16x32_bf16 v[144:147],a[248:251],a[228:231],v[24:27]
s_waitcnt lgkmcnt(3)
ds_read_b64 a[246:247],v63 offset:57416
ds_read_b128 a[252:255],v13 offset:768
v_cvt_pk_bf16_f32 v104,v104,v108
v_cvt_pk_bf16_f32 v128,v128,v132
v_accvgpr_write_b32 a32,v96
v_accvgpr_write_b32 a33,v104
v_lshlrev_b32_sdwa v52,1,v52 dst_sel:DWORD dst_unused:UNUSED_PAD src0_sel:DWORD src1_sel:WORD_1
v_mfma_f32_16x16x32_bf16 v[148:151],a[248:251],a[232:235],v[24:27]
s_waitcnt lgkmcnt(3)
ds_read_b128 v[28:31],v4 offset:3744
ds_read_u16_d16_hi v42,v42
v_cvt_pk_bf16_f32 v152,v152,v156
v_accvgpr_write_b32 a34,v128
v_cvt_pk_bf16_f32 v97,v97,v101
v_mfma_f32_16x16x32_bf16 v[120:123],a[248:251],a[236:239],v[24:27]
s_waitcnt lgkmcnt(3)
ds_read_u16 v41,v41
v_accvgpr_write_b32 a35,v152
v_cvt_pk_bf16_f32 v105,v105,v109
v_accvgpr_write_b32 a36,v97
v_cvt_pk_bf16_f32 v129,v129,v133
v_lshlrev_b32_sdwa v54,1,v54 dst_sel:DWORD dst_unused:UNUSED_PAD src0_sel:DWORD src1_sel:WORD_1
v_mfma_f32_16x16x32_bf16 v[124:127],a[248:251],a[244:247],v[24:27]
s_waitcnt lgkmcnt(2)
ds_read_b128 a[248:251],v13 offset:1024
ds_read_b128 v[24:27],v4 offset:3840
v_accvgpr_write_b32 a37,v105
v_cvt_pk_bf16_f32 v153,v153,v157
v_accvgpr_write_b32 a38,v129
v_cvt_pk_bf16_f32 v98,v98,v102
v_mfma_f32_16x16x32_bf16 v[80:83],a[252:255],a[228:231],v[28:31]
s_nop 0
ds_read_b64 a[228:229],v60 offset:57472
ds_read_b64 a[230:231],v60 offset:57480
v_accvgpr_write_b32 a39,v153
v_cvt_pk_bf16_f32 v106,v106,v110
v_accvgpr_write_b32 a40,v98
v_accvgpr_write_b32 a41,v106
v_lshlrev_b32_sdwa v53,1,v53 dst_sel:DWORD dst_unused:UNUSED_PAD src0_sel:DWORD src1_sel:WORD_1
v_mfma_f32_16x16x32_bf16 v[84:87],a[252:255],a[232:235],v[28:31]
ds_read_b64 a[232:233],v61 offset:57472
ds_read_b64 a[234:235],v61 offset:57480
v_cvt_pk_bf16_f32 v130,v130,v134
v_accvgpr_write_b32 a42,v130
v_cvt_pk_bf16_f32 v154,v154,v158
v_accvgpr_write_b32 a43,v154
v_mfma_f32_16x16x32_bf16 v[88:91],a[252:255],a[236:239],v[28:31]
s_nop 0
ds_read_b64 a[236:237],v62 offset:57472
ds_read_b64 a[238:239],v62 offset:57480
v_cvt_pk_bf16_f32 v99,v99,v103
v_accvgpr_write_b32 a44,v99
v_cvt_pk_bf16_f32 v107,v107,v111
v_accvgpr_write_b32 a45,v107
v_mfma_f32_16x16x32_bf16 v[92:95],a[252:255],a[244:247],v[28:31]
s_waitcnt lgkmcnt(4)
ds_read_b64 a[244:245],v63 offset:57472
ds_read_b64 a[246:247],v63 offset:57480
v_cvt_pk_bf16_f32 v131,v131,v135
v_accvgpr_write_b32 a46,v131
v_cvt_pk_bf16_f32 v155,v155,v159
v_lshlrev_b32_sdwa v55,1,v55 dst_sel:DWORD dst_unused:UNUSED_PAD src0_sel:DWORD src1_sel:WORD_1
v_accvgpr_write_b32 a47,v155
v_mfma_f32_16x16x32_bf16 v[152:155],a[248:251],a[228:231],v[24:27]
s_waitcnt lgkmcnt(4)
ds_read_b128 a[252:255],v13 offset:1280
ds_read_b128 v[28:31],v4 offset:3872
v_mfma_f32_16x16x32_bf16 v[156:159],a[248:251],a[232:235],v[24:27]
s_waitcnt lgkmcnt(4)
ds_read_u16_d16_hi v43,v43
ds_read_u16 v52,v52
v_lshlrev_b32_sdwa v56,1,v56 dst_sel:DWORD dst_unused:UNUSED_PAD src0_sel:DWORD src1_sel:WORD_1
v_mfma_f32_16x16x32_bf16 v[128:131],a[248:251],a[236:239],v[24:27]
s_waitcnt lgkmcnt(4)
ds_read_u16_d16_hi v54,v54
v_lshlrev_b32_sdwa v58,1,v58 dst_sel:DWORD dst_unused:UNUSED_PAD src0_sel:DWORD src1_sel:WORD_1
v_mfma_f32_16x16x32_bf16 v[132:135],a[248:251],a[244:247],v[24:27]
s_waitcnt lgkmcnt(3)
ds_read_b64_tr_b16 a[248:249],v3 offset:64
ds_read_b64_tr_b16 a[250:251],v3 offset:1600
v_mfma_f32_16x16x32_bf16 v[96:99],a[252:255],a[228:231],v[28:31]
ds_read_b64_tr_b16 a[228:229],v3 offset:72
ds_read_b64_tr_b16 a[230:231],v3 offset:1608
v_mfma_f32_16x16x32_bf16 v[100:103],a[252:255],a[232:235],v[28:31]
ds_read_b64_tr_b16 a[232:233],v3 offset:128
ds_read_b64_tr_b16 a[234:235],v3 offset:1664
v_lshlrev_b32_sdwa v57,1,v57 dst_sel:DWORD dst_unused:UNUSED_PAD src0_sel:DWORD src1_sel:WORD_1
v_mfma_f32_16x16x32_bf16 v[104:107],a[252:255],a[236:239],v[28:31]
ds_read_b64_tr_b16 a[236:237],v3 offset:136
ds_read_b64_tr_b16 a[238:239],v3 offset:1672
v_mfma_f32_16x16x32_bf16 v[108:111],a[252:255],a[244:247],v[28:31]
ds_read_u16 v53,v53
ds_read_u16_d16_hi v55,v55
v_lshlrev_b32_sdwa v59,1,v59 dst_sel:DWORD dst_unused:UNUSED_PAD src0_sel:DWORD src1_sel:WORD_1
v_mfma_f32_16x16x32_bf16 v[60:63],a[192:195],a[92:95],v[16:19]
ds_read_u16 v56,v56
v_mfma_f32_16x16x32_bf16 v[60:63],a[216:219],a[76:79],v[60:63]
s_waitcnt vmcnt(18)
ds_read_u16_d16_hi v58,v58
v_mfma_f32_16x16x32_bf16 v[60:63],a[220:223],a[60:63],v[60:63]
s_barrier
ds_read_u16 v57,v57
ds_read_u16_d16_hi v59,v59
v_mfma_f32_16x16x32_bf16 v[24:27],a[220:223],a[0:3],v[16:19]
s_add_u32 m0,s57,24752
v_lshlrev_b32_sdwa v44,1,v44 dst_sel:DWORD dst_unused:UNUSED_PAD src0_sel:DWORD src1_sel:WORD_1
v_lshlrev_b32_sdwa v46,1,v46 dst_sel:DWORD dst_unused:UNUSED_PAD src0_sel:DWORD src1_sel:WORD_1
v_mfma_f32_16x16x32_bf16 v[24:27],a[216:219],a[16:19],v[24:27]
s_add_u32 s89,s56,5603328
ds_read_u16 v44,v44
v_lshl_add_u64 v[36:37],v[36:37],0,v[38:39]
v_mfma_f32_16x16x32_bf16 v[24:27],a[192:195],a[32:35],v[24:27]
s_waitcnt lgkmcnt(11)
buffer_load_dwordx4 v6,s[36:39],s89 offen lds
v_lshl_add_u64 v[38:39],v[40:41],0,v[42:43]
v_mfma_f32_16x16x32_bf16 v[28:31],a[196:199],a[32:35],v[20:23]
ds_read_u16_d16_hi v46,v46
v_lshlrev_b32_sdwa v45,1,v45 dst_sel:DWORD dst_unused:UNUSED_PAD src0_sel:DWORD src1_sel:WORD_1
v_mfma_f32_16x16x32_bf16 v[28:31],a[200:203],a[16:19],v[28:31]
s_waitcnt lgkmcnt(6)
ds_read_u16 v45,v45
v_lshl_add_u64 v[40:41],v[52:53],0,v[54:55]
v_mfma_f32_16x16x32_bf16 v[28:31],a[212:215],a[0:3],v[28:31]
v_lshlrev_b32_sdwa v47,1,v47 dst_sel:DWORD dst_unused:UNUSED_PAD src0_sel:DWORD src1_sel:WORD_1
v_lshlrev_b32_sdwa v48,1,v48 dst_sel:DWORD dst_unused:UNUSED_PAD src0_sel:DWORD src1_sel:WORD_1
v_mfma_f32_16x16x32_bf16 v[52:55],a[212:215],a[4:7],v[20:23]
s_nop 0
ds_read_u16_d16_hi v47,v47
v_lshlrev_b32_sdwa v50,1,v50 dst_sel:DWORD dst_unused:UNUSED_PAD src0_sel:DWORD src1_sel:WORD_1
v_mfma_f32_16x16x32_bf16 v[52:55],a[200:203],a[20:23],v[52:55]
s_waitcnt lgkmcnt(4)
ds_read_u16 v48,v48
v_lshl_add_u64 v[42:43],v[56:57],0,v[58:59]
v_mfma_f32_16x16x32_bf16 v[52:55],a[196:199],a[36:39],v[52:55]
ds_read_u16_d16_hi v50,v50
v_lshlrev_b32_sdwa v49,1,v49 dst_sel:DWORD dst_unused:UNUSED_PAD src0_sel:DWORD src1_sel:WORD_1
v_mfma_f32_16x16x32_bf16 v[56:59],a[192:195],a[36:39],v[16:19]
ds_read_u16 v49,v49
v_lshlrev_b32_sdwa v51,1,v51 dst_sel:DWORD dst_unused:UNUSED_PAD src0_sel:DWORD src1_sel:WORD_1
v_mfma_f32_16x16x32_bf16 v[56:59],a[216:219],a[20:23],v[56:59]
ds_read_u16_d16_hi v51,v51
v_lshlrev_b32_sdwa v60,1,v60 dst_sel:DWORD dst_unused:UNUSED_PAD src0_sel:DWORD src1_sel:WORD_1
v_mfma_f32_16x16x32_bf16 v[56:59],a[220:223],a[4:7],v[56:59]
ds_read_u16 v60,v60
v_lshlrev_b32_sdwa v62,1,v62 dst_sel:DWORD dst_unused:UNUSED_PAD src0_sel:DWORD src1_sel:WORD_1
v_mfma_f32_16x16x32_bf16 v[232:235],a[240:243],v[36:39],v[232:235]
ds_read_u16_d16_hi v62,v62
v_lshlrev_b32_sdwa v61,1,v61 dst_sel:DWORD dst_unused:UNUSED_PAD src0_sel:DWORD src1_sel:WORD_1
v_mfma_f32_16x16x32_bf16 v[236:239],a[240:243],v[40:43],v[236:239]
ds_read_u16 v61,v61
v_lshlrev_b32_sdwa v63,1,v63 dst_sel:DWORD dst_unused:UNUSED_PAD src0_sel:DWORD src1_sel:WORD_1
v_mfma_f32_16x16x32_bf16 v[164:167],a[224:227],v[40:43],v[164:167]
ds_read_u16_d16_hi v63,v63
v_lshlrev_b32_sdwa v32,1,v32 dst_sel:DWORD dst_unused:UNUSED_PAD src0_sel:DWORD src1_sel:WORD_1
v_mfma_f32_16x16x32_bf16 v[160:163],a[224:227],v[36:39],v[160:163]
ds_read_u16 v32,v32
v_lshlrev_b32_sdwa v34,1,v34 dst_sel:DWORD dst_unused:UNUSED_PAD src0_sel:DWORD src1_sel:WORD_1
v_mfma_f32_16x16x32_bf16 v[240:243],a[248:251],v[36:39],v[240:243]
ds_read_u16_d16_hi v34,v34
v_lshlrev_b32_sdwa v33,1,v33 dst_sel:DWORD dst_unused:UNUSED_PAD src0_sel:DWORD src1_sel:WORD_1
v_mfma_f32_16x16x32_bf16 v[244:247],a[248:251],v[40:43],v[244:247]
ds_read_u16 v33,v33
v_lshlrev_b32_sdwa v35,1,v35 dst_sel:DWORD dst_unused:UNUSED_PAD src0_sel:DWORD src1_sel:WORD_1
v_mfma_f32_16x16x32_bf16 v[180:183],a[228:231],v[40:43],v[180:183]
ds_read_u16_d16_hi v35,v35
v_mfma_f32_16x16x32_bf16 v[176:179],a[228:231],v[36:39],v[176:179]
v_mfma_f32_16x16x32_bf16 v[248:251],a[232:235],v[36:39],v[248:251]
v_mfma_f32_16x16x32_bf16 v[252:255],a[232:235],v[40:43],v[252:255]
v_mfma_f32_16x16x32_bf16 v[196:199],a[236:239],v[40:43],v[196:199]
v_mfma_f32_16x16x32_bf16 v[192:195],a[236:239],v[36:39],v[192:195]
v_mfma_f32_16x16x32_bf16 v[40:43],a[220:223],a[8:11],v[16:19]
v_lshlrev_b32_sdwa v24,1,v24 dst_sel:DWORD dst_unused:UNUSED_PAD src0_sel:DWORD src1_sel:WORD_1
v_fma_mixlo_f16 v8,v53,s99,0
v_mfma_f32_16x16x32_bf16 v[40:43],a[216:219],a[24:27],v[40:43]
s_waitcnt lgkmcnt(2)
ds_read_u16 v24,v24
v_lshl_add_u64 v[44:45],v[44:45],0,v[46:47]
v_mfma_f32_16x16x32_bf16 v[40:43],a[192:195],a[40:43],v[40:43]
v_lshl_add_u64 v[46:47],v[48:49],0,v[50:51]
v_lshlrev_b32_sdwa v26,1,v26 dst_sel:DWORD dst_unused:UNUSED_PAD src0_sel:DWORD src1_sel:WORD_1
v_mfma_f32_16x16x32_bf16 v[36:39],a[196:199],a[40:43],v[20:23]
ds_read_u16_d16_hi v26,v26
v_fma_mixhi_f16 v8,v55,s99,0
v_mfma_f32_16x16x32_bf16 v[36:39],a[200:203],a[24:27],v[36:39]
v_lshl_add_u64 v[48:49],v[60:61],0,v[62:63]
v_lshlrev_b32_sdwa v25,1,v25 dst_sel:DWORD dst_unused:UNUSED_PAD src0_sel:DWORD src1_sel:WORD_1
v_pk_fma_f16 v9,v8,v8,s54
v_mfma_f32_16x16x32_bf16 v[36:39],a[212:215],a[8:11],v[36:39]
ds_read_u16 v25,v25
v_lshlrev_b32_sdwa v27,1,v27 dst_sel:DWORD dst_unused:UNUSED_PAD src0_sel:DWORD src1_sel:WORD_1
v_mfma_f32_16x16x32_bf16 v[60:63],a[212:215],a[12:15],v[20:23]
ds_read_b128 v[20:23],v4 offset:128
ds_read_b64_tr_b16 a[212:213],v1 offset:4096
v_pk_mul_f16 v9,v8,v9
v_mfma_f32_16x16x32_bf16 v[60:63],a[200:203],a[28:31],v[60:63]
s_waitcnt lgkmcnt(5)
ds_read_b64_tr_b16 a[214:215],v1 offset:5120
ds_read_b64_tr_b16 a[200:201],v2 offset:4096
v_lshl_add_u64 v[50:51],v[32:33],0,v[34:35]
v_lshlrev_b32_sdwa v28,1,v28 dst_sel:DWORD dst_unused:UNUSED_PAD src0_sel:DWORD src1_sel:WORD_1
v_mfma_f32_16x16x32_bf16 v[60:63],a[196:199],a[44:47],v[60:63]
ds_read_b64_tr_b16 a[202:203],v2 offset:5120
ds_read_b64_tr_b16 a[196:197],v2 offset:2048
v_exp_f16 v8,v9
v_mfma_f32_16x16x32_bf16 v[32:35],a[192:195],a[44:47],v[16:19]
ds_read_b128 v[16:19],v4 offset:160
ds_read_b64_tr_b16 a[198:199],v2 offset:3072
v_lshlrev_b32_sdwa v30,1,v30 dst_sel:DWORD dst_unused:UNUSED_PAD src0_sel:DWORD src1_sel:WORD_1
v_mfma_f32_16x16x32_bf16 v[32:35],a[216:219],a[28:31],v[32:35]
ds_read_b64_tr_b16 a[192:193],v2 offset:0
ds_read_b64_tr_b16 a[194:195],v2 offset:1024
v_lshlrev_b32_sdwa v29,1,v29 dst_sel:DWORD dst_unused:UNUSED_PAD src0_sel:DWORD src1_sel:WORD_1
v_mfma_f32_16x16x32_bf16 v[32:35],a[220:223],a[12:15],v[32:35]
ds_read_u16_d16_hi v27,v27
v_exp_f16_sdwa v8,v9 dst_sel:WORD_1 dst_unused:UNUSED_PRESERVE src0_sel:WORD_1
v_mfma_f32_16x16x32_bf16 v[208:211],a[240:243],v[44:47],v[208:211]
ds_read_u16 v28,v28
v_lshlrev_b32_sdwa v31,1,v31 dst_sel:DWORD dst_unused:UNUSED_PAD src0_sel:DWORD src1_sel:WORD_1
v_mfma_f32_16x16x32_bf16 v[212:215],a[240:243],v[48:51],v[212:215]
ds_read_u16_d16_hi v30,v30
v_lshlrev_b32_sdwa v56,1,v56 dst_sel:DWORD dst_unused:UNUSED_PAD src0_sel:DWORD src1_sel:WORD_1
v_mfma_f32_16x16x32_bf16 v[172:175],a[224:227],v[48:51],v[172:175]
ds_read_u16 v29,v29
v_pk_fma_f16 v8,v8,s55,s55
v_mfma_f32_16x16x32_bf16 v[168:171],a[224:227],v[44:47],v[168:171]
v_rcp_f16 v9,v8
v_lshlrev_b32_sdwa v58,1,v58 dst_sel:DWORD dst_unused:UNUSED_PAD src0_sel:DWORD src1_sel:WORD_1
v_mfma_f32_16x16x32_bf16 v[216:219],a[248:251],v[44:47],v[216:219]
ds_read_u16_d16_hi v31,v31
v_lshlrev_b32_sdwa v57,1,v57 dst_sel:DWORD dst_unused:UNUSED_PAD src0_sel:DWORD src1_sel:WORD_1
v_mfma_f32_16x16x32_bf16 v[220:223],a[248:251],v[48:51],v[220:223]
ds_read_u16 v56,v56
v_lshlrev_b32_sdwa v59,1,v59 dst_sel:DWORD dst_unused:UNUSED_PAD src0_sel:DWORD src1_sel:WORD_1
v_mfma_f32_16x16x32_bf16 v[188:191],a[228:231],v[48:51],v[188:191]
ds_read_u16_d16_hi v58,v58
v_lshlrev_b32_sdwa v52,1,v52 dst_sel:DWORD dst_unused:UNUSED_PAD src0_sel:DWORD src1_sel:WORD_1
v_mfma_f32_16x16x32_bf16 v[184:187],a[228:231],v[44:47],v[184:187]
v_rcp_f16_sdwa v9,v8 dst_sel:WORD_1 dst_unused:UNUSED_PRESERVE src0_sel:WORD_1
v_mfma_f32_16x16x32_bf16 v[224:227],a[232:235],v[44:47],v[224:227]
ds_read_u16 v57,v57
ds_read_u16_d16_hi v59,v59
v_mfma_f32_16x16x32_bf16 v[228:231],a[232:235],v[48:51],v[228:231]
ds_read_u16 v52,v52
v_lshlrev_b32_sdwa v54,1,v54 dst_sel:DWORD dst_unused:UNUSED_PAD src0_sel:DWORD src1_sel:WORD_1
v_mfma_f32_16x16x32_bf16 v[204:207],a[236:239],v[48:51],v[204:207]
ds_read_u16_d16_hi v54,v54
v_fma_mix_f32 v53,v53,v9,0 op_sel_hi:[0,1,0]
v_mfma_f32_16x16x32_bf16 v[200:203],a[236:239],v[44:47],v[200:203]
s_waitcnt lgkmcnt(7)
v_fma_mix_f32 v55,v55,v9,0 op_sel:[0,1,0] op_sel_hi:[0,1,0]
v_mfma_f32_16x16x32_bf16 v[48:51],a[204:207],a[48:51],v[20:23]
s_add_u32 m0,s57,28296
v_lshlrev_b32_sdwa v40,1,v40 dst_sel:DWORD dst_unused:UNUSED_PAD src0_sel:DWORD src1_sel:WORD_1
v_lshlrev_b32_sdwa v42,1,v42 dst_sel:DWORD dst_unused:UNUSED_PAD src0_sel:DWORD src1_sel:WORD_1
v_mfma_f32_16x16x32_bf16 v[48:51],a[208:211],a[64:67],v[48:51]
s_add_u32 s89,s56,5701632
ds_read_u16 v40,v40
v_lshl_add_u64 v[24:25],v[24:25],0,v[26:27]
v_mfma_f32_16x16x32_bf16 v[48:51],a[212:215],a[80:83],v[48:51]
s_waitcnt lgkmcnt(6)
buffer_load_dwordx4 v6,s[36:39],s89 offen lds
v_lshl_add_u64 v[26:27],v[28:29],0,v[30:31]
v_mfma_f32_16x16x32_bf16 v[44:47],a[200:203],a[80:83],v[16:19]
ds_read_u16_d16_hi v42,v42
v_lshlrev_b32_sdwa v41,1,v41 dst_sel:DWORD dst_unused:UNUSED_PAD src0_sel:DWORD src1_sel:WORD_1
v_mfma_f32_16x16x32_bf16 v[44:47],a[196:199],a[64:67],v[44:47]
s_waitcnt lgkmcnt(4)
ds_read_u16 v41,v41
v_lshl_add_u64 v[28:29],v[56:57],0,v[58:59]
v_mfma_f32_16x16x32_bf16 v[44:47],a[192:195],a[48:51],v[44:47]
v_lshlrev_b32_sdwa v43,1,v43 dst_sel:DWORD dst_unused:UNUSED_PAD src0_sel:DWORD src1_sel:WORD_1
v_lshlrev_b32_sdwa v36,1,v36 dst_sel:DWORD dst_unused:UNUSED_PAD src0_sel:DWORD src1_sel:WORD_1
v_mfma_f32_16x16x32_bf16 v[56:59],a[192:195],a[52:55],v[16:19]
s_waitcnt lgkmcnt(3)
ds_read_u16_d16_hi v43,v43
v_or_b32 v30,v52,v54
v_mfma_f32_16x16x32_bf16 v[56:59],a[196:199],a[68:71],v[56:59]
ds_read_u16 v36,v36
v_cvt_pk_bf16_f32 v31,v53,v55
v_lshlrev_b32_sdwa v38,1,v38 dst_sel:DWORD dst_unused:UNUSED_PAD src0_sel:DWORD src1_sel:WORD_1
v_mfma_f32_16x16x32_bf16 v[56:59],a[200:203],a[84:87],v[56:59]
ds_read_u16_d16_hi v38,v38
v_lshlrev_b32_sdwa v37,1,v37 dst_sel:DWORD dst_unused:UNUSED_PAD src0_sel:DWORD src1_sel:WORD_1
v_mfma_f32_16x16x32_bf16 v[52:55],a[212:215],a[84:87],v[20:23]
ds_read_u16 v37,v37
v_lshlrev_b32_sdwa v39,1,v39 dst_sel:DWORD dst_unused:UNUSED_PAD src0_sel:DWORD src1_sel:WORD_1
v_mfma_f32_16x16x32_bf16 v[52:55],a[208:211],a[68:71],v[52:55]
ds_read_u16_d16_hi v39,v39
v_lshlrev_b32_sdwa v32,1,v32 dst_sel:DWORD dst_unused:UNUSED_PAD src0_sel:DWORD src1_sel:WORD_1
v_mfma_f32_16x16x32_bf16 v[52:55],a[204:207],a[52:55],v[52:55]
s_add_u32 m0,s57,31832
ds_read_u16 v32,v32
v_lshlrev_b32_sdwa v34,1,v34 dst_sel:DWORD dst_unused:UNUSED_PAD src0_sel:DWORD src1_sel:WORD_1
v_mfma_f32_16x16x32_bf16 v[136:139],a[240:243],v[24:27],v[136:139]
s_add_u32 s89,s56,5799936
ds_read_u16_d16_hi v34,v34
v_lshlrev_b32_sdwa v33,1,v33 dst_sel:DWORD dst_unused:UNUSED_PAD src0_sel:DWORD src1_sel:WORD_1
v_mfma_f32_16x16x32_bf16 v[140:143],a[240:243],v[28:31],v[140:143]
buffer_load_dwordx4 v6,s[36:39],s89 offen lds
v_lshlrev_b32_sdwa v35,1,v35 dst_sel:DWORD dst_unused:UNUSED_PAD src0_sel:DWORD src1_sel:WORD_1
v_mfma_f32_16x16x32_bf16 v[68:71],a[224:227],v[28:31],v[68:71]
ds_read_u16 v33,v33
ds_read_u16_d16_hi v35,v35
v_mfma_f32_16x16x32_bf16 v[64:67],a[224:227],v[24:27],v[64:67]
ds_read_b64_tr_b16 a[244:245],v3 offset:6160
v_lshlrev_b32_sdwa v60,1,v60 dst_sel:DWORD dst_unused:UNUSED_PAD src0_sel:DWORD src1_sel:WORD_1
v_mfma_f32_16x16x32_bf16 v[144:147],a[248:251],v[24:27],v[144:147]
ds_read_u16 v60,v60
v_lshlrev_b32_sdwa v62,1,v62 dst_sel:DWORD dst_unused:UNUSED_PAD src0_sel:DWORD src1_sel:WORD_1
v_mfma_f32_16x16x32_bf16 v[148:151],a[248:251],v[28:31],v[148:151]
ds_read_u16_d16_hi v62,v62
v_lshlrev_b32_sdwa v61,1,v61 dst_sel:DWORD dst_unused:UNUSED_PAD src0_sel:DWORD src1_sel:WORD_1
v_mfma_f32_16x16x32_bf16 v[84:87],a[228:231],v[28:31],v[84:87]
ds_read_u16 v61,v61
v_lshlrev_b32_sdwa v63,1,v63 dst_sel:DWORD dst_unused:UNUSED_PAD src0_sel:DWORD src1_sel:WORD_1
v_mfma_f32_16x16x32_bf16 v[80:83],a[228:231],v[24:27],v[80:83]
ds_read_u16_d16_hi v63,v63
v_mfma_f32_16x16x32_bf16 v[152:155],a[232:235],v[24:27],v[152:155]
ds_read_b64_tr_b16 a[246:247],v3 offset:7696
v_mfma_f32_16x16x32_bf16 v[156:159],a[232:235],v[28:31],v[156:159]
TP5958_%=:
s_cbranch_execz TR0_%=
ds_read_b64_tr_b16 a[252:253],v3 offset:6168
v_mfma_f32_16x16x32_bf16 v[100:103],a[236:239],v[28:31],v[100:103]
ds_read_b64_tr_b16 a[254:255],v3 offset:7704
v_mfma_f32_16x16x32_bf16 v[96:99],a[236:239],v[24:27],v[96:99]
v_mfma_f32_16x16x32_bf16 v[28:31],a[204:207],a[56:59],v[20:23]
s_nop 0
ds_read_b64_tr_b16 a[216:217],v1 offset:6160
v_lshlrev_b32_sdwa v48,1,v48 dst_sel:DWORD dst_unused:UNUSED_PAD src0_sel:DWORD src1_sel:WORD_1
v_mfma_f32_16x16x32_bf16 v[28:31],a[208:211],a[72:75],v[28:31]
s_waitcnt lgkmcnt(7)
ds_read_u16 v48,v48
v_lshl_add_u64 v[40:41],v[40:41],0,v[42:43]
v_fma_mixlo_f16 v8,v57,s99,0
v_mfma_f32_16x16x32_bf16 v[28:31],a[212:215],a[88:91],v[28:31]
ds_read_b64_tr_b16 a[218:219],v1 offset:7184
v_lshl_add_u64 v[42:43],v[36:37],0,v[38:39]
v_lshlrev_b32_sdwa v50,1,v50 dst_sel:DWORD dst_unused:UNUSED_PAD src0_sel:DWORD src1_sel:WORD_1
v_mfma_f32_16x16x32_bf16 v[24:27],a[200:203],a[88:91],v[16:19]
ds_read_u16_d16_hi v50,v50
v_fma_mixhi_f16 v8,v59,s99,0
v_mfma_f32_16x16x32_bf16 v[24:27],a[196:199],a[72:75],v[24:27]
ds_read_b64_tr_b16 a[220:221],v1 offset:8208
v_lshl_add_u64 v[36:37],v[32:33],0,v[34:35]
v_lshlrev_b32_sdwa v49,1,v49 dst_sel:DWORD dst_unused:UNUSED_PAD src0_sel:DWORD src1_sel:WORD_1
v_pk_fma_f16 v9,v8,v8,s54
v_mfma_f32_16x16x32_bf16 v[24:27],a[192:195],a[56:59],v[24:27]
ds_read_u16 v49,v49
v_lshlrev_b32_sdwa v51,1,v51 dst_sel:DWORD dst_unused:UNUSED_PAD src0_sel:DWORD src1_sel:WORD_1
v_mfma_f32_16x16x32_bf16 v[32:35],a[192:195],a[60:63],v[16:19]
ds_read_u16_d16_hi v51,v51
v_pk_mul_f16 v9,v8,v9
v_mfma_f32_16x16x32_bf16 v[32:35],a[196:199],a[76:79],v[32:35]
s_waitcnt lgkmcnt(7)
ds_read_b64_tr_b16 a[222:223],v1 offset:9232
v_lshl_add_u64 v[38:39],v[60:61],0,v[62:63]
v_lshlrev_b32_sdwa v44,1,v44 dst_sel:DWORD dst_unused:UNUSED_PAD src0_sel:DWORD src1_sel:WORD_1
v_mfma_f32_16x16x32_bf16 v[32:35],a[200:203],a[92:95],v[32:35]
v_exp_f16 v8,v9
v_mfma_f32_16x16x32_bf16 v[60:63],a[212:215],a[92:95],v[20:23]
ds_read_u16 v44,v44
v_lshlrev_b32_sdwa v46,1,v46 dst_sel:DWORD dst_unused:UNUSED_PAD src0_sel:DWORD src1_sel:WORD_1
v_mfma_f32_16x16x32_bf16 v[60:63],a[208:211],a[76:79],v[60:63]
ds_read_u16_d16_hi v46,v46
v_lshlrev_b32_sdwa v45,1,v45 dst_sel:DWORD dst_unused:UNUSED_PAD src0_sel:DWORD src1_sel:WORD_1
v_mfma_f32_16x16x32_bf16 v[60:63],a[204:207],a[60:63],v[60:63]
v_exp_f16_sdwa v8,v9 dst_sel:WORD_1 dst_unused:UNUSED_PRESERVE src0_sel:WORD_1
v_mfma_f32_16x16x32_bf16 v[112:115],a[240:243],v[40:43],v[112:115]
ds_read_u16 v45,v45
v_lshlrev_b32_sdwa v47,1,v47 dst_sel:DWORD dst_unused:UNUSED_PAD src0_sel:DWORD src1_sel:WORD_1
v_mfma_f32_16x16x32_bf16 v[116:119],a[240:243],v[36:39],v[116:119]
ds_read_u16_d16_hi v47,v47
v_lshlrev_b32_sdwa v52,1,v52 dst_sel:DWORD dst_unused:UNUSED_PAD src0_sel:DWORD src1_sel:WORD_1
v_mfma_f32_16x16x32_bf16 v[76:79],a[224:227],v[36:39],v[76:79]
ds_read_u16 v52,v52
v_pk_fma_f16 v8,v8,s55,s55
v_mfma_f32_16x16x32_bf16 v[72:75],a[224:227],v[40:43],v[72:75]
ds_read_b64_tr_b16 a[240:241],v3 offset:6224
v_rcp_f16_e64 v9,v8
v_lshlrev_b32_sdwa v54,1,v54 dst_sel:DWORD dst_unused:UNUSED_PAD src0_sel:DWORD src1_sel:WORD_1
v_mfma_f32_16x16x32_bf16 v[120:123],a[248:251],v[40:43],v[120:123]
ds_read_u16_d16_hi v54,v54
v_lshlrev_b32_sdwa v53,1,v53 dst_sel:DWORD dst_unused:UNUSED_PAD src0_sel:DWORD src1_sel:WORD_1
v_mfma_f32_16x16x32_bf16 v[124:127],a[248:251],v[36:39],v[124:127]
ds_read_u16 v53,v53
ds_read_b64_tr_b16 a[242:243],v3 offset:7760
v_mfma_f32_16x16x32_bf16 v[92:95],a[228:231],v[36:39],v[92:95]
ds_read_b64_tr_b16 a[224:225],v3 offset:6232
v_lshlrev_b32_sdwa v55,1,v55 dst_sel:DWORD dst_unused:UNUSED_PAD src0_sel:DWORD src1_sel:WORD_1
v_mfma_f32_16x16x32_bf16 v[88:91],a[228:231],v[40:43],v[88:91]
ds_read_u16_d16_hi v55,v55
v_rcp_f16_sdwa v9,v8 dst_sel:WORD_1 dst_unused:UNUSED_PRESERVE src0_sel:WORD_1
v_mfma_f32_16x16x32_bf16 v[128:131],a[232:235],v[40:43],v[128:131]
ds_read_b64_tr_b16 a[226:227],v3 offset:7768
ds_read_b64_tr_b16 a[248:249],v3 offset:6288
v_lshlrev_b32_sdwa v56,1,v56 dst_sel:DWORD dst_unused:UNUSED_PAD src0_sel:DWORD src1_sel:WORD_1
v_mfma_f32_16x16x32_bf16 v[132:135],a[232:235],v[36:39],v[132:135]
ds_read_u16 v56,v56
v_lshlrev_b32_sdwa v58,1,v58 dst_sel:DWORD dst_unused:UNUSED_PAD src0_sel:DWORD src1_sel:WORD_1
v_mfma_f32_16x16x32_bf16 v[108:111],a[236:239],v[36:39],v[108:111]
ds_read_u16_d16_hi v58,v58
v_fma_mix_f32 v57,v57,v9,0 op_sel_hi:[0,1,0]
v_mfma_f32_16x16x32_bf16 v[104:107],a[236:239],v[40:43],v[104:107]
ds_read_b64_tr_b16 a[250:251],v3 offset:7824
v_fma_mix_f32 v59,v59,v9,0 op_sel:[0,1,0] op_sel_hi:[0,1,0]
v_mfma_f32_16x16x32_bf16 v[36:39],a[204:207],a[0:3],v[20:23]
s_add_u32 m0,s57,35360
ds_read_b64_tr_b16 a[228:229],v3 offset:6296
v_lshlrev_b32_sdwa v28,1,v28 dst_sel:DWORD dst_unused:UNUSED_PAD src0_sel:DWORD src1_sel:WORD_1
v_mfma_f32_16x16x32_bf16 v[36:39],a[208:211],a[16:19],v[36:39]
s_waitcnt lgkmcnt(10)
ds_read_b64_tr_b16 a[230:231],v3 offset:7832
v_lshl_add_u64 v[48:49],v[48:49],0,v[50:51]
v_mfma_f32_16x16x32_bf16 v[36:39],a[212:215],a[32:35],v[36:39]
s_add_u32 s89,s56,5898240
ds_read_u16 v28,v28
v_lshl_add_u64 v[50:51],v[44:45],0,v[46:47]
v_lshlrev_b32_sdwa v30,1,v30 dst_sel:DWORD dst_unused:UNUSED_PAD src0_sel:DWORD src1_sel:WORD_1
v_mfma_f32_16x16x32_bf16 v[40:43],a[200:203],a[32:35],v[16:19]
buffer_load_dwordx4 v6,s[36:39],s89 offen lds
v_lshlrev_b32_sdwa v29,1,v29 dst_sel:DWORD dst_unused:UNUSED_PAD src0_sel:DWORD src1_sel:WORD_1
v_mfma_f32_16x16x32_bf16 v[40:43],a[196:199],a[16:19],v[40:43]
s_waitcnt lgkmcnt(8)
ds_read_u16_d16_hi v30,v30
v_lshl_add_u64 v[44:45],v[52:53],0,v[54:55]
v_mfma_f32_16x16x32_bf16 v[40:43],a[192:195],a[0:3],v[40:43]
ds_read_u16 v29,v29
v_lshlrev_b32_sdwa v31,1,v31 dst_sel:DWORD dst_unused:UNUSED_PAD src0_sel:DWORD src1_sel:WORD_1
v_mfma_f32_16x16x32_bf16 v[52:55],a[192:195],a[4:7],v[16:19]
s_waitcnt lgkmcnt(6)
ds_read_u16_d16_hi v31,v31
v_or_b32 v46,v56,v58
v_lshlrev_b32_sdwa v24,1,v24 dst_sel:DWORD dst_unused:UNUSED_PAD src0_sel:DWORD src1_sel:WORD_1
v_mfma_f32_16x16x32_bf16 v[52:55],a[196:199],a[20:23],v[52:55]
ds_read_u16 v24,v24
v_cvt_pk_bf16_f32 v47,v57,v59
v_lshlrev_b32_sdwa v26,1,v26 dst_sel:DWORD dst_unused:UNUSED_PAD src0_sel:DWORD src1_sel:WORD_1
v_mfma_f32_16x16x32_bf16 v[52:55],a[200:203],a[36:39],v[52:55]
ds_read_u16_d16_hi v26,v26
v_lshlrev_b32_sdwa v25,1,v25 dst_sel:DWORD dst_unused:UNUSED_PAD src0_sel:DWORD src1_sel:WORD_1
v_mfma_f32_16x16x32_bf16 v[56:59],a[212:215],a[36:39],v[20:23]
ds_read_u16 v25,v25
v_lshlrev_b32_sdwa v27,1,v27 dst_sel:DWORD dst_unused:UNUSED_PAD src0_sel:DWORD src1_sel:WORD_1
v_mfma_f32_16x16x32_bf16 v[56:59],a[208:211],a[20:23],v[56:59]
ds_read_u16_d16_hi v27,v27
v_lshlrev_b32_sdwa v60,1,v60 dst_sel:DWORD dst_unused:UNUSED_PAD src0_sel:DWORD src1_sel:WORD_1
v_mfma_f32_16x16x32_bf16 v[56:59],a[204:207],a[4:7],v[56:59]
ds_read_u16 v60,v60
v_lshlrev_b32_sdwa v62,1,v62 dst_sel:DWORD dst_unused:UNUSED_PAD src0_sel:DWORD src1_sel:WORD_1
v_mfma_f32_16x16x32_bf16 v[232:235],a[244:247],v[48:51],v[232:235]
ds_read_u16_d16_hi v62,v62
v_lshlrev_b32_sdwa v61,1,v61 dst_sel:DWORD dst_unused:UNUSED_PAD src0_sel:DWORD src1_sel:WORD_1
v_mfma_f32_16x16x32_bf16 v[236:239],a[244:247],v[44:47],v[236:239]
ds_read_u16 v61,v61
v_lshlrev_b32_sdwa v63,1,v63 dst_sel:DWORD dst_unused:UNUSED_PAD src0_sel:DWORD src1_sel:WORD_1
v_mfma_f32_16x16x32_bf16 v[164:167],a[252:255],v[44:47],v[164:167]
ds_read_u16_d16_hi v63,v63
v_lshlrev_b32_sdwa v32,1,v32 dst_sel:DWORD dst_unused:UNUSED_PAD src0_sel:DWORD src1_sel:WORD_1
v_mfma_f32_16x16x32_bf16 v[160:163],a[252:255],v[48:51],v[160:163]
ds_read_u16 v32,v32
v_lshlrev_b32_sdwa v34,1,v34 dst_sel:DWORD dst_unused:UNUSED_PAD src0_sel:DWORD src1_sel:WORD_1
v_mfma_f32_16x16x32_bf16 v[240:243],a[240:243],v[48:51],v[240:243]
ds_read_u16_d16_hi v34,v34
v_lshlrev_b32_sdwa v33,1,v33 dst_sel:DWORD dst_unused:UNUSED_PAD src0_sel:DWORD src1_sel:WORD_1
v_mfma_f32_16x16x32_bf16 v[244:247],a[240:243],v[44:47],v[244:247]
ds_read_u16 v33,v33
v_lshlrev_b32_sdwa v35,1,v35 dst_sel:DWORD dst_unused:UNUSED_PAD src0_sel:DWORD src1_sel:WORD_1
v_mfma_f32_16x16x32_bf16 v[180:183],a[224:227],v[44:47],v[180:183]
ds_read_u16_d16_hi v35,v35
v_mfma_f32_16x16x32_bf16 v[176:179],a[224:227],v[48:51],v[176:179]
s_waitcnt lgkmcnt(8)
v_mfma_f32_16x16x32_bf16 v[248:251],a[248:251],v[48:51],v[248:251]
s_add_u32 m0,s57,38896
v_mfma_f32_16x16x32_bf16 v[252:255],a[248:251],v[44:47],v[252:255]
s_add_u32 s89,s56,5996544
v_mfma_f32_16x16x32_bf16 v[196:199],a[228:231],v[44:47],v[196:199]
buffer_load_dwordx4 v6,s[36:39],s89 offen lds
v_mfma_f32_16x16x32_bf16 v[192:195],a[228:231],v[48:51],v[192:195]
v_mfma_f32_16x16x32_bf16 v[44:47],a[204:207],a[8:11],v[20:23]
v_lshlrev_b32_sdwa v36,1,v36 dst_sel:DWORD dst_unused:UNUSED_PAD src0_sel:DWORD src1_sel:WORD_1
v_fma_mixlo_f16 v8,v53,s99,0
v_mfma_f32_16x16x32_bf16 v[44:47],a[208:211],a[24:27],v[44:47]
ds_read_u16 v36,v36
v_lshl_add_u64 v[28:29],v[28:29],0,v[30:31]
v_mfma_f32_16x16x32_bf16 v[44:47],a[212:215],a[40:43],v[44:47]
v_lshl_add_u64 v[30:31],v[24:25],0,v[26:27]
v_lshlrev_b32_sdwa v38,1,v38 dst_sel:DWORD dst_unused:UNUSED_PAD src0_sel:DWORD src1_sel:WORD_1
v_mfma_f32_16x16x32_bf16 v[48:51],a[200:203],a[40:43],v[16:19]
ds_read_u16_d16_hi v38,v38
v_fma_mixhi_f16 v8,v55,s99,0
v_mfma_f32_16x16x32_bf16 v[48:51],a[196:199],a[24:27],v[48:51]
s_waitcnt lgkmcnt(2)
v_lshl_add_u64 v[24:25],v[60:61],0,v[62:63]
v_lshlrev_b32_sdwa v37,1,v37 dst_sel:DWORD dst_unused:UNUSED_PAD src0_sel:DWORD src1_sel:WORD_1
v_pk_fma_f16 v9,v8,v8,s54
v_mfma_f32_16x16x32_bf16 v[48:51],a[192:195],a[8:11],v[48:51]
ds_read_u16 v37,v37
v_lshlrev_b32_sdwa v39,1,v39 dst_sel:DWORD dst_unused:UNUSED_PAD src0_sel:DWORD src1_sel:WORD_1
v_mfma_f32_16x16x32_bf16 v[60:63],a[192:195],a[12:15],v[16:19]
ds_read_b128 v[16:19],v4 offset:256
v_pk_mul_f16 v9,v8,v9
v_mfma_f32_16x16x32_bf16 v[60:63],a[196:199],a[28:31],v[60:63]
ds_read_u16_d16_hi v39,v39
ds_read_b64_tr_b16 a[192:193],v1 offset:10256
v_lshl_add_u64 v[26:27],v[32:33],0,v[34:35]
v_lshlrev_b32_sdwa v40,1,v40 dst_sel:DWORD dst_unused:UNUSED_PAD src0_sel:DWORD src1_sel:WORD_1
v_mfma_f32_16x16x32_bf16 v[60:63],a[200:203],a[44:47],v[60:63]
ds_read_b64_tr_b16 a[194:195],v1 offset:11280
v_exp_f16_e64 v8,v9
v_mfma_f32_16x16x32_bf16 v[32:35],a[212:215],a[44:47],v[20:23]
ds_read_b128 v[20:23],v4 offset:288
ds_read_u16 v40,v40
v_lshlrev_b32_sdwa v42,1,v42 dst_sel:DWORD dst_unused:UNUSED_PAD src0_sel:DWORD src1_sel:WORD_1
v_mfma_f32_16x16x32_bf16 v[32:35],a[208:211],a[28:31],v[32:35]
ds_read_u16_d16_hi v42,v42
ds_read_b64_tr_b16 a[196:197],v2 offset:10256
v_lshlrev_b32_sdwa v41,1,v41 dst_sel:DWORD dst_unused:UNUSED_PAD src0_sel:DWORD src1_sel:WORD_1
v_mfma_f32_16x16x32_bf16 v[32:35],a[204:207],a[12:15],v[32:35]
ds_read_u16 v41,v41
v_exp_f16_sdwa v8,v9 dst_sel:WORD_1 dst_unused:UNUSED_PRESERVE src0_sel:WORD_1
v_mfma_f32_16x16x32_bf16 v[208:211],a[244:247],v[28:31],v[208:211]
ds_read_b64_tr_b16 a[198:199],v2 offset:11280
ds_read_b64_tr_b16 a[200:201],v2 offset:8208
v_lshlrev_b32_sdwa v43,1,v43 dst_sel:DWORD dst_unused:UNUSED_PAD src0_sel:DWORD src1_sel:WORD_1
v_mfma_f32_16x16x32_bf16 v[212:215],a[244:247],v[24:27],v[212:215]
ds_read_u16_d16_hi v43,v43
v_lshlrev_b32_sdwa v56,1,v56 dst_sel:DWORD dst_unused:UNUSED_PAD src0_sel:DWORD src1_sel:WORD_1
v_mfma_f32_16x16x32_bf16 v[172:175],a[252:255],v[24:27],v[172:175]
ds_read_b64_tr_b16 a[202:203],v2 offset:9232
v_pk_fma_f16 v8,v8,s55,s55
v_mfma_f32_16x16x32_bf16 v[168:171],a[252:255],v[28:31],v[168:171]
v_rcp_f16 v9,v8
v_lshlrev_b32_sdwa v58,1,v58 dst_sel:DWORD dst_unused:UNUSED_PAD src0_sel:DWORD src1_sel:WORD_1
v_mfma_f32_16x16x32_bf16 v[216:219],a[240:243],v[28:31],v[216:219]
ds_read_u16 v56,v56
v_lshlrev_b32_sdwa v57,1,v57 dst_sel:DWORD dst_unused:UNUSED_PAD src0_sel:DWORD src1_sel:WORD_1
v_mfma_f32_16x16x32_bf16 v[220:223],a[240:243],v[24:27],v[220:223]
ds_read_u16_d16_hi v58,v58
v_lshlrev_b32_sdwa v59,1,v59 dst_sel:DWORD dst_unused:UNUSED_PAD src0_sel:DWORD src1_sel:WORD_1
v_mfma_f32_16x16x32_bf16 v[188:191],a[224:227],v[24:27],v[188:191]
ds_read_u16 v57,v57
v_lshlrev_b32_sdwa v52,1,v52 dst_sel:DWORD dst_unused:UNUSED_PAD src0_sel:DWORD src1_sel:WORD_1
v_mfma_f32_16x16x32_bf16 v[184:187],a[224:227],v[28:31],v[184:187]
ds_read_u16_d16_hi v59,v59
v_rcp_f16_sdwa v9,v8 dst_sel:WORD_1 dst_unused:UNUSED_PRESERVE src0_sel:WORD_1
v_mfma_f32_16x16x32_bf16 v[224:227],a[248:251],v[28:31],v[224:227]
ds_read_b64_tr_b16 a[212:213],v2 offset:6160
v_lshlrev_b32_sdwa v54,1,v54 dst_sel:DWORD dst_unused:UNUSED_PAD src0_sel:DWORD src1_sel:WORD_1
v_mfma_f32_16x16x32_bf16 v[228:231],a[248:251],v[24:27],v[228:231]
ds_read_b64_tr_b16 a[214:215],v2 offset:7184
v_fma_mix_f32 v53,v53,v9,0 op_sel_hi:[0,1,0]
v_mfma_f32_16x16x32_bf16 v[204:207],a[228:231],v[24:27],v[204:207]
ds_read_u16 v52,v52
v_fma_mix_f32 v55,v55,v9,0 op_sel:[0,1,0] op_sel_hi:[0,1,0]
v_mfma_f32_16x16x32_bf16 v[200:203],a[228:231],v[28:31],v[200:203]
s_waitcnt lgkmcnt(7)
ds_read_u16_d16_hi v54,v54
v_mfma_f32_16x16x32_bf16 v[24:27],a[216:219],a[48:51],v[16:19]
v_lshlrev_b32_sdwa v44,1,v44 dst_sel:DWORD dst_unused:UNUSED_PAD src0_sel:DWORD src1_sel:WORD_1
v_lshlrev_b32_sdwa v46,1,v46 dst_sel:DWORD dst_unused:UNUSED_PAD src0_sel:DWORD src1_sel:WORD_1
v_mfma_f32_16x16x32_bf16 v[24:27],a[220:223],a[64:67],v[24:27]
s_add_u32 m0,s57,42440
ds_read_u16 v44,v44
v_lshl_add_u64 v[36:37],v[36:37],0,v[38:39]
v_mfma_f32_16x16x32_bf16 v[24:27],a[192:195],a[80:83],v[24:27]
s_add_u32 s89,s56,6094848
ds_read_u16_d16_hi v46,v46
v_lshl_add_u64 v[38:39],v[40:41],0,v[42:43]
v_mfma_f32_16x16x32_bf16 v[28:31],a[196:199],a[80:83],v[20:23]
buffer_load_dwordx4 v6,s[36:39],s89 offen lds
v_lshlrev_b32_sdwa v45,1,v45 dst_sel:DWORD dst_unused:UNUSED_PAD src0_sel:DWORD src1_sel:WORD_1
v_mfma_f32_16x16x32_bf16 v[28:31],a[200:203],a[64:67],v[28:31]
s_waitcnt lgkmcnt(4)
ds_read_u16 v45,v45
v_lshl_add_u64 v[40:41],v[56:57],0,v[58:59]
v_mfma_f32_16x16x32_bf16 v[28:31],a[212:215],a[48:51],v[28:31]
v_lshlrev_b32_sdwa v47,1,v47 dst_sel:DWORD dst_unused:UNUSED_PAD src0_sel:DWORD src1_sel:WORD_1
v_lshlrev_b32_sdwa v48,1,v48 dst_sel:DWORD dst_unused:UNUSED_PAD src0_sel:DWORD src1_sel:WORD_1
v_mfma_f32_16x16x32_bf16 v[56:59],a[212:215],a[52:55],v[20:23]
s_waitcnt lgkmcnt(3)
ds_read_u16_d16_hi v47,v47
v_or_b32 v42,v52,v54
v_mfma_f32_16x16x32_bf16 v[56:59],a[200:203],a[68:71],v[56:59]
s_waitcnt vmcnt(14)
ds_read_u16 v48,v48
v_cvt_pk_bf16_f32 v43,v53,v55
v_lshlrev_b32_sdwa v50,1,v50 dst_sel:DWORD dst_unused:UNUSED_PAD src0_sel:DWORD src1_sel:WORD_1
v_mfma_f32_16x16x32_bf16 v[56:59],a[196:199],a[84:87],v[56:59]
s_barrier
ds_read_u16_d16_hi v50,v50
v_lshlrev_b32_sdwa v49,1,v49 dst_sel:DWORD dst_unused:UNUSED_PAD src0_sel:DWORD src1_sel:WORD_1
v_mfma_f32_16x16x32_bf16 v[52:55],a[192:195],a[84:87],v[16:19]
ds_read_u16 v49,v49
v_lshlrev_b32_sdwa v51,1,v51 dst_sel:DWORD dst_unused:UNUSED_PAD src0_sel:DWORD src1_sel:WORD_1
v_mfma_f32_16x16x32_bf16 v[52:55],a[220:223],a[68:71],v[52:55]
s_add_u32 m0,s58,0
ds_read_u16_d16_hi v51,v51
v_lshlrev_b32_sdwa v32,1,v32 dst_sel:DWORD dst_unused:UNUSED_PAD src0_sel:DWORD src1_sel:WORD_1
v_mfma_f32_16x16x32_bf16 v[52:55],a[216:219],a[52:55],v[52:55]
s_add_u32 s69,s69,s59
ds_read_u16 v32,v32
v_lshlrev_b32_sdwa v34,1,v34 dst_sel:DWORD dst_unused:UNUSED_PAD src0_sel:DWORD src1_sel:WORD_1
v_mfma_f32_16x16x32_bf16 v[136:139],a[244:247],v[36:39],v[136:139]
buffer_load_dwordx4 v5,s[44:47],s69 offen offset:0 lds
v_lshlrev_b32_sdwa v33,1,v33 dst_sel:DWORD dst_unused:UNUSED_PAD src0_sel:DWORD src1_sel:WORD_1
v_mfma_f32_16x16x32_bf16 v[140:143],a[244:247],v[40:43],v[140:143]
ds_read_u16_d16_hi v34,v34
v_lshlrev_b32_sdwa v35,1,v35 dst_sel:DWORD dst_unused:UNUSED_PAD src0_sel:DWORD src1_sel:WORD_1
v_mfma_f32_16x16x32_bf16 v[68:71],a[252:255],v[40:43],v[68:71]
ds_read_u16 v33,v33
ds_read_u16_d16_hi v35,v35
v_mfma_f32_16x16x32_bf16 v[64:67],a[252:255],v[36:39],v[64:67]
ds_read_b64_tr_b16 a[232:233],v3 offset:12320
v_lshlrev_b32_sdwa v60,1,v60 dst_sel:DWORD dst_unused:UNUSED_PAD src0_sel:DWORD src1_sel:WORD_1
v_mfma_f32_16x16x32_bf16 v[144:147],a[240:243],v[36:39],v[144:147]
s_add_u32 s69,s69,s53
ds_read_u16 v60,v60
v_lshlrev_b32_sdwa v62,1,v62 dst_sel:DWORD dst_unused:UNUSED_PAD src0_sel:DWORD src1_sel:WORD_1
v_mfma_f32_16x16x32_bf16 v[148:151],a[240:243],v[40:43],v[148:151]
buffer_load_dwordx4 v5,s[44:47],s69 offen offset:1024 lds
ds_read_u16_d16_hi v62,v62
v_mfma_f32_16x16x32_bf16 v[84:87],a[224:227],v[40:43],v[84:87]
ds_read_b64_tr_b16 a[234:235],v3 offset:13856
v_lshlrev_b32_sdwa v61,1,v61 dst_sel:DWORD dst_unused:UNUSED_PAD src0_sel:DWORD src1_sel:WORD_1
v_mfma_f32_16x16x32_bf16 v[80:83],a[224:227],v[36:39],v[80:83]
ds_read_u16 v61,v61
v_lshlrev_b32_sdwa v63,1,v63 dst_sel:DWORD dst_unused:UNUSED_PAD src0_sel:DWORD src1_sel:WORD_1
v_mfma_f32_16x16x32_bf16 v[152:155],a[248:251],v[36:39],v[152:155]
ds_read_u16_d16_hi v63,v63
v_mfma_f32_16x16x32_bf16 v[156:159],a[248:251],v[40:43],v[156:159]
s_add_u32 s69,s69,s53
ds_read_b64_tr_b16 a[236:237],v3 offset:12328
v_mfma_f32_16x16x32_bf16 v[100:103],a[228:231],v[40:43],v[100:103]
buffer_load_dwordx4 v5,s[44:47],s69 offen offset:2048 lds
v_mfma_f32_16x16x32_bf16 v[96:99],a[228:231],v[36:39],v[96:99]
ds_read_b64_tr_b16 a[238:239],v3 offset:13864
v_mfma_f32_16x16x32_bf16 v[40:43],a[216:219],a[56:59],v[16:19]
ds_read_b64_tr_b16 a[208:209],v1 offset:12320
v_lshlrev_b32_sdwa v24,1,v24 dst_sel:DWORD dst_unused:UNUSED_PAD src0_sel:DWORD src1_sel:WORD_1
v_mfma_f32_16x16x32_bf16 v[40:43],a[220:223],a[72:75],v[40:43]
s_waitcnt lgkmcnt(7)
ds_read_u16 v24,v24
v_lshl_add_u64 v[44:45],v[44:45],0,v[46:47]
v_fma_mixlo_f16 v8,v57,s99,0
v_mfma_f32_16x16x32_bf16 v[40:43],a[192:195],a[88:91],v[40:43]
ds_read_b64_tr_b16 a[210:211],v1 offset:13344
v_lshl_add_u64 v[46:47],v[48:49],0,v[50:51]
v_lshlrev_b32_sdwa v26,1,v26 dst_sel:DWORD dst_unused:UNUSED_PAD src0_sel:DWORD src1_sel:WORD_1
v_mfma_f32_16x16x32_bf16 v[36:39],a[196:199],a[88:91],v[20:23]
ds_read_u16_d16_hi v26,v26
v_fma_mixhi_f16 v8,v59,s99,0
v_mfma_f32_16x16x32_bf16 v[36:39],a[200:203],a[72:75],v[36:39]
ds_read_b64_tr_b16 a[204:205],v1 offset:14368
v_lshl_add_u64 v[48:49],v[32:33],0,v[34:35]
v_lshlrev_b32_sdwa v25,1,v25 dst_sel:DWORD dst_unused:UNUSED_PAD src0_sel:DWORD src1_sel:WORD_1
v_pk_fma_f16 v9,v8,v8,s54
v_mfma_f32_16x16x32_bf16 v[36:39],a[212:215],a[56:59],v[36:39]
ds_read_u16 v25,v25
v_lshlrev_b32_sdwa v27,1,v27 dst_sel:DWORD dst_unused:UNUSED_PAD src0_sel:DWORD src1_sel:WORD_1
v_mfma_f32_16x16x32_bf16 v[32:35],a[212:215],a[60:63],v[20:23]
ds_read_u16_d16_hi v27,v27
v_pk_mul_f16 v9,v8,v9
v_mfma_f32_16x16x32_bf16 v[32:35],a[200:203],a[76:79],v[32:35]
s_waitcnt lgkmcnt(8)
ds_read_b64_tr_b16 a[206:207],v1 offset:15392
v_lshl_add_u64 v[50:51],v[60:61],0,v[62:63]
v_lshlrev_b32_sdwa v28,1,v28 dst_sel:DWORD dst_unused:UNUSED_PAD src0_sel:DWORD src1_sel:WORD_1
v_mfma_f32_16x16x32_bf16 v[32:35],a[196:199],a[92:95],v[32:35]
v_exp_f16 v8,v9
v_mfma_f32_16x16x32_bf16 v[60:63],a[192:195],a[92:95],v[16:19]
ds_read_u16 v28,v28
v_lshlrev_b32_sdwa v30,1,v30 dst_sel:DWORD dst_unused:UNUSED_PAD src0_sel:DWORD src1_sel:WORD_1
v_mfma_f32_16x16x32_bf16 v[60:63],a[220:223],a[76:79],v[60:63]
ds_read_u16_d16_hi v30,v30
v_lshlrev_b32_sdwa v29,1,v29 dst_sel:DWORD dst_unused:UNUSED_PAD src0_sel:DWORD src1_sel:WORD_1
v_mfma_f32_16x16x32_bf16 v[60:63],a[216:219],a[60:63],v[60:63]
v_exp_f16_sdwa v8,v9 dst_sel:WORD_1 dst_unused:UNUSED_PRESERVE src0_sel:WORD_1
v_mfma_f32_16x16x32_bf16 v[112:115],a[244:247],v[44:47],v[112:115]
ds_read_u16 v29,v29
v_lshlrev_b32_sdwa v31,1,v31 dst_sel:DWORD dst_unused:UNUSED_PAD src0_sel:DWORD src1_sel:WORD_1
v_mfma_f32_16x16x32_bf16 v[116:119],a[244:247],v[48:51],v[116:119]
ds_read_u16_d16_hi v31,v31
v_lshlrev_b32_sdwa v52,1,v52 dst_sel:DWORD dst_unused:UNUSED_PAD src0_sel:DWORD src1_sel:WORD_1
v_mfma_f32_16x16x32_bf16 v[76:79],a[252:255],v[48:51],v[76:79]
ds_read_u16 v52,v52
v_pk_fma_f16 v8,v8,s55,s55
v_mfma_f32_16x16x32_bf16 v[72:75],a[252:255],v[44:47],v[72:75]
ds_read_b64_tr_b16 a[244:245],v3 offset:12384
v_rcp_f16_e64 v9,v8
v_lshlrev_b32_sdwa v54,1,v54 dst_sel:DWORD dst_unused:UNUSED_PAD src0_sel:DWORD src1_sel:WORD_1
v_mfma_f32_16x16x32_bf16 v[120:123],a[240:243],v[44:47],v[120:123]
ds_read_u16_d16_hi v54,v54
v_lshlrev_b32_sdwa v53,1,v53 dst_sel:DWORD dst_unused:UNUSED_PAD src0_sel:DWORD src1_sel:WORD_1
v_mfma_f32_16x16x32_bf16 v[124:127],a[240:243],v[48:51],v[124:127]
ds_read_u16 v53,v53
ds_read_b64_tr_b16 a[246:247],v3 offset:13920
v_mfma_f32_16x16x32_bf16 v[92:95],a[224:227],v[48:51],v[92:95]
ds_read_b64_tr_b16 a[252:253],v3 offset:12392
v_lshlrev_b32_sdwa v55,1,v55 dst_sel:DWORD dst_unused:UNUSED_PAD src0_sel:DWORD src1_sel:WORD_1
v_mfma_f32_16x16x32_bf16 v[88:91],a[224:227],v[44:47],v[88:91]
ds_read_u16_d16_hi v55,v55
v_rcp_f16_sdwa v9,v8 dst_sel:WORD_1 dst_unused:UNUSED_PRESERVE src0_sel:WORD_1
v_mfma_f32_16x16x32_bf16 v[128:131],a[248:251],v[44:47],v[128:131]
ds_read_b64_tr_b16 a[254:255],v3 offset:13928
ds_read_b64_tr_b16 a[240:241],v3 offset:12448
v_lshlrev_b32_sdwa v56,1,v56 dst_sel:DWORD dst_unused:UNUSED_PAD src0_sel:DWORD src1_sel:WORD_1
v_mfma_f32_16x16x32_bf16 v[132:135],a[248:251],v[48:51],v[132:135]
ds_read_u16 v56,v56
v_lshlrev_b32_sdwa v58,1,v58 dst_sel:DWORD dst_unused:UNUSED_PAD src0_sel:DWORD src1_sel:WORD_1
v_mfma_f32_16x16x32_bf16 v[108:111],a[228:231],v[48:51],v[108:111]
ds_read_u16_d16_hi v58,v58
v_fma_mix_f32 v57,v57,v9,0 op_sel_hi:[0,1,0]
v_mfma_f32_16x16x32_bf16 v[104:107],a[228:231],v[44:47],v[104:107]
ds_read_b64_tr_b16 a[242:243],v3 offset:13984
v_fma_mix_f32 v59,v59,v9,0 op_sel:[0,1,0] op_sel_hi:[0,1,0]
v_mfma_f32_16x16x32_bf16 v[48:51],a[216:219],a[0:3],v[16:19]
s_add_u32 m0,s58,6160
ds_read_b64_tr_b16 a[224:225],v3 offset:12456
v_lshlrev_b32_sdwa v40,1,v40 dst_sel:DWORD dst_unused:UNUSED_PAD src0_sel:DWORD src1_sel:WORD_1
v_mfma_f32_16x16x32_bf16 v[48:51],a[220:223],a[16:19],v[48:51]
s_waitcnt lgkmcnt(10)
ds_read_b64_tr_b16 a[226:227],v3 offset:13992
v_lshl_add_u64 v[24:25],v[24:25],0,v[26:27]
v_mfma_f32_16x16x32_bf16 v[48:51],a[192:195],a[32:35],v[48:51]
s_add_u32 s69,s69,s59
ds_read_u16 v40,v40
v_lshl_add_u64 v[26:27],v[28:29],0,v[30:31]
v_lshlrev_b32_sdwa v42,1,v42 dst_sel:DWORD dst_unused:UNUSED_PAD src0_sel:DWORD src1_sel:WORD_1
v_mfma_f32_16x16x32_bf16 v[44:47],a[196:199],a[32:35],v[20:23]
buffer_load_dwordx4 v5,s[44:47],s69 offen offset:0 lds
v_lshlrev_b32_sdwa v41,1,v41 dst_sel:DWORD dst_unused:UNUSED_PAD src0_sel:DWORD src1_sel:WORD_1
v_mfma_f32_16x16x32_bf16 v[44:47],a[200:203],a[16:19],v[44:47]
s_waitcnt lgkmcnt(8)
ds_read_u16_d16_hi v42,v42
v_lshl_add_u64 v[28:29],v[52:53],0,v[54:55]
v_mfma_f32_16x16x32_bf16 v[44:47],a[212:215],a[0:3],v[44:47]
s_add_u32 m0,s57,45976
ds_read_u16 v41,v41
v_lshlrev_b32_sdwa v43,1,v43 dst_sel:DWORD dst_unused:UNUSED_PAD src0_sel:DWORD src1_sel:WORD_1
v_mfma_f32_16x16x32_bf16 v[52:55],a[212:215],a[4:7],v[20:23]
s_waitcnt lgkmcnt(6)
ds_read_u16_d16_hi v43,v43
v_or_b32 v30,v56,v58
v_lshlrev_b32_sdwa v36,1,v36 dst_sel:DWORD dst_unused:UNUSED_PAD src0_sel:DWORD src1_sel:WORD_1
v_mfma_f32_16x16x32_bf16 v[52:55],a[200:203],a[20:23],v[52:55]
s_add_u32 s89,s56,6193152
ds_read_u16 v36,v36
v_cvt_pk_bf16_f32 v31,v57,v59
v_lshlrev_b32_sdwa v38,1,v38 dst_sel:DWORD dst_unused:UNUSED_PAD src0_sel:DWORD src1_sel:WORD_1
v_mfma_f32_16x16x32_bf16 v[52:55],a[196:199],a[36:39],v[52:55]
buffer_load_dwordx4 v6,s[36:39],s89 offen lds
v_lshlrev_b32_sdwa v37,1,v37 dst_sel:DWORD dst_unused:UNUSED_PAD src0_sel:DWORD src1_sel:WORD_1
v_mfma_f32_16x16x32_bf16 v[56:59],a[192:195],a[36:39],v[16:19]
s_add_u32 m0,s58,6160
ds_read_u16_d16_hi v38,v38
v_lshlrev_b32_sdwa v39,1,v39 dst_sel:DWORD dst_unused:UNUSED_PAD src0_sel:DWORD src1_sel:WORD_1
v_mfma_f32_16x16x32_bf16 v[56:59],a[220:223],a[20:23],v[56:59]
s_add_u32 s69,s69,s53
ds_read_u16 v37,v37
v_lshlrev_b32_sdwa v60,1,v60 dst_sel:DWORD dst_unused:UNUSED_PAD src0_sel:DWORD src1_sel:WORD_1
v_mfma_f32_16x16x32_bf16 v[56:59],a[216:219],a[4:7],v[56:59]
buffer_load_dwordx4 v5,s[44:47],s69 offen offset:1024 lds
v_lshlrev_b32_sdwa v62,1,v62 dst_sel:DWORD dst_unused:UNUSED_PAD src0_sel:DWORD src1_sel:WORD_1
v_mfma_f32_16x16x32_bf16 v[232:235],a[232:235],v[24:27],v[232:235]
s_add_u32 s69,s69,s53
ds_read_u16_d16_hi v39,v39
v_lshlrev_b32_sdwa v61,1,v61 dst_sel:DWORD dst_unused:UNUSED_PAD src0_sel:DWORD src1_sel:WORD_1
v_mfma_f32_16x16x32_bf16 v[236:239],a[232:235],v[28:31],v[236:239]
buffer_load_dwordx4 v5,s[44:47],s69 offen offset:2048 lds
v_lshlrev_b32_sdwa v63,1,v63 dst_sel:DWORD dst_unused:UNUSED_PAD src0_sel:DWORD src1_sel:WORD_1
v_mfma_f32_16x16x32_bf16 v[164:167],a[236:239],v[28:31],v[164:167]
s_add_u32 m0,s57,49504
ds_read_u16 v60,v60
v_lshlrev_b32_sdwa v32,1,v32 dst_sel:DWORD dst_unused:UNUSED_PAD src0_sel:DWORD src1_sel:WORD_1
v_mfma_f32_16x16x32_bf16 v[160:163],a[236:239],v[24:27],v[160:163]
s_add_u32 s89,s56,6291456
ds_read_u16_d16_hi v62,v62
v_lshlrev_b32_sdwa v34,1,v34 dst_sel:DWORD dst_unused:UNUSED_PAD src0_sel:DWORD src1_sel:WORD_1
v_mfma_f32_16x16x32_bf16 v[240:243],a[244:247],v[24:27],v[240:243]
buffer_load_dwordx4 v6,s[36:39],s89 offen lds
ds_read_u16 v61,v61
v_mfma_f32_16x16x32_bf16 v[244:247],a[244:247],v[28:31],v[244:247]
ds_read_u16_d16_hi v63,v63
v_lshlrev_b32_sdwa v33,1,v33 dst_sel:DWORD dst_unused:UNUSED_PAD src0_sel:DWORD src1_sel:WORD_1
v_mfma_f32_16x16x32_bf16 v[180:183],a[252:255],v[28:31],v[180:183]
ds_read_u16 v32,v32
v_lshlrev_b32_sdwa v35,1,v35 dst_sel:DWORD dst_unused:UNUSED_PAD src0_sel:DWORD src1_sel:WORD_1
v_mfma_f32_16x16x32_bf16 v[176:179],a[252:255],v[24:27],v[176:179]
s_waitcnt lgkmcnt(6)
ds_read_u16_d16_hi v34,v34
v_mfma_f32_16x16x32_bf16 v[248:251],a[240:243],v[24:27],v[248:251]
ds_read_u16 v33,v33
v_mfma_f32_16x16x32_bf16 v[252:255],a[240:243],v[28:31],v[252:255]
ds_read_u16_d16_hi v35,v35
v_mfma_f32_16x16x32_bf16 v[196:199],a[224:227],v[28:31],v[196:199]
v_mfma_f32_16x16x32_bf16 v[192:195],a[224:227],v[24:27],v[192:195]
v_mfma_f32_16x16x32_bf16 v[28:31],a[216:219],a[8:11],v[16:19]
v_lshlrev_b32_sdwa v48,1,v48 dst_sel:DWORD dst_unused:UNUSED_PAD src0_sel:DWORD src1_sel:WORD_1
v_fma_mixlo_f16 v8,v53,s99,0
v_mfma_f32_16x16x32_bf16 v[28:31],a[220:223],a[24:27],v[28:31]
ds_read_u16 v48,v48
v_lshl_add_u64 v[40:41],v[40:41],0,v[42:43]
v_mfma_f32_16x16x32_bf16 v[28:31],a[192:195],a[40:43],v[28:31]
s_waitcnt lgkmcnt(5)
v_lshl_add_u64 v[42:43],v[36:37],0,v[38:39]
v_lshlrev_b32_sdwa v50,1,v50 dst_sel:DWORD dst_unused:UNUSED_PAD src0_sel:DWORD src1_sel:WORD_1
v_mfma_f32_16x16x32_bf16 v[24:27],a[196:199],a[40:43],v[20:23]
ds_read_u16_d16_hi v50,v50
v_fma_mixhi_f16 v8,v55,s99,0
v_mfma_f32_16x16x32_bf16 v[24:27],a[200:203],a[24:27],v[24:27]
v_lshl_add_u64 v[36:37],v[60:61],0,v[62:63]
v_lshlrev_b32_sdwa v49,1,v49 dst_sel:DWORD dst_unused:UNUSED_PAD src0_sel:DWORD src1_sel:WORD_1
v_pk_fma_f16 v9,v8,v8,s54
v_mfma_f32_16x16x32_bf16 v[24:27],a[212:215],a[8:11],v[24:27]
ds_read_u16 v49,v49
v_lshlrev_b32_sdwa v51,1,v51 dst_sel:DWORD dst_unused:UNUSED_PAD src0_sel:DWORD src1_sel:WORD_1
v_mfma_f32_16x16x32_bf16 v[60:63],a[212:215],a[12:15],v[20:23]
ds_read_b128 v[20:23],v4 offset:384
v_pk_mul_f16 v9,v8,v9
v_mfma_f32_16x16x32_bf16 v[60:63],a[200:203],a[28:31],v[60:63]
s_waitcnt lgkmcnt(4)
ds_read_u16_d16_hi v51,v51
ds_read_b64_tr_b16 a[212:213],v1 offset:16416
v_lshl_add_u64 v[38:39],v[32:33],0,v[34:35]
v_lshlrev_b32_sdwa v44,1,v44 dst_sel:DWORD dst_unused:UNUSED_PAD src0_sel:DWORD src1_sel:WORD_1
v_mfma_f32_16x16x32_bf16 v[60:63],a[196:199],a[44:47],v[60:63]
ds_read_b64_tr_b16 a[214:215],v1 offset:17440
v_exp_f16_e64 v8,v9
v_mfma_f32_16x16x32_bf16 v[32:35],a[192:195],a[44:47],v[16:19]
ds_read_b128 v[16:19],v4 offset:416
ds_read_u16 v44,v44
v_lshlrev_b32_sdwa v46,1,v46 dst_sel:DWORD dst_unused:UNUSED_PAD src0_sel:DWORD src1_sel:WORD_1
v_mfma_f32_16x16x32_bf16 v[32:35],a[220:223],a[28:31],v[32:35]
ds_read_u16_d16_hi v46,v46
ds_read_b64_tr_b16 a[200:201],v2 offset:16416
v_lshlrev_b32_sdwa v45,1,v45 dst_sel:DWORD dst_unused:UNUSED_PAD src0_sel:DWORD src1_sel:WORD_1
v_mfma_f32_16x16x32_bf16 v[32:35],a[216:219],a[12:15],v[32:35]
ds_read_u16 v45,v45
v_exp_f16_sdwa v8,v9 dst_sel:WORD_1 dst_unused:UNUSED_PRESERVE src0_sel:WORD_1
v_mfma_f32_16x16x32_bf16 v[208:211],a[232:235],v[40:43],v[208:211]
ds_read_b64_tr_b16 a[202:203],v2 offset:17440
ds_read_b64_tr_b16 a[196:197],v2 offset:14368
v_lshlrev_b32_sdwa v47,1,v47 dst_sel:DWORD dst_unused:UNUSED_PAD src0_sel:DWORD src1_sel:WORD_1
v_mfma_f32_16x16x32_bf16 v[212:215],a[232:235],v[36:39],v[212:215]
ds_read_u16_d16_hi v47,v47
v_lshlrev_b32_sdwa v56,1,v56 dst_sel:DWORD dst_unused:UNUSED_PAD src0_sel:DWORD src1_sel:WORD_1
v_mfma_f32_16x16x32_bf16 v[172:175],a[236:239],v[36:39],v[172:175]
ds_read_b64_tr_b16 a[198:199],v2 offset:15392
v_pk_fma_f16 v8,v8,s55,s55
v_mfma_f32_16x16x32_bf16 v[168:171],a[236:239],v[40:43],v[168:171]
TP6487_%=:
s_cbranch_execz TR0_%=
v_rcp_f16 v9,v8
v_lshlrev_b32_sdwa v58,1,v58 dst_sel:DWORD dst_unused:UNUSED_PAD src0_sel:DWORD src1_sel:WORD_1
v_mfma_f32_16x16x32_bf16 v[216:219],a[244:247],v[40:43],v[216:219]
ds_read_u16 v56,v56
v_lshlrev_b32_sdwa v57,1,v57 dst_sel:DWORD dst_unused:UNUSED_PAD src0_sel:DWORD src1_sel:WORD_1
v_mfma_f32_16x16x32_bf16 v[220:223],a[244:247],v[36:39],v[220:223]
ds_read_u16_d16_hi v58,v58
v_lshlrev_b32_sdwa v59,1,v59 dst_sel:DWORD dst_unused:UNUSED_PAD src0_sel:DWORD src1_sel:WORD_1
v_mfma_f32_16x16x32_bf16 v[188:191],a[252:255],v[36:39],v[188:191]
ds_read_u16 v57,v57
v_lshlrev_b32_sdwa v52,1,v52 dst_sel:DWORD dst_unused:UNUSED_PAD src0_sel:DWORD src1_sel:WORD_1
v_mfma_f32_16x16x32_bf16 v[184:187],a[252:255],v[40:43],v[184:187]
ds_read_u16_d16_hi v59,v59
v_rcp_f16_sdwa v9,v8 dst_sel:WORD_1 dst_unused:UNUSED_PRESERVE src0_sel:WORD_1
v_mfma_f32_16x16x32_bf16 v[224:227],a[240:243],v[40:43],v[224:227]
ds_read_b64_tr_b16 a[192:193],v2 offset:12320
v_lshlrev_b32_sdwa v54,1,v54 dst_sel:DWORD dst_unused:UNUSED_PAD src0_sel:DWORD src1_sel:WORD_1
v_mfma_f32_16x16x32_bf16 v[228:231],a[240:243],v[36:39],v[228:231]
ds_read_b64_tr_b16 a[194:195],v2 offset:13344
v_fma_mix_f32 v53,v53,v9,0 op_sel_hi:[0,1,0]
v_mfma_f32_16x16x32_bf16 v[204:207],a[224:227],v[36:39],v[204:207]
ds_read_u16 v52,v52
v_fma_mix_f32 v55,v55,v9,0 op_sel:[0,1,0] op_sel_hi:[0,1,0]
v_mfma_f32_16x16x32_bf16 v[200:203],a[224:227],v[40:43],v[200:203]
s_waitcnt lgkmcnt(7)
ds_read_u16_d16_hi v54,v54
v_mfma_f32_16x16x32_bf16 v[36:39],a[208:211],a[48:51],v[20:23]
s_add_u32 m0,s57,53040
v_lshlrev_b32_sdwa v28,1,v28 dst_sel:DWORD dst_unused:UNUSED_PAD src0_sel:DWORD src1_sel:WORD_1
v_lshlrev_b32_sdwa v30,1,v30 dst_sel:DWORD dst_unused:UNUSED_PAD src0_sel:DWORD src1_sel:WORD_1
v_mfma_f32_16x16x32_bf16 v[36:39],a[204:207],a[64:67],v[36:39]
s_add_u32 s89,s56,6389760
ds_read_u16 v28,v28
v_lshl_add_u64 v[48:49],v[48:49],0,v[50:51]
v_mfma_f32_16x16x32_bf16 v[36:39],a[212:215],a[80:83],v[36:39]
buffer_load_dwordx4 v6,s[36:39],s89 offen lds
v_lshl_add_u64 v[50:51],v[44:45],0,v[46:47]
v_mfma_f32_16x16x32_bf16 v[40:43],a[200:203],a[80:83],v[16:19]
ds_read_u16_d16_hi v30,v30
v_lshlrev_b32_sdwa v29,1,v29 dst_sel:DWORD dst_unused:UNUSED_PAD src0_sel:DWORD src1_sel:WORD_1
v_mfma_f32_16x16x32_bf16 v[40:43],a[196:199],a[64:67],v[40:43]
s_waitcnt lgkmcnt(4)
ds_read_u16 v29,v29
v_lshl_add_u64 v[44:45],v[56:57],0,v[58:59]
v_mfma_f32_16x16x32_bf16 v[40:43],a[192:195],a[48:51],v[40:43]
v_lshlrev_b32_sdwa v31,1,v31 dst_sel:DWORD dst_unused:UNUSED_PAD src0_sel:DWORD src1_sel:WORD_1
v_lshlrev_b32_sdwa v24,1,v24 dst_sel:DWORD dst_unused:UNUSED_PAD src0_sel:DWORD src1_sel:WORD_1
v_mfma_f32_16x16x32_bf16 v[56:59],a[192:195],a[52:55],v[16:19]
s_waitcnt lgkmcnt(3)
ds_read_u16_d16_hi v31,v31
v_or_b32_e64 v46,v52,v54
v_mfma_f32_16x16x32_bf16 v[56:59],a[196:199],a[68:71],v[56:59]
ds_read_u16 v24,v24
v_cvt_pk_bf16_f32 v47,v53,v55
v_lshlrev_b32_sdwa v26,1,v26 dst_sel:DWORD dst_unused:UNUSED_PAD src0_sel:DWORD src1_sel:WORD_1
v_mfma_f32_16x16x32_bf16 v[56:59],a[200:203],a[84:87],v[56:59]
ds_read_u16_d16_hi v26,v26
v_lshlrev_b32_sdwa v25,1,v25 dst_sel:DWORD dst_unused:UNUSED_PAD src0_sel:DWORD src1_sel:WORD_1
v_mfma_f32_16x16x32_bf16 v[52:55],a[212:215],a[84:87],v[20:23]
ds_read_u16 v25,v25
v_lshlrev_b32_sdwa v27,1,v27 dst_sel:DWORD dst_unused:UNUSED_PAD src0_sel:DWORD src1_sel:WORD_1
v_mfma_f32_16x16x32_bf16 v[52:55],a[204:207],a[68:71],v[52:55]
ds_read_u16_d16_hi v27,v27
v_lshlrev_b32_sdwa v32,1,v32 dst_sel:DWORD dst_unused:UNUSED_PAD src0_sel:DWORD src1_sel:WORD_1
v_mfma_f32_16x16x32_bf16 v[52:55],a[208:211],a[52:55],v[52:55]
ds_read_u16 v32,v32
v_lshlrev_b32_sdwa v34,1,v34 dst_sel:DWORD dst_unused:UNUSED_PAD src0_sel:DWORD src1_sel:WORD_1
v_mfma_f32_16x16x32_bf16 v[136:139],a[232:235],v[48:51],v[136:139]
ds_read_u16_d16_hi v34,v34
v_lshlrev_b32_sdwa v33,1,v33 dst_sel:DWORD dst_unused:UNUSED_PAD src0_sel:DWORD src1_sel:WORD_1
v_mfma_f32_16x16x32_bf16 v[140:143],a[232:235],v[44:47],v[140:143]
ds_read_u16 v33,v33
v_lshlrev_b32_sdwa v35,1,v35 dst_sel:DWORD dst_unused:UNUSED_PAD src0_sel:DWORD src1_sel:WORD_1
v_mfma_f32_16x16x32_bf16 v[68:71],a[236:239],v[44:47],v[68:71]
ds_read_u16_d16_hi v35,v35
ds_read_b64_tr_b16 a[248:249],v3 offset:18480
v_mfma_f32_16x16x32_bf16 v[64:67],a[236:239],v[48:51],v[64:67]
ds_read_b64_tr_b16 a[250:251],v3 offset:20016
v_lshlrev_b32_sdwa v60,1,v60 dst_sel:DWORD dst_unused:UNUSED_PAD src0_sel:DWORD src1_sel:WORD_1
v_mfma_f32_16x16x32_bf16 v[144:147],a[244:247],v[48:51],v[144:147]
ds_read_u16 v60,v60
v_lshlrev_b32_sdwa v62,1,v62 dst_sel:DWORD dst_unused:UNUSED_PAD src0_sel:DWORD src1_sel:WORD_1
v_mfma_f32_16x16x32_bf16 v[148:151],a[244:247],v[44:47],v[148:151]
ds_read_u16_d16_hi v62,v62
v_lshlrev_b32_sdwa v61,1,v61 dst_sel:DWORD dst_unused:UNUSED_PAD src0_sel:DWORD src1_sel:WORD_1
v_mfma_f32_16x16x32_bf16 v[84:87],a[252:255],v[44:47],v[84:87]
ds_read_u16 v61,v61
v_lshlrev_b32_sdwa v63,1,v63 dst_sel:DWORD dst_unused:UNUSED_PAD src0_sel:DWORD src1_sel:WORD_1
v_mfma_f32_16x16x32_bf16 v[80:83],a[252:255],v[48:51],v[80:83]
ds_read_u16_d16_hi v63,v63
v_mfma_f32_16x16x32_bf16 v[152:155],a[240:243],v[48:51],v[152:155]
ds_read_b64_tr_b16 a[228:229],v3 offset:18488
v_mfma_f32_16x16x32_bf16 v[156:159],a[240:243],v[44:47],v[156:159]
ds_read_b64_tr_b16 a[230:231],v3 offset:20024
v_mfma_f32_16x16x32_bf16 v[100:103],a[224:227],v[44:47],v[100:103]
v_mfma_f32_16x16x32_bf16 v[96:99],a[224:227],v[48:51],v[96:99]
v_mfma_f32_16x16x32_bf16 v[44:47],a[208:211],a[56:59],v[20:23]
ds_read_b64_tr_b16 a[220:221],v1 offset:18480
v_lshlrev_b32_sdwa v36,1,v36 dst_sel:DWORD dst_unused:UNUSED_PAD src0_sel:DWORD src1_sel:WORD_1
v_mfma_f32_16x16x32_bf16 v[44:47],a[204:207],a[72:75],v[44:47]
s_waitcnt lgkmcnt(6)
ds_read_u16 v36,v36
v_lshl_add_u64 v[28:29],v[28:29],0,v[30:31]
v_fma_mixlo_f16 v8,v57,s99,0
v_mfma_f32_16x16x32_bf16 v[44:47],a[212:215],a[88:91],v[44:47]
ds_read_b64_tr_b16 a[222:223],v1 offset:19504
v_lshl_add_u64 v[30:31],v[24:25],0,v[26:27]
v_lshlrev_b32_sdwa v38,1,v38 dst_sel:DWORD dst_unused:UNUSED_PAD src0_sel:DWORD src1_sel:WORD_1
v_mfma_f32_16x16x32_bf16 v[48:51],a[200:203],a[88:91],v[16:19]
ds_read_u16_d16_hi v38,v38
v_fma_mixhi_f16 v8,v59,s99,0
v_mfma_f32_16x16x32_bf16 v[48:51],a[196:199],a[72:75],v[48:51]
ds_read_b64_tr_b16 a[216:217],v1 offset:20528
v_lshl_add_u64 v[24:25],v[32:33],0,v[34:35]
v_lshlrev_b32_sdwa v37,1,v37 dst_sel:DWORD dst_unused:UNUSED_PAD src0_sel:DWORD src1_sel:WORD_1
v_pk_fma_f16 v9,v8,v8,s54
v_mfma_f32_16x16x32_bf16 v[48:51],a[192:195],a[56:59],v[48:51]
ds_read_u16 v37,v37
v_lshlrev_b32_sdwa v39,1,v39 dst_sel:DWORD dst_unused:UNUSED_PAD src0_sel:DWORD src1_sel:WORD_1
v_mfma_f32_16x16x32_bf16 v[32:35],a[192:195],a[60:63],v[16:19]
ds_read_u16_d16_hi v39,v39
v_pk_mul_f16 v9,v8,v9
v_mfma_f32_16x16x32_bf16 v[32:35],a[196:199],a[76:79],v[32:35]
s_waitcnt lgkmcnt(7)
ds_read_b64_tr_b16 a[218:219],v1 offset:21552
v_lshl_add_u64 v[26:27],v[60:61],0,v[62:63]
v_lshlrev_b32_sdwa v40,1,v40 dst_sel:DWORD dst_unused:UNUSED_PAD src0_sel:DWORD src1_sel:WORD_1
v_mfma_f32_16x16x32_bf16 v[32:35],a[200:203],a[92:95],v[32:35]
v_exp_f16 v8,v9
v_mfma_f32_16x16x32_bf16 v[60:63],a[212:215],a[92:95],v[20:23]
ds_read_u16 v40,v40
v_lshlrev_b32_sdwa v42,1,v42 dst_sel:DWORD dst_unused:UNUSED_PAD src0_sel:DWORD src1_sel:WORD_1
v_mfma_f32_16x16x32_bf16 v[60:63],a[204:207],a[76:79],v[60:63]
ds_read_u16_d16_hi v42,v42
v_lshlrev_b32_sdwa v41,1,v41 dst_sel:DWORD dst_unused:UNUSED_PAD src0_sel:DWORD src1_sel:WORD_1
v_mfma_f32_16x16x32_bf16 v[60:63],a[208:211],a[60:63],v[60:63]
v_exp_f16_sdwa v8,v9 dst_sel:WORD_1 dst_unused:UNUSED_PRESERVE src0_sel:WORD_1
v_mfma_f32_16x16x32_bf16 v[112:115],a[232:235],v[28:31],v[112:115]
ds_read_u16 v41,v41
v_lshlrev_b32_sdwa v43,1,v43 dst_sel:DWORD dst_unused:UNUSED_PAD src0_sel:DWORD src1_sel:WORD_1
v_mfma_f32_16x16x32_bf16 v[116:119],a[232:235],v[24:27],v[116:119]
ds_read_u16_d16_hi v43,v43
v_lshlrev_b32_sdwa v52,1,v52 dst_sel:DWORD dst_unused:UNUSED_PAD src0_sel:DWORD src1_sel:WORD_1
v_mfma_f32_16x16x32_bf16 v[76:79],a[236:239],v[24:27],v[76:79]
ds_read_u16 v52,v52
v_pk_fma_f16 v8,v8,s55,s55
v_mfma_f32_16x16x32_bf16 v[72:75],a[236:239],v[28:31],v[72:75]
ds_read_b64_tr_b16 a[232:233],v3 offset:18544
v_rcp_f16_e64 v9,v8
v_lshlrev_b32_sdwa v54,1,v54 dst_sel:DWORD dst_unused:UNUSED_PAD src0_sel:DWORD src1_sel:WORD_1
v_mfma_f32_16x16x32_bf16 v[120:123],a[244:247],v[28:31],v[120:123]
ds_read_u16_d16_hi v54,v54
v_lshlrev_b32_sdwa v53,1,v53 dst_sel:DWORD dst_unused:UNUSED_PAD src0_sel:DWORD src1_sel:WORD_1
v_mfma_f32_16x16x32_bf16 v[124:127],a[244:247],v[24:27],v[124:127]
ds_read_u16 v53,v53
ds_read_b64_tr_b16 a[234:235],v3 offset:20080
v_mfma_f32_16x16x32_bf16 v[92:95],a[252:255],v[24:27],v[92:95]
ds_read_b64_tr_b16 a[236:237],v3 offset:18552
v_lshlrev_b32_sdwa v55,1,v55 dst_sel:DWORD dst_unused:UNUSED_PAD src0_sel:DWORD src1_sel:WORD_1
v_mfma_f32_16x16x32_bf16 v[88:91],a[252:255],v[28:31],v[88:91]
ds_read_u16_d16_hi v55,v55
v_rcp_f16_sdwa v9,v8 dst_sel:WORD_1 dst_unused:UNUSED_PRESERVE src0_sel:WORD_1
v_mfma_f32_16x16x32_bf16 v[128:131],a[240:243],v[28:31],v[128:131]
ds_read_b64_tr_b16 a[238:239],v3 offset:20088
ds_read_b64_tr_b16 a[244:245],v3 offset:18608
v_lshlrev_b32_sdwa v56,1,v56 dst_sel:DWORD dst_unused:UNUSED_PAD src0_sel:DWORD src1_sel:WORD_1
v_mfma_f32_16x16x32_bf16 v[132:135],a[240:243],v[24:27],v[132:135]
ds_read_u16 v56,v56
v_lshlrev_b32_sdwa v58,1,v58 dst_sel:DWORD dst_unused:UNUSED_PAD src0_sel:DWORD src1_sel:WORD_1
v_mfma_f32_16x16x32_bf16 v[108:111],a[224:227],v[24:27],v[108:111]
ds_read_u16_d16_hi v58,v58
v_fma_mix_f32 v57,v57,v9,0 op_sel_hi:[0,1,0]
v_mfma_f32_16x16x32_bf16 v[104:107],a[224:227],v[28:31],v[104:107]
ds_read_b64_tr_b16 a[246:247],v3 offset:20144
v_fma_mix_f32 v59,v59,v9,0 op_sel:[0,1,0] op_sel_hi:[0,1,0]
v_mfma_f32_16x16x32_bf16 v[24:27],a[208:211],a[0:3],v[20:23]
ds_read_b64_tr_b16 a[252:253],v3 offset:18616
v_lshlrev_b32_sdwa v44,1,v44 dst_sel:DWORD dst_unused:UNUSED_PAD src0_sel:DWORD src1_sel:WORD_1
v_mfma_f32_16x16x32_bf16 v[24:27],a[204:207],a[16:19],v[24:27]
s_waitcnt lgkmcnt(10)
ds_read_b64_tr_b16 a[254:255],v3 offset:20152
v_lshl_add_u64 v[36:37],v[36:37],0,v[38:39]
v_mfma_f32_16x16x32_bf16 v[24:27],a[212:215],a[32:35],v[24:27]
ds_read_u16 v44,v44
v_lshl_add_u64 v[38:39],v[40:41],0,v[42:43]
v_lshlrev_b32_sdwa v46,1,v46 dst_sel:DWORD dst_unused:UNUSED_PAD src0_sel:DWORD src1_sel:WORD_1
v_mfma_f32_16x16x32_bf16 v[28:31],a[200:203],a[32:35],v[16:19]
ds_read_u16_d16_hi v46,v46
v_lshlrev_b32_sdwa v45,1,v45 dst_sel:DWORD dst_unused:UNUSED_PAD src0_sel:DWORD src1_sel:WORD_1
v_mfma_f32_16x16x32_bf16 v[28:31],a[196:199],a[16:19],v[28:31]
s_waitcnt lgkmcnt(9)
ds_read_u16 v45,v45
v_lshl_add_u64 v[40:41],v[52:53],0,v[54:55]
v_mfma_f32_16x16x32_bf16 v[28:31],a[192:195],a[0:3],v[28:31]
v_lshlrev_b32_sdwa v47,1,v47 dst_sel:DWORD dst_unused:UNUSED_PAD src0_sel:DWORD src1_sel:WORD_1
v_lshlrev_b32_sdwa v48,1,v48 dst_sel:DWORD dst_unused:UNUSED_PAD src0_sel:DWORD src1_sel:WORD_1
v_mfma_f32_16x16x32_bf16 v[52:55],a[192:195],a[4:7],v[16:19]
s_waitcnt lgkmcnt(6)
ds_read_u16_d16_hi v47,v47
v_or_b32 v42,v56,v58
v_mfma_f32_16x16x32_bf16 v[52:55],a[196:199],a[20:23],v[52:55]
ds_read_u16 v48,v48
v_cvt_pk_bf16_f32 v43,v57,v59
v_lshlrev_b32_sdwa v50,1,v50 dst_sel:DWORD dst_unused:UNUSED_PAD src0_sel:DWORD src1_sel:WORD_1
v_mfma_f32_16x16x32_bf16 v[52:55],a[200:203],a[36:39],v[52:55]
ds_read_u16_d16_hi v50,v50
v_lshlrev_b32_sdwa v49,1,v49 dst_sel:DWORD dst_unused:UNUSED_PAD src0_sel:DWORD src1_sel:WORD_1
v_mfma_f32_16x16x32_bf16 v[56:59],a[212:215],a[36:39],v[20:23]
ds_read_u16 v49,v49
v_lshlrev_b32_sdwa v51,1,v51 dst_sel:DWORD dst_unused:UNUSED_PAD src0_sel:DWORD src1_sel:WORD_1
v_mfma_f32_16x16x32_bf16 v[56:59],a[204:207],a[20:23],v[56:59]
ds_read_u16_d16_hi v51,v51
v_lshlrev_b32_sdwa v60,1,v60 dst_sel:DWORD dst_unused:UNUSED_PAD src0_sel:DWORD src1_sel:WORD_1
v_mfma_f32_16x16x32_bf16 v[56:59],a[208:211],a[4:7],v[56:59]
ds_read_u16 v60,v60
v_lshlrev_b32_sdwa v62,1,v62 dst_sel:DWORD dst_unused:UNUSED_PAD src0_sel:DWORD src1_sel:WORD_1
v_mfma_f32_16x16x32_bf16 v[232:235],a[248:251],v[36:39],v[232:235]
ds_read_u16_d16_hi v62,v62
v_lshlrev_b32_sdwa v61,1,v61 dst_sel:DWORD dst_unused:UNUSED_PAD src0_sel:DWORD src1_sel:WORD_1
v_mfma_f32_16x16x32_bf16 v[236:239],a[248:251],v[40:43],v[236:239]
ds_read_u16 v61,v61
v_lshlrev_b32_sdwa v63,1,v63 dst_sel:DWORD dst_unused:UNUSED_PAD src0_sel:DWORD src1_sel:WORD_1
v_mfma_f32_16x16x32_bf16 v[164:167],a[228:231],v[40:43],v[164:167]
ds_read_u16_d16_hi v63,v63
v_lshlrev_b32_sdwa v32,1,v32 dst_sel:DWORD dst_unused:UNUSED_PAD src0_sel:DWORD src1_sel:WORD_1
v_mfma_f32_16x16x32_bf16 v[160:163],a[228:231],v[36:39],v[160:163]
ds_read_u16 v32,v32
v_lshlrev_b32_sdwa v34,1,v34 dst_sel:DWORD dst_unused:UNUSED_PAD src0_sel:DWORD src1_sel:WORD_1
v_mfma_f32_16x16x32_bf16 v[240:243],a[232:235],v[36:39],v[240:243]
ds_read_u16_d16_hi v34,v34
v_lshlrev_b32_sdwa v33,1,v33 dst_sel:DWORD dst_unused:UNUSED_PAD src0_sel:DWORD src1_sel:WORD_1
v_mfma_f32_16x16x32_bf16 v[244:247],a[232:235],v[40:43],v[244:247]
ds_read_u16 v33,v33
v_lshlrev_b32_sdwa v35,1,v35 dst_sel:DWORD dst_unused:UNUSED_PAD src0_sel:DWORD src1_sel:WORD_1
v_mfma_f32_16x16x32_bf16 v[180:183],a[236:239],v[40:43],v[180:183]
ds_read_u16_d16_hi v35,v35
v_mfma_f32_16x16x32_bf16 v[176:179],a[236:239],v[36:39],v[176:179]
s_waitcnt lgkmcnt(8)
v_mfma_f32_16x16x32_bf16 v[248:251],a[244:247],v[36:39],v[248:251]
v_mfma_f32_16x16x32_bf16 v[252:255],a[244:247],v[40:43],v[252:255]
v_mfma_f32_16x16x32_bf16 v[196:199],a[252:255],v[40:43],v[196:199]
v_mfma_f32_16x16x32_bf16 v[192:195],a[252:255],v[36:39],v[192:195]
v_mfma_f32_16x16x32_bf16 v[40:43],a[208:211],a[8:11],v[20:23]
v_lshlrev_b32_sdwa v24,1,v24 dst_sel:DWORD dst_unused:UNUSED_PAD src0_sel:DWORD src1_sel:WORD_1
v_fma_mixlo_f16 v8,v53,s99,0
v_mfma_f32_16x16x32_bf16 v[40:43],a[204:207],a[24:27],v[40:43]
ds_read_u16 v24,v24
v_lshl_add_u64 v[44:45],v[44:45],0,v[46:47]
v_mfma_f32_16x16x32_bf16 v[40:43],a[212:215],a[40:43],v[40:43]
v_lshl_add_u64 v[46:47],v[48:49],0,v[50:51]
v_lshlrev_b32_sdwa v26,1,v26 dst_sel:DWORD dst_unused:UNUSED_PAD src0_sel:DWORD src1_sel:WORD_1
v_mfma_f32_16x16x32_bf16 v[36:39],a[200:203],a[40:43],v[16:19]
ds_read_u16_d16_hi v26,v26
v_fma_mixhi_f16 v8,v55,s99,0
v_mfma_f32_16x16x32_bf16 v[36:39],a[196:199],a[24:27],v[36:39]
s_waitcnt lgkmcnt(2)
v_lshl_add_u64 v[48:49],v[60:61],0,v[62:63]
v_lshlrev_b32_sdwa v25,1,v25 dst_sel:DWORD dst_unused:UNUSED_PAD src0_sel:DWORD src1_sel:WORD_1
v_pk_fma_f16 v9,v8,v8,s54
v_mfma_f32_16x16x32_bf16 v[36:39],a[192:195],a[8:11],v[36:39]
ds_read_u16 v25,v25
v_lshlrev_b32_sdwa v27,1,v27 dst_sel:DWORD dst_unused:UNUSED_PAD src0_sel:DWORD src1_sel:WORD_1
v_mfma_f32_16x16x32_bf16 v[60:63],a[192:195],a[12:15],v[16:19]
ds_read_b128 v[16:19],v4 offset:512
v_pk_mul_f16 v9,v8,v9
v_mfma_f32_16x16x32_bf16 v[60:63],a[196:199],a[28:31],v[60:63]
ds_read_u16_d16_hi v27,v27
ds_read_b64_tr_b16 a[192:193],v1 offset:22576
v_lshl_add_u64 v[50:51],v[32:33],0,v[34:35]
v_lshlrev_b32_sdwa v28,1,v28 dst_sel:DWORD dst_unused:UNUSED_PAD src0_sel:DWORD src1_sel:WORD_1
v_mfma_f32_16x16x32_bf16 v[60:63],a[200:203],a[44:47],v[60:63]
ds_read_b64_tr_b16 a[194:195],v1 offset:23600
v_exp_f16 v8,v9
v_mfma_f32_16x16x32_bf16 v[32:35],a[212:215],a[44:47],v[20:23]
ds_read_b128 v[20:23],v4 offset:544
ds_read_u16 v28,v28
v_lshlrev_b32_sdwa v30,1,v30 dst_sel:DWORD dst_unused:UNUSED_PAD src0_sel:DWORD src1_sel:WORD_1
v_mfma_f32_16x16x32_bf16 v[32:35],a[204:207],a[28:31],v[32:35]
ds_read_u16_d16_hi v30,v30
ds_read_b64_tr_b16 a[196:197],v2 offset:22576
v_lshlrev_b32_sdwa v29,1,v29 dst_sel:DWORD dst_unused:UNUSED_PAD src0_sel:DWORD src1_sel:WORD_1
v_mfma_f32_16x16x32_bf16 v[32:35],a[208:211],a[12:15],v[32:35]
ds_read_u16 v29,v29
v_exp_f16_sdwa v8,v9 dst_sel:WORD_1 dst_unused:UNUSED_PRESERVE src0_sel:WORD_1
v_mfma_f32_16x16x32_bf16 v[208:211],a[248:251],v[44:47],v[208:211]
ds_read_b64_tr_b16 a[198:199],v2 offset:23600
ds_read_b64_tr_b16 a[200:201],v2 offset:20528
v_lshlrev_b32_sdwa v31,1,v31 dst_sel:DWORD dst_unused:UNUSED_PAD src0_sel:DWORD src1_sel:WORD_1
v_mfma_f32_16x16x32_bf16 v[212:215],a[248:251],v[48:51],v[212:215]
ds_read_u16_d16_hi v31,v31
v_lshlrev_b32_sdwa v56,1,v56 dst_sel:DWORD dst_unused:UNUSED_PAD src0_sel:DWORD src1_sel:WORD_1
v_mfma_f32_16x16x32_bf16 v[172:175],a[228:231],v[48:51],v[172:175]
ds_read_b64_tr_b16 a[202:203],v2 offset:21552
v_pk_fma_f16 v8,v8,s55,s55
v_mfma_f32_16x16x32_bf16 v[168:171],a[228:231],v[44:47],v[168:171]
v_rcp_f16 v9,v8
v_lshlrev_b32_sdwa v58,1,v58 dst_sel:DWORD dst_unused:UNUSED_PAD src0_sel:DWORD src1_sel:WORD_1
v_mfma_f32_16x16x32_bf16 v[216:219],a[232:235],v[44:47],v[216:219]
ds_read_u16 v56,v56
v_lshlrev_b32_sdwa v57,1,v57 dst_sel:DWORD dst_unused:UNUSED_PAD src0_sel:DWORD src1_sel:WORD_1
v_mfma_f32_16x16x32_bf16 v[220:223],a[232:235],v[48:51],v[220:223]
ds_read_u16_d16_hi v58,v58
v_lshlrev_b32_sdwa v59,1,v59 dst_sel:DWORD dst_unused:UNUSED_PAD src0_sel:DWORD src1_sel:WORD_1
v_mfma_f32_16x16x32_bf16 v[188:191],a[236:239],v[48:51],v[188:191]
ds_read_u16 v57,v57
v_lshlrev_b32_sdwa v52,1,v52 dst_sel:DWORD dst_unused:UNUSED_PAD src0_sel:DWORD src1_sel:WORD_1
v_mfma_f32_16x16x32_bf16 v[184:187],a[236:239],v[44:47],v[184:187]
ds_read_u16_d16_hi v59,v59
v_rcp_f16_sdwa v9,v8 dst_sel:WORD_1 dst_unused:UNUSED_PRESERVE src0_sel:WORD_1
v_mfma_f32_16x16x32_bf16 v[224:227],a[244:247],v[44:47],v[224:227]
ds_read_b64_tr_b16 a[212:213],v2 offset:18480
v_lshlrev_b32_sdwa v54,1,v54 dst_sel:DWORD dst_unused:UNUSED_PAD src0_sel:DWORD src1_sel:WORD_1
v_mfma_f32_16x16x32_bf16 v[228:231],a[244:247],v[48:51],v[228:231]
ds_read_b64_tr_b16 a[214:215],v2 offset:19504
v_fma_mix_f32 v53,v53,v9,0 op_sel_hi:[0,1,0]
v_mfma_f32_16x16x32_bf16 v[204:207],a[252:255],v[48:51],v[204:207]
ds_read_u16 v52,v52
v_fma_mix_f32 v55,v55,v9,0 op_sel:[0,1,0] op_sel_hi:[0,1,0]
v_mfma_f32_16x16x32_bf16 v[200:203],a[252:255],v[44:47],v[200:203]
s_waitcnt lgkmcnt(7)
ds_read_u16_d16_hi v54,v54
v_mfma_f32_16x16x32_bf16 v[48:51],a[220:223],a[48:51],v[16:19]
v_lshlrev_b32_sdwa v40,1,v40 dst_sel:DWORD dst_unused:UNUSED_PAD src0_sel:DWORD src1_sel:WORD_1
v_lshlrev_b32_sdwa v42,1,v42 dst_sel:DWORD dst_unused:UNUSED_PAD src0_sel:DWORD src1_sel:WORD_1
v_mfma_f32_16x16x32_bf16 v[48:51],a[216:219],a[64:67],v[48:51]
ds_read_u16 v40,v40
v_lshl_add_u64 v[24:25],v[24:25],0,v[26:27]
v_mfma_f32_16x16x32_bf16 v[48:51],a[192:195],a[80:83],v[48:51]
ds_read_u16_d16_hi v42,v42
v_lshl_add_u64 v[26:27],v[28:29],0,v[30:31]
v_mfma_f32_16x16x32_bf16 v[44:47],a[196:199],a[80:83],v[20:23]
v_lshlrev_b32_sdwa v41,1,v41 dst_sel:DWORD dst_unused:UNUSED_PAD src0_sel:DWORD src1_sel:WORD_1
v_lshlrev_b32_sdwa v43,1,v43 dst_sel:DWORD dst_unused:UNUSED_PAD src0_sel:DWORD src1_sel:WORD_1
v_mfma_f32_16x16x32_bf16 v[44:47],a[200:203],a[64:67],v[44:47]
s_waitcnt lgkmcnt(4)
ds_read_u16 v41,v41
v_lshl_add_u64 v[28:29],v[56:57],0,v[58:59]
v_mfma_f32_16x16x32_bf16 v[44:47],a[212:215],a[48:51],v[44:47]
ds_read_u16_d16_hi v43,v43
v_lshlrev_b32_sdwa v36,1,v36 dst_sel:DWORD dst_unused:UNUSED_PAD src0_sel:DWORD src1_sel:WORD_1
v_mfma_f32_16x16x32_bf16 v[56:59],a[212:215],a[52:55],v[20:23]
s_waitcnt lgkmcnt(4)
ds_read_u16 v36,v36
v_or_b32 v30,v52,v54
v_mfma_f32_16x16x32_bf16 v[56:59],a[200:203],a[68:71],v[56:59]
s_waitcnt vmcnt(6)
v_cvt_pk_bf16_f32 v31,v53,v55
v_lshlrev_b32_sdwa v38,1,v38 dst_sel:DWORD dst_unused:UNUSED_PAD src0_sel:DWORD src1_sel:WORD_1
v_mfma_f32_16x16x32_bf16 v[56:59],a[196:199],a[84:87],v[56:59]
s_barrier
ds_read_u16_d16_hi v38,v38
v_lshlrev_b32_sdwa v37,1,v37 dst_sel:DWORD dst_unused:UNUSED_PAD src0_sel:DWORD src1_sel:WORD_1
v_mfma_f32_16x16x32_bf16 v[52:55],a[192:195],a[84:87],v[16:19]
ds_read_u16 v37,v37
v_lshlrev_b32_sdwa v39,1,v39 dst_sel:DWORD dst_unused:UNUSED_PAD src0_sel:DWORD src1_sel:WORD_1
v_mfma_f32_16x16x32_bf16 v[52:55],a[216:219],a[68:71],v[52:55]
s_add_u32 m0,s58,12320
ds_read_u16_d16_hi v39,v39
v_lshlrev_b32_sdwa v32,1,v32 dst_sel:DWORD dst_unused:UNUSED_PAD src0_sel:DWORD src1_sel:WORD_1
v_mfma_f32_16x16x32_bf16 v[52:55],a[220:223],a[52:55],v[52:55]
s_add_u32 s69,s69,s59
ds_read_u16 v32,v32
v_lshlrev_b32_sdwa v34,1,v34 dst_sel:DWORD dst_unused:UNUSED_PAD src0_sel:DWORD src1_sel:WORD_1
v_mfma_f32_16x16x32_bf16 v[136:139],a[248:251],v[24:27],v[136:139]
buffer_load_dwordx4 v5,s[44:47],s69 offen offset:0 lds
v_lshlrev_b32_sdwa v33,1,v33 dst_sel:DWORD dst_unused:UNUSED_PAD src0_sel:DWORD src1_sel:WORD_1
v_mfma_f32_16x16x32_bf16 v[140:143],a[248:251],v[28:31],v[140:143]
ds_read_u16_d16_hi v34,v34
v_lshlrev_b32_sdwa v35,1,v35 dst_sel:DWORD dst_unused:UNUSED_PAD src0_sel:DWORD src1_sel:WORD_1
v_mfma_f32_16x16x32_bf16 v[68:71],a[228:231],v[28:31],v[68:71]
ds_read_u16 v33,v33
ds_read_u16_d16_hi v35,v35
v_mfma_f32_16x16x32_bf16 v[64:67],a[228:231],v[24:27],v[64:67]
ds_read_b64_tr_b16 a[240:241],v3 offset:0
v_lshlrev_b32_sdwa v60,1,v60 dst_sel:DWORD dst_unused:UNUSED_PAD src0_sel:DWORD src1_sel:WORD_1
v_mfma_f32_16x16x32_bf16 v[144:147],a[232:235],v[24:27],v[144:147]
s_add_u32 s69,s69,s53
ds_read_u16 v60,v60
v_lshlrev_b32_sdwa v62,1,v62 dst_sel:DWORD dst_unused:UNUSED_PAD src0_sel:DWORD src1_sel:WORD_1
v_mfma_f32_16x16x32_bf16 v[148:151],a[232:235],v[28:31],v[148:151]
buffer_load_dwordx4 v5,s[44:47],s69 offen offset:1024 lds
ds_read_u16_d16_hi v62,v62
v_mfma_f32_16x16x32_bf16 v[84:87],a[236:239],v[28:31],v[84:87]
ds_read_b64_tr_b16 a[242:243],v3 offset:1536
v_lshlrev_b32_sdwa v61,1,v61 dst_sel:DWORD dst_unused:UNUSED_PAD src0_sel:DWORD src1_sel:WORD_1
v_mfma_f32_16x16x32_bf16 v[80:83],a[236:239],v[24:27],v[80:83]
ds_read_u16 v61,v61
v_lshlrev_b32_sdwa v63,1,v63 dst_sel:DWORD dst_unused:UNUSED_PAD src0_sel:DWORD src1_sel:WORD_1
v_mfma_f32_16x16x32_bf16 v[152:155],a[244:247],v[24:27],v[152:155]
ds_read_u16_d16_hi v63,v63
v_mfma_f32_16x16x32_bf16 v[156:159],a[244:247],v[28:31],v[156:159]
s_add_u32 s69,s69,s53
ds_read_b64_tr_b16 a[224:225],v3 offset:8
v_mfma_f32_16x16x32_bf16 v[100:103],a[252:255],v[28:31],v[100:103]
buffer_load_dwordx4 v5,s[44:47],s69 offen offset:2048 lds
v_mfma_f32_16x16x32_bf16 v[96:99],a[252:255],v[24:27],v[96:99]
ds_read_b64_tr_b16 a[226:227],v3 offset:1544
v_mfma_f32_16x16x32_bf16 v[28:31],a[220:223],a[56:59],v[16:19]
ds_read_b64_tr_b16 a[204:205],v1 offset:0
v_lshlrev_b32_sdwa v48,1,v48 dst_sel:DWORD dst_unused:UNUSED_PAD src0_sel:DWORD src1_sel:WORD_1
v_mfma_f32_16x16x32_bf16 v[28:31],a[216:219],a[72:75],v[28:31]
s_waitcnt lgkmcnt(7)
ds_read_u16 v48,v48
v_lshl_add_u64 v[40:41],v[40:41],0,v[42:43]
v_fma_mixlo_f16 v8,v57,s99,0
v_mfma_f32_16x16x32_bf16 v[28:31],a[192:195],a[88:91],v[28:31]
ds_read_b64_tr_b16 a[206:207],v1 offset:1024
v_lshl_add_u64 v[42:43],v[36:37],0,v[38:39]
v_lshlrev_b32_sdwa v50,1,v50 dst_sel:DWORD dst_unused:UNUSED_PAD src0_sel:DWORD src1_sel:WORD_1
v_mfma_f32_16x16x32_bf16 v[24:27],a[196:199],a[88:91],v[20:23]
ds_read_u16_d16_hi v50,v50
v_fma_mixhi_f16 v8,v59,s99,0
v_mfma_f32_16x16x32_bf16 v[24:27],a[200:203],a[72:75],v[24:27]
ds_read_b64_tr_b16 a[208:209],v1 offset:2048
v_lshl_add_u64 v[36:37],v[32:33],0,v[34:35]
v_lshlrev_b32_sdwa v49,1,v49 dst_sel:DWORD dst_unused:UNUSED_PAD src0_sel:DWORD src1_sel:WORD_1
v_pk_fma_f16 v9,v8,v8,s54
v_mfma_f32_16x16x32_bf16 v[24:27],a[212:215],a[56:59],v[24:27]
ds_read_u16 v49,v49
v_lshlrev_b32_sdwa v51,1,v51 dst_sel:DWORD dst_unused:UNUSED_PAD src0_sel:DWORD src1_sel:WORD_1
v_mfma_f32_16x16x32_bf16 v[32:35],a[212:215],a[60:63],v[20:23]
ds_read_u16_d16_hi v51,v51
v_pk_mul_f16 v9,v8,v9
v_mfma_f32_16x16x32_bf16 v[32:35],a[200:203],a[76:79],v[32:35]
s_waitcnt lgkmcnt(8)
ds_read_b64_tr_b16 a[210:211],v1 offset:3072
v_lshl_add_u64 v[38:39],v[60:61],0,v[62:63]
v_lshlrev_b32_sdwa v44,1,v44 dst_sel:DWORD dst_unused:UNUSED_PAD src0_sel:DWORD src1_sel:WORD_1
v_mfma_f32_16x16x32_bf16 v[32:35],a[196:199],a[92:95],v[32:35]
v_exp_f16 v8,v9
v_mfma_f32_16x16x32_bf16 v[60:63],a[192:195],a[92:95],v[16:19]
ds_read_u16 v44,v44
v_lshlrev_b32_sdwa v46,1,v46 dst_sel:DWORD dst_unused:UNUSED_PAD src0_sel:DWORD src1_sel:WORD_1
v_mfma_f32_16x16x32_bf16 v[60:63],a[216:219],a[76:79],v[60:63]
ds_read_u16_d16_hi v46,v46
v_lshlrev_b32_sdwa v45,1,v45 dst_sel:DWORD dst_unused:UNUSED_PAD src0_sel:DWORD src1_sel:WORD_1
v_mfma_f32_16x16x32_bf16 v[60:63],a[220:223],a[60:63],v[60:63]
v_exp_f16_sdwa v8,v9 dst_sel:WORD_1 dst_unused:UNUSED_PRESERVE src0_sel:WORD_1
v_mfma_f32_16x16x32_bf16 v[112:115],a[248:251],v[40:43],v[112:115]
ds_read_u16 v45,v45
v_lshlrev_b32_sdwa v47,1,v47 dst_sel:DWORD dst_unused:UNUSED_PAD src0_sel:DWORD src1_sel:WORD_1
v_mfma_f32_16x16x32_bf16 v[116:119],a[248:251],v[36:39],v[116:119]
ds_read_u16_d16_hi v47,v47
v_lshlrev_b32_sdwa v52,1,v52 dst_sel:DWORD dst_unused:UNUSED_PAD src0_sel:DWORD src1_sel:WORD_1
v_mfma_f32_16x16x32_bf16 v[76:79],a[228:231],v[36:39],v[76:79]
ds_read_u16 v52,v52
v_pk_fma_f16 v8,v8,s55,s55
v_mfma_f32_16x16x32_bf16 v[72:75],a[228:231],v[40:43],v[72:75]
ds_read_b64_tr_b16 a[248:249],v3 offset:64
v_rcp_f16_e64 v9,v8
v_lshlrev_b32_sdwa v54,1,v54 dst_sel:DWORD dst_unused:UNUSED_PAD src0_sel:DWORD src1_sel:WORD_1
v_mfma_f32_16x16x32_bf16 v[120:123],a[232:235],v[40:43],v[120:123]
ds_read_u16_d16_hi v54,v54
v_lshlrev_b32_sdwa v53,1,v53 dst_sel:DWORD dst_unused:UNUSED_PAD src0_sel:DWORD src1_sel:WORD_1
v_mfma_f32_16x16x32_bf16 v[124:127],a[232:235],v[36:39],v[124:127]
ds_read_u16 v53,v53
ds_read_b64_tr_b16 a[250:251],v3 offset:1600
v_mfma_f32_16x16x32_bf16 v[92:95],a[236:239],v[36:39],v[92:95]
ds_read_b64_tr_b16 a[228:229],v3 offset:72
v_lshlrev_b32_sdwa v55,1,v55 dst_sel:DWORD dst_unused:UNUSED_PAD src0_sel:DWORD src1_sel:WORD_1
v_mfma_f32_16x16x32_bf16 v[88:91],a[236:239],v[40:43],v[88:91]
ds_read_u16_d16_hi v55,v55
v_rcp_f16_sdwa v9,v8 dst_sel:WORD_1 dst_unused:UNUSED_PRESERVE src0_sel:WORD_1
v_mfma_f32_16x16x32_bf16 v[128:131],a[244:247],v[40:43],v[128:131]
ds_read_b64_tr_b16 a[230:231],v3 offset:1608
ds_read_b64_tr_b16 a[232:233],v3 offset:128
v_lshlrev_b32_sdwa v56,1,v56 dst_sel:DWORD dst_unused:UNUSED_PAD src0_sel:DWORD src1_sel:WORD_1
v_mfma_f32_16x16x32_bf16 v[132:135],a[244:247],v[36:39],v[132:135]
ds_read_u16 v56,v56
v_lshlrev_b32_sdwa v58,1,v58 dst_sel:DWORD dst_unused:UNUSED_PAD src0_sel:DWORD src1_sel:WORD_1
v_mfma_f32_16x16x32_bf16 v[108:111],a[252:255],v[36:39],v[108:111]
ds_read_u16_d16_hi v58,v58
v_fma_mix_f32 v57,v57,v9,0 op_sel_hi:[0,1,0]
v_mfma_f32_16x16x32_bf16 v[104:107],a[252:255],v[40:43],v[104:107]
ds_read_b64_tr_b16 a[234:235],v3 offset:1664
v_fma_mix_f32 v59,v59,v9,0 op_sel:[0,1,0] op_sel_hi:[0,1,0]
v_mfma_f32_16x16x32_bf16 v[36:39],a[220:223],a[0:3],v[16:19]
s_add_u32 m0,s58,18480
ds_read_b64_tr_b16 a[236:237],v3 offset:136
v_lshlrev_b32_sdwa v28,1,v28 dst_sel:DWORD dst_unused:UNUSED_PAD src0_sel:DWORD src1_sel:WORD_1
v_mfma_f32_16x16x32_bf16 v[36:39],a[216:219],a[16:19],v[36:39]
s_waitcnt lgkmcnt(10)
ds_read_b64_tr_b16 a[238:239],v3 offset:1672
v_lshl_add_u64 v[48:49],v[48:49],0,v[50:51]
v_mfma_f32_16x16x32_bf16 v[36:39],a[192:195],a[32:35],v[36:39]
s_add_u32 s69,s69,s59
ds_read_u16 v28,v28
v_lshl_add_u64 v[50:51],v[44:45],0,v[46:47]
v_lshlrev_b32_sdwa v30,1,v30 dst_sel:DWORD dst_unused:UNUSED_PAD src0_sel:DWORD src1_sel:WORD_1
v_mfma_f32_16x16x32_bf16 v[40:43],a[196:199],a[32:35],v[20:23]
buffer_load_dwordx4 v5,s[44:47],s69 offen offset:0 lds
v_lshlrev_b32_sdwa v29,1,v29 dst_sel:DWORD dst_unused:UNUSED_PAD src0_sel:DWORD src1_sel:WORD_1
v_mfma_f32_16x16x32_bf16 v[40:43],a[200:203],a[16:19],v[40:43]
s_waitcnt lgkmcnt(8)
ds_read_u16_d16_hi v30,v30
v_lshl_add_u64 v[44:45],v[52:53],0,v[54:55]
v_mfma_f32_16x16x32_bf16 v[40:43],a[212:215],a[0:3],v[40:43]
s_add_u32 s69,s69,s53
ds_read_u16 v29,v29
v_lshlrev_b32_sdwa v31,1,v31 dst_sel:DWORD dst_unused:UNUSED_PAD src0_sel:DWORD src1_sel:WORD_1
v_mfma_f32_16x16x32_bf16 v[52:55],a[212:215],a[4:7],v[20:23]
s_waitcnt lgkmcnt(6)
buffer_load_dwordx4 v5,s[44:47],s69 offen offset:1024 lds
v_or_b32 v46,v56,v58
v_lshlrev_b32_sdwa v24,1,v24 dst_sel:DWORD dst_unused:UNUSED_PAD src0_sel:DWORD src1_sel:WORD_1
v_mfma_f32_16x16x32_bf16 v[52:55],a[200:203],a[20:23],v[52:55]
s_add_u32 s69,s69,s53
ds_read_u16_d16_hi v31,v31
v_cvt_pk_bf16_f32 v47,v57,v59
v_lshlrev_b32_sdwa v26,1,v26 dst_sel:DWORD dst_unused:UNUSED_PAD src0_sel:DWORD src1_sel:WORD_1
v_mfma_f32_16x16x32_bf16 v[52:55],a[196:199],a[36:39],v[52:55]
buffer_load_dwordx4 v5,s[44:47],s69 offen offset:2048 lds
v_lshlrev_b32_sdwa v25,1,v25 dst_sel:DWORD dst_unused:UNUSED_PAD src0_sel:DWORD src1_sel:WORD_1
v_mfma_f32_16x16x32_bf16 v[56:59],a[192:195],a[36:39],v[16:19]
ds_read_u16 v24,v24
v_lshlrev_b32_sdwa v27,1,v27 dst_sel:DWORD dst_unused:UNUSED_PAD src0_sel:DWORD src1_sel:WORD_1
v_mfma_f32_16x16x32_bf16 v[56:59],a[216:219],a[20:23],v[56:59]
ds_read_u16_d16_hi v26,v26
v_lshlrev_b32_sdwa v60,1,v60 dst_sel:DWORD dst_unused:UNUSED_PAD src0_sel:DWORD src1_sel:WORD_1
v_mfma_f32_16x16x32_bf16 v[56:59],a[220:223],a[4:7],v[56:59]
ds_read_u16 v25,v25
v_lshlrev_b32_sdwa v62,1,v62 dst_sel:DWORD dst_unused:UNUSED_PAD src0_sel:DWORD src1_sel:WORD_1
v_mfma_f32_16x16x32_bf16 v[232:235],a[240:243],v[48:51],v[232:235]
ds_read_u16_d16_hi v27,v27
v_lshlrev_b32_sdwa v61,1,v61 dst_sel:DWORD dst_unused:UNUSED_PAD src0_sel:DWORD src1_sel:WORD_1
v_mfma_f32_16x16x32_bf16 v[236:239],a[240:243],v[44:47],v[236:239]
ds_read_u16 v60,v60
v_lshlrev_b32_sdwa v63,1,v63 dst_sel:DWORD dst_unused:UNUSED_PAD src0_sel:DWORD src1_sel:WORD_1
v_mfma_f32_16x16x32_bf16 v[164:167],a[224:227],v[44:47],v[164:167]
TP7016_%=:
s_cbranch_execz TR0_%=
ds_read_u16_d16_hi v62,v62
v_lshlrev_b32_sdwa v32,1,v32 dst_sel:DWORD dst_unused:UNUSED_PAD src0_sel:DWORD src1_sel:WORD_1
v_mfma_f32_16x16x32_bf16 v[160:163],a[224:227],v[48:51],v[160:163]
ds_read_u16 v61,v61
v_lshlrev_b32_sdwa v34,1,v34 dst_sel:DWORD dst_unused:UNUSED_PAD src0_sel:DWORD src1_sel:WORD_1
v_mfma_f32_16x16x32_bf16 v[240:243],a[248:251],v[48:51],v[240:243]
ds_read_u16_d16_hi v63,v63
v_lshlrev_b32_sdwa v33,1,v33 dst_sel:DWORD dst_unused:UNUSED_PAD src0_sel:DWORD src1_sel:WORD_1
v_mfma_f32_16x16x32_bf16 v[244:247],a[248:251],v[44:47],v[244:247]
ds_read_u16 v32,v32
v_lshlrev_b32_sdwa v35,1,v35 dst_sel:DWORD dst_unused:UNUSED_PAD src0_sel:DWORD src1_sel:WORD_1
v_mfma_f32_16x16x32_bf16 v[180:183],a[228:231],v[44:47],v[180:183]
s_nop 0
ds_read_u16_d16_hi v34,v34
v_mfma_f32_16x16x32_bf16 v[176:179],a[228:231],v[48:51],v[176:179]
s_waitcnt lgkmcnt(8)
ds_read_u16 v33,v33
v_mfma_f32_16x16x32_bf16 v[248:251],a[232:235],v[48:51],v[248:251]
ds_read_u16_d16_hi v35,v35
v_mfma_f32_16x16x32_bf16 v[252:255],a[232:235],v[44:47],v[252:255]
v_mfma_f32_16x16x32_bf16 v[196:199],a[236:239],v[44:47],v[196:199]
v_mfma_f32_16x16x32_bf16 v[192:195],a[236:239],v[48:51],v[192:195]
v_mfma_f32_16x16x32_bf16 v[44:47],a[220:223],a[8:11],v[16:19]
v_mul_f32_dpp v232,v14,v232 row_newbcast:0 row_mask:0xf bank_mask:0xf
s_add_u32 s10,s61,1572864
v_mul_f32_dpp v233,v14,v233 row_newbcast:1 row_mask:0xf bank_mask:0xf
v_mul_f32_dpp v234,v14,v234 row_newbcast:2 row_mask:0xf bank_mask:0xf
v_lshlrev_b32_sdwa v36,1,v36 dst_sel:DWORD dst_unused:UNUSED_PAD src0_sel:DWORD src1_sel:WORD_1
v_mfma_f32_16x16x32_bf16 v[44:47],a[216:219],a[24:27],v[44:47]
ds_read_u16 v36,v36
v_lshl_add_u64 v[28:29],v[28:29],0,v[30:31]
v_mul_f32_dpp v235,v14,v235 row_newbcast:3 row_mask:0xf bank_mask:0xf
v_mul_f32_dpp v160,v14,v160 row_newbcast:4 row_mask:0xf bank_mask:0xf
v_mul_f32_dpp v161,v14,v161 row_newbcast:5 row_mask:0xf bank_mask:0xf
v_mfma_f32_16x16x32_bf16 v[44:47],a[192:195],a[40:43],v[44:47]
s_waitcnt lgkmcnt(4)
v_lshl_add_u64 v[30:31],v[24:25],0,v[26:27]
v_mul_f32_dpp v162,v14,v162 row_newbcast:6 row_mask:0xf bank_mask:0xf
v_mul_f32_dpp v163,v14,v163 row_newbcast:7 row_mask:0xf bank_mask:0xf
v_lshlrev_b32_sdwa v38,1,v38 dst_sel:DWORD dst_unused:UNUSED_PAD src0_sel:DWORD src1_sel:WORD_1
v_cvt_pk_bf16_f32 v232,v232,v233
v_mfma_f32_16x16x32_bf16 v[48:51],a[196:199],a[40:43],v[20:23]
s_nop 0
ds_read_u16_d16_hi v38,v38
v_cvt_pk_bf16_f32 v233,v234,v235
v_cvt_pk_bf16_f32 v234,v160,v161
v_lshlrev_b32_sdwa v37,1,v37 dst_sel:DWORD dst_unused:UNUSED_PAD src0_sel:DWORD src1_sel:WORD_1
v_cvt_pk_bf16_f32 v235,v162,v163
v_mfma_f32_16x16x32_bf16 v[48:51],a[200:203],a[24:27],v[48:51]
buffer_store_dwordx4 v[232:235],v7,s[40:43],s10 offen offset:0 sc1
v_lshl_add_u64 v[24:25],v[60:61],0,v[62:63]
v_mul_f32_dpp v240,v14,v240 row_newbcast:8 row_mask:0xf bank_mask:0xf
v_mul_f32_dpp v241,v14,v241 row_newbcast:9 row_mask:0xf bank_mask:0xf
v_mul_f32_dpp v242,v14,v242 row_newbcast:10 row_mask:0xf bank_mask:0xf
v_mfma_f32_16x16x32_bf16 v[48:51],a[212:215],a[8:11],v[48:51]
ds_read_u16 v37,v37
v_mul_f32_dpp v243,v14,v243 row_newbcast:11 row_mask:0xf bank_mask:0xf
v_mul_f32_dpp v176,v14,v176 row_newbcast:12 row_mask:0xf bank_mask:0xf
v_lshlrev_b32_sdwa v39,1,v39 dst_sel:DWORD dst_unused:UNUSED_PAD src0_sel:DWORD src1_sel:WORD_1
v_mul_f32_dpp v177,v14,v177 row_newbcast:13 row_mask:0xf bank_mask:0xf
v_mfma_f32_16x16x32_bf16 v[60:63],a[212:215],a[12:15],v[20:23]
s_nop 0
ds_read_b128 v[20:23],v4 offset:640
v_mul_f32_dpp v178,v14,v178 row_newbcast:14 row_mask:0xf bank_mask:0xf
v_mul_f32_dpp v179,v14,v179 row_newbcast:15 row_mask:0xf bank_mask:0xf
v_lshlrev_b32_sdwa v40,1,v40 dst_sel:DWORD dst_unused:UNUSED_PAD src0_sel:DWORD src1_sel:WORD_1
v_cvt_pk_bf16_f32 v240,v240,v241
v_mfma_f32_16x16x32_bf16 v[60:63],a[200:203],a[28:31],v[60:63]
s_waitcnt lgkmcnt(4)
ds_read_u16_d16_hi v39,v39
ds_read_b64_tr_b16 a[212:213],v1 offset:4096
v_lshl_add_u64 v[26:27],v[32:33],0,v[34:35]
v_cvt_pk_bf16_f32 v241,v242,v243
v_cvt_pk_bf16_f32 v242,v176,v177
v_cvt_pk_bf16_f32 v243,v178,v179
v_lshlrev_b32_sdwa v42,1,v42 dst_sel:DWORD dst_unused:UNUSED_PAD src0_sel:DWORD src1_sel:WORD_1
v_mfma_f32_16x16x32_bf16 v[60:63],a[196:199],a[44:47],v[60:63]
ds_read_b64_tr_b16 a[214:215],v1 offset:5120
ds_read_u16 v40,v40
v_mul_f32_dpp v248,v15,v248 row_newbcast:0 row_mask:0xf bank_mask:0xf
v_mul_f32_dpp v249,v15,v249 row_newbcast:1 row_mask:0xf bank_mask:0xf
v_mul_f32_dpp v250,v15,v250 row_newbcast:2 row_mask:0xf bank_mask:0xf
v_mfma_f32_16x16x32_bf16 v[32:35],a[192:195],a[44:47],v[16:19]
ds_read_b128 v[16:19],v4 offset:672
ds_read_u16_d16_hi v42,v42
v_mul_f32_dpp v251,v15,v251 row_newbcast:3 row_mask:0xf bank_mask:0xf
v_mul_f32_dpp v192,v15,v192 row_newbcast:4 row_mask:0xf bank_mask:0xf
v_lshlrev_b32_sdwa v41,1,v41 dst_sel:DWORD dst_unused:UNUSED_PAD src0_sel:DWORD src1_sel:WORD_1
v_mul_f32_dpp v193,v15,v193 row_newbcast:5 row_mask:0xf bank_mask:0xf
v_mfma_f32_16x16x32_bf16 v[32:35],a[216:219],a[28:31],v[32:35]
ds_read_u16 v41,v41
ds_read_b64_tr_b16 a[200:201],v2 offset:4096
v_mul_f32_dpp v194,v15,v194 row_newbcast:6 row_mask:0xf bank_mask:0xf
v_mul_f32_dpp v195,v15,v195 row_newbcast:7 row_mask:0xf bank_mask:0xf
v_lshlrev_b32_sdwa v43,1,v43 dst_sel:DWORD dst_unused:UNUSED_PAD src0_sel:DWORD src1_sel:WORD_1
v_cvt_pk_bf16_f32 v248,v248,v249
v_mfma_f32_16x16x32_bf16 v[32:35],a[220:223],a[12:15],v[32:35]
ds_read_u16_d16_hi v43,v43
ds_read_b64_tr_b16 a[202:203],v2 offset:5120
v_cvt_pk_bf16_f32 v249,v250,v251
v_cvt_pk_bf16_f32 v250,v192,v193
v_cvt_pk_bf16_f32 v251,v194,v195
v_mfma_f32_16x16x32_bf16 v[208:211],a[240:243],v[28:31],v[208:211]
ds_read_b64_tr_b16 a[196:197],v2 offset:2048
v_mul_f32_dpp v236,v14,v236 row_newbcast:0 row_mask:0xf bank_mask:0xf
v_mul_f32_dpp v237,v14,v237 row_newbcast:1 row_mask:0xf bank_mask:0xf
v_lshlrev_b32_sdwa v56,1,v56 dst_sel:DWORD dst_unused:UNUSED_PAD src0_sel:DWORD src1_sel:WORD_1
v_mul_f32_dpp v238,v14,v238 row_newbcast:2 row_mask:0xf bank_mask:0xf
v_mfma_f32_16x16x32_bf16 v[212:215],a[240:243],v[24:27],v[212:215]
ds_read_b64_tr_b16 a[198:199],v2 offset:3072
v_mul_f32_dpp v239,v14,v239 row_newbcast:3 row_mask:0xf bank_mask:0xf
v_mul_f32_dpp v164,v14,v164 row_newbcast:4 row_mask:0xf bank_mask:0xf
v_lshlrev_b32_sdwa v58,1,v58 dst_sel:DWORD dst_unused:UNUSED_PAD src0_sel:DWORD src1_sel:WORD_1
v_mul_f32_dpp v165,v14,v165 row_newbcast:5 row_mask:0xf bank_mask:0xf
v_mfma_f32_16x16x32_bf16 v[172:175],a[224:227],v[24:27],v[172:175]
ds_read_u16 v56,v56
v_mul_f32_dpp v166,v14,v166 row_newbcast:6 row_mask:0xf bank_mask:0xf
v_mul_f32_dpp v167,v14,v167 row_newbcast:7 row_mask:0xf bank_mask:0xf
v_cvt_pk_bf16_f32 v236,v236,v237
v_lshlrev_b32_sdwa v57,1,v57 dst_sel:DWORD dst_unused:UNUSED_PAD src0_sel:DWORD src1_sel:WORD_1
v_mfma_f32_16x16x32_bf16 v[168:171],a[224:227],v[28:31],v[168:171]
buffer_store_dwordx4 v[240:243],v7,s[40:43],s10 offen offset:64 sc1
ds_read_u16_d16_hi v58,v58
v_cvt_pk_bf16_f32 v237,v238,v239
v_cvt_pk_bf16_f32 v238,v164,v165
v_cvt_pk_bf16_f32 v239,v166,v167
v_mfma_f32_16x16x32_bf16 v[216:219],a[248:251],v[28:31],v[216:219]
ds_read_u16 v57,v57
v_mul_f32_dpp v244,v14,v244 row_newbcast:8 row_mask:0xf bank_mask:0xf
v_mul_f32_dpp v245,v14,v245 row_newbcast:9 row_mask:0xf bank_mask:0xf
v_lshlrev_b32_sdwa v59,1,v59 dst_sel:DWORD dst_unused:UNUSED_PAD src0_sel:DWORD src1_sel:WORD_1
v_mul_f32_dpp v246,v14,v246 row_newbcast:10 row_mask:0xf bank_mask:0xf
v_mfma_f32_16x16x32_bf16 v[220:223],a[248:251],v[24:27],v[220:223]
ds_read_u16_d16_hi v59,v59
v_mul_f32_dpp v247,v14,v247 row_newbcast:11 row_mask:0xf bank_mask:0xf
v_mul_f32_dpp v180,v14,v180 row_newbcast:12 row_mask:0xf bank_mask:0xf
v_lshlrev_b32_sdwa v52,1,v52 dst_sel:DWORD dst_unused:UNUSED_PAD src0_sel:DWORD src1_sel:WORD_1
v_mul_f32_dpp v181,v14,v181 row_newbcast:13 row_mask:0xf bank_mask:0xf
v_mfma_f32_16x16x32_bf16 v[188:191],a[228:231],v[24:27],v[188:191]
ds_read_b64_tr_b16 a[192:193],v2 offset:0
v_mul_f32_dpp v182,v14,v182 row_newbcast:14 row_mask:0xf bank_mask:0xf
v_mul_f32_dpp v183,v14,v183 row_newbcast:15 row_mask:0xf bank_mask:0xf
v_cvt_pk_bf16_f32 v244,v244,v245
v_mfma_f32_16x16x32_bf16 v[184:187],a[228:231],v[28:31],v[184:187]
ds_read_b64_tr_b16 a[194:195],v2 offset:1024
v_cvt_pk_bf16_f32 v245,v246,v247
v_cvt_pk_bf16_f32 v246,v180,v181
v_lshlrev_b32_sdwa v54,1,v54 dst_sel:DWORD dst_unused:UNUSED_PAD src0_sel:DWORD src1_sel:WORD_1
v_cvt_pk_bf16_f32 v247,v182,v183
v_mfma_f32_16x16x32_bf16 v[224:227],a[232:235],v[28:31],v[224:227]
ds_read_u16 v52,v52
v_mul_f32_dpp v252,v15,v252 row_newbcast:0 row_mask:0xf bank_mask:0xf
v_mul_f32_dpp v253,v15,v253 row_newbcast:1 row_mask:0xf bank_mask:0xf
v_lshlrev_b32_sdwa v53,1,v53 dst_sel:DWORD dst_unused:UNUSED_PAD src0_sel:DWORD src1_sel:WORD_1
v_mul_f32_dpp v254,v15,v254 row_newbcast:2 row_mask:0xf bank_mask:0xf
v_mfma_f32_16x16x32_bf16 v[228:231],a[232:235],v[24:27],v[228:231]
ds_read_u16_d16_hi v54,v54
v_mul_f32_dpp v255,v15,v255 row_newbcast:3 row_mask:0xf bank_mask:0xf
v_mul_f32_dpp v196,v15,v196 row_newbcast:4 row_mask:0xf bank_mask:0xf
v_mul_f32_dpp v197,v15,v197 row_newbcast:5 row_mask:0xf bank_mask:0xf
v_lshlrev_b32_sdwa v55,1,v55 dst_sel:DWORD dst_unused:UNUSED_PAD src0_sel:DWORD src1_sel:WORD_1
v_mfma_f32_16x16x32_bf16 v[204:207],a[236:239],v[24:27],v[204:207]
s_nop 0
ds_read_u16 v53,v53
v_mul_f32_dpp v198,v15,v198 row_newbcast:6 row_mask:0xf bank_mask:0xf
v_mul_f32_dpp v199,v15,v199 row_newbcast:7 row_mask:0xf bank_mask:0xf
v_cvt_pk_bf16_f32 v252,v252,v253
v_mfma_f32_16x16x32_bf16 v[200:203],a[236:239],v[28:31],v[200:203]
s_waitcnt lgkmcnt(8)
ds_read_u16_d16_hi v55,v55
v_cvt_pk_bf16_f32 v253,v254,v255
v_cvt_pk_bf16_f32 v254,v196,v197
v_cvt_pk_bf16_f32 v255,v198,v199
v_mfma_f32_16x16x32_bf16 v[24:27],a[204:207],a[0:3],v[20:23]
v_mul_f32_dpp v208,v14,v208 row_newbcast:0 row_mask:0xf bank_mask:0xf
s_add_u32 s11,s61,1671168
v_accvgpr_write_b32 a64,0
v_accvgpr_write_b32 a65,0
v_lshlrev_b32_sdwa v44,1,v44 dst_sel:DWORD dst_unused:UNUSED_PAD src0_sel:DWORD src1_sel:WORD_1
v_mfma_f32_16x16x32_bf16 v[24:27],a[208:211],a[16:19],v[24:27]
ds_read_u16 v44,v44
v_lshl_add_u64 v[36:37],v[36:37],0,v[38:39]
v_accvgpr_write_b32 a66,0
v_accvgpr_write_b32 a67,0
v_mul_f32_dpp v209,v14,v209 row_newbcast:1 row_mask:0xf bank_mask:0xf
v_mfma_f32_16x16x32_bf16 v[24:27],a[212:215],a[32:35],v[24:27]
v_lshl_add_u64 v[38:39],v[40:41],0,v[42:43]
v_mul_f32_dpp v210,v14,v210 row_newbcast:2 row_mask:0xf bank_mask:0xf
v_mul_f32_dpp v211,v14,v211 row_newbcast:3 row_mask:0xf bank_mask:0xf
v_lshlrev_b32_sdwa v46,1,v46 dst_sel:DWORD dst_unused:UNUSED_PAD src0_sel:DWORD src1_sel:WORD_1
v_mul_f32_dpp v168,v14,v168 row_newbcast:4 row_mask:0xf bank_mask:0xf
v_mfma_f32_16x16x32_bf16 v[28:31],a[200:203],a[32:35],v[16:19]
s_nop 0
buffer_store_dwordx4 v[248:251],v7,s[40:43],s10 offen offset:128 sc1
v_mul_f32_dpp v169,v14,v169 row_newbcast:5 row_mask:0xf bank_mask:0xf
v_mul_f32_dpp v170,v14,v170 row_newbcast:6 row_mask:0xf bank_mask:0xf
v_lshlrev_b32_sdwa v45,1,v45 dst_sel:DWORD dst_unused:UNUSED_PAD src0_sel:DWORD src1_sel:WORD_1
v_mul_f32_dpp v171,v14,v171 row_newbcast:7 row_mask:0xf bank_mask:0xf
v_mfma_f32_16x16x32_bf16 v[28:31],a[196:199],a[16:19],v[28:31]
s_waitcnt lgkmcnt(5)
ds_read_u16_d16_hi v46,v46
v_lshl_add_u64 v[40:41],v[56:57],0,v[58:59]
v_cvt_pk_bf16_f32 v208,v208,v209
v_cvt_pk_bf16_f32 v209,v210,v211
v_cvt_pk_bf16_f32 v210,v168,v169
v_mfma_f32_16x16x32_bf16 v[28:31],a[192:195],a[0:3],v[28:31]
ds_read_u16 v45,v45
v_cvt_pk_bf16_f32 v211,v170,v171
v_mul_f32_dpp v216,v14,v216 row_newbcast:8 row_mask:0xf bank_mask:0xf
v_lshlrev_b32_sdwa v47,1,v47 dst_sel:DWORD dst_unused:UNUSED_PAD src0_sel:DWORD src1_sel:WORD_1
v_accvgpr_write_b32 a72,v208
v_mfma_f32_16x16x32_bf16 v[56:59],a[192:195],a[4:7],v[16:19]
s_nop 0
ds_read_u16_d16_hi v47,v47
v_mul_f32_dpp v217,v14,v217 row_newbcast:9 row_mask:0xf bank_mask:0xf
v_mul_f32_dpp v218,v14,v218 row_newbcast:10 row_mask:0xf bank_mask:0xf
v_lshlrev_b32_sdwa v48,1,v48 dst_sel:DWORD dst_unused:UNUSED_PAD src0_sel:DWORD src1_sel:WORD_1
v_accvgpr_write_b32 a73,v209
v_mfma_f32_16x16x32_bf16 v[56:59],a[196:199],a[20:23],v[56:59]
s_waitcnt lgkmcnt(4)
ds_read_u16 v48,v48
v_lshl_add_u64 v[42:43],v[52:53],0,v[54:55]
v_mul_f32_dpp v219,v14,v219 row_newbcast:11 row_mask:0xf bank_mask:0xf
v_mul_f32_dpp v184,v14,v184 row_newbcast:12 row_mask:0xf bank_mask:0xf
v_accvgpr_write_b32 a74,v210
v_lshlrev_b32_sdwa v50,1,v50 dst_sel:DWORD dst_unused:UNUSED_PAD src0_sel:DWORD src1_sel:WORD_1
v_mfma_f32_16x16x32_bf16 v[56:59],a[200:203],a[36:39],v[56:59]
s_waitcnt vmcnt(9)
s_barrier
ds_read_u16_d16_hi v50,v50
v_mul_f32_dpp v185,v14,v185 row_newbcast:13 row_mask:0xf bank_mask:0xf
v_mul_f32_dpp v186,v14,v186 row_newbcast:14 row_mask:0xf bank_mask:0xf
v_accvgpr_write_b32 a75,v211
v_mfma_f32_16x16x32_bf16 v[52:55],a[212:215],a[36:39],v[20:23]
ds_read_b64_tr_b16 a[244:245],v3 offset:6160
v_mul_f32_dpp v187,v14,v187 row_newbcast:15 row_mask:0xf bank_mask:0xf
v_cvt_pk_bf16_f32 v216,v216,v217
v_lshlrev_b32_sdwa v49,1,v49 dst_sel:DWORD dst_unused:UNUSED_PAD src0_sel:DWORD src1_sel:WORD_1
v_cvt_pk_bf16_f32 v217,v218,v219
v_mfma_f32_16x16x32_bf16 v[52:55],a[208:211],a[20:23],v[52:55]
s_add_u32 m0,s58,0
ds_read_u16 v49,v49
v_cvt_pk_bf16_f32 v218,v184,v185
v_cvt_pk_bf16_f32 v219,v186,v187
v_lshlrev_b32_sdwa v51,1,v51 dst_sel:DWORD dst_unused:UNUSED_PAD src0_sel:DWORD src1_sel:WORD_1
v_mul_f32_dpp v224,v15,v224 row_newbcast:0 row_mask:0xf bank_mask:0xf
v_mfma_f32_16x16x32_bf16 v[52:55],a[204:207],a[4:7],v[52:55]
s_add_u32 s69,s69,s59
ds_read_u16_d16_hi v51,v51
v_accvgpr_write_b32 a76,v216
v_mul_f32_dpp v225,v15,v225 row_newbcast:1 row_mask:0xf bank_mask:0xf
v_mul_f32_dpp v226,v15,v226 row_newbcast:2 row_mask:0xf bank_mask:0xf
v_mfma_f32_16x16x32_bf16 v[136:139],a[240:243],v[36:39],v[136:139]
s_nop 0
buffer_load_dwordx4 v5,s[44:47],s69 offen offset:0 lds
v_accvgpr_write_b32 a77,v217
v_mul_f32_dpp v227,v15,v227 row_newbcast:3 row_mask:0xf bank_mask:0xf
v_lshlrev_b32_sdwa v32,1,v32 dst_sel:DWORD dst_unused:UNUSED_PAD src0_sel:DWORD src1_sel:WORD_1
v_mul_f32_dpp v200,v15,v200 row_newbcast:4 row_mask:0xf bank_mask:0xf
v_mfma_f32_16x16x32_bf16 v[140:143],a[240:243],v[40:43],v[140:143]
s_add_u32 s69,s69,s53
ds_read_u16 v32,v32
v_accvgpr_write_b32 a78,v218
v_mul_f32_dpp v201,v15,v201 row_newbcast:5 row_mask:0xf bank_mask:0xf
v_lshlrev_b32_sdwa v34,1,v34 dst_sel:DWORD dst_unused:UNUSED_PAD src0_sel:DWORD src1_sel:WORD_1
v_mul_f32_dpp v202,v15,v202 row_newbcast:6 row_mask:0xf bank_mask:0xf
v_mfma_f32_16x16x32_bf16 v[68:71],a[224:227],v[40:43],v[68:71]
buffer_load_dwordx4 v5,s[44:47],s69 offen offset:1024 lds
ds_read_u16_d16_hi v34,v34
v_accvgpr_write_b32 a79,v219
v_mul_f32_dpp v203,v15,v203 row_newbcast:7 row_mask:0xf bank_mask:0xf
v_cvt_pk_bf16_f32 v224,v224,v225
v_lshlrev_b32_sdwa v33,1,v33 dst_sel:DWORD dst_unused:UNUSED_PAD src0_sel:DWORD src1_sel:WORD_1
v_mfma_f32_16x16x32_bf16 v[64:67],a[224:227],v[36:39],v[64:67]
s_nop 0
buffer_store_dwordx4 v[236:239],v7,s[40:43],s10 offen offset:192 sc1
ds_read_u16 v33,v33
v_cvt_pk_bf16_f32 v225,v226,v227
v_cvt_pk_bf16_f32 v226,v200,v201
v_cvt_pk_bf16_f32 v227,v202,v203
v_mfma_f32_16x16x32_bf16 v[144:147],a[248:251],v[36:39],v[144:147]
s_add_u32 s69,s69,s53
ds_read_b64_tr_b16 a[246:247],v3 offset:7696
v_mul_f32_dpp v212,v14,v212 row_newbcast:0 row_mask:0xf bank_mask:0xf
v_accvgpr_write_b32 a80,v224
v_lshlrev_b32_sdwa v35,1,v35 dst_sel:DWORD dst_unused:UNUSED_PAD src0_sel:DWORD src1_sel:WORD_1
v_mul_f32_dpp v213,v14,v213 row_newbcast:1 row_mask:0xf bank_mask:0xf
v_mfma_f32_16x16x32_bf16 v[148:151],a[248:251],v[40:43],v[148:151]
buffer_load_dwordx4 v5,s[44:47],s69 offen offset:2048 lds
v_mul_f32_dpp v214,v14,v214 row_newbcast:2 row_mask:0xf bank_mask:0xf
v_accvgpr_write_b32 a81,v225
v_lshlrev_b32_sdwa v60,1,v60 dst_sel:DWORD dst_unused:UNUSED_PAD src0_sel:DWORD src1_sel:WORD_1
v_mul_f32_dpp v215,v14,v215 row_newbcast:3 row_mask:0xf bank_mask:0xf
v_mfma_f32_16x16x32_bf16 v[84:87],a[228:231],v[40:43],v[84:87]
ds_read_u16_d16_hi v35,v35
v_mul_f32_dpp v172,v14,v172 row_newbcast:4 row_mask:0xf bank_mask:0xf
v_accvgpr_write_b32 a82,v226
v_mul_f32_dpp v173,v14,v173 row_newbcast:5 row_mask:0xf bank_mask:0xf
v_mfma_f32_16x16x32_bf16 v[80:83],a[228:231],v[36:39],v[80:83]
ds_read_u16 v60,v60
v_mul_f32_dpp v174,v14,v174 row_newbcast:6 row_mask:0xf bank_mask:0xf
v_accvgpr_write_b32 a83,v227
v_lshlrev_b32_sdwa v62,1,v62 dst_sel:DWORD dst_unused:UNUSED_PAD src0_sel:DWORD src1_sel:WORD_1
v_mul_f32_dpp v175,v14,v175 row_newbcast:7 row_mask:0xf bank_mask:0xf
v_mfma_f32_16x16x32_bf16 v[152:155],a[232:235],v[36:39],v[152:155]
ds_read_u16_d16_hi v62,v62
v_cvt_pk_bf16_f32 v212,v212,v213
v_cvt_pk_bf16_f32 v213,v214,v215
v_lshlrev_b32_sdwa v61,1,v61 dst_sel:DWORD dst_unused:UNUSED_PAD src0_sel:DWORD src1_sel:WORD_1
v_cvt_pk_bf16_f32 v214,v172,v173
v_mfma_f32_16x16x32_bf16 v[156:159],a[232:235],v[40:43],v[156:159]
ds_read_u16 v61,v61
v_cvt_pk_bf16_f32 v215,v174,v175
v_mfma_f32_32x32x16_bf16 v[160:175],a[64:67],a[64:67],0
v_mul_f32_dpp v220,v14,v220 row_newbcast:8 row_mask:0xf bank_mask:0xf
v_lshlrev_b32_sdwa v63,1,v63 dst_sel:DWORD dst_unused:UNUSED_PAD src0_sel:DWORD src1_sel:WORD_1
v_accvgpr_write_b32 a84,v212
v_mul_f32_dpp v221,v14,v221 row_newbcast:9 row_mask:0xf bank_mask:0xf
v_mul_f32_dpp v222,v14,v222 row_newbcast:10 row_mask:0xf bank_mask:0xf
v_mfma_f32_16x16x32_bf16 v[100:103],a[236:239],v[40:43],v[100:103]
ds_read_u16_d16_hi v63,v63
v_accvgpr_write_b32 a85,v213
v_mfma_f32_16x16x32_bf16 v[96:99],a[236:239],v[36:39],v[96:99]
ds_read_b64_tr_b16 a[252:253],v3 offset:6168
v_mul_f32_dpp v223,v14,v223 row_newbcast:11 row_mask:0xf bank_mask:0xf
v_mul_f32_dpp v188,v14,v188 row_newbcast:12 row_mask:0xf bank_mask:0xf
ds_read_b64_tr_b16 a[254:255],v3 offset:7704
v_add_u32_sdwa v36,s60,v10 dst_sel:DWORD dst_unused:UNUSED_PAD src0_sel:DWORD src1_sel:WORD_1
v_subrev_u32 v8,88400,v36
v_min_u32_e64 v36,v36,v8
v_add_u32 v37,7072,v36
v_subrev_u32 v8,88400,v37
v_min_u32_e64 v37,v37,v8
s_waitcnt lgkmcnt(15)
v_lshl_add_u64 v[44:45],v[44:45],0,v[46:47]
s_waitcnt lgkmcnt(11)
v_lshl_add_u64 v[46:47],v[48:49],0,v[50:51]
s_waitcnt lgkmcnt(6)
v_lshl_add_u64 v[48:49],v[32:33],0,v[34:35]
s_waitcnt lgkmcnt(2)
v_lshl_add_u64 v[50:51],v[60:61],0,v[62:63]
v_mfma_f32_16x16x32_bf16 v[112:115],a[240:243],v[44:47],v[112:115]
v_xad_u32 v40,v11,0,v36
v_xad_u32 v41,v11,8,v37
v_xad_u32 v42,v11,16,v36
v_xad_u32 v43,v11,24,v37
v_mfma_f32_16x16x32_bf16 v[116:119],a[240:243],v[48:51],v[116:119]
ds_read_b64 a[48:49],v40 offset:0
v_accvgpr_write_b32 a86,v214
v_mul_f32_dpp v189,v14,v189 row_newbcast:13 row_mask:0xf bank_mask:0xf
v_mul_f32_dpp v190,v14,v190 row_newbcast:14 row_mask:0xf bank_mask:0xf
v_accvgpr_write_b32 a87,v215
v_mfma_f32_16x16x32_bf16 v[76:79],a[224:227],v[48:51],v[76:79]
buffer_store_dwordx4 v[244:247],v7,s[40:43],s10 offen offset:256 sc1
ds_read_b64 a[50:51],v40 offset:192
v_mul_f32_dpp v191,v14,v191 row_newbcast:15 row_mask:0xf bank_mask:0xf
v_cvt_pk_bf16_f32 v220,v220,v221
v_cvt_pk_bf16_f32 v221,v222,v223
v_mfma_f32_16x16x32_bf16 v[72:75],a[224:227],v[44:47],v[72:75]
ds_read_b64 a[52:53],v41 offset:0
v_cvt_pk_bf16_f32 v222,v188,v189
v_cvt_pk_bf16_f32 v223,v190,v191
v_mfma_f32_32x32x16_bf16 v[176:191],a[64:67],a[64:67],0
v_mfma_f32_16x16x32_bf16 v[120:123],a[248:251],v[44:47],v[120:123]
ds_read_b64 a[54:55],v41 offset:192
v_mul_f32_dpp v228,v15,v228 row_newbcast:0 row_mask:0xf bank_mask:0xf
v_accvgpr_write_b32 a88,v220
v_mul_f32_dpp v229,v15,v229 row_newbcast:1 row_mask:0xf bank_mask:0xf
v_mfma_f32_16x16x32_bf16 v[124:127],a[248:251],v[48:51],v[124:127]
ds_read_b64 a[56:57],v40 offset:8
v_mul_f32_dpp v230,v15,v230 row_newbcast:2 row_mask:0xf bank_mask:0xf
v_accvgpr_write_b32 a89,v221
v_mul_f32_dpp v231,v15,v231 row_newbcast:3 row_mask:0xf bank_mask:0xf
v_mfma_f32_16x16x32_bf16 v[92:95],a[228:231],v[48:51],v[92:95]
ds_read_b64 a[58:59],v40 offset:200
v_mul_f32_dpp v204,v15,v204 row_newbcast:4 row_mask:0xf bank_mask:0xf
v_accvgpr_write_b32 a90,v222
v_mul_f32_dpp v205,v15,v205 row_newbcast:5 row_mask:0xf bank_mask:0xf
v_mfma_f32_16x16x32_bf16 v[88:91],a[228:231],v[44:47],v[88:91]
ds_read_b64 a[60:61],v41 offset:8
v_mul_f32_dpp v206,v15,v206 row_newbcast:6 row_mask:0xf bank_mask:0xf
v_accvgpr_write_b32 a91,v223
v_mul_f32_dpp v207,v15,v207 row_newbcast:7 row_mask:0xf bank_mask:0xf
v_mfma_f32_16x16x32_bf16 v[128:131],a[232:235],v[44:47],v[128:131]
ds_read_b64 a[62:63],v41 offset:200
v_cvt_pk_bf16_f32 v228,v228,v229
v_cvt_pk_bf16_f32 v229,v230,v231
v_cvt_pk_bf16_f32 v230,v204,v205
v_mfma_f32_16x16x32_bf16 v[132:135],a[232:235],v[48:51],v[132:135]
v_cvt_pk_bf16_f32 v231,v206,v207
v_mfma_f32_32x32x16_bf16 v[192:207],a[64:67],a[64:67],0
v_accvgpr_write_b32 a92,v228
v_mfma_f32_16x16x32_bf16 v[108:111],a[236:239],v[48:51],v[108:111]
v_accvgpr_write_b32 a93,v229
v_accvgpr_write_b32 a94,v230
v_accvgpr_write_b32 a95,v231
v_mfma_f32_16x16x32_bf16 v[104:107],a[236:239],v[44:47],v[104:107]
v_mfma_f32_32x32x16_bf16 v[208:223],a[64:67],a[64:67],0
v_mov_b64 v[32:33],0
v_mov_b64 v[34:35],0
ds_read_b64 a[64:65],v42 offset:0
ds_read_b64 a[66:67],v42 offset:192
ds_read_b64 a[68:69],v43 offset:0
ds_read_b64 a[70:71],v43 offset:192
s_waitcnt lgkmcnt(8)
v_smfmac_f32_16x16x64_bf16 v[160:163],a[96:99],a[48:55],v10
s_waitcnt lgkmcnt(4)
ds_read_b64 a[48:49],v42 offset:8
ds_read_b64 a[50:51],v42 offset:200
ds_read_b64 a[52:53],v43 offset:8
ds_read_b64 a[54:55],v43 offset:200
v_smfmac_f32_16x16x64_bf16 v[164:167],a[100:103],a[56:63],v10
s_waitcnt lgkmcnt(4)
ds_read_b64 a[56:57],v40 offset:64
ds_read_b64 a[58:59],v40 offset:256
ds_read_b64 a[60:61],v41 offset:64
ds_read_b64 a[62:63],v41 offset:256
v_smfmac_f32_16x16x64_bf16 v[168:171],a[104:107],a[64:71],v10
s_waitcnt lgkmcnt(4)
buffer_store_dwordx4 v[252:255],v7,s[40:43],s10 offen offset:320 sc1
ds_read_b64 a[64:65],v40 offset:72
ds_read_b64 a[66:67],v40 offset:264
ds_read_b64 a[68:69],v41 offset:72
ds_read_b64 a[70:71],v41 offset:264
v_and_b32 v44,8,v11
v_pk_mul_f32 v[48:49],v[160:161],v[160:161]
v_pk_mul_f32 v[50:51],v[162:163],v[162:163]
v_smfmac_f32_16x16x64_bf16 v[172:175],a[108:111],a[48:55],v10
s_waitcnt lgkmcnt(4)
ds_read_b64 a[48:49],v42 offset:64
ds_read_b64 a[50:51],v42 offset:256
ds_read_b64 a[52:53],v43 offset:64
ds_read_b64 a[54:55],v43 offset:256
v_mul_u32_u24 v44,884,v44
s_add_u32 s64,s60,3536
v_pk_add_f32 v[60:61],v[160:161],v[164:165]
v_pk_fma_f32 v[48:49],v[164:165],v[164:165],v[48:49]
v_pk_add_f32 v[62:63],v[162:163],v[166:167]
v_pk_fma_f32 v[50:51],v[166:167],v[166:167],v[50:51]
v_smfmac_f32_16x16x64_bf16 v[176:179],a[128:131],a[56:63],v10
s_waitcnt lgkmcnt(4)
ds_read_b64 a[56:57],v42 offset:72
ds_read_b64 a[58:59],v42 offset:264
ds_read_b64 a[60:61],v43 offset:72
ds_read_b64 a[62:63],v43 offset:264
v_add_u32 v47,s64,v44
v_pk_add_f32 v[60:61],v[60:61],v[168:169]
v_pk_fma_f32 v[48:49],v[168:169],v[168:169],v[48:49]
v_pk_add_f32 v[62:63],v[62:63],v[170:171]
v_pk_fma_f32 v[50:51],v[170:171],v[170:171],v[50:51]
v_smfmac_f32_16x16x64_bf16 v[180:183],a[132:135],a[64:71],v10
s_waitcnt lgkmcnt(4)
ds_read_b64 a[64:65],v40 offset:128
ds_read_b64 a[66:67],v40 offset:320
ds_read_b64 a[68:69],v41 offset:128
ds_read_b64 a[70:71],v41 offset:320
v_subrev_u32 v8,88400,v47
v_pk_add_f32 v[60:61],v[60:61],v[172:173]
v_pk_fma_f32 v[48:49],v[172:173],v[172:173],v[48:49]
v_pk_add_f32 v[62:63],v[62:63],v[174:175]
v_pk_fma_f32 v[50:51],v[174:175],v[174:175],v[50:51]
v_smfmac_f32_16x16x64_bf16 v[184:187],a[136:139],a[48:55],v10
s_waitcnt lgkmcnt(4)
ds_read_b64 a[48:49],v40 offset:136
ds_read_b64 a[50:51],v40 offset:328
ds_read_b64 a[52:53],v41 offset:136
ds_read_b64 a[54:55],v41 offset:328
v_min_u32 v47,v47,v8
v_pk_add_f32 v[60:61],v[60:61],v[176:177]
v_pk_fma_f32 v[48:49],v[176:177],v[176:177],v[48:49]
v_pk_add_f32 v[62:63],v[62:63],v[178:179]
v_pk_fma_f32 v[50:51],v[178:179],v[178:179],v[50:51]
v_smfmac_f32_16x16x64_bf16 v[188:191],a[140:143],a[56:63],v10
s_waitcnt lgkmcnt(4)
ds_read_b64 a[56:57],v42 offset:128
ds_read_b64 a[58:59],v42 offset:320
ds_read_b64 a[60:61],v43 offset:128
ds_read_b64 a[62:63],v43 offset:320
v_xad_u32 v40,v11,32,v36
v_xad_u32 v41,v11,40,v37
v_pk_add_f32 v[60:61],v[60:61],v[180:181]
v_pk_fma_f32 v[48:49],v[180:181],v[180:181],v[48:49]
v_pk_add_f32 v[62:63],v[62:63],v[182:183]
v_pk_fma_f32 v[50:51],v[182:183],v[182:183],v[50:51]
v_smfmac_f32_16x16x64_bf16 v[192:195],a[160:163],a[64:71],v10
s_waitcnt lgkmcnt(4)
ds_read_b64 a[64:65],v42 offset:136
ds_read_b64 a[66:67],v42 offset:328
ds_read_b64 a[68:69],v43 offset:136
ds_read_b64 a[70:71],v43 offset:328
v_add_u32 v46,3536,v47
v_pk_add_f32 v[60:61],v[60:61],v[184:185]
v_pk_fma_f32 v[48:49],v[184:185],v[184:185],v[48:49]
v_pk_add_f32 v[62:63],v[62:63],v[186:187]
v_pk_fma_f32 v[50:51],v[186:187],v[186:187],v[50:51]
v_smfmac_f32_16x16x64_bf16 v[196:199],a[164:167],a[48:55],v10
s_waitcnt lgkmcnt(4)
buffer_store_dwordx4 a[72:75],v7,s[40:43],s11 offen offset:0 sc1
ds_read_b64 a[48:49],v40 offset:0
ds_read_b64 a[50:51],v40 offset:192
ds_read_b64 a[52:53],v41 offset:0
ds_read_b64 a[54:55],v41 offset:192
v_xad_u32 v42,v11,48,v36
v_xad_u32 v43,v11,56,v37
v_pk_add_f32 v[60:61],v[60:61],v[188:189]
v_pk_fma_f32 v[48:49],v[188:189],v[188:189],v[48:49]
v_pk_add_f32 v[62:63],v[62:63],v[190:191]
v_pk_fma_f32 v[50:51],v[190:191],v[190:191],v[50:51]
v_smfmac_f32_16x16x64_bf16 v[200:203],a[168:171],a[56:63],v10
s_waitcnt lgkmcnt(4)
ds_read_b64 a[56:57],v40 offset:8
ds_read_b64 a[58:59],v40 offset:200
ds_read_b64 a[60:61],v41 offset:8
ds_read_b64 a[62:63],v41 offset:200
v_subrev_u32 v8,88400,v46
v_pk_add_f32 v[60:61],v[60:61],v[192:193]
v_pk_fma_f32 v[48:49],v[192:193],v[192:193],v[48:49]
v_pk_add_f32 v[62:63],v[62:63],v[194:195]
v_pk_fma_f32 v[50:51],v[194:195],v[194:195],v[50:51]
v_smfmac_f32_16x16x64_bf16 v[204:207],a[172:175],a[64:71],v10
s_waitcnt lgkmcnt(4)
ds_read_b64 a[64:65],v40 offset:64
ds_read_b64 a[66:67],v40 offset:256
ds_read_b64 a[68:69],v41 offset:64
ds_read_b64 a[70:71],v41 offset:256
v_min_u32 v46,v46,v8
v_pk_add_f32 v[60:61],v[60:61],v[196:197]
v_pk_fma_f32 v[48:49],v[196:197],v[196:197],v[48:49]
v_pk_add_f32 v[62:63],v[62:63],v[198:199]
v_pk_fma_f32 v[50:51],v[198:199],v[198:199],v[50:51]
v_smfmac_f32_16x16x64_bf16 v[208:211],a[112:115],a[48:55],v10
s_waitcnt lgkmcnt(4)
TE10_%=:
s_cbranch_execz TP9685_%=
s_nop 0
ds_read_b64 a[48:49],v40 offset:72
ds_read_b64 a[50:51],v40 offset:264
ds_read_b64 a[52:53],v41 offset:72
ds_read_b64 a[54:55],v41 offset:264
v_xor_b32 v9,0x80008,v12
v_pk_add_f32 v[60:61],v[60:61],v[200:201]
v_pk_fma_f32 v[48:49],v[200:201],v[200:201],v[48:49]
v_pk_add_f32 v[62:63],v[62:63],v[202:203]
v_pk_fma_f32 v[50:51],v[202:203],v[202:203],v[50:51]
v_mfma_f32_32x32x16_bf16 v[224:239],v[32:35],v[32:35],0
v_smfmac_f32_16x16x64_bf16 v[212:215],a[116:119],a[56:63],v10
s_waitcnt lgkmcnt(4)
ds_read_b64 a[56:57],v40 offset:128
ds_read_b64 a[58:59],v40 offset:320
ds_read_b64 a[60:61],v41 offset:128
ds_read_b64 a[62:63],v41 offset:320
v_add_u32_sdwa v44,v12,v47 dst_sel:DWORD dst_unused:UNUSED_PAD src0_sel:WORD_0 src1_sel:DWORD
v_pk_add_f32 v[60:61],v[60:61],v[204:205]
v_pk_fma_f32 v[48:49],v[204:205],v[204:205],v[48:49]
v_pk_add_f32 v[62:63],v[62:63],v[206:207]
v_pk_fma_f32 v[50:51],v[206:207],v[206:207],v[50:51]
v_smfmac_f32_16x16x64_bf16 v[216:219],a[144:147],a[64:71],v10
s_waitcnt lgkmcnt(4)
ds_read_b64 a[64:65],v40 offset:136
ds_read_b64 a[66:67],v40 offset:328
ds_read_b64 a[68:69],v41 offset:136
ds_read_b64 a[70:71],v41 offset:328
v_add_u32_sdwa v45,v12,v47 dst_sel:DWORD dst_unused:UNUSED_PAD src0_sel:WORD_1 src1_sel:DWORD
v_pk_add_f32 v[60:61],v[60:61],v[208:209]
v_pk_fma_f32 v[48:49],v[208:209],v[208:209],v[48:49]
v_pk_add_f32 v[62:63],v[62:63],v[210:211]
v_pk_fma_f32 v[50:51],v[210:211],v[210:211],v[50:51]
v_smfmac_f32_16x16x64_bf16 v[220:223],a[148:151],a[48:55],v10
s_waitcnt lgkmcnt(4)
buffer_store_dwordx4 a[76:79],v7,s[40:43],s11 offen offset:64 sc1
ds_read_b64 a[48:49],v42 offset:0
ds_read_b64 a[50:51],v42 offset:192
ds_read_b64 a[52:53],v43 offset:0
ds_read_b64 a[54:55],v43 offset:192
v_add_u32_sdwa v47,v9,v46 dst_sel:DWORD dst_unused:UNUSED_PAD src0_sel:WORD_1 src1_sel:DWORD
v_pk_add_f32 v[60:61],v[60:61],v[212:213]
v_pk_fma_f32 v[48:49],v[212:213],v[212:213],v[48:49]
v_pk_add_f32 v[62:63],v[62:63],v[214:215]
v_pk_fma_f32 v[50:51],v[214:215],v[214:215],v[50:51]
v_smfmac_f32_16x16x64_bf16 v[224:227],a[176:179],a[56:63],v10
s_waitcnt lgkmcnt(4)
ds_read_b64 a[56:57],v42 offset:8
ds_read_b64 a[58:59],v42 offset:200
ds_read_b64 a[60:61],v43 offset:8
ds_read_b64 a[62:63],v43 offset:200
v_add_u32_sdwa v46,v9,v46 dst_sel:DWORD dst_unused:UNUSED_PAD src0_sel:WORD_0 src1_sel:DWORD
v_pk_add_f32 v[60:61],v[60:61],v[216:217]
v_pk_fma_f32 v[48:49],v[216:217],v[216:217],v[48:49]
v_pk_add_f32 v[62:63],v[62:63],v[218:219]
v_pk_fma_f32 v[50:51],v[218:219],v[218:219],v[50:51]
v_mfma_f32_32x32x16_bf16 v[240:255],v[32:35],v[32:35],0
v_smfmac_f32_16x16x64_bf16 v[228:231],a[180:183],a[64:71],v10
s_waitcnt lgkmcnt(4)
ds_read_b64 a[64:65],v42 offset:64
ds_read_b64 a[66:67],v42 offset:256
ds_read_b64 a[68:69],v43 offset:64
ds_read_b64 a[70:71],v43 offset:256
v_pk_add_f32 v[60:61],v[60:61],v[220:221]
v_pk_fma_f32 v[48:49],v[220:221],v[220:221],v[48:49]
v_pk_add_f32 v[62:63],v[62:63],v[222:223]
v_pk_fma_f32 v[50:51],v[222:223],v[222:223],v[50:51]
v_smfmac_f32_16x16x64_bf16 v[232:235],a[120:123],a[48:55],v10
s_waitcnt lgkmcnt(4)
ds_read_b64 a[48:49],v42 offset:72
ds_read_b64 a[50:51],v42 offset:264
ds_read_b64 a[52:53],v43 offset:72
ds_read_b64 a[54:55],v43 offset:264
v_pk_add_f32 v[60:61],v[60:61],v[224:225]
v_pk_fma_f32 v[48:49],v[224:225],v[224:225],v[48:49]
v_pk_add_f32 v[62:63],v[62:63],v[226:227]
v_pk_fma_f32 v[50:51],v[226:227],v[226:227],v[50:51]
v_smfmac_f32_16x16x64_bf16 v[236:239],a[124:127],a[56:63],v10
s_waitcnt lgkmcnt(4)
ds_read_b64 a[56:57],v42 offset:128
ds_read_b64 a[58:59],v42 offset:320
ds_read_b64 a[60:61],v43 offset:128
ds_read_b64 a[62:63],v43 offset:320
v_pk_add_f32 v[60:61],v[60:61],v[228:229]
v_pk_fma_f32 v[48:49],v[228:229],v[228:229],v[48:49]
v_pk_add_f32 v[62:63],v[62:63],v[230:231]
v_pk_fma_f32 v[50:51],v[230:231],v[230:231],v[50:51]
v_smfmac_f32_16x16x64_bf16 v[240:243],a[152:155],a[64:71],v10
s_waitcnt lgkmcnt(4)
ds_read_b64 a[64:65],v42 offset:136
ds_read_b64 a[66:67],v42 offset:328
ds_read_b64 a[68:69],v43 offset:136
ds_read_b64 a[70:71],v43 offset:328
v_pk_add_f32 v[60:61],v[60:61],v[232:233]
v_pk_fma_f32 v[48:49],v[232:233],v[232:233],v[48:49]
v_pk_add_f32 v[62:63],v[62:63],v[234:235]
v_pk_fma_f32 v[50:51],v[234:235],v[234:235],v[50:51]
v_smfmac_f32_16x16x64_bf16 v[244:247],a[156:159],a[48:55],v10
s_waitcnt lgkmcnt(4)
ds_read_b64 a[240:241],v44 offset:57344
ds_read_b64 a[242:243],v44 offset:57352
ds_read_b64 a[224:225],v45 offset:57344
ds_read_b64 a[226:227],v45 offset:57352
ds_read_b128 a[232:235],v13 offset:0
v_pk_add_f32 v[60:61],v[60:61],v[236:237]
v_pk_fma_f32 v[48:49],v[236:237],v[236:237],v[48:49]
v_pk_add_f32 v[62:63],v[62:63],v[238:239]
v_pk_fma_f32 v[50:51],v[238:239],v[238:239],v[50:51]
v_smfmac_f32_16x16x64_bf16 v[248:251],a[184:187],a[56:63],v10
s_waitcnt lgkmcnt(5)
buffer_store_dwordx4 a[80:83],v7,s[40:43],s11 offen offset:128 sc1
ds_read_b64 a[248:249],v46 offset:57344
ds_read_b64 a[250:251],v46 offset:57352
ds_read_b64 a[228:229],v47 offset:57344
ds_read_b64 a[230:231],v47 offset:57352
ds_read_b128 a[236:239],v13 offset:256
v_pk_add_f32 v[60:61],v[60:61],v[240:241]
v_pk_fma_f32 v[48:49],v[240:241],v[240:241],v[48:49]
v_pk_add_f32 v[62:63],v[62:63],v[242:243]
v_pk_fma_f32 v[50:51],v[242:243],v[242:243],v[50:51]
v_smfmac_f32_16x16x64_bf16 v[252:255],a[188:191],a[64:71],v10
ds_read_b128 v[40:43],v4 offset:3584
ds_read_b128 v[36:39],v4 offset:3616
v_pk_add_f32 v[60:61],v[60:61],v[244:245]
v_pk_fma_f32 v[48:49],v[244:245],v[244:245],v[48:49]
v_pk_add_f32 v[62:63],v[62:63],v[246:247]
v_pk_fma_f32 v[50:51],v[246:247],v[246:247],v[50:51]
v_pk_add_f32 v[60:61],v[60:61],v[248:249]
v_pk_fma_f32 v[48:49],v[248:249],v[248:249],v[48:49]
v_pk_add_f32 v[62:63],v[62:63],v[250:251]
v_pk_fma_f32 v[50:51],v[250:251],v[250:251],v[50:51]
v_pk_add_f32 v[60:61],v[60:61],v[252:253]
v_pk_fma_f32 v[48:49],v[252:253],v[252:253],v[48:49]
v_pk_add_f32 v[62:63],v[62:63],v[254:255]
v_pk_fma_f32 v[50:51],v[254:255],v[254:255],v[50:51]
s_nop 0
v_permlane32_swap_b32 v60,v62
v_permlane32_swap_b32 v61,v63
v_permlane32_swap_b32 v48,v50
v_permlane32_swap_b32_e64 v49,v51
v_pk_add_f32 v[60:61],v[60:61],v[62:63]
v_pk_add_f32 v[48:49],v[48:49],v[50:51]
s_nop 0
v_permlane16_swap_b32 v60,v61
v_permlane16_swap_b32 v48,v49
v_add_f32 v8,v60,v61
v_add_f32_e64 v9,v48,v49
v_mul_f32 v8,0x3c2aaaab,v8
v_mul_f32 v9,0x3c2aaaab,v9
v_cmp_ngt_f32 vcc,0x5d800000,v9
v_fma_f32 v9,-v8,v8,v9
v_max_f32 v9,0,v9
s_or_b64 s[90:91],s[90:91],vcc
v_add_f32 v9,0x3727c5ac,v9
v_rsq_f32 v9,v9
s_nop 0
v_mul_f32 v60,s96,v9
v_mul_f32 v61,s96,v9
v_mul_f32 v48,-v8,v60
v_mul_f32 v49,-v8,v60
v_permlane16_swap_b32 v60,v61
s_nop 0
v_permlane16_swap_b32 v48,v49
v_mov_b64 v[62:63],v[60:61]
v_mov_b64 v[50:51],v[48:49]
s_nop 0
v_permlane32_swap_b32 v60,v62
v_permlane32_swap_b32 v61,v63
v_permlane32_swap_b32 v48,v50
v_permlane32_swap_b32 v49,v51
s_or_b32 s64,s90,s91
s_lshl_b32 s65,s52,2
v_mov_b32 v8,s64
v_mov_b32 v9,s65
ds_write_b32 v9,v8 offset:11648
v_pk_fma_f32 v[160:161],v[160:161],v[60:61],v[48:49]
v_pk_fma_f32 v[162:163],v[162:163],v[62:63],v[50:51]
v_pk_fma_f32 v[164:165],v[164:165],v[60:61],v[48:49]
v_pk_fma_f32 v[166:167],v[166:167],v[62:63],v[50:51]
v_pk_fma_f32 v[168:169],v[168:169],v[60:61],v[48:49]
v_pk_fma_f32 v[170:171],v[170:171],v[62:63],v[50:51]
v_pk_fma_f32 v[172:173],v[172:173],v[60:61],v[48:49]
v_pk_fma_f32 v[174:175],v[174:175],v[62:63],v[50:51]
v_pk_fma_f32 v[208:209],v[208:209],v[60:61],v[48:49]
v_pk_fma_f32 v[210:211],v[210:211],v[62:63],v[50:51]
v_pk_fma_f32 v[212:213],v[212:213],v[60:61],v[48:49]
v_pk_fma_f32 v[214:215],v[214:215],v[62:63],v[50:51]
v_pk_fma_f32 v[232:233],v[232:233],v[60:61],v[48:49]
v_pk_fma_f32 v[234:235],v[234:235],v[62:63],v[50:51]
v_pk_fma_f32 v[236:237],v[236:237],v[60:61],v[48:49]
v_pk_fma_f32 v[238:239],v[238:239],v[62:63],v[50:51]
v_pk_fma_f32 v[176:177],v[176:177],v[60:61],v[48:49]
v_pk_fma_f32 v[178:179],v[178:179],v[62:63],v[50:51]
v_pk_fma_f32 v[180:181],v[180:181],v[60:61],v[48:49]
v_pk_fma_f32 v[182:183],v[182:183],v[62:63],v[50:51]
v_pk_fma_f32 v[184:185],v[184:185],v[60:61],v[48:49]
v_pk_fma_f32 v[186:187],v[186:187],v[62:63],v[50:51]
v_pk_fma_f32 v[188:189],v[188:189],v[60:61],v[48:49]
v_pk_fma_f32 v[190:191],v[190:191],v[62:63],v[50:51]
v_pk_fma_f32 v[216:217],v[216:217],v[60:61],v[48:49]
v_pk_fma_f32 v[218:219],v[218:219],v[62:63],v[50:51]
v_pk_fma_f32 v[220:221],v[220:221],v[60:61],v[48:49]
v_pk_fma_f32 v[222:223],v[222:223],v[62:63],v[50:51]
v_pk_fma_f32 v[240:241],v[240:241],v[60:61],v[48:49]
v_pk_fma_f32 v[242:243],v[242:243],v[62:63],v[50:51]
v_pk_fma_f32 v[244:245],v[244:245],v[60:61],v[48:49]
v_pk_fma_f32 v[246:247],v[246:247],v[62:63],v[50:51]
v_pk_fma_f32 v[192:193],v[192:193],v[60:61],v[48:49]
v_pk_fma_f32 v[194:195],v[194:195],v[62:63],v[50:51]
v_pk_fma_f32 v[196:197],v[196:197],v[60:61],v[48:49]
v_pk_fma_f32 v[198:199],v[198:199],v[62:63],v[50:51]
v_pk_fma_f32 v[200:201],v[200:201],v[60:61],v[48:49]
v_pk_fma_f32 v[202:203],v[202:203],v[62:63],v[50:51]
v_pk_fma_f32 v[204:205],v[204:205],v[60:61],v[48:49]
v_pk_fma_f32 v[206:207],v[206:207],v[62:63],v[50:51]
v_pk_fma_f32 v[224:225],v[224:225],v[60:61],v[48:49]
v_pk_fma_f32 v[226:227],v[226:227],v[62:63],v[50:51]
v_pk_fma_f32 v[228:229],v[228:229],v[60:61],v[48:49]
v_pk_fma_f32 v[230:231],v[230:231],v[62:63],v[50:51]
v_pk_fma_f32 v[248:249],v[248:249],v[60:61],v[48:49]
v_pk_fma_f32 v[250:251],v[250:251],v[62:63],v[50:51]
v_pk_fma_f32 v[252:253],v[252:253],v[60:61],v[48:49]
v_pk_fma_f32 v[254:255],v[254:255],v[62:63],v[50:51]
v_mfma_f32_16x16x32_bf16 v[32:35],a[204:207],a[8:11],v[20:23]
buffer_store_dwordx4 a[84:87],v7,s[40:43],s11 offen offset:192 sc1
v_cvt_pk_bf16_f32 v160,v160,v164
v_cvt_pk_bf16_f32 v168,v168,v172
v_cvt_pk_bf16_f32 v208,v208,v212
v_lshlrev_b32_sdwa v24,1,v24 dst_sel:DWORD dst_unused:UNUSED_PAD src0_sel:DWORD src1_sel:WORD_1
v_mfma_f32_16x16x32_bf16 v[32:35],a[208:211],a[24:27],v[32:35]
ds_read_u16 v24,v24
v_accvgpr_write_b32 a48,v160
v_accvgpr_write_b32 a49,v168
v_cvt_pk_bf16_f32 v232,v232,v236
v_accvgpr_write_b32 a50,v208
v_mfma_f32_16x16x32_bf16 v[32:35],a[212:215],a[40:43],v[32:35]
ds_read_b64_tr_b16 a[216:217],v1 offset:6160
v_cvt_pk_bf16_f32 v161,v161,v165
v_accvgpr_write_b32 a51,v232
v_cvt_pk_bf16_f32 v169,v169,v173
v_accvgpr_write_b32 a52,v161
v_mfma_f32_16x16x32_bf16 v[60:63],a[200:203],a[40:43],v[16:19]
ds_read_b64_tr_b16 a[218:219],v1 offset:7184
v_cvt_pk_bf16_f32 v209,v209,v213
v_accvgpr_write_b32 a53,v169
v_cvt_pk_bf16_f32 v233,v233,v237
v_lshlrev_b32_sdwa v26,1,v26 dst_sel:DWORD dst_unused:UNUSED_PAD src0_sel:DWORD src1_sel:WORD_1
v_accvgpr_write_b32 a54,v209
v_mfma_f32_16x16x32_bf16 v[60:63],a[196:199],a[24:27],v[60:63]
ds_read_u16_d16_hi v26,v26
v_cvt_pk_bf16_f32 v162,v162,v166
v_accvgpr_write_b32 a55,v233
v_cvt_pk_bf16_f32 v170,v170,v174
v_accvgpr_write_b32 a56,v162
v_mfma_f32_16x16x32_bf16 v[60:63],a[192:195],a[8:11],v[60:63]
buffer_store_dwordx4 a[88:91],v7,s[40:43],s11 offen offset:256 sc1
v_accvgpr_write_b32 a57,v170
v_cvt_pk_bf16_f32 v210,v210,v214
v_accvgpr_write_b32 a58,v210
v_lshlrev_b32_sdwa v25,1,v25 dst_sel:DWORD dst_unused:UNUSED_PAD src0_sel:DWORD src1_sel:WORD_1
v_cvt_pk_bf16_f32 v234,v234,v238
v_mfma_f32_16x16x32_bf16 v[48:51],a[192:195],a[12:15],v[16:19]
ds_read_u16 v25,v25
v_accvgpr_write_b32 a59,v234
v_cvt_pk_bf16_f32 v163,v163,v167
v_accvgpr_write_b32 a60,v163
v_mfma_f32_16x16x32_bf16 v[48:51],a[196:199],a[28:31],v[48:51]
ds_read_b64_tr_b16 a[220:221],v1 offset:8208
v_cvt_pk_bf16_f32 v171,v171,v175
v_accvgpr_write_b32 a61,v171
v_cvt_pk_bf16_f32 v211,v211,v215
v_accvgpr_write_b32 a62,v211
v_lshlrev_b32_sdwa v27,1,v27 dst_sel:DWORD dst_unused:UNUSED_PAD src0_sel:DWORD src1_sel:WORD_1
v_mfma_f32_16x16x32_bf16 v[48:51],a[200:203],a[44:47],v[48:51]
s_waitcnt lgkmcnt(6)
ds_read_u16_d16_hi v27,v27
v_cvt_pk_bf16_f32 v235,v235,v239
v_accvgpr_write_b32 a63,v235
v_cvt_pk_bf16_f32 v176,v176,v180
v_cvt_pk_bf16_f32 v184,v184,v188
v_mfma_f32_16x16x32_bf16 v[232:235],a[232:235],a[240:243],v[40:43]
s_nop 0
ds_read_b64_tr_b16 a[222:223],v1 offset:9232
v_cvt_pk_bf16_f32 v216,v216,v220
v_accvgpr_write_b32 a64,v176
v_accvgpr_write_b32 a65,v184
v_cvt_pk_bf16_f32 v240,v240,v244
v_lshlrev_b32_sdwa v28,1,v28 dst_sel:DWORD dst_unused:UNUSED_PAD src0_sel:DWORD src1_sel:WORD_1
v_mfma_f32_16x16x32_bf16 v[236:239],a[232:235],a[224:227],v[40:43]
ds_read_u16 v28,v28
v_accvgpr_write_b32 a66,v216
v_cvt_pk_bf16_f32 v177,v177,v181
v_accvgpr_write_b32 a67,v240
v_cvt_pk_bf16_f32 v185,v185,v189
v_mfma_f32_16x16x32_bf16 v[208:211],a[232:235],a[248:251],v[40:43]
v_accvgpr_write_b32 a68,v177
v_cvt_pk_bf16_f32 v217,v217,v221
v_accvgpr_write_b32 a69,v185
v_lshlrev_b32_sdwa v30,1,v30 dst_sel:DWORD dst_unused:UNUSED_PAD src0_sel:DWORD src1_sel:WORD_1
v_cvt_pk_bf16_f32 v241,v241,v245
v_mfma_f32_16x16x32_bf16 v[212:215],a[232:235],a[228:231],v[40:43]
ds_read_b128 a[232:235],v13 offset:512
ds_read_b128 v[40:43],v4 offset:3712
v_accvgpr_write_b32 a70,v217
v_cvt_pk_bf16_f32 v178,v178,v182
v_accvgpr_write_b32 a71,v241
v_mfma_f32_16x16x32_bf16 v[160:163],a[236:239],a[240:243],v[36:39]
ds_read_b64 a[240:241],v44 offset:57408
ds_read_b64 a[242:243],v44 offset:57416
v_cvt_pk_bf16_f32 v186,v186,v190
v_accvgpr_write_b32 a72,v178
v_accvgpr_write_b32 a73,v186
v_cvt_pk_bf16_f32 v218,v218,v222
v_lshlrev_b32_sdwa v29,1,v29 dst_sel:DWORD dst_unused:UNUSED_PAD src0_sel:DWORD src1_sel:WORD_1
v_mfma_f32_16x16x32_bf16 v[164:167],a[236:239],a[224:227],v[36:39]
ds_read_b64 a[224:225],v45 offset:57408
ds_read_b64 a[226:227],v45 offset:57416
v_accvgpr_write_b32 a74,v218
v_cvt_pk_bf16_f32 v242,v242,v246
v_accvgpr_write_b32 a75,v242
v_cvt_pk_bf16_f32 v179,v179,v183
v_mfma_f32_16x16x32_bf16 v[168:171],a[236:239],a[248:251],v[36:39]
ds_read_b64 a[248:249],v46 offset:57408
ds_read_b64 a[250:251],v46 offset:57416
v_accvgpr_write_b32 a76,v179
v_cvt_pk_bf16_f32 v187,v187,v191
v_accvgpr_write_b32 a77,v187
v_cvt_pk_bf16_f32 v219,v219,v223
v_lshlrev_b32_sdwa v31,1,v31 dst_sel:DWORD dst_unused:UNUSED_PAD src0_sel:DWORD src1_sel:WORD_1
v_mfma_f32_16x16x32_bf16 v[172:175],a[236:239],a[228:231],v[36:39]
s_waitcnt lgkmcnt(4)
buffer_store_dwordx4 a[92:95],v7,s[40:43],s11 offen offset:320 sc1
ds_read_b64 a[228:229],v47 offset:57408
v_accvgpr_write_b32 a78,v219
v_cvt_pk_bf16_f32 v243,v243,v247
v_accvgpr_write_b32 a79,v243
v_cvt_pk_bf16_f32 v192,v192,v196
v_mfma_f32_16x16x32_bf16 v[240:243],a[232:235],a[240:243],v[40:43]
s_waitcnt lgkmcnt(3)
ds_read_b64 a[230:231],v47 offset:57416
ds_read_b128 a[236:239],v13 offset:768
v_cvt_pk_bf16_f32 v200,v200,v204
v_cvt_pk_bf16_f32 v224,v224,v228
v_accvgpr_write_b32 a80,v192
v_accvgpr_write_b32 a81,v200
v_lshlrev_b32_sdwa v52,1,v52 dst_sel:DWORD dst_unused:UNUSED_PAD src0_sel:DWORD src1_sel:WORD_1
v_mfma_f32_16x16x32_bf16 v[244:247],a[232:235],a[224:227],v[40:43]
s_waitcnt lgkmcnt(3)
ds_read_b128 v[36:39],v4 offset:3744
ds_read_u16_d16_hi v30,v30
v_cvt_pk_bf16_f32 v248,v248,v252
v_accvgpr_write_b32 a82,v224
v_cvt_pk_bf16_f32 v193,v193,v197
v_mfma_f32_16x16x32_bf16 v[216:219],a[232:235],a[248:251],v[40:43]
s_waitcnt lgkmcnt(3)
ds_read_u16 v29,v29
v_accvgpr_write_b32 a83,v248
v_cvt_pk_bf16_f32 v201,v201,v205
v_accvgpr_write_b32 a84,v193
v_cvt_pk_bf16_f32 v225,v225,v229
v_lshlrev_b32_sdwa v54,1,v54 dst_sel:DWORD dst_unused:UNUSED_PAD src0_sel:DWORD src1_sel:WORD_1
v_mfma_f32_16x16x32_bf16 v[220:223],a[232:235],a[228:231],v[40:43]
s_waitcnt lgkmcnt(2)
ds_read_b128 a[232:235],v13 offset:1024
ds_read_b128 v[40:43],v4 offset:3840
v_accvgpr_write_b32 a85,v201
v_cvt_pk_bf16_f32 v249,v249,v253
v_accvgpr_write_b32 a86,v225
v_cvt_pk_bf16_f32 v194,v194,v198
v_mfma_f32_16x16x32_bf16 v[176:179],a[236:239],a[240:243],v[36:39]
s_nop 0
ds_read_b64 a[240:241],v44 offset:57472
ds_read_b64 a[242:243],v44 offset:57480
v_accvgpr_write_b32 a87,v249
v_cvt_pk_bf16_f32 v202,v202,v206
v_accvgpr_write_b32 a88,v194
v_accvgpr_write_b32 a89,v202
v_lshlrev_b32_sdwa v53,1,v53 dst_sel:DWORD dst_unused:UNUSED_PAD src0_sel:DWORD src1_sel:WORD_1
v_mfma_f32_16x16x32_bf16 v[180:183],a[236:239],a[224:227],v[36:39]
ds_read_b64 a[224:225],v45 offset:57472
ds_read_b64 a[226:227],v45 offset:57480
v_cvt_pk_bf16_f32 v226,v226,v230
v_accvgpr_write_b32 a90,v226
v_cvt_pk_bf16_f32 v250,v250,v254
v_accvgpr_write_b32 a91,v250
v_mfma_f32_16x16x32_bf16 v[184:187],a[236:239],a[248:251],v[36:39]
s_nop 0
ds_read_b64 a[248:249],v46 offset:57472
ds_read_b64 a[250:251],v46 offset:57480
v_cvt_pk_bf16_f32 v195,v195,v199
v_accvgpr_write_b32 a92,v195
v_cvt_pk_bf16_f32 v203,v203,v207
v_accvgpr_write_b32 a93,v203
v_mfma_f32_16x16x32_bf16 v[188:191],a[236:239],a[228:231],v[36:39]
s_waitcnt lgkmcnt(4)
ds_read_b64 a[228:229],v47 offset:57472
ds_read_b64 a[230:231],v47 offset:57480
v_cvt_pk_bf16_f32 v227,v227,v231
v_accvgpr_write_b32 a94,v227
v_cvt_pk_bf16_f32 v251,v251,v255
v_lshlrev_b32_sdwa v55,1,v55 dst_sel:DWORD dst_unused:UNUSED_PAD src0_sel:DWORD src1_sel:WORD_1
v_accvgpr_write_b32 a95,v251
v_mfma_f32_16x16x32_bf16 v[248:251],a[232:235],a[240:243],v[40:43]
s_waitcnt lgkmcnt(4)
ds_read_b128 a[236:239],v13 offset:1280
ds_read_b128 v[36:39],v4 offset:3872
v_mfma_f32_16x16x32_bf16 v[252:255],a[232:235],a[224:227],v[40:43]
s_waitcnt lgkmcnt(4)
ds_read_u16_d16_hi v31,v31
ds_read_u16 v52,v52
v_lshlrev_b32_sdwa v56,1,v56 dst_sel:DWORD dst_unused:UNUSED_PAD src0_sel:DWORD src1_sel:WORD_1
v_mfma_f32_16x16x32_bf16 v[224:227],a[232:235],a[248:251],v[40:43]
s_waitcnt lgkmcnt(4)
ds_read_u16_d16_hi v54,v54
v_lshlrev_b32_sdwa v58,1,v58 dst_sel:DWORD dst_unused:UNUSED_PAD src0_sel:DWORD src1_sel:WORD_1
v_mfma_f32_16x16x32_bf16 v[228:231],a[232:235],a[228:231],v[40:43]
s_waitcnt lgkmcnt(3)
ds_read_b64_tr_b16 a[232:233],v3 offset:6224
ds_read_b64_tr_b16 a[234:235],v3 offset:7760
v_mfma_f32_16x16x32_bf16 v[192:195],a[236:239],a[240:243],v[36:39]
ds_read_b64_tr_b16 a[240:241],v3 offset:6232
ds_read_b64_tr_b16 a[242:243],v3 offset:7768
v_mfma_f32_16x16x32_bf16 v[196:199],a[236:239],a[224:227],v[36:39]
ds_read_b64_tr_b16 a[224:225],v3 offset:6288
ds_read_b64_tr_b16 a[226:227],v3 offset:7824
v_lshlrev_b32_sdwa v57,1,v57 dst_sel:DWORD dst_unused:UNUSED_PAD src0_sel:DWORD src1_sel:WORD_1
v_mfma_f32_16x16x32_bf16 v[200:203],a[236:239],a[248:251],v[36:39]
ds_read_b64_tr_b16 a[248:249],v3 offset:6296
ds_read_b64_tr_b16 a[250:251],v3 offset:7832
v_mfma_f32_16x16x32_bf16 v[204:207],a[236:239],a[228:231],v[36:39]
ds_read_u16 v53,v53
ds_read_u16_d16_hi v55,v55
v_lshlrev_b32_sdwa v59,1,v59 dst_sel:DWORD dst_unused:UNUSED_PAD src0_sel:DWORD src1_sel:WORD_1
v_mfma_f32_16x16x32_bf16 v[44:47],a[212:215],a[44:47],v[20:23]
ds_read_u16 v56,v56
v_mfma_f32_16x16x32_bf16 v[44:47],a[208:211],a[28:31],v[44:47]
s_waitcnt vmcnt(18)
ds_read_u16_d16_hi v58,v58
v_mfma_f32_16x16x32_bf16 v[44:47],a[204:207],a[12:15],v[44:47]
s_barrier
ds_read_u16 v57,v57
ds_read_u16_d16_hi v59,v59
v_mfma_f32_16x16x32_bf16 v[40:43],a[204:207],a[48:51],v[20:23]
v_lshlrev_b32_sdwa v32,1,v32 dst_sel:DWORD dst_unused:UNUSED_PAD src0_sel:DWORD src1_sel:WORD_1
v_lshlrev_b32_sdwa v34,1,v34 dst_sel:DWORD dst_unused:UNUSED_PAD src0_sel:DWORD src1_sel:WORD_1
v_mfma_f32_16x16x32_bf16 v[40:43],a[208:211],a[64:67],v[40:43]
ds_read_u16 v32,v32
v_lshl_add_u64 v[24:25],v[24:25],0,v[26:27]
v_mfma_f32_16x16x32_bf16 v[40:43],a[212:215],a[80:83],v[40:43]
s_waitcnt lgkmcnt(11)
ds_read_u16_d16_hi v34,v34
v_lshl_add_u64 v[26:27],v[28:29],0,v[30:31]
v_mfma_f32_16x16x32_bf16 v[36:39],a[200:203],a[80:83],v[16:19]
v_lshlrev_b32_sdwa v33,1,v33 dst_sel:DWORD dst_unused:UNUSED_PAD src0_sel:DWORD src1_sel:WORD_1
v_lshlrev_b32_sdwa v35,1,v35 dst_sel:DWORD dst_unused:UNUSED_PAD src0_sel:DWORD src1_sel:WORD_1
v_mfma_f32_16x16x32_bf16 v[36:39],a[196:199],a[64:67],v[36:39]
s_waitcnt lgkmcnt(6)
ds_read_u16 v33,v33
v_lshl_add_u64 v[28:29],v[52:53],0,v[54:55]
v_mfma_f32_16x16x32_bf16 v[36:39],a[192:195],a[48:51],v[36:39]
ds_read_u16_d16_hi v35,v35
v_lshlrev_b32_sdwa v60,1,v60 dst_sel:DWORD dst_unused:UNUSED_PAD src0_sel:DWORD src1_sel:WORD_1
v_mfma_f32_16x16x32_bf16 v[52:55],a[192:195],a[52:55],v[16:19]
ds_read_u16 v60,v60
v_lshlrev_b32_sdwa v62,1,v62 dst_sel:DWORD dst_unused:UNUSED_PAD src0_sel:DWORD src1_sel:WORD_1
v_mfma_f32_16x16x32_bf16 v[52:55],a[196:199],a[68:71],v[52:55]
s_waitcnt lgkmcnt(5)
ds_read_u16_d16_hi v62,v62
v_lshl_add_u64 v[30:31],v[56:57],0,v[58:59]
v_mfma_f32_16x16x32_bf16 v[52:55],a[200:203],a[84:87],v[52:55]
s_nop 0
v_lshlrev_b32_sdwa v61,1,v61 dst_sel:DWORD dst_unused:UNUSED_PAD src0_sel:DWORD src1_sel:WORD_1
v_lshlrev_b32_sdwa v63,1,v63 dst_sel:DWORD dst_unused:UNUSED_PAD src0_sel:DWORD src1_sel:WORD_1
v_mfma_f32_16x16x32_bf16 v[56:59],a[212:215],a[84:87],v[20:23]
ds_read_u16 v61,v61
v_lshlrev_b32_sdwa v44,1,v44 dst_sel:DWORD dst_unused:UNUSED_PAD src0_sel:DWORD src1_sel:WORD_1
v_mfma_f32_16x16x32_bf16 v[56:59],a[208:211],a[68:71],v[56:59]
ds_read_u16_d16_hi v63,v63
v_lshlrev_b32_sdwa v46,1,v46 dst_sel:DWORD dst_unused:UNUSED_PAD src0_sel:DWORD src1_sel:WORD_1
v_mfma_f32_16x16x32_bf16 v[56:59],a[204:207],a[52:55],v[56:59]
ds_read_u16 v44,v44
v_lshlrev_b32_sdwa v45,1,v45 dst_sel:DWORD dst_unused:UNUSED_PAD src0_sel:DWORD src1_sel:WORD_1
v_mfma_f32_16x16x32_bf16 v[136:139],a[244:247],v[24:27],v[136:139]
ds_read_u16_d16_hi v46,v46
v_lshlrev_b32_sdwa v47,1,v47 dst_sel:DWORD dst_unused:UNUSED_PAD src0_sel:DWORD src1_sel:WORD_1
v_mfma_f32_16x16x32_bf16 v[140:143],a[244:247],v[28:31],v[140:143]
ds_read_u16 v45,v45
v_lshlrev_b32_sdwa v48,1,v48 dst_sel:DWORD dst_unused:UNUSED_PAD src0_sel:DWORD src1_sel:WORD_1
v_mfma_f32_16x16x32_bf16 v[68:71],a[252:255],v[28:31],v[68:71]
ds_read_u16_d16_hi v47,v47
v_lshlrev_b32_sdwa v50,1,v50 dst_sel:DWORD dst_unused:UNUSED_PAD src0_sel:DWORD src1_sel:WORD_1
v_mfma_f32_16x16x32_bf16 v[64:67],a[252:255],v[24:27],v[64:67]
ds_read_u16 v48,v48
v_lshlrev_b32_sdwa v49,1,v49 dst_sel:DWORD dst_unused:UNUSED_PAD src0_sel:DWORD src1_sel:WORD_1
v_mfma_f32_16x16x32_bf16 v[144:147],a[232:235],v[24:27],v[144:147]
ds_read_u16_d16_hi v50,v50
v_lshlrev_b32_sdwa v51,1,v51 dst_sel:DWORD dst_unused:UNUSED_PAD src0_sel:DWORD src1_sel:WORD_1
v_mfma_f32_16x16x32_bf16 v[148:151],a[232:235],v[28:31],v[148:151]
ds_read_u16 v49,v49
v_mfma_f32_16x16x32_bf16 v[84:87],a[240:243],v[28:31],v[84:87]
ds_read_u16_d16_hi v51,v51
v_mfma_f32_16x16x32_bf16 v[80:83],a[240:243],v[24:27],v[80:83]
v_mfma_f32_16x16x32_bf16 v[152:155],a[224:227],v[24:27],v[152:155]
v_mfma_f32_16x16x32_bf16 v[156:159],a[224:227],v[28:31],v[156:159]
v_mfma_f32_16x16x32_bf16 v[100:103],a[248:251],v[28:31],v[100:103]
v_mfma_f32_16x16x32_bf16 v[96:99],a[248:251],v[24:27],v[96:99]
v_mfma_f32_16x16x32_bf16 v[28:31],a[204:207],a[56:59],v[20:23]
v_lshlrev_b32_sdwa v40,1,v40 dst_sel:DWORD dst_unused:UNUSED_PAD src0_sel:DWORD src1_sel:WORD_1
v_fma_mixlo_f16 v8,v53,s99,0
v_mfma_f32_16x16x32_bf16 v[28:31],a[208:211],a[72:75],v[28:31]
s_waitcnt lgkmcnt(2)
ds_read_u16 v40,v40
v_lshl_add_u64 v[32:33],v[32:33],0,v[34:35]
v_mfma_f32_16x16x32_bf16 v[28:31],a[212:215],a[88:91],v[28:31]
v_lshl_add_u64 v[34:35],v[60:61],0,v[62:63]
v_lshlrev_b32_sdwa v42,1,v42 dst_sel:DWORD dst_unused:UNUSED_PAD src0_sel:DWORD src1_sel:WORD_1
v_mfma_f32_16x16x32_bf16 v[24:27],a[200:203],a[88:91],v[16:19]
ds_read_u16_d16_hi v42,v42
v_fma_mixhi_f16 v8,v55,s99,0
v_mfma_f32_16x16x32_bf16 v[24:27],a[196:199],a[72:75],v[24:27]
v_lshl_add_u64 v[60:61],v[44:45],0,v[46:47]
v_lshlrev_b32_sdwa v41,1,v41 dst_sel:DWORD dst_unused:UNUSED_PAD src0_sel:DWORD src1_sel:WORD_1
v_pk_fma_f16 v9,v8,v8,s54
v_mfma_f32_16x16x32_bf16 v[24:27],a[192:195],a[56:59],v[24:27]
ds_read_u16 v41,v41
v_lshlrev_b32_sdwa v43,1,v43 dst_sel:DWORD dst_unused:UNUSED_PAD src0_sel:DWORD src1_sel:WORD_1
v_mfma_f32_16x16x32_bf16 v[44:47],a[192:195],a[60:63],v[16:19]
ds_read_b128 v[16:19],v4 offset:768
ds_read_b64_tr_b16 a[192:193],v1 offset:10256
v_pk_mul_f16 v9,v8,v9
v_mfma_f32_16x16x32_bf16 v[44:47],a[196:199],a[76:79],v[44:47]
s_waitcnt lgkmcnt(5)
ds_read_b64_tr_b16 a[194:195],v1 offset:11280
ds_read_b64_tr_b16 a[196:197],v2 offset:10256
v_lshl_add_u64 v[62:63],v[48:49],0,v[50:51]
v_lshlrev_b32_sdwa v36,1,v36 dst_sel:DWORD dst_unused:UNUSED_PAD src0_sel:DWORD src1_sel:WORD_1
v_mfma_f32_16x16x32_bf16 v[44:47],a[200:203],a[92:95],v[44:47]
ds_read_b64_tr_b16 a[198:199],v2 offset:11280
ds_read_b64_tr_b16 a[200:201],v2 offset:8208
v_exp_f16 v8,v9
v_mfma_f32_16x16x32_bf16 v[48:51],a[212:215],a[92:95],v[20:23]
ds_read_b128 v[20:23],v4 offset:800
ds_read_b64_tr_b16 a[202:203],v2 offset:9232
v_lshlrev_b32_sdwa v38,1,v38 dst_sel:DWORD dst_unused:UNUSED_PAD src0_sel:DWORD src1_sel:WORD_1
v_mfma_f32_16x16x32_bf16 v[48:51],a[208:211],a[76:79],v[48:51]
TE11_%=:
s_cbranch_execz TP10210_%=
ds_read_b64_tr_b16 a[212:213],v2 offset:6160
ds_read_b64_tr_b16 a[214:215],v2 offset:7184
v_lshlrev_b32_sdwa v37,1,v37 dst_sel:DWORD dst_unused:UNUSED_PAD src0_sel:DWORD src1_sel:WORD_1
v_mfma_f32_16x16x32_bf16 v[48:51],a[204:207],a[60:63],v[48:51]
ds_read_u16_d16_hi v43,v43
v_exp_f16_sdwa v8,v9 dst_sel:WORD_1 dst_unused:UNUSED_PRESERVE src0_sel:WORD_1
v_mfma_f32_16x16x32_bf16 v[112:115],a[244:247],v[32:35],v[112:115]
ds_read_u16 v36,v36
v_lshlrev_b32_sdwa v39,1,v39 dst_sel:DWORD dst_unused:UNUSED_PAD src0_sel:DWORD src1_sel:WORD_1
v_mfma_f32_16x16x32_bf16 v[116:119],a[244:247],v[60:63],v[116:119]
ds_read_u16_d16_hi v38,v38
v_lshlrev_b32_sdwa v56,1,v56 dst_sel:DWORD dst_unused:UNUSED_PAD src0_sel:DWORD src1_sel:WORD_1
v_mfma_f32_16x16x32_bf16 v[76:79],a[252:255],v[60:63],v[76:79]
ds_read_u16 v37,v37
v_pk_fma_f16 v8,v8,s55,s55
v_mfma_f32_16x16x32_bf16 v[72:75],a[252:255],v[32:35],v[72:75]
v_rcp_f16_e64 v9,v8
v_lshlrev_b32_sdwa v58,1,v58 dst_sel:DWORD dst_unused:UNUSED_PAD src0_sel:DWORD src1_sel:WORD_1
v_mfma_f32_16x16x32_bf16 v[120:123],a[232:235],v[32:35],v[120:123]
ds_read_u16_d16_hi v39,v39
v_lshlrev_b32_sdwa v57,1,v57 dst_sel:DWORD dst_unused:UNUSED_PAD src0_sel:DWORD src1_sel:WORD_1
v_mfma_f32_16x16x32_bf16 v[124:127],a[232:235],v[60:63],v[124:127]
ds_read_u16 v56,v56
v_lshlrev_b32_sdwa v59,1,v59 dst_sel:DWORD dst_unused:UNUSED_PAD src0_sel:DWORD src1_sel:WORD_1
v_mfma_f32_16x16x32_bf16 v[92:95],a[240:243],v[60:63],v[92:95]
ds_read_u16_d16_hi v58,v58
v_lshlrev_b32_sdwa v52,1,v52 dst_sel:DWORD dst_unused:UNUSED_PAD src0_sel:DWORD src1_sel:WORD_1
v_mfma_f32_16x16x32_bf16 v[88:91],a[240:243],v[32:35],v[88:91]
v_rcp_f16_sdwa v9,v8 dst_sel:WORD_1 dst_unused:UNUSED_PRESERVE src0_sel:WORD_1
v_mfma_f32_16x16x32_bf16 v[128:131],a[224:227],v[32:35],v[128:131]
ds_read_u16 v57,v57
ds_read_u16_d16_hi v59,v59
v_mfma_f32_16x16x32_bf16 v[132:135],a[224:227],v[60:63],v[132:135]
ds_read_u16 v52,v52
v_lshlrev_b32_sdwa v54,1,v54 dst_sel:DWORD dst_unused:UNUSED_PAD src0_sel:DWORD src1_sel:WORD_1
v_mfma_f32_16x16x32_bf16 v[108:111],a[248:251],v[60:63],v[108:111]
ds_read_u16_d16_hi v54,v54
v_fma_mix_f32 v53,v53,v9,0 op_sel_hi:[0,1,0]
v_mfma_f32_16x16x32_bf16 v[104:107],a[248:251],v[32:35],v[104:107]
s_waitcnt lgkmcnt(7)
v_fma_mix_f32 v55,v55,v9,0 op_sel:[0,1,0] op_sel_hi:[0,1,0]
v_mfma_f32_16x16x32_bf16 v[60:63],a[216:219],a[0:3],v[16:19]
s_add_u32 m0,s62,0
v_lshlrev_b32_sdwa v28,1,v28 dst_sel:DWORD dst_unused:UNUSED_PAD src0_sel:DWORD src1_sel:WORD_1
v_lshlrev_b32_sdwa v30,1,v30 dst_sel:DWORD dst_unused:UNUSED_PAD src0_sel:DWORD src1_sel:WORD_1
v_mfma_f32_16x16x32_bf16 v[60:63],a[220:223],a[16:19],v[60:63]
s_mov_b32 s68,s87
ds_read_u16 v28,v28
v_lshl_add_u64 v[40:41],v[40:41],0,v[42:43]
v_mfma_f32_16x16x32_bf16 v[60:63],a[192:195],a[32:35],v[60:63]
s_waitcnt lgkmcnt(6)
buffer_load_dwordx4 v5,s[44:47],s68 offen offset:0 lds
v_lshl_add_u64 v[42:43],v[36:37],0,v[38:39]
v_mfma_f32_16x16x32_bf16 v[32:35],a[196:199],a[32:35],v[20:23]
s_add_u32 s68,s68,s53
ds_read_u16_d16_hi v30,v30
v_lshlrev_b32_sdwa v29,1,v29 dst_sel:DWORD dst_unused:UNUSED_PAD src0_sel:DWORD src1_sel:WORD_1
v_mfma_f32_16x16x32_bf16 v[32:35],a[200:203],a[16:19],v[32:35]
s_waitcnt lgkmcnt(4)
buffer_load_dwordx4 v5,s[44:47],s68 offen offset:1024 lds
v_lshl_add_u64 v[36:37],v[56:57],0,v[58:59]
v_mfma_f32_16x16x32_bf16 v[32:35],a[212:215],a[0:3],v[32:35]
ds_read_u16 v29,v29
v_lshlrev_b32_sdwa v31,1,v31 dst_sel:DWORD dst_unused:UNUSED_PAD src0_sel:DWORD src1_sel:WORD_1
v_mfma_f32_16x16x32_bf16 v[56:59],a[212:215],a[4:7],v[20:23]
s_waitcnt lgkmcnt(3)
ds_read_u16_d16_hi v31,v31
v_or_b32 v38,v52,v54
v_lshlrev_b32_sdwa v24,1,v24 dst_sel:DWORD dst_unused:UNUSED_PAD src0_sel:DWORD src1_sel:WORD_1
v_mfma_f32_16x16x32_bf16 v[56:59],a[200:203],a[20:23],v[56:59]
ds_read_u16 v24,v24
v_cvt_pk_bf16_f32 v39,v53,v55
v_lshlrev_b32_sdwa v26,1,v26 dst_sel:DWORD dst_unused:UNUSED_PAD src0_sel:DWORD src1_sel:WORD_1
v_mfma_f32_16x16x32_bf16 v[56:59],a[196:199],a[36:39],v[56:59]
ds_read_u16_d16_hi v26,v26
v_lshlrev_b32_sdwa v25,1,v25 dst_sel:DWORD dst_unused:UNUSED_PAD src0_sel:DWORD src1_sel:WORD_1
v_mfma_f32_16x16x32_bf16 v[52:55],a[192:195],a[36:39],v[16:19]
ds_read_u16 v25,v25
v_lshlrev_b32_sdwa v27,1,v27 dst_sel:DWORD dst_unused:UNUSED_PAD src0_sel:DWORD src1_sel:WORD_1
v_mfma_f32_16x16x32_bf16 v[52:55],a[220:223],a[20:23],v[52:55]
ds_read_u16_d16_hi v27,v27
v_lshlrev_b32_sdwa v48,1,v48 dst_sel:DWORD dst_unused:UNUSED_PAD src0_sel:DWORD src1_sel:WORD_1
v_mfma_f32_16x16x32_bf16 v[52:55],a[216:219],a[4:7],v[52:55]
ds_read_u16 v48,v48
v_lshlrev_b32_sdwa v50,1,v50 dst_sel:DWORD dst_unused:UNUSED_PAD src0_sel:DWORD src1_sel:WORD_1
v_mfma_f32_16x16x32_bf16 v[232:235],a[244:247],v[40:43],v[232:235]
ds_read_u16_d16_hi v50,v50
v_lshlrev_b32_sdwa v49,1,v49 dst_sel:DWORD dst_unused:UNUSED_PAD src0_sel:DWORD src1_sel:WORD_1
v_mfma_f32_16x16x32_bf16 v[236:239],a[244:247],v[36:39],v[236:239]
ds_read_u16 v49,v49
v_lshlrev_b32_sdwa v51,1,v51 dst_sel:DWORD dst_unused:UNUSED_PAD src0_sel:DWORD src1_sel:WORD_1
v_mfma_f32_16x16x32_bf16 v[164:167],a[252:255],v[36:39],v[164:167]
ds_read_u16_d16_hi v51,v51
ds_read_b64_tr_b16 a[228:229],v3 offset:12320
v_mfma_f32_16x16x32_bf16 v[160:163],a[252:255],v[40:43],v[160:163]
s_add_u32 s68,s68,s53
ds_read_b64_tr_b16 a[230:231],v3 offset:13856
v_lshlrev_b32_sdwa v44,1,v44 dst_sel:DWORD dst_unused:UNUSED_PAD src0_sel:DWORD src1_sel:WORD_1
v_mfma_f32_16x16x32_bf16 v[240:243],a[232:235],v[40:43],v[240:243]
buffer_load_dwordx4 v5,s[44:47],s68 offen offset:2048 lds
v_lshlrev_b32_sdwa v46,1,v46 dst_sel:DWORD dst_unused:UNUSED_PAD src0_sel:DWORD src1_sel:WORD_1
v_mfma_f32_16x16x32_bf16 v[244:247],a[232:235],v[36:39],v[244:247]
ds_read_u16 v44,v44
v_lshlrev_b32_sdwa v45,1,v45 dst_sel:DWORD dst_unused:UNUSED_PAD src0_sel:DWORD src1_sel:WORD_1
v_mfma_f32_16x16x32_bf16 v[180:183],a[240:243],v[36:39],v[180:183]
ds_read_u16_d16_hi v46,v46
v_lshlrev_b32_sdwa v47,1,v47 dst_sel:DWORD dst_unused:UNUSED_PAD src0_sel:DWORD src1_sel:WORD_1
v_mfma_f32_16x16x32_bf16 v[176:179],a[240:243],v[40:43],v[176:179]
ds_read_u16 v45,v45
v_mfma_f32_16x16x32_bf16 v[248:251],a[224:227],v[40:43],v[248:251]
ds_read_u16_d16_hi v47,v47
v_mfma_f32_16x16x32_bf16 v[252:255],a[224:227],v[36:39],v[252:255]
ds_read_b64_tr_b16 a[236:237],v3 offset:12328
v_mfma_f32_16x16x32_bf16 v[196:199],a[248:251],v[36:39],v[196:199]
ds_read_b64_tr_b16 a[238:239],v3 offset:13864
v_mfma_f32_16x16x32_bf16 v[192:195],a[248:251],v[40:43],v[192:195]
v_mfma_f32_16x16x32_bf16 v[36:39],a[216:219],a[8:11],v[16:19]
ds_read_b64_tr_b16 a[208:209],v1 offset:12320
v_lshlrev_b32_sdwa v60,1,v60 dst_sel:DWORD dst_unused:UNUSED_PAD src0_sel:DWORD src1_sel:WORD_1
v_mfma_f32_16x16x32_bf16 v[36:39],a[220:223],a[24:27],v[36:39]
s_waitcnt lgkmcnt(7)
ds_read_u16 v60,v60
v_lshl_add_u64 v[28:29],v[28:29],0,v[30:31]
v_fma_mixlo_f16 v8,v57,s99,0
v_mfma_f32_16x16x32_bf16 v[36:39],a[192:195],a[40:43],v[36:39]
ds_read_b64_tr_b16 a[210:211],v1 offset:13344
v_lshl_add_u64 v[30:31],v[24:25],0,v[26:27]
v_lshlrev_b32_sdwa v62,1,v62 dst_sel:DWORD dst_unused:UNUSED_PAD src0_sel:DWORD src1_sel:WORD_1
v_mfma_f32_16x16x32_bf16 v[40:43],a[196:199],a[40:43],v[20:23]
ds_read_u16_d16_hi v62,v62
v_fma_mixhi_f16 v8,v59,s99,0
v_mfma_f32_16x16x32_bf16 v[40:43],a[200:203],a[24:27],v[40:43]
ds_read_b64_tr_b16 a[204:205],v1 offset:14368
v_lshl_add_u64 v[24:25],v[48:49],0,v[50:51]
v_lshlrev_b32_sdwa v61,1,v61 dst_sel:DWORD dst_unused:UNUSED_PAD src0_sel:DWORD src1_sel:WORD_1
v_pk_fma_f16 v9,v8,v8,s54
v_mfma_f32_16x16x32_bf16 v[40:43],a[212:215],a[8:11],v[40:43]
ds_read_u16 v61,v61
v_lshlrev_b32_sdwa v63,1,v63 dst_sel:DWORD dst_unused:UNUSED_PAD src0_sel:DWORD src1_sel:WORD_1
v_mfma_f32_16x16x32_bf16 v[48:51],a[212:215],a[12:15],v[20:23]
ds_read_u16_d16_hi v63,v63
v_pk_mul_f16 v9,v8,v9
v_mfma_f32_16x16x32_bf16 v[48:51],a[200:203],a[28:31],v[48:51]
s_waitcnt lgkmcnt(7)
ds_read_b64_tr_b16 a[206:207],v1 offset:15392
v_lshl_add_u64 v[26:27],v[44:45],0,v[46:47]
v_lshlrev_b32_sdwa v32,1,v32 dst_sel:DWORD dst_unused:UNUSED_PAD src0_sel:DWORD src1_sel:WORD_1
v_mfma_f32_16x16x32_bf16 v[48:51],a[196:199],a[44:47],v[48:51]
v_exp_f16 v8,v9
v_mfma_f32_16x16x32_bf16 v[44:47],a[192:195],a[44:47],v[16:19]
ds_read_u16 v32,v32
v_lshlrev_b32_sdwa v34,1,v34 dst_sel:DWORD dst_unused:UNUSED_PAD src0_sel:DWORD src1_sel:WORD_1
v_mfma_f32_16x16x32_bf16 v[44:47],a[220:223],a[28:31],v[44:47]
ds_read_u16_d16_hi v34,v34
v_lshlrev_b32_sdwa v33,1,v33 dst_sel:DWORD dst_unused:UNUSED_PAD src0_sel:DWORD src1_sel:WORD_1
v_mfma_f32_16x16x32_bf16 v[44:47],a[216:219],a[12:15],v[44:47]
v_exp_f16_sdwa v8,v9 dst_sel:WORD_1 dst_unused:UNUSED_PRESERVE src0_sel:WORD_1
v_mfma_f32_16x16x32_bf16 v[208:211],a[244:247],v[28:31],v[208:211]
ds_read_u16 v33,v33
v_lshlrev_b32_sdwa v35,1,v35 dst_sel:DWORD dst_unused:UNUSED_PAD src0_sel:DWORD src1_sel:WORD_1
v_mfma_f32_16x16x32_bf16 v[212:215],a[244:247],v[24:27],v[212:215]
ds_read_u16_d16_hi v35,v35
v_lshlrev_b32_sdwa v52,1,v52 dst_sel:DWORD dst_unused:UNUSED_PAD src0_sel:DWORD src1_sel:WORD_1
v_mfma_f32_16x16x32_bf16 v[172:175],a[252:255],v[24:27],v[172:175]
ds_read_u16 v52,v52
v_pk_fma_f16 v8,v8,s55,s55
v_mfma_f32_16x16x32_bf16 v[168:171],a[252:255],v[28:31],v[168:171]
ds_read_b64_tr_b16 a[244:245],v3 offset:12384
v_rcp_f16 v9,v8
v_lshlrev_b32_sdwa v54,1,v54 dst_sel:DWORD dst_unused:UNUSED_PAD src0_sel:DWORD src1_sel:WORD_1
v_mfma_f32_16x16x32_bf16 v[216:219],a[232:235],v[28:31],v[216:219]
ds_read_u16_d16_hi v54,v54
v_lshlrev_b32_sdwa v53,1,v53 dst_sel:DWORD dst_unused:UNUSED_PAD src0_sel:DWORD src1_sel:WORD_1
v_mfma_f32_16x16x32_bf16 v[220:223],a[232:235],v[24:27],v[220:223]
ds_read_u16 v53,v53
ds_read_b64_tr_b16 a[246:247],v3 offset:13920
v_mfma_f32_16x16x32_bf16 v[188:191],a[240:243],v[24:27],v[188:191]
ds_read_b64_tr_b16 a[252:253],v3 offset:12392
v_lshlrev_b32_sdwa v55,1,v55 dst_sel:DWORD dst_unused:UNUSED_PAD src0_sel:DWORD src1_sel:WORD_1
v_mfma_f32_16x16x32_bf16 v[184:187],a[240:243],v[28:31],v[184:187]
ds_read_u16_d16_hi v55,v55
v_rcp_f16_sdwa v9,v8 dst_sel:WORD_1 dst_unused:UNUSED_PRESERVE src0_sel:WORD_1
v_mfma_f32_16x16x32_bf16 v[224:227],a[224:227],v[28:31],v[224:227]
ds_read_b64_tr_b16 a[254:255],v3 offset:13928
ds_read_b64_tr_b16 a[232:233],v3 offset:12448
v_lshlrev_b32_sdwa v56,1,v56 dst_sel:DWORD dst_unused:UNUSED_PAD src0_sel:DWORD src1_sel:WORD_1
v_mfma_f32_16x16x32_bf16 v[228:231],a[224:227],v[24:27],v[228:231]
ds_read_u16 v56,v56
v_lshlrev_b32_sdwa v58,1,v58 dst_sel:DWORD dst_unused:UNUSED_PAD src0_sel:DWORD src1_sel:WORD_1
v_mfma_f32_16x16x32_bf16 v[204:207],a[248:251],v[24:27],v[204:207]
ds_read_u16_d16_hi v58,v58
v_fma_mix_f32 v57,v57,v9,0 op_sel_hi:[0,1,0]
v_mfma_f32_16x16x32_bf16 v[200:203],a[248:251],v[28:31],v[200:203]
ds_read_b64_tr_b16 a[234:235],v3 offset:13984
v_fma_mix_f32 v59,v59,v9,0 op_sel:[0,1,0] op_sel_hi:[0,1,0]
v_mfma_f32_16x16x32_bf16 v[24:27],a[216:219],a[48:51],v[16:19]
s_add_u32 m0,s62,6160
ds_read_b64_tr_b16 a[240:241],v3 offset:12456
v_lshlrev_b32_sdwa v36,1,v36 dst_sel:DWORD dst_unused:UNUSED_PAD src0_sel:DWORD src1_sel:WORD_1
v_mfma_f32_16x16x32_bf16 v[24:27],a[220:223],a[64:67],v[24:27]
s_waitcnt lgkmcnt(10)
ds_read_b64_tr_b16 a[242:243],v3 offset:13992
v_lshl_add_u64 v[60:61],v[60:61],0,v[62:63]
v_mfma_f32_16x16x32_bf16 v[24:27],a[192:195],a[80:83],v[24:27]
s_add_u32 s68,s68,s59
ds_read_u16 v36,v36
v_lshl_add_u64 v[62:63],v[32:33],0,v[34:35]
v_lshlrev_b32_sdwa v38,1,v38 dst_sel:DWORD dst_unused:UNUSED_PAD src0_sel:DWORD src1_sel:WORD_1
v_mfma_f32_16x16x32_bf16 v[28:31],a[196:199],a[80:83],v[20:23]
buffer_load_dwordx4 v5,s[44:47],s68 offen offset:0 lds
v_lshlrev_b32_sdwa v37,1,v37 dst_sel:DWORD dst_unused:UNUSED_PAD src0_sel:DWORD src1_sel:WORD_1
v_mfma_f32_16x16x32_bf16 v[28:31],a[200:203],a[64:67],v[28:31]
s_waitcnt lgkmcnt(8)
ds_read_u16_d16_hi v38,v38
v_lshl_add_u64 v[32:33],v[52:53],0,v[54:55]
v_mfma_f32_16x16x32_bf16 v[28:31],a[212:215],a[48:51],v[28:31]
s_add_u32 s68,s68,s53
ds_read_u16 v37,v37
v_lshlrev_b32_sdwa v39,1,v39 dst_sel:DWORD dst_unused:UNUSED_PAD src0_sel:DWORD src1_sel:WORD_1
v_mfma_f32_16x16x32_bf16 v[52:55],a[212:215],a[52:55],v[20:23]
s_waitcnt lgkmcnt(6)
buffer_load_dwordx4 v5,s[44:47],s68 offen offset:1024 lds
v_or_b32 v34,v56,v58
v_lshlrev_b32_sdwa v40,1,v40 dst_sel:DWORD dst_unused:UNUSED_PAD src0_sel:DWORD src1_sel:WORD_1
v_mfma_f32_16x16x32_bf16 v[52:55],a[200:203],a[68:71],v[52:55]
s_add_u32 s68,s68,s53
ds_read_u16_d16_hi v39,v39
v_cvt_pk_bf16_f32 v35,v57,v59
v_lshlrev_b32_sdwa v42,1,v42 dst_sel:DWORD dst_unused:UNUSED_PAD src0_sel:DWORD src1_sel:WORD_1
v_mfma_f32_16x16x32_bf16 v[52:55],a[196:199],a[84:87],v[52:55]
buffer_load_dwordx4 v5,s[44:47],s68 offen offset:2048 lds
v_lshlrev_b32_sdwa v41,1,v41 dst_sel:DWORD dst_unused:UNUSED_PAD src0_sel:DWORD src1_sel:WORD_1
v_mfma_f32_16x16x32_bf16 v[56:59],a[192:195],a[84:87],v[16:19]
ds_read_u16 v40,v40
v_lshlrev_b32_sdwa v43,1,v43 dst_sel:DWORD dst_unused:UNUSED_PAD src0_sel:DWORD src1_sel:WORD_1
v_mfma_f32_16x16x32_bf16 v[56:59],a[220:223],a[68:71],v[56:59]
ds_read_u16_d16_hi v42,v42
v_lshlrev_b32_sdwa v44,1,v44 dst_sel:DWORD dst_unused:UNUSED_PAD src0_sel:DWORD src1_sel:WORD_1
v_mfma_f32_16x16x32_bf16 v[56:59],a[216:219],a[52:55],v[56:59]
ds_read_u16 v41,v41
v_lshlrev_b32_sdwa v46,1,v46 dst_sel:DWORD dst_unused:UNUSED_PAD src0_sel:DWORD src1_sel:WORD_1
v_mfma_f32_16x16x32_bf16 v[136:139],a[228:231],v[60:63],v[136:139]
ds_read_u16_d16_hi v43,v43
v_lshlrev_b32_sdwa v45,1,v45 dst_sel:DWORD dst_unused:UNUSED_PAD src0_sel:DWORD src1_sel:WORD_1
v_mfma_f32_16x16x32_bf16 v[140:143],a[228:231],v[32:35],v[140:143]
ds_read_u16 v44,v44
v_lshlrev_b32_sdwa v47,1,v47 dst_sel:DWORD dst_unused:UNUSED_PAD src0_sel:DWORD src1_sel:WORD_1
v_mfma_f32_16x16x32_bf16 v[68:71],a[236:239],v[32:35],v[68:71]
ds_read_u16_d16_hi v46,v46
v_lshlrev_b32_sdwa v48,1,v48 dst_sel:DWORD dst_unused:UNUSED_PAD src0_sel:DWORD src1_sel:WORD_1
v_mfma_f32_16x16x32_bf16 v[64:67],a[236:239],v[60:63],v[64:67]
ds_read_u16 v45,v45
v_lshlrev_b32_sdwa v50,1,v50 dst_sel:DWORD dst_unused:UNUSED_PAD src0_sel:DWORD src1_sel:WORD_1
v_mfma_f32_16x16x32_bf16 v[144:147],a[244:247],v[60:63],v[144:147]
s_add_u32 m0,s62,12320
ds_read_u16_d16_hi v47,v47
v_lshlrev_b32_sdwa v49,1,v49 dst_sel:DWORD dst_unused:UNUSED_PAD src0_sel:DWORD src1_sel:WORD_1
v_mfma_f32_16x16x32_bf16 v[148:151],a[244:247],v[32:35],v[148:151]
s_add_u32 s68,s68,s59
ds_read_u16 v48,v48
v_lshlrev_b32_sdwa v51,1,v51 dst_sel:DWORD dst_unused:UNUSED_PAD src0_sel:DWORD src1_sel:WORD_1
v_mfma_f32_16x16x32_bf16 v[84:87],a[252:255],v[32:35],v[84:87]
buffer_load_dwordx4 v5,s[44:47],s68 offen offset:0 lds
v_mfma_f32_16x16x32_bf16 v[80:83],a[252:255],v[60:63],v[80:83]
s_waitcnt lgkmcnt(7)
ds_read_u16_d16_hi v50,v50
v_mfma_f32_16x16x32_bf16 v[152:155],a[232:235],v[60:63],v[152:155]
ds_read_u16 v49,v49
v_mfma_f32_16x16x32_bf16 v[156:159],a[232:235],v[32:35],v[156:159]
ds_read_u16_d16_hi v51,v51
v_mfma_f32_16x16x32_bf16 v[100:103],a[240:243],v[32:35],v[100:103]
v_mfma_f32_16x16x32_bf16 v[96:99],a[240:243],v[60:63],v[96:99]
v_mfma_f32_16x16x32_bf16 v[32:35],a[216:219],a[56:59],v[16:19]
v_lshlrev_b32_sdwa v24,1,v24 dst_sel:DWORD dst_unused:UNUSED_PAD src0_sel:DWORD src1_sel:WORD_1
v_fma_mixlo_f16 v8,v53,s99,0
v_mfma_f32_16x16x32_bf16 v[32:35],a[220:223],a[72:75],v[32:35]
ds_read_u16 v24,v24
v_lshl_add_u64 v[36:37],v[36:37],0,v[38:39]
v_mfma_f32_16x16x32_bf16 v[32:35],a[192:195],a[88:91],v[32:35]
s_waitcnt lgkmcnt(4)
v_lshl_add_u64 v[38:39],v[40:41],0,v[42:43]
v_lshlrev_b32_sdwa v26,1,v26 dst_sel:DWORD dst_unused:UNUSED_PAD src0_sel:DWORD src1_sel:WORD_1
v_mfma_f32_16x16x32_bf16 v[60:63],a[196:199],a[88:91],v[20:23]
ds_read_u16_d16_hi v26,v26
v_fma_mixhi_f16 v8,v55,s99,0
v_mfma_f32_16x16x32_bf16 v[60:63],a[200:203],a[72:75],v[60:63]
v_lshl_add_u64 v[40:41],v[44:45],0,v[46:47]
v_lshlrev_b32_sdwa v25,1,v25 dst_sel:DWORD dst_unused:UNUSED_PAD src0_sel:DWORD src1_sel:WORD_1
v_pk_fma_f16 v9,v8,v8,s54
v_mfma_f32_16x16x32_bf16 v[60:63],a[212:215],a[56:59],v[60:63]
ds_read_u16 v25,v25
v_lshlrev_b32_sdwa v27,1,v27 dst_sel:DWORD dst_unused:UNUSED_PAD src0_sel:DWORD src1_sel:WORD_1
v_mfma_f32_16x16x32_bf16 v[44:47],a[212:215],a[60:63],v[20:23]
ds_read_b128 v[20:23],v4 offset:896
v_pk_mul_f16 v9,v8,v9
v_mfma_f32_16x16x32_bf16 v[44:47],a[200:203],a[76:79],v[44:47]
s_waitcnt lgkmcnt(4)
ds_read_u16_d16_hi v27,v27
ds_read_b64_tr_b16 a[212:213],v1 offset:16416
v_lshl_add_u64 v[42:43],v[48:49],0,v[50:51]
v_lshlrev_b32_sdwa v28,1,v28 dst_sel:DWORD dst_unused:UNUSED_PAD src0_sel:DWORD src1_sel:WORD_1
v_mfma_f32_16x16x32_bf16 v[44:47],a[196:199],a[92:95],v[44:47]
ds_read_b64_tr_b16 a[214:215],v1 offset:17440
v_exp_f16 v8,v9
v_mfma_f32_16x16x32_bf16 v[48:51],a[192:195],a[92:95],v[16:19]
ds_read_b128 v[16:19],v4 offset:928
ds_read_u16 v28,v28
v_lshlrev_b32_sdwa v30,1,v30 dst_sel:DWORD dst_unused:UNUSED_PAD src0_sel:DWORD src1_sel:WORD_1
v_mfma_f32_16x16x32_bf16 v[48:51],a[220:223],a[76:79],v[48:51]
ds_read_u16_d16_hi v30,v30
ds_read_b64_tr_b16 a[200:201],v2 offset:16416
v_lshlrev_b32_sdwa v29,1,v29 dst_sel:DWORD dst_unused:UNUSED_PAD src0_sel:DWORD src1_sel:WORD_1
v_mfma_f32_16x16x32_bf16 v[48:51],a[216:219],a[60:63],v[48:51]
ds_read_u16 v29,v29
v_exp_f16_sdwa v8,v9 dst_sel:WORD_1 dst_unused:UNUSED_PRESERVE src0_sel:WORD_1
v_mfma_f32_16x16x32_bf16 v[112:115],a[228:231],v[36:39],v[112:115]
ds_read_b64_tr_b16 a[202:203],v2 offset:17440
ds_read_b64_tr_b16 a[196:197],v2 offset:14368
v_lshlrev_b32_sdwa v31,1,v31 dst_sel:DWORD dst_unused:UNUSED_PAD src0_sel:DWORD src1_sel:WORD_1
v_mfma_f32_16x16x32_bf16 v[116:119],a[228:231],v[40:43],v[116:119]
ds_read_u16_d16_hi v31,v31
v_lshlrev_b32_sdwa v56,1,v56 dst_sel:DWORD dst_unused:UNUSED_PAD src0_sel:DWORD src1_sel:WORD_1
v_mfma_f32_16x16x32_bf16 v[76:79],a[236:239],v[40:43],v[76:79]
ds_read_b64_tr_b16 a[198:199],v2 offset:15392
v_pk_fma_f16 v8,v8,s55,s55
v_mfma_f32_16x16x32_bf16 v[72:75],a[236:239],v[36:39],v[72:75]
v_rcp_f16 v9,v8
v_lshlrev_b32_sdwa v58,1,v58 dst_sel:DWORD dst_unused:UNUSED_PAD src0_sel:DWORD src1_sel:WORD_1
v_mfma_f32_16x16x32_bf16 v[120:123],a[244:247],v[36:39],v[120:123]
ds_read_u16 v56,v56
v_lshlrev_b32_sdwa v57,1,v57 dst_sel:DWORD dst_unused:UNUSED_PAD src0_sel:DWORD src1_sel:WORD_1
v_mfma_f32_16x16x32_bf16 v[124:127],a[244:247],v[40:43],v[124:127]
ds_read_u16_d16_hi v58,v58
v_lshlrev_b32_sdwa v59,1,v59 dst_sel:DWORD dst_unused:UNUSED_PAD src0_sel:DWORD src1_sel:WORD_1
v_mfma_f32_16x16x32_bf16 v[92:95],a[252:255],v[40:43],v[92:95]
ds_read_u16 v57,v57
v_lshlrev_b32_sdwa v52,1,v52 dst_sel:DWORD dst_unused:UNUSED_PAD src0_sel:DWORD src1_sel:WORD_1
v_mfma_f32_16x16x32_bf16 v[88:91],a[252:255],v[36:39],v[88:91]
ds_read_u16_d16_hi v59,v59
v_rcp_f16_sdwa v9,v8 dst_sel:WORD_1 dst_unused:UNUSED_PRESERVE src0_sel:WORD_1
v_mfma_f32_16x16x32_bf16 v[128:131],a[232:235],v[36:39],v[128:131]
ds_read_b64_tr_b16 a[192:193],v2 offset:12320
v_lshlrev_b32_sdwa v54,1,v54 dst_sel:DWORD dst_unused:UNUSED_PAD src0_sel:DWORD src1_sel:WORD_1
v_mfma_f32_16x16x32_bf16 v[132:135],a[232:235],v[40:43],v[132:135]
ds_read_b64_tr_b16 a[194:195],v2 offset:13344
v_fma_mix_f32 v53,v53,v9,0 op_sel_hi:[0,1,0]
v_mfma_f32_16x16x32_bf16 v[108:111],a[240:243],v[40:43],v[108:111]
ds_read_u16 v52,v52
v_fma_mix_f32 v55,v55,v9,0 op_sel:[0,1,0] op_sel_hi:[0,1,0]
v_mfma_f32_16x16x32_bf16 v[104:107],a[240:243],v[36:39],v[104:107]
s_waitcnt lgkmcnt(7)
ds_read_u16_d16_hi v54,v54
v_mfma_f32_16x16x32_bf16 v[40:43],a[208:211],a[0:3],v[20:23]
s_add_u32 s68,s68,s53
v_lshlrev_b32_sdwa v32,1,v32 dst_sel:DWORD dst_unused:UNUSED_PAD src0_sel:DWORD src1_sel:WORD_1
v_lshlrev_b32_sdwa v34,1,v34 dst_sel:DWORD dst_unused:UNUSED_PAD src0_sel:DWORD src1_sel:WORD_1
v_mfma_f32_16x16x32_bf16 v[40:43],a[204:207],a[16:19],v[40:43]
buffer_load_dwordx4 v5,s[44:47],s68 offen offset:1024 lds
v_lshl_add_u64 v[24:25],v[24:25],0,v[26:27]
v_mfma_f32_16x16x32_bf16 v[40:43],a[212:215],a[32:35],v[40:43]
s_add_u32 s68,s68,s53
ds_read_u16 v32,v32
v_lshl_add_u64 v[26:27],v[28:29],0,v[30:31]
v_mfma_f32_16x16x32_bf16 v[36:39],a[200:203],a[32:35],v[16:19]
buffer_load_dwordx4 v5,s[44:47],s68 offen offset:2048 lds
v_lshlrev_b32_sdwa v33,1,v33 dst_sel:DWORD dst_unused:UNUSED_PAD src0_sel:DWORD src1_sel:WORD_1
v_mfma_f32_16x16x32_bf16 v[36:39],a[196:199],a[16:19],v[36:39]
s_waitcnt lgkmcnt(3)
ds_read_u16_d16_hi v34,v34
v_lshl_add_u64 v[28:29],v[56:57],0,v[58:59]
v_mfma_f32_16x16x32_bf16 v[36:39],a[192:195],a[0:3],v[36:39]
ds_read_u16 v33,v33
v_lshlrev_b32_sdwa v35,1,v35 dst_sel:DWORD dst_unused:UNUSED_PAD src0_sel:DWORD src1_sel:WORD_1
v_mfma_f32_16x16x32_bf16 v[56:59],a[192:195],a[4:7],v[16:19]
s_waitcnt lgkmcnt(3)
ds_read_u16_d16_hi v35,v35
v_or_b32 v30,v52,v54
v_lshlrev_b32_sdwa v60,1,v60 dst_sel:DWORD dst_unused:UNUSED_PAD src0_sel:DWORD src1_sel:WORD_1
v_mfma_f32_16x16x32_bf16 v[56:59],a[196:199],a[20:23],v[56:59]
s_waitcnt vmcnt(17)
ds_read_u16 v60,v60
v_cvt_pk_bf16_f32 v31,v53,v55
v_lshlrev_b32_sdwa v62,1,v62 dst_sel:DWORD dst_unused:UNUSED_PAD src0_sel:DWORD src1_sel:WORD_1
v_mfma_f32_16x16x32_bf16 v[56:59],a[200:203],a[36:39],v[56:59]
s_barrier
ds_read_u16_d16_hi v62,v62
v_lshlrev_b32_sdwa v61,1,v61 dst_sel:DWORD dst_unused:UNUSED_PAD src0_sel:DWORD src1_sel:WORD_1
v_mfma_f32_16x16x32_bf16 v[52:55],a[212:215],a[36:39],v[20:23]
s_add_u32 m0,s62,18480
ds_read_u16 v61,v61
v_lshlrev_b32_sdwa v63,1,v63 dst_sel:DWORD dst_unused:UNUSED_PAD src0_sel:DWORD src1_sel:WORD_1
v_mfma_f32_16x16x32_bf16 v[52:55],a[204:207],a[20:23],v[52:55]
s_add_u32 s68,s68,s59
ds_read_u16_d16_hi v63,v63
v_lshlrev_b32_sdwa v48,1,v48 dst_sel:DWORD dst_unused:UNUSED_PAD src0_sel:DWORD src1_sel:WORD_1
v_mfma_f32_16x16x32_bf16 v[52:55],a[208:211],a[4:7],v[52:55]
buffer_load_dwordx4 v5,s[44:47],s68 offen offset:0 lds
v_lshlrev_b32_sdwa v50,1,v50 dst_sel:DWORD dst_unused:UNUSED_PAD src0_sel:DWORD src1_sel:WORD_1
v_mfma_f32_16x16x32_bf16 v[232:235],a[228:231],v[24:27],v[232:235]
s_add_u32 m0,s58,6160
ds_read_u16 v48,v48
v_lshlrev_b32_sdwa v49,1,v49 dst_sel:DWORD dst_unused:UNUSED_PAD src0_sel:DWORD src1_sel:WORD_1
v_mfma_f32_16x16x32_bf16 v[236:239],a[228:231],v[28:31],v[236:239]
s_add_u32 s69,s69,s59
ds_read_u16_d16_hi v50,v50
v_lshlrev_b32_sdwa v51,1,v51 dst_sel:DWORD dst_unused:UNUSED_PAD src0_sel:DWORD src1_sel:WORD_1
v_mfma_f32_16x16x32_bf16 v[164:167],a[236:239],v[28:31],v[164:167]
buffer_load_dwordx4 v5,s[44:47],s69 offen offset:0 lds
ds_read_u16 v49,v49
v_mfma_f32_16x16x32_bf16 v[160:163],a[236:239],v[24:27],v[160:163]
ds_read_u16_d16_hi v51,v51
ds_read_b64_tr_b16 a[224:225],v3 offset:18480
v_mfma_f32_16x16x32_bf16 v[240:243],a[244:247],v[24:27],v[240:243]
s_add_u32 s69,s69,s53
ds_read_b64_tr_b16 a[226:227],v3 offset:20016
v_lshlrev_b32_sdwa v44,1,v44 dst_sel:DWORD dst_unused:UNUSED_PAD src0_sel:DWORD src1_sel:WORD_1
v_mfma_f32_16x16x32_bf16 v[244:247],a[244:247],v[28:31],v[244:247]
buffer_load_dwordx4 v5,s[44:47],s69 offen offset:1024 lds
v_lshlrev_b32_sdwa v46,1,v46 dst_sel:DWORD dst_unused:UNUSED_PAD src0_sel:DWORD src1_sel:WORD_1
v_mfma_f32_16x16x32_bf16 v[180:183],a[252:255],v[28:31],v[180:183]
ds_read_u16 v44,v44
v_lshlrev_b32_sdwa v45,1,v45 dst_sel:DWORD dst_unused:UNUSED_PAD src0_sel:DWORD src1_sel:WORD_1
v_mfma_f32_16x16x32_bf16 v[176:179],a[252:255],v[24:27],v[176:179]
s_add_u32 m0,s62,18480
ds_read_u16_d16_hi v46,v46
v_lshlrev_b32_sdwa v47,1,v47 dst_sel:DWORD dst_unused:UNUSED_PAD src0_sel:DWORD src1_sel:WORD_1
v_mfma_f32_16x16x32_bf16 v[248:251],a[232:235],v[24:27],v[248:251]
s_add_u32 s68,s68,s53
ds_read_u16 v45,v45
v_mfma_f32_16x16x32_bf16 v[252:255],a[232:235],v[28:31],v[252:255]
buffer_load_dwordx4 v5,s[44:47],s68 offen offset:1024 lds
v_mfma_f32_16x16x32_bf16 v[196:199],a[240:243],v[28:31],v[196:199]
s_add_u32 m0,s58,6160
ds_read_u16_d16_hi v47,v47
v_mfma_f32_16x16x32_bf16 v[192:195],a[240:243],v[24:27],v[192:195]
s_add_u32 s69,s69,s53
ds_read_b64_tr_b16 a[248:249],v3 offset:18488
v_mfma_f32_16x16x32_bf16 v[28:31],a[208:211],a[8:11],v[20:23]
buffer_load_dwordx4 v5,s[44:47],s69 offen offset:2048 lds
v_lshlrev_b32_sdwa v40,1,v40 dst_sel:DWORD dst_unused:UNUSED_PAD src0_sel:DWORD src1_sel:WORD_1
v_mfma_f32_16x16x32_bf16 v[28:31],a[204:207],a[24:27],v[28:31]
s_waitcnt lgkmcnt(5)
ds_read_u16 v40,v40
v_lshl_add_u64 v[32:33],v[32:33],0,v[34:35]
v_fma_mixlo_f16 v8,v57,s99,0
v_mfma_f32_16x16x32_bf16 v[28:31],a[212:215],a[40:43],v[28:31]
ds_read_b64_tr_b16 a[250:251],v3 offset:20024
v_lshl_add_u64 v[34:35],v[60:61],0,v[62:63]
v_lshlrev_b32_sdwa v42,1,v42 dst_sel:DWORD dst_unused:UNUSED_PAD src0_sel:DWORD src1_sel:WORD_1
v_mfma_f32_16x16x32_bf16 v[24:27],a[200:203],a[40:43],v[16:19]
ds_read_u16_d16_hi v42,v42
v_fma_mixhi_f16 v8,v59,s99,0
v_mfma_f32_16x16x32_bf16 v[24:27],a[196:199],a[24:27],v[24:27]
ds_read_b64_tr_b16 a[220:221],v1 offset:18480
v_lshl_add_u64 v[60:61],v[48:49],0,v[50:51]
v_lshlrev_b32_sdwa v41,1,v41 dst_sel:DWORD dst_unused:UNUSED_PAD src0_sel:DWORD src1_sel:WORD_1
v_pk_fma_f16 v9,v8,v8,s54
v_mfma_f32_16x16x32_bf16 v[24:27],a[192:195],a[8:11],v[24:27]
ds_read_u16 v41,v41
v_lshlrev_b32_sdwa v43,1,v43 dst_sel:DWORD dst_unused:UNUSED_PAD src0_sel:DWORD src1_sel:WORD_1
v_mfma_f32_16x16x32_bf16 v[48:51],a[192:195],a[12:15],v[16:19]
ds_read_u16_d16_hi v43,v43
v_pk_mul_f16 v9,v8,v9
v_mfma_f32_16x16x32_bf16 v[48:51],a[196:199],a[28:31],v[48:51]
s_waitcnt lgkmcnt(7)
ds_read_b64_tr_b16 a[222:223],v1 offset:19504
v_lshl_add_u64 v[62:63],v[44:45],0,v[46:47]
v_lshlrev_b32_sdwa v36,1,v36 dst_sel:DWORD dst_unused:UNUSED_PAD src0_sel:DWORD src1_sel:WORD_1
v_mfma_f32_16x16x32_bf16 v[48:51],a[200:203],a[44:47],v[48:51]
v_exp_f16 v8,v9
v_mfma_f32_16x16x32_bf16 v[44:47],a[212:215],a[44:47],v[20:23]
ds_read_u16 v36,v36
v_lshlrev_b32_sdwa v38,1,v38 dst_sel:DWORD dst_unused:UNUSED_PAD src0_sel:DWORD src1_sel:WORD_1
v_mfma_f32_16x16x32_bf16 v[44:47],a[204:207],a[28:31],v[44:47]
ds_read_u16_d16_hi v38,v38
v_lshlrev_b32_sdwa v37,1,v37 dst_sel:DWORD dst_unused:UNUSED_PAD src0_sel:DWORD src1_sel:WORD_1
v_mfma_f32_16x16x32_bf16 v[44:47],a[208:211],a[12:15],v[44:47]
v_exp_f16_sdwa v8,v9 dst_sel:WORD_1 dst_unused:UNUSED_PRESERVE src0_sel:WORD_1
v_mfma_f32_16x16x32_bf16 v[208:211],a[228:231],v[32:35],v[208:211]
ds_read_u16 v37,v37
v_lshlrev_b32_sdwa v39,1,v39 dst_sel:DWORD dst_unused:UNUSED_PAD src0_sel:DWORD src1_sel:WORD_1
v_mfma_f32_16x16x32_bf16 v[212:215],a[228:231],v[60:63],v[212:215]
ds_read_u16_d16_hi v39,v39
v_lshlrev_b32_sdwa v52,1,v52 dst_sel:DWORD dst_unused:UNUSED_PAD src0_sel:DWORD src1_sel:WORD_1
v_mfma_f32_16x16x32_bf16 v[172:175],a[236:239],v[60:63],v[172:175]
ds_read_u16 v52,v52
v_pk_fma_f16 v8,v8,s55,s55
v_mfma_f32_16x16x32_bf16 v[168:171],a[236:239],v[32:35],v[168:171]
ds_read_b64_tr_b16 a[228:229],v3 offset:18544
v_rcp_f16_e64 v9,v8
v_lshlrev_b32_sdwa v54,1,v54 dst_sel:DWORD dst_unused:UNUSED_PAD src0_sel:DWORD src1_sel:WORD_1
v_mfma_f32_16x16x32_bf16 v[216:219],a[244:247],v[32:35],v[216:219]
ds_read_u16_d16_hi v54,v54
v_lshlrev_b32_sdwa v53,1,v53 dst_sel:DWORD dst_unused:UNUSED_PAD src0_sel:DWORD src1_sel:WORD_1
v_mfma_f32_16x16x32_bf16 v[220:223],a[244:247],v[60:63],v[220:223]
ds_read_u16 v53,v53
ds_read_b64_tr_b16 a[230:231],v3 offset:20080
v_mfma_f32_16x16x32_bf16 v[188:191],a[252:255],v[60:63],v[188:191]
ds_read_b64_tr_b16 a[236:237],v3 offset:18552
v_lshlrev_b32_sdwa v55,1,v55 dst_sel:DWORD dst_unused:UNUSED_PAD src0_sel:DWORD src1_sel:WORD_1
v_mfma_f32_16x16x32_bf16 v[184:187],a[252:255],v[32:35],v[184:187]
ds_read_u16_d16_hi v55,v55
v_rcp_f16_sdwa v9,v8 dst_sel:WORD_1 dst_unused:UNUSED_PRESERVE src0_sel:WORD_1
v_mfma_f32_16x16x32_bf16 v[224:227],a[232:235],v[32:35],v[224:227]
ds_read_b64_tr_b16 a[238:239],v3 offset:20088
ds_read_b64_tr_b16 a[244:245],v3 offset:18608
v_lshlrev_b32_sdwa v56,1,v56 dst_sel:DWORD dst_unused:UNUSED_PAD src0_sel:DWORD src1_sel:WORD_1
v_mfma_f32_16x16x32_bf16 v[228:231],a[232:235],v[60:63],v[228:231]
ds_read_u16 v56,v56
v_lshlrev_b32_sdwa v58,1,v58 dst_sel:DWORD dst_unused:UNUSED_PAD src0_sel:DWORD src1_sel:WORD_1
v_mfma_f32_16x16x32_bf16 v[204:207],a[240:243],v[60:63],v[204:207]
ds_read_u16_d16_hi v58,v58
v_fma_mix_f32 v57,v57,v9,0 op_sel_hi:[0,1,0]
v_mfma_f32_16x16x32_bf16 v[200:203],a[240:243],v[32:35],v[200:203]
ds_read_b64_tr_b16 a[246:247],v3 offset:20144
v_fma_mix_f32 v59,v59,v9,0 op_sel:[0,1,0] op_sel_hi:[0,1,0]
v_mfma_f32_16x16x32_bf16 v[60:63],a[208:211],a[48:51],v[20:23]
s_add_u32 m0,s58,12320
ds_read_b64_tr_b16 a[252:253],v3 offset:18616
v_lshlrev_b32_sdwa v28,1,v28 dst_sel:DWORD dst_unused:UNUSED_PAD src0_sel:DWORD src1_sel:WORD_1
v_mfma_f32_16x16x32_bf16 v[60:63],a[204:207],a[64:67],v[60:63]
s_waitcnt lgkmcnt(10)
ds_read_b64_tr_b16 a[254:255],v3 offset:20152
v_lshl_add_u64 v[40:41],v[40:41],0,v[42:43]
v_mfma_f32_16x16x32_bf16 v[60:63],a[212:215],a[80:83],v[60:63]
s_add_u32 s69,s69,s59
ds_read_u16 v28,v28
v_lshl_add_u64 v[42:43],v[36:37],0,v[38:39]
v_lshlrev_b32_sdwa v30,1,v30 dst_sel:DWORD dst_unused:UNUSED_PAD src0_sel:DWORD src1_sel:WORD_1
v_mfma_f32_16x16x32_bf16 v[32:35],a[200:203],a[80:83],v[16:19]
buffer_load_dwordx4 v5,s[44:47],s69 offen offset:0 lds
TE12_%=:
s_cbranch_execz TP10728_%=
v_lshlrev_b32_sdwa v29,1,v29 dst_sel:DWORD dst_unused:UNUSED_PAD src0_sel:DWORD src1_sel:WORD_1
v_mfma_f32_16x16x32_bf16 v[32:35],a[196:199],a[64:67],v[32:35]
s_waitcnt lgkmcnt(8)
ds_read_u16_d16_hi v30,v30
v_lshl_add_u64 v[36:37],v[52:53],0,v[54:55]
v_mfma_f32_16x16x32_bf16 v[32:35],a[192:195],a[48:51],v[32:35]
s_add_u32 s69,s69,s53
ds_read_u16 v29,v29
v_lshlrev_b32_sdwa v31,1,v31 dst_sel:DWORD dst_unused:UNUSED_PAD src0_sel:DWORD src1_sel:WORD_1
v_mfma_f32_16x16x32_bf16 v[52:55],a[192:195],a[52:55],v[16:19]
s_waitcnt lgkmcnt(6)
buffer_load_dwordx4 v5,s[44:47],s69 offen offset:1024 lds
v_or_b32_e64 v38,v56,v58
v_lshlrev_b32_sdwa v24,1,v24 dst_sel:DWORD dst_unused:UNUSED_PAD src0_sel:DWORD src1_sel:WORD_1
v_mfma_f32_16x16x32_bf16 v[52:55],a[196:199],a[68:71],v[52:55]
s_add_u32 m0,s62,18480
ds_read_u16_d16_hi v31,v31
v_cvt_pk_bf16_f32 v39,v57,v59
v_lshlrev_b32_sdwa v26,1,v26 dst_sel:DWORD dst_unused:UNUSED_PAD src0_sel:DWORD src1_sel:WORD_1
v_mfma_f32_16x16x32_bf16 v[52:55],a[200:203],a[84:87],v[52:55]
s_add_u32 s68,s68,s53
ds_read_u16 v24,v24
v_lshlrev_b32_sdwa v25,1,v25 dst_sel:DWORD dst_unused:UNUSED_PAD src0_sel:DWORD src1_sel:WORD_1
v_mfma_f32_16x16x32_bf16 v[56:59],a[212:215],a[84:87],v[20:23]
buffer_load_dwordx4 v5,s[44:47],s68 offen offset:2048 lds
ds_read_u16_d16_hi v26,v26
v_mfma_f32_16x16x32_bf16 v[56:59],a[204:207],a[68:71],v[56:59]
s_add_u32 m0,s58,12320
ds_read_u16 v25,v25
v_lshlrev_b32_sdwa v27,1,v27 dst_sel:DWORD dst_unused:UNUSED_PAD src0_sel:DWORD src1_sel:WORD_1
v_mfma_f32_16x16x32_bf16 v[56:59],a[208:211],a[52:55],v[56:59]
s_add_u32 s69,s69,s53
ds_read_u16_d16_hi v27,v27
v_lshlrev_b32_sdwa v44,1,v44 dst_sel:DWORD dst_unused:UNUSED_PAD src0_sel:DWORD src1_sel:WORD_1
v_mfma_f32_16x16x32_bf16 v[136:139],a[224:227],v[40:43],v[136:139]
buffer_load_dwordx4 v5,s[44:47],s69 offen offset:2048 lds
v_lshlrev_b32_sdwa v46,1,v46 dst_sel:DWORD dst_unused:UNUSED_PAD src0_sel:DWORD src1_sel:WORD_1
v_mfma_f32_16x16x32_bf16 v[140:143],a[224:227],v[36:39],v[140:143]
s_add_u32 m0,s62,24640
ds_read_u16 v44,v44
v_lshlrev_b32_sdwa v45,1,v45 dst_sel:DWORD dst_unused:UNUSED_PAD src0_sel:DWORD src1_sel:WORD_1
v_mfma_f32_16x16x32_bf16 v[68:71],a[248:251],v[36:39],v[68:71]
s_add_u32 s68,s68,s59
ds_read_u16_d16_hi v46,v46
v_lshlrev_b32_sdwa v47,1,v47 dst_sel:DWORD dst_unused:UNUSED_PAD src0_sel:DWORD src1_sel:WORD_1
v_mfma_f32_16x16x32_bf16 v[64:67],a[248:251],v[40:43],v[64:67]
buffer_load_dwordx4 v5,s[44:47],s68 offen offset:0 lds
ds_read_u16 v45,v45
v_mfma_f32_16x16x32_bf16 v[144:147],a[228:231],v[40:43],v[144:147]
s_add_u32 s68,s68,s53
ds_read_u16_d16_hi v47,v47
v_lshlrev_b32_sdwa v48,1,v48 dst_sel:DWORD dst_unused:UNUSED_PAD src0_sel:DWORD src1_sel:WORD_1
v_mfma_f32_16x16x32_bf16 v[148:151],a[228:231],v[36:39],v[148:151]
buffer_load_dwordx4 v5,s[44:47],s68 offen offset:1024 lds
v_lshlrev_b32_sdwa v50,1,v50 dst_sel:DWORD dst_unused:UNUSED_PAD src0_sel:DWORD src1_sel:WORD_1
v_mfma_f32_16x16x32_bf16 v[84:87],a[236:239],v[36:39],v[84:87]
ds_read_u16 v48,v48
v_lshlrev_b32_sdwa v49,1,v49 dst_sel:DWORD dst_unused:UNUSED_PAD src0_sel:DWORD src1_sel:WORD_1
v_mfma_f32_16x16x32_bf16 v[80:83],a[236:239],v[40:43],v[80:83]
s_waitcnt lgkmcnt(6)
ds_read_u16_d16_hi v50,v50
v_lshlrev_b32_sdwa v51,1,v51 dst_sel:DWORD dst_unused:UNUSED_PAD src0_sel:DWORD src1_sel:WORD_1
v_mfma_f32_16x16x32_bf16 v[152:155],a[244:247],v[40:43],v[152:155]
ds_read_u16 v49,v49
v_mfma_f32_16x16x32_bf16 v[156:159],a[244:247],v[36:39],v[156:159]
s_add_u32 s68,s68,s53
ds_read_u16_d16_hi v51,v51
v_mfma_f32_16x16x32_bf16 v[100:103],a[252:255],v[36:39],v[100:103]
buffer_load_dwordx4 v5,s[44:47],s68 offen offset:2048 lds
v_mfma_f32_16x16x32_bf16 v[96:99],a[252:255],v[40:43],v[96:99]
ds_read_b64_tr_b16 a[216:217],v1 offset:20528
v_mfma_f32_16x16x32_bf16 v[36:39],a[208:211],a[56:59],v[20:23]
ds_read_b64_tr_b16 a[218:219],v1 offset:21552
v_lshlrev_b32_sdwa v60,1,v60 dst_sel:DWORD dst_unused:UNUSED_PAD src0_sel:DWORD src1_sel:WORD_1
v_mfma_f32_16x16x32_bf16 v[36:39],a[204:207],a[72:75],v[36:39]
ds_read_u16 v60,v60
v_lshl_add_u64 v[28:29],v[28:29],0,v[30:31]
v_fma_mixlo_f16 v8,v53,s99,0
v_mfma_f32_16x16x32_bf16 v[36:39],a[212:215],a[88:91],v[36:39]
s_waitcnt lgkmcnt(7)
v_lshl_add_u64 v[30:31],v[24:25],0,v[26:27]
v_lshlrev_b32_sdwa v62,1,v62 dst_sel:DWORD dst_unused:UNUSED_PAD src0_sel:DWORD src1_sel:WORD_1
v_mfma_f32_16x16x32_bf16 v[40:43],a[200:203],a[88:91],v[16:19]
ds_read_u16_d16_hi v62,v62
v_fma_mixhi_f16 v8,v55,s99,0
v_mfma_f32_16x16x32_bf16 v[40:43],a[196:199],a[72:75],v[40:43]
v_lshl_add_u64 v[24:25],v[44:45],0,v[46:47]
v_lshlrev_b32_sdwa v61,1,v61 dst_sel:DWORD dst_unused:UNUSED_PAD src0_sel:DWORD src1_sel:WORD_1
v_pk_fma_f16 v9,v8,v8,s54
v_mfma_f32_16x16x32_bf16 v[40:43],a[192:195],a[56:59],v[40:43]
ds_read_u16 v61,v61
v_lshlrev_b32_sdwa v63,1,v63 dst_sel:DWORD dst_unused:UNUSED_PAD src0_sel:DWORD src1_sel:WORD_1
v_mfma_f32_16x16x32_bf16 v[44:47],a[192:195],a[60:63],v[16:19]
ds_read_b128 v[16:19],v4 offset:1024
v_pk_mul_f16 v9,v8,v9
v_mfma_f32_16x16x32_bf16 v[44:47],a[196:199],a[76:79],v[44:47]
s_waitcnt lgkmcnt(6)
ds_read_u16_d16_hi v63,v63
ds_read_b64_tr_b16 a[192:193],v1 offset:22576
v_lshl_add_u64 v[26:27],v[48:49],0,v[50:51]
v_lshlrev_b32_sdwa v32,1,v32 dst_sel:DWORD dst_unused:UNUSED_PAD src0_sel:DWORD src1_sel:WORD_1
v_mfma_f32_16x16x32_bf16 v[44:47],a[200:203],a[92:95],v[44:47]
ds_read_b64_tr_b16 a[194:195],v1 offset:23600
v_exp_f16_e64 v8,v9
v_mfma_f32_16x16x32_bf16 v[48:51],a[212:215],a[92:95],v[20:23]
ds_read_b128 v[20:23],v4 offset:1056
ds_read_u16 v32,v32
v_lshlrev_b32_sdwa v34,1,v34 dst_sel:DWORD dst_unused:UNUSED_PAD src0_sel:DWORD src1_sel:WORD_1
v_mfma_f32_16x16x32_bf16 v[48:51],a[204:207],a[76:79],v[48:51]
ds_read_u16_d16_hi v34,v34
ds_read_b64_tr_b16 a[196:197],v2 offset:22576
v_lshlrev_b32_sdwa v33,1,v33 dst_sel:DWORD dst_unused:UNUSED_PAD src0_sel:DWORD src1_sel:WORD_1
v_mfma_f32_16x16x32_bf16 v[48:51],a[208:211],a[60:63],v[48:51]
ds_read_u16 v33,v33
v_exp_f16_sdwa v8,v9 dst_sel:WORD_1 dst_unused:UNUSED_PRESERVE src0_sel:WORD_1
v_mfma_f32_16x16x32_bf16 v[112:115],a[224:227],v[28:31],v[112:115]
ds_read_b64_tr_b16 a[198:199],v2 offset:23600
ds_read_b64_tr_b16 a[200:201],v2 offset:20528
v_lshlrev_b32_sdwa v35,1,v35 dst_sel:DWORD dst_unused:UNUSED_PAD src0_sel:DWORD src1_sel:WORD_1
v_mfma_f32_16x16x32_bf16 v[116:119],a[224:227],v[24:27],v[116:119]
ds_read_u16_d16_hi v35,v35
v_lshlrev_b32_sdwa v56,1,v56 dst_sel:DWORD dst_unused:UNUSED_PAD src0_sel:DWORD src1_sel:WORD_1
v_mfma_f32_16x16x32_bf16 v[76:79],a[248:251],v[24:27],v[76:79]
ds_read_b64_tr_b16 a[202:203],v2 offset:21552
v_pk_fma_f16 v8,v8,s55,s55
v_mfma_f32_16x16x32_bf16 v[72:75],a[248:251],v[28:31],v[72:75]
v_rcp_f16 v9,v8
v_lshlrev_b32_sdwa v58,1,v58 dst_sel:DWORD dst_unused:UNUSED_PAD src0_sel:DWORD src1_sel:WORD_1
v_mfma_f32_16x16x32_bf16 v[120:123],a[228:231],v[28:31],v[120:123]
ds_read_u16 v56,v56
v_lshlrev_b32_sdwa v57,1,v57 dst_sel:DWORD dst_unused:UNUSED_PAD src0_sel:DWORD src1_sel:WORD_1
v_mfma_f32_16x16x32_bf16 v[124:127],a[228:231],v[24:27],v[124:127]
ds_read_u16_d16_hi v58,v58
v_lshlrev_b32_sdwa v59,1,v59 dst_sel:DWORD dst_unused:UNUSED_PAD src0_sel:DWORD src1_sel:WORD_1
v_mfma_f32_16x16x32_bf16 v[92:95],a[236:239],v[24:27],v[92:95]
ds_read_u16 v57,v57
v_lshlrev_b32_sdwa v52,1,v52 dst_sel:DWORD dst_unused:UNUSED_PAD src0_sel:DWORD src1_sel:WORD_1
v_mfma_f32_16x16x32_bf16 v[88:91],a[236:239],v[28:31],v[88:91]
ds_read_u16_d16_hi v59,v59
v_rcp_f16_sdwa v9,v8 dst_sel:WORD_1 dst_unused:UNUSED_PRESERVE src0_sel:WORD_1
v_mfma_f32_16x16x32_bf16 v[128:131],a[244:247],v[28:31],v[128:131]
ds_read_b64_tr_b16 a[212:213],v2 offset:18480
v_lshlrev_b32_sdwa v54,1,v54 dst_sel:DWORD dst_unused:UNUSED_PAD src0_sel:DWORD src1_sel:WORD_1
v_mfma_f32_16x16x32_bf16 v[132:135],a[244:247],v[24:27],v[132:135]
ds_read_b64_tr_b16 a[214:215],v2 offset:19504
v_fma_mix_f32 v53,v53,v9,0 op_sel_hi:[0,1,0]
v_mfma_f32_16x16x32_bf16 v[108:111],a[252:255],v[24:27],v[108:111]
ds_read_u16 v52,v52
v_fma_mix_f32 v55,v55,v9,0 op_sel:[0,1,0] op_sel_hi:[0,1,0]
v_mfma_f32_16x16x32_bf16 v[104:107],a[252:255],v[28:31],v[104:107]
s_waitcnt lgkmcnt(7)
ds_read_u16_d16_hi v54,v54
v_mfma_f32_16x16x32_bf16 v[24:27],a[220:223],a[0:3],v[16:19]
v_lshlrev_b32_sdwa v36,1,v36 dst_sel:DWORD dst_unused:UNUSED_PAD src0_sel:DWORD src1_sel:WORD_1
v_lshlrev_b32_sdwa v38,1,v38 dst_sel:DWORD dst_unused:UNUSED_PAD src0_sel:DWORD src1_sel:WORD_1
v_mfma_f32_16x16x32_bf16 v[24:27],a[216:219],a[16:19],v[24:27]
ds_read_u16 v36,v36
v_lshl_add_u64 v[60:61],v[60:61],0,v[62:63]
v_mfma_f32_16x16x32_bf16 v[24:27],a[192:195],a[32:35],v[24:27]
ds_read_u16_d16_hi v38,v38
v_lshl_add_u64 v[62:63],v[32:33],0,v[34:35]
v_mfma_f32_16x16x32_bf16 v[28:31],a[196:199],a[32:35],v[20:23]
v_lshlrev_b32_sdwa v37,1,v37 dst_sel:DWORD dst_unused:UNUSED_PAD src0_sel:DWORD src1_sel:WORD_1
v_lshlrev_b32_sdwa v39,1,v39 dst_sel:DWORD dst_unused:UNUSED_PAD src0_sel:DWORD src1_sel:WORD_1
v_mfma_f32_16x16x32_bf16 v[28:31],a[200:203],a[16:19],v[28:31]
s_waitcnt lgkmcnt(4)
ds_read_u16 v37,v37
v_lshl_add_u64 v[32:33],v[56:57],0,v[58:59]
v_mfma_f32_16x16x32_bf16 v[28:31],a[212:215],a[0:3],v[28:31]
ds_read_u16_d16_hi v39,v39
v_lshlrev_b32_sdwa v40,1,v40 dst_sel:DWORD dst_unused:UNUSED_PAD src0_sel:DWORD src1_sel:WORD_1
v_mfma_f32_16x16x32_bf16 v[56:59],a[212:215],a[4:7],v[20:23]
s_waitcnt lgkmcnt(4)
ds_read_u16 v40,v40
v_or_b32 v34,v52,v54
v_mfma_f32_16x16x32_bf16 v[56:59],a[200:203],a[20:23],v[56:59]
v_cvt_pk_bf16_f32 v35,v53,v55
v_lshlrev_b32_sdwa v42,1,v42 dst_sel:DWORD dst_unused:UNUSED_PAD src0_sel:DWORD src1_sel:WORD_1
v_mfma_f32_16x16x32_bf16 v[56:59],a[196:199],a[36:39],v[56:59]
ds_read_u16_d16_hi v42,v42
v_lshlrev_b32_sdwa v41,1,v41 dst_sel:DWORD dst_unused:UNUSED_PAD src0_sel:DWORD src1_sel:WORD_1
v_mfma_f32_16x16x32_bf16 v[52:55],a[192:195],a[36:39],v[16:19]
ds_read_u16 v41,v41
v_lshlrev_b32_sdwa v43,1,v43 dst_sel:DWORD dst_unused:UNUSED_PAD src0_sel:DWORD src1_sel:WORD_1
v_mfma_f32_16x16x32_bf16 v[52:55],a[216:219],a[20:23],v[52:55]
ds_read_u16_d16_hi v43,v43
v_lshlrev_b32_sdwa v48,1,v48 dst_sel:DWORD dst_unused:UNUSED_PAD src0_sel:DWORD src1_sel:WORD_1
v_mfma_f32_16x16x32_bf16 v[52:55],a[220:223],a[4:7],v[52:55]
ds_read_u16 v48,v48
v_lshlrev_b32_sdwa v50,1,v50 dst_sel:DWORD dst_unused:UNUSED_PAD src0_sel:DWORD src1_sel:WORD_1
v_mfma_f32_16x16x32_bf16 v[232:235],a[224:227],v[60:63],v[232:235]
ds_read_u16_d16_hi v50,v50
v_lshlrev_b32_sdwa v49,1,v49 dst_sel:DWORD dst_unused:UNUSED_PAD src0_sel:DWORD src1_sel:WORD_1
v_mfma_f32_16x16x32_bf16 v[236:239],a[224:227],v[32:35],v[236:239]
ds_read_u16 v49,v49
v_lshlrev_b32_sdwa v51,1,v51 dst_sel:DWORD dst_unused:UNUSED_PAD src0_sel:DWORD src1_sel:WORD_1
v_mfma_f32_16x16x32_bf16 v[164:167],a[248:251],v[32:35],v[164:167]
ds_read_u16_d16_hi v51,v51
ds_read_b64_tr_b16 a[232:233],v3 offset:0
v_mfma_f32_16x16x32_bf16 v[160:163],a[248:251],v[60:63],v[160:163]
ds_read_b64_tr_b16 a[234:235],v3 offset:1536
v_lshlrev_b32_sdwa v44,1,v44 dst_sel:DWORD dst_unused:UNUSED_PAD src0_sel:DWORD src1_sel:WORD_1
v_mfma_f32_16x16x32_bf16 v[240:243],a[228:231],v[60:63],v[240:243]
ds_read_u16 v44,v44
v_lshlrev_b32_sdwa v46,1,v46 dst_sel:DWORD dst_unused:UNUSED_PAD src0_sel:DWORD src1_sel:WORD_1
v_mfma_f32_16x16x32_bf16 v[244:247],a[228:231],v[32:35],v[244:247]
ds_read_u16_d16_hi v46,v46
v_lshlrev_b32_sdwa v45,1,v45 dst_sel:DWORD dst_unused:UNUSED_PAD src0_sel:DWORD src1_sel:WORD_1
v_mfma_f32_16x16x32_bf16 v[180:183],a[236:239],v[32:35],v[180:183]
ds_read_u16 v45,v45
v_lshlrev_b32_sdwa v47,1,v47 dst_sel:DWORD dst_unused:UNUSED_PAD src0_sel:DWORD src1_sel:WORD_1
v_mfma_f32_16x16x32_bf16 v[176:179],a[236:239],v[60:63],v[176:179]
ds_read_u16_d16_hi v47,v47
v_mfma_f32_16x16x32_bf16 v[248:251],a[244:247],v[60:63],v[248:251]
ds_read_b64_tr_b16 a[240:241],v3 offset:8
v_mfma_f32_16x16x32_bf16 v[252:255],a[244:247],v[32:35],v[252:255]
ds_read_b64_tr_b16 a[242:243],v3 offset:1544
v_mfma_f32_16x16x32_bf16 v[196:199],a[252:255],v[32:35],v[196:199]
v_mfma_f32_16x16x32_bf16 v[192:195],a[252:255],v[60:63],v[192:195]
v_mfma_f32_16x16x32_bf16 v[32:35],a[220:223],a[8:11],v[16:19]
ds_read_b64_tr_b16 a[204:205],v1 offset:0
v_lshlrev_b32_sdwa v24,1,v24 dst_sel:DWORD dst_unused:UNUSED_PAD src0_sel:DWORD src1_sel:WORD_1
v_mfma_f32_16x16x32_bf16 v[32:35],a[216:219],a[24:27],v[32:35]
s_waitcnt lgkmcnt(6)
ds_read_u16 v24,v24
v_lshl_add_u64 v[36:37],v[36:37],0,v[38:39]
v_fma_mixlo_f16 v8,v57,s99,0
v_mfma_f32_16x16x32_bf16 v[32:35],a[192:195],a[40:43],v[32:35]
ds_read_b64_tr_b16 a[206:207],v1 offset:1024
v_lshl_add_u64 v[38:39],v[40:41],0,v[42:43]
v_lshlrev_b32_sdwa v26,1,v26 dst_sel:DWORD dst_unused:UNUSED_PAD src0_sel:DWORD src1_sel:WORD_1
v_mfma_f32_16x16x32_bf16 v[60:63],a[196:199],a[40:43],v[20:23]
ds_read_u16_d16_hi v26,v26
v_fma_mixhi_f16 v8,v59,s99,0
v_mfma_f32_16x16x32_bf16 v[60:63],a[200:203],a[24:27],v[60:63]
ds_read_b64_tr_b16 a[208:209],v1 offset:2048
v_lshl_add_u64 v[40:41],v[48:49],0,v[50:51]
v_lshlrev_b32_sdwa v25,1,v25 dst_sel:DWORD dst_unused:UNUSED_PAD src0_sel:DWORD src1_sel:WORD_1
v_pk_fma_f16 v9,v8,v8,s54
v_mfma_f32_16x16x32_bf16 v[60:63],a[212:215],a[8:11],v[60:63]
ds_read_u16 v25,v25
v_lshlrev_b32_sdwa v27,1,v27 dst_sel:DWORD dst_unused:UNUSED_PAD src0_sel:DWORD src1_sel:WORD_1
v_mfma_f32_16x16x32_bf16 v[48:51],a[212:215],a[12:15],v[20:23]
ds_read_u16_d16_hi v27,v27
v_pk_mul_f16 v9,v8,v9
v_mfma_f32_16x16x32_bf16 v[48:51],a[200:203],a[28:31],v[48:51]
s_waitcnt lgkmcnt(7)
ds_read_b64_tr_b16 a[210:211],v1 offset:3072
v_lshl_add_u64 v[42:43],v[44:45],0,v[46:47]
v_lshlrev_b32_sdwa v28,1,v28 dst_sel:DWORD dst_unused:UNUSED_PAD src0_sel:DWORD src1_sel:WORD_1
v_mfma_f32_16x16x32_bf16 v[48:51],a[196:199],a[44:47],v[48:51]
v_exp_f16 v8,v9
v_mfma_f32_16x16x32_bf16 v[44:47],a[192:195],a[44:47],v[16:19]
ds_read_u16 v28,v28
v_lshlrev_b32_sdwa v30,1,v30 dst_sel:DWORD dst_unused:UNUSED_PAD src0_sel:DWORD src1_sel:WORD_1
v_mfma_f32_16x16x32_bf16 v[44:47],a[216:219],a[28:31],v[44:47]
ds_read_u16_d16_hi v30,v30
v_lshlrev_b32_sdwa v29,1,v29 dst_sel:DWORD dst_unused:UNUSED_PAD src0_sel:DWORD src1_sel:WORD_1
v_mfma_f32_16x16x32_bf16 v[44:47],a[220:223],a[12:15],v[44:47]
v_exp_f16_sdwa v8,v9 dst_sel:WORD_1 dst_unused:UNUSED_PRESERVE src0_sel:WORD_1
v_mfma_f32_16x16x32_bf16 v[208:211],a[224:227],v[36:39],v[208:211]
ds_read_u16 v29,v29
v_lshlrev_b32_sdwa v31,1,v31 dst_sel:DWORD dst_unused:UNUSED_PAD src0_sel:DWORD src1_sel:WORD_1
v_mfma_f32_16x16x32_bf16 v[212:215],a[224:227],v[40:43],v[212:215]
ds_read_u16_d16_hi v31,v31
v_lshlrev_b32_sdwa v52,1,v52 dst_sel:DWORD dst_unused:UNUSED_PAD src0_sel:DWORD src1_sel:WORD_1
v_mfma_f32_16x16x32_bf16 v[172:175],a[248:251],v[40:43],v[172:175]
ds_read_u16 v52,v52
v_pk_fma_f16 v8,v8,s55,s55
v_mfma_f32_16x16x32_bf16 v[168:171],a[248:251],v[36:39],v[168:171]
ds_read_b64_tr_b16 a[224:225],v3 offset:64
v_rcp_f16 v9,v8
v_lshlrev_b32_sdwa v54,1,v54 dst_sel:DWORD dst_unused:UNUSED_PAD src0_sel:DWORD src1_sel:WORD_1
v_mfma_f32_16x16x32_bf16 v[216:219],a[228:231],v[36:39],v[216:219]
ds_read_u16_d16_hi v54,v54
v_lshlrev_b32_sdwa v53,1,v53 dst_sel:DWORD dst_unused:UNUSED_PAD src0_sel:DWORD src1_sel:WORD_1
v_mfma_f32_16x16x32_bf16 v[220:223],a[228:231],v[40:43],v[220:223]
ds_read_u16 v53,v53
ds_read_b64_tr_b16 a[226:227],v3 offset:1600
v_mfma_f32_16x16x32_bf16 v[188:191],a[236:239],v[40:43],v[188:191]
ds_read_b64_tr_b16 a[248:249],v3 offset:72
v_lshlrev_b32_sdwa v55,1,v55 dst_sel:DWORD dst_unused:UNUSED_PAD src0_sel:DWORD src1_sel:WORD_1
v_mfma_f32_16x16x32_bf16 v[184:187],a[236:239],v[36:39],v[184:187]
ds_read_u16_d16_hi v55,v55
v_rcp_f16_sdwa v9,v8 dst_sel:WORD_1 dst_unused:UNUSED_PRESERVE src0_sel:WORD_1
v_mfma_f32_16x16x32_bf16 v[224:227],a[244:247],v[36:39],v[224:227]
ds_read_b64_tr_b16 a[250:251],v3 offset:1608
ds_read_b64_tr_b16 a[228:229],v3 offset:128
v_lshlrev_b32_sdwa v56,1,v56 dst_sel:DWORD dst_unused:UNUSED_PAD src0_sel:DWORD src1_sel:WORD_1
v_mfma_f32_16x16x32_bf16 v[228:231],a[244:247],v[40:43],v[228:231]
ds_read_u16 v56,v56
v_lshlrev_b32_sdwa v58,1,v58 dst_sel:DWORD dst_unused:UNUSED_PAD src0_sel:DWORD src1_sel:WORD_1
v_mfma_f32_16x16x32_bf16 v[204:207],a[252:255],v[40:43],v[204:207]
ds_read_u16_d16_hi v58,v58
v_fma_mix_f32 v57,v57,v9,0 op_sel_hi:[0,1,0]
v_mfma_f32_16x16x32_bf16 v[200:203],a[252:255],v[36:39],v[200:203]
ds_read_b64_tr_b16 a[230:231],v3 offset:1664
v_fma_mix_f32 v59,v59,v9,0 op_sel:[0,1,0] op_sel_hi:[0,1,0]
v_mbcnt_lo_u32_b32 v8,-1,0
N_qs20_%=:
s_add_u32 s64,s16,N_qs20_%=-N_pf_%=+0
v_and_b32 v8,3,v8
s_mov_b32 m0,12288
v_lshl_add_u32 v8,v8,7,s64
v_min_u32 v8,N_mainend_%=-N_pf_%=+2048,v8
global_load_lds_dword v8,s[14:15]
v_mfma_f32_16x16x32_bf16 v[40:43],a[220:223],a[48:51],v[16:19]
ds_read_b64_tr_b16 a[236:237],v3 offset:136
v_lshlrev_b32_sdwa v32,1,v32 dst_sel:DWORD dst_unused:UNUSED_PAD src0_sel:DWORD src1_sel:WORD_1
v_mfma_f32_16x16x32_bf16 v[40:43],a[216:219],a[64:67],v[40:43]
s_waitcnt lgkmcnt(10)
ds_read_b64_tr_b16 a[238:239],v3 offset:1672
v_lshl_add_u64 v[24:25],v[24:25],0,v[26:27]
v_mfma_f32_16x16x32_bf16 v[40:43],a[192:195],a[80:83],v[40:43]
ds_read_u16 v32,v32
v_lshl_add_u64 v[26:27],v[28:29],0,v[30:31]
v_lshlrev_b32_sdwa v34,1,v34 dst_sel:DWORD dst_unused:UNUSED_PAD src0_sel:DWORD src1_sel:WORD_1
v_mfma_f32_16x16x32_bf16 v[36:39],a[196:199],a[80:83],v[20:23]
ds_read_u16_d16_hi v34,v34
v_lshlrev_b32_sdwa v33,1,v33 dst_sel:DWORD dst_unused:UNUSED_PAD src0_sel:DWORD src1_sel:WORD_1
v_mfma_f32_16x16x32_bf16 v[36:39],a[200:203],a[64:67],v[36:39]
s_waitcnt lgkmcnt(9)
ds_read_u16 v33,v33
v_lshl_add_u64 v[28:29],v[52:53],0,v[54:55]
v_mfma_f32_16x16x32_bf16 v[36:39],a[212:215],a[48:51],v[36:39]
v_lshlrev_b32_sdwa v35,1,v35 dst_sel:DWORD dst_unused:UNUSED_PAD src0_sel:DWORD src1_sel:WORD_1
v_lshlrev_b32_sdwa v60,1,v60 dst_sel:DWORD dst_unused:UNUSED_PAD src0_sel:DWORD src1_sel:WORD_1
v_mfma_f32_16x16x32_bf16 v[52:55],a[212:215],a[52:55],v[20:23]
s_waitcnt lgkmcnt(6)
ds_read_u16_d16_hi v35,v35
v_or_b32 v30,v56,v58
v_mfma_f32_16x16x32_bf16 v[52:55],a[200:203],a[68:71],v[52:55]
ds_read_u16 v60,v60
v_cvt_pk_bf16_f32 v31,v57,v59
v_lshlrev_b32_sdwa v62,1,v62 dst_sel:DWORD dst_unused:UNUSED_PAD src0_sel:DWORD src1_sel:WORD_1
v_mfma_f32_16x16x32_bf16 v[52:55],a[196:199],a[84:87],v[52:55]
ds_read_u16_d16_hi v62,v62
v_lshlrev_b32_sdwa v61,1,v61 dst_sel:DWORD dst_unused:UNUSED_PAD src0_sel:DWORD src1_sel:WORD_1
v_mfma_f32_16x16x32_bf16 v[56:59],a[192:195],a[84:87],v[16:19]
ds_read_u16 v61,v61
v_lshlrev_b32_sdwa v63,1,v63 dst_sel:DWORD dst_unused:UNUSED_PAD src0_sel:DWORD src1_sel:WORD_1
v_mfma_f32_16x16x32_bf16 v[56:59],a[216:219],a[68:71],v[56:59]
ds_read_u16_d16_hi v63,v63
v_lshlrev_b32_sdwa v44,1,v44 dst_sel:DWORD dst_unused:UNUSED_PAD src0_sel:DWORD src1_sel:WORD_1
v_mfma_f32_16x16x32_bf16 v[56:59],a[220:223],a[52:55],v[56:59]
ds_read_u16 v44,v44
v_lshlrev_b32_sdwa v46,1,v46 dst_sel:DWORD dst_unused:UNUSED_PAD src0_sel:DWORD src1_sel:WORD_1
v_mfma_f32_16x16x32_bf16 v[136:139],a[232:235],v[24:27],v[136:139]
ds_read_u16_d16_hi v46,v46
v_lshlrev_b32_sdwa v45,1,v45 dst_sel:DWORD dst_unused:UNUSED_PAD src0_sel:DWORD src1_sel:WORD_1
v_mfma_f32_16x16x32_bf16 v[140:143],a[232:235],v[28:31],v[140:143]
ds_read_u16 v45,v45
v_lshlrev_b32_sdwa v47,1,v47 dst_sel:DWORD dst_unused:UNUSED_PAD src0_sel:DWORD src1_sel:WORD_1
v_mfma_f32_16x16x32_bf16 v[68:71],a[240:243],v[28:31],v[68:71]
ds_read_u16_d16_hi v47,v47
v_lshlrev_b32_sdwa v48,1,v48 dst_sel:DWORD dst_unused:UNUSED_PAD src0_sel:DWORD src1_sel:WORD_1
v_mfma_f32_16x16x32_bf16 v[64:67],a[240:243],v[24:27],v[64:67]
ds_read_u16 v48,v48
v_lshlrev_b32_sdwa v50,1,v50 dst_sel:DWORD dst_unused:UNUSED_PAD src0_sel:DWORD src1_sel:WORD_1
v_mfma_f32_16x16x32_bf16 v[144:147],a[224:227],v[24:27],v[144:147]
ds_read_u16_d16_hi v50,v50
v_lshlrev_b32_sdwa v49,1,v49 dst_sel:DWORD dst_unused:UNUSED_PAD src0_sel:DWORD src1_sel:WORD_1
v_mfma_f32_16x16x32_bf16 v[148:151],a[224:227],v[28:31],v[148:151]
ds_read_u16 v49,v49
v_lshlrev_b32_sdwa v51,1,v51 dst_sel:DWORD dst_unused:UNUSED_PAD src0_sel:DWORD src1_sel:WORD_1
v_mfma_f32_16x16x32_bf16 v[84:87],a[248:251],v[28:31],v[84:87]
ds_read_u16_d16_hi v51,v51
v_mfma_f32_16x16x32_bf16 v[80:83],a[248:251],v[24:27],v[80:83]
s_waitcnt lgkmcnt(8)
v_mfma_f32_16x16x32_bf16 v[152:155],a[228:231],v[24:27],v[152:155]
v_mfma_f32_16x16x32_bf16 v[156:159],a[228:231],v[28:31],v[156:159]
v_mfma_f32_16x16x32_bf16 v[100:103],a[236:239],v[28:31],v[100:103]
v_mfma_f32_16x16x32_bf16 v[96:99],a[236:239],v[24:27],v[96:99]
v_mfma_f32_16x16x32_bf16 v[28:31],a[220:223],a[56:59],v[16:19]
v_lshlrev_b32_sdwa v40,1,v40 dst_sel:DWORD dst_unused:UNUSED_PAD src0_sel:DWORD src1_sel:WORD_1
v_fma_mixlo_f16 v8,v53,s99,0
v_mfma_f32_16x16x32_bf16 v[28:31],a[216:219],a[72:75],v[28:31]
ds_read_u16 v40,v40
v_lshl_add_u64 v[32:33],v[32:33],0,v[34:35]
v_mfma_f32_16x16x32_bf16 v[28:31],a[192:195],a[88:91],v[28:31]
v_lshl_add_u64 v[34:35],v[60:61],0,v[62:63]
v_lshlrev_b32_sdwa v42,1,v42 dst_sel:DWORD dst_unused:UNUSED_PAD src0_sel:DWORD src1_sel:WORD_1
v_mfma_f32_16x16x32_bf16 v[24:27],a[196:199],a[88:91],v[20:23]
ds_read_u16_d16_hi v42,v42
v_fma_mixhi_f16 v8,v55,s99,0
v_mfma_f32_16x16x32_bf16 v[24:27],a[200:203],a[72:75],v[24:27]
s_waitcnt lgkmcnt(2)
v_lshl_add_u64 v[60:61],v[44:45],0,v[46:47]
v_lshlrev_b32_sdwa v41,1,v41 dst_sel:DWORD dst_unused:UNUSED_PAD src0_sel:DWORD src1_sel:WORD_1
v_pk_fma_f16 v9,v8,v8,s54
v_mfma_f32_16x16x32_bf16 v[24:27],a[212:215],a[56:59],v[24:27]
ds_read_u16 v41,v41
v_lshlrev_b32_sdwa v43,1,v43 dst_sel:DWORD dst_unused:UNUSED_PAD src0_sel:DWORD src1_sel:WORD_1
v_mfma_f32_16x16x32_bf16 v[44:47],a[212:215],a[60:63],v[20:23]
ds_read_b128 v[20:23],v4 offset:1152
v_pk_mul_f16 v9,v8,v9
v_mfma_f32_16x16x32_bf16 v[44:47],a[200:203],a[76:79],v[44:47]
ds_read_u16_d16_hi v43,v43
ds_read_b64_tr_b16 a[212:213],v1 offset:4096
v_lshl_add_u64 v[62:63],v[48:49],0,v[50:51]
v_lshlrev_b32_sdwa v36,1,v36 dst_sel:DWORD dst_unused:UNUSED_PAD src0_sel:DWORD src1_sel:WORD_1
v_mfma_f32_16x16x32_bf16 v[44:47],a[196:199],a[92:95],v[44:47]
ds_read_b64_tr_b16 a[214:215],v1 offset:5120
v_exp_f16 v8,v9
v_mfma_f32_16x16x32_bf16 v[48:51],a[192:195],a[92:95],v[16:19]
ds_read_b128 v[16:19],v4 offset:1184
ds_read_u16 v36,v36
v_lshlrev_b32_sdwa v38,1,v38 dst_sel:DWORD dst_unused:UNUSED_PAD src0_sel:DWORD src1_sel:WORD_1
v_mfma_f32_16x16x32_bf16 v[48:51],a[216:219],a[76:79],v[48:51]
ds_read_u16_d16_hi v38,v38
ds_read_b64_tr_b16 a[200:201],v2 offset:4096
v_lshlrev_b32_sdwa v37,1,v37 dst_sel:DWORD dst_unused:UNUSED_PAD src0_sel:DWORD src1_sel:WORD_1
v_mfma_f32_16x16x32_bf16 v[48:51],a[220:223],a[60:63],v[48:51]
ds_read_u16 v37,v37
v_exp_f16_sdwa v8,v9 dst_sel:WORD_1 dst_unused:UNUSED_PRESERVE src0_sel:WORD_1
v_mfma_f32_16x16x32_bf16 v[112:115],a[232:235],v[32:35],v[112:115]
ds_read_b64_tr_b16 a[202:203],v2 offset:5120
ds_read_b64_tr_b16 a[196:197],v2 offset:2048
v_lshlrev_b32_sdwa v39,1,v39 dst_sel:DWORD dst_unused:UNUSED_PAD src0_sel:DWORD src1_sel:WORD_1
v_mfma_f32_16x16x32_bf16 v[116:119],a[232:235],v[60:63],v[116:119]
ds_read_u16_d16_hi v39,v39
v_lshlrev_b32_sdwa v56,1,v56 dst_sel:DWORD dst_unused:UNUSED_PAD src0_sel:DWORD src1_sel:WORD_1
v_mfma_f32_16x16x32_bf16 v[76:79],a[240:243],v[60:63],v[76:79]
ds_read_b64_tr_b16 a[198:199],v2 offset:3072
v_pk_fma_f16 v8,v8,s55,s55
v_mfma_f32_16x16x32_bf16 v[72:75],a[240:243],v[32:35],v[72:75]
v_rcp_f16 v9,v8
v_lshlrev_b32_sdwa v58,1,v58 dst_sel:DWORD dst_unused:UNUSED_PAD src0_sel:DWORD src1_sel:WORD_1
v_mfma_f32_16x16x32_bf16 v[120:123],a[224:227],v[32:35],v[120:123]
ds_read_u16 v56,v56
v_lshlrev_b32_sdwa v57,1,v57 dst_sel:DWORD dst_unused:UNUSED_PAD src0_sel:DWORD src1_sel:WORD_1
v_mfma_f32_16x16x32_bf16 v[124:127],a[224:227],v[60:63],v[124:127]
ds_read_u16_d16_hi v58,v58
v_lshlrev_b32_sdwa v59,1,v59 dst_sel:DWORD dst_unused:UNUSED_PAD src0_sel:DWORD src1_sel:WORD_1
v_mfma_f32_16x16x32_bf16 v[92:95],a[248:251],v[60:63],v[92:95]
ds_read_u16 v57,v57
v_lshlrev_b32_sdwa v52,1,v52 dst_sel:DWORD dst_unused:UNUSED_PAD src0_sel:DWORD src1_sel:WORD_1
v_mfma_f32_16x16x32_bf16 v[88:91],a[248:251],v[32:35],v[88:91]
ds_read_u16_d16_hi v59,v59
v_rcp_f16_sdwa v9,v8 dst_sel:WORD_1 dst_unused:UNUSED_PRESERVE src0_sel:WORD_1
v_mfma_f32_16x16x32_bf16 v[128:131],a[228:231],v[32:35],v[128:131]
ds_read_b64_tr_b16 a[192:193],v2 offset:0
v_lshlrev_b32_sdwa v54,1,v54 dst_sel:DWORD dst_unused:UNUSED_PAD src0_sel:DWORD src1_sel:WORD_1
v_mfma_f32_16x16x32_bf16 v[132:135],a[228:231],v[60:63],v[132:135]
ds_read_b64_tr_b16 a[194:195],v2 offset:1024
v_fma_mix_f32 v53,v53,v9,0 op_sel_hi:[0,1,0]
v_mfma_f32_16x16x32_bf16 v[108:111],a[236:239],v[60:63],v[108:111]
ds_read_u16 v52,v52
v_fma_mix_f32 v55,v55,v9,0 op_sel:[0,1,0] op_sel_hi:[0,1,0]
v_mfma_f32_16x16x32_bf16 v[104:107],a[236:239],v[32:35],v[104:107]
s_waitcnt lgkmcnt(7)
ds_read_u16_d16_hi v54,v54
v_mfma_f32_16x16x32_bf16 v[60:63],a[204:207],a[0:3],v[20:23]
v_lshlrev_b32_sdwa v28,1,v28 dst_sel:DWORD dst_unused:UNUSED_PAD src0_sel:DWORD src1_sel:WORD_1
v_lshlrev_b32_sdwa v30,1,v30 dst_sel:DWORD dst_unused:UNUSED_PAD src0_sel:DWORD src1_sel:WORD_1
v_mfma_f32_16x16x32_bf16 v[60:63],a[208:211],a[16:19],v[60:63]
ds_read_u16 v28,v28
v_lshl_add_u64 v[40:41],v[40:41],0,v[42:43]
v_mfma_f32_16x16x32_bf16 v[60:63],a[212:215],a[32:35],v[60:63]
ds_read_u16_d16_hi v30,v30
v_lshl_add_u64 v[42:43],v[36:37],0,v[38:39]
v_mfma_f32_16x16x32_bf16 v[32:35],a[200:203],a[32:35],v[16:19]
v_lshlrev_b32_sdwa v29,1,v29 dst_sel:DWORD dst_unused:UNUSED_PAD src0_sel:DWORD src1_sel:WORD_1
v_lshlrev_b32_sdwa v31,1,v31 dst_sel:DWORD dst_unused:UNUSED_PAD src0_sel:DWORD src1_sel:WORD_1
v_mfma_f32_16x16x32_bf16 v[32:35],a[196:199],a[16:19],v[32:35]
s_waitcnt lgkmcnt(4)
ds_read_u16 v29,v29
v_lshl_add_u64 v[36:37],v[56:57],0,v[58:59]
v_mfma_f32_16x16x32_bf16 v[32:35],a[192:195],a[0:3],v[32:35]
ds_read_u16_d16_hi v31,v31
v_lshlrev_b32_sdwa v24,1,v24 dst_sel:DWORD dst_unused:UNUSED_PAD src0_sel:DWORD src1_sel:WORD_1
v_mfma_f32_16x16x32_bf16 v[56:59],a[192:195],a[4:7],v[16:19]
s_waitcnt lgkmcnt(4)
ds_read_u16 v24,v24
v_or_b32 v38,v52,v54
v_mfma_f32_16x16x32_bf16 v[56:59],a[196:199],a[20:23],v[56:59]
s_waitcnt vmcnt(4)
v_cvt_pk_bf16_f32 v39,v53,v55
v_lshlrev_b32_sdwa v26,1,v26 dst_sel:DWORD dst_unused:UNUSED_PAD src0_sel:DWORD src1_sel:WORD_1
v_mfma_f32_16x16x32_bf16 v[56:59],a[200:203],a[36:39],v[56:59]
s_barrier
ds_read_u16_d16_hi v26,v26
v_lshlrev_b32_sdwa v25,1,v25 dst_sel:DWORD dst_unused:UNUSED_PAD src0_sel:DWORD src1_sel:WORD_1
v_mfma_f32_16x16x32_bf16 v[52:55],a[212:215],a[36:39],v[20:23]
ds_read_u16 v25,v25
v_lshlrev_b32_sdwa v27,1,v27 dst_sel:DWORD dst_unused:UNUSED_PAD src0_sel:DWORD src1_sel:WORD_1
v_mfma_f32_16x16x32_bf16 v[52:55],a[208:211],a[20:23],v[52:55]
s_add_u32 m0,s58,18480
ds_read_u16_d16_hi v27,v27
v_lshlrev_b32_sdwa v48,1,v48 dst_sel:DWORD dst_unused:UNUSED_PAD src0_sel:DWORD src1_sel:WORD_1
v_mfma_f32_16x16x32_bf16 v[52:55],a[204:207],a[4:7],v[52:55]
s_mov_b32 s69,s88
ds_read_u16 v48,v48
v_lshlrev_b32_sdwa v50,1,v50 dst_sel:DWORD dst_unused:UNUSED_PAD src0_sel:DWORD src1_sel:WORD_1
v_mfma_f32_16x16x32_bf16 v[232:235],a[232:235],v[40:43],v[232:235]
buffer_load_dwordx4 v5,s[44:47],s69 offen offset:0 lds
v_lshlrev_b32_sdwa v49,1,v49 dst_sel:DWORD dst_unused:UNUSED_PAD src0_sel:DWORD src1_sel:WORD_1
v_mfma_f32_16x16x32_bf16 v[236:239],a[232:235],v[36:39],v[236:239]
ds_read_u16_d16_hi v50,v50
v_lshlrev_b32_sdwa v51,1,v51 dst_sel:DWORD dst_unused:UNUSED_PAD src0_sel:DWORD src1_sel:WORD_1
v_mfma_f32_16x16x32_bf16 v[164:167],a[240:243],v[36:39],v[164:167]
ds_read_u16 v49,v49
ds_read_u16_d16_hi v51,v51
v_mfma_f32_16x16x32_bf16 v[160:163],a[240:243],v[40:43],v[160:163]
ds_read_b64_tr_b16 a[244:245],v3 offset:6160
v_lshlrev_b32_sdwa v44,1,v44 dst_sel:DWORD dst_unused:UNUSED_PAD src0_sel:DWORD src1_sel:WORD_1
v_mfma_f32_16x16x32_bf16 v[240:243],a[224:227],v[40:43],v[240:243]
s_add_u32 s69,s69,s53
ds_read_u16 v44,v44
v_lshlrev_b32_sdwa v46,1,v46 dst_sel:DWORD dst_unused:UNUSED_PAD src0_sel:DWORD src1_sel:WORD_1
v_mfma_f32_16x16x32_bf16 v[244:247],a[224:227],v[36:39],v[244:247]
buffer_load_dwordx4 v5,s[44:47],s69 offen offset:1024 lds
ds_read_u16_d16_hi v46,v46
v_mfma_f32_16x16x32_bf16 v[180:183],a[248:251],v[36:39],v[180:183]
ds_read_b64_tr_b16 a[246:247],v3 offset:7696
v_lshlrev_b32_sdwa v45,1,v45 dst_sel:DWORD dst_unused:UNUSED_PAD src0_sel:DWORD src1_sel:WORD_1
v_mfma_f32_16x16x32_bf16 v[176:179],a[248:251],v[40:43],v[176:179]
ds_read_u16 v45,v45
v_lshlrev_b32_sdwa v47,1,v47 dst_sel:DWORD dst_unused:UNUSED_PAD src0_sel:DWORD src1_sel:WORD_1
v_mfma_f32_16x16x32_bf16 v[248:251],a[228:231],v[40:43],v[248:251]
ds_read_u16_d16_hi v47,v47
v_mfma_f32_16x16x32_bf16 v[252:255],a[228:231],v[36:39],v[252:255]
s_add_u32 s69,s69,s53
ds_read_b64_tr_b16 a[252:253],v3 offset:6168
v_mfma_f32_16x16x32_bf16 v[196:199],a[236:239],v[36:39],v[196:199]
buffer_load_dwordx4 v5,s[44:47],s69 offen offset:2048 lds
v_mfma_f32_16x16x32_bf16 v[192:195],a[236:239],v[40:43],v[192:195]
ds_read_b64_tr_b16 a[254:255],v3 offset:7704
v_add_u32 v11,88272,v1
v_add_u32 v12,88272,v2
v_mfma_f32_16x16x32_bf16 v[36:39],a[204:207],a[8:11],v[20:23]
ds_read_b64_tr_b16 a[96:97],v11 offset:0
TE13_%=:
s_cbranch_execz TR1_%=
ds_read_b64_tr_b16 a[98:99],v11 offset:1024
v_lshlrev_b32_sdwa v60,1,v60 dst_sel:DWORD dst_unused:UNUSED_PAD src0_sel:DWORD src1_sel:WORD_1
v_mfma_f32_16x16x32_bf16 v[36:39],a[208:211],a[24:27],v[36:39]
s_waitcnt lgkmcnt(8)
ds_read_b64_tr_b16 a[100:101],v11 offset:2048
ds_read_b64_tr_b16 a[102:103],v11 offset:3072
v_lshl_add_u64 v[28:29],v[28:29],0,v[30:31]
v_fma_mixlo_f16 v8,v57,s99,0
v_mfma_f32_16x16x32_bf16 v[36:39],a[212:215],a[40:43],v[36:39]
s_nop 0
ds_read_u16 v60,v60
ds_read_b64_tr_b16 a[104:105],v11 offset:4096
v_lshl_add_u64 v[30:31],v[24:25],0,v[26:27]
v_lshlrev_b32_sdwa v62,1,v62 dst_sel:DWORD dst_unused:UNUSED_PAD src0_sel:DWORD src1_sel:WORD_1
v_mfma_f32_16x16x32_bf16 v[40:43],a[200:203],a[40:43],v[16:19]
ds_read_u16_d16_hi v62,v62
ds_read_b64_tr_b16 a[106:107],v11 offset:5120
v_fma_mixhi_f16 v8,v59,s99,0
v_mfma_f32_16x16x32_bf16 v[40:43],a[196:199],a[24:27],v[40:43]
ds_read_b64_tr_b16 a[108:109],v12 offset:4096
v_lshl_add_u64 v[24:25],v[48:49],0,v[50:51]
v_lshlrev_b32_sdwa v61,1,v61 dst_sel:DWORD dst_unused:UNUSED_PAD src0_sel:DWORD src1_sel:WORD_1
v_pk_fma_f16 v9,v8,v8,s54
v_mfma_f32_16x16x32_bf16 v[40:43],a[192:195],a[8:11],v[40:43]
ds_read_u16 v61,v61
v_lshlrev_b32_sdwa v63,1,v63 dst_sel:DWORD dst_unused:UNUSED_PAD src0_sel:DWORD src1_sel:WORD_1
v_mfma_f32_16x16x32_bf16 v[48:51],a[192:195],a[12:15],v[16:19]
s_nop 0
ds_read_u16_d16_hi v63,v63
v_pk_mul_f16 v9,v8,v9
v_mfma_f32_16x16x32_bf16 v[48:51],a[196:199],a[28:31],v[48:51]
s_waitcnt lgkmcnt(12)
ds_read_b64_tr_b16 a[110:111],v12 offset:5120
ds_read_b64_tr_b16 a[112:113],v12 offset:2048
v_lshl_add_u64 v[26:27],v[44:45],0,v[46:47]
v_lshlrev_b32_sdwa v32,1,v32 dst_sel:DWORD dst_unused:UNUSED_PAD src0_sel:DWORD src1_sel:WORD_1
v_mfma_f32_16x16x32_bf16 v[48:51],a[200:203],a[44:47],v[48:51]
ds_read_u16 v32,v32
v_exp_f16 v8,v9
v_mfma_f32_16x16x32_bf16 v[44:47],a[212:215],a[44:47],v[20:23]
ds_read_b64_tr_b16 a[114:115],v12 offset:3072
v_lshlrev_b32_sdwa v34,1,v34 dst_sel:DWORD dst_unused:UNUSED_PAD src0_sel:DWORD src1_sel:WORD_1
v_mfma_f32_16x16x32_bf16 v[44:47],a[208:211],a[28:31],v[44:47]
ds_read_u16_d16_hi v34,v34
v_lshlrev_b32_sdwa v33,1,v33 dst_sel:DWORD dst_unused:UNUSED_PAD src0_sel:DWORD src1_sel:WORD_1
v_mfma_f32_16x16x32_bf16 v[44:47],a[204:207],a[12:15],v[44:47]
v_exp_f16_sdwa v8,v9 dst_sel:WORD_1 dst_unused:UNUSED_PRESERVE src0_sel:WORD_1
v_mfma_f32_16x16x32_bf16 v[208:211],a[232:235],v[28:31],v[208:211]
ds_read_u16 v33,v33
v_lshlrev_b32_sdwa v35,1,v35 dst_sel:DWORD dst_unused:UNUSED_PAD src0_sel:DWORD src1_sel:WORD_1
v_mfma_f32_16x16x32_bf16 v[212:215],a[232:235],v[24:27],v[212:215]
ds_read_u16_d16_hi v35,v35
ds_read_b64_tr_b16 a[232:233],v3 offset:6224
v_lshlrev_b32_sdwa v52,1,v52 dst_sel:DWORD dst_unused:UNUSED_PAD src0_sel:DWORD src1_sel:WORD_1
v_mfma_f32_16x16x32_bf16 v[172:175],a[240:243],v[24:27],v[172:175]
ds_read_u16 v52,v52
v_pk_fma_f16 v8,v8,s55,s55
v_mfma_f32_16x16x32_bf16 v[168:171],a[240:243],v[28:31],v[168:171]
ds_read_b64_tr_b16 a[234:235],v3 offset:7760
v_rcp_f16 v9,v8
v_lshlrev_b32_sdwa v54,1,v54 dst_sel:DWORD dst_unused:UNUSED_PAD src0_sel:DWORD src1_sel:WORD_1
v_mfma_f32_16x16x32_bf16 v[216:219],a[224:227],v[28:31],v[216:219]
ds_read_u16_d16_hi v54,v54
ds_read_b64_tr_b16 a[240:241],v3 offset:6232
v_mfma_f32_16x16x32_bf16 v[220:223],a[224:227],v[24:27],v[220:223]
ds_read_b64_tr_b16 a[242:243],v3 offset:7768
ds_read_b64_tr_b16 a[224:225],v3 offset:6288
v_lshlrev_b32_sdwa v53,1,v53 dst_sel:DWORD dst_unused:UNUSED_PAD src0_sel:DWORD src1_sel:WORD_1
v_mfma_f32_16x16x32_bf16 v[188:191],a[248:251],v[24:27],v[188:191]
ds_read_u16 v53,v53
ds_read_b64_tr_b16 a[226:227],v3 offset:7824
v_lshlrev_b32_sdwa v55,1,v55 dst_sel:DWORD dst_unused:UNUSED_PAD src0_sel:DWORD src1_sel:WORD_1
v_mfma_f32_16x16x32_bf16 v[184:187],a[248:251],v[28:31],v[184:187]
ds_read_u16_d16_hi v55,v55
v_rcp_f16_sdwa v9,v8 dst_sel:WORD_1 dst_unused:UNUSED_PRESERVE src0_sel:WORD_1
v_mfma_f32_16x16x32_bf16 v[224:227],a[228:231],v[28:31],v[224:227]
ds_read_b64_tr_b16 a[248:249],v3 offset:6296
ds_read_b64_tr_b16 a[250:251],v3 offset:7832
v_lshlrev_b32_sdwa v56,1,v56 dst_sel:DWORD dst_unused:UNUSED_PAD src0_sel:DWORD src1_sel:WORD_1
v_mfma_f32_16x16x32_bf16 v[228:231],a[228:231],v[24:27],v[228:231]
ds_read_u16 v56,v56
ds_read_b64_tr_b16 a[116:117],v12 offset:0
v_lshlrev_b32_sdwa v58,1,v58 dst_sel:DWORD dst_unused:UNUSED_PAD src0_sel:DWORD src1_sel:WORD_1
v_mfma_f32_16x16x32_bf16 v[204:207],a[236:239],v[24:27],v[204:207]
ds_read_u16_d16_hi v58,v58
v_fma_mix_f32 v57,v57,v9,0 op_sel_hi:[0,1,0]
v_mfma_f32_16x16x32_bf16 v[200:203],a[236:239],v[28:31],v[200:203]
ds_read_b64_tr_b16 a[118:119],v12 offset:1024
ds_read_b64_tr_b16 a[120:121],v12 offset:6160
v_fma_mix_f32 v59,v59,v9,0 op_sel:[0,1,0] op_sel_hi:[0,1,0]
v_mfma_f32_16x16x32_bf16 v[24:27],a[204:207],a[48:51],v[20:23]
ds_read_b64_tr_b16 a[122:123],v12 offset:7184
ds_read_b64_tr_b16 a[216:217],v1 offset:6160
v_lshlrev_b32_sdwa v36,1,v36 dst_sel:DWORD dst_unused:UNUSED_PAD src0_sel:DWORD src1_sel:WORD_1
v_mfma_f32_16x16x32_bf16 v[24:27],a[208:211],a[64:67],v[24:27]
s_waitcnt lgkmcnt(14)
ds_read_u16 v36,v36
ds_read_b64_tr_b16 a[218:219],v1 offset:7184
v_lshl_add_u64 v[60:61],v[60:61],0,v[62:63]
v_mfma_f32_16x16x32_bf16 v[24:27],a[212:215],a[80:83],v[24:27]
ds_read_b64_tr_b16 a[124:125],v12 offset:8208
v_lshl_add_u64 v[62:63],v[32:33],0,v[34:35]
v_lshlrev_b32_sdwa v38,1,v38 dst_sel:DWORD dst_unused:UNUSED_PAD src0_sel:DWORD src1_sel:WORD_1
v_mfma_f32_16x16x32_bf16 v[28:31],a[200:203],a[80:83],v[16:19]
ds_read_u16_d16_hi v38,v38
v_lshlrev_b32_sdwa v37,1,v37 dst_sel:DWORD dst_unused:UNUSED_PAD src0_sel:DWORD src1_sel:WORD_1
v_mfma_f32_16x16x32_bf16 v[28:31],a[196:199],a[64:67],v[28:31]
s_waitcnt lgkmcnt(13)
ds_read_u16 v37,v37
v_lshl_add_u64 v[32:33],v[52:53],0,v[54:55]
v_mfma_f32_16x16x32_bf16 v[28:31],a[192:195],a[48:51],v[28:31]
ds_read_b64_tr_b16 a[126:127],v12 offset:9232
v_lshlrev_b32_sdwa v39,1,v39 dst_sel:DWORD dst_unused:UNUSED_PAD src0_sel:DWORD src1_sel:WORD_1
v_mfma_f32_16x16x32_bf16 v[52:55],a[192:195],a[52:55],v[16:19]
s_waitcnt lgkmcnt(10)
ds_read_u16_d16_hi v39,v39
v_or_b32 v34,v56,v58
v_lshlrev_b32_sdwa v40,1,v40 dst_sel:DWORD dst_unused:UNUSED_PAD src0_sel:DWORD src1_sel:WORD_1
v_mfma_f32_16x16x32_bf16 v[52:55],a[196:199],a[68:71],v[52:55]
ds_read_u16 v40,v40
ds_read_b64_tr_b16 a[220:221],v1 offset:8208
v_cvt_pk_bf16_f32 v35,v57,v59
v_lshlrev_b32_sdwa v42,1,v42 dst_sel:DWORD dst_unused:UNUSED_PAD src0_sel:DWORD src1_sel:WORD_1
v_mfma_f32_16x16x32_bf16 v[52:55],a[200:203],a[84:87],v[52:55]
ds_read_u16_d16_hi v42,v42
v_lshlrev_b32_sdwa v41,1,v41 dst_sel:DWORD dst_unused:UNUSED_PAD src0_sel:DWORD src1_sel:WORD_1
v_mfma_f32_16x16x32_bf16 v[56:59],a[212:215],a[84:87],v[20:23]
ds_read_u16 v41,v41
v_lshlrev_b32_sdwa v43,1,v43 dst_sel:DWORD dst_unused:UNUSED_PAD src0_sel:DWORD src1_sel:WORD_1
v_mfma_f32_16x16x32_bf16 v[56:59],a[208:211],a[68:71],v[56:59]
ds_read_u16_d16_hi v43,v43
v_lshlrev_b32_sdwa v44,1,v44 dst_sel:DWORD dst_unused:UNUSED_PAD src0_sel:DWORD src1_sel:WORD_1
v_mfma_f32_16x16x32_bf16 v[56:59],a[204:207],a[52:55],v[56:59]
ds_read_u16 v44,v44
v_lshlrev_b32_sdwa v46,1,v46 dst_sel:DWORD dst_unused:UNUSED_PAD src0_sel:DWORD src1_sel:WORD_1
v_mfma_f32_16x16x32_bf16 v[136:139],a[244:247],v[60:63],v[136:139]
ds_read_u16_d16_hi v46,v46
v_lshlrev_b32_sdwa v45,1,v45 dst_sel:DWORD dst_unused:UNUSED_PAD src0_sel:DWORD src1_sel:WORD_1
v_mfma_f32_16x16x32_bf16 v[140:143],a[244:247],v[32:35],v[140:143]
ds_read_u16 v45,v45
v_lshlrev_b32_sdwa v47,1,v47 dst_sel:DWORD dst_unused:UNUSED_PAD src0_sel:DWORD src1_sel:WORD_1
v_mfma_f32_16x16x32_bf16 v[68:71],a[252:255],v[32:35],v[68:71]
ds_read_u16_d16_hi v47,v47
v_lshlrev_b32_sdwa v48,1,v48 dst_sel:DWORD dst_unused:UNUSED_PAD src0_sel:DWORD src1_sel:WORD_1
v_mfma_f32_16x16x32_bf16 v[64:67],a[252:255],v[60:63],v[64:67]
ds_read_u16 v48,v48
ds_read_b64_tr_b16 a[222:223],v1 offset:9232
v_mfma_f32_16x16x32_bf16 v[144:147],a[232:235],v[60:63],v[144:147]
ds_read_b64_tr_b16 a[128:129],v12 offset:10256
v_lshlrev_b32_sdwa v50,1,v50 dst_sel:DWORD dst_unused:UNUSED_PAD src0_sel:DWORD src1_sel:WORD_1
v_mfma_f32_16x16x32_bf16 v[148:151],a[232:235],v[32:35],v[148:151]
ds_read_u16_d16_hi v50,v50
v_lshlrev_b32_sdwa v49,1,v49 dst_sel:DWORD dst_unused:UNUSED_PAD src0_sel:DWORD src1_sel:WORD_1
v_mfma_f32_16x16x32_bf16 v[84:87],a[240:243],v[32:35],v[84:87]
ds_read_u16 v49,v49
v_lshlrev_b32_sdwa v51,1,v51 dst_sel:DWORD dst_unused:UNUSED_PAD src0_sel:DWORD src1_sel:WORD_1
v_mfma_f32_16x16x32_bf16 v[80:83],a[240:243],v[60:63],v[80:83]
ds_read_u16_d16_hi v51,v51
v_mfma_f32_16x16x32_bf16 v[152:155],a[224:227],v[60:63],v[152:155]
ds_read_b64_tr_b16 a[130:131],v12 offset:11280
v_mfma_f32_16x16x32_bf16 v[156:159],a[224:227],v[32:35],v[156:159]
ds_read_b64_tr_b16 a[132:133],v11 offset:10256
v_mfma_f32_16x16x32_bf16 v[100:103],a[248:251],v[32:35],v[100:103]
ds_read_b64_tr_b16 a[134:135],v11 offset:11280
v_mfma_f32_16x16x32_bf16 v[96:99],a[248:251],v[60:63],v[96:99]
ds_read_b64_tr_b16 a[136:137],v11 offset:8208
v_mfma_f32_16x16x32_bf16 v[32:35],a[204:207],a[56:59],v[20:23]
s_nop 0
ds_read_b64_tr_b16 a[138:139],v11 offset:9232
v_lshlrev_b32_sdwa v24,1,v24 dst_sel:DWORD dst_unused:UNUSED_PAD src0_sel:DWORD src1_sel:WORD_1
v_mfma_f32_16x16x32_bf16 v[32:35],a[208:211],a[72:75],v[32:35]
s_waitcnt lgkmcnt(8)
ds_read_u16 v24,v24
v_lshl_add_u64 v[36:37],v[36:37],0,v[38:39]
v_fma_mixlo_f16 v8,v53,s99,0
v_mfma_f32_16x16x32_bf16 v[32:35],a[212:215],a[88:91],v[32:35]
ds_read_b64_tr_b16 a[140:141],v11 offset:6160
v_lshl_add_u64 v[38:39],v[40:41],0,v[42:43]
v_lshlrev_b32_sdwa v26,1,v26 dst_sel:DWORD dst_unused:UNUSED_PAD src0_sel:DWORD src1_sel:WORD_1
v_mfma_f32_16x16x32_bf16 v[60:63],a[200:203],a[88:91],v[16:19]
ds_read_u16_d16_hi v26,v26
v_fma_mixhi_f16 v8,v55,s99,0
v_mfma_f32_16x16x32_bf16 v[60:63],a[196:199],a[72:75],v[60:63]
ds_read_b64_tr_b16 a[142:143],v11 offset:7184
v_lshl_add_u64 v[40:41],v[44:45],0,v[46:47]
v_lshlrev_b32_sdwa v25,1,v25 dst_sel:DWORD dst_unused:UNUSED_PAD src0_sel:DWORD src1_sel:WORD_1
v_pk_fma_f16 v9,v8,v8,s54
v_mfma_f32_16x16x32_bf16 v[60:63],a[192:195],a[56:59],v[60:63]
ds_read_u16 v25,v25
v_lshlrev_b32_sdwa v27,1,v27 dst_sel:DWORD dst_unused:UNUSED_PAD src0_sel:DWORD src1_sel:WORD_1
v_mfma_f32_16x16x32_bf16 v[44:47],a[192:195],a[60:63],v[16:19]
s_nop 0
ds_read_b128 v[16:19],v4 offset:1280
v_pk_mul_f16 v9,v8,v9
v_mfma_f32_16x16x32_bf16 v[44:47],a[196:199],a[76:79],v[44:47]
s_waitcnt lgkmcnt(8)
ds_read_u16_d16_hi v27,v27
ds_read_b64_tr_b16 a[192:193],v1 offset:10256
v_lshl_add_u64 v[42:43],v[48:49],0,v[50:51]
v_lshlrev_b32_sdwa v28,1,v28 dst_sel:DWORD dst_unused:UNUSED_PAD src0_sel:DWORD src1_sel:WORD_1
v_mfma_f32_16x16x32_bf16 v[44:47],a[200:203],a[92:95],v[44:47]
ds_read_b64_tr_b16 a[194:195],v1 offset:11280
v_exp_f16 v8,v9
v_mfma_f32_16x16x32_bf16 v[48:51],a[212:215],a[92:95],v[20:23]
ds_read_b128 v[20:23],v4 offset:1312
ds_read_u16 v28,v28
v_lshlrev_b32_sdwa v30,1,v30 dst_sel:DWORD dst_unused:UNUSED_PAD src0_sel:DWORD src1_sel:WORD_1
v_mfma_f32_16x16x32_bf16 v[48:51],a[208:211],a[76:79],v[48:51]
ds_read_u16_d16_hi v30,v30
ds_read_b64_tr_b16 a[196:197],v2 offset:10256
v_lshlrev_b32_sdwa v29,1,v29 dst_sel:DWORD dst_unused:UNUSED_PAD src0_sel:DWORD src1_sel:WORD_1
v_mfma_f32_16x16x32_bf16 v[48:51],a[204:207],a[60:63],v[48:51]
ds_read_u16 v29,v29
v_exp_f16_sdwa v8,v9 dst_sel:WORD_1 dst_unused:UNUSED_PRESERVE src0_sel:WORD_1
v_mfma_f32_16x16x32_bf16 v[112:115],a[244:247],v[36:39],v[112:115]
ds_read_b64_tr_b16 a[198:199],v2 offset:11280
ds_read_b64_tr_b16 a[200:201],v2 offset:8208
v_lshlrev_b32_sdwa v31,1,v31 dst_sel:DWORD dst_unused:UNUSED_PAD src0_sel:DWORD src1_sel:WORD_1
v_mfma_f32_16x16x32_bf16 v[116:119],a[244:247],v[40:43],v[116:119]
ds_read_u16_d16_hi v31,v31
v_lshlrev_b32_sdwa v56,1,v56 dst_sel:DWORD dst_unused:UNUSED_PAD src0_sel:DWORD src1_sel:WORD_1
v_mfma_f32_16x16x32_bf16 v[76:79],a[252:255],v[40:43],v[76:79]
ds_read_b64_tr_b16 a[202:203],v2 offset:9232
v_pk_fma_f16 v8,v8,s55,s55
v_mfma_f32_16x16x32_bf16 v[72:75],a[252:255],v[36:39],v[72:75]
ds_read_u16 v56,v56
v_rcp_f16 v9,v8
v_lshlrev_b32_sdwa v58,1,v58 dst_sel:DWORD dst_unused:UNUSED_PAD src0_sel:DWORD src1_sel:WORD_1
v_mfma_f32_16x16x32_bf16 v[120:123],a[232:235],v[36:39],v[120:123]
ds_read_u16_d16_hi v58,v58
ds_read_b64_tr_b16 a[212:213],v2 offset:6160
v_mfma_f32_16x16x32_bf16 v[124:127],a[232:235],v[40:43],v[124:127]
ds_read_b64_tr_b16 a[214:215],v2 offset:7184
v_lshlrev_b32_sdwa v57,1,v57 dst_sel:DWORD dst_unused:UNUSED_PAD src0_sel:DWORD src1_sel:WORD_1
v_mfma_f32_16x16x32_bf16 v[92:95],a[240:243],v[40:43],v[92:95]
ds_read_u16 v57,v57
v_lshlrev_b32_sdwa v59,1,v59 dst_sel:DWORD dst_unused:UNUSED_PAD src0_sel:DWORD src1_sel:WORD_1
v_mfma_f32_16x16x32_bf16 v[88:91],a[240:243],v[36:39],v[88:91]
v_rcp_f16_sdwa v9,v8 dst_sel:WORD_1 dst_unused:UNUSED_PRESERVE src0_sel:WORD_1
v_mfma_f32_16x16x32_bf16 v[128:131],a[224:227],v[36:39],v[128:131]
ds_read_u16_d16_hi v59,v59
v_lshlrev_b32_sdwa v52,1,v52 dst_sel:DWORD dst_unused:UNUSED_PAD src0_sel:DWORD src1_sel:WORD_1
v_mfma_f32_16x16x32_bf16 v[132:135],a[224:227],v[40:43],v[132:135]
ds_read_u16 v52,v52
v_lshlrev_b32_sdwa v54,1,v54 dst_sel:DWORD dst_unused:UNUSED_PAD src0_sel:DWORD src1_sel:WORD_1
v_mfma_f32_16x16x32_bf16 v[108:111],a[248:251],v[40:43],v[108:111]
ds_read_u16_d16_hi v54,v54
v_fma_mix_f32 v53,v53,v9,0 op_sel_hi:[0,1,0]
v_mfma_f32_16x16x32_bf16 v[104:107],a[248:251],v[36:39],v[104:107]
s_waitcnt lgkmcnt(8)
ds_read_b64_tr_b16 a[144:145],v11 offset:12320
v_fma_mix_f32 v55,v55,v9,0 op_sel:[0,1,0] op_sel_hi:[0,1,0]
v_mfma_f32_16x16x32_bf16 v[40:43],a[216:219],a[0:3],v[16:19]
ds_read_b64_tr_b16 a[146:147],v11 offset:13344
v_lshlrev_b32_sdwa v32,1,v32 dst_sel:DWORD dst_unused:UNUSED_PAD src0_sel:DWORD src1_sel:WORD_1
v_mfma_f32_16x16x32_bf16 v[40:43],a[220:223],a[16:19],v[40:43]
ds_read_u16 v32,v32
v_lshl_add_u64 v[24:25],v[24:25],0,v[26:27]
v_mfma_f32_16x16x32_bf16 v[40:43],a[192:195],a[32:35],v[40:43]
ds_read_b64_tr_b16 a[148:149],v11 offset:14368
v_lshl_add_u64 v[26:27],v[28:29],0,v[30:31]
v_lshlrev_b32_sdwa v34,1,v34 dst_sel:DWORD dst_unused:UNUSED_PAD src0_sel:DWORD src1_sel:WORD_1
v_mfma_f32_16x16x32_bf16 v[36:39],a[196:199],a[32:35],v[20:23]
ds_read_u16_d16_hi v34,v34
v_lshlrev_b32_sdwa v33,1,v33 dst_sel:DWORD dst_unused:UNUSED_PAD src0_sel:DWORD src1_sel:WORD_1
v_mfma_f32_16x16x32_bf16 v[36:39],a[200:203],a[16:19],v[36:39]
s_waitcnt lgkmcnt(7)
ds_read_u16 v33,v33
v_lshl_add_u64 v[28:29],v[56:57],0,v[58:59]
v_mfma_f32_16x16x32_bf16 v[36:39],a[212:215],a[0:3],v[36:39]
ds_read_b64_tr_b16 a[150:151],v11 offset:15392
v_lshlrev_b32_sdwa v35,1,v35 dst_sel:DWORD dst_unused:UNUSED_PAD src0_sel:DWORD src1_sel:WORD_1
v_mfma_f32_16x16x32_bf16 v[56:59],a[212:215],a[4:7],v[20:23]
s_waitcnt lgkmcnt(7)
ds_read_u16_d16_hi v35,v35
v_or_b32 v30,v52,v54
v_lshlrev_b32_sdwa v60,1,v60 dst_sel:DWORD dst_unused:UNUSED_PAD src0_sel:DWORD src1_sel:WORD_1
v_mfma_f32_16x16x32_bf16 v[56:59],a[200:203],a[20:23],v[56:59]
ds_read_u16 v60,v60
v_cvt_pk_bf16_f32 v31,v53,v55
v_lshlrev_b32_sdwa v62,1,v62 dst_sel:DWORD dst_unused:UNUSED_PAD src0_sel:DWORD src1_sel:WORD_1
v_mfma_f32_16x16x32_bf16 v[56:59],a[196:199],a[36:39],v[56:59]
ds_read_u16_d16_hi v62,v62
v_lshlrev_b32_sdwa v61,1,v61 dst_sel:DWORD dst_unused:UNUSED_PAD src0_sel:DWORD src1_sel:WORD_1
v_mfma_f32_16x16x32_bf16 v[52:55],a[192:195],a[36:39],v[16:19]
ds_read_u16 v61,v61
v_lshlrev_b32_sdwa v63,1,v63 dst_sel:DWORD dst_unused:UNUSED_PAD src0_sel:DWORD src1_sel:WORD_1
v_mfma_f32_16x16x32_bf16 v[52:55],a[220:223],a[20:23],v[52:55]
ds_read_u16_d16_hi v63,v63
v_lshlrev_b32_sdwa v48,1,v48 dst_sel:DWORD dst_unused:UNUSED_PAD src0_sel:DWORD src1_sel:WORD_1
v_mfma_f32_16x16x32_bf16 v[52:55],a[216:219],a[4:7],v[52:55]
ds_read_u16 v48,v48
v_lshlrev_b32_sdwa v50,1,v50 dst_sel:DWORD dst_unused:UNUSED_PAD src0_sel:DWORD src1_sel:WORD_1
v_mfma_f32_16x16x32_bf16 v[232:235],a[244:247],v[24:27],v[232:235]
ds_read_u16_d16_hi v50,v50
ds_read_b64_tr_b16 a[152:153],v11 offset:16416
v_mfma_f32_16x16x32_bf16 v[236:239],a[244:247],v[28:31],v[236:239]
ds_read_b64_tr_b16 a[154:155],v11 offset:17440
v_lshlrev_b32_sdwa v49,1,v49 dst_sel:DWORD dst_unused:UNUSED_PAD src0_sel:DWORD src1_sel:WORD_1
v_mfma_f32_16x16x32_bf16 v[164:167],a[252:255],v[28:31],v[164:167]
ds_read_u16 v49,v49
ds_read_b64_tr_b16 a[228:229],v3 offset:12320
v_mfma_f32_16x16x32_bf16 v[160:163],a[252:255],v[24:27],v[160:163]
ds_read_b64_tr_b16 a[230:231],v3 offset:13856
ds_read_b64_tr_b16 a[236:237],v3 offset:12328
v_mfma_f32_16x16x32_bf16 v[240:243],a[232:235],v[24:27],v[240:243]
ds_read_b64_tr_b16 a[238:239],v3 offset:13864
v_lshlrev_b32_sdwa v51,1,v51 dst_sel:DWORD dst_unused:UNUSED_PAD src0_sel:DWORD src1_sel:WORD_1
v_mfma_f32_16x16x32_bf16 v[244:247],a[232:235],v[28:31],v[244:247]
ds_read_u16_d16_hi v51,v51
v_lshlrev_b32_sdwa v44,1,v44 dst_sel:DWORD dst_unused:UNUSED_PAD src0_sel:DWORD src1_sel:WORD_1
v_mfma_f32_16x16x32_bf16 v[180:183],a[240:243],v[28:31],v[180:183]
ds_read_u16 v44,v44
v_lshlrev_b32_sdwa v46,1,v46 dst_sel:DWORD dst_unused:UNUSED_PAD src0_sel:DWORD src1_sel:WORD_1
v_mfma_f32_16x16x32_bf16 v[176:179],a[240:243],v[24:27],v[176:179]
ds_read_u16_d16_hi v46,v46
v_lshlrev_b32_sdwa v45,1,v45 dst_sel:DWORD dst_unused:UNUSED_PAD src0_sel:DWORD src1_sel:WORD_1
v_mfma_f32_16x16x32_bf16 v[248:251],a[224:227],v[24:27],v[248:251]
ds_read_u16 v45,v45
v_lshlrev_b32_sdwa v47,1,v47 dst_sel:DWORD dst_unused:UNUSED_PAD src0_sel:DWORD src1_sel:WORD_1
v_mfma_f32_16x16x32_bf16 v[252:255],a[224:227],v[28:31],v[252:255]
ds_read_u16_d16_hi v47,v47
v_mfma_f32_16x16x32_bf16 v[196:199],a[248:251],v[28:31],v[196:199]
ds_read_b64_tr_b16 a[156:157],v12 offset:16416
v_mfma_f32_16x16x32_bf16 v[192:195],a[248:251],v[24:27],v[192:195]
ds_read_b64_tr_b16 a[158:159],v12 offset:17440
v_mfma_f32_16x16x32_bf16 v[28:31],a[216:219],a[8:11],v[16:19]
ds_read_b64_tr_b16 a[160:161],v12 offset:14368
v_lshlrev_b32_sdwa v40,1,v40 dst_sel:DWORD dst_unused:UNUSED_PAD src0_sel:DWORD src1_sel:WORD_1
v_mfma_f32_16x16x32_bf16 v[28:31],a[220:223],a[24:27],v[28:31]
s_waitcnt lgkmcnt(8)
ds_read_u16 v40,v40
v_lshl_add_u64 v[32:33],v[32:33],0,v[34:35]
v_fma_mixlo_f16 v8,v57,s99,0
v_mfma_f32_16x16x32_bf16 v[28:31],a[192:195],a[40:43],v[28:31]
ds_read_b64_tr_b16 a[162:163],v12 offset:15392
v_lshl_add_u64 v[34:35],v[60:61],0,v[62:63]
v_lshlrev_b32_sdwa v42,1,v42 dst_sel:DWORD dst_unused:UNUSED_PAD src0_sel:DWORD src1_sel:WORD_1
v_mfma_f32_16x16x32_bf16 v[24:27],a[196:199],a[40:43],v[20:23]
ds_read_u16_d16_hi v42,v42
v_fma_mixhi_f16 v8,v59,s99,0
v_mfma_f32_16x16x32_bf16 v[24:27],a[200:203],a[24:27],v[24:27]
s_waitcnt lgkmcnt(8)
ds_read_b64_tr_b16 a[164:165],v12 offset:12320
v_lshl_add_u64 v[60:61],v[48:49],0,v[50:51]
v_lshlrev_b32_sdwa v41,1,v41 dst_sel:DWORD dst_unused:UNUSED_PAD src0_sel:DWORD src1_sel:WORD_1
v_pk_fma_f16 v9,v8,v8,s54
v_mfma_f32_16x16x32_bf16 v[24:27],a[212:215],a[8:11],v[24:27]
ds_read_u16 v41,v41
v_lshlrev_b32_sdwa v43,1,v43 dst_sel:DWORD dst_unused:UNUSED_PAD src0_sel:DWORD src1_sel:WORD_1
v_mfma_f32_16x16x32_bf16 v[48:51],a[212:215],a[12:15],v[20:23]
ds_read_u16_d16_hi v43,v43
v_pk_mul_f16 v9,v8,v9
v_mfma_f32_16x16x32_bf16 v[48:51],a[200:203],a[28:31],v[48:51]
s_waitcnt lgkmcnt(8)
ds_read_b64_tr_b16 a[166:167],v12 offset:13344
v_lshl_add_u64 v[62:63],v[44:45],0,v[46:47]
v_lshlrev_b32_sdwa v36,1,v36 dst_sel:DWORD dst_unused:UNUSED_PAD src0_sel:DWORD src1_sel:WORD_1
v_mfma_f32_16x16x32_bf16 v[48:51],a[196:199],a[44:47],v[48:51]
v_exp_f16 v8,v9
v_mfma_f32_16x16x32_bf16 v[44:47],a[192:195],a[44:47],v[16:19]
ds_read_u16 v36,v36
v_lshlrev_b32_sdwa v38,1,v38 dst_sel:DWORD dst_unused:UNUSED_PAD src0_sel:DWORD src1_sel:WORD_1
v_mfma_f32_16x16x32_bf16 v[44:47],a[220:223],a[28:31],v[44:47]
ds_read_u16_d16_hi v38,v38
v_lshlrev_b32_sdwa v37,1,v37 dst_sel:DWORD dst_unused:UNUSED_PAD src0_sel:DWORD src1_sel:WORD_1
v_mfma_f32_16x16x32_bf16 v[44:47],a[216:219],a[12:15],v[44:47]
v_exp_f16_sdwa v8,v9 dst_sel:WORD_1 dst_unused:UNUSED_PRESERVE src0_sel:WORD_1
v_mfma_f32_16x16x32_bf16 v[208:211],a[244:247],v[32:35],v[208:211]
ds_read_u16 v37,v37
v_lshlrev_b32_sdwa v39,1,v39 dst_sel:DWORD dst_unused:UNUSED_PAD src0_sel:DWORD src1_sel:WORD_1
v_mfma_f32_16x16x32_bf16 v[212:215],a[244:247],v[60:63],v[212:215]
ds_read_u16_d16_hi v39,v39
v_lshlrev_b32_sdwa v52,1,v52 dst_sel:DWORD dst_unused:UNUSED_PAD src0_sel:DWORD src1_sel:WORD_1
v_mfma_f32_16x16x32_bf16 v[172:175],a[252:255],v[60:63],v[172:175]
ds_read_u16 v52,v52
v_pk_fma_f16 v8,v8,s55,s55
v_mfma_f32_16x16x32_bf16 v[168:171],a[252:255],v[32:35],v[168:171]
ds_read_b64_tr_b16 a[244:245],v3 offset:12384
v_rcp_f16_e64 v9,v8
v_lshlrev_b32_sdwa v54,1,v54 dst_sel:DWORD dst_unused:UNUSED_PAD src0_sel:DWORD src1_sel:WORD_1
v_mfma_f32_16x16x32_bf16 v[216:219],a[232:235],v[32:35],v[216:219]
ds_read_u16_d16_hi v54,v54
ds_read_b64_tr_b16 a[246:247],v3 offset:13920
v_mfma_f32_16x16x32_bf16 v[220:223],a[232:235],v[60:63],v[220:223]
ds_read_b64_tr_b16 a[252:253],v3 offset:12392
ds_read_b64_tr_b16 a[254:255],v3 offset:13928
v_lshlrev_b32_sdwa v53,1,v53 dst_sel:DWORD dst_unused:UNUSED_PAD src0_sel:DWORD src1_sel:WORD_1
v_mfma_f32_16x16x32_bf16 v[188:191],a[240:243],v[60:63],v[188:191]
ds_read_u16 v53,v53
ds_read_b64_tr_b16 a[232:233],v3 offset:12448
v_lshlrev_b32_sdwa v55,1,v55 dst_sel:DWORD dst_unused:UNUSED_PAD src0_sel:DWORD src1_sel:WORD_1
v_mfma_f32_16x16x32_bf16 v[184:187],a[240:243],v[32:35],v[184:187]
ds_read_u16_d16_hi v55,v55
v_rcp_f16_sdwa v9,v8 dst_sel:WORD_1 dst_unused:UNUSED_PRESERVE src0_sel:WORD_1
v_mfma_f32_16x16x32_bf16 v[224:227],a[224:227],v[32:35],v[224:227]
ds_read_b64_tr_b16 a[234:235],v3 offset:13984
ds_read_b64_tr_b16 a[240:241],v3 offset:12456
v_lshlrev_b32_sdwa v56,1,v56 dst_sel:DWORD dst_unused:UNUSED_PAD src0_sel:DWORD src1_sel:WORD_1
v_mfma_f32_16x16x32_bf16 v[228:231],a[224:227],v[60:63],v[228:231]
ds_read_u16 v56,v56
v_lshlrev_b32_sdwa v58,1,v58 dst_sel:DWORD dst_unused:UNUSED_PAD src0_sel:DWORD src1_sel:WORD_1
v_mfma_f32_16x16x32_bf16 v[204:207],a[248:251],v[60:63],v[204:207]
ds_read_u16_d16_hi v58,v58
v_fma_mix_f32 v57,v57,v9,0 op_sel_hi:[0,1,0]
v_mfma_f32_16x16x32_bf16 v[200:203],a[248:251],v[32:35],v[200:203]
ds_read_b64_tr_b16 a[242:243],v3 offset:13992
ds_read_b64_tr_b16 a[168:169],v12 offset:18480
v_fma_mix_f32 v59,v59,v9,0 op_sel:[0,1,0] op_sel_hi:[0,1,0]
v_mfma_f32_16x16x32_bf16 v[60:63],a[216:219],a[48:51],v[16:19]
ds_read_b64_tr_b16 a[170:171],v12 offset:19504
ds_read_b64_tr_b16 a[172:173],v12 offset:20528
v_lshlrev_b32_sdwa v28,1,v28 dst_sel:DWORD dst_unused:UNUSED_PAD src0_sel:DWORD src1_sel:WORD_1
v_mfma_f32_16x16x32_bf16 v[60:63],a[220:223],a[64:67],v[60:63]
s_waitcnt lgkmcnt(13)
ds_read_u16 v28,v28
ds_read_b64_tr_b16 a[174:175],v12 offset:21552
v_lshl_add_u64 v[40:41],v[40:41],0,v[42:43]
v_mfma_f32_16x16x32_bf16 v[60:63],a[192:195],a[80:83],v[60:63]
ds_read_b64_tr_b16 a[208:209],v1 offset:12320
v_lshl_add_u64 v[42:43],v[36:37],0,v[38:39]
v_lshlrev_b32_sdwa v30,1,v30 dst_sel:DWORD dst_unused:UNUSED_PAD src0_sel:DWORD src1_sel:WORD_1
v_mfma_f32_16x16x32_bf16 v[32:35],a[196:199],a[80:83],v[20:23]
ds_read_u16_d16_hi v30,v30
v_lshlrev_b32_sdwa v29,1,v29 dst_sel:DWORD dst_unused:UNUSED_PAD src0_sel:DWORD src1_sel:WORD_1
v_mfma_f32_16x16x32_bf16 v[32:35],a[200:203],a[64:67],v[32:35]
s_waitcnt lgkmcnt(12)
ds_read_u16 v29,v29
v_lshl_add_u64 v[36:37],v[52:53],0,v[54:55]
v_mfma_f32_16x16x32_bf16 v[32:35],a[212:215],a[48:51],v[32:35]
ds_read_b64_tr_b16 a[210:211],v1 offset:13344
v_lshlrev_b32_sdwa v31,1,v31 dst_sel:DWORD dst_unused:UNUSED_PAD src0_sel:DWORD src1_sel:WORD_1
v_mfma_f32_16x16x32_bf16 v[52:55],a[212:215],a[52:55],v[20:23]
s_waitcnt lgkmcnt(10)
ds_read_u16_d16_hi v31,v31
v_or_b32 v38,v56,v58
v_lshlrev_b32_sdwa v24,1,v24 dst_sel:DWORD dst_unused:UNUSED_PAD src0_sel:DWORD src1_sel:WORD_1
v_mfma_f32_16x16x32_bf16 v[52:55],a[200:203],a[68:71],v[52:55]
ds_read_u16 v24,v24
ds_read_b64_tr_b16 a[204:205],v1 offset:14368
v_cvt_pk_bf16_f32 v39,v57,v59
v_lshlrev_b32_sdwa v26,1,v26 dst_sel:DWORD dst_unused:UNUSED_PAD src0_sel:DWORD src1_sel:WORD_1
v_mfma_f32_16x16x32_bf16 v[52:55],a[196:199],a[84:87],v[52:55]
ds_read_u16_d16_hi v26,v26
v_lshlrev_b32_sdwa v25,1,v25 dst_sel:DWORD dst_unused:UNUSED_PAD src0_sel:DWORD src1_sel:WORD_1
v_mfma_f32_16x16x32_bf16 v[56:59],a[192:195],a[84:87],v[16:19]
ds_read_u16 v25,v25
v_lshlrev_b32_sdwa v27,1,v27 dst_sel:DWORD dst_unused:UNUSED_PAD src0_sel:DWORD src1_sel:WORD_1
v_mfma_f32_16x16x32_bf16 v[56:59],a[220:223],a[68:71],v[56:59]
ds_read_u16_d16_hi v27,v27
v_lshlrev_b32_sdwa v44,1,v44 dst_sel:DWORD dst_unused:UNUSED_PAD src0_sel:DWORD src1_sel:WORD_1
v_mfma_f32_16x16x32_bf16 v[56:59],a[216:219],a[52:55],v[56:59]
ds_read_u16 v44,v44
v_lshlrev_b32_sdwa v46,1,v46 dst_sel:DWORD dst_unused:UNUSED_PAD src0_sel:DWORD src1_sel:WORD_1
v_mfma_f32_16x16x32_bf16 v[136:139],a[228:231],v[40:43],v[136:139]
ds_read_u16_d16_hi v46,v46
v_lshlrev_b32_sdwa v45,1,v45 dst_sel:DWORD dst_unused:UNUSED_PAD src0_sel:DWORD src1_sel:WORD_1
v_mfma_f32_16x16x32_bf16 v[140:143],a[228:231],v[36:39],v[140:143]
ds_read_u16 v45,v45
v_lshlrev_b32_sdwa v47,1,v47 dst_sel:DWORD dst_unused:UNUSED_PAD src0_sel:DWORD src1_sel:WORD_1
v_mfma_f32_16x16x32_bf16 v[68:71],a[236:239],v[36:39],v[68:71]
ds_read_u16_d16_hi v47,v47
v_lshlrev_b32_sdwa v48,1,v48 dst_sel:DWORD dst_unused:UNUSED_PAD src0_sel:DWORD src1_sel:WORD_1
v_mfma_f32_16x16x32_bf16 v[64:67],a[236:239],v[40:43],v[64:67]
ds_read_u16 v48,v48
ds_read_b64_tr_b16 a[206:207],v1 offset:15392
v_mfma_f32_16x16x32_bf16 v[144:147],a[244:247],v[40:43],v[144:147]
ds_read_b64_tr_b16 a[176:177],v12 offset:22576
v_lshlrev_b32_sdwa v50,1,v50 dst_sel:DWORD dst_unused:UNUSED_PAD src0_sel:DWORD src1_sel:WORD_1
v_mfma_f32_16x16x32_bf16 v[148:151],a[244:247],v[36:39],v[148:151]
ds_read_u16_d16_hi v50,v50
v_lshlrev_b32_sdwa v49,1,v49 dst_sel:DWORD dst_unused:UNUSED_PAD src0_sel:DWORD src1_sel:WORD_1
v_mfma_f32_16x16x32_bf16 v[84:87],a[252:255],v[36:39],v[84:87]
ds_read_u16 v49,v49
v_lshlrev_b32_sdwa v51,1,v51 dst_sel:DWORD dst_unused:UNUSED_PAD src0_sel:DWORD src1_sel:WORD_1
v_mfma_f32_16x16x32_bf16 v[80:83],a[252:255],v[40:43],v[80:83]
ds_read_u16_d16_hi v51,v51
v_mfma_f32_16x16x32_bf16 v[152:155],a[232:235],v[40:43],v[152:155]
ds_read_b64_tr_b16 a[178:179],v12 offset:23600
v_mfma_f32_16x16x32_bf16 v[156:159],a[232:235],v[36:39],v[156:159]
s_waitcnt lgkmcnt(9)
ds_read_b64_tr_b16 a[180:181],v11 offset:22576
v_mfma_f32_16x16x32_bf16 v[100:103],a[240:243],v[36:39],v[100:103]
ds_read_b64_tr_b16 a[182:183],v11 offset:23600
v_mfma_f32_16x16x32_bf16 v[96:99],a[240:243],v[40:43],v[96:99]
ds_read_b64_tr_b16 a[184:185],v11 offset:20528
v_mfma_f32_16x16x32_bf16 v[36:39],a[216:219],a[56:59],v[16:19]
ds_read_b64_tr_b16 a[186:187],v11 offset:21552
v_lshlrev_b32_sdwa v60,1,v60 dst_sel:DWORD dst_unused:UNUSED_PAD src0_sel:DWORD src1_sel:WORD_1
v_mfma_f32_16x16x32_bf16 v[36:39],a[220:223],a[72:75],v[36:39]
ds_read_u16 v60,v60
v_lshl_add_u64 v[28:29],v[28:29],0,v[30:31]
v_fma_mixlo_f16 v8,v53,s99,0
v_mfma_f32_16x16x32_bf16 v[36:39],a[192:195],a[88:91],v[36:39]
ds_read_b64_tr_b16 a[188:189],v11 offset:18480
v_lshl_add_u64 v[30:31],v[24:25],0,v[26:27]
v_lshlrev_b32_sdwa v62,1,v62 dst_sel:DWORD dst_unused:UNUSED_PAD src0_sel:DWORD src1_sel:WORD_1
v_mfma_f32_16x16x32_bf16 v[40:43],a[196:199],a[88:91],v[20:23]
ds_read_u16_d16_hi v62,v62
v_fma_mixhi_f16 v8,v55,s99,0
v_mfma_f32_16x16x32_bf16 v[40:43],a[200:203],a[72:75],v[40:43]
s_waitcnt lgkmcnt(8)
ds_read_b64_tr_b16 a[190:191],v11 offset:19504
v_lshl_add_u64 v[24:25],v[44:45],0,v[46:47]
v_lshlrev_b32_sdwa v61,1,v61 dst_sel:DWORD dst_unused:UNUSED_PAD src0_sel:DWORD src1_sel:WORD_1
v_pk_fma_f16 v9,v8,v8,s54
v_mfma_f32_16x16x32_bf16 v[40:43],a[212:215],a[56:59],v[40:43]
ds_read_u16 v61,v61
v_lshlrev_b32_sdwa v63,1,v63 dst_sel:DWORD dst_unused:UNUSED_PAD src0_sel:DWORD src1_sel:WORD_1
v_mfma_f32_16x16x32_bf16 v[44:47],a[212:215],a[60:63],v[20:23]
ds_read_b128 v[20:23],v4 offset:1408
v_pk_mul_f16 v9,v8,v9
v_mfma_f32_16x16x32_bf16 v[44:47],a[200:203],a[76:79],v[44:47]
ds_read_u16_d16_hi v63,v63
ds_read_b64_tr_b16 a[212:213],v1 offset:16416
v_lshl_add_u64 v[26:27],v[48:49],0,v[50:51]
v_lshlrev_b32_sdwa v32,1,v32 dst_sel:DWORD dst_unused:UNUSED_PAD src0_sel:DWORD src1_sel:WORD_1
v_mfma_f32_16x16x32_bf16 v[44:47],a[196:199],a[92:95],v[44:47]
ds_read_b64_tr_b16 a[214:215],v1 offset:17440
v_exp_f16_e64 v8,v9
v_mfma_f32_16x16x32_bf16 v[48:51],a[192:195],a[92:95],v[16:19]
ds_read_b128 v[16:19],v4 offset:1440
ds_read_u16 v32,v32
v_lshlrev_b32_sdwa v34,1,v34 dst_sel:DWORD dst_unused:UNUSED_PAD src0_sel:DWORD src1_sel:WORD_1
v_mfma_f32_16x16x32_bf16 v[48:51],a[220:223],a[76:79],v[48:51]
ds_read_u16_d16_hi v34,v34
ds_read_b64_tr_b16 a[200:201],v2 offset:16416
v_lshlrev_b32_sdwa v33,1,v33 dst_sel:DWORD dst_unused:UNUSED_PAD src0_sel:DWORD src1_sel:WORD_1
v_mfma_f32_16x16x32_bf16 v[48:51],a[216:219],a[60:63],v[48:51]
TP9685_%=:
s_cbranch_execz TR1_%=
ds_read_u16 v33,v33
v_exp_f16_sdwa v8,v9 dst_sel:WORD_1 dst_unused:UNUSED_PRESERVE src0_sel:WORD_1
v_mfma_f32_16x16x32_bf16 v[112:115],a[228:231],v[28:31],v[112:115]
ds_read_b64_tr_b16 a[202:203],v2 offset:17440
ds_read_b64_tr_b16 a[196:197],v2 offset:14368
v_lshlrev_b32_sdwa v35,1,v35 dst_sel:DWORD dst_unused:UNUSED_PAD src0_sel:DWORD src1_sel:WORD_1
v_mfma_f32_16x16x32_bf16 v[116:119],a[228:231],v[24:27],v[116:119]
ds_read_u16_d16_hi v35,v35
v_lshlrev_b32_sdwa v56,1,v56 dst_sel:DWORD dst_unused:UNUSED_PAD src0_sel:DWORD src1_sel:WORD_1
v_mfma_f32_16x16x32_bf16 v[76:79],a[236:239],v[24:27],v[76:79]
ds_read_b64_tr_b16 a[198:199],v2 offset:15392
v_pk_fma_f16 v8,v8,s55,s55
v_mfma_f32_16x16x32_bf16 v[72:75],a[236:239],v[28:31],v[72:75]
v_rcp_f16 v9,v8
v_lshlrev_b32_sdwa v58,1,v58 dst_sel:DWORD dst_unused:UNUSED_PAD src0_sel:DWORD src1_sel:WORD_1
v_mfma_f32_16x16x32_bf16 v[120:123],a[244:247],v[28:31],v[120:123]
ds_read_u16 v56,v56
v_lshlrev_b32_sdwa v57,1,v57 dst_sel:DWORD dst_unused:UNUSED_PAD src0_sel:DWORD src1_sel:WORD_1
v_mfma_f32_16x16x32_bf16 v[124:127],a[244:247],v[24:27],v[124:127]
ds_read_u16_d16_hi v58,v58
v_lshlrev_b32_sdwa v59,1,v59 dst_sel:DWORD dst_unused:UNUSED_PAD src0_sel:DWORD src1_sel:WORD_1
v_mfma_f32_16x16x32_bf16 v[92:95],a[252:255],v[24:27],v[92:95]
ds_read_u16 v57,v57
v_lshlrev_b32_sdwa v52,1,v52 dst_sel:DWORD dst_unused:UNUSED_PAD src0_sel:DWORD src1_sel:WORD_1
v_mfma_f32_16x16x32_bf16 v[88:91],a[252:255],v[28:31],v[88:91]
ds_read_u16_d16_hi v59,v59
v_rcp_f16_sdwa v9,v8 dst_sel:WORD_1 dst_unused:UNUSED_PRESERVE src0_sel:WORD_1
v_mfma_f32_16x16x32_bf16 v[128:131],a[232:235],v[28:31],v[128:131]
ds_read_b64_tr_b16 a[192:193],v2 offset:12320
v_lshlrev_b32_sdwa v54,1,v54 dst_sel:DWORD dst_unused:UNUSED_PAD src0_sel:DWORD src1_sel:WORD_1
v_mfma_f32_16x16x32_bf16 v[132:135],a[232:235],v[24:27],v[132:135]
ds_read_b64_tr_b16 a[194:195],v2 offset:13344
v_fma_mix_f32 v53,v53,v9,0 op_sel_hi:[0,1,0]
v_mfma_f32_16x16x32_bf16 v[108:111],a[240:243],v[24:27],v[108:111]
ds_read_u16 v52,v52
v_fma_mix_f32 v55,v55,v9,0 op_sel:[0,1,0] op_sel_hi:[0,1,0]
v_mfma_f32_16x16x32_bf16 v[104:107],a[240:243],v[28:31],v[104:107]
s_waitcnt lgkmcnt(7)
ds_read_u16_d16_hi v54,v54
v_mfma_f32_16x16x32_bf16 v[24:27],a[208:211],a[0:3],v[20:23]
v_lshlrev_b32_sdwa v36,1,v36 dst_sel:DWORD dst_unused:UNUSED_PAD src0_sel:DWORD src1_sel:WORD_1
v_lshlrev_b32_sdwa v38,1,v38 dst_sel:DWORD dst_unused:UNUSED_PAD src0_sel:DWORD src1_sel:WORD_1
v_mfma_f32_16x16x32_bf16 v[24:27],a[204:207],a[16:19],v[24:27]
ds_read_u16 v36,v36
v_lshl_add_u64 v[60:61],v[60:61],0,v[62:63]
v_mfma_f32_16x16x32_bf16 v[24:27],a[212:215],a[32:35],v[24:27]
ds_read_u16_d16_hi v38,v38
v_lshl_add_u64 v[62:63],v[32:33],0,v[34:35]
v_mfma_f32_16x16x32_bf16 v[28:31],a[200:203],a[32:35],v[16:19]
v_lshlrev_b32_sdwa v37,1,v37 dst_sel:DWORD dst_unused:UNUSED_PAD src0_sel:DWORD src1_sel:WORD_1
v_lshlrev_b32_sdwa v39,1,v39 dst_sel:DWORD dst_unused:UNUSED_PAD src0_sel:DWORD src1_sel:WORD_1
v_mfma_f32_16x16x32_bf16 v[28:31],a[196:199],a[16:19],v[28:31]
s_waitcnt lgkmcnt(4)
ds_read_u16 v37,v37
v_lshl_add_u64 v[32:33],v[56:57],0,v[58:59]
v_mfma_f32_16x16x32_bf16 v[28:31],a[192:195],a[0:3],v[28:31]
ds_read_u16_d16_hi v39,v39
v_lshlrev_b32_sdwa v40,1,v40 dst_sel:DWORD dst_unused:UNUSED_PAD src0_sel:DWORD src1_sel:WORD_1
v_mfma_f32_16x16x32_bf16 v[56:59],a[192:195],a[4:7],v[16:19]
s_waitcnt lgkmcnt(4)
ds_read_u16 v40,v40
v_or_b32_e64 v34,v52,v54
v_mfma_f32_16x16x32_bf16 v[56:59],a[196:199],a[20:23],v[56:59]
s_waitcnt vmcnt(0)
v_cvt_pk_bf16_f32 v35,v53,v55
v_lshlrev_b32_sdwa v42,1,v42 dst_sel:DWORD dst_unused:UNUSED_PAD src0_sel:DWORD src1_sel:WORD_1
v_mfma_f32_16x16x32_bf16 v[56:59],a[200:203],a[36:39],v[56:59]
s_barrier
ds_read_u16_d16_hi v42,v42
v_lshlrev_b32_sdwa v41,1,v41 dst_sel:DWORD dst_unused:UNUSED_PAD src0_sel:DWORD src1_sel:WORD_1
v_mfma_f32_16x16x32_bf16 v[52:55],a[212:215],a[36:39],v[20:23]
ds_read_u16 v41,v41
v_lshlrev_b32_sdwa v43,1,v43 dst_sel:DWORD dst_unused:UNUSED_PAD src0_sel:DWORD src1_sel:WORD_1
v_mfma_f32_16x16x32_bf16 v[52:55],a[204:207],a[20:23],v[52:55]
ds_read_u16_d16_hi v43,v43
v_lshlrev_b32_sdwa v48,1,v48 dst_sel:DWORD dst_unused:UNUSED_PAD src0_sel:DWORD src1_sel:WORD_1
v_mfma_f32_16x16x32_bf16 v[52:55],a[208:211],a[4:7],v[52:55]
ds_read_u16 v48,v48
v_lshlrev_b32_sdwa v50,1,v50 dst_sel:DWORD dst_unused:UNUSED_PAD src0_sel:DWORD src1_sel:WORD_1
v_mfma_f32_16x16x32_bf16 v[232:235],a[228:231],v[60:63],v[232:235]
ds_read_u16_d16_hi v50,v50
v_lshlrev_b32_sdwa v49,1,v49 dst_sel:DWORD dst_unused:UNUSED_PAD src0_sel:DWORD src1_sel:WORD_1
v_mfma_f32_16x16x32_bf16 v[236:239],a[228:231],v[32:35],v[236:239]
ds_read_u16 v49,v49
v_lshlrev_b32_sdwa v51,1,v51 dst_sel:DWORD dst_unused:UNUSED_PAD src0_sel:DWORD src1_sel:WORD_1
v_mfma_f32_16x16x32_bf16 v[164:167],a[236:239],v[32:35],v[164:167]
ds_read_u16_d16_hi v51,v51
ds_read_b64_tr_b16 a[224:225],v3 offset:18480
v_mfma_f32_16x16x32_bf16 v[160:163],a[236:239],v[60:63],v[160:163]
ds_read_b64_tr_b16 a[226:227],v3 offset:20016
v_lshlrev_b32_sdwa v44,1,v44 dst_sel:DWORD dst_unused:UNUSED_PAD src0_sel:DWORD src1_sel:WORD_1
v_mfma_f32_16x16x32_bf16 v[240:243],a[244:247],v[60:63],v[240:243]
ds_read_u16 v44,v44
v_lshlrev_b32_sdwa v46,1,v46 dst_sel:DWORD dst_unused:UNUSED_PAD src0_sel:DWORD src1_sel:WORD_1
v_mfma_f32_16x16x32_bf16 v[244:247],a[244:247],v[32:35],v[244:247]
ds_read_u16_d16_hi v46,v46
v_lshlrev_b32_sdwa v45,1,v45 dst_sel:DWORD dst_unused:UNUSED_PAD src0_sel:DWORD src1_sel:WORD_1
v_mfma_f32_16x16x32_bf16 v[180:183],a[252:255],v[32:35],v[180:183]
ds_read_u16 v45,v45
v_lshlrev_b32_sdwa v47,1,v47 dst_sel:DWORD dst_unused:UNUSED_PAD src0_sel:DWORD src1_sel:WORD_1
v_mfma_f32_16x16x32_bf16 v[176:179],a[252:255],v[60:63],v[176:179]
ds_read_u16_d16_hi v47,v47
v_mfma_f32_16x16x32_bf16 v[248:251],a[232:235],v[60:63],v[248:251]
ds_read_b64_tr_b16 a[248:249],v3 offset:18488
v_mfma_f32_16x16x32_bf16 v[252:255],a[232:235],v[32:35],v[252:255]
ds_read_b64_tr_b16 a[250:251],v3 offset:20024
v_mfma_f32_16x16x32_bf16 v[196:199],a[240:243],v[32:35],v[196:199]
v_mfma_f32_16x16x32_bf16 v[192:195],a[240:243],v[60:63],v[192:195]
v_mfma_f32_16x16x32_bf16 v[32:35],a[208:211],a[8:11],v[20:23]
ds_read_b64_tr_b16 a[220:221],v1 offset:18480
v_lshlrev_b32_sdwa v24,1,v24 dst_sel:DWORD dst_unused:UNUSED_PAD src0_sel:DWORD src1_sel:WORD_1
v_mfma_f32_16x16x32_bf16 v[32:35],a[204:207],a[24:27],v[32:35]
s_waitcnt lgkmcnt(6)
ds_read_u16 v24,v24
v_lshl_add_u64 v[36:37],v[36:37],0,v[38:39]
v_fma_mixlo_f16 v8,v57,s99,0
v_mfma_f32_16x16x32_bf16 v[32:35],a[212:215],a[40:43],v[32:35]
ds_read_b64_tr_b16 a[222:223],v1 offset:19504
v_lshl_add_u64 v[38:39],v[40:41],0,v[42:43]
v_lshlrev_b32_sdwa v26,1,v26 dst_sel:DWORD dst_unused:UNUSED_PAD src0_sel:DWORD src1_sel:WORD_1
v_mfma_f32_16x16x32_bf16 v[60:63],a[200:203],a[40:43],v[16:19]
ds_read_u16_d16_hi v26,v26
v_fma_mixhi_f16 v8,v59,s99,0
v_mfma_f32_16x16x32_bf16 v[60:63],a[196:199],a[24:27],v[60:63]
ds_read_b64_tr_b16 a[216:217],v1 offset:20528
v_lshl_add_u64 v[40:41],v[48:49],0,v[50:51]
v_lshlrev_b32_sdwa v25,1,v25 dst_sel:DWORD dst_unused:UNUSED_PAD src0_sel:DWORD src1_sel:WORD_1
v_pk_fma_f16 v9,v8,v8,s54
v_mfma_f32_16x16x32_bf16 v[60:63],a[192:195],a[8:11],v[60:63]
ds_read_u16 v25,v25
v_lshlrev_b32_sdwa v27,1,v27 dst_sel:DWORD dst_unused:UNUSED_PAD src0_sel:DWORD src1_sel:WORD_1
v_mfma_f32_16x16x32_bf16 v[48:51],a[192:195],a[12:15],v[16:19]
ds_read_u16_d16_hi v27,v27
v_pk_mul_f16 v9,v8,v9
v_mfma_f32_16x16x32_bf16 v[48:51],a[196:199],a[28:31],v[48:51]
s_waitcnt lgkmcnt(7)
ds_read_b64_tr_b16 a[218:219],v1 offset:21552
v_lshl_add_u64 v[42:43],v[44:45],0,v[46:47]
v_lshlrev_b32_sdwa v28,1,v28 dst_sel:DWORD dst_unused:UNUSED_PAD src0_sel:DWORD src1_sel:WORD_1
v_mfma_f32_16x16x32_bf16 v[48:51],a[200:203],a[44:47],v[48:51]
v_exp_f16 v8,v9
v_mfma_f32_16x16x32_bf16 v[44:47],a[212:215],a[44:47],v[20:23]
ds_read_u16 v28,v28
v_lshlrev_b32_sdwa v30,1,v30 dst_sel:DWORD dst_unused:UNUSED_PAD src0_sel:DWORD src1_sel:WORD_1
v_mfma_f32_16x16x32_bf16 v[44:47],a[204:207],a[28:31],v[44:47]
ds_read_u16_d16_hi v30,v30
v_lshlrev_b32_sdwa v29,1,v29 dst_sel:DWORD dst_unused:UNUSED_PAD src0_sel:DWORD src1_sel:WORD_1
v_mfma_f32_16x16x32_bf16 v[44:47],a[208:211],a[12:15],v[44:47]
ds_read_b64_tr_b16 a[0:1],v3 offset:38992
ds_read_b64_tr_b16 a[2:3],v3 offset:40528
v_exp_f16_sdwa v8,v9 dst_sel:WORD_1 dst_unused:UNUSED_PRESERVE src0_sel:WORD_1
v_mfma_f32_16x16x32_bf16 v[208:211],a[228:231],v[36:39],v[208:211]
ds_read_b64_tr_b16 a[4:5],v3 offset:45152
ds_read_b64_tr_b16 a[6:7],v3 offset:46688
v_lshlrev_b32_sdwa v31,1,v31 dst_sel:DWORD dst_unused:UNUSED_PAD src0_sel:DWORD src1_sel:WORD_1
v_mfma_f32_16x16x32_bf16 v[212:215],a[228:231],v[40:43],v[212:215]
ds_read_u16 v29,v29
ds_read_u16_d16_hi v31,v31
v_lshlrev_b32_sdwa v52,1,v52 dst_sel:DWORD dst_unused:UNUSED_PAD src0_sel:DWORD src1_sel:WORD_1
v_mfma_f32_16x16x32_bf16 v[172:175],a[236:239],v[40:43],v[172:175]
ds_read_u16 v52,v52
ds_read_b64_tr_b16 a[8:9],v3 offset:45160
v_pk_fma_f16 v8,v8,s55,s55
v_mfma_f32_16x16x32_bf16 v[168:171],a[236:239],v[36:39],v[168:171]
ds_read_b64_tr_b16 a[10:11],v3 offset:46696
v_rcp_f16 v9,v8
v_lshlrev_b32_sdwa v54,1,v54 dst_sel:DWORD dst_unused:UNUSED_PAD src0_sel:DWORD src1_sel:WORD_1
v_mfma_f32_16x16x32_bf16 v[216:219],a[244:247],v[36:39],v[216:219]
ds_read_u16_d16_hi v54,v54
ds_read_b64_tr_b16 a[12:13],v3 offset:39000
v_mfma_f32_16x16x32_bf16 v[220:223],a[244:247],v[40:43],v[220:223]
ds_read_b64_tr_b16 a[14:15],v3 offset:40536
ds_read_b64_tr_b16 a[228:229],v3 offset:18544
v_lshlrev_b32_sdwa v53,1,v53 dst_sel:DWORD dst_unused:UNUSED_PAD src0_sel:DWORD src1_sel:WORD_1
v_mfma_f32_16x16x32_bf16 v[188:191],a[252:255],v[40:43],v[188:191]
ds_read_u16 v53,v53
ds_read_b64_tr_b16 a[230:231],v3 offset:20080
v_lshlrev_b32_sdwa v55,1,v55 dst_sel:DWORD dst_unused:UNUSED_PAD src0_sel:DWORD src1_sel:WORD_1
v_mfma_f32_16x16x32_bf16 v[184:187],a[252:255],v[36:39],v[184:187]
ds_read_u16_d16_hi v55,v55
v_rcp_f16_sdwa v9,v8 dst_sel:WORD_1 dst_unused:UNUSED_PRESERVE src0_sel:WORD_1
v_mfma_f32_16x16x32_bf16 v[224:227],a[232:235],v[36:39],v[224:227]
ds_read_b64_tr_b16 a[236:237],v3 offset:18552
ds_read_b64_tr_b16 a[238:239],v3 offset:20088
v_lshlrev_b32_sdwa v56,1,v56 dst_sel:DWORD dst_unused:UNUSED_PAD src0_sel:DWORD src1_sel:WORD_1
v_mfma_f32_16x16x32_bf16 v[228:231],a[232:235],v[40:43],v[228:231]
ds_read_u16 v56,v56
ds_read_b64_tr_b16 a[244:245],v3 offset:18608
v_lshlrev_b32_sdwa v58,1,v58 dst_sel:DWORD dst_unused:UNUSED_PAD src0_sel:DWORD src1_sel:WORD_1
v_mfma_f32_16x16x32_bf16 v[204:207],a[240:243],v[40:43],v[204:207]
ds_read_u16_d16_hi v58,v58
ds_read_b64_tr_b16 a[246:247],v3 offset:20144
v_fma_mix_f32 v57,v57,v9,0 op_sel_hi:[0,1,0]
v_mfma_f32_16x16x32_bf16 v[200:203],a[240:243],v[36:39],v[200:203]
ds_read_b64_tr_b16 a[252:253],v3 offset:18616
ds_read_b64_tr_b16 a[254:255],v3 offset:20152
v_fma_mix_f32 v59,v59,v9,0 op_sel:[0,1,0] op_sel_hi:[0,1,0]
v_mfma_f32_16x16x32_bf16 v[40:43],a[208:211],a[48:51],v[20:23]
ds_read_b64_tr_b16 a[16:17],v3 offset:39056
ds_read_b64_tr_b16 a[18:19],v3 offset:40592
v_lshlrev_b32_sdwa v32,1,v32 dst_sel:DWORD dst_unused:UNUSED_PAD src0_sel:DWORD src1_sel:WORD_1
v_mfma_f32_16x16x32_bf16 v[40:43],a[204:207],a[64:67],v[40:43]
s_waitcnt lgkmcnt(15)
ds_read_u16 v32,v32
ds_read_b64_tr_b16 a[20:21],v3 offset:45216
v_lshl_add_u64 v[24:25],v[24:25],0,v[26:27]
v_mfma_f32_16x16x32_bf16 v[40:43],a[212:215],a[80:83],v[40:43]
ds_read_b64_tr_b16 a[22:23],v3 offset:46752
ds_read_b64_tr_b16 a[24:25],v3 offset:45224
v_lshl_add_u64 v[26:27],v[28:29],0,v[30:31]
v_lshlrev_b32_sdwa v34,1,v34 dst_sel:DWORD dst_unused:UNUSED_PAD src0_sel:DWORD src1_sel:WORD_1
v_mfma_f32_16x16x32_bf16 v[36:39],a[200:203],a[80:83],v[16:19]
ds_read_u16_d16_hi v34,v34
v_lshlrev_b32_sdwa v33,1,v33 dst_sel:DWORD dst_unused:UNUSED_PAD src0_sel:DWORD src1_sel:WORD_1
v_mfma_f32_16x16x32_bf16 v[36:39],a[196:199],a[64:67],v[36:39]
s_waitcnt lgkmcnt(15)
ds_read_u16 v33,v33
v_lshl_add_u64 v[28:29],v[52:53],0,v[54:55]
v_mfma_f32_16x16x32_bf16 v[36:39],a[192:195],a[48:51],v[36:39]
ds_read_b64_tr_b16 a[26:27],v3 offset:46760
v_lshlrev_b32_sdwa v35,1,v35 dst_sel:DWORD dst_unused:UNUSED_PAD src0_sel:DWORD src1_sel:WORD_1
v_mfma_f32_16x16x32_bf16 v[52:55],a[192:195],a[52:55],v[16:19]
s_waitcnt lgkmcnt(12)
ds_read_u16_d16_hi v35,v35
v_or_b32_e64 v30,v56,v58
v_lshlrev_b32_sdwa v60,1,v60 dst_sel:DWORD dst_unused:UNUSED_PAD src0_sel:DWORD src1_sel:WORD_1
v_mfma_f32_16x16x32_bf16 v[52:55],a[196:199],a[68:71],v[52:55]
ds_read_u16 v60,v60
ds_read_b64_tr_b16 a[28:29],v3 offset:39064
v_cvt_pk_bf16_f32 v31,v57,v59
v_lshlrev_b32_sdwa v62,1,v62 dst_sel:DWORD dst_unused:UNUSED_PAD src0_sel:DWORD src1_sel:WORD_1
v_mfma_f32_16x16x32_bf16 v[52:55],a[200:203],a[84:87],v[52:55]
ds_read_u16_d16_hi v62,v62
v_lshlrev_b32_sdwa v61,1,v61 dst_sel:DWORD dst_unused:UNUSED_PAD src0_sel:DWORD src1_sel:WORD_1
v_mfma_f32_16x16x32_bf16 v[56:59],a[212:215],a[84:87],v[20:23]
ds_read_u16 v61,v61
v_lshlrev_b32_sdwa v63,1,v63 dst_sel:DWORD dst_unused:UNUSED_PAD src0_sel:DWORD src1_sel:WORD_1
v_mfma_f32_16x16x32_bf16 v[56:59],a[204:207],a[68:71],v[56:59]
ds_read_u16_d16_hi v63,v63
v_lshlrev_b32_sdwa v44,1,v44 dst_sel:DWORD dst_unused:UNUSED_PAD src0_sel:DWORD src1_sel:WORD_1
v_mfma_f32_16x16x32_bf16 v[56:59],a[208:211],a[52:55],v[56:59]
ds_read_u16 v44,v44
v_lshlrev_b32_sdwa v46,1,v46 dst_sel:DWORD dst_unused:UNUSED_PAD src0_sel:DWORD src1_sel:WORD_1
v_mfma_f32_16x16x32_bf16 v[136:139],a[224:227],v[24:27],v[136:139]
ds_read_u16_d16_hi v46,v46
v_lshlrev_b32_sdwa v45,1,v45 dst_sel:DWORD dst_unused:UNUSED_PAD src0_sel:DWORD src1_sel:WORD_1
v_mfma_f32_16x16x32_bf16 v[140:143],a[224:227],v[28:31],v[140:143]
ds_read_u16 v45,v45
v_lshlrev_b32_sdwa v47,1,v47 dst_sel:DWORD dst_unused:UNUSED_PAD src0_sel:DWORD src1_sel:WORD_1
v_mfma_f32_16x16x32_bf16 v[68:71],a[248:251],v[28:31],v[68:71]
ds_read_u16_d16_hi v47,v47
v_lshlrev_b32_sdwa v48,1,v48 dst_sel:DWORD dst_unused:UNUSED_PAD src0_sel:DWORD src1_sel:WORD_1
v_mfma_f32_16x16x32_bf16 v[64:67],a[248:251],v[24:27],v[64:67]
ds_read_u16 v48,v48
ds_read_b64_tr_b16 a[30:31],v3 offset:40600
v_mfma_f32_16x16x32_bf16 v[144:147],a[228:231],v[24:27],v[144:147]
ds_read_b64_tr_b16 a[32:33],v3 offset:39120
v_lshlrev_b32_sdwa v50,1,v50 dst_sel:DWORD dst_unused:UNUSED_PAD src0_sel:DWORD src1_sel:WORD_1
v_mfma_f32_16x16x32_bf16 v[148:151],a[228:231],v[28:31],v[148:151]
ds_read_u16_d16_hi v50,v50
v_lshlrev_b32_sdwa v49,1,v49 dst_sel:DWORD dst_unused:UNUSED_PAD src0_sel:DWORD src1_sel:WORD_1
v_mfma_f32_16x16x32_bf16 v[84:87],a[236:239],v[28:31],v[84:87]
ds_read_u16 v49,v49
v_lshlrev_b32_sdwa v51,1,v51 dst_sel:DWORD dst_unused:UNUSED_PAD src0_sel:DWORD src1_sel:WORD_1
v_mfma_f32_16x16x32_bf16 v[80:83],a[236:239],v[24:27],v[80:83]
s_waitcnt lgkmcnt(9)
ds_read_u16_d16_hi v51,v51
v_mfma_f32_16x16x32_bf16 v[152:155],a[244:247],v[24:27],v[152:155]
s_nop 0
ds_read_b64_tr_b16 a[34:35],v3 offset:40656
v_mfma_f32_16x16x32_bf16 v[156:159],a[244:247],v[28:31],v[156:159]
ds_read_b64_tr_b16 a[36:37],v3 offset:45280
v_mfma_f32_16x16x32_bf16 v[100:103],a[252:255],v[28:31],v[100:103]
ds_read_b64_tr_b16 a[38:39],v3 offset:46816
v_mfma_f32_16x16x32_bf16 v[96:99],a[252:255],v[24:27],v[96:99]
ds_read_b64_tr_b16 a[40:41],v3 offset:45288
v_mfma_f32_16x16x32_bf16 v[28:31],a[208:211],a[56:59],v[20:23]
ds_read_b64_tr_b16 a[42:43],v3 offset:46824
v_mul_f32_dpp v136,v14,v136 row_newbcast:0 row_mask:0xf bank_mask:0xf
s_add_u32 s8,s61,3145728
v_lshlrev_b32_sdwa v40,1,v40 dst_sel:DWORD dst_unused:UNUSED_PAD src0_sel:DWORD src1_sel:WORD_1
v_mfma_f32_16x16x32_bf16 v[28:31],a[204:207],a[72:75],v[28:31]
ds_read_u16 v40,v40
v_lshl_add_u64 v[32:33],v[32:33],0,v[34:35]
v_mul_f32_dpp v137,v14,v137 row_newbcast:1 row_mask:0xf bank_mask:0xf
v_mul_f32_dpp v138,v14,v138 row_newbcast:2 row_mask:0xf bank_mask:0xf
v_mfma_f32_16x16x32_bf16 v[28:31],a[212:215],a[88:91],v[28:31]
ds_read_b64_tr_b16 a[44:45],v3 offset:39128
v_lshl_add_u64 v[34:35],v[60:61],0,v[62:63]
v_mul_f32_dpp v139,v14,v139 row_newbcast:3 row_mask:0xf bank_mask:0xf
v_lshlrev_b32_sdwa v42,1,v42 dst_sel:DWORD dst_unused:UNUSED_PAD src0_sel:DWORD src1_sel:WORD_1
v_mfma_f32_16x16x32_bf16 v[24:27],a[200:203],a[88:91],v[16:19]
ds_read_u16_d16_hi v42,v42
v_mul_f32_dpp v64,v14,v64 row_newbcast:4 row_mask:0xf bank_mask:0xf
v_lshlrev_b32_sdwa v41,1,v41 dst_sel:DWORD dst_unused:UNUSED_PAD src0_sel:DWORD src1_sel:WORD_1
v_mul_f32_dpp v65,v14,v65 row_newbcast:5 row_mask:0xf bank_mask:0xf
v_mfma_f32_16x16x32_bf16 v[24:27],a[196:199],a[72:75],v[24:27]
s_waitcnt lgkmcnt(8)
ds_read_u16 v41,v41
v_lshl_add_u64 v[60:61],v[44:45],0,v[46:47]
v_mul_f32_dpp v66,v14,v66 row_newbcast:6 row_mask:0xf bank_mask:0xf
v_mfma_f32_16x16x32_bf16 v[24:27],a[192:195],a[56:59],v[24:27]
s_nop 0
ds_read_b64_tr_b16 a[46:47],v3 offset:40664
v_mul_f32_dpp v67,v14,v67 row_newbcast:7 row_mask:0xf bank_mask:0xf
v_lshlrev_b32_sdwa v43,1,v43 dst_sel:DWORD dst_unused:UNUSED_PAD src0_sel:DWORD src1_sel:WORD_1
v_cvt_pk_bf16_f32 v136,v136,v137
v_mfma_f32_16x16x32_bf16 v[44:47],a[192:195],a[60:63],v[16:19]
ds_read_b128 v[16:19],v4 offset:0
v_cvt_pk_bf16_f32 v137,v138,v139
v_lshlrev_b32_sdwa v36,1,v36 dst_sel:DWORD dst_unused:UNUSED_PAD src0_sel:DWORD src1_sel:WORD_1
v_mfma_f32_16x16x32_bf16 v[44:47],a[196:199],a[76:79],v[44:47]
ds_read_u16_d16_hi v43,v43
ds_read_b64_tr_b16 a[192:193],v1 offset:22576
v_lshl_add_u64 v[62:63],v[48:49],0,v[50:51]
v_cvt_pk_bf16_f32 v138,v64,v65
v_cvt_pk_bf16_f32 v139,v66,v67
v_lshlrev_b32_sdwa v38,1,v38 dst_sel:DWORD dst_unused:UNUSED_PAD src0_sel:DWORD src1_sel:WORD_1
v_mfma_f32_16x16x32_bf16 v[44:47],a[200:203],a[92:95],v[44:47]
buffer_store_dwordx4 v[136:139],v7,s[40:43],s8 offen offset:0 sc1
ds_read_b64_tr_b16 a[194:195],v1 offset:23600
v_mul_f32_dpp v144,v14,v144 row_newbcast:8 row_mask:0xf bank_mask:0xf
v_mfma_f32_16x16x32_bf16 v[48:51],a[212:215],a[92:95],v[20:23]
ds_read_b128 v[20:23],v4 offset:32
ds_read_u16 v36,v36
v_mul_f32_dpp v145,v14,v145 row_newbcast:9 row_mask:0xf bank_mask:0xf
v_lshlrev_b32_sdwa v37,1,v37 dst_sel:DWORD dst_unused:UNUSED_PAD src0_sel:DWORD src1_sel:WORD_1
v_mul_f32_dpp v146,v14,v146 row_newbcast:10 row_mask:0xf bank_mask:0xf
v_mfma_f32_16x16x32_bf16 v[48:51],a[204:207],a[76:79],v[48:51]
ds_read_u16_d16_hi v38,v38
ds_read_u16 v37,v37
v_mul_f32_dpp v147,v14,v147 row_newbcast:11 row_mask:0xf bank_mask:0xf
v_lshlrev_b32_sdwa v39,1,v39 dst_sel:DWORD dst_unused:UNUSED_PAD src0_sel:DWORD src1_sel:WORD_1
v_mfma_f32_16x16x32_bf16 v[48:51],a[208:211],a[60:63],v[48:51]
ds_read_u16_d16_hi v39,v39
ds_read_b64_tr_b16 a[196:197],v2 offset:22576
v_mul_f32_dpp v80,v14,v80 row_newbcast:12 row_mask:0xf bank_mask:0xf
v_mul_f32_dpp v81,v14,v81 row_newbcast:13 row_mask:0xf bank_mask:0xf
v_mfma_f32_16x16x32_bf16 v[112:115],a[224:227],v[32:35],v[112:115]
ds_read_b64_tr_b16 a[198:199],v2 offset:23600
ds_read_b64_tr_b16 a[200:201],v2 offset:20528
v_lshlrev_b32_sdwa v56,1,v56 dst_sel:DWORD dst_unused:UNUSED_PAD src0_sel:DWORD src1_sel:WORD_1
v_mul_f32_dpp v82,v14,v82 row_newbcast:14 row_mask:0xf bank_mask:0xf
v_mfma_f32_16x16x32_bf16 v[116:119],a[224:227],v[60:63],v[116:119]
ds_read_b64_tr_b16 a[202:203],v2 offset:21552
v_mul_f32_dpp v83,v14,v83 row_newbcast:15 row_mask:0xf bank_mask:0xf
v_lshlrev_b32_sdwa v58,1,v58 dst_sel:DWORD dst_unused:UNUSED_PAD src0_sel:DWORD src1_sel:WORD_1
v_cvt_pk_bf16_f32 v144,v144,v145
v_mfma_f32_16x16x32_bf16 v[76:79],a[248:251],v[60:63],v[76:79]
ds_read_u16 v56,v56
v_cvt_pk_bf16_f32 v145,v146,v147
v_lshlrev_b32_sdwa v57,1,v57 dst_sel:DWORD dst_unused:UNUSED_PAD src0_sel:DWORD src1_sel:WORD_1
v_mfma_f32_16x16x32_bf16 v[72:75],a[248:251],v[32:35],v[72:75]
ds_read_u16_d16_hi v58,v58
v_cvt_pk_bf16_f32 v146,v80,v81
v_cvt_pk_bf16_f32 v147,v82,v83
v_mfma_f32_16x16x32_bf16 v[120:123],a[228:231],v[32:35],v[120:123]
ds_read_u16 v57,v57
v_mul_f32_dpp v152,v15,v152 row_newbcast:0 row_mask:0xf bank_mask:0xf
v_lshlrev_b32_sdwa v59,1,v59 dst_sel:DWORD dst_unused:UNUSED_PAD src0_sel:DWORD src1_sel:WORD_1
v_mfma_f32_16x16x32_bf16 v[124:127],a[228:231],v[60:63],v[124:127]
ds_read_u16_d16_hi v59,v59
v_mul_f32_dpp v153,v15,v153 row_newbcast:1 row_mask:0xf bank_mask:0xf
v_lshlrev_b32_sdwa v52,1,v52 dst_sel:DWORD dst_unused:UNUSED_PAD src0_sel:DWORD src1_sel:WORD_1
v_mul_f32_dpp v154,v15,v154 row_newbcast:2 row_mask:0xf bank_mask:0xf
v_mfma_f32_16x16x32_bf16 v[92:95],a[236:239],v[60:63],v[92:95]
ds_read_b64_tr_b16 a[212:213],v2 offset:18480
v_mul_f32_dpp v155,v15,v155 row_newbcast:3 row_mask:0xf bank_mask:0xf
v_mfma_f32_16x16x32_bf16 v[88:91],a[236:239],v[32:35],v[88:91]
ds_read_b64_tr_b16 a[214:215],v2 offset:19504
v_mul_f32_dpp v96,v15,v96 row_newbcast:4 row_mask:0xf bank_mask:0xf
v_lshlrev_b32_sdwa v54,1,v54 dst_sel:DWORD dst_unused:UNUSED_PAD src0_sel:DWORD src1_sel:WORD_1
v_mul_f32_dpp v97,v15,v97 row_newbcast:5 row_mask:0xf bank_mask:0xf
v_mfma_f32_16x16x32_bf16 v[128:131],a[244:247],v[32:35],v[128:131]
buffer_store_dwordx4 v[144:147],v7,s[40:43],s8 offen offset:64 sc1
v_mul_f32_dpp v98,v15,v98 row_newbcast:6 row_mask:0xf bank_mask:0xf
v_lshlrev_b32_sdwa v53,1,v53 dst_sel:DWORD dst_unused:UNUSED_PAD src0_sel:DWORD src1_sel:WORD_1
v_mfma_f32_16x16x32_bf16 v[132:135],a[244:247],v[60:63],v[132:135]
ds_read_u16 v52,v52
v_mul_f32_dpp v99,v15,v99 row_newbcast:7 row_mask:0xf bank_mask:0xf
v_cvt_pk_bf16_f32 v152,v152,v153
v_lshlrev_b32_sdwa v55,1,v55 dst_sel:DWORD dst_unused:UNUSED_PAD src0_sel:DWORD src1_sel:WORD_1
v_mfma_f32_16x16x32_bf16 v[108:111],a[252:255],v[60:63],v[108:111]
s_nop 0
ds_read_u16_d16_hi v54,v54
v_cvt_pk_bf16_f32 v153,v154,v155
v_mfma_f32_16x16x32_bf16 v[104:107],a[252:255],v[32:35],v[104:107]
s_waitcnt lgkmcnt(7)
ds_read_u16 v53,v53
ds_read_u16_d16_hi v55,v55
v_cvt_pk_bf16_f32 v154,v96,v97
v_cvt_pk_bf16_f32 v155,v98,v99
v_mfma_f32_16x16x32_bf16 v[60:63],a[220:223],a[48:51],v[16:19]
v_mul_f32_dpp v140,v14,v140 row_newbcast:0 row_mask:0xf bank_mask:0xf
v_lshlrev_b32_sdwa v28,1,v28 dst_sel:DWORD dst_unused:UNUSED_PAD src0_sel:DWORD src1_sel:WORD_1
v_mfma_f32_16x16x32_bf16 v[60:63],a[216:219],a[64:67],v[60:63]
ds_read_u16 v28,v28
v_lshl_add_u64 v[40:41],v[40:41],0,v[42:43]
v_mul_f32_dpp v141,v14,v141 row_newbcast:1 row_mask:0xf bank_mask:0xf
v_mul_f32_dpp v142,v14,v142 row_newbcast:2 row_mask:0xf bank_mask:0xf
v_mfma_f32_16x16x32_bf16 v[60:63],a[192:195],a[80:83],v[60:63]
v_lshl_add_u64 v[42:43],v[36:37],0,v[38:39]
v_mul_f32_dpp v143,v14,v143 row_newbcast:3 row_mask:0xf bank_mask:0xf
v_lshlrev_b32_sdwa v30,1,v30 dst_sel:DWORD dst_unused:UNUSED_PAD src0_sel:DWORD src1_sel:WORD_1
v_mfma_f32_16x16x32_bf16 v[32:35],a[196:199],a[80:83],v[20:23]
ds_read_u16_d16_hi v30,v30
v_mul_f32_dpp v68,v14,v68 row_newbcast:4 row_mask:0xf bank_mask:0xf
v_lshlrev_b32_sdwa v29,1,v29 dst_sel:DWORD dst_unused:UNUSED_PAD src0_sel:DWORD src1_sel:WORD_1
v_mul_f32_dpp v69,v14,v69 row_newbcast:5 row_mask:0xf bank_mask:0xf
v_mfma_f32_16x16x32_bf16 v[32:35],a[200:203],a[64:67],v[32:35]
s_waitcnt lgkmcnt(6)
ds_read_u16 v29,v29
v_lshl_add_u64 v[36:37],v[56:57],0,v[58:59]
v_mul_f32_dpp v70,v14,v70 row_newbcast:6 row_mask:0xf bank_mask:0xf
v_mfma_f32_16x16x32_bf16 v[32:35],a[212:215],a[48:51],v[32:35]
v_mul_f32_dpp v71,v14,v71 row_newbcast:7 row_mask:0xf bank_mask:0xf
v_lshlrev_b32_sdwa v31,1,v31 dst_sel:DWORD dst_unused:UNUSED_PAD src0_sel:DWORD src1_sel:WORD_1
v_cvt_pk_bf16_f32 v140,v140,v141
v_mfma_f32_16x16x32_bf16 v[56:59],a[212:215],a[52:55],v[20:23]
ds_read_u16_d16_hi v31,v31
v_cvt_pk_bf16_f32 v141,v142,v143
v_lshlrev_b32_sdwa v24,1,v24 dst_sel:DWORD dst_unused:UNUSED_PAD src0_sel:DWORD src1_sel:WORD_1
v_mfma_f32_16x16x32_bf16 v[56:59],a[200:203],a[68:71],v[56:59]
s_waitcnt lgkmcnt(4)
ds_read_u16 v24,v24
v_lshl_add_u64 v[38:39],v[52:53],0,v[54:55]
v_cvt_pk_bf16_f32 v142,v68,v69
v_cvt_pk_bf16_f32 v143,v70,v71
v_lshlrev_b32_sdwa v26,1,v26 dst_sel:DWORD dst_unused:UNUSED_PAD src0_sel:DWORD src1_sel:WORD_1
v_mfma_f32_16x16x32_bf16 v[56:59],a[196:199],a[84:87],v[56:59]
ds_read_u16_d16_hi v26,v26
v_mul_f32_dpp v148,v14,v148 row_newbcast:8 row_mask:0xf bank_mask:0xf
v_mfma_f32_16x16x32_bf16 v[52:55],a[192:195],a[84:87],v[16:19]
buffer_store_dwordx4 v[152:155],v7,s[40:43],s8 offen offset:128 sc1
v_mul_f32_dpp v149,v14,v149 row_newbcast:9 row_mask:0xf bank_mask:0xf
v_lshlrev_b32_sdwa v25,1,v25 dst_sel:DWORD dst_unused:UNUSED_PAD src0_sel:DWORD src1_sel:WORD_1
v_mul_f32_dpp v150,v14,v150 row_newbcast:10 row_mask:0xf bank_mask:0xf
v_mfma_f32_16x16x32_bf16 v[52:55],a[216:219],a[68:71],v[52:55]
ds_read_u16 v25,v25
v_mul_f32_dpp v151,v14,v151 row_newbcast:11 row_mask:0xf bank_mask:0xf
v_lshlrev_b32_sdwa v27,1,v27 dst_sel:DWORD dst_unused:UNUSED_PAD src0_sel:DWORD src1_sel:WORD_1
v_mfma_f32_16x16x32_bf16 v[52:55],a[220:223],a[52:55],v[52:55]
ds_read_u16_d16_hi v27,v27
v_mul_f32_dpp v84,v14,v84 row_newbcast:12 row_mask:0xf bank_mask:0xf
v_mul_f32_dpp v85,v14,v85 row_newbcast:13 row_mask:0xf bank_mask:0xf
v_mfma_f32_16x16x32_bf16 v[232:235],a[224:227],v[40:43],v[232:235]
ds_read_b64_tr_b16 a[232:233],v3 offset:32832
v_lshlrev_b32_sdwa v48,1,v48 dst_sel:DWORD dst_unused:UNUSED_PAD src0_sel:DWORD src1_sel:WORD_1
v_mul_f32_dpp v86,v14,v86 row_newbcast:14 row_mask:0xf bank_mask:0xf
v_mfma_f32_16x16x32_bf16 v[236:239],a[224:227],v[36:39],v[236:239]
ds_read_u16 v48,v48
v_mul_f32_dpp v87,v14,v87 row_newbcast:15 row_mask:0xf bank_mask:0xf
v_lshlrev_b32_sdwa v50,1,v50 dst_sel:DWORD dst_unused:UNUSED_PAD src0_sel:DWORD src1_sel:WORD_1
v_cvt_pk_bf16_f32 v148,v148,v149
v_mfma_f32_16x16x32_bf16 v[164:167],a[248:251],v[36:39],v[164:167]
ds_read_u16_d16_hi v50,v50
v_cvt_pk_bf16_f32 v149,v150,v151
v_lshlrev_b32_sdwa v49,1,v49 dst_sel:DWORD dst_unused:UNUSED_PAD src0_sel:DWORD src1_sel:WORD_1
v_mfma_f32_16x16x32_bf16 v[160:163],a[248:251],v[40:43],v[160:163]
ds_read_u16 v49,v49
v_cvt_pk_bf16_f32 v150,v84,v85
v_cvt_pk_bf16_f32 v151,v86,v87
v_mfma_f32_16x16x32_bf16 v[240:243],a[228:231],v[40:43],v[240:243]
ds_read_b64_tr_b16 a[234:235],v3 offset:34368
v_mul_f32_dpp v156,v15,v156 row_newbcast:0 row_mask:0xf bank_mask:0xf
v_lshlrev_b32_sdwa v51,1,v51 dst_sel:DWORD dst_unused:UNUSED_PAD src0_sel:DWORD src1_sel:WORD_1
v_mfma_f32_16x16x32_bf16 v[244:247],a[228:231],v[36:39],v[244:247]
ds_read_u16_d16_hi v51,v51
v_mul_f32_dpp v157,v15,v157 row_newbcast:1 row_mask:0xf bank_mask:0xf
v_lshlrev_b32_sdwa v44,1,v44 dst_sel:DWORD dst_unused:UNUSED_PAD src0_sel:DWORD src1_sel:WORD_1
v_mul_f32_dpp v158,v15,v158 row_newbcast:2 row_mask:0xf bank_mask:0xf
v_mfma_f32_16x16x32_bf16 v[180:183],a[236:239],v[36:39],v[180:183]
ds_read_u16 v44,v44
v_mul_f32_dpp v159,v15,v159 row_newbcast:3 row_mask:0xf bank_mask:0xf
v_mfma_f32_16x16x32_bf16 v[176:179],a[236:239],v[40:43],v[176:179]
ds_read_b64_tr_b16 a[240:241],v3 offset:32840
v_mul_f32_dpp v100,v15,v100 row_newbcast:4 row_mask:0xf bank_mask:0xf
v_lshlrev_b32_sdwa v46,1,v46 dst_sel:DWORD dst_unused:UNUSED_PAD src0_sel:DWORD src1_sel:WORD_1
v_mul_f32_dpp v101,v15,v101 row_newbcast:5 row_mask:0xf bank_mask:0xf
v_mfma_f32_16x16x32_bf16 v[248:251],a[244:247],v[40:43],v[248:251]
ds_read_u16_d16_hi v46,v46
v_mul_f32_dpp v102,v15,v102 row_newbcast:6 row_mask:0xf bank_mask:0xf
v_lshlrev_b32_sdwa v45,1,v45 dst_sel:DWORD dst_unused:UNUSED_PAD src0_sel:DWORD src1_sel:WORD_1
v_mfma_f32_16x16x32_bf16 v[252:255],a[244:247],v[36:39],v[252:255]
ds_read_u16 v45,v45
v_mul_f32_dpp v103,v15,v103 row_newbcast:7 row_mask:0xf bank_mask:0xf
v_cvt_pk_bf16_f32 v156,v156,v157
v_lshlrev_b32_sdwa v47,1,v47 dst_sel:DWORD dst_unused:UNUSED_PAD src0_sel:DWORD src1_sel:WORD_1
v_mfma_f32_16x16x32_bf16 v[196:199],a[252:255],v[36:39],v[196:199]
buffer_store_dwordx4 v[140:143],v7,s[40:43],s8 offen offset:192 sc1
v_cvt_pk_bf16_f32 v157,v158,v159
v_mfma_f32_16x16x32_bf16 v[192:195],a[252:255],v[40:43],v[192:195]
ds_read_u16_d16_hi v47,v47
v_cvt_pk_bf16_f32 v158,v100,v101
v_cvt_pk_bf16_f32 v159,v102,v103
v_mfma_f32_16x16x32_bf16 v[36:39],a[220:223],a[56:59],v[16:19]
s_nop 0
ds_read_b128 v[64:67],v4 offset:128
ds_read_b128 v[68:71],v4 offset:160
v_mul_f32_dpp v112,v14,v112 row_newbcast:0 row_mask:0xf bank_mask:0xf
s_add_u32 s9,s61,3244032
v_lshlrev_b32_sdwa v60,1,v60 dst_sel:DWORD dst_unused:UNUSED_PAD src0_sel:DWORD src1_sel:WORD_1
v_mfma_f32_16x16x32_bf16 v[36:39],a[216:219],a[72:75],v[36:39]
s_waitcnt lgkmcnt(8)
ds_read_u16 v60,v60
ds_read_b128 v[80:83],v4 offset:256
v_lshl_add_u64 v[28:29],v[28:29],0,v[30:31]
v_mul_f32_dpp v113,v14,v113 row_newbcast:1 row_mask:0xf bank_mask:0xf
v_mul_f32_dpp v114,v14,v114 row_newbcast:2 row_mask:0xf bank_mask:0xf
v_mfma_f32_16x16x32_bf16 v[36:39],a[192:195],a[88:91],v[36:39]
ds_read_b128 v[84:87],v4 offset:288
v_lshl_add_u64 v[30:31],v[24:25],0,v[26:27]
v_mul_f32_dpp v115,v14,v115 row_newbcast:3 row_mask:0xf bank_mask:0xf
v_lshlrev_b32_sdwa v62,1,v62 dst_sel:DWORD dst_unused:UNUSED_PAD src0_sel:DWORD src1_sel:WORD_1
v_mfma_f32_16x16x32_bf16 v[40:43],a[196:199],a[88:91],v[20:23]
ds_read_u16_d16_hi v62,v62
v_mul_f32_dpp v72,v14,v72 row_newbcast:4 row_mask:0xf bank_mask:0xf
v_lshlrev_b32_sdwa v61,1,v61 dst_sel:DWORD dst_unused:UNUSED_PAD src0_sel:DWORD src1_sel:WORD_1
v_mul_f32_dpp v73,v14,v73 row_newbcast:5 row_mask:0xf bank_mask:0xf
v_mfma_f32_16x16x32_bf16 v[40:43],a[200:203],a[72:75],v[40:43]
s_waitcnt lgkmcnt(9)
ds_read_u16 v61,v61
v_lshl_add_u64 v[24:25],v[48:49],0,v[50:51]
v_mul_f32_dpp v74,v14,v74 row_newbcast:6 row_mask:0xf bank_mask:0xf
v_mfma_f32_16x16x32_bf16 v[40:43],a[212:215],a[56:59],v[40:43]
TP10210_%=:
s_cbranch_execz TR1_%=
ds_read_b64_tr_b16 a[242:243],v3 offset:34376
v_mul_f32_dpp v75,v14,v75 row_newbcast:7 row_mask:0xf bank_mask:0xf
v_lshlrev_b32_sdwa v63,1,v63 dst_sel:DWORD dst_unused:UNUSED_PAD src0_sel:DWORD src1_sel:WORD_1
v_cvt_pk_bf16_f32 v112,v112,v113
v_mfma_f32_16x16x32_bf16 v[48:51],a[212:215],a[60:63],v[20:23]
s_nop 0
ds_read_b64_tr_b16 a[212:213],v3 offset:51312
ds_read_b64_tr_b16 a[214:215],v3 offset:52848
v_cvt_pk_bf16_f32 v113,v114,v115
v_lshlrev_b32_sdwa v32,1,v32 dst_sel:DWORD dst_unused:UNUSED_PAD src0_sel:DWORD src1_sel:WORD_1
v_mfma_f32_16x16x32_bf16 v[48:51],a[200:203],a[76:79],v[48:51]
s_waitcnt lgkmcnt(10)
ds_read_b64_tr_b16 a[200:201],v3 offset:57472
ds_read_b64_tr_b16 a[202:203],v3 offset:59008
v_lshl_add_u64 v[26:27],v[44:45],0,v[46:47]
v_cvt_pk_bf16_f32 v114,v72,v73
v_cvt_pk_bf16_f32 v115,v74,v75
v_lshlrev_b32_sdwa v34,1,v34 dst_sel:DWORD dst_unused:UNUSED_PAD src0_sel:DWORD src1_sel:WORD_1
v_mfma_f32_16x16x32_bf16 v[48:51],a[196:199],a[92:95],v[48:51]
ds_read_u16_d16_hi v63,v63
ds_read_u16 v32,v32
v_mul_f32_dpp v120,v14,v120 row_newbcast:8 row_mask:0xf bank_mask:0xf
v_mfma_f32_16x16x32_bf16 v[44:47],a[192:195],a[92:95],v[16:19]
ds_read_u16_d16_hi v34,v34
ds_read_b64_tr_b16 a[196:197],v3 offset:57480
v_mul_f32_dpp v121,v14,v121 row_newbcast:9 row_mask:0xf bank_mask:0xf
v_lshlrev_b32_sdwa v33,1,v33 dst_sel:DWORD dst_unused:UNUSED_PAD src0_sel:DWORD src1_sel:WORD_1
v_mul_f32_dpp v122,v14,v122 row_newbcast:10 row_mask:0xf bank_mask:0xf
v_mfma_f32_16x16x32_bf16 v[44:47],a[216:219],a[76:79],v[44:47]
buffer_store_dwordx4 v[148:151],v7,s[40:43],s8 offen offset:256 sc1
ds_read_u16 v33,v33
v_mul_f32_dpp v123,v14,v123 row_newbcast:11 row_mask:0xf bank_mask:0xf
v_lshlrev_b32_sdwa v35,1,v35 dst_sel:DWORD dst_unused:UNUSED_PAD src0_sel:DWORD src1_sel:WORD_1
v_mfma_f32_16x16x32_bf16 v[44:47],a[220:223],a[60:63],v[44:47]
ds_read_u16_d16_hi v35,v35
ds_read_b64_tr_b16 a[198:199],v3 offset:59016
v_mul_f32_dpp v88,v14,v88 row_newbcast:12 row_mask:0xf bank_mask:0xf
v_mul_f32_dpp v89,v14,v89 row_newbcast:13 row_mask:0xf bank_mask:0xf
v_mfma_f32_16x16x32_bf16 v[208:211],a[224:227],v[28:31],v[208:211]
ds_read_b64_tr_b16 a[192:193],v3 offset:51320
ds_read_b64_tr_b16 a[194:195],v3 offset:52856
v_lshlrev_b32_sdwa v52,1,v52 dst_sel:DWORD dst_unused:UNUSED_PAD src0_sel:DWORD src1_sel:WORD_1
v_mul_f32_dpp v90,v14,v90 row_newbcast:14 row_mask:0xf bank_mask:0xf
v_mfma_f32_16x16x32_bf16 v[212:215],a[224:227],v[24:27],v[212:215]
ds_read_u16 v52,v52
ds_read_b64_tr_b16 a[224:225],v3 offset:32896
v_mul_f32_dpp v91,v14,v91 row_newbcast:15 row_mask:0xf bank_mask:0xf
v_lshlrev_b32_sdwa v54,1,v54 dst_sel:DWORD dst_unused:UNUSED_PAD src0_sel:DWORD src1_sel:WORD_1
v_cvt_pk_bf16_f32 v120,v120,v121
v_mfma_f32_16x16x32_bf16 v[172:175],a[248:251],v[24:27],v[172:175]
ds_read_u16_d16_hi v54,v54
ds_read_b64_tr_b16 a[226:227],v3 offset:34432
v_cvt_pk_bf16_f32 v121,v122,v123
v_lshlrev_b32_sdwa v53,1,v53 dst_sel:DWORD dst_unused:UNUSED_PAD src0_sel:DWORD src1_sel:WORD_1
v_mfma_f32_16x16x32_bf16 v[168:171],a[248:251],v[28:31],v[168:171]
ds_read_u16 v53,v53
ds_read_b64_tr_b16 a[248:249],v3 offset:32904
v_cvt_pk_bf16_f32 v122,v88,v89
v_cvt_pk_bf16_f32 v123,v90,v91
v_mfma_f32_16x16x32_bf16 v[216:219],a[228:231],v[28:31],v[216:219]
ds_read_b64_tr_b16 a[250:251],v3 offset:34440
ds_read_b128 v[96:99],v4 offset:384
v_mul_f32_dpp v128,v15,v128 row_newbcast:0 row_mask:0xf bank_mask:0xf
v_lshlrev_b32_sdwa v55,1,v55 dst_sel:DWORD dst_unused:UNUSED_PAD src0_sel:DWORD src1_sel:WORD_1
v_mfma_f32_16x16x32_bf16 v[220:223],a[228:231],v[24:27],v[220:223]
ds_read_u16_d16_hi v55,v55
ds_read_b128 v[100:103],v4 offset:416
v_mul_f32_dpp v129,v15,v129 row_newbcast:1 row_mask:0xf bank_mask:0xf
v_lshlrev_b32_sdwa v56,1,v56 dst_sel:DWORD dst_unused:UNUSED_PAD src0_sel:DWORD src1_sel:WORD_1
v_mul_f32_dpp v130,v15,v130 row_newbcast:2 row_mask:0xf bank_mask:0xf
v_mfma_f32_16x16x32_bf16 v[188:191],a[236:239],v[24:27],v[188:191]
ds_read_u16 v56,v56
ds_read_b64_tr_b16 a[228:229],v3 offset:32960
v_mul_f32_dpp v131,v15,v131 row_newbcast:3 row_mask:0xf bank_mask:0xf
v_mfma_f32_16x16x32_bf16 v[184:187],a[236:239],v[28:31],v[184:187]
ds_read_b64_tr_b16 a[230:231],v3 offset:34496
ds_read_b64_tr_b16 a[236:237],v3 offset:32968
v_mul_f32_dpp v104,v15,v104 row_newbcast:4 row_mask:0xf bank_mask:0xf
v_lshlrev_b32_sdwa v58,1,v58 dst_sel:DWORD dst_unused:UNUSED_PAD src0_sel:DWORD src1_sel:WORD_1
v_mul_f32_dpp v105,v15,v105 row_newbcast:5 row_mask:0xf bank_mask:0xf
v_mfma_f32_16x16x32_bf16 v[224:227],a[244:247],v[28:31],v[224:227]
ds_read_u16_d16_hi v58,v58
ds_read_b64_tr_b16 a[238:239],v3 offset:34504
v_mul_f32_dpp v106,v15,v106 row_newbcast:6 row_mask:0xf bank_mask:0xf
v_lshlrev_b32_sdwa v57,1,v57 dst_sel:DWORD dst_unused:UNUSED_PAD src0_sel:DWORD src1_sel:WORD_1
v_mfma_f32_16x16x32_bf16 v[228:231],a[244:247],v[24:27],v[228:231]
buffer_store_dwordx4 v[156:159],v7,s[40:43],s8 offen offset:320 sc1
ds_read_u16 v57,v57
v_mul_f32_dpp v107,v15,v107 row_newbcast:7 row_mask:0xf bank_mask:0xf
v_cvt_pk_bf16_f32 v128,v128,v129
v_lshlrev_b32_sdwa v59,1,v59 dst_sel:DWORD dst_unused:UNUSED_PAD src0_sel:DWORD src1_sel:WORD_1
v_mfma_f32_16x16x32_bf16 v[204:207],a[252:255],v[24:27],v[204:207]
s_nop 0
ds_read_u16_d16_hi v59,v59
ds_read_b64_tr_b16 a[216:217],v3 offset:51376
v_cvt_pk_bf16_f32 v129,v130,v131
v_mfma_f32_16x16x32_bf16 v[200:203],a[252:255],v[28:31],v[200:203]
s_waitcnt lgkmcnt(15)
ds_read_b64_tr_b16 a[218:219],v3 offset:52912
ds_read_b128 v[20:23],v4 offset:512
v_cvt_pk_bf16_f32 v130,v104,v105
v_cvt_pk_bf16_f32 v131,v106,v107
v_mfma_f32_16x16x32_bf16 v[24:27],a[96:99],a[48:51],v[64:67]
ds_read_b128 v[16:19],v4 offset:544
ds_read_b64_tr_b16 a[220:221],v3 offset:57536
v_mul_f32_dpp v116,v14,v116 row_newbcast:0 row_mask:0xf bank_mask:0xf
v_lshlrev_b32_sdwa v36,1,v36 dst_sel:DWORD dst_unused:UNUSED_PAD src0_sel:DWORD src1_sel:WORD_1
v_mfma_f32_16x16x32_bf16 v[24:27],a[100:103],a[64:67],v[24:27]
ds_read_u16 v36,v36
ds_read_b64_tr_b16 a[222:223],v3 offset:59072
v_lshl_add_u64 v[60:61],v[60:61],0,v[62:63]
v_mul_f32_dpp v117,v14,v117 row_newbcast:1 row_mask:0xf bank_mask:0xf
v_mul_f32_dpp v118,v14,v118 row_newbcast:2 row_mask:0xf bank_mask:0xf
v_mfma_f32_16x16x32_bf16 v[24:27],a[104:107],a[80:83],v[24:27]
ds_read_b64_tr_b16 a[204:205],v3 offset:57544
v_lshl_add_u64 v[62:63],v[32:33],0,v[34:35]
v_mul_f32_dpp v119,v14,v119 row_newbcast:3 row_mask:0xf bank_mask:0xf
v_lshlrev_b32_sdwa v38,1,v38 dst_sel:DWORD dst_unused:UNUSED_PAD src0_sel:DWORD src1_sel:WORD_1
v_mfma_f32_16x16x32_bf16 v[28:31],a[108:111],a[80:83],v[68:71]
ds_read_u16_d16_hi v38,v38
v_mul_f32_dpp v76,v14,v76 row_newbcast:4 row_mask:0xf bank_mask:0xf
v_lshlrev_b32_sdwa v37,1,v37 dst_sel:DWORD dst_unused:UNUSED_PAD src0_sel:DWORD src1_sel:WORD_1
v_mul_f32_dpp v77,v14,v77 row_newbcast:5 row_mask:0xf bank_mask:0xf
v_mfma_f32_16x16x32_bf16 v[28:31],a[112:115],a[64:67],v[28:31]
s_waitcnt lgkmcnt(13)
ds_read_u16 v37,v37
v_lshl_add_u64 v[32:33],v[52:53],0,v[54:55]
v_mul_f32_dpp v78,v14,v78 row_newbcast:6 row_mask:0xf bank_mask:0xf
v_mfma_f32_16x16x32_bf16 v[28:31],a[116:119],a[48:51],v[28:31]
s_nop 0
ds_read_b64_tr_b16 a[206:207],v3 offset:59080
v_mul_f32_dpp v79,v14,v79 row_newbcast:7 row_mask:0xf bank_mask:0xf
v_lshlrev_b32_sdwa v39,1,v39 dst_sel:DWORD dst_unused:UNUSED_PAD src0_sel:DWORD src1_sel:WORD_1
v_cvt_pk_bf16_f32 v116,v116,v117
v_mfma_f32_16x16x32_bf16 v[52:55],a[120:123],a[48:51],v[84:87]
s_nop 0
ds_read_u16_d16_hi v39,v39
v_cvt_pk_bf16_f32 v117,v118,v119
v_lshlrev_b32_sdwa v40,1,v40 dst_sel:DWORD dst_unused:UNUSED_PAD src0_sel:DWORD src1_sel:WORD_1
v_mfma_f32_16x16x32_bf16 v[52:55],a[124:127],a[64:67],v[52:55]
s_waitcnt lgkmcnt(11)
ds_read_u16 v40,v40
ds_read_b64_tr_b16 a[208:209],v3 offset:51384
v_lshl_add_u64 v[34:35],v[56:57],0,v[58:59]
v_cvt_pk_bf16_f32 v118,v76,v77
v_cvt_pk_bf16_f32 v119,v78,v79
v_lshlrev_b32_sdwa v42,1,v42 dst_sel:DWORD dst_unused:UNUSED_PAD src0_sel:DWORD src1_sel:WORD_1
v_mfma_f32_16x16x32_bf16 v[52:55],a[128:131],a[80:83],v[52:55]
buffer_store_dwordx4 v[112:115],v7,s[40:43],s9 offen offset:0 sc1
ds_read_u16_d16_hi v42,v42
v_mul_f32_dpp v124,v14,v124 row_newbcast:8 row_mask:0xf bank_mask:0xf
v_mfma_f32_16x16x32_bf16 v[56:59],a[132:135],a[80:83],v[80:83]
ds_read_b64_tr_b16 a[210:211],v3 offset:52920
v_mul_f32_dpp v125,v14,v125 row_newbcast:9 row_mask:0xf bank_mask:0xf
v_lshlrev_b32_sdwa v41,1,v41 dst_sel:DWORD dst_unused:UNUSED_PAD src0_sel:DWORD src1_sel:WORD_1
v_mul_f32_dpp v126,v14,v126 row_newbcast:10 row_mask:0xf bank_mask:0xf
v_mfma_f32_16x16x32_bf16 v[56:59],a[136:139],a[64:67],v[56:59]
ds_read_u16 v41,v41
v_mul_f32_dpp v127,v14,v127 row_newbcast:11 row_mask:0xf bank_mask:0xf
v_lshlrev_b32_sdwa v43,1,v43 dst_sel:DWORD dst_unused:UNUSED_PAD src0_sel:DWORD src1_sel:WORD_1
v_mfma_f32_16x16x32_bf16 v[56:59],a[140:143],a[48:51],v[56:59]
ds_read_u16_d16_hi v43,v43
v_mul_f32_dpp v92,v14,v92 row_newbcast:12 row_mask:0xf bank_mask:0xf
v_mul_f32_dpp v93,v14,v93 row_newbcast:13 row_mask:0xf bank_mask:0xf
v_mfma_f32_16x16x32_bf16 v[232:235],a[232:235],v[60:63],v[232:235]
ds_read_b64_tr_b16 a[244:245],v3 offset:51440
v_lshlrev_b32_sdwa v44,1,v44 dst_sel:DWORD dst_unused:UNUSED_PAD src0_sel:DWORD src1_sel:WORD_1
v_mul_f32_dpp v94,v14,v94 row_newbcast:14 row_mask:0xf bank_mask:0xf
v_mfma_f32_16x16x32_bf16 v[236:239],a[232:235],v[32:35],v[236:239]
ds_read_u16 v44,v44
v_mul_f32_dpp v95,v14,v95 row_newbcast:15 row_mask:0xf bank_mask:0xf
v_lshlrev_b32_sdwa v46,1,v46 dst_sel:DWORD dst_unused:UNUSED_PAD src0_sel:DWORD src1_sel:WORD_1
v_cvt_pk_bf16_f32 v124,v124,v125
v_mfma_f32_16x16x32_bf16 v[164:167],a[240:243],v[32:35],v[164:167]
ds_read_u16_d16_hi v46,v46
v_cvt_pk_bf16_f32 v125,v126,v127
v_lshlrev_b32_sdwa v45,1,v45 dst_sel:DWORD dst_unused:UNUSED_PAD src0_sel:DWORD src1_sel:WORD_1
v_mfma_f32_16x16x32_bf16 v[160:163],a[240:243],v[60:63],v[160:163]
ds_read_u16 v45,v45
v_cvt_pk_bf16_f32 v126,v92,v93
v_cvt_pk_bf16_f32 v127,v94,v95
v_mfma_f32_16x16x32_bf16 v[240:243],a[224:227],v[60:63],v[240:243]
ds_read_b64_tr_b16 a[246:247],v3 offset:52976
v_mul_f32_dpp v132,v15,v132 row_newbcast:0 row_mask:0xf bank_mask:0xf
v_lshlrev_b32_sdwa v47,1,v47 dst_sel:DWORD dst_unused:UNUSED_PAD src0_sel:DWORD src1_sel:WORD_1
v_mfma_f32_16x16x32_bf16 v[244:247],a[224:227],v[32:35],v[244:247]
ds_read_u16_d16_hi v47,v47
v_mul_f32_dpp v133,v15,v133 row_newbcast:1 row_mask:0xf bank_mask:0xf
v_lshlrev_b32_sdwa v48,1,v48 dst_sel:DWORD dst_unused:UNUSED_PAD src0_sel:DWORD src1_sel:WORD_1
v_mul_f32_dpp v134,v15,v134 row_newbcast:2 row_mask:0xf bank_mask:0xf
v_mfma_f32_16x16x32_bf16 v[180:183],a[248:251],v[32:35],v[180:183]
ds_read_u16 v48,v48
v_mul_f32_dpp v135,v15,v135 row_newbcast:3 row_mask:0xf bank_mask:0xf
v_mfma_f32_16x16x32_bf16 v[176:179],a[248:251],v[60:63],v[176:179]
ds_read_b64_tr_b16 a[252:253],v3 offset:57600
v_mul_f32_dpp v108,v15,v108 row_newbcast:4 row_mask:0xf bank_mask:0xf
v_lshlrev_b32_sdwa v50,1,v50 dst_sel:DWORD dst_unused:UNUSED_PAD src0_sel:DWORD src1_sel:WORD_1
v_mul_f32_dpp v109,v15,v109 row_newbcast:5 row_mask:0xf bank_mask:0xf
v_mfma_f32_16x16x32_bf16 v[248:251],a[228:231],v[60:63],v[248:251]
ds_read_u16_d16_hi v50,v50
v_mul_f32_dpp v110,v15,v110 row_newbcast:6 row_mask:0xf bank_mask:0xf
v_lshlrev_b32_sdwa v49,1,v49 dst_sel:DWORD dst_unused:UNUSED_PAD src0_sel:DWORD src1_sel:WORD_1
v_mfma_f32_16x16x32_bf16 v[252:255],a[228:231],v[32:35],v[252:255]
buffer_store_dwordx4 v[120:123],v7,s[40:43],s9 offen offset:64 sc1
v_mul_f32_dpp v111,v15,v111 row_newbcast:7 row_mask:0xf bank_mask:0xf
v_cvt_pk_bf16_f32 v132,v132,v133
v_lshlrev_b32_sdwa v51,1,v51 dst_sel:DWORD dst_unused:UNUSED_PAD src0_sel:DWORD src1_sel:WORD_1
v_mfma_f32_16x16x32_bf16 v[196:199],a[236:239],v[32:35],v[196:199]
ds_read_u16 v49,v49
v_cvt_pk_bf16_f32 v133,v134,v135
v_mfma_f32_16x16x32_bf16 v[192:195],a[236:239],v[60:63],v[192:195]
ds_read_u16_d16_hi v51,v51
v_cvt_pk_bf16_f32 v134,v108,v109
v_cvt_pk_bf16_f32 v135,v110,v111
v_mfma_f32_16x16x32_bf16 v[32:35],a[144:147],a[48:51],v[96:99]
ds_read_b64_tr_b16 a[254:255],v3 offset:59136
v_lshlrev_b32_sdwa v24,1,v24 dst_sel:DWORD dst_unused:UNUSED_PAD src0_sel:DWORD src1_sel:WORD_1
v_mfma_f32_16x16x32_bf16 v[32:35],a[148:151],a[64:67],v[32:35]
s_waitcnt lgkmcnt(7)
ds_read_u16 v24,v24
v_lshl_add_u64 v[36:37],v[36:37],0,v[38:39]
v_mfma_f32_16x16x32_bf16 v[32:35],a[152:155],a[80:83],v[32:35]
s_nop 0
v_lshl_add_u64 v[38:39],v[40:41],0,v[42:43]
v_lshlrev_b32_sdwa v26,1,v26 dst_sel:DWORD dst_unused:UNUSED_PAD src0_sel:DWORD src1_sel:WORD_1
v_mfma_f32_16x16x32_bf16 v[60:63],a[156:159],a[80:83],v[100:103]
ds_read_u16_d16_hi v26,v26
v_lshlrev_b32_sdwa v25,1,v25 dst_sel:DWORD dst_unused:UNUSED_PAD src0_sel:DWORD src1_sel:WORD_1
v_mfma_f32_16x16x32_bf16 v[60:63],a[160:163],a[64:67],v[60:63]
s_waitcnt lgkmcnt(6)
ds_read_u16 v25,v25
v_lshl_add_u64 v[40:41],v[44:45],0,v[46:47]
v_mfma_f32_16x16x32_bf16 v[60:63],a[164:167],a[48:51],v[60:63]
v_lshlrev_b32_sdwa v27,1,v27 dst_sel:DWORD dst_unused:UNUSED_PAD src0_sel:DWORD src1_sel:WORD_1
v_lshlrev_b32_sdwa v28,1,v28 dst_sel:DWORD dst_unused:UNUSED_PAD src0_sel:DWORD src1_sel:WORD_1
v_mfma_f32_16x16x32_bf16 v[44:47],a[168:171],a[48:51],v[16:19]
ds_read_u16_d16_hi v27,v27
v_lshlrev_b32_sdwa v30,1,v30 dst_sel:DWORD dst_unused:UNUSED_PAD src0_sel:DWORD src1_sel:WORD_1
v_mfma_f32_16x16x32_bf16 v[44:47],a[172:175],a[64:67],v[44:47]
s_waitcnt lgkmcnt(5)
ds_read_u16 v28,v28
v_lshl_add_u64 v[42:43],v[48:49],0,v[50:51]
v_mfma_f32_16x16x32_bf16 v[44:47],a[176:179],a[80:83],v[44:47]
ds_read_u16_d16_hi v30,v30
v_lshlrev_b32_sdwa v29,1,v29 dst_sel:DWORD dst_unused:UNUSED_PAD src0_sel:DWORD src1_sel:WORD_1
v_mfma_f32_16x16x32_bf16 v[48:51],a[180:183],a[80:83],v[20:23]
ds_read_u16 v29,v29
v_lshlrev_b32_sdwa v31,1,v31 dst_sel:DWORD dst_unused:UNUSED_PAD src0_sel:DWORD src1_sel:WORD_1
v_mfma_f32_16x16x32_bf16 v[48:51],a[184:187],a[64:67],v[48:51]
ds_read_u16_d16_hi v31,v31
v_lshlrev_b32_sdwa v56,1,v56 dst_sel:DWORD dst_unused:UNUSED_PAD src0_sel:DWORD src1_sel:WORD_1
v_mfma_f32_16x16x32_bf16 v[48:51],a[188:191],a[48:51],v[48:51]
ds_read_u16 v56,v56
v_lshlrev_b32_sdwa v58,1,v58 dst_sel:DWORD dst_unused:UNUSED_PAD src0_sel:DWORD src1_sel:WORD_1
v_mfma_f32_16x16x32_bf16 v[208:211],a[232:235],v[36:39],v[208:211]
ds_read_u16_d16_hi v58,v58
v_lshlrev_b32_sdwa v57,1,v57 dst_sel:DWORD dst_unused:UNUSED_PAD src0_sel:DWORD src1_sel:WORD_1
v_mfma_f32_16x16x32_bf16 v[212:215],a[232:235],v[40:43],v[212:215]
buffer_store_dwordx4 v[128:131],v7,s[40:43],s9 offen offset:128 sc1
v_lshlrev_b32_sdwa v59,1,v59 dst_sel:DWORD dst_unused:UNUSED_PAD src0_sel:DWORD src1_sel:WORD_1
v_mfma_f32_16x16x32_bf16 v[172:175],a[240:243],v[40:43],v[172:175]
ds_read_u16 v57,v57
v_lshlrev_b32_sdwa v52,1,v52 dst_sel:DWORD dst_unused:UNUSED_PAD src0_sel:DWORD src1_sel:WORD_1
v_mfma_f32_16x16x32_bf16 v[168:171],a[240:243],v[36:39],v[168:171]
ds_read_u16_d16_hi v59,v59
ds_read_u16 v52,v52
v_mfma_f32_16x16x32_bf16 v[216:219],a[224:227],v[36:39],v[216:219]
ds_read_b64_tr_b16 a[232:233],v3 offset:57608
v_lshlrev_b32_sdwa v54,1,v54 dst_sel:DWORD dst_unused:UNUSED_PAD src0_sel:DWORD src1_sel:WORD_1
v_mfma_f32_16x16x32_bf16 v[220:223],a[224:227],v[40:43],v[220:223]
ds_read_u16_d16_hi v54,v54
v_lshlrev_b32_sdwa v53,1,v53 dst_sel:DWORD dst_unused:UNUSED_PAD src0_sel:DWORD src1_sel:WORD_1
v_mfma_f32_16x16x32_bf16 v[188:191],a[248:251],v[40:43],v[188:191]
ds_read_u16 v53,v53
v_lshlrev_b32_sdwa v55,1,v55 dst_sel:DWORD dst_unused:UNUSED_PAD src0_sel:DWORD src1_sel:WORD_1
v_mfma_f32_16x16x32_bf16 v[184:187],a[248:251],v[36:39],v[184:187]
ds_read_u16_d16_hi v55,v55
v_mfma_f32_16x16x32_bf16 v[224:227],a[228:231],v[36:39],v[224:227]
ds_read_b64_tr_b16 a[234:235],v3 offset:59144
v_mfma_f32_16x16x32_bf16 v[228:231],a[228:231],v[40:43],v[228:231]
ds_read_b64_tr_b16 a[240:241],v3 offset:51448
v_mfma_f32_16x16x32_bf16 v[204:207],a[236:239],v[40:43],v[204:207]
ds_read_b64_tr_b16 a[242:243],v3 offset:52984
v_mfma_f32_16x16x32_bf16 v[200:203],a[236:239],v[36:39],v[200:203]
v_mfma_f32_16x16x32_bf16 v[40:43],a[96:99],a[52:55],v[64:67]
v_lshlrev_b32_sdwa v32,1,v32 dst_sel:DWORD dst_unused:UNUSED_PAD src0_sel:DWORD src1_sel:WORD_1
v_lshlrev_b32_sdwa v34,1,v34 dst_sel:DWORD dst_unused:UNUSED_PAD src0_sel:DWORD src1_sel:WORD_1
v_mfma_f32_16x16x32_bf16 v[40:43],a[100:103],a[68:71],v[40:43]
s_waitcnt lgkmcnt(6)
ds_read_u16 v32,v32
v_lshl_add_u64 v[24:25],v[24:25],0,v[26:27]
v_mfma_f32_16x16x32_bf16 v[40:43],a[104:107],a[84:87],v[40:43]
ds_read_u16_d16_hi v34,v34
v_lshl_add_u64 v[26:27],v[28:29],0,v[30:31]
v_mfma_f32_16x16x32_bf16 v[36:39],a[108:111],a[84:87],v[68:71]
v_lshlrev_b32_sdwa v33,1,v33 dst_sel:DWORD dst_unused:UNUSED_PAD src0_sel:DWORD src1_sel:WORD_1
v_lshlrev_b32_sdwa v35,1,v35 dst_sel:DWORD dst_unused:UNUSED_PAD src0_sel:DWORD src1_sel:WORD_1
v_mfma_f32_16x16x32_bf16 v[36:39],a[112:115],a[68:71],v[36:39]
ds_read_u16 v33,v33
v_lshl_add_u64 v[28:29],v[56:57],0,v[58:59]
v_mfma_f32_16x16x32_bf16 v[36:39],a[116:119],a[52:55],v[36:39]
ds_read_u16_d16_hi v35,v35
v_lshlrev_b32_sdwa v60,1,v60 dst_sel:DWORD dst_unused:UNUSED_PAD src0_sel:DWORD src1_sel:WORD_1
v_mfma_f32_16x16x32_bf16 v[56:59],a[120:123],a[52:55],v[84:87]
ds_read_u16 v60,v60
v_lshlrev_b32_sdwa v62,1,v62 dst_sel:DWORD dst_unused:UNUSED_PAD src0_sel:DWORD src1_sel:WORD_1
v_mfma_f32_16x16x32_bf16 v[56:59],a[124:127],a[68:71],v[56:59]
s_waitcnt lgkmcnt(5)
ds_read_u16_d16_hi v62,v62
v_lshl_add_u64 v[30:31],v[52:53],0,v[54:55]
v_mfma_f32_16x16x32_bf16 v[56:59],a[128:131],a[84:87],v[56:59]
buffer_store_dwordx4 v[116:119],v7,s[40:43],s9 offen offset:192 sc1
v_lshlrev_b32_sdwa v61,1,v61 dst_sel:DWORD dst_unused:UNUSED_PAD src0_sel:DWORD src1_sel:WORD_1
v_mfma_f32_16x16x32_bf16 v[52:55],a[132:135],a[84:87],v[80:83]
ds_read_u16 v61,v61
v_lshlrev_b32_sdwa v63,1,v63 dst_sel:DWORD dst_unused:UNUSED_PAD src0_sel:DWORD src1_sel:WORD_1
v_mfma_f32_16x16x32_bf16 v[52:55],a[136:139],a[68:71],v[52:55]
ds_read_u16_d16_hi v63,v63
v_lshlrev_b32_sdwa v48,1,v48 dst_sel:DWORD dst_unused:UNUSED_PAD src0_sel:DWORD src1_sel:WORD_1
v_mfma_f32_16x16x32_bf16 v[52:55],a[140:143],a[52:55],v[52:55]
ds_read_u16 v48,v48
v_lshlrev_b32_sdwa v50,1,v50 dst_sel:DWORD dst_unused:UNUSED_PAD src0_sel:DWORD src1_sel:WORD_1
v_mfma_f32_16x16x32_bf16 v[232:235],a[0:3],v[24:27],v[232:235]
ds_read_u16_d16_hi v50,v50
v_lshlrev_b32_sdwa v49,1,v49 dst_sel:DWORD dst_unused:UNUSED_PAD src0_sel:DWORD src1_sel:WORD_1
v_mfma_f32_16x16x32_bf16 v[232:235],a[4:7],v[28:31],v[232:235]
ds_read_u16 v49,v49
v_lshlrev_b32_sdwa v51,1,v51 dst_sel:DWORD dst_unused:UNUSED_PAD src0_sel:DWORD src1_sel:WORD_1
v_mfma_f32_16x16x32_bf16 v[160:163],a[8:11],v[28:31],v[160:163]
ds_read_u16_d16_hi v51,v51
v_lshlrev_b32_sdwa v44,1,v44 dst_sel:DWORD dst_unused:UNUSED_PAD src0_sel:DWORD src1_sel:WORD_1
v_mfma_f32_16x16x32_bf16 v[160:163],a[12:15],v[24:27],v[160:163]
ds_read_u16 v44,v44
v_lshlrev_b32_sdwa v46,1,v46 dst_sel:DWORD dst_unused:UNUSED_PAD src0_sel:DWORD src1_sel:WORD_1
v_mfma_f32_16x16x32_bf16 v[240:243],a[16:19],v[24:27],v[240:243]
ds_read_u16_d16_hi v46,v46
v_lshlrev_b32_sdwa v45,1,v45 dst_sel:DWORD dst_unused:UNUSED_PAD src0_sel:DWORD src1_sel:WORD_1
v_mfma_f32_16x16x32_bf16 v[240:243],a[20:23],v[28:31],v[240:243]
ds_read_u16 v45,v45
v_lshlrev_b32_sdwa v47,1,v47 dst_sel:DWORD dst_unused:UNUSED_PAD src0_sel:DWORD src1_sel:WORD_1
v_mfma_f32_16x16x32_bf16 v[176:179],a[24:27],v[28:31],v[176:179]
ds_read_u16_d16_hi v47,v47
v_mfma_f32_16x16x32_bf16 v[176:179],a[28:31],v[24:27],v[176:179]
v_mfma_f32_16x16x32_bf16 v[248:251],a[32:35],v[24:27],v[248:251]
v_mfma_f32_16x16x32_bf16 v[248:251],a[36:39],v[28:31],v[248:251]
v_mfma_f32_16x16x32_bf16 v[192:195],a[40:43],v[28:31],v[192:195]
v_mfma_f32_16x16x32_bf16 v[192:195],a[44:47],v[24:27],v[192:195]
v_mfma_f32_16x16x32_bf16 v[28:31],a[144:147],a[52:55],v[96:99]
v_lshlrev_b32_sdwa v40,1,v40 dst_sel:DWORD dst_unused:UNUSED_PAD src0_sel:DWORD src1_sel:WORD_1
v_lshlrev_b32_sdwa v42,1,v42 dst_sel:DWORD dst_unused:UNUSED_PAD src0_sel:DWORD src1_sel:WORD_1
v_mfma_f32_16x16x32_bf16 v[28:31],a[148:151],a[68:71],v[28:31]
s_waitcnt lgkmcnt(2)
ds_read_u16 v40,v40
v_lshl_add_u64 v[32:33],v[32:33],0,v[34:35]
v_mfma_f32_16x16x32_bf16 v[28:31],a[152:155],a[84:87],v[28:31]
ds_read_u16_d16_hi v42,v42
v_lshl_add_u64 v[34:35],v[60:61],0,v[62:63]
v_mfma_f32_16x16x32_bf16 v[24:27],a[156:159],a[84:87],v[100:103]
v_lshlrev_b32_sdwa v41,1,v41 dst_sel:DWORD dst_unused:UNUSED_PAD src0_sel:DWORD src1_sel:WORD_1
v_lshlrev_b32_sdwa v43,1,v43 dst_sel:DWORD dst_unused:UNUSED_PAD src0_sel:DWORD src1_sel:WORD_1
v_mfma_f32_16x16x32_bf16 v[24:27],a[160:163],a[68:71],v[24:27]
buffer_store_dwordx4 v[124:127],v7,s[40:43],s9 offen offset:256 sc1
v_lshl_add_u64 v[60:61],v[48:49],0,v[50:51]
v_mfma_f32_16x16x32_bf16 v[24:27],a[164:167],a[52:55],v[24:27]
ds_read_u16 v41,v41
v_lshlrev_b32_sdwa v36,1,v36 dst_sel:DWORD dst_unused:UNUSED_PAD src0_sel:DWORD src1_sel:WORD_1
v_mfma_f32_16x16x32_bf16 v[48:51],a[168:171],a[52:55],v[16:19]
ds_read_u16_d16_hi v43,v43
v_lshlrev_b32_sdwa v38,1,v38 dst_sel:DWORD dst_unused:UNUSED_PAD src0_sel:DWORD src1_sel:WORD_1
v_mfma_f32_16x16x32_bf16 v[48:51],a[172:175],a[68:71],v[48:51]
s_waitcnt lgkmcnt(4)
ds_read_u16 v36,v36
v_lshl_add_u64 v[62:63],v[44:45],0,v[46:47]
v_mfma_f32_16x16x32_bf16 v[48:51],a[176:179],a[84:87],v[48:51]
ds_read_u16_d16_hi v38,v38
v_lshlrev_b32_sdwa v37,1,v37 dst_sel:DWORD dst_unused:UNUSED_PAD src0_sel:DWORD src1_sel:WORD_1
v_mfma_f32_16x16x32_bf16 v[44:47],a[180:183],a[84:87],v[20:23]
ds_read_u16 v37,v37
v_lshlrev_b32_sdwa v39,1,v39 dst_sel:DWORD dst_unused:UNUSED_PAD src0_sel:DWORD src1_sel:WORD_1
v_mfma_f32_16x16x32_bf16 v[44:47],a[184:187],a[68:71],v[44:47]
ds_read_u16_d16_hi v39,v39
v_lshlrev_b32_sdwa v52,1,v52 dst_sel:DWORD dst_unused:UNUSED_PAD src0_sel:DWORD src1_sel:WORD_1
v_mfma_f32_16x16x32_bf16 v[44:47],a[188:191],a[52:55],v[44:47]
ds_read_u16 v52,v52
v_lshlrev_b32_sdwa v54,1,v54 dst_sel:DWORD dst_unused:UNUSED_PAD src0_sel:DWORD src1_sel:WORD_1
v_mfma_f32_16x16x32_bf16 v[232:235],a[212:215],v[32:35],v[232:235]
ds_read_u16_d16_hi v54,v54
v_lshlrev_b32_sdwa v53,1,v53 dst_sel:DWORD dst_unused:UNUSED_PAD src0_sel:DWORD src1_sel:WORD_1
v_mfma_f32_16x16x32_bf16 v[232:235],a[200:203],v[60:63],v[232:235]
ds_read_u16 v53,v53
v_lshlrev_b32_sdwa v55,1,v55 dst_sel:DWORD dst_unused:UNUSED_PAD src0_sel:DWORD src1_sel:WORD_1
v_mfma_f32_16x16x32_bf16 v[160:163],a[196:199],v[60:63],v[160:163]
ds_read_u16_d16_hi v55,v55
v_lshlrev_b32_sdwa v56,1,v56 dst_sel:DWORD dst_unused:UNUSED_PAD src0_sel:DWORD src1_sel:WORD_1
v_mfma_f32_16x16x32_bf16 v[160:163],a[192:195],v[32:35],v[160:163]
ds_read_u16 v56,v56
v_lshlrev_b32_sdwa v58,1,v58 dst_sel:DWORD dst_unused:UNUSED_PAD src0_sel:DWORD src1_sel:WORD_1
v_mfma_f32_16x16x32_bf16 v[240:243],a[216:219],v[32:35],v[240:243]
ds_read_u16_d16_hi v58,v58
v_lshlrev_b32_sdwa v57,1,v57 dst_sel:DWORD dst_unused:UNUSED_PAD src0_sel:DWORD src1_sel:WORD_1
v_mfma_f32_16x16x32_bf16 v[240:243],a[220:223],v[60:63],v[240:243]
ds_read_u16 v57,v57
v_lshlrev_b32_sdwa v59,1,v59 dst_sel:DWORD dst_unused:UNUSED_PAD src0_sel:DWORD src1_sel:WORD_1
v_mfma_f32_16x16x32_bf16 v[176:179],a[204:207],v[60:63],v[176:179]
ds_read_u16_d16_hi v59,v59
v_mfma_f32_16x16x32_bf16 v[176:179],a[208:211],v[32:35],v[176:179]
v_mfma_f32_16x16x32_bf16 v[248:251],a[244:247],v[32:35],v[248:251]
v_mfma_f32_16x16x32_bf16 v[248:251],a[252:255],v[60:63],v[248:251]
v_mfma_f32_16x16x32_bf16 v[192:195],a[232:235],v[60:63],v[192:195]
v_mfma_f32_16x16x32_bf16 v[192:195],a[240:243],v[32:35],v[192:195]
buffer_store_dwordx4 v[132:135],v7,s[40:43],s9 offen offset:320 sc1
v_mfma_f32_16x16x32_bf16 v[60:63],a[96:99],a[56:59],v[64:67]
v_lshlrev_b32_sdwa v28,1,v28 dst_sel:DWORD dst_unused:UNUSED_PAD src0_sel:DWORD src1_sel:WORD_1
v_mul_f32_dpp v232,v14,v232 row_newbcast:0 row_mask:0xf bank_mask:0xf
s_add_u32 s10,s61,4718592
v_mfma_f32_16x16x32_bf16 v[60:63],a[100:103],a[72:75],v[60:63]
s_waitcnt lgkmcnt(2)
ds_read_u16 v28,v28
v_lshl_add_u64 v[40:41],v[40:41],0,v[42:43]
v_mfma_f32_16x16x32_bf16 v[60:63],a[104:107],a[88:91],v[60:63]
v_lshl_add_u64 v[42:43],v[36:37],0,v[38:39]
v_lshlrev_b32_sdwa v30,1,v30 dst_sel:DWORD dst_unused:UNUSED_PAD src0_sel:DWORD src1_sel:WORD_1
v_mul_f32_dpp v233,v14,v233 row_newbcast:1 row_mask:0xf bank_mask:0xf
v_mfma_f32_16x16x32_bf16 v[32:35],a[108:111],a[88:91],v[68:71]
ds_read_u16_d16_hi v30,v30
v_lshlrev_b32_sdwa v29,1,v29 dst_sel:DWORD dst_unused:UNUSED_PAD src0_sel:DWORD src1_sel:WORD_1
v_mul_f32_dpp v234,v14,v234 row_newbcast:2 row_mask:0xf bank_mask:0xf
v_mfma_f32_16x16x32_bf16 v[32:35],a[112:115],a[72:75],v[32:35]
ds_read_u16 v29,v29
v_lshl_add_u64 v[36:37],v[52:53],0,v[54:55]
v_mfma_f32_16x16x32_bf16 v[32:35],a[116:119],a[56:59],v[32:35]
v_lshlrev_b32_sdwa v31,1,v31 dst_sel:DWORD dst_unused:UNUSED_PAD src0_sel:DWORD src1_sel:WORD_1
v_mul_f32_dpp v235,v14,v235 row_newbcast:3 row_mask:0xf bank_mask:0xf
v_mfma_f32_16x16x32_bf16 v[52:55],a[120:123],a[56:59],v[84:87]
ds_read_u16_d16_hi v31,v31
v_lshlrev_b32_sdwa v24,1,v24 dst_sel:DWORD dst_unused:UNUSED_PAD src0_sel:DWORD src1_sel:WORD_1
v_mul_f32_dpp v160,v14,v160 row_newbcast:4 row_mask:0xf bank_mask:0xf
v_mfma_f32_16x16x32_bf16 v[52:55],a[124:127],a[72:75],v[52:55]
s_waitcnt lgkmcnt(4)
ds_read_u16 v24,v24
v_lshl_add_u64 v[38:39],v[56:57],0,v[58:59]
v_mul_f32_dpp v161,v14,v161 row_newbcast:5 row_mask:0xf bank_mask:0xf
v_lshlrev_b32_sdwa v26,1,v26 dst_sel:DWORD dst_unused:UNUSED_PAD src0_sel:DWORD src1_sel:WORD_1
v_mfma_f32_16x16x32_bf16 v[52:55],a[128:131],a[88:91],v[52:55]
ds_read_u16_d16_hi v26,v26
v_mul_f32_dpp v162,v14,v162 row_newbcast:6 row_mask:0xf bank_mask:0xf
v_mfma_f32_16x16x32_bf16 v[56:59],a[132:135],a[88:91],v[80:83]
v_lshlrev_b32_sdwa v25,1,v25 dst_sel:DWORD dst_unused:UNUSED_PAD src0_sel:DWORD src1_sel:WORD_1
v_mul_f32_dpp v163,v14,v163 row_newbcast:7 row_mask:0xf bank_mask:0xf
v_mfma_f32_16x16x32_bf16 v[56:59],a[136:139],a[72:75],v[56:59]
ds_read_u16 v25,v25
v_lshlrev_b32_sdwa v27,1,v27 dst_sel:DWORD dst_unused:UNUSED_PAD src0_sel:DWORD src1_sel:WORD_1
v_mfma_f32_16x16x32_bf16 v[56:59],a[140:143],a[56:59],v[56:59]
ds_read_u16_d16_hi v27,v27
v_cvt_pk_bf16_f32 v232,v232,v233
v_mfma_f32_16x16x32_bf16 v[236:239],a[0:3],v[40:43],v[236:239]
v_lshlrev_b32_sdwa v44,1,v44 dst_sel:DWORD dst_unused:UNUSED_PAD src0_sel:DWORD src1_sel:WORD_1
v_cvt_pk_bf16_f32 v233,v234,v235
v_mfma_f32_16x16x32_bf16 v[236:239],a[4:7],v[36:39],v[236:239]
ds_read_u16 v44,v44
v_lshlrev_b32_sdwa v46,1,v46 dst_sel:DWORD dst_unused:UNUSED_PAD src0_sel:DWORD src1_sel:WORD_1
v_mfma_f32_16x16x32_bf16 v[164:167],a[8:11],v[36:39],v[164:167]
ds_read_u16_d16_hi v46,v46
v_cvt_pk_bf16_f32 v234,v160,v161
v_lshlrev_b32_sdwa v45,1,v45 dst_sel:DWORD dst_unused:UNUSED_PAD src0_sel:DWORD src1_sel:WORD_1
v_mfma_f32_16x16x32_bf16 v[164:167],a[12:15],v[40:43],v[164:167]
ds_read_u16 v45,v45
v_cvt_pk_bf16_f32 v235,v162,v163
v_mfma_f32_16x16x32_bf16 v[244:247],a[16:19],v[40:43],v[244:247]
buffer_store_dwordx4 v[232:235],v7,s[40:43],s10 offen offset:0 sc1
v_lshlrev_b32_sdwa v47,1,v47 dst_sel:DWORD dst_unused:UNUSED_PAD src0_sel:DWORD src1_sel:WORD_1
v_mfma_f32_16x16x32_bf16 v[244:247],a[20:23],v[36:39],v[244:247]
ds_read_u16_d16_hi v47,v47
v_mul_f32_dpp v240,v14,v240 row_newbcast:8 row_mask:0xf bank_mask:0xf
v_lshlrev_b32_sdwa v48,1,v48 dst_sel:DWORD dst_unused:UNUSED_PAD src0_sel:DWORD src1_sel:WORD_1
v_mfma_f32_16x16x32_bf16 v[180:183],a[24:27],v[36:39],v[180:183]
ds_read_u16 v48,v48
v_mul_f32_dpp v241,v14,v241 row_newbcast:9 row_mask:0xf bank_mask:0xf
v_mfma_f32_16x16x32_bf16 v[180:183],a[28:31],v[40:43],v[180:183]
v_lshlrev_b32_sdwa v50,1,v50 dst_sel:DWORD dst_unused:UNUSED_PAD src0_sel:DWORD src1_sel:WORD_1
v_mul_f32_dpp v242,v14,v242 row_newbcast:10 row_mask:0xf bank_mask:0xf
v_mfma_f32_16x16x32_bf16 v[252:255],a[32:35],v[40:43],v[252:255]
ds_read_u16_d16_hi v50,v50
v_lshlrev_b32_sdwa v49,1,v49 dst_sel:DWORD dst_unused:UNUSED_PAD src0_sel:DWORD src1_sel:WORD_1
v_mfma_f32_16x16x32_bf16 v[252:255],a[36:39],v[36:39],v[252:255]
ds_read_u16 v49,v49
v_mul_f32_dpp v243,v14,v243 row_newbcast:11 row_mask:0xf bank_mask:0xf
v_lshlrev_b32_sdwa v51,1,v51 dst_sel:DWORD dst_unused:UNUSED_PAD src0_sel:DWORD src1_sel:WORD_1
v_mfma_f32_16x16x32_bf16 v[196:199],a[40:43],v[36:39],v[196:199]
ds_read_u16_d16_hi v51,v51
v_mul_f32_dpp v176,v14,v176 row_newbcast:12 row_mask:0xf bank_mask:0xf
v_mfma_f32_16x16x32_bf16 v[196:199],a[44:47],v[40:43],v[196:199]
v_mul_f32_dpp v177,v14,v177 row_newbcast:13 row_mask:0xf bank_mask:0xf
v_mfma_f32_16x16x32_bf16 v[36:39],a[144:147],a[56:59],v[96:99]
v_lshlrev_b32_sdwa v60,1,v60 dst_sel:DWORD dst_unused:UNUSED_PAD src0_sel:DWORD src1_sel:WORD_1
v_mul_f32_dpp v178,v14,v178 row_newbcast:14 row_mask:0xf bank_mask:0xf
v_mfma_f32_16x16x32_bf16 v[36:39],a[148:151],a[72:75],v[36:39]
s_waitcnt lgkmcnt(5)
ds_read_u16 v60,v60
v_lshl_add_u64 v[28:29],v[28:29],0,v[30:31]
v_mfma_f32_16x16x32_bf16 v[36:39],a[152:155],a[88:91],v[36:39]
s_nop 0
v_lshl_add_u64 v[30:31],v[24:25],0,v[26:27]
v_lshlrev_b32_sdwa v62,1,v62 dst_sel:DWORD dst_unused:UNUSED_PAD src0_sel:DWORD src1_sel:WORD_1
v_mul_f32_dpp v179,v14,v179 row_newbcast:15 row_mask:0xf bank_mask:0xf
v_mfma_f32_16x16x32_bf16 v[40:43],a[156:159],a[88:91],v[100:103]
ds_read_u16_d16_hi v62,v62
v_lshlrev_b32_sdwa v61,1,v61 dst_sel:DWORD dst_unused:UNUSED_PAD src0_sel:DWORD src1_sel:WORD_1
v_cvt_pk_bf16_f32 v240,v240,v241
v_mfma_f32_16x16x32_bf16 v[40:43],a[160:163],a[72:75],v[40:43]
s_waitcnt lgkmcnt(5)
ds_read_u16 v61,v61
v_lshl_add_u64 v[24:25],v[44:45],0,v[46:47]
v_mfma_f32_16x16x32_bf16 v[40:43],a[164:167],a[56:59],v[40:43]
v_lshlrev_b32_sdwa v63,1,v63 dst_sel:DWORD dst_unused:UNUSED_PAD src0_sel:DWORD src1_sel:WORD_1
v_cvt_pk_bf16_f32 v241,v242,v243
v_mfma_f32_16x16x32_bf16 v[44:47],a[168:171],a[56:59],v[16:19]
ds_read_u16_d16_hi v63,v63
TP10728_%=:
s_cbranch_execz TR1_%=
v_lshlrev_b32_sdwa v32,1,v32 dst_sel:DWORD dst_unused:UNUSED_PAD src0_sel:DWORD src1_sel:WORD_1
v_cvt_pk_bf16_f32 v242,v176,v177
v_mfma_f32_16x16x32_bf16 v[44:47],a[172:175],a[72:75],v[44:47]
s_waitcnt lgkmcnt(4)
ds_read_u16 v32,v32
v_lshl_add_u64 v[26:27],v[48:49],0,v[50:51]
v_cvt_pk_bf16_f32 v243,v178,v179
v_lshlrev_b32_sdwa v34,1,v34 dst_sel:DWORD dst_unused:UNUSED_PAD src0_sel:DWORD src1_sel:WORD_1
v_mfma_f32_16x16x32_bf16 v[44:47],a[176:179],a[88:91],v[44:47]
buffer_store_dwordx4 v[240:243],v7,s[40:43],s10 offen offset:64 sc1
v_mul_f32_dpp v248,v15,v248 row_newbcast:0 row_mask:0xf bank_mask:0xf
v_mfma_f32_16x16x32_bf16 v[48:51],a[180:183],a[88:91],v[20:23]
ds_read_u16_d16_hi v34,v34
v_lshlrev_b32_sdwa v33,1,v33 dst_sel:DWORD dst_unused:UNUSED_PAD src0_sel:DWORD src1_sel:WORD_1
v_mfma_f32_16x16x32_bf16 v[48:51],a[184:187],a[72:75],v[48:51]
ds_read_u16 v33,v33
v_mul_f32_dpp v249,v15,v249 row_newbcast:1 row_mask:0xf bank_mask:0xf
v_lshlrev_b32_sdwa v35,1,v35 dst_sel:DWORD dst_unused:UNUSED_PAD src0_sel:DWORD src1_sel:WORD_1
v_mfma_f32_16x16x32_bf16 v[48:51],a[188:191],a[56:59],v[48:51]
ds_read_u16_d16_hi v35,v35
v_mul_f32_dpp v250,v15,v250 row_newbcast:2 row_mask:0xf bank_mask:0xf
v_mfma_f32_16x16x32_bf16 v[236:239],a[212:215],v[28:31],v[236:239]
v_lshlrev_b32_sdwa v56,1,v56 dst_sel:DWORD dst_unused:UNUSED_PAD src0_sel:DWORD src1_sel:WORD_1
v_mul_f32_dpp v251,v15,v251 row_newbcast:3 row_mask:0xf bank_mask:0xf
v_mfma_f32_16x16x32_bf16 v[236:239],a[200:203],v[24:27],v[236:239]
ds_read_u16 v56,v56
v_lshlrev_b32_sdwa v58,1,v58 dst_sel:DWORD dst_unused:UNUSED_PAD src0_sel:DWORD src1_sel:WORD_1
v_mfma_f32_16x16x32_bf16 v[164:167],a[196:199],v[24:27],v[164:167]
ds_read_u16_d16_hi v58,v58
v_mul_f32_dpp v192,v15,v192 row_newbcast:4 row_mask:0xf bank_mask:0xf
v_lshlrev_b32_sdwa v57,1,v57 dst_sel:DWORD dst_unused:UNUSED_PAD src0_sel:DWORD src1_sel:WORD_1
v_mfma_f32_16x16x32_bf16 v[164:167],a[192:195],v[28:31],v[164:167]
ds_read_u16 v57,v57
v_mul_f32_dpp v193,v15,v193 row_newbcast:5 row_mask:0xf bank_mask:0xf
v_mfma_f32_16x16x32_bf16 v[244:247],a[216:219],v[28:31],v[244:247]
v_lshlrev_b32_sdwa v59,1,v59 dst_sel:DWORD dst_unused:UNUSED_PAD src0_sel:DWORD src1_sel:WORD_1
v_mul_f32_dpp v194,v15,v194 row_newbcast:6 row_mask:0xf bank_mask:0xf
v_mfma_f32_16x16x32_bf16 v[244:247],a[220:223],v[24:27],v[244:247]
ds_read_u16_d16_hi v59,v59
v_lshlrev_b32_sdwa v52,1,v52 dst_sel:DWORD dst_unused:UNUSED_PAD src0_sel:DWORD src1_sel:WORD_1
v_mfma_f32_16x16x32_bf16 v[180:183],a[204:207],v[24:27],v[180:183]
ds_read_u16 v52,v52
v_mul_f32_dpp v195,v15,v195 row_newbcast:7 row_mask:0xf bank_mask:0xf
v_mfma_f32_16x16x32_bf16 v[180:183],a[208:211],v[28:31],v[180:183]
v_lshlrev_b32_sdwa v54,1,v54 dst_sel:DWORD dst_unused:UNUSED_PAD src0_sel:DWORD src1_sel:WORD_1
v_cvt_pk_bf16_f32 v248,v248,v249
v_mfma_f32_16x16x32_bf16 v[252:255],a[244:247],v[28:31],v[252:255]
ds_read_u16_d16_hi v54,v54
v_lshlrev_b32_sdwa v53,1,v53 dst_sel:DWORD dst_unused:UNUSED_PAD src0_sel:DWORD src1_sel:WORD_1
v_mfma_f32_16x16x32_bf16 v[252:255],a[252:255],v[24:27],v[252:255]
ds_read_u16 v53,v53
v_cvt_pk_bf16_f32 v249,v250,v251
v_lshlrev_b32_sdwa v55,1,v55 dst_sel:DWORD dst_unused:UNUSED_PAD src0_sel:DWORD src1_sel:WORD_1
v_mfma_f32_16x16x32_bf16 v[196:199],a[232:235],v[24:27],v[196:199]
ds_read_u16_d16_hi v55,v55
v_cvt_pk_bf16_f32 v250,v192,v193
v_mfma_f32_16x16x32_bf16 v[196:199],a[240:243],v[28:31],v[196:199]
v_cvt_pk_bf16_f32 v251,v194,v195
v_mfma_f32_16x16x32_bf16 v[24:27],a[96:99],a[60:63],v[64:67]
buffer_store_dwordx4 v[248:251],v7,s[40:43],s10 offen offset:128 sc1
v_lshlrev_b32_sdwa v36,1,v36 dst_sel:DWORD dst_unused:UNUSED_PAD src0_sel:DWORD src1_sel:WORD_1
v_mfma_f32_16x16x32_bf16 v[24:27],a[100:103],a[76:79],v[24:27]
s_waitcnt lgkmcnt(5)
ds_read_u16 v36,v36
v_lshl_add_u64 v[60:61],v[60:61],0,v[62:63]
v_mul_f32_dpp v236,v14,v236 row_newbcast:0 row_mask:0xf bank_mask:0xf
v_mfma_f32_16x16x32_bf16 v[24:27],a[104:107],a[92:95],v[24:27]
v_lshl_add_u64 v[62:63],v[32:33],0,v[34:35]
v_lshlrev_b32_sdwa v38,1,v38 dst_sel:DWORD dst_unused:UNUSED_PAD src0_sel:DWORD src1_sel:WORD_1
v_mul_f32_dpp v237,v14,v237 row_newbcast:1 row_mask:0xf bank_mask:0xf
v_mfma_f32_16x16x32_bf16 v[28:31],a[108:111],a[92:95],v[68:71]
ds_read_u16_d16_hi v38,v38
v_lshlrev_b32_sdwa v37,1,v37 dst_sel:DWORD dst_unused:UNUSED_PAD src0_sel:DWORD src1_sel:WORD_1
v_mul_f32_dpp v238,v14,v238 row_newbcast:2 row_mask:0xf bank_mask:0xf
v_mfma_f32_16x16x32_bf16 v[28:31],a[112:115],a[76:79],v[28:31]
s_waitcnt lgkmcnt(5)
ds_read_u16 v37,v37
v_lshl_add_u64 v[32:33],v[56:57],0,v[58:59]
v_mfma_f32_16x16x32_bf16 v[28:31],a[116:119],a[60:63],v[28:31]
v_lshlrev_b32_sdwa v39,1,v39 dst_sel:DWORD dst_unused:UNUSED_PAD src0_sel:DWORD src1_sel:WORD_1
v_mul_f32_dpp v239,v14,v239 row_newbcast:3 row_mask:0xf bank_mask:0xf
v_mfma_f32_16x16x32_bf16 v[56:59],a[120:123],a[60:63],v[84:87]
ds_read_u16_d16_hi v39,v39
v_lshlrev_b32_sdwa v40,1,v40 dst_sel:DWORD dst_unused:UNUSED_PAD src0_sel:DWORD src1_sel:WORD_1
v_mul_f32_dpp v164,v14,v164 row_newbcast:4 row_mask:0xf bank_mask:0xf
v_mfma_f32_16x16x32_bf16 v[56:59],a[124:127],a[76:79],v[56:59]
s_waitcnt lgkmcnt(4)
ds_read_u16 v40,v40
v_lshl_add_u64 v[34:35],v[52:53],0,v[54:55]
v_mul_f32_dpp v165,v14,v165 row_newbcast:5 row_mask:0xf bank_mask:0xf
v_lshlrev_b32_sdwa v42,1,v42 dst_sel:DWORD dst_unused:UNUSED_PAD src0_sel:DWORD src1_sel:WORD_1
v_mfma_f32_16x16x32_bf16 v[56:59],a[128:131],a[92:95],v[56:59]
ds_read_u16_d16_hi v42,v42
v_mul_f32_dpp v166,v14,v166 row_newbcast:6 row_mask:0xf bank_mask:0xf
v_mfma_f32_16x16x32_bf16 v[52:55],a[132:135],a[92:95],v[80:83]
v_lshlrev_b32_sdwa v41,1,v41 dst_sel:DWORD dst_unused:UNUSED_PAD src0_sel:DWORD src1_sel:WORD_1
v_mul_f32_dpp v167,v14,v167 row_newbcast:7 row_mask:0xf bank_mask:0xf
v_mfma_f32_16x16x32_bf16 v[52:55],a[136:139],a[76:79],v[52:55]
ds_read_u16 v41,v41
v_lshlrev_b32_sdwa v43,1,v43 dst_sel:DWORD dst_unused:UNUSED_PAD src0_sel:DWORD src1_sel:WORD_1
v_mfma_f32_16x16x32_bf16 v[52:55],a[140:143],a[60:63],v[52:55]
ds_read_u16_d16_hi v43,v43
v_cvt_pk_bf16_f32 v236,v236,v237
v_mfma_f32_16x16x32_bf16 v[208:211],a[0:3],v[60:63],v[208:211]
v_lshlrev_b32_sdwa v48,1,v48 dst_sel:DWORD dst_unused:UNUSED_PAD src0_sel:DWORD src1_sel:WORD_1
v_cvt_pk_bf16_f32 v237,v238,v239
v_mfma_f32_16x16x32_bf16 v[208:211],a[4:7],v[32:35],v[208:211]
ds_read_u16 v48,v48
v_lshlrev_b32_sdwa v50,1,v50 dst_sel:DWORD dst_unused:UNUSED_PAD src0_sel:DWORD src1_sel:WORD_1
v_mfma_f32_16x16x32_bf16 v[168:171],a[8:11],v[32:35],v[168:171]
ds_read_u16_d16_hi v50,v50
v_cvt_pk_bf16_f32 v238,v164,v165
v_lshlrev_b32_sdwa v49,1,v49 dst_sel:DWORD dst_unused:UNUSED_PAD src0_sel:DWORD src1_sel:WORD_1
v_mfma_f32_16x16x32_bf16 v[168:171],a[12:15],v[60:63],v[168:171]
ds_read_u16 v49,v49
v_cvt_pk_bf16_f32 v239,v166,v167
v_mfma_f32_16x16x32_bf16 v[216:219],a[16:19],v[60:63],v[216:219]
buffer_store_dwordx4 v[236:239],v7,s[40:43],s10 offen offset:192 sc1
v_lshlrev_b32_sdwa v51,1,v51 dst_sel:DWORD dst_unused:UNUSED_PAD src0_sel:DWORD src1_sel:WORD_1
v_mfma_f32_16x16x32_bf16 v[216:219],a[20:23],v[32:35],v[216:219]
ds_read_u16_d16_hi v51,v51
v_mul_f32_dpp v244,v14,v244 row_newbcast:8 row_mask:0xf bank_mask:0xf
v_lshlrev_b32_sdwa v44,1,v44 dst_sel:DWORD dst_unused:UNUSED_PAD src0_sel:DWORD src1_sel:WORD_1
v_mfma_f32_16x16x32_bf16 v[184:187],a[24:27],v[32:35],v[184:187]
ds_read_u16 v44,v44
v_mul_f32_dpp v245,v14,v245 row_newbcast:9 row_mask:0xf bank_mask:0xf
v_mfma_f32_16x16x32_bf16 v[184:187],a[28:31],v[60:63],v[184:187]
v_lshlrev_b32_sdwa v46,1,v46 dst_sel:DWORD dst_unused:UNUSED_PAD src0_sel:DWORD src1_sel:WORD_1
v_mul_f32_dpp v246,v14,v246 row_newbcast:10 row_mask:0xf bank_mask:0xf
v_mfma_f32_16x16x32_bf16 v[224:227],a[32:35],v[60:63],v[224:227]
ds_read_u16_d16_hi v46,v46
v_lshlrev_b32_sdwa v45,1,v45 dst_sel:DWORD dst_unused:UNUSED_PAD src0_sel:DWORD src1_sel:WORD_1
v_mfma_f32_16x16x32_bf16 v[224:227],a[36:39],v[32:35],v[224:227]
ds_read_u16 v45,v45
v_mul_f32_dpp v247,v14,v247 row_newbcast:11 row_mask:0xf bank_mask:0xf
v_lshlrev_b32_sdwa v47,1,v47 dst_sel:DWORD dst_unused:UNUSED_PAD src0_sel:DWORD src1_sel:WORD_1
v_mfma_f32_16x16x32_bf16 v[200:203],a[40:43],v[32:35],v[200:203]
ds_read_u16_d16_hi v47,v47
v_mul_f32_dpp v180,v14,v180 row_newbcast:12 row_mask:0xf bank_mask:0xf
v_mfma_f32_16x16x32_bf16 v[200:203],a[44:47],v[60:63],v[200:203]
v_mul_f32_dpp v181,v14,v181 row_newbcast:13 row_mask:0xf bank_mask:0xf
v_mov_b32 v10,0
v_mfma_f32_16x16x32_bf16 v[32:35],a[144:147],a[60:63],v[96:99]
ds_read_b128 v[72:75],v10 offset:11648
v_lshlrev_b32_sdwa v24,1,v24 dst_sel:DWORD dst_unused:UNUSED_PAD src0_sel:DWORD src1_sel:WORD_1
v_mfma_f32_16x16x32_bf16 v[32:35],a[148:151],a[76:79],v[32:35]
s_waitcnt lgkmcnt(6)
ds_read_u16 v24,v24
v_lshl_add_u64 v[36:37],v[36:37],0,v[38:39]
v_mul_f32_dpp v182,v14,v182 row_newbcast:14 row_mask:0xf bank_mask:0xf
v_mfma_f32_16x16x32_bf16 v[32:35],a[152:155],a[92:95],v[32:35]
v_lshl_add_u64 v[38:39],v[40:41],0,v[42:43]
v_lshlrev_b32_sdwa v26,1,v26 dst_sel:DWORD dst_unused:UNUSED_PAD src0_sel:DWORD src1_sel:WORD_1
v_mul_f32_dpp v183,v14,v183 row_newbcast:15 row_mask:0xf bank_mask:0xf
v_mfma_f32_16x16x32_bf16 v[60:63],a[156:159],a[92:95],v[100:103]
ds_read_u16_d16_hi v26,v26
v_lshlrev_b32_sdwa v25,1,v25 dst_sel:DWORD dst_unused:UNUSED_PAD src0_sel:DWORD src1_sel:WORD_1
v_cvt_pk_bf16_f32 v244,v244,v245
v_mfma_f32_16x16x32_bf16 v[60:63],a[160:163],a[76:79],v[60:63]
s_waitcnt lgkmcnt(6)
ds_read_u16 v25,v25
v_lshl_add_u64 v[40:41],v[48:49],0,v[50:51]
v_mfma_f32_16x16x32_bf16 v[60:63],a[164:167],a[60:63],v[60:63]
v_lshlrev_b32_sdwa v27,1,v27 dst_sel:DWORD dst_unused:UNUSED_PAD src0_sel:DWORD src1_sel:WORD_1
v_cvt_pk_bf16_f32 v245,v246,v247
v_mfma_f32_16x16x32_bf16 v[48:51],a[168:171],a[60:63],v[16:19]
ds_read_u16_d16_hi v27,v27
v_lshlrev_b32_sdwa v28,1,v28 dst_sel:DWORD dst_unused:UNUSED_PAD src0_sel:DWORD src1_sel:WORD_1
v_cvt_pk_bf16_f32 v246,v180,v181
v_mfma_f32_16x16x32_bf16 v[48:51],a[172:175],a[76:79],v[48:51]
s_waitcnt lgkmcnt(5)
ds_read_u16 v28,v28
v_lshl_add_u64 v[42:43],v[44:45],0,v[46:47]
v_cvt_pk_bf16_f32 v247,v182,v183
v_lshlrev_b32_sdwa v30,1,v30 dst_sel:DWORD dst_unused:UNUSED_PAD src0_sel:DWORD src1_sel:WORD_1
v_mfma_f32_16x16x32_bf16 v[48:51],a[176:179],a[92:95],v[48:51]
buffer_store_dwordx4 v[244:247],v7,s[40:43],s10 offen offset:256 sc1
v_mul_f32_dpp v252,v15,v252 row_newbcast:0 row_mask:0xf bank_mask:0xf
v_mfma_f32_16x16x32_bf16 v[44:47],a[180:183],a[92:95],v[20:23]
ds_read_u16_d16_hi v30,v30
v_lshlrev_b32_sdwa v29,1,v29 dst_sel:DWORD dst_unused:UNUSED_PAD src0_sel:DWORD src1_sel:WORD_1
v_mfma_f32_16x16x32_bf16 v[44:47],a[184:187],a[76:79],v[44:47]
ds_read_u16 v29,v29
v_mul_f32_dpp v253,v15,v253 row_newbcast:1 row_mask:0xf bank_mask:0xf
v_lshlrev_b32_sdwa v31,1,v31 dst_sel:DWORD dst_unused:UNUSED_PAD src0_sel:DWORD src1_sel:WORD_1
v_mfma_f32_16x16x32_bf16 v[44:47],a[188:191],a[60:63],v[44:47]
ds_read_u16_d16_hi v31,v31
v_mul_f32_dpp v254,v15,v254 row_newbcast:2 row_mask:0xf bank_mask:0xf
v_mfma_f32_16x16x32_bf16 v[208:211],a[212:215],v[36:39],v[208:211]
v_lshlrev_b32_sdwa v52,1,v52 dst_sel:DWORD dst_unused:UNUSED_PAD src0_sel:DWORD src1_sel:WORD_1
v_mul_f32_dpp v255,v15,v255 row_newbcast:3 row_mask:0xf bank_mask:0xf
v_mfma_f32_16x16x32_bf16 v[208:211],a[200:203],v[40:43],v[208:211]
ds_read_u16 v52,v52
v_lshlrev_b32_sdwa v54,1,v54 dst_sel:DWORD dst_unused:UNUSED_PAD src0_sel:DWORD src1_sel:WORD_1
v_mfma_f32_16x16x32_bf16 v[168:171],a[196:199],v[40:43],v[168:171]
ds_read_u16_d16_hi v54,v54
v_mul_f32_dpp v196,v15,v196 row_newbcast:4 row_mask:0xf bank_mask:0xf
v_lshlrev_b32_sdwa v53,1,v53 dst_sel:DWORD dst_unused:UNUSED_PAD src0_sel:DWORD src1_sel:WORD_1
v_mfma_f32_16x16x32_bf16 v[168:171],a[192:195],v[36:39],v[168:171]
ds_read_u16 v53,v53
v_mul_f32_dpp v197,v15,v197 row_newbcast:5 row_mask:0xf bank_mask:0xf
v_mfma_f32_16x16x32_bf16 v[216:219],a[216:219],v[36:39],v[216:219]
v_lshlrev_b32_sdwa v55,1,v55 dst_sel:DWORD dst_unused:UNUSED_PAD src0_sel:DWORD src1_sel:WORD_1
v_mul_f32_dpp v198,v15,v198 row_newbcast:6 row_mask:0xf bank_mask:0xf
v_mfma_f32_16x16x32_bf16 v[216:219],a[220:223],v[40:43],v[216:219]
ds_read_u16_d16_hi v55,v55
v_lshlrev_b32_sdwa v56,1,v56 dst_sel:DWORD dst_unused:UNUSED_PAD src0_sel:DWORD src1_sel:WORD_1
v_mfma_f32_16x16x32_bf16 v[184:187],a[204:207],v[40:43],v[184:187]
ds_read_u16 v56,v56
v_mul_f32_dpp v199,v15,v199 row_newbcast:7 row_mask:0xf bank_mask:0xf
v_mfma_f32_16x16x32_bf16 v[184:187],a[208:211],v[36:39],v[184:187]
v_lshlrev_b32_sdwa v58,1,v58 dst_sel:DWORD dst_unused:UNUSED_PAD src0_sel:DWORD src1_sel:WORD_1
v_cvt_pk_bf16_f32 v252,v252,v253
v_mfma_f32_16x16x32_bf16 v[224:227],a[244:247],v[36:39],v[224:227]
ds_read_u16_d16_hi v58,v58
v_lshlrev_b32_sdwa v57,1,v57 dst_sel:DWORD dst_unused:UNUSED_PAD src0_sel:DWORD src1_sel:WORD_1
v_mfma_f32_16x16x32_bf16 v[224:227],a[252:255],v[40:43],v[224:227]
ds_read_u16 v57,v57
v_cvt_pk_bf16_f32 v253,v254,v255
v_lshlrev_b32_sdwa v59,1,v59 dst_sel:DWORD dst_unused:UNUSED_PAD src0_sel:DWORD src1_sel:WORD_1
v_mfma_f32_16x16x32_bf16 v[200:203],a[232:235],v[40:43],v[200:203]
ds_read_u16_d16_hi v59,v59
v_cvt_pk_bf16_f32 v254,v196,v197
v_mfma_f32_16x16x32_bf16 v[200:203],a[240:243],v[36:39],v[200:203]
v_cvt_pk_bf16_f32 v255,v198,v199
s_waitcnt lgkmcnt(12)
v_lshl_add_u64 v[24:25],v[24:25],0,v[26:27]
s_waitcnt lgkmcnt(8)
v_lshl_add_u64 v[26:27],v[28:29],0,v[30:31]
s_waitcnt lgkmcnt(4)
v_lshl_add_u64 v[28:29],v[52:53],0,v[54:55]
s_waitcnt lgkmcnt(0)
v_lshl_add_u64 v[30:31],v[56:57],0,v[58:59]
v_mfma_f32_16x16x32_bf16 v[212:215],a[0:3],v[24:27],v[212:215]
buffer_store_dwordx4 v[252:255],v7,s[40:43],s10 offen offset:320 sc1
v_mul_f32_dpp v208,v14,v208 row_newbcast:0 row_mask:0xf bank_mask:0xf
s_add_u32 s11,s61,4816896
v_mul_f32_dpp v209,v14,v209 row_newbcast:1 row_mask:0xf bank_mask:0xf
v_mul_f32_dpp v210,v14,v210 row_newbcast:2 row_mask:0xf bank_mask:0xf
v_lshlrev_b32_sdwa v32,1,v32 dst_sel:DWORD dst_unused:UNUSED_PAD src0_sel:DWORD src1_sel:WORD_1
v_mfma_f32_16x16x32_bf16 v[212:215],a[4:7],v[28:31],v[212:215]
ds_read_u16 v32,v32
v_mul_f32_dpp v211,v14,v211 row_newbcast:3 row_mask:0xf bank_mask:0xf
v_mul_f32_dpp v168,v14,v168 row_newbcast:4 row_mask:0xf bank_mask:0xf
v_lshlrev_b32_sdwa v34,1,v34 dst_sel:DWORD dst_unused:UNUSED_PAD src0_sel:DWORD src1_sel:WORD_1
v_mul_f32_dpp v169,v14,v169 row_newbcast:5 row_mask:0xf bank_mask:0xf
v_mfma_f32_16x16x32_bf16 v[172:175],a[8:11],v[28:31],v[172:175]
ds_read_u16_d16_hi v34,v34
v_mul_f32_dpp v170,v14,v170 row_newbcast:6 row_mask:0xf bank_mask:0xf
v_mul_f32_dpp v171,v14,v171 row_newbcast:7 row_mask:0xf bank_mask:0xf
v_lshlrev_b32_sdwa v33,1,v33 dst_sel:DWORD dst_unused:UNUSED_PAD src0_sel:DWORD src1_sel:WORD_1
v_cvt_pk_bf16_f32 v208,v208,v209
v_mfma_f32_16x16x32_bf16 v[172:175],a[12:15],v[24:27],v[172:175]
ds_read_u16 v33,v33
v_cvt_pk_bf16_f32 v209,v210,v211
v_lshlrev_b32_sdwa v35,1,v35 dst_sel:DWORD dst_unused:UNUSED_PAD src0_sel:DWORD src1_sel:WORD_1
v_cvt_pk_bf16_f32 v210,v168,v169
v_lshlrev_b32_sdwa v60,1,v60 dst_sel:DWORD dst_unused:UNUSED_PAD src0_sel:DWORD src1_sel:WORD_1
v_cvt_pk_bf16_f32 v211,v170,v171
v_mfma_f32_16x16x32_bf16 v[220:223],a[16:19],v[24:27],v[220:223]
buffer_store_dwordx4 v[208:211],v7,s[40:43],s11 offen offset:0 sc1
v_mul_f32_dpp v216,v14,v216 row_newbcast:8 row_mask:0xf bank_mask:0xf
v_mul_f32_dpp v217,v14,v217 row_newbcast:9 row_mask:0xf bank_mask:0xf
v_lshlrev_b32_sdwa v62,1,v62 dst_sel:DWORD dst_unused:UNUSED_PAD src0_sel:DWORD src1_sel:WORD_1
v_mul_f32_dpp v218,v14,v218 row_newbcast:10 row_mask:0xf bank_mask:0xf
v_mfma_f32_16x16x32_bf16 v[220:223],a[20:23],v[28:31],v[220:223]
ds_read_u16_d16_hi v35,v35
v_mul_f32_dpp v219,v14,v219 row_newbcast:11 row_mask:0xf bank_mask:0xf
v_lshlrev_b32_sdwa v61,1,v61 dst_sel:DWORD dst_unused:UNUSED_PAD src0_sel:DWORD src1_sel:WORD_1
v_mul_f32_dpp v184,v14,v184 row_newbcast:12 row_mask:0xf bank_mask:0xf
v_lshlrev_b32_sdwa v63,1,v63 dst_sel:DWORD dst_unused:UNUSED_PAD src0_sel:DWORD src1_sel:WORD_1
v_mul_f32_dpp v185,v14,v185 row_newbcast:13 row_mask:0xf bank_mask:0xf
v_mfma_f32_16x16x32_bf16 v[188:191],a[24:27],v[28:31],v[188:191]
ds_read_u16 v60,v60
ds_read_u16_d16_hi v62,v62
v_mul_f32_dpp v186,v14,v186 row_newbcast:14 row_mask:0xf bank_mask:0xf
v_mul_f32_dpp v187,v14,v187 row_newbcast:15 row_mask:0xf bank_mask:0xf
v_lshlrev_b32_sdwa v44,1,v44 dst_sel:DWORD dst_unused:UNUSED_PAD src0_sel:DWORD src1_sel:WORD_1
v_cvt_pk_bf16_f32 v216,v216,v217
v_mfma_f32_16x16x32_bf16 v[188:191],a[28:31],v[24:27],v[188:191]
ds_read_u16 v61,v61
ds_read_u16_d16_hi v63,v63
v_cvt_pk_bf16_f32 v217,v218,v219
v_cvt_pk_bf16_f32 v218,v184,v185
v_lshlrev_b32_sdwa v46,1,v46 dst_sel:DWORD dst_unused:UNUSED_PAD src0_sel:DWORD src1_sel:WORD_1
v_cvt_pk_bf16_f32 v219,v186,v187
v_lshlrev_b32_sdwa v45,1,v45 dst_sel:DWORD dst_unused:UNUSED_PAD src0_sel:DWORD src1_sel:WORD_1
v_mfma_f32_16x16x32_bf16 v[228:231],a[32:35],v[24:27],v[228:231]
buffer_store_dwordx4 v[216:219],v7,s[40:43],s11 offen offset:64 sc1
ds_read_u16 v44,v44
v_mul_f32_dpp v224,v15,v224 row_newbcast:0 row_mask:0xf bank_mask:0xf
v_mul_f32_dpp v225,v15,v225 row_newbcast:1 row_mask:0xf bank_mask:0xf
v_lshlrev_b32_sdwa v47,1,v47 dst_sel:DWORD dst_unused:UNUSED_PAD src0_sel:DWORD src1_sel:WORD_1
v_mul_f32_dpp v226,v15,v226 row_newbcast:2 row_mask:0xf bank_mask:0xf
v_mfma_f32_16x16x32_bf16 v[228:231],a[36:39],v[28:31],v[228:231]
ds_read_u16_d16_hi v46,v46
ds_read_u16 v45,v45
v_mul_f32_dpp v227,v15,v227 row_newbcast:3 row_mask:0xf bank_mask:0xf
v_mul_f32_dpp v200,v15,v200 row_newbcast:4 row_mask:0xf bank_mask:0xf
v_lshlrev_b32_sdwa v48,1,v48 dst_sel:DWORD dst_unused:UNUSED_PAD src0_sel:DWORD src1_sel:WORD_1
v_mul_f32_dpp v201,v15,v201 row_newbcast:5 row_mask:0xf bank_mask:0xf
v_mfma_f32_16x16x32_bf16 v[204:207],a[40:43],v[28:31],v[204:207]
ds_read_u16_d16_hi v47,v47
v_mul_f32_dpp v202,v15,v202 row_newbcast:6 row_mask:0xf bank_mask:0xf
v_lshlrev_b32_sdwa v50,1,v50 dst_sel:DWORD dst_unused:UNUSED_PAD src0_sel:DWORD src1_sel:WORD_1
v_mul_f32_dpp v203,v15,v203 row_newbcast:7 row_mask:0xf bank_mask:0xf
v_lshlrev_b32_sdwa v49,1,v49 dst_sel:DWORD dst_unused:UNUSED_PAD src0_sel:DWORD src1_sel:WORD_1
v_cvt_pk_bf16_f32 v224,v224,v225
v_mfma_f32_16x16x32_bf16 v[204:207],a[44:47],v[24:27],v[204:207]
ds_read_u16 v48,v48
ds_read_u16_d16_hi v50,v50
v_cvt_pk_bf16_f32 v225,v226,v227
v_cvt_pk_bf16_f32 v226,v200,v201
v_cvt_pk_bf16_f32 v227,v202,v203
v_lshlrev_b32_sdwa v51,1,v51 dst_sel:DWORD dst_unused:UNUSED_PAD src0_sel:DWORD src1_sel:WORD_1
ds_read_u16 v49,v49
ds_read_u16_d16_hi v51,v51
s_waitcnt lgkmcnt(12)
v_lshl_add_u64 v[32:33],v[32:33],0,v[34:35]
s_waitcnt lgkmcnt(8)
v_lshl_add_u64 v[34:35],v[60:61],0,v[62:63]
s_waitcnt lgkmcnt(4)
v_lshl_add_u64 v[60:61],v[44:45],0,v[46:47]
s_waitcnt lgkmcnt(0)
v_lshl_add_u64 v[62:63],v[48:49],0,v[50:51]
buffer_store_dwordx4 v[224:227],v7,s[40:43],s11 offen offset:128 sc1
v_mfma_f32_16x16x32_bf16 v[212:215],a[212:215],v[32:35],v[212:215]
v_mfma_f32_16x16x32_bf16 v[212:215],a[200:203],v[60:63],v[212:215]
v_mfma_f32_16x16x32_bf16 v[172:175],a[196:199],v[60:63],v[172:175]
v_mfma_f32_16x16x32_bf16 v[172:175],a[192:195],v[32:35],v[172:175]
v_mfma_f32_16x16x32_bf16 v[220:223],a[216:219],v[32:35],v[220:223]
v_mfma_f32_16x16x32_bf16 v[220:223],a[220:223],v[60:63],v[220:223]
s_nop 0
v_mfma_f32_16x16x32_bf16 v[188:191],a[204:207],v[60:63],v[188:191]
s_nop 2
v_mul_f32_dpp v212,v14,v212 row_newbcast:0 row_mask:0xf bank_mask:0xf
v_mul_f32_dpp v213,v14,v213 row_newbcast:1 row_mask:0xf bank_mask:0xf
v_mul_f32_dpp v214,v14,v214 row_newbcast:2 row_mask:0xf bank_mask:0xf
v_mfma_f32_16x16x32_bf16 v[188:191],a[208:211],v[32:35],v[188:191]
v_mul_f32_dpp v215,v14,v215 row_newbcast:3 row_mask:0xf bank_mask:0xf
v_mul_f32_dpp v172,v14,v172 row_newbcast:4 row_mask:0xf bank_mask:0xf
v_mul_f32_dpp v173,v14,v173 row_newbcast:5 row_mask:0xf bank_mask:0xf
v_mfma_f32_16x16x32_bf16 v[228:231],a[244:247],v[32:35],v[228:231]
v_mul_f32_dpp v174,v14,v174 row_newbcast:6 row_mask:0xf bank_mask:0xf
v_mul_f32_dpp v175,v14,v175 row_newbcast:7 row_mask:0xf bank_mask:0xf
v_cvt_pk_bf16_f32 v212,v212,v213
v_mfma_f32_16x16x32_bf16 v[228:231],a[252:255],v[60:63],v[228:231]
v_cvt_pk_bf16_f32 v213,v214,v215
v_cvt_pk_bf16_f32 v214,v172,v173
v_cvt_pk_bf16_f32 v215,v174,v175
buffer_store_dwordx4 v[212:215],v7,s[40:43],s11 offen offset:192 sc1
v_mfma_f32_16x16x32_bf16 v[204:207],a[232:235],v[60:63],v[204:207]
v_mul_f32_dpp v220,v14,v220 row_newbcast:8 row_mask:0xf bank_mask:0xf
v_mul_f32_dpp v221,v14,v221 row_newbcast:9 row_mask:0xf bank_mask:0xf
v_mul_f32_dpp v222,v14,v222 row_newbcast:10 row_mask:0xf bank_mask:0xf
v_mfma_f32_16x16x32_bf16 v[204:207],a[240:243],v[32:35],v[204:207]
v_mul_f32_dpp v223,v14,v223 row_newbcast:11 row_mask:0xf bank_mask:0xf
v_mul_f32_dpp v188,v14,v188 row_newbcast:12 row_mask:0xf bank_mask:0xf
v_mul_f32_dpp v189,v14,v189 row_newbcast:13 row_mask:0xf bank_mask:0xf
v_mul_f32_dpp v190,v14,v190 row_newbcast:14 row_mask:0xf bank_mask:0xf
v_mul_f32_dpp v191,v14,v191 row_newbcast:15 row_mask:0xf bank_mask:0xf
v_cvt_pk_bf16_f32 v220,v220,v221
v_cvt_pk_bf16_f32 v221,v222,v223
v_cvt_pk_bf16_f32 v222,v188,v189
v_cvt_pk_bf16_f32 v223,v190,v191
buffer_store_dwordx4 v[220:223],v7,s[40:43],s11 offen offset:256 sc1
v_mul_f32_dpp v228,v15,v228 row_newbcast:0 row_mask:0xf bank_mask:0xf
v_mul_f32_dpp v229,v15,v229 row_newbcast:1 row_mask:0xf bank_mask:0xf
v_mul_f32_dpp v230,v15,v230 row_newbcast:2 row_mask:0xf bank_mask:0xf
v_mul_f32_dpp v231,v15,v231 row_newbcast:3 row_mask:0xf bank_mask:0xf
v_mul_f32_dpp v204,v15,v204 row_newbcast:4 row_mask:0xf bank_mask:0xf
v_mul_f32_dpp v205,v15,v205 row_newbcast:5 row_mask:0xf bank_mask:0xf
v_mul_f32_dpp v206,v15,v206 row_newbcast:6 row_mask:0xf bank_mask:0xf
v_mul_f32_dpp v207,v15,v207 row_newbcast:7 row_mask:0xf bank_mask:0xf
v_cvt_pk_bf16_f32 v228,v228,v229
v_cvt_pk_bf16_f32 v229,v230,v231
v_cvt_pk_bf16_f32 v230,v204,v205
v_cvt_pk_bf16_f32 v231,v206,v207
buffer_store_dwordx4 v[228:231],v7,s[40:43],s11 offen offset:320 sc1
v_or3_b32 v72,v72,v73,v74
v_or_b32 v72,v72,v75
s_nop 0
v_readfirstlane_b32 s64,v72
s_cmp_lg_u32 s64,0
s_cbranch_scc1 N_fallback_%=
s_endpgm
N_mainend_%=:
.fill 512,4,0xbf800000
N_fallback_%=:
s_waitcnt vmcnt(0) lgkmcnt(0)
s_barrier
)ASM"
    :: "s"(blockIdx.x), "v"(tid), "s"(ka)
    : "v1", "v2", "v3", "v4", "v5", "v6", "v7", "v8", "v9", "v10", "v11", "v12", "v13", "v14", "v15", "v16", "v17", "v18", "v19", "v20", "v21", "v22", "v23", "v24", "v25", "v26", "v27", "v28", "v29", "v30", "v31", "v32", "v33", "v34", "v35", "v36", "v37", "v38", "v39", "v40", "v41", "v42", "v43", "v44", "v45", "v46", "v47", "v48", "v49", "v50", "v51", "v52", "v53", "v54", "v55", "v56", "v57", "v58", "v59", "v60", "v61", "v62", "v63", "v64", "v65", "v66", "v67", "v68", "v69", "v70", "v71", "v72", "v73", "v74", "v75", "v76", "v77", "v78", "v79", "v80", "v81", "v82", "v83", "v84", "v85", "v86", "v87", "v88", "v89", "v90", "v91", "v92", "v93", "v94", "v95", "v96", "v97", "v98", "v99", "v100", "v101", "v102", "v103", "v104", "v105", "v106", "v107", "v108", "v109", "v110", "v111", "v112", "v113", "v114", "v115", "v116", "v117", "v118", "v119", "v120", "v121", "v122", "v123", "v124", "v125", "v126", "v127", "v128", "v129", "v130", "v131", "v132", "v133", "v134", "v135", "v136", "v137", "v138", "v139", "v140", "v141", "v142", "v143", "v144", "v145", "v146", "v147", "v148", "v149", "v150", "v151", "v152", "v153", "v154", "v155", "v156", "v157", "v158", "v159", "v160", "v161", "v162", "v163", "v164", "v165", "v166", "v167", "v168", "v169", "v170", "v171", "v172", "v173", "v174", "v175", "v176", "v177", "v178", "v179", "v180", "v181", "v182", "v183", "v184", "v185", "v186", "v187", "v188", "v189", "v190", "v191", "v192", "v193", "v194", "v195", "v196", "v197", "v198", "v199", "v200", "v201", "v202", "v203", "v204", "v205", "v206", "v207", "v208", "v209", "v210", "v211", "v212", "v213", "v214", "v215", "v216", "v217", "v218", "v219", "v220", "v221", "v222", "v223", "v224", "v225", "v226", "v227", "v228", "v229", "v230", "v231", "v232", "v233", "v234", "v235", "v236", "v237", "v238", "v239", "v240", "v241", "v242", "v243", "v244", "v245", "v246", "v247", "v248", "v249", "v250", "v251", "v252", "v253", "v254", "v255", "a0", "a1", "a2", "a3", "a4", "a5", "a6", "a7", "a8", "a9", "a10", "a11", "a12", "a13", "a14", "a15", "a16", "a17", "a18", "a19", "a20", "a21", "a22", "a23", "a24", "a25", "a26", "a27", "a28", "a29", "a30", "a31", "a32", "a33", "a34", "a35", "a36", "a37", "a38", "a39", "a40", "a41", "a42", "a43", "a44", "a45", "a46", "a47", "a48", "a49", "a50", "a51", "a52", "a53", "a54", "a55", "a56", "a57", "a58", "a59", "a60", "a61", "a62", "a63", "a64", "a65", "a66", "a67", "a68", "a69", "a70", "a71", "a72", "a73", "a74", "a75", "a76", "a77", "a78", "a79", "a80", "a81", "a82", "a83", "a84", "a85", "a86", "a87", "a88", "a89", "a90", "a91", "a92", "a93", "a94", "a95", "a96", "a97", "a98", "a99", "a100", "a101", "a102", "a103", "a104", "a105", "a106", "a107", "a108", "a109", "a110", "a111", "a112", "a113", "a114", "a115", "a116", "a117", "a118", "a119", "a120", "a121", "a122", "a123", "a124", "a125", "a126", "a127", "a128", "a129", "a130", "a131", "a132", "a133", "a134", "a135", "a136", "a137", "a138", "a139", "a140", "a141", "a142", "a143", "a144", "a145", "a146", "a147", "a148", "a149", "a150", "a151", "a152", "a153", "a154", "a155", "a156", "a157", "a158", "a159", "a160", "a161", "a162", "a163", "a164", "a165", "a166", "a167", "a168", "a169", "a170", "a171", "a172", "a173", "a174", "a175", "a176", "a177", "a178", "a179", "a180", "a181", "a182", "a183", "a184", "a185", "a186", "a187", "a188", "a189", "a190", "a191", "a192", "a193", "a194", "a195", "a196", "a197", "a198", "a199", "a200", "a201", "a202", "a203", "a204", "a205", "a206", "a207", "a208", "a209", "a210", "a211", "a212", "a213", "a214", "a215", "a216", "a217", "a218", "a219", "a220", "a221", "a222", "a223", "a224", "a225", "a226", "a227", "a228", "a229", "a230", "a231", "a232", "a233", "a234", "a235", "a236", "a237", "a238", "a239", "a240", "a241", "a242", "a243", "a244", "a245", "a246", "a247", "a248", "a249", "a250", "a251", "a252", "a253", "a254", "a255", "s4", "s5", "s6", "s7", "s8", "s9", "s10", "s11", "s12", "s13", "s14", "s15", "s16", "s17", "s18", "s19", "s20", "s21", "s22", "s23", "s24", "s25", "s26", "s27", "s28", "s29", "s30", "s31", "s32", "s33", "s34", "s35", "s36", "s37", "s38", "s39", "s40", "s41", "s42", "s43", "s44", "s45", "s46", "s47", "s48", "s49", "s50", "s51", "s52", "s53", "s54", "s55", "s56", "s57", "s58", "s59", "s60", "s61", "s62", "s63", "s64", "s65", "s66", "s67", "s68", "s69", "s70", "s71", "s72", "s73", "s74", "s75", "s76", "s77", "s78", "s79", "s80", "s81", "s82", "s83", "s84", "s85", "s86", "s87", "s88", "s89", "s90", "s91", "s92", "s93", "s94", "s95", "s96", "s97", "s98", "s99", "vcc", "scc", "memory");
    asm volatile("" : "+s"(ka));
    typedef const unsigned short* __attribute__((address_space(1))) gp;
    unsigned long long const __attribute__((address_space(4)))* kp = (unsigned long long const __attribute__((address_space(4)))*)ka;
    const unsigned short* x = (const unsigned short*)kp[0]; const unsigned short* dw = (const unsigned short*)kp[1];
    const unsigned short* w1 = (const unsigned short*)kp[2]; const unsigned short* b1 = (const unsigned short*)kp[3];
    const unsigned short* w2 = (const unsigned short*)kp[4]; const unsigned short* b2 = (const unsigned short*)kp[5];
    const unsigned short* gamma = (const unsigned short*)kp[6]; unsigned short* out = (unsigned short*)kp[7];
    extern __shared__ float sm[];
    float* cv = sm; float* hid = sm + 16 * 96; float* stt = hid + 16 * 384;
    const int x0 = (blockIdx.x >> 3) * 16, y0 = (blockIdx.x & 7) * 64;
    for (int y = y0; y < y0 + 64; ++y) {
        for (int i = tid; i < 16 * 96; i += 256) {
            int p = i / 96, c = i - p * 96; float s = 0.f;
            for (int ky = 0; ky < 3; ++ky) for (int kx = 0; kx < 3; ++kx) {
                int yy = y + ky - 1, xx = x0 + p + kx - 1;
                float v = (yy >= 0 && yy < 512 && xx >= 0 && xx < 512) ? bf2f(x[((size_t)yy * 512 + xx) * 96 + c]) : 0.f;
                s = fmaf(v, bf2f(dw[(ky * 3 + kx) * 96 + c]), s);
            }
            cv[i] = s;
        }
        __syncthreads();
        if (tid < 16) {
            float m = 0.f; for (int c = 0; c < 96; ++c) m += cv[tid * 96 + c];
            m *= (1.f / 96); float q = 0.f;
            for (int c = 0; c < 96; ++c) { float d = cv[tid * 96 + c] - m; q = fmaf(d, d, q); }
            stt[tid] = m; stt[16 + tid] = 1.f / sqrtf(q * (1.f / 96) + 1e-5f);
        }
        __syncthreads();
        for (int i = tid; i < 16 * 96; i += 256) { int p = i / 96; cv[i] = rbf((cv[i] - stt[p]) * stt[16 + p]); }
        __syncthreads();
        for (int e = tid; e < 384; e += 256) {
            float a[16];
#pragma unroll
            for (int p = 0; p < 16; ++p) a[p] = 0.f;
            for (int c = 0; c < 96; ++c) {
                float w = bf2f(w1[c * 384 + e]);
#pragma unroll
                for (int p = 0; p < 16; ++p) a[p] = fmaf(cv[p * 96 + c], w, a[p]);
            }
            float bb = bf2f(b1[e]);
#pragma unroll
            for (int p = 0; p < 16; ++p) {
                float h = a[p] + bb;
                hid[p * 384 + e] = rbf(0.5f * h * (1.f + tanhf(0.7978845608028654f * (h + 0.044715f * h * h * h))));
            }
        }
        __syncthreads();
        for (int i = tid; i < 16 * 96; i += 256) {
            int p = i / 96, c = i - p * 96; float s = 0.f;
            for (int e = 0; e < 384; ++e) s = fmaf(hid[p * 384 + e], bf2f(w2[e * 96 + c]), s);
            size_t o = ((size_t)y * 512 + x0 + p) * 96 + c;
            out[o] = f2bf(bf2f(x[o]) + bf2f(gamma[c]) * (s + bf2f(b2[c])));
        }
        __syncthreads();
    }

}

extern "C" void kernel_launch(void* const* d_in, const int* in_sizes, int n_in,
                              void* d_out, int out_size, void* d_ws, size_t ws_size,
                              hipStream_t stream) {
    (void)hipFuncSetAttribute((const void*)convnext_kernel, hipFuncAttributeMaxDynamicSharedMemorySize, LDS_BYTES);
    convnext_kernel<<<256, 256, LDS_BYTES, stream>>>(
        (const unsigned short*)d_in[0], (const unsigned short*)d_in[1], (const unsigned short*)d_in[2], (const unsigned short*)d_in[3],
        (const unsigned short*)d_in[4], (const unsigned short*)d_in[5], (const unsigned short*)d_in[6], (unsigned short*)d_out);
}
```

```cpp
#include <hip/hip_runtime.h>
#include <stdint.h>

constexpr int LDS_BYTES = 163584;

__device__ __forceinline__ float bf2f(unsigned short b) { return __uint_as_float((unsigned)b << 16); }
__device__ __forceinline__ unsigned short f2bf(float f) { unsigned r; asm("v_cvt_pk_bf16_f32 %0, %1, %1" : "=v"(r) : "v"(f)); return (unsigned short)r; }
__device__ __forceinline__ float rbf(float f) { return bf2f(f2bf(f)); }

__global__ __launch_bounds__(256, 1) __attribute__((aligned(4096))) void convnext_kernel(
    const unsigned short* __restrict__ x_, const unsigned short* __restrict__ dw_, const unsigned short* __restrict__ w1_,
    const unsigned short* __restrict__ b1_, const unsigned short* __restrict__ w2_, const unsigned short* __restrict__ b2_,
    const unsigned short* __restrict__ gamma_, unsigned short* __restrict__ out_) {
    const unsigned tid = threadIdx.x;
    unsigned long long ka = (unsigned long long)__builtin_amdgcn_kernarg_segment_ptr();
    asm volatile(
R"ASM(
s_load_dwordx16 s[20:35],%2,0
v_lshrrev_b32 v27,6,%1
v_and_b32 v24,63,%1
v_readfirstlane_b32 s52,v27
s_lshr_b32 s70,%0,3
v_and_b32_e64 v61,8,v24
v_lshl_add_u32 v62,s70,2,v27
v_mul_u32_u24 v61,15,v61
s_getpc_b64 s[14:15]
N_pf_%=:
v_or_b32 v61,7,v61
v_and_b32 v62,v61,v62
v_and_b32_e64 v61,7,v24
v_lshl_add_u32 v62,v62,3,v61
v_lshlrev_b32_e64 v62,7,v62
v_min_u32 v62,N_mainend_%=-N_pf_%=+2048,v62
global_load_dword v255,v62,s[14:15]
v_mul_u32_u24 v30,52,v27
v_add_u32 v30,v30,v24
v_subrev_u32_e64 v29,4,v30
v_max_i32 v29,0xc0,v29
v_min_u32_e64 v30,v30,v29
v_mul_u32_u24 v28,0xaaab,v30
s_and_b32 s64,%0,7
s_lshr_b32 s70,%0,3
v_lshrrev_b32 v28,19,v28
s_lshl_b32 s71,s64,6
s_mul_i32 s68,s70,3072
v_bfe_u32 v27,v28,2,2
s_sub_u32 s65,s71,1
s_mul_i32 s56,s65,98304
v_xor_b32 v27,v30,v27
s_add_u32 s56,s56,s68
s_cmp_lt_i32 s65,0
s_cselect_b32 s89,0x70000000,s56
v_lshlrev_b32 v27,4,v27
s_cmp_eq_u32 s70,0
s_cselect_b32 s67,0,99
s_cmp_eq_u32 s70,31
s_cselect_b32 s67,17,s67
v_mov_b32 v29,0x7fff0000
v_cmp_eq_u32_e32 vcc,s67,v28
s_mul_i32 s57,s52,832
s_add_u32 s57,s57,73728
v_cndmask_b32_e32 v6,v27,v29,vcc
s_mov_b32 s38,50331840
s_mov_b32 s39,0x00020000
s_mov_b32 s50,1728
s_mov_b32 s51,0x00020000
v_lshlrev_b32_e64 v8,4,v24
v_mul_u32_u24 v44,79,v24
v_add_u32 v45,5056,v44
v_lshrrev_b32 v44,10,v44
v_lshrrev_b32 v45,10,v45
v_sub_u32 v44,v24,v44
v_sub_u32 v45,v24,v45
v_lshlrev_b32_e64 v44,4,v44
v_add_lshl_u32 v45,v45,64,4
v_min_u32 v28,191,%1
v_lshlrev_b32_e64 v32,2,v28
v_min_u32 v34,95,%1
v_lshlrev_b32_e64 v33,1,v34
s_waitcnt lgkmcnt(0)
s_sub_u32 s36,s20,192
s_subb_u32 s37,s21,0
s_and_b32 s37,s37,0xffff
s_add_u32 m0,s57,0
s_nop 0
buffer_load_dwordx4 v6,s[36:39],s89 offen lds
s_add_u32 m0,s57,3536
s_add_u32 s89,s56,98304
buffer_load_dwordx4 v6,s[36:39],s89 offen lds
s_add_u32 m0,s57,7080
s_add_u32 s89,s56,196608
s_nop 0
buffer_load_dwordx4 v6,s[36:39],s89 offen lds
s_mov_b32 s48,s22
s_and_b32 s49,s23,0xffff
s_mov_b32 m0,12288
global_load_ushort v37,v33,s[32:33]
buffer_load_dwordx4 v44,s[48:51],0 offen lds
s_mov_b32 m0,13312
global_load_dword v35,v32,s[26:27]
buffer_load_dwordx4 v45,s[48:51],0 offen lds
global_load_ushort v36,v33,s[30:31]
v_and_b32 v27,15,v24
v_lshrrev_b32 v28,4,v24
s_mul_i32 s66,s52,192
v_min_u32_e64 v27,11,v27
v_mul_u32_u24 v28,18432,v28
v_lshl_add_u32 v27,v27,4,s66
s_mul_i32 s66,s52,18432
v_add_u32 v172,v27,v28
v_add_u32 v173,s66,v8
global_load_dwordx4 v[160:163],v172,s[24:25]
global_load_dwordx4 v[164:167],v172,s[24:25] offset:3840
global_load_dwordx4 v[168:171],v173,s[28:29]
s_and_b32 s64,s52,1
s_mul_i32 s65,s64,48
v_bfe_u32 v27,v24,3,2
v_and_b32 v28,4,v24
v_lshrrev_b32 v29,5,v24
v_add3_u32 v27,v27,v28,s65
v_lshl_add_u32 v27,v29,3,v27
v_mul_u32_u24 v27,768,v27
v_and_b32 v28,3,v24
v_xor_b32 v28,v28,v29
v_lshl_add_u32 v27,v28,4,v27
s_mul_i32 s65,s64,3072
v_add_u32_e64 v28,s65,v8
s_mul_i32 s67,s64,3080
s_add_u32 s67,s67,40896
s_add_u32 s65,s65,16256
s_cmp_lt_u32 s52,2
s_cselect_b32 s58,s65,s67
s_cselect_b64 vcc,-1,0
s_movk_i32 s59,6144
s_cselect_b32 s59,-22464,s59
s_cselect_b32 s53,11264,0
s_cselect_b32 s88,0,67584
s_cselect_b32 s87,64,0
s_cselect_b32 s69,0,0x70000000
s_mov_b32 s62,32832
s_cselect_b32 s62,88272,s62
s_cselect_b64 s[44:45],s[24:25],s[28:29]
s_add_u32 s62,s62,s58
s_and_b32 s45,s45,0xffff
v_cndmask_b32_e32 v5,v28,v27,vcc
s_add_u32 m0,s57,10616
s_add_u32 s89,s56,294912
buffer_load_dwordx4 v6,s[36:39],s89 offen lds
s_mov_b32 s46,73728
s_mov_b32 s47,0x00020000
s_mov_b32 s99,0x72800000
s_mov_b32 s98,0xffff0000
s_mov_b64 s[90:91],0
v_mov_b32 v96,0x409d3ef8
v_lshlrev_b32 v102,4,%1
v_lshlrev_b32 v104,19,%1
v_cmp_eq_u32_e64 s[94:95],0,%1
v_add_u32 v103,0x8000,v102
s_mov_b32 s4,0x3f000000
s_mov_b32 s5,0x3fc00000
s_mov_b32 s6,0x40200000
s_mov_b32 s7,0x40600000
s_mov_b32 s8,0x40900000
s_mov_b32 s9,0x40b00000
s_mov_b32 s10,0x40d00000
s_mov_b32 s11,0x40f00000
s_mov_b32 s12,0x3f80222e
s_mov_b32 s13,0x13089abf
s_mov_b32 s96,0xebefe000
v_add_u32 v28,0x10000,v104
s_add_u32 m0,s57,14144
s_add_u32 s89,s56,393216
buffer_load_dwordx4 v6,s[36:39],s89 offen lds
v_mul_f32_e64 v98,s99,v104
v_fma_f32 v100,v28,s99,-v98
v_pk_fma_f32 v[64:65],v[100:101],s[4:5],v[98:99] op_sel_hi:[0,1,0]
v_pk_fma_f32 v[66:67],v[100:101],s[6:7],v[98:99] op_sel_hi:[0,1,0]
v_pk_fma_f32 v[68:69],v[100:101],s[8:9],v[98:99] op_sel_hi:[0,1,0]
v_pk_fma_f32 v[70:71],v[100:101],s[10:11],v[98:99] op_sel_hi:[0,1,0]
v_cndmask_b32_e64 v64,v64,0,s[94:95]
v_pk_mul_f32 v[72:73],v[64:65],v[64:65]
v_pk_mul_f32 v[74:75],v[66:67],v[66:67]
v_pk_mul_f32 v[76:77],v[68:69],v[68:69]
v_pk_mul_f32 v[78:79],v[70:71],v[70:71]
v_pk_fma_f32 v[72:73],v[72:73],s[12:13],v[96:97] op_sel_hi:[1,0,0]
v_pk_fma_f32 v[74:75],v[74:75],s[12:13],v[96:97] op_sel_hi:[1,0,0]
v_pk_fma_f32 v[76:77],v[76:77],s[12:13],v[96:97] op_sel_hi:[1,0,0]
v_pk_fma_f32 v[78:79],v[78:79],s[12:13],v[96:97] op_sel_hi:[1,0,0]
s_add_u32 m0,s57,17680
s_add_u32 s89,s56,491520
buffer_load_dwordx4 v6,s[36:39],s89 offen lds
v_pk_mul_f32 v[72:73],v[72:73],v[64:65]
v_pk_mul_f32 v[74:75],v[74:75],v[66:67]
v_pk_mul_f32 v[76:77],v[76:77],v[68:69]
v_pk_mul_f32 v[78:79],v[78:79],v[70:71]
v_exp_f32 v72,v72
v_exp_f32 v73,v73
v_exp_f32 v74,v74
v_exp_f32 v75,v75
v_exp_f32 v76,v76
v_exp_f32 v77,v77
s_add_u32 m0,s57,21224
s_add_u32 s89,s56,589824
buffer_load_dwordx4 v6,s[36:39],s89 offen lds
v_exp_f32 v78,v78
v_exp_f32 v79,v79
v_pk_fma_f32 v[72:73],v[72:73],s[96:97],s[96:97] op_sel_hi:[1,0,0]
v_pk_fma_f32 v[74:75],v[74:75],s[96:97],s[96:97] op_sel_hi:[1,0,0]
v_pk_fma_f32 v[76:77],v[76:77],s[96:97],s[96:97] op_sel_hi:[1,0,0]
v_pk_fma_f32 v[78:79],v[78:79],s[96:97],s[96:97] op_sel_hi:[1,0,0]
v_rcp_f32 v72,v72
v_rcp_f32 v73,v73
v_rcp_f32 v74,v74
v_rcp_f32 v75,v75
s_add_u32 m0,s57,24760
s_add_u32 s89,s56,688128
buffer_load_dwordx4 v6,s[36:39],s89 offen lds
v_rcp_f32 v76,v76
v_rcp_f32 v77,v77
v_rcp_f32 v78,v78
v_rcp_f32 v79,v79
v_pk_mul_f32 v[72:73],v[72:73],v[64:65]
v_pk_mul_f32 v[74:75],v[74:75],v[66:67]
v_pk_mul_f32 v[76:77],v[76:77],v[68:69]
v_pk_mul_f32 v[78:79],v[78:79],v[70:71]
v_pk_fma_f32 v[64:65],v[64:65],s[12:13],v[72:73] op_sel:[0,1,0] op_sel_hi:[1,1,1]
v_pk_fma_f32 v[66:67],v[66:67],s[12:13],v[74:75] op_sel:[0,1,0] op_sel_hi:[1,1,1]
v_pk_fma_f32 v[68:69],v[68:69],s[12:13],v[76:77] op_sel:[0,1,0] op_sel_hi:[1,1,1]
s_add_u32 m0,s57,28288
s_add_u32 s89,s56,786432
buffer_load_dwordx4 v6,s[36:39],s89 offen lds
v_pk_fma_f32 v[70:71],v[70:71],s[12:13],v[78:79] op_sel:[0,1,0] op_sel_hi:[1,1,1]
v_cvt_pk_bf16_f32 v80,v72,v73
v_cvt_pk_bf16_f32 v81,v74,v75
v_cvt_pk_bf16_f32 v82,v76,v77
v_cvt_pk_bf16_f32 v83,v78,v79
v_cvt_pk_bf16_f32 v84,v64,v65
v_cvt_pk_bf16_f32 v85,v66,v67
v_cvt_pk_bf16_f32 v86,v68,v69
v_cvt_pk_bf16_f32 v87,v70,v71
ds_write_b128 v102,v[80:83] offset:0
ds_write_b128 v103,v[84:87] offset:32768
v_add_u32 v104,0x8000000,v104
v_add_u32 v28,0x10000,v104
v_mul_f32_e64 v98,s99,v104
v_fma_f32 v100,v28,s99,-v98
s_add_u32 m0,s57,31824
s_add_u32 s89,s56,884736
buffer_load_dwordx4 v6,s[36:39],s89 offen lds
v_pk_fma_f32 v[64:65],v[100:101],s[4:5],v[98:99] op_sel_hi:[0,1,0]
v_pk_fma_f32 v[66:67],v[100:101],s[6:7],v[98:99] op_sel_hi:[0,1,0]
v_pk_fma_f32 v[68:69],v[100:101],s[8:9],v[98:99] op_sel_hi:[0,1,0]
v_pk_fma_f32 v[70:71],v[100:101],s[10:11],v[98:99] op_sel_hi:[0,1,0]
v_pk_mul_f32 v[72:73],v[64:65],v[64:65]
v_pk_mul_f32 v[74:75],v[66:67],v[66:67]
v_pk_mul_f32 v[76:77],v[68:69],v[68:69]
v_pk_mul_f32 v[78:79],v[70:71],v[70:71]
v_pk_fma_f32 v[72:73],v[72:73],s[12:13],v[96:97] op_sel_hi:[1,0,0]
v_pk_fma_f32 v[74:75],v[74:75],s[12:13],v[96:97] op_sel_hi:[1,0,0]
v_pk_fma_f32 v[76:77],v[76:77],s[12:13],v[96:97] op_sel_hi:[1,0,0]
v_pk_fma_f32 v[78:79],v[78:79],s[12:13],v[96:97] op_sel_hi:[1,0,0]
v_pk_mul_f32 v[72:73],v[72:73],v[64:65]
v_pk_mul_f32 v[74:75],v[74:75],v[66:67]
v_pk_mul_f32 v[76:77],v[76:77],v[68:69]
s_add_u32 m0,s57,35368
s_add_u32 s89,s56,983040
buffer_load_dwordx4 v6,s[36:39],s89 offen lds
v_pk_mul_f32 v[78:79],v[78:79],v[70:71]
v_exp_f32 v72,v72
v_exp_f32 v73,v73
v_exp_f32 v74,v74
v_exp_f32 v75,v75
v_exp_f32 v76,v76
v_exp_f32 v77,v77
v_exp_f32_e64 v78,v78
s_add_u32 m0,s57,38904
s_add_u32 s89,s56,1081344
buffer_load_dwordx4 v6,s[36:39],s89 offen lds
v_exp_f32_e64 v79,v79
v_pk_fma_f32 v[72:73],v[72:73],s[96:97],s[96:97] op_sel_hi:[1,0,0]
v_pk_fma_f32 v[74:75],v[74:75],s[96:97],s[96:97] op_sel_hi:[1,0,0]
v_pk_fma_f32 v[76:77],v[76:77],s[96:97],s[96:97] op_sel_hi:[1,0,0]
v_pk_fma_f32 v[78:79],v[78:79],s[96:97],s[96:97] op_sel_hi:[1,0,0]
v_rcp_f32 v72,v72
v_rcp_f32 v73,v73
v_rcp_f32 v74,v74
v_rcp_f32 v75,v75
v_rcp_f32 v76,v76
v_rcp_f32 v77,v77
v_rcp_f32 v78,v78
v_rcp_f32 v79,v79
v_pk_mul_f32 v[72:73],v[72:73],v[64:65]
v_pk_mul_f32 v[74:75],v[74:75],v[66:67]
v_pk_mul_f32 v[76:77],v[76:77],v[68:69]
v_pk_mul_f32 v[78:79],v[78:79],v[70:71]
v_pk_fma_f32 v[64:65],v[64:65],s[12:13],v[72:73] op_sel:[0,1,0] op_sel_hi:[1,1,1]
v_pk_fma_f32 v[66:67],v[66:67],s[12:13],v[74:75] op_sel:[0,1,0] op_sel_hi:[1,1,1]
v_pk_fma_f32 v[68:69],v[68:69],s[12:13],v[76:77] op_sel:[0,1,0] op_sel_hi:[1,1,1]
v_pk_fma_f32 v[70:71],v[70:71],s[12:13],v[78:79] op_sel:[0,1,0] op_sel_hi:[1,1,1]
v_cvt_pk_bf16_f32 v88,v72,v73
v_cvt_pk_bf16_f32 v89,v74,v75
v_cvt_pk_bf16_f32 v90,v76,v77
v_cvt_pk_bf16_f32 v91,v78,v79
v_cvt_pk_bf16_f32 v92,v64,v65
v_cvt_pk_bf16_f32 v93,v66,v67
v_cvt_pk_bf16_f32 v94,v68,v69
v_cvt_pk_bf16_f32 v95,v70,v71
ds_write_b128 v102,v[88:91] offset:4096
ds_write_b128 v103,v[92:95] offset:36864
s_mov_b32 s96,0x8befe000
s_lshl_b32 s66,s52,2
s_mul_i32 s60,s66,3536
s_add_u32 s66,s66,s71
s_lshl2_add_u32 s16,s70,s52
s_mul_i32 s16,s16,512
v_and_b32 v25,15,v24
v_lshrrev_b32_e64 v26,4,v24
s_mul_i32 s61,s66,98304
s_add_u32 s61,s61,s68
s_mov_b32 s54,0x44ea44ea
s_mov_b32 s55,0x877f877f
s_mov_b64 s[40:41],s[34:35]
s_and_b32 s41,s41,0xffff
s_mov_b32 s42,50331648
s_mov_b32 s43,0x00020000
v_mov_b32 v41,0x70000
s_add_u32 m0,s57,42432
s_add_u32 s89,s56,1179648
buffer_load_dwordx4 v6,s[36:39],s89 offen lds
v_and_b32 v39,8,v24
v_and_b32 v40,7,v24
v_bfe_u32 v27,v24,1,1
v_sub_u32_e64 v27,v26,v27
v_cmp_gt_u32_e64 s[92:93],3,v27
v_and_b32 v28,1,v24
v_mul_u32_u24 v27,3,v27
v_sub_u32_e64 v27,v27,v28
v_cmp_eq_u32_e64 s[94:95],0,v28
v_lshrrev_b32_e64 v29,2,v25
v_mul_i32_i24 v27,208,v27
v_lshl_add_u32 v27,v29,1,v27
v_mul_u32_u24 v10,0x5555,v29
v_add_u32 v27,12288,v27
v_cndmask_b32_e64 v45,v41,v27,s[92:93]
v_cndmask_b32_e64 v44,v41,v45,s[94:95]
v_cndmask_b32_e64 v47,v45,v41,s[94:95]
s_waitcnt vmcnt(13)
s_nop 0
ds_read_u16 a96,v44 offset:0
ds_read_u16 a97,v45 offset:208
ds_read_u16 a98,v45 offset:416
ds_read_u16 a99,v47 offset:624
ds_read_u16 a100,v44 offset:8
ds_read_u16 a101,v45 offset:216
ds_read_u16 a102,v45 offset:424
ds_read_u16 a103,v47 offset:632
v_mov_b64 v[20:21],0
v_mov_b64 v[22:23],0
v_cmp_eq_u32_e64 s[92:93],v26,v29
v_lshlrev_b32_e64 v27,4,v25
v_add_u32 v27,10112,v27
v_cndmask_b32_e64 v13,v41,v27,s[92:93]
v_lshrrev_b32 v27,2,v39
v_lshrrev_b32 v28,5,v24
v_add_u32_e64 v27,v27,v28
v_mul_u32_u24 v27,3536,v27
v_lshl_or_b32 v10,v27,16,v10
v_and_b32 v27,1,v26
v_add_u32 v27,v27,v40
v_mul_u32_u24 v28,384,v27
v_lshrrev_b32_e64 v27,1,v27
v_lshl_add_u32 v28,v27,4,v28
v_mfma_f32_32x32x16_bf16 v[64:79],v[20:23],v[20:23],0
ds_read_u16 a104,v44 offset:16
ds_read_u16 a105,v45 offset:224
ds_read_u16 a106,v45 offset:432
ds_read_u16 a107,v47 offset:640
ds_read_u16 a108,v44 offset:24
ds_read_u16 a109,v45 offset:232
ds_read_u16 a110,v45 offset:440
ds_read_u16 a111,v47 offset:648
v_add_u32 v30,73728,v39
s_add_u32 m0,s57,45968
s_add_u32 s89,s56,1277952
buffer_load_dwordx4 v6,s[36:39],s89 offen lds
v_add_u32 v11,v28,v30
s_movk_i32 s66,16384
v_lshl_add_u32 v27,v40,1,1
v_lshrrev_b32 v28,2,v27
v_xor_b32 v28,v28,v26
v_mul_u32_u24 v27,192,v27
v_lshl_add_u32 v27,v28,4,v27
v_add3_u32 v12,v27,v39,s66
v_lshl_add_u32 v27,v40,1,2
v_lshrrev_b32 v28,2,v27
v_xor_b32 v28,v28,v26
v_mul_u32_u24 v27,192,v27
v_lshl_add_u32 v27,v28,4,v27
v_add3_u32 v27,v27,v39,s66
v_lshl_or_b32 v12,v27,16,v12
v_mfma_f32_32x32x16_bf16 v[80:95],v[20:23],v[20:23],0
ds_read_u16 a112,v44 offset:32
ds_read_u16 a113,v45 offset:240
ds_read_u16 a114,v45 offset:448
ds_read_u16 a115,v47 offset:656
ds_read_u16 a116,v44 offset:40
ds_read_u16 a117,v45 offset:248
ds_read_u16 a118,v45 offset:456
ds_read_u16 a119,v47 offset:664
v_mul_u32_u24 v27,24576,v39
v_mul_u32_u24 v28,384,v40
v_lshl_add_u32 v27,v26,4,v27
v_add_u32 v7,v27,v28
v_lshrrev_b32 v27,3,v25
v_bfe_u32 v28,v25,2,1
v_lshl_add_u32 v28,v27,3,v28
v_lshl_add_u32 v28,v26,1,v28
v_lshlrev_b32_e64 v28,6,v28
v_lshl_add_u32 v28,v27,4,v28
v_and_b32_e64 v29,1,v25
v_lshl_add_u32 v28,v29,5,v28
v_bfe_u32 v29,v25,1,1
v_lshl_add_u32 v28,v29,3,v28
v_xor_b32_e64 v29,16,v28
v_add_u32 v1,16256,v28
v_mfma_f32_32x32x16_bf16 v[96:111],v[20:23],v[20:23],0
ds_read_u16 a120,v44 offset:48
ds_read_u16 a121,v45 offset:256
ds_read_u16 a122,v45 offset:464
ds_read_u16 a123,v47 offset:672
ds_read_u16 a124,v44 offset:56
ds_read_u16 a125,v45 offset:264
ds_read_u16 a126,v45 offset:472
ds_read_u16 a127,v47 offset:680
v_add_u32 v2,16256,v29
s_add_u32 m0,s57,49512
s_add_u32 s89,s56,1376256
buffer_load_dwordx4 v6,s[36:39],s89 offen lds
v_lshrrev_b32_e64 v27,3,v25
v_bfe_u32 v28,v25,2,1
v_lshl_add_u32 v27,v28,1,v27
v_lshrrev_b32_e64 v28,1,v26
v_lshl_add_u32 v27,v28,2,v27
v_mul_u32_u24 v27,192,v27
v_and_b32_e64 v29,1,v26
v_mul_u32_u24 v30,3080,v29
v_add_u32 v27,v27,v30
v_and_b32 v30,3,v25
v_lshl_add_u32 v27,v30,4,v27
v_add_u32 v3,40896,v27
v_lshlrev_b32_e64 v29,6,v29
v_lshl_add_u32 v29,v28,4,v29
v_add_u32 v4,8192,v29
v_mfma_f32_32x32x16_bf16 v[112:127],v[20:23],v[20:23],0
ds_read_u16 a128,v44 offset:64
ds_read_u16 a129,v45 offset:272
ds_read_u16 a130,v45 offset:480
ds_read_u16 a131,v47 offset:688
ds_read_u16 a132,v44 offset:72
ds_read_u16 a133,v45 offset:280
ds_read_u16 a134,v45 offset:488
ds_read_u16 a135,v47 offset:696
v_lshrrev_b32_e64 v27,5,v34
v_bfe_u32 v28,v34,2,1
v_and_b32 v29,3,v34
v_lshlrev_b32 v49,7,v27
v_lshl_add_u32 v49,v28,5,v49
v_lshl_add_u32 v49,v29,2,v49
v_bfe_u32 v30,v34,3,1
v_lshl_add_u32 v49,v30,6,v49
v_bfe_u32 v30,v34,4,1
v_lshl_add_u32 v49,v30,4,v49
v_add_u32 v49,11776,v49
v_lshrrev_b32_e64 v55,6,v34
v_bfe_u32 v30,v34,3,2
v_lshl_add_u32 v55,v55,2,v30
v_and_b32_e64 v30,1,v27
v_lshl_add_u32 v55,v55,1,v30
v_mfma_f32_32x32x16_bf16 v[128:143],v[20:23],v[20:23],0
ds_read_u16 a136,v44 offset:80
ds_read_u16 a137,v45 offset:288
ds_read_u16 a138,v45 offset:496
ds_read_u16 a139,v47 offset:704
ds_read_u16 a140,v44 offset:88
ds_read_u16 a141,v45 offset:296
ds_read_u16 a142,v45 offset:504
ds_read_u16 a143,v47 offset:712
v_and_b32_e64 v30,7,v34
s_add_u32 m0,s57,53048
s_add_u32 s89,s56,1474560
buffer_load_dwordx4 v6,s[36:39],s89 offen lds
v_lshl_add_u32 v55,v55,3,v30
v_lshlrev_b32 v55,2,v55
v_lshlrev_b32 v15,2,v24
v_lshl_add_u32 v50,v27,1,v28
v_lshlrev_b32_e64 v50,4,v50
v_bfe_u32 v30,v34,3,2
v_lshl_add_u32 v50,v30,2,v50
v_add_u32 v50,v50,v29
v_lshlrev_b32 v50,4,v50
v_add_u32 v50,10112,v50
v_lshlrev_b32_e64 v51,4,v29
v_cmp_eq_u32_e64 s[94:95],1,v28
v_lshlrev_b32_e64 v48,1,v32
v_add_u32 v48,8192,v48
v_mov_b32_e64 v53,0
v_mfma_f32_32x32x16_bf16 v[144:159],v[20:23],v[20:23],0
ds_read_u16 a144,v44 offset:96
ds_read_u16 a145,v45 offset:304
ds_read_u16 a146,v45 offset:512
ds_read_u16 a147,v47 offset:720
ds_read_u16 a148,v44 offset:104
ds_read_u16 a149,v45 offset:312
ds_read_u16 a150,v45 offset:520
ds_read_u16 a151,v47 offset:728
v_lshlrev_b32 v37,16,v37
v_lshlrev_b32 v36,16,v36
v_mov_b32 v27,0x45800000
v_mov_b32 v28,0x53800000
v_cmp_nlt_f32_e64 s[90:91],|v37|,v27
v_cmp_nlt_f32_e64 vcc,|v36|,v28
v_mov_b32 v27,0x42800000
v_mul_f32 v37,0x6c800000,v37
v_mul_f32 v36,0x12800000,v36
v_rcp_f32 v56,v37
v_lshlrev_b32 v42,16,v35
v_and_b32 v43,s98,v35
s_or_b64 s[90:91],s[90:91],vcc
v_cmp_nlt_f32_e64 vcc,|v42|,v27
v_cmp_nlt_f32_e64 s[92:93],|v43|,v27
v_med3_f32 v56,v56,1.0,-1.0
ds_read_u16 a152,v44 offset:112
ds_read_u16 a153,v45 offset:320
ds_read_u16 a154,v45 offset:528
ds_read_u16 a155,v47 offset:736
ds_read_u16 a156,v44 offset:120
ds_read_u16 a157,v45 offset:328
ds_read_u16 a158,v45 offset:536
ds_read_u16 a159,v47 offset:744
v_pk_mul_f32 v[42:43],v[42:43],s[96:97] op_sel_hi:[1,0]
s_add_u32 m0,s57,56576
s_add_u32 s89,s56,1572864
buffer_load_dwordx4 v6,s[36:39],s89 offen lds
v_cvt_pk_bf16_f32 v52,v56,v53
ds_write_b64 v48,v[42:43]
ds_write_b32 v49,v36
v_lshlrev_b32 v54,16,v52
v_rcp_f32 v54,v54
v_lshlrev_b64 v[52:53],v51,v[52:53]
s_or_b64 s[90:91],s[90:91],vcc
s_or_b64 s[90:91],s[90:91],s[92:93]
v_cndmask_b32_e64 v56,v52,0,s[94:95]
v_cndmask_b32_e64 v58,0,v52,s[94:95]
v_cndmask_b32_e64 v57,v53,0,s[94:95]
v_cndmask_b32_e64 v59,0,v53,s[94:95]
ds_write_b32 v15,v7 offset:14848
ds_write_b32 v15,v13 offset:15104
s_mul_i32 s66,s52,1536
ds_read_u16 a160,v44 offset:128
ds_read_u16 a161,v45 offset:336
ds_read_u16 a162,v45 offset:544
ds_read_u16 a163,v47 offset:752
ds_read_u16 a164,v44 offset:136
ds_read_u16 a165,v45 offset:344
ds_read_u16 a166,v45 offset:552
ds_read_u16 a167,v47 offset:760
s_add_u32 m0,s57,60112
s_add_u32 s89,s56,1671168
buffer_load_dwordx4 v6,s[36:39],s89 offen lds
s_add_u32 m0,s58,18480
s_nop 0
buffer_load_dwordx4 v5,s[44:47],s69 offen offset:0 lds
s_mov_b64 exec,0
s_cmp_eq_u32 s52,0
s_cbranch_scc1 TE00_%=
s_cmp_eq_u32 s52,1
s_cbranch_scc1 TE01_%=
s_cmp_eq_u32 s52,2
s_cbranch_scc1 TE02_%=
s_branch TE03_%=
TR0_%=:
s_mov_b64 exec,-1
s_add_u32 s66,s66,16256
v_lshl_add_u32 v7,v24,3,s66
ds_write_b128 v50,v[56:59]
ds_write_b32 v55,v54 offset:14336
s_waitcnt vmcnt(16)
s_nop 0
v_and_b32 v160,0x7fff7fff,v160
v_and_b32 v161,0x7fff7fff,v161
v_and_b32 v162,0x7fff7fff,v162
v_and_b32 v163,0x7fff7fff,v163
v_and_b32 v164,0x7fff7fff,v164
v_and_b32 v165,0x7fff7fff,v165
v_and_b32 v166,0x7fff7fff,v166
v_and_b32 v167,0x7fff7fff,v167
v_and_b32 v168,0x7fff7fff,v168
v_and_b32 v169,0x7fff7fff,v169
v_and_b32 v170,0x7fff7fff,v170
ds_read_u16 a168,v44 offset:144
ds_read_u16 a169,v45 offset:352
ds_read_u16 a170,v45 offset:560
TE00_%=:
s_cbranch_execz TP2753_%=
ds_read_u16 a171,v47 offset:768
ds_read_u16 a172,v44 offset:152
ds_read_u16 a173,v45 offset:360
ds_read_u16 a174,v45 offset:568
ds_read_u16 a175,v47 offset:776
v_and_b32 v171,0x7fff7fff,v171
s_add_u32 s69,s69,s53
buffer_load_dwordx4 v5,s[44:47],s69 offen offset:1024 lds
v_pk_max_u16 v160,v160,v164
v_pk_max_u16 v161,v161,v165
v_pk_max_u16 v162,v162,v166
v_pk_max_u16 v163,v163,v167
v_mov_b64 v[164:165],v[160:161]
v_mov_b64 v[166:167],v[162:163]
s_nop 0
v_permlane32_swap_b32 v160,v164
v_permlane32_swap_b32 v161,v165
v_permlane32_swap_b32 v162,v166
v_permlane32_swap_b32_e64 v163,v167
v_pk_max_u16 v160,v160,v164
v_pk_max_u16 v161,v161,v165
v_pk_max_u16 v162,v162,v166
v_pk_max_u16 v163,v163,v167
v_mov_b64_e64 v[164:165],v[160:161]
ds_read_u16 a176,v44 offset:160
ds_read_u16 a177,v45 offset:368
ds_read_u16 a178,v45 offset:576
ds_read_u16 a179,v47 offset:784
ds_read_u16 a180,v44 offset:168
ds_read_u16 a181,v45 offset:376
ds_read_u16 a182,v45 offset:584
ds_read_u16 a183,v47 offset:792
v_mov_b64 v[166:167],v[162:163]
v_permlane16_swap_b32 v160,v164
v_permlane16_swap_b32 v161,v165
v_permlane16_swap_b32 v162,v166
v_permlane16_swap_b32_e64 v163,v167
v_pk_max_u16 v160,v160,v164
v_pk_max_u16 v161,v161,v165
v_pk_max_u16 v162,v162,v166
v_pk_max_u16 v163,v163,v167
v_pk_min_u16 v160,v160,v161
v_pk_min_u16 v162,v162,v163
v_pk_min_u16 v160,v160,v162
v_pk_min_u16 v160,v160,v160 op_sel:[0,1] op_sel_hi:[1,0]
v_pk_max_u16 v168,v168,v169
v_pk_max_u16 v170,v170,v171
s_add_u32 s69,s69,s53
s_nop 0
buffer_load_dwordx4 v5,s[44:47],s69 offen offset:2048 lds
v_pk_max_u16 v168,v168,v170
ds_read_u16 a184,v44 offset:176
ds_read_u16 a185,v45 offset:384
ds_read_u16 a186,v45 offset:592
ds_read_u16 a187,v47 offset:800
ds_read_u16 a188,v44 offset:184
ds_read_u16 a189,v45 offset:392
ds_read_u16 a190,v45 offset:600
ds_read_u16 a191,v47 offset:808
v_pk_max_u16 v168,v168,v168 op_sel:[0,1] op_sel_hi:[1,0]
v_cmp_gt_u16_e32 vcc,0x3880,v160
s_nop 0
s_or_b64 s[90:91],s[90:91],vcc
v_cmp_le_u16_e32 vcc,0x5380,v168
s_nop 0
s_or_b64 s[90:91],s[90:91],vcc
v_accvgpr_write_b32 a88,0
v_accvgpr_write_b32 a89,0
v_accvgpr_write_b32 a90,0
v_accvgpr_write_b32 a91,0
s_add_u32 m0,s58,0
s_mov_b32 s69,s87
buffer_load_dwordx4 v5,s[44:47],s69 offen offset:0 lds
s_add_u32 s69,s69,s53
buffer_load_dwordx4 v5,s[44:47],s69 offen offset:1024 lds
s_add_u32 s69,s69,s53
buffer_load_dwordx4 v5,s[44:47],s69 offen offset:2048 lds
s_waitcnt lgkmcnt(0)
s_waitcnt vmcnt(6)
s_barrier
v_add_u32_sdwa v28,s60,v10 dst_sel:DWORD dst_unused:UNUSED_PAD src0_sel:DWORD src1_sel:WORD_1
v_subrev_u32 v8,88400,v28
v_min_u32 v28,v28,v8
v_add_u32 v29,7072,v28
v_subrev_u32 v8,88400,v29
v_min_u32_e64 v29,v29,v8
v_xad_u32 v24,v11,0,v28
v_xad_u32 v25,v11,8,v29
v_xad_u32 v26,v11,16,v28
v_xad_u32 v27,v11,24,v29
ds_read_b64 a[0:1],v24 offset:0
ds_read_b64 a[2:3],v24 offset:192
ds_read_b64 a[4:5],v25 offset:0
ds_read_b64 a[6:7],v25 offset:192
ds_read_b64 a[8:9],v24 offset:8
ds_read_b64 a[10:11],v24 offset:200
ds_read_b64 a[12:13],v25 offset:8
ds_read_b64 a[14:15],v25 offset:200
ds_read_b64 a[16:17],v26 offset:0
ds_read_b64 a[18:19],v26 offset:192
ds_read_b64 a[20:21],v27 offset:0
ds_read_b64 a[22:23],v27 offset:192
s_waitcnt lgkmcnt(8)
v_smfmac_f32_16x16x64_bf16 v[64:67],a[96:99],a[0:7],v10
s_waitcnt lgkmcnt(4)
s_add_u32 m0,s58,6160
s_add_u32 s69,s69,s59
buffer_load_dwordx4 v5,s[44:47],s69 offen offset:0 lds
ds_read_b64 a[0:1],v26 offset:8
ds_read_b64 a[2:3],v26 offset:200
ds_read_b64 a[4:5],v27 offset:8
ds_read_b64 a[6:7],v27 offset:200
v_smfmac_f32_16x16x64_bf16 v[68:71],a[100:103],a[8:15],v10
s_waitcnt lgkmcnt(4)
s_add_u32 m0,s57,63656
s_add_u32 s89,s56,1769472
buffer_load_dwordx4 v6,s[36:39],s89 offen lds
ds_read_b64 a[8:9],v24 offset:64
ds_read_b64 a[10:11],v24 offset:256
ds_read_b64 a[12:13],v25 offset:64
ds_read_b64 a[14:15],v25 offset:256
v_smfmac_f32_16x16x64_bf16 v[72:75],a[104:107],a[16:23],v10
s_waitcnt lgkmcnt(4)
ds_read_b64 a[16:17],v24 offset:72
ds_read_b64 a[18:19],v24 offset:264
ds_read_b64 a[20:21],v25 offset:72
ds_read_b64 a[22:23],v25 offset:264
v_and_b32_e64 v248,8,v11
v_pk_mul_f32 v[40:41],v[64:65],v[64:65]
v_pk_mul_f32 v[42:43],v[66:67],v[66:67]
v_smfmac_f32_16x16x64_bf16 v[76:79],a[108:111],a[0:7],v10
s_waitcnt lgkmcnt(4)
ds_read_b64 a[0:1],v26 offset:64
ds_read_b64 a[2:3],v26 offset:256
ds_read_b64 a[4:5],v27 offset:64
ds_read_b64 a[6:7],v27 offset:256
v_mul_u32_u24 v248,884,v248
s_add_u32 s64,s60,3536
v_pk_add_f32 v[36:37],v[64:65],v[68:69]
v_pk_fma_f32 v[40:41],v[68:69],v[68:69],v[40:41]
v_pk_add_f32 v[38:39],v[66:67],v[70:71]
v_pk_fma_f32 v[42:43],v[70:71],v[70:71],v[42:43]
v_smfmac_f32_16x16x64_bf16 v[80:83],a[128:131],a[8:15],v10
s_waitcnt lgkmcnt(4)
s_add_u32 m0,s57,67192
s_add_u32 s89,s56,1867776
buffer_load_dwordx4 v6,s[36:39],s89 offen lds
ds_read_b64 a[8:9],v26 offset:72
ds_read_b64 a[10:11],v26 offset:264
ds_read_b64 a[12:13],v27 offset:72
ds_read_b64 a[14:15],v27 offset:264
v_add_u32 v251,s64,v248
v_pk_add_f32 v[36:37],v[36:37],v[72:73]
v_pk_fma_f32 v[40:41],v[72:73],v[72:73],v[40:41]
v_pk_add_f32 v[38:39],v[38:39],v[74:75]
v_pk_fma_f32 v[42:43],v[74:75],v[74:75],v[42:43]
v_smfmac_f32_16x16x64_bf16 v[84:87],a[132:135],a[16:23],v10
s_waitcnt lgkmcnt(4)
s_nop 0
ds_read_b64 a[16:17],v24 offset:128
ds_read_b64 a[18:19],v24 offset:320
ds_read_b64 a[20:21],v25 offset:128
ds_read_b64 a[22:23],v25 offset:320
v_subrev_u32 v8,88400,v251
v_pk_add_f32 v[36:37],v[36:37],v[76:77]
v_pk_fma_f32 v[40:41],v[76:77],v[76:77],v[40:41]
v_pk_add_f32 v[38:39],v[38:39],v[78:79]
v_pk_fma_f32 v[42:43],v[78:79],v[78:79],v[42:43]
v_smfmac_f32_16x16x64_bf16 v[88:91],a[136:139],a[0:7],v10
s_waitcnt lgkmcnt(4)
ds_read_b64 a[0:1],v24 offset:136
ds_read_b64 a[2:3],v24 offset:328
ds_read_b64 a[4:5],v25 offset:136
ds_read_b64 a[6:7],v25 offset:328
v_min_u32 v251,v251,v8
v_pk_add_f32 v[36:37],v[36:37],v[80:81]
v_pk_fma_f32 v[40:41],v[80:81],v[80:81],v[40:41]
v_pk_add_f32 v[38:39],v[38:39],v[82:83]
v_pk_fma_f32 v[42:43],v[82:83],v[82:83],v[42:43]
v_smfmac_f32_16x16x64_bf16 v[92:95],a[140:143],a[8:15],v10
s_waitcnt lgkmcnt(4)
s_add_u32 m0,s58,6160
s_add_u32 s69,s69,s53
buffer_load_dwordx4 v5,s[44:47],s69 offen offset:1024 lds
ds_read_b64 a[8:9],v26 offset:128
ds_read_b64 a[10:11],v26 offset:320
ds_read_b64 a[12:13],v27 offset:128
ds_read_b64 a[14:15],v27 offset:320
v_xad_u32 v24,v11,32,v28
v_xad_u32 v25,v11,40,v29
v_pk_add_f32 v[36:37],v[36:37],v[84:85]
v_pk_fma_f32 v[40:41],v[84:85],v[84:85],v[40:41]
v_pk_add_f32 v[38:39],v[38:39],v[86:87]
v_pk_fma_f32 v[42:43],v[86:87],v[86:87],v[42:43]
v_smfmac_f32_16x16x64_bf16 v[96:99],a[160:163],a[16:23],v10
s_waitcnt lgkmcnt(4)
ds_read_b64 a[16:17],v26 offset:136
ds_read_b64 a[18:19],v26 offset:328
ds_read_b64 a[20:21],v27 offset:136
ds_read_b64 a[22:23],v27 offset:328
v_add_u32 v250,3536,v251
v_pk_add_f32 v[36:37],v[36:37],v[88:89]
v_pk_fma_f32 v[40:41],v[88:89],v[88:89],v[40:41]
v_pk_add_f32 v[38:39],v[38:39],v[90:91]
v_pk_fma_f32 v[42:43],v[90:91],v[90:91],v[42:43]
v_smfmac_f32_16x16x64_bf16 v[100:103],a[164:167],a[0:7],v10
s_waitcnt lgkmcnt(4)
s_add_u32 m0,s57,70720
s_add_u32 s89,s56,1966080
buffer_load_dwordx4 v6,s[36:39],s89 offen lds
ds_read_b64 a[0:1],v24 offset:0
ds_read_b64 a[2:3],v24 offset:192
ds_read_b64 a[4:5],v25 offset:0
ds_read_b64 a[6:7],v25 offset:192
v_xad_u32 v26,v11,48,v28
v_xad_u32 v27,v11,56,v29
v_pk_add_f32 v[36:37],v[36:37],v[92:93]
v_pk_fma_f32 v[40:41],v[92:93],v[92:93],v[40:41]
v_pk_add_f32 v[38:39],v[38:39],v[94:95]
v_pk_fma_f32 v[42:43],v[94:95],v[94:95],v[42:43]
v_smfmac_f32_16x16x64_bf16 v[104:107],a[168:171],a[8:15],v10
s_waitcnt lgkmcnt(4)
s_nop 0
ds_read_b64 a[8:9],v24 offset:8
ds_read_b64 a[10:11],v24 offset:200
ds_read_b64 a[12:13],v25 offset:8
ds_read_b64 a[14:15],v25 offset:200
v_subrev_u32 v8,88400,v250
v_pk_add_f32 v[36:37],v[36:37],v[96:97]
v_pk_fma_f32 v[40:41],v[96:97],v[96:97],v[40:41]
v_pk_add_f32 v[38:39],v[38:39],v[98:99]
v_pk_fma_f32 v[42:43],v[98:99],v[98:99],v[42:43]
v_smfmac_f32_16x16x64_bf16 v[108:111],a[172:175],a[16:23],v10
s_waitcnt lgkmcnt(4)
ds_read_b64 a[16:17],v24 offset:64
ds_read_b64 a[18:19],v24 offset:256
ds_read_b64 a[20:21],v25 offset:64
ds_read_b64 a[22:23],v25 offset:256
v_min_u32 v250,v250,v8
v_pk_add_f32 v[36:37],v[36:37],v[100:101]
v_pk_fma_f32 v[40:41],v[100:101],v[100:101],v[40:41]
v_pk_add_f32 v[38:39],v[38:39],v[102:103]
v_pk_fma_f32 v[42:43],v[102:103],v[102:103],v[42:43]
v_smfmac_f32_16x16x64_bf16 v[112:115],a[112:115],a[0:7],v10
s_waitcnt lgkmcnt(4)
s_add_u32 m0,s58,6160
s_add_u32 s69,s69,s53
buffer_load_dwordx4 v5,s[44:47],s69 offen offset:2048 lds
ds_read_b64 a[0:1],v24 offset:72
ds_read_b64 a[2:3],v24 offset:264
ds_read_b64 a[4:5],v25 offset:72
ds_read_b64 a[6:7],v25 offset:264
v_xor_b32 v9,0x80008,v12
v_pk_add_f32 v[36:37],v[36:37],v[104:105]
v_pk_fma_f32 v[40:41],v[104:105],v[104:105],v[40:41]
v_pk_add_f32 v[38:39],v[38:39],v[106:107]
v_pk_fma_f32 v[42:43],v[106:107],v[106:107],v[42:43]
v_smfmac_f32_16x16x64_bf16 v[116:119],a[116:119],a[8:15],v10
s_waitcnt lgkmcnt(4)
ds_read_b64 a[8:9],v24 offset:128
ds_read_b64 a[10:11],v24 offset:320
ds_read_b64 a[12:13],v25 offset:128
ds_read_b64 a[14:15],v25 offset:320
v_add_u32_sdwa v248,v12,v251 dst_sel:DWORD dst_unused:UNUSED_PAD src0_sel:WORD_0 src1_sel:DWORD
v_pk_add_f32 v[36:37],v[36:37],v[108:109]
v_pk_fma_f32 v[40:41],v[108:109],v[108:109],v[40:41]
v_pk_add_f32 v[38:39],v[38:39],v[110:111]
v_pk_fma_f32 v[42:43],v[110:111],v[110:111],v[42:43]
v_smfmac_f32_16x16x64_bf16 v[120:123],a[144:147],a[16:23],v10
s_waitcnt lgkmcnt(4)
s_add_u32 m0,s57,74256
s_add_u32 s89,s56,2064384
buffer_load_dwordx4 v6,s[36:39],s89 offen lds
ds_read_b64 a[16:17],v24 offset:136
ds_read_b64 a[18:19],v24 offset:328
ds_read_b64 a[20:21],v25 offset:136
ds_read_b64 a[22:23],v25 offset:328
v_add_u32_sdwa v249,v12,v251 dst_sel:DWORD dst_unused:UNUSED_PAD src0_sel:WORD_1 src1_sel:DWORD
v_pk_add_f32 v[36:37],v[36:37],v[112:113]
v_pk_fma_f32 v[40:41],v[112:113],v[112:113],v[40:41]
v_pk_add_f32 v[38:39],v[38:39],v[114:115]
v_pk_fma_f32 v[42:43],v[114:115],v[114:115],v[42:43]
v_smfmac_f32_16x16x64_bf16 v[124:127],a[148:151],a[0:7],v10
s_waitcnt lgkmcnt(4)
ds_read_b64 a[0:1],v26 offset:0
ds_read_b64 a[2:3],v26 offset:192
ds_read_b64 a[4:5],v27 offset:0
ds_read_b64 a[6:7],v27 offset:192
v_add_u32_sdwa v251,v9,v250 dst_sel:DWORD dst_unused:UNUSED_PAD src0_sel:WORD_1 src1_sel:DWORD
v_pk_add_f32 v[36:37],v[36:37],v[116:117]
v_pk_fma_f32 v[40:41],v[116:117],v[116:117],v[40:41]
v_pk_add_f32 v[38:39],v[38:39],v[118:119]
v_pk_fma_f32 v[42:43],v[118:119],v[118:119],v[42:43]
v_smfmac_f32_16x16x64_bf16 v[128:131],a[176:179],a[8:15],v10
s_waitcnt lgkmcnt(4)
ds_read_b64 a[8:9],v26 offset:8
ds_read_b64 a[10:11],v26 offset:200
ds_read_b64 a[12:13],v27 offset:8
ds_read_b64 a[14:15],v27 offset:200
v_add_u32_sdwa v250,v9,v250 dst_sel:DWORD dst_unused:UNUSED_PAD src0_sel:WORD_0 src1_sel:DWORD
v_pk_add_f32 v[36:37],v[36:37],v[120:121]
v_pk_fma_f32 v[40:41],v[120:121],v[120:121],v[40:41]
v_pk_add_f32 v[38:39],v[38:39],v[122:123]
v_pk_fma_f32 v[42:43],v[122:123],v[122:123],v[42:43]
v_smfmac_f32_16x16x64_bf16 v[132:135],a[180:183],a[16:23],v10
s_waitcnt lgkmcnt(4)
s_add_u32 m0,s58,12320
s_add_u32 s69,s69,s59
buffer_load_dwordx4 v5,s[44:47],s69 offen offset:0 lds
ds_read_b64 a[16:17],v26 offset:64
ds_read_b64 a[18:19],v26 offset:256
ds_read_b64 a[20:21],v27 offset:64
ds_read_b64 a[22:23],v27 offset:256
v_pk_add_f32 v[36:37],v[36:37],v[124:125]
v_pk_fma_f32 v[40:41],v[124:125],v[124:125],v[40:41]
v_pk_add_f32 v[38:39],v[38:39],v[126:127]
v_pk_fma_f32 v[42:43],v[126:127],v[126:127],v[42:43]
v_smfmac_f32_16x16x64_bf16 v[136:139],a[120:123],a[0:7],v10
s_waitcnt lgkmcnt(4)
ds_read_b64 a[0:1],v26 offset:72
ds_read_b64 a[2:3],v26 offset:264
ds_read_b64 a[4:5],v27 offset:72
ds_read_b64 a[6:7],v27 offset:264
v_pk_add_f32 v[36:37],v[36:37],v[128:129]
v_pk_fma_f32 v[40:41],v[128:129],v[128:129],v[40:41]
v_pk_add_f32 v[38:39],v[38:39],v[130:131]
v_pk_fma_f32 v[42:43],v[130:131],v[130:131],v[42:43]
v_smfmac_f32_16x16x64_bf16 v[140:143],a[124:127],a[8:15],v10
s_waitcnt lgkmcnt(4)
s_add_u32 m0,s57,77800
s_add_u32 s89,s56,2162688
buffer_load_dwordx4 v6,s[36:39],s89 offen lds
ds_read_b64 a[8:9],v26 offset:128
ds_read_b64 a[10:11],v26 offset:320
ds_read_b64 a[12:13],v27 offset:128
ds_read_b64 a[14:15],v27 offset:320
v_pk_add_f32 v[36:37],v[36:37],v[132:133]
v_pk_fma_f32 v[40:41],v[132:133],v[132:133],v[40:41]
v_pk_add_f32 v[38:39],v[38:39],v[134:135]
v_pk_fma_f32 v[42:43],v[134:135],v[134:135],v[42:43]
v_smfmac_f32_16x16x64_bf16 v[144:147],a[152:155],a[16:23],v10
s_waitcnt lgkmcnt(4)
ds_read_b64 a[16:17],v26 offset:136
ds_read_b64 a[18:19],v26 offset:328
ds_read_b64 a[20:21],v27 offset:136
ds_read_b64 a[22:23],v27 offset:328
v_pk_add_f32 v[36:37],v[36:37],v[136:137]
v_pk_fma_f32 v[40:41],v[136:137],v[136:137],v[40:41]
v_pk_add_f32 v[38:39],v[38:39],v[138:139]
v_pk_fma_f32 v[42:43],v[138:139],v[138:139],v[42:43]
v_smfmac_f32_16x16x64_bf16 v[148:151],a[156:159],a[0:7],v10
s_waitcnt lgkmcnt(4)
ds_read_b64 a[24:25],v248 offset:57344
ds_read_b64 a[26:27],v248 offset:57352
ds_read_b64 a[36:37],v249 offset:57344
ds_read_b64 a[38:39],v249 offset:57352
v_pk_add_f32 v[36:37],v[36:37],v[140:141]
v_pk_fma_f32 v[40:41],v[140:141],v[140:141],v[40:41]
v_pk_add_f32 v[38:39],v[38:39],v[142:143]
v_pk_fma_f32 v[42:43],v[142:143],v[142:143],v[42:43]
v_smfmac_f32_16x16x64_bf16 v[152:155],a[184:187],a[8:15],v10
s_waitcnt lgkmcnt(4)
s_add_u32 m0,s58,12320
s_add_u32 s69,s69,s53
buffer_load_dwordx4 v5,s[44:47],s69 offen offset:1024 lds
ds_read_b64 a[28:29],v248 offset:57408
ds_read_b64 a[30:31],v248 offset:57416
ds_read_b64 a[40:41],v249 offset:57408
ds_read_b64 a[42:43],v249 offset:57416
v_pk_add_f32 v[36:37],v[36:37],v[144:145]
v_pk_fma_f32 v[40:41],v[144:145],v[144:145],v[40:41]
v_pk_add_f32 v[38:39],v[38:39],v[146:147]
v_pk_fma_f32 v[42:43],v[146:147],v[146:147],v[42:43]
v_smfmac_f32_16x16x64_bf16 v[156:159],a[188:191],a[16:23],v10
ds_read_b64 a[32:33],v248 offset:57472
ds_read_b64 a[34:35],v248 offset:57480
ds_read_b64 a[44:45],v249 offset:57472
ds_read_b64 a[46:47],v249 offset:57480
v_pk_add_f32 v[36:37],v[36:37],v[148:149]
v_pk_fma_f32 v[40:41],v[148:149],v[148:149],v[40:41]
v_pk_add_f32 v[38:39],v[38:39],v[150:151]
v_pk_fma_f32 v[42:43],v[150:151],v[150:151],v[42:43]
v_pk_add_f32 v[36:37],v[36:37],v[152:153]
v_pk_fma_f32 v[40:41],v[152:153],v[152:153],v[40:41]
v_pk_add_f32 v[38:39],v[38:39],v[154:155]
v_pk_fma_f32 v[42:43],v[154:155],v[154:155],v[42:43]
v_pk_add_f32 v[36:37],v[36:37],v[156:157]
v_pk_fma_f32 v[40:41],v[156:157],v[156:157],v[40:41]
v_pk_add_f32 v[38:39],v[38:39],v[158:159]
v_pk_fma_f32 v[42:43],v[158:159],v[158:159],v[42:43]
s_nop 0
v_permlane32_swap_b32 v36,v38
v_permlane32_swap_b32 v37,v39
v_permlane32_swap_b32 v40,v42
v_permlane32_swap_b32_e64 v41,v43
v_pk_add_f32 v[36:37],v[36:37],v[38:39]
v_pk_add_f32 v[40:41],v[40:41],v[42:43]
s_add_u32 s60,s60,56576
s_sub_u32 s64,s60,88400
s_min_u32 s60,s60,s64
v_permlane16_swap_b32 v36,v37
v_permlane16_swap_b32 v40,v41
v_add_f32 v8,v36,v37
v_add_f32_e64 v9,v40,v41
v_mul_f32 v8,0x3c2aaaab,v8
v_mul_f32 v9,0x3c2aaaab,v9
v_cmp_ngt_f32 vcc,0x5d800000,v9
v_fma_f32 v9,-v8,v8,v9
v_max_f32 v9,0,v9
s_or_b64 s[90:91],s[90:91],vcc
v_add_f32 v9,0x3727c5ac,v9
v_rsq_f32 v9,v9
s_nop 0
v_mul_f32 v36,s96,v9
v_mul_f32 v37,s96,v9
v_mul_f32 v40,-v8,v36
v_mul_f32 v41,-v8,v36
v_permlane16_swap_b32 v36,v37
s_nop 0
v_permlane16_swap_b32 v40,v41
v_mov_b64 v[38:39],v[36:37]
v_mov_b64 v[42:43],v[40:41]
s_nop 0
v_permlane32_swap_b32 v36,v38
v_permlane32_swap_b32 v37,v39
v_permlane32_swap_b32 v40,v42
v_permlane32_swap_b32 v41,v43
s_waitcnt vmcnt(10)
s_barrier
ds_read_b64_tr_b16 a[192:193],v1 offset:18480
ds_read_b64_tr_b16 a[194:195],v1 offset:19504
ds_read_b64_tr_b16 a[196:197],v1 offset:20528
ds_read_b64_tr_b16 a[198:199],v1 offset:21552
v_pk_fma_f32 v[64:65],v[64:65],v[36:37],v[40:41]
v_pk_fma_f32 v[66:67],v[66:67],v[38:39],v[42:43]
v_pk_fma_f32 v[68:69],v[68:69],v[36:37],v[40:41]
v_pk_fma_f32 v[70:71],v[70:71],v[38:39],v[42:43]
v_pk_fma_f32 v[72:73],v[72:73],v[36:37],v[40:41]
v_pk_fma_f32 v[74:75],v[74:75],v[38:39],v[42:43]
v_pk_fma_f32 v[76:77],v[76:77],v[36:37],v[40:41]
v_pk_fma_f32 v[78:79],v[78:79],v[38:39],v[42:43]
v_pk_fma_f32 v[112:113],v[112:113],v[36:37],v[40:41]
v_pk_fma_f32 v[114:115],v[114:115],v[38:39],v[42:43]
ds_read_b64_tr_b16 a[200:201],v1 offset:22576
ds_read_b64_tr_b16 a[202:203],v1 offset:23600
ds_read_b64_tr_b16 a[204:205],v2 offset:22576
ds_read_b64_tr_b16 a[206:207],v2 offset:23600
v_pk_fma_f32 v[116:117],v[116:117],v[36:37],v[40:41]
v_pk_fma_f32 v[118:119],v[118:119],v[38:39],v[42:43]
s_add_u32 m0,s57,81336
s_add_u32 s89,s56,2260992
buffer_load_dwordx4 v6,s[36:39],s89 offen lds
v_pk_fma_f32 v[136:137],v[136:137],v[36:37],v[40:41]
v_pk_fma_f32 v[138:139],v[138:139],v[38:39],v[42:43]
v_pk_fma_f32 v[140:141],v[140:141],v[36:37],v[40:41]
v_pk_fma_f32 v[142:143],v[142:143],v[38:39],v[42:43]
v_pk_fma_f32 v[80:81],v[80:81],v[36:37],v[40:41]
v_pk_fma_f32 v[82:83],v[82:83],v[38:39],v[42:43]
v_pk_fma_f32 v[84:85],v[84:85],v[36:37],v[40:41]
v_pk_fma_f32 v[86:87],v[86:87],v[38:39],v[42:43]
ds_read_b64_tr_b16 a[208:209],v2 offset:20528
ds_read_b64_tr_b16 a[210:211],v2 offset:21552
ds_read_b64_tr_b16 a[212:213],v2 offset:18480
ds_read_b64_tr_b16 a[214:215],v2 offset:19504
v_pk_fma_f32 v[88:89],v[88:89],v[36:37],v[40:41]
v_pk_fma_f32 v[90:91],v[90:91],v[38:39],v[42:43]
v_pk_fma_f32 v[92:93],v[92:93],v[36:37],v[40:41]
v_pk_fma_f32 v[94:95],v[94:95],v[38:39],v[42:43]
s_add_u32 m0,s58,12320
s_add_u32 s69,s69,s53
s_nop 0
buffer_load_dwordx4 v5,s[44:47],s69 offen offset:2048 lds
v_pk_fma_f32 v[120:121],v[120:121],v[36:37],v[40:41]
v_pk_fma_f32 v[122:123],v[122:123],v[38:39],v[42:43]
v_pk_fma_f32 v[124:125],v[124:125],v[36:37],v[40:41]
v_pk_fma_f32 v[126:127],v[126:127],v[38:39],v[42:43]
v_pk_fma_f32 v[144:145],v[144:145],v[36:37],v[40:41]
v_pk_fma_f32 v[146:147],v[146:147],v[38:39],v[42:43]
ds_read_b128 v[16:19],v4 offset:0
ds_read_b128 v[20:23],v4 offset:32
ds_read_b64 v[232:233],v7 offset:18480
ds_read_b64 v[234:235],v7 offset:18992
v_pk_fma_f32 v[148:149],v[148:149],v[36:37],v[40:41]
v_pk_fma_f32 v[150:151],v[150:151],v[38:39],v[42:43]
v_pk_fma_f32 v[96:97],v[96:97],v[36:37],v[40:41]
v_pk_fma_f32 v[98:99],v[98:99],v[38:39],v[42:43]
v_pk_fma_f32 v[100:101],v[100:101],v[36:37],v[40:41]
v_pk_fma_f32 v[102:103],v[102:103],v[38:39],v[42:43]
s_add_u32 m0,s57,84864
s_add_u32 s89,s56,2359296
buffer_load_dwordx4 v6,s[36:39],s89 offen lds
v_pk_fma_f32 v[104:105],v[104:105],v[36:37],v[40:41]
v_pk_fma_f32 v[106:107],v[106:107],v[38:39],v[42:43]
v_pk_fma_f32 v[108:109],v[108:109],v[36:37],v[40:41]
v_pk_fma_f32 v[110:111],v[110:111],v[38:39],v[42:43]
ds_read_b64 v[236:237],v7 offset:19504
v_pk_fma_f32 v[128:129],v[128:129],v[36:37],v[40:41]
v_pk_fma_f32 v[130:131],v[130:131],v[38:39],v[42:43]
v_pk_fma_f32 v[132:133],v[132:133],v[36:37],v[40:41]
v_pk_fma_f32 v[134:135],v[134:135],v[38:39],v[42:43]
v_pk_fma_f32 v[152:153],v[152:153],v[36:37],v[40:41]
v_pk_fma_f32 v[154:155],v[154:155],v[38:39],v[42:43]
v_pk_fma_f32 v[156:157],v[156:157],v[36:37],v[40:41]
v_pk_fma_f32 v[158:159],v[158:159],v[38:39],v[42:43]
v_cvt_pk_bf16_f32 v160,v64,v68
v_cvt_pk_bf16_f32 v161,v72,v76
v_cvt_pk_bf16_f32 v162,v112,v116
v_cvt_pk_bf16_f32 v163,v136,v140
v_cvt_pk_bf16_f32 v164,v65,v69
v_cvt_pk_bf16_f32 v165,v73,v77
v_cvt_pk_bf16_f32 v166,v113,v117
v_cvt_pk_bf16_f32 v167,v137,v141
v_cvt_pk_bf16_f32 v176,v80,v84
v_cvt_pk_bf16_f32 v177,v88,v92
v_cvt_pk_bf16_f32 v178,v120,v124
v_cvt_pk_bf16_f32 v179,v144,v148
v_cvt_pk_bf16_f32 v180,v81,v85
v_cvt_pk_bf16_f32 v181,v89,v93
v_cvt_pk_bf16_f32 v182,v121,v125
v_cvt_pk_bf16_f32 v183,v145,v149
v_cvt_pk_bf16_f32 v192,v96,v100
v_cvt_pk_bf16_f32 v193,v104,v108
v_cvt_pk_bf16_f32 v194,v128,v132
v_cvt_pk_bf16_f32 v195,v152,v156
v_cvt_pk_bf16_f32 v196,v97,v101
v_cvt_pk_bf16_f32 v197,v105,v109
v_cvt_pk_bf16_f32 v198,v129,v133
v_cvt_pk_bf16_f32 v199,v153,v157
s_waitcnt lgkmcnt(0)
v_or3_b32 v252,v232,v233,0
v_or3_b32 v252,v234,v235,v252
v_or3_b32 v252,v236,v237,v252
v_mfma_f32_16x16x32_bf16 v[44:47],a[192:195],v[160:163],v[16:19]
ds_read_b128 a[48:51],v13 offset:0
v_cvt_pk_bf16_f32 v168,v66,v70
v_cvt_pk_bf16_f32 v169,v74,v78
v_mfma_f32_16x16x32_bf16 v[44:47],a[196:199],v[176:179],v[44:47]
ds_read_b128 v[208:211],v4 offset:3584
v_cvt_pk_bf16_f32 v170,v114,v118
v_cvt_pk_bf16_f32 v171,v138,v142
v_mfma_f32_16x16x32_bf16 v[44:47],a[200:203],v[192:195],v[44:47]
ds_read_b128 a[52:55],v13 offset:256
v_cvt_pk_bf16_f32 v172,v67,v71
v_cvt_pk_bf16_f32 v173,v75,v79
v_mfma_f32_16x16x32_bf16 v[48:51],a[204:207],v[192:195],v[20:23]
ds_read_b128 v[212:215],v4 offset:3616
v_cvt_pk_bf16_f32 v174,v115,v119
v_cvt_pk_bf16_f32 v175,v139,v143
v_mfma_f32_16x16x32_bf16 v[48:51],a[208:211],v[176:179],v[48:51]
ds_read_b128 a[56:59],v13 offset:512
v_cvt_pk_bf16_f32 v184,v82,v86
v_cvt_pk_bf16_f32 v185,v90,v94
v_mfma_f32_16x16x32_bf16 v[48:51],a[212:215],v[160:163],v[48:51]
ds_read_b128 v[216:219],v4 offset:3712
v_cvt_pk_bf16_f32 v186,v122,v126
v_cvt_pk_bf16_f32 v187,v146,v150
v_mfma_f32_16x16x32_bf16 v[52:55],a[212:215],v[164:167],v[20:23]
TE01_%=:
s_cbranch_execz TP3285_%=
ds_read_b128 a[60:63],v13 offset:768
v_cvt_pk_bf16_f32 v188,v83,v87
v_cvt_pk_bf16_f32 v189,v91,v95
v_mfma_f32_16x16x32_bf16 v[52:55],a[208:211],v[180:183],v[52:55]
ds_read_b128 v[220:223],v4 offset:3744
v_cvt_pk_bf16_f32 v190,v123,v127
v_cvt_pk_bf16_f32 v191,v147,v151
v_mfma_f32_16x16x32_bf16 v[52:55],a[204:207],v[196:199],v[52:55]
ds_read_b128 a[64:67],v13 offset:1024
v_cvt_pk_bf16_f32 v200,v98,v102
v_cvt_pk_bf16_f32 v201,v106,v110
v_mfma_f32_16x16x32_bf16 v[56:59],a[200:203],v[196:199],v[16:19]
ds_read_b128 v[224:227],v4 offset:3840
v_cvt_pk_bf16_f32 v202,v130,v134
v_cvt_pk_bf16_f32 v203,v154,v158
v_mfma_f32_16x16x32_bf16 v[56:59],a[196:199],v[180:183],v[56:59]
ds_read_b128 a[68:71],v13 offset:1280
v_cvt_pk_bf16_f32 v204,v99,v103
v_cvt_pk_bf16_f32 v205,v107,v111
v_mfma_f32_16x16x32_bf16 v[56:59],a[192:195],v[164:167],v[56:59]
ds_read_b128 v[228:231],v4 offset:3872
v_cvt_pk_bf16_f32 v206,v131,v135
v_cvt_pk_bf16_f32 v207,v155,v159
v_mfma_f32_16x16x32_bf16 v[60:63],a[192:195],v[168:171],v[16:19]
ds_read_b64 v[14:15],v7 offset:0
ds_read_b64 a[0:1],v250 offset:57344
v_lshlrev_b32_sdwa v44,1,v44 dst_sel:DWORD dst_unused:UNUSED_PAD src0_sel:DWORD src1_sel:WORD_1
v_mfma_f32_16x16x32_bf16 v[60:63],a[196:199],v[184:187],v[60:63]
ds_read_b64 a[2:3],v250 offset:57352
ds_read_b64 a[12:13],v251 offset:57344
v_fma_mixlo_f16 v8,v53,s99,0
v_mfma_f32_16x16x32_bf16 v[60:63],a[200:203],v[200:203],v[60:63]
ds_read_b64 a[14:15],v251 offset:57352
ds_read_b64 a[4:5],v250 offset:57408
v_lshlrev_b32_sdwa v46,1,v46 dst_sel:DWORD dst_unused:UNUSED_PAD src0_sel:DWORD src1_sel:WORD_1
v_mfma_f32_16x16x32_bf16 v[32:35],a[204:207],v[200:203],v[20:23]
ds_read_b64 a[6:7],v250 offset:57416
ds_read_b64 a[16:17],v251 offset:57408
v_fma_mixhi_f16 v8,v55,s99,0
v_mfma_f32_16x16x32_bf16 v[32:35],a[208:211],v[184:187],v[32:35]
ds_read_b64 a[18:19],v251 offset:57416
ds_read_b64 a[8:9],v250 offset:57472
v_lshlrev_b32_sdwa v45,1,v45 dst_sel:DWORD dst_unused:UNUSED_PAD src0_sel:DWORD src1_sel:WORD_1
v_pk_fma_f16 v9,v8,v8,s54
v_mfma_f32_16x16x32_bf16 v[32:35],a[212:215],v[168:171],v[32:35]
s_waitcnt lgkmcnt(9)
ds_read_b64 a[10:11],v250 offset:57480
ds_read_b64 a[20:21],v251 offset:57472
v_or3_b32 v252,v14,v15,v252
v_lshlrev_b32_sdwa v47,1,v47 dst_sel:DWORD dst_unused:UNUSED_PAD src0_sel:DWORD src1_sel:WORD_1
v_mfma_f32_16x16x32_bf16 v[36:39],a[212:215],v[172:175],v[20:23]
ds_read_b64 v[14:15],v7 offset:512
ds_read_b128 v[20:23],v4 offset:128
v_pk_mul_f16 v9,v8,v9
v_mfma_f32_16x16x32_bf16 v[36:39],a[208:211],v[188:191],v[36:39]
ds_read_b64 a[22:23],v251 offset:57480
v_exp_f16_e64 v8,v9
v_lshlrev_b32_sdwa v48,1,v48 dst_sel:DWORD dst_unused:UNUSED_PAD src0_sel:DWORD src1_sel:WORD_1
v_mfma_f32_16x16x32_bf16 v[36:39],a[204:207],v[204:207],v[36:39]
ds_read_b64_tr_b16 a[216:217],v1 offset:0
ds_read_b64_tr_b16 a[218:219],v1 offset:1024
v_mfma_f32_16x16x32_bf16 v[40:43],a[200:203],v[204:207],v[16:19]
ds_read_b128 v[16:19],v4 offset:160
ds_read_b64_tr_b16 a[220:221],v1 offset:2048
v_lshlrev_b32_sdwa v50,1,v50 dst_sel:DWORD dst_unused:UNUSED_PAD src0_sel:DWORD src1_sel:WORD_1
v_mfma_f32_16x16x32_bf16 v[40:43],a[196:199],v[188:191],v[40:43]
ds_read_b64_tr_b16 a[222:223],v1 offset:3072
ds_read_u16 v44,v44
v_lshlrev_b32_sdwa v49,1,v49 dst_sel:DWORD dst_unused:UNUSED_PAD src0_sel:DWORD src1_sel:WORD_1
v_mfma_f32_16x16x32_bf16 v[40:43],a[192:195],v[172:175],v[40:43]
s_waitcnt lgkmcnt(8)
ds_read_u16_d16_hi v46,v46
ds_read_u16 v45,v45
v_or3_b32 v252,v14,v15,v252
v_mfma_f32_16x16x32_bf16 v[136:139],a[48:51],a[24:27],v[208:211]
ds_read_b64 v[14:15],v7 offset:1024
ds_read_u16_d16_hi v47,v47
v_exp_f16_sdwa v8,v9 dst_sel:WORD_1 dst_unused:UNUSED_PRESERVE src0_sel:WORD_1
v_lshlrev_b32_sdwa v51,1,v51 dst_sel:DWORD dst_unused:UNUSED_PAD src0_sel:DWORD src1_sel:WORD_1
v_mfma_f32_16x16x32_bf16 v[140:143],a[48:51],a[36:39],v[208:211]
ds_read_b64_tr_b16 a[72:73],v1 offset:4096
ds_read_b64_tr_b16 a[74:75],v1 offset:5120
v_lshlrev_b32_sdwa v56,1,v56 dst_sel:DWORD dst_unused:UNUSED_PAD src0_sel:DWORD src1_sel:WORD_1
v_mfma_f32_16x16x32_bf16 v[68:71],a[52:55],a[36:39],v[212:215]
ds_read_u16 v48,v48
ds_read_u16_d16_hi v50,v50
v_pk_fma_f16 v8,v8,s55,s55
v_mfma_f32_16x16x32_bf16 v[64:67],a[52:55],a[24:27],v[212:215]
ds_read_u16 v49,v49
v_rcp_f16 v9,v8
v_lshlrev_b32_sdwa v58,1,v58 dst_sel:DWORD dst_unused:UNUSED_PAD src0_sel:DWORD src1_sel:WORD_1
v_mfma_f32_16x16x32_bf16 v[144:147],a[56:59],a[28:31],v[216:219]
ds_read_u16_d16_hi v51,v51
ds_read_b64_tr_b16 a[76:77],v2 offset:4096
v_mfma_f32_16x16x32_bf16 v[148:151],a[56:59],a[40:43],v[216:219]
s_waitcnt lgkmcnt(8)
ds_read_b64_tr_b16 a[78:79],v2 offset:5120
ds_read_b64_tr_b16 a[80:81],v2 offset:2048
v_or3_b32 v252,v14,v15,v252
v_lshlrev_b32_sdwa v57,1,v57 dst_sel:DWORD dst_unused:UNUSED_PAD src0_sel:DWORD src1_sel:WORD_1
v_mfma_f32_16x16x32_bf16 v[84:87],a[60:63],a[40:43],v[220:223]
ds_read_b64_tr_b16 a[82:83],v2 offset:3072
ds_read_u16 v56,v56
v_lshlrev_b32_sdwa v59,1,v59 dst_sel:DWORD dst_unused:UNUSED_PAD src0_sel:DWORD src1_sel:WORD_1
v_mfma_f32_16x16x32_bf16 v[80:83],a[60:63],a[28:31],v[220:223]
ds_read_u16_d16_hi v58,v58
v_rcp_f16_sdwa v9,v8 dst_sel:WORD_1 dst_unused:UNUSED_PRESERVE src0_sel:WORD_1
v_mfma_f32_16x16x32_bf16 v[152:155],a[64:67],a[32:35],v[224:227]
ds_read_u16 v57,v57
ds_read_u16_d16_hi v59,v59
v_lshlrev_b32_sdwa v52,1,v52 dst_sel:DWORD dst_unused:UNUSED_PAD src0_sel:DWORD src1_sel:WORD_1
v_mfma_f32_16x16x32_bf16 v[156:159],a[64:67],a[44:47],v[224:227]
ds_read_b64_tr_b16 a[84:85],v2 offset:0
ds_read_b64_tr_b16 a[86:87],v2 offset:1024
v_lshlrev_b32_sdwa v54,1,v54 dst_sel:DWORD dst_unused:UNUSED_PAD src0_sel:DWORD src1_sel:WORD_1
v_mfma_f32_16x16x32_bf16 v[100:103],a[68:71],a[44:47],v[228:231]
s_waitcnt vmcnt(7)
ds_read_u16 v52,v52
ds_read_u16_d16_hi v54,v54
v_fma_mix_f32 v53,v53,v9,0 op_sel_hi:[0,1,0]
v_mfma_f32_16x16x32_bf16 v[96:99],a[68:71],a[32:35],v[228:231]
s_barrier
ds_read_b64_tr_b16 a[224:225],v3 offset:0
ds_read_b64_tr_b16 a[226:227],v3 offset:1536
v_fma_mix_f32 v55,v55,v9,0 op_sel:[0,1,0] op_sel_hi:[0,1,0]
v_mfma_f32_16x16x32_bf16 v[24:27],a[216:219],v[160:163],v[20:23]
s_add_u32 m0,s57,0
ds_read_b64_tr_b16 a[228:229],v3 offset:8
ds_read_b64_tr_b16 a[230:231],v3 offset:1544
v_lshlrev_b32_sdwa v60,1,v60 dst_sel:DWORD dst_unused:UNUSED_PAD src0_sel:DWORD src1_sel:WORD_1
v_mfma_f32_16x16x32_bf16 v[24:27],a[220:223],v[176:179],v[24:27]
s_waitcnt lgkmcnt(15)
ds_read_b64_tr_b16 a[232:233],v3 offset:64
ds_read_b64_tr_b16 a[234:235],v3 offset:1600
v_lshl_add_u64 v[44:45],v[44:45],0,v[46:47]
v_mfma_f32_16x16x32_bf16 v[24:27],a[72:75],v[192:195],v[24:27]
s_waitcnt lgkmcnt(15)
ds_read_b64_tr_b16 a[236:237],v3 offset:72
ds_read_b64_tr_b16 a[238:239],v3 offset:1608
v_lshl_add_u64 v[46:47],v[48:49],0,v[50:51]
v_mfma_f32_16x16x32_bf16 v[28:31],a[76:79],v[192:195],v[16:19]
s_waitcnt lgkmcnt(15)
ds_read_b64_tr_b16 a[240:241],v3 offset:128
ds_read_b64_tr_b16 a[242:243],v3 offset:1664
v_lshlrev_b32_sdwa v62,1,v62 dst_sel:DWORD dst_unused:UNUSED_PAD src0_sel:DWORD src1_sel:WORD_1
v_mfma_f32_16x16x32_bf16 v[28:31],a[80:83],v[176:179],v[28:31]
s_waitcnt lgkmcnt(12)
ds_read_b64_tr_b16 a[244:245],v3 offset:136
v_lshl_add_u64 v[48:49],v[56:57],0,v[58:59]
v_mfma_f32_16x16x32_bf16 v[28:31],a[84:87],v[160:163],v[28:31]
s_add_u32 s89,s56,2457600
ds_read_b64_tr_b16 a[246:247],v3 offset:1672
v_lshlrev_b32_sdwa v61,1,v61 dst_sel:DWORD dst_unused:UNUSED_PAD src0_sel:DWORD src1_sel:WORD_1
v_mfma_f32_16x16x32_bf16 v[56:59],a[84:87],v[164:167],v[16:19]
s_waitcnt lgkmcnt(12)
buffer_load_dwordx4 v6,s[36:39],s89 offen lds
v_or_b32_e64 v50,v52,v54
v_mfma_f32_16x16x32_bf16 v[56:59],a[80:83],v[180:183],v[56:59]
s_add_u32 m0,s58,18480
ds_read_u16 v60,v60
v_cvt_pk_bf16_f32 v51,v53,v55
v_lshlrev_b32_sdwa v63,1,v63 dst_sel:DWORD dst_unused:UNUSED_PAD src0_sel:DWORD src1_sel:WORD_1
v_mfma_f32_16x16x32_bf16 v[56:59],a[76:79],v[196:199],v[56:59]
s_add_u32 s69,s69,s59
ds_read_u16_d16_hi v62,v62
v_lshlrev_b32_sdwa v32,1,v32 dst_sel:DWORD dst_unused:UNUSED_PAD src0_sel:DWORD src1_sel:WORD_1
v_mfma_f32_16x16x32_bf16 v[52:55],a[72:75],v[196:199],v[20:23]
buffer_load_dwordx4 v5,s[44:47],s69 offen offset:0 lds
v_lshlrev_b32_sdwa v34,1,v34 dst_sel:DWORD dst_unused:UNUSED_PAD src0_sel:DWORD src1_sel:WORD_1
v_mfma_f32_16x16x32_bf16 v[52:55],a[220:223],v[180:183],v[52:55]
s_add_u32 s69,s69,s53
ds_read_u16 v61,v61
v_lshlrev_b32_sdwa v33,1,v33 dst_sel:DWORD dst_unused:UNUSED_PAD src0_sel:DWORD src1_sel:WORD_1
v_mfma_f32_16x16x32_bf16 v[52:55],a[216:219],v[164:167],v[52:55]
s_waitcnt lgkmcnt(7)
buffer_load_dwordx4 v5,s[44:47],s69 offen offset:1024 lds
ds_read_u16_d16_hi v63,v63
v_mfma_f32_16x16x32_bf16 v[136:139],a[224:227],v[44:47],v[136:139]
s_add_u32 s69,s69,s53
ds_read_u16 v32,v32
ds_read_u16_d16_hi v34,v34
v_mfma_f32_16x16x32_bf16 v[140:143],a[224:227],v[48:51],v[140:143]
buffer_load_dwordx4 v5,s[44:47],s69 offen offset:2048 lds
v_lshlrev_b32_sdwa v35,1,v35 dst_sel:DWORD dst_unused:UNUSED_PAD src0_sel:DWORD src1_sel:WORD_1
v_mfma_f32_16x16x32_bf16 v[68:71],a[228:231],v[48:51],v[68:71]
ds_read_u16 v33,v33
ds_read_u16_d16_hi v35,v35
v_mfma_f32_16x16x32_bf16 v[64:67],a[228:231],v[44:47],v[64:67]
ds_read_b64_tr_b16 a[248:249],v3 offset:6160
v_lshlrev_b32_sdwa v40,1,v40 dst_sel:DWORD dst_unused:UNUSED_PAD src0_sel:DWORD src1_sel:WORD_1
v_mfma_f32_16x16x32_bf16 v[144:147],a[232:235],v[44:47],v[144:147]
s_add_u32 m0,s57,3544
ds_read_u16 v40,v40
ds_read_b64_tr_b16 a[250:251],v3 offset:7696
v_mfma_f32_16x16x32_bf16 v[148:151],a[232:235],v[48:51],v[148:151]
s_add_u32 s89,s56,2555904
ds_read_b64_tr_b16 a[252:253],v3 offset:6168
v_lshlrev_b32_sdwa v42,1,v42 dst_sel:DWORD dst_unused:UNUSED_PAD src0_sel:DWORD src1_sel:WORD_1
v_mfma_f32_16x16x32_bf16 v[84:87],a[236:239],v[48:51],v[84:87]
buffer_load_dwordx4 v6,s[36:39],s89 offen lds
ds_read_u16_d16_hi v42,v42
v_mfma_f32_16x16x32_bf16 v[80:83],a[236:239],v[44:47],v[80:83]
s_waitcnt lgkmcnt(10)
ds_read_b64_tr_b16 a[254:255],v3 offset:7704
v_lshlrev_b32_sdwa v41,1,v41 dst_sel:DWORD dst_unused:UNUSED_PAD src0_sel:DWORD src1_sel:WORD_1
v_mfma_f32_16x16x32_bf16 v[152:155],a[240:243],v[44:47],v[152:155]
ds_read_u16 v41,v41
v_lshlrev_b32_sdwa v43,1,v43 dst_sel:DWORD dst_unused:UNUSED_PAD src0_sel:DWORD src1_sel:WORD_1
v_mfma_f32_16x16x32_bf16 v[156:159],a[240:243],v[48:51],v[156:159]
ds_read_u16_d16_hi v43,v43
v_lshlrev_b32_sdwa v36,1,v36 dst_sel:DWORD dst_unused:UNUSED_PAD src0_sel:DWORD src1_sel:WORD_1
v_mfma_f32_16x16x32_bf16 v[100:103],a[244:247],v[48:51],v[100:103]
ds_read_u16 v36,v36
v_lshlrev_b32_sdwa v38,1,v38 dst_sel:DWORD dst_unused:UNUSED_PAD src0_sel:DWORD src1_sel:WORD_1
v_mfma_f32_16x16x32_bf16 v[96:99],a[244:247],v[44:47],v[96:99]
ds_read_u16_d16_hi v38,v38
v_lshlrev_b32_sdwa v37,1,v37 dst_sel:DWORD dst_unused:UNUSED_PAD src0_sel:DWORD src1_sel:WORD_1
v_mfma_f32_16x16x32_bf16 v[112:115],a[48:51],a[0:3],v[208:211]
ds_read_u16 v37,v37
v_lshlrev_b32_sdwa v39,1,v39 dst_sel:DWORD dst_unused:UNUSED_PAD src0_sel:DWORD src1_sel:WORD_1
v_mfma_f32_16x16x32_bf16 v[116:119],a[48:51],a[12:15],v[208:211]
ds_read_u16_d16_hi v39,v39
ds_read_b64_tr_b16 a[92:93],v3 offset:6224
v_mfma_f32_16x16x32_bf16 v[76:79],a[52:55],a[12:15],v[212:215]
ds_read_b64_tr_b16 a[94:95],v3 offset:7760
v_mfma_f32_16x16x32_bf16 v[72:75],a[52:55],a[0:3],v[212:215]
ds_read_b64_tr_b16 a[48:49],v3 offset:6232
v_mfma_f32_16x16x32_bf16 v[120:123],a[56:59],a[4:7],v[216:219]
ds_read_b64_tr_b16 a[50:51],v3 offset:7768
v_mfma_f32_16x16x32_bf16 v[124:127],a[56:59],a[16:19],v[216:219]
ds_read_b64_tr_b16 a[56:57],v3 offset:6288
v_mfma_f32_16x16x32_bf16 v[92:95],a[60:63],a[16:19],v[220:223]
ds_read_b64_tr_b16 a[58:59],v3 offset:7824
v_mfma_f32_16x16x32_bf16 v[88:91],a[60:63],a[4:7],v[220:223]
v_mfma_f32_16x16x32_bf16 v[128:131],a[64:67],a[8:11],v[224:227]
v_mfma_f32_16x16x32_bf16 v[132:135],a[64:67],a[20:23],v[224:227]
ds_read_b64_tr_b16 a[64:65],v3 offset:6296
v_mfma_f32_16x16x32_bf16 v[108:111],a[68:71],a[20:23],v[228:231]
ds_read_b64_tr_b16 a[66:67],v3 offset:7832
v_mfma_f32_16x16x32_bf16 v[104:107],a[68:71],a[8:11],v[228:231]
s_add_u32 m0,s57,7080
v_mfma_f32_16x16x32_bf16 v[48:51],a[216:219],v[168:171],v[20:23]
s_add_u32 s89,s56,2654208
ds_read_b64 v[14:15],v7 offset:6160
ds_read_b64_tr_b16 a[212:213],v1 offset:6160
v_lshlrev_b32_sdwa v24,1,v24 dst_sel:DWORD dst_unused:UNUSED_PAD src0_sel:DWORD src1_sel:WORD_1
v_mfma_f32_16x16x32_bf16 v[48:51],a[220:223],v[184:187],v[48:51]
s_waitcnt lgkmcnt(6)
buffer_load_dwordx4 v6,s[36:39],s89 offen lds
ds_read_b64_tr_b16 a[214:215],v1 offset:7184
v_lshl_add_u64 v[60:61],v[60:61],0,v[62:63]
v_fma_mixlo_f16 v8,v57,s99,0
v_mfma_f32_16x16x32_bf16 v[48:51],a[72:75],v[200:203],v[48:51]
ds_read_b64_tr_b16 a[208:209],v1 offset:8208
ds_read_b64_tr_b16 a[210:211],v1 offset:9232
v_lshl_add_u64 v[62:63],v[32:33],0,v[34:35]
v_lshlrev_b32_sdwa v26,1,v26 dst_sel:DWORD dst_unused:UNUSED_PAD src0_sel:DWORD src1_sel:WORD_1
v_mfma_f32_16x16x32_bf16 v[44:47],a[76:79],v[200:203],v[16:19]
ds_read_u16 v24,v24
ds_read_u16_d16_hi v26,v26
v_fma_mixhi_f16 v8,v59,s99,0
v_mfma_f32_16x16x32_bf16 v[44:47],a[80:83],v[184:187],v[44:47]
ds_read_b64_tr_b16 a[204:205],v1 offset:10256
ds_read_b64_tr_b16 a[206:207],v1 offset:11280
v_lshl_add_u64 v[32:33],v[40:41],0,v[42:43]
v_lshlrev_b32_sdwa v25,1,v25 dst_sel:DWORD dst_unused:UNUSED_PAD src0_sel:DWORD src1_sel:WORD_1
v_pk_fma_f16 v9,v8,v8,s54
v_mfma_f32_16x16x32_bf16 v[44:47],a[84:87],v[168:171],v[44:47]
s_waitcnt lgkmcnt(8)
ds_read_u16 v25,v25
ds_read_b64_tr_b16 a[200:201],v2 offset:10256
v_or3_b32 v252,v14,v15,v252
v_lshlrev_b32_sdwa v27,1,v27 dst_sel:DWORD dst_unused:UNUSED_PAD src0_sel:DWORD src1_sel:WORD_1
v_mfma_f32_16x16x32_bf16 v[40:43],a[84:87],v[172:175],v[16:19]
s_nop 0
ds_read_b64 v[14:15],v7 offset:6672
ds_read_b128 v[16:19],v4 offset:256
v_pk_mul_f16 v9,v8,v9
v_mfma_f32_16x16x32_bf16 v[40:43],a[80:83],v[188:191],v[40:43]
ds_read_u16_d16_hi v27,v27
ds_read_b64_tr_b16 a[202:203],v2 offset:11280
v_lshl_add_u64 v[34:35],v[36:37],0,v[38:39]
v_lshlrev_b32_sdwa v28,1,v28 dst_sel:DWORD dst_unused:UNUSED_PAD src0_sel:DWORD src1_sel:WORD_1
v_mfma_f32_16x16x32_bf16 v[40:43],a[76:79],v[204:207],v[40:43]
ds_read_u16 v28,v28
v_exp_f16 v8,v9
v_mfma_f32_16x16x32_bf16 v[36:39],a[72:75],v[204:207],v[20:23]
ds_read_b128 v[20:23],v4 offset:288
ds_read_b64_tr_b16 a[196:197],v2 offset:8208
v_lshlrev_b32_sdwa v30,1,v30 dst_sel:DWORD dst_unused:UNUSED_PAD src0_sel:DWORD src1_sel:WORD_1
v_mfma_f32_16x16x32_bf16 v[36:39],a[220:223],v[188:191],v[36:39]
ds_read_u16_d16_hi v30,v30
v_lshlrev_b32_sdwa v29,1,v29 dst_sel:DWORD dst_unused:UNUSED_PAD src0_sel:DWORD src1_sel:WORD_1
v_mfma_f32_16x16x32_bf16 v[36:39],a[216:219],v[172:175],v[36:39]
s_waitcnt lgkmcnt(7)
ds_read_u16 v29,v29
v_or3_b32 v252,v14,v15,v252
v_mfma_f32_16x16x32_bf16 v[112:115],a[224:227],v[60:63],v[112:115]
s_add_u32 m0,s57,10608
ds_read_b64 v[14:15],v7 offset:7184
v_exp_f16_sdwa v8,v9 dst_sel:WORD_1 dst_unused:UNUSED_PRESERVE src0_sel:WORD_1
v_lshlrev_b32_sdwa v31,1,v31 dst_sel:DWORD dst_unused:UNUSED_PAD src0_sel:DWORD src1_sel:WORD_1
v_mfma_f32_16x16x32_bf16 v[116:119],a[224:227],v[32:35],v[116:119]
s_add_u32 s89,s56,2752512
ds_read_u16_d16_hi v31,v31
v_lshlrev_b32_sdwa v52,1,v52 dst_sel:DWORD dst_unused:UNUSED_PAD src0_sel:DWORD src1_sel:WORD_1
v_mfma_f32_16x16x32_bf16 v[76:79],a[228:231],v[32:35],v[76:79]
buffer_load_dwordx4 v6,s[36:39],s89 offen lds
ds_read_b64_tr_b16 a[198:199],v2 offset:9232
v_pk_fma_f16 v8,v8,s55,s55
v_mfma_f32_16x16x32_bf16 v[72:75],a[228:231],v[60:63],v[72:75]
v_rcp_f16 v9,v8
v_lshlrev_b32_sdwa v54,1,v54 dst_sel:DWORD dst_unused:UNUSED_PAD src0_sel:DWORD src1_sel:WORD_1
v_mfma_f32_16x16x32_bf16 v[120:123],a[232:235],v[60:63],v[120:123]
ds_read_u16 v52,v52
v_lshlrev_b32_sdwa v53,1,v53 dst_sel:DWORD dst_unused:UNUSED_PAD src0_sel:DWORD src1_sel:WORD_1
v_mfma_f32_16x16x32_bf16 v[124:127],a[232:235],v[32:35],v[124:127]
s_waitcnt lgkmcnt(3)
ds_read_u16_d16_hi v54,v54
v_or3_b32 v252,v14,v15,v252
v_mfma_f32_16x16x32_bf16 v[92:95],a[236:239],v[32:35],v[92:95]
ds_read_u16 v53,v53
v_lshlrev_b32_sdwa v55,1,v55 dst_sel:DWORD dst_unused:UNUSED_PAD src0_sel:DWORD src1_sel:WORD_1
v_mfma_f32_16x16x32_bf16 v[88:91],a[236:239],v[60:63],v[88:91]
v_rcp_f16_sdwa v9,v8 dst_sel:WORD_1 dst_unused:UNUSED_PRESERVE src0_sel:WORD_1
v_mfma_f32_16x16x32_bf16 v[128:131],a[240:243],v[60:63],v[128:131]
ds_read_u16_d16_hi v55,v55
v_lshlrev_b32_sdwa v56,1,v56 dst_sel:DWORD dst_unused:UNUSED_PAD src0_sel:DWORD src1_sel:WORD_1
v_mfma_f32_16x16x32_bf16 v[132:135],a[240:243],v[32:35],v[132:135]
ds_read_b64_tr_b16 a[192:193],v2 offset:6160
ds_read_b64_tr_b16 a[194:195],v2 offset:7184
v_lshlrev_b32_sdwa v58,1,v58 dst_sel:DWORD dst_unused:UNUSED_PAD src0_sel:DWORD src1_sel:WORD_1
v_mfma_f32_16x16x32_bf16 v[108:111],a[244:247],v[32:35],v[108:111]
ds_read_u16 v56,v56
v_fma_mix_f32 v57,v57,v9,0 op_sel_hi:[0,1,0]
v_mfma_f32_16x16x32_bf16 v[104:107],a[244:247],v[60:63],v[104:107]
ds_read_u16_d16_hi v58,v58
v_fma_mix_f32 v59,v59,v9,0 op_sel:[0,1,0] op_sel_hi:[0,1,0]
v_mfma_f32_16x16x32_bf16 v[32:35],a[212:215],v[160:163],v[16:19]
v_lshlrev_b32_sdwa v48,1,v48 dst_sel:DWORD dst_unused:UNUSED_PAD src0_sel:DWORD src1_sel:WORD_1
v_mfma_f32_32x32x16_bf16 v[208:223],a[88:91],a[88:91],0
v_lshlrev_b32_sdwa v50,1,v50 dst_sel:DWORD dst_unused:UNUSED_PAD src0_sel:DWORD src1_sel:WORD_1
v_lshlrev_b32_sdwa v49,1,v49 dst_sel:DWORD dst_unused:UNUSED_PAD src0_sel:DWORD src1_sel:WORD_1
v_lshlrev_b32_sdwa v51,1,v51 dst_sel:DWORD dst_unused:UNUSED_PAD src0_sel:DWORD src1_sel:WORD_1
v_lshlrev_b32_sdwa v44,1,v44 dst_sel:DWORD dst_unused:UNUSED_PAD src0_sel:DWORD src1_sel:WORD_1
v_lshlrev_b32_sdwa v46,1,v46 dst_sel:DWORD dst_unused:UNUSED_PAD src0_sel:DWORD src1_sel:WORD_1
v_mfma_f32_16x16x32_bf16 v[32:35],a[208:211],v[176:179],v[32:35]
s_add_u32 m0,s57,14144
ds_read_u16 v48,v48
ds_read_u16_d16_hi v50,v50
v_lshl_add_u64 v[24:25],v[24:25],0,v[26:27]
v_mfma_f32_16x16x32_bf16 v[32:35],a[204:207],v[192:195],v[32:35]
s_waitcnt lgkmcnt(8)
ds_read_u16 v49,v49
v_lshl_add_u64 v[26:27],v[28:29],0,v[30:31]
v_mfma_f32_16x16x32_bf16 v[60:63],a[200:203],v[192:195],v[20:23]
s_add_u32 s89,s56,2850816
ds_read_u16_d16_hi v51,v51
v_mfma_f32_32x32x16_bf16 v[224:239],a[88:91],a[88:91],0
v_accvgpr_write_b32 a0,v160
v_accvgpr_write_b32 a1,v161
v_accvgpr_write_b32 a2,v162
v_accvgpr_write_b32 a3,v163
v_lshlrev_b32_sdwa v45,1,v45 dst_sel:DWORD dst_unused:UNUSED_PAD src0_sel:DWORD src1_sel:WORD_1
v_mfma_f32_16x16x32_bf16 v[60:63],a[196:199],v[176:179],v[60:63]
s_waitcnt lgkmcnt(6)
buffer_load_dwordx4 v6,s[36:39],s89 offen lds
v_lshl_add_u64 v[28:29],v[52:53],0,v[54:55]
v_mfma_f32_16x16x32_bf16 v[60:63],a[192:195],a[0:3],v[60:63]
ds_read_u16 v44,v44
v_lshlrev_b32_sdwa v47,1,v47 dst_sel:DWORD dst_unused:UNUSED_PAD src0_sel:DWORD src1_sel:WORD_1
v_mfma_f32_16x16x32_bf16 v[52:55],a[192:195],v[164:167],v[20:23]
s_waitcnt lgkmcnt(5)
ds_read_u16_d16_hi v46,v46
v_or_b32_e64 v30,v56,v58
v_mfma_f32_16x16x32_bf16 v[52:55],a[196:199],v[180:183],v[52:55]
s_waitcnt vmcnt(9)
ds_read_u16 v45,v45
v_cvt_pk_bf16_f32 v31,v57,v59
v_mfma_f32_16x16x32_bf16 v[52:55],a[200:203],v[196:199],v[52:55]
s_barrier
ds_read_u16_d16_hi v47,v47
v_accvgpr_write_b32 a4,v164
v_mfma_f32_16x16x32_bf16 v[56:59],a[204:207],v[196:199],v[16:19]
ds_read_b64_tr_b16 a[224:225],v3 offset:12320
v_lshlrev_b32_sdwa v36,1,v36 dst_sel:DWORD dst_unused:UNUSED_PAD src0_sel:DWORD src1_sel:WORD_1
v_mfma_f32_16x16x32_bf16 v[56:59],a[208:211],v[180:183],v[56:59]
ds_read_u16 v36,v36
v_accvgpr_write_b32 a5,v165
v_mfma_f32_16x16x32_bf16 v[56:59],a[212:215],v[164:167],v[56:59]
s_add_u32 m0,s58,0
ds_read_b64_tr_b16 a[226:227],v3 offset:13856
v_lshlrev_b32_sdwa v38,1,v38 dst_sel:DWORD dst_unused:UNUSED_PAD src0_sel:DWORD src1_sel:WORD_1
v_mfma_f32_16x16x32_bf16 v[136:139],a[248:251],v[24:27],v[136:139]
s_add_u32 s69,s69,s59
ds_read_u16_d16_hi v38,v38
v_accvgpr_write_b32 a6,v166
v_mfma_f32_16x16x32_bf16 v[140:143],a[248:251],v[28:31],v[140:143]
buffer_load_dwordx4 v5,s[44:47],s69 offen offset:0 lds
v_lshlrev_b32_sdwa v37,1,v37 dst_sel:DWORD dst_unused:UNUSED_PAD src0_sel:DWORD src1_sel:WORD_1
v_mfma_f32_16x16x32_bf16 v[68:71],a[252:255],v[28:31],v[68:71]
ds_read_u16 v37,v37
v_lshlrev_b32_sdwa v39,1,v39 dst_sel:DWORD dst_unused:UNUSED_PAD src0_sel:DWORD src1_sel:WORD_1
v_mfma_f32_16x16x32_bf16 v[64:67],a[252:255],v[24:27],v[64:67]
ds_read_u16_d16_hi v39,v39
v_accvgpr_write_b32 a7,v167
v_mfma_f32_16x16x32_bf16 v[144:147],a[92:95],v[24:27],v[144:147]
s_add_u32 s69,s69,s53
ds_read_b64_tr_b16 a[228:229],v3 offset:12328
v_lshlrev_b32_sdwa v40,1,v40 dst_sel:DWORD dst_unused:UNUSED_PAD src0_sel:DWORD src1_sel:WORD_1
v_mfma_f32_16x16x32_bf16 v[148:151],a[92:95],v[28:31],v[148:151]
buffer_load_dwordx4 v5,s[44:47],s69 offen offset:1024 lds
ds_read_u16 v40,v40
v_mfma_f32_16x16x32_bf16 v[84:87],a[48:51],v[28:31],v[84:87]
s_add_u32 m0,s57,17688
ds_read_b64_tr_b16 a[230:231],v3 offset:13864
ds_read_b64_tr_b16 a[232:233],v3 offset:12384
v_accvgpr_write_b32 a8,v168
v_mfma_f32_16x16x32_bf16 v[80:83],a[48:51],v[24:27],v[80:83]
s_add_u32 s89,s56,2949120
ds_read_b64_tr_b16 a[234:235],v3 offset:13920
v_lshlrev_b32_sdwa v42,1,v42 dst_sel:DWORD dst_unused:UNUSED_PAD src0_sel:DWORD src1_sel:WORD_1
v_mfma_f32_16x16x32_bf16 v[152:155],a[56:59],v[24:27],v[152:155]
buffer_load_dwordx4 v6,s[36:39],s89 offen lds
ds_read_u16_d16_hi v42,v42
v_lshlrev_b32_sdwa v41,1,v41 dst_sel:DWORD dst_unused:UNUSED_PAD src0_sel:DWORD src1_sel:WORD_1
v_accvgpr_write_b32 a9,v169
v_mfma_f32_16x16x32_bf16 v[156:159],a[56:59],v[28:31],v[156:159]
s_add_u32 m0,s58,0
ds_read_u16 v41,v41
ds_read_b64_tr_b16 a[236:237],v3 offset:12392
v_lshlrev_b32_sdwa v43,1,v43 dst_sel:DWORD dst_unused:UNUSED_PAD src0_sel:DWORD src1_sel:WORD_1
v_mfma_f32_16x16x32_bf16 v[100:103],a[64:67],v[28:31],v[100:103]
s_add_u32 s69,s69,s53
ds_read_u16_d16_hi v43,v43
ds_read_b64_tr_b16 a[238:239],v3 offset:13928
v_accvgpr_write_b32 a10,v170
v_mfma_f32_16x16x32_bf16 v[96:99],a[64:67],v[24:27],v[96:99]
buffer_load_dwordx4 v5,s[44:47],s69 offen offset:2048 lds
ds_read_b64_tr_b16 a[240:241],v3 offset:12448
v_accvgpr_write_b32 a11,v171
s_nop 1
v_mfma_f32_16x16x32_bf16 v[28:31],a[212:215],a[8:11],v[16:19]
ds_read_b64 v[14:15],v7 offset:12320
ds_read_b64_tr_b16 a[52:53],v1 offset:12320
v_lshlrev_b32_sdwa v32,1,v32 dst_sel:DWORD dst_unused:UNUSED_PAD src0_sel:DWORD src1_sel:WORD_1
v_mfma_f32_16x16x32_bf16 v[28:31],a[208:211],v[184:187],v[28:31]
s_waitcnt lgkmcnt(12)
ds_read_b64_tr_b16 a[54:55],v1 offset:13344
ds_read_b64_tr_b16 a[60:61],v1 offset:14368
v_lshl_add_u64 v[48:49],v[48:49],0,v[50:51]
v_fma_mixlo_f16 v8,v53,s99,0
v_mfma_f32_16x16x32_bf16 v[28:31],a[204:207],v[200:203],v[28:31]
s_add_u32 m0,s58,6160
ds_read_b64_tr_b16 a[62:63],v1 offset:15392
ds_read_u16 v32,v32
v_lshl_add_u64 v[50:51],v[44:45],0,v[46:47]
v_lshlrev_b32_sdwa v34,1,v34 dst_sel:DWORD dst_unused:UNUSED_PAD src0_sel:DWORD src1_sel:WORD_1
v_mfma_f32_16x16x32_bf16 v[24:27],a[200:203],v[200:203],v[20:23]
s_add_u32 s69,s69,s59
ds_read_u16_d16_hi v34,v34
ds_read_b64_tr_b16 a[68:69],v1 offset:16416
v_fma_mixhi_f16 v8,v55,s99,0
v_mfma_f32_16x16x32_bf16 v[24:27],a[196:199],v[184:187],v[24:27]
buffer_load_dwordx4 v5,s[44:47],s69 offen offset:0 lds
ds_read_b64_tr_b16 a[70:71],v1 offset:17440
v_lshl_add_u64 v[44:45],v[36:37],0,v[38:39]
v_lshlrev_b32_sdwa v33,1,v33 dst_sel:DWORD dst_unused:UNUSED_PAD src0_sel:DWORD src1_sel:WORD_1
v_pk_fma_f16 v9,v8,v8,s54
v_mfma_f32_16x16x32_bf16 v[24:27],a[192:195],a[8:11],v[24:27]
s_waitcnt lgkmcnt(8)
ds_read_u16 v33,v33
ds_read_b64_tr_b16 a[84:85],v2 offset:16416
v_or3_b32 v252,v14,v15,v252
v_lshlrev_b32_sdwa v35,1,v35 dst_sel:DWORD dst_unused:UNUSED_PAD src0_sel:DWORD src1_sel:WORD_1
v_accvgpr_write_b32 a12,v172
v_mfma_f32_16x16x32_bf16 v[36:39],a[192:195],v[172:175],v[20:23]
s_add_u32 s69,s69,s53
ds_read_b64 v[14:15],v7 offset:12832
ds_read_b128 v[20:23],v4 offset:384
v_pk_mul_f16 v9,v8,v9
v_mfma_f32_16x16x32_bf16 v[36:39],a[196:199],v[188:191],v[36:39]
buffer_load_dwordx4 v5,s[44:47],s69 offen offset:1024 lds
ds_read_u16_d16_hi v35,v35
v_lshl_add_u64 v[46:47],v[40:41],0,v[42:43]
v_lshlrev_b32_sdwa v60,1,v60 dst_sel:DWORD dst_unused:UNUSED_PAD src0_sel:DWORD src1_sel:WORD_1
v_mfma_f32_16x16x32_bf16 v[36:39],a[200:203],v[204:207],v[36:39]
s_add_u32 m0,s57,21224
ds_read_u16 v60,v60
v_exp_f16_e64 v8,v9
v_mfma_f32_16x16x32_bf16 v[40:43],a[204:207],v[204:207],v[16:19]
s_add_u32 s89,s56,3047424
ds_read_b128 v[16:19],v4 offset:416
ds_read_b64_tr_b16 a[86:87],v2 offset:17440
v_lshlrev_b32_sdwa v62,1,v62 dst_sel:DWORD dst_unused:UNUSED_PAD src0_sel:DWORD src1_sel:WORD_1
v_mfma_f32_16x16x32_bf16 v[40:43],a[208:211],v[188:191],v[40:43]
buffer_load_dwordx4 v6,s[36:39],s89 offen lds
ds_read_u16_d16_hi v62,v62
v_lshlrev_b32_sdwa v61,1,v61 dst_sel:DWORD dst_unused:UNUSED_PAD src0_sel:DWORD src1_sel:WORD_1
v_mfma_f32_16x16x32_bf16 v[40:43],a[212:215],v[172:175],v[40:43]
s_waitcnt lgkmcnt(6)
ds_read_u16 v61,v61
ds_read_b64_tr_b16 a[80:81],v2 offset:14368
v_or3_b32 v252,v14,v15,v252
v_accvgpr_write_b32 a13,v173
v_mfma_f32_16x16x32_bf16 v[112:115],a[248:251],v[48:51],v[112:115]
s_add_u32 m0,s58,6160
ds_read_b64 v[14:15],v7 offset:13344
ds_read_b64_tr_b16 a[82:83],v2 offset:15392
v_exp_f16_sdwa v8,v9 dst_sel:WORD_1 dst_unused:UNUSED_PRESERVE src0_sel:WORD_1
v_lshlrev_b32_sdwa v63,1,v63 dst_sel:DWORD dst_unused:UNUSED_PAD src0_sel:DWORD src1_sel:WORD_1
v_mfma_f32_16x16x32_bf16 v[116:119],a[248:251],v[44:47],v[116:119]
s_add_u32 s69,s69,s53
ds_read_u16_d16_hi v63,v63
ds_read_b64_tr_b16 a[76:77],v2 offset:12320
v_lshlrev_b32_sdwa v56,1,v56 dst_sel:DWORD dst_unused:UNUSED_PAD src0_sel:DWORD src1_sel:WORD_1
v_mfma_f32_16x16x32_bf16 v[76:79],a[252:255],v[44:47],v[76:79]
buffer_load_dwordx4 v5,s[44:47],s69 offen offset:2048 lds
ds_read_u16 v56,v56
v_pk_fma_f16 v8,v8,s55,s55
v_mfma_f32_16x16x32_bf16 v[72:75],a[252:255],v[48:51],v[72:75]
ds_read_b64_tr_b16 a[78:79],v2 offset:13344
v_rcp_f16_e64 v9,v8
v_lshlrev_b32_sdwa v58,1,v58 dst_sel:DWORD dst_unused:UNUSED_PAD src0_sel:DWORD src1_sel:WORD_1
v_mfma_f32_16x16x32_bf16 v[120:123],a[92:95],v[48:51],v[120:123]
ds_read_u16_d16_hi v58,v58
v_lshlrev_b32_sdwa v57,1,v57 dst_sel:DWORD dst_unused:UNUSED_PAD src0_sel:DWORD src1_sel:WORD_1
v_mfma_f32_16x16x32_bf16 v[124:127],a[92:95],v[44:47],v[124:127]
s_waitcnt lgkmcnt(6)
ds_read_u16 v57,v57
v_or3_b32 v252,v14,v15,v252
TE02_%=:
s_cbranch_execz TP3814_%=
v_accvgpr_write_b32 a14,v174
v_mfma_f32_16x16x32_bf16 v[92:95],a[48:51],v[44:47],v[92:95]
s_add_u32 m0,s57,24752
ds_read_b64_tr_b16 a[242:243],v3 offset:13984
v_lshlrev_b32_sdwa v59,1,v59 dst_sel:DWORD dst_unused:UNUSED_PAD src0_sel:DWORD src1_sel:WORD_1
v_mfma_f32_16x16x32_bf16 v[88:91],a[48:51],v[48:51],v[88:91]
s_add_u32 s89,s56,3145728
v_rcp_f16_sdwa v9,v8 dst_sel:WORD_1 dst_unused:UNUSED_PRESERVE src0_sel:WORD_1
v_mfma_f32_16x16x32_bf16 v[128:131],a[56:59],v[48:51],v[128:131]
s_nop 0
buffer_load_dwordx4 v6,s[36:39],s89 offen lds
ds_read_u16_d16_hi v59,v59
v_lshlrev_b32_sdwa v52,1,v52 dst_sel:DWORD dst_unused:UNUSED_PAD src0_sel:DWORD src1_sel:WORD_1
v_mfma_f32_16x16x32_bf16 v[132:135],a[56:59],v[44:47],v[132:135]
ds_read_u16 v52,v52
v_lshlrev_b32_sdwa v54,1,v54 dst_sel:DWORD dst_unused:UNUSED_PAD src0_sel:DWORD src1_sel:WORD_1
v_mfma_f32_16x16x32_bf16 v[108:111],a[64:67],v[44:47],v[108:111]
ds_read_u16_d16_hi v54,v54
v_fma_mix_f32 v53,v53,v9,0 op_sel_hi:[0,1,0]
v_mfma_f32_16x16x32_bf16 v[104:107],a[64:67],v[48:51],v[104:107]
ds_read_b64_tr_b16 a[244:245],v3 offset:12456
v_accvgpr_write_b32 a15,v175
v_fma_mix_f32 v55,v55,v9,0 op_sel:[0,1,0] op_sel_hi:[0,1,0]
v_mfma_f32_16x16x32_bf16 v[44:47],a[52:55],a[0:3],v[20:23]
ds_read_b64_tr_b16 a[246:247],v3 offset:13992
v_lshlrev_b32_sdwa v28,1,v28 dst_sel:DWORD dst_unused:UNUSED_PAD src0_sel:DWORD src1_sel:WORD_1
v_mfma_f32_16x16x32_bf16 v[44:47],a[60:63],v[176:179],v[44:47]
ds_read_u16 v28,v28
v_lshl_add_u64 v[32:33],v[32:33],0,v[34:35]
v_mfma_f32_32x32x16_bf16 v[160:175],a[88:91],a[88:91],0
v_lshlrev_b32_sdwa v30,1,v30 dst_sel:DWORD dst_unused:UNUSED_PAD src0_sel:DWORD src1_sel:WORD_1
v_accvgpr_write_b32 a16,v176
v_lshlrev_b32_sdwa v29,1,v29 dst_sel:DWORD dst_unused:UNUSED_PAD src0_sel:DWORD src1_sel:WORD_1
v_accvgpr_write_b32 a17,v177
v_lshlrev_b32_sdwa v31,1,v31 dst_sel:DWORD dst_unused:UNUSED_PAD src0_sel:DWORD src1_sel:WORD_1
v_mfma_f32_16x16x32_bf16 v[44:47],a[68:71],v[192:195],v[44:47]
s_waitcnt lgkmcnt(7)
ds_read_u16_d16_hi v30,v30
v_lshl_add_u64 v[34:35],v[60:61],0,v[62:63]
v_mfma_f32_16x16x32_bf16 v[48:51],a[84:87],v[192:195],v[16:19]
ds_read_u16 v29,v29
v_accvgpr_write_b32 a18,v178
v_mfma_f32_16x16x32_bf16 v[48:51],a[80:83],v[176:179],v[48:51]
s_waitcnt lgkmcnt(7)
ds_read_u16_d16_hi v31,v31
v_lshl_add_u64 v[60:61],v[56:57],0,v[58:59]
v_mfma_f32_16x16x32_bf16 v[48:51],a[76:79],a[0:3],v[48:51]
v_lshlrev_b32_sdwa v24,1,v24 dst_sel:DWORD dst_unused:UNUSED_PAD src0_sel:DWORD src1_sel:WORD_1
v_lshlrev_b32_sdwa v26,1,v26 dst_sel:DWORD dst_unused:UNUSED_PAD src0_sel:DWORD src1_sel:WORD_1
v_mfma_f32_16x16x32_bf16 v[56:59],a[76:79],a[4:7],v[16:19]
s_waitcnt lgkmcnt(6)
ds_read_u16 v24,v24
v_or_b32 v62,v52,v54
v_mfma_f32_16x16x32_bf16 v[56:59],a[80:83],v[180:183],v[56:59]
s_waitcnt vmcnt(13)
ds_read_u16_d16_hi v26,v26
v_cvt_pk_bf16_f32 v63,v53,v55
v_mfma_f32_16x16x32_bf16 v[56:59],a[84:87],v[196:199],v[56:59]
s_barrier
v_accvgpr_write_b32 a19,v179
v_lshlrev_b32_sdwa v25,1,v25 dst_sel:DWORD dst_unused:UNUSED_PAD src0_sel:DWORD src1_sel:WORD_1
v_mfma_f32_16x16x32_bf16 v[52:55],a[68:71],v[196:199],v[20:23]
s_add_u32 m0,s57,28288
ds_read_u16 v25,v25
v_accvgpr_write_b32 a20,v180
v_mfma_f32_16x16x32_bf16 v[52:55],a[60:63],v[180:183],v[52:55]
s_add_u32 s89,s56,3244032
ds_read_b64_tr_b16 a[92:93],v3 offset:18480
v_lshlrev_b32_sdwa v27,1,v27 dst_sel:DWORD dst_unused:UNUSED_PAD src0_sel:DWORD src1_sel:WORD_1
v_mfma_f32_16x16x32_bf16 v[52:55],a[52:55],a[4:7],v[52:55]
buffer_load_dwordx4 v6,s[36:39],s89 offen lds
v_accvgpr_write_b32 a21,v181
v_mfma_f32_16x16x32_bf16 v[136:139],a[224:227],v[32:35],v[136:139]
ds_read_u16_d16_hi v27,v27
v_lshlrev_b32_sdwa v40,1,v40 dst_sel:DWORD dst_unused:UNUSED_PAD src0_sel:DWORD src1_sel:WORD_1
v_mfma_f32_16x16x32_bf16 v[140:143],a[224:227],v[60:63],v[140:143]
ds_read_u16 v40,v40
v_lshlrev_b32_sdwa v42,1,v42 dst_sel:DWORD dst_unused:UNUSED_PAD src0_sel:DWORD src1_sel:WORD_1
v_accvgpr_write_b32 a22,v182
v_mfma_f32_16x16x32_bf16 v[68:71],a[228:231],v[60:63],v[68:71]
ds_read_u16_d16_hi v42,v42
v_lshlrev_b32_sdwa v41,1,v41 dst_sel:DWORD dst_unused:UNUSED_PAD src0_sel:DWORD src1_sel:WORD_1
v_mfma_f32_16x16x32_bf16 v[64:67],a[228:231],v[32:35],v[64:67]
ds_read_u16 v41,v41
v_accvgpr_write_b32 a23,v183
v_mfma_f32_16x16x32_bf16 v[144:147],a[232:235],v[32:35],v[144:147]
ds_read_b64_tr_b16 a[94:95],v3 offset:20016
ds_read_b64_tr_b16 a[48:49],v3 offset:18488
v_lshlrev_b32_sdwa v43,1,v43 dst_sel:DWORD dst_unused:UNUSED_PAD src0_sel:DWORD src1_sel:WORD_1
v_mfma_f32_16x16x32_bf16 v[148:151],a[232:235],v[60:63],v[148:151]
ds_read_u16_d16_hi v43,v43
v_accvgpr_write_b32 a24,v184
v_lshlrev_b32_sdwa v36,1,v36 dst_sel:DWORD dst_unused:UNUSED_PAD src0_sel:DWORD src1_sel:WORD_1
v_mfma_f32_16x16x32_bf16 v[84:87],a[236:239],v[60:63],v[84:87]
ds_read_u16 v36,v36
ds_read_b64_tr_b16 a[50:51],v3 offset:20024
v_accvgpr_write_b32 a25,v185
v_mfma_f32_16x16x32_bf16 v[80:83],a[236:239],v[32:35],v[80:83]
ds_read_b64_tr_b16 a[56:57],v3 offset:18544
v_lshlrev_b32_sdwa v38,1,v38 dst_sel:DWORD dst_unused:UNUSED_PAD src0_sel:DWORD src1_sel:WORD_1
v_mfma_f32_16x16x32_bf16 v[152:155],a[240:243],v[32:35],v[152:155]
ds_read_u16_d16_hi v38,v38
ds_read_b64_tr_b16 a[58:59],v3 offset:20080
v_lshlrev_b32_sdwa v37,1,v37 dst_sel:DWORD dst_unused:UNUSED_PAD src0_sel:DWORD src1_sel:WORD_1
v_accvgpr_write_b32 a26,v186
v_mfma_f32_16x16x32_bf16 v[156:159],a[240:243],v[60:63],v[156:159]
s_waitcnt lgkmcnt(11)
ds_read_u16 v37,v37
ds_read_b64_tr_b16 a[64:65],v3 offset:18552
v_lshlrev_b32_sdwa v39,1,v39 dst_sel:DWORD dst_unused:UNUSED_PAD src0_sel:DWORD src1_sel:WORD_1
v_mfma_f32_16x16x32_bf16 v[100:103],a[244:247],v[60:63],v[100:103]
s_nop 0
ds_read_u16_d16_hi v39,v39
ds_read_b64_tr_b16 a[66:67],v3 offset:20088
v_accvgpr_write_b32 a27,v187
v_mfma_f32_16x16x32_bf16 v[96:99],a[244:247],v[32:35],v[96:99]
s_add_u32 m0,s57,31832
ds_read_b64_tr_b16 a[248:249],v3 offset:18608
v_accvgpr_write_b32 a28,v188
v_mfma_f32_16x16x32_bf16 v[60:63],a[52:55],a[8:11],v[20:23]
s_add_u32 s89,s56,3342336
ds_read_b64 v[14:15],v7 offset:18480
ds_read_b64_tr_b16 a[72:73],v1 offset:18480
v_lshlrev_b32_sdwa v44,1,v44 dst_sel:DWORD dst_unused:UNUSED_PAD src0_sel:DWORD src1_sel:WORD_1
v_mfma_f32_16x16x32_bf16 v[60:63],a[60:63],a[24:27],v[60:63]
buffer_load_dwordx4 v6,s[36:39],s89 offen lds
ds_read_b64_tr_b16 a[74:75],v1 offset:19504
v_lshl_add_u64 v[28:29],v[28:29],0,v[30:31]
v_fma_mixlo_f16 v8,v57,s99,0
v_mfma_f32_16x16x32_bf16 v[60:63],a[68:71],v[200:203],v[60:63]
ds_read_b64_tr_b16 a[220:221],v1 offset:20528
ds_read_b64_tr_b16 a[222:223],v1 offset:21552
v_lshl_add_u64 v[30:31],v[24:25],0,v[26:27]
v_lshlrev_b32_sdwa v46,1,v46 dst_sel:DWORD dst_unused:UNUSED_PAD src0_sel:DWORD src1_sel:WORD_1
v_mfma_f32_16x16x32_bf16 v[32:35],a[84:87],v[200:203],v[16:19]
ds_read_u16 v44,v44
ds_read_u16_d16_hi v46,v46
v_fma_mixhi_f16 v8,v59,s99,0
v_mfma_f32_16x16x32_bf16 v[32:35],a[80:83],a[24:27],v[32:35]
s_waitcnt lgkmcnt(14)
ds_read_b64_tr_b16 a[216:217],v1 offset:22576
ds_read_b64_tr_b16 a[218:219],v1 offset:23600
v_lshl_add_u64 v[24:25],v[40:41],0,v[42:43]
v_lshlrev_b32_sdwa v45,1,v45 dst_sel:DWORD dst_unused:UNUSED_PAD src0_sel:DWORD src1_sel:WORD_1
v_pk_fma_f16 v9,v8,v8,s54
v_mfma_f32_16x16x32_bf16 v[32:35],a[76:79],a[8:11],v[32:35]
s_waitcnt lgkmcnt(8)
ds_read_u16 v45,v45
ds_read_b64_tr_b16 a[192:193],v2 offset:22576
v_or3_b32 v252,v14,v15,v252
v_lshlrev_b32_sdwa v47,1,v47 dst_sel:DWORD dst_unused:UNUSED_PAD src0_sel:DWORD src1_sel:WORD_1
v_accvgpr_write_b32 a29,v189
v_mfma_f32_16x16x32_bf16 v[40:43],a[76:79],a[12:15],v[16:19]
ds_read_b64 v[14:15],v7 offset:18992
ds_read_b128 v[16:19],v4 offset:512
v_pk_mul_f16 v9,v8,v9
v_mfma_f32_16x16x32_bf16 v[40:43],a[80:83],v[188:191],v[40:43]
ds_read_u16_d16_hi v47,v47
ds_read_b64_tr_b16 a[194:195],v2 offset:23600
v_lshl_add_u64 v[26:27],v[36:37],0,v[38:39]
v_lshlrev_b32_sdwa v48,1,v48 dst_sel:DWORD dst_unused:UNUSED_PAD src0_sel:DWORD src1_sel:WORD_1
v_mfma_f32_16x16x32_bf16 v[40:43],a[84:87],v[204:207],v[40:43]
ds_read_u16 v48,v48
v_exp_f16 v8,v9
v_mfma_f32_16x16x32_bf16 v[36:39],a[68:71],v[204:207],v[20:23]
ds_read_b128 v[20:23],v4 offset:544
ds_read_b64_tr_b16 a[196:197],v2 offset:20528
v_lshlrev_b32_sdwa v50,1,v50 dst_sel:DWORD dst_unused:UNUSED_PAD src0_sel:DWORD src1_sel:WORD_1
v_mfma_f32_16x16x32_bf16 v[36:39],a[60:63],v[188:191],v[36:39]
ds_read_u16_d16_hi v50,v50
ds_read_b64_tr_b16 a[198:199],v2 offset:21552
v_lshlrev_b32_sdwa v49,1,v49 dst_sel:DWORD dst_unused:UNUSED_PAD src0_sel:DWORD src1_sel:WORD_1
v_mfma_f32_16x16x32_bf16 v[36:39],a[52:55],a[12:15],v[36:39]
s_waitcnt lgkmcnt(8)
ds_read_u16 v49,v49
ds_read_b64_tr_b16 a[200:201],v2 offset:18480
v_or3_b32 v252,v14,v15,v252
v_accvgpr_write_b32 a30,v190
v_mfma_f32_16x16x32_bf16 v[112:115],a[224:227],v[28:31],v[112:115]
s_add_u32 m0,s57,35368
ds_read_b64 v[14:15],v7 offset:19504
ds_read_b64_tr_b16 a[202:203],v2 offset:19504
v_exp_f16_sdwa v8,v9 dst_sel:WORD_1 dst_unused:UNUSED_PRESERVE src0_sel:WORD_1
v_lshlrev_b32_sdwa v51,1,v51 dst_sel:DWORD dst_unused:UNUSED_PAD src0_sel:DWORD src1_sel:WORD_1
v_mfma_f32_16x16x32_bf16 v[116:119],a[224:227],v[24:27],v[116:119]
s_add_u32 s89,s56,3440640
ds_read_u16_d16_hi v51,v51
v_lshlrev_b32_sdwa v52,1,v52 dst_sel:DWORD dst_unused:UNUSED_PAD src0_sel:DWORD src1_sel:WORD_1
v_mfma_f32_16x16x32_bf16 v[76:79],a[228:231],v[24:27],v[76:79]
buffer_load_dwordx4 v6,s[36:39],s89 offen lds
ds_read_u16 v52,v52
v_pk_fma_f16 v8,v8,s55,s55
v_mfma_f32_16x16x32_bf16 v[72:75],a[228:231],v[28:31],v[72:75]
v_rcp_f16 v9,v8
v_lshlrev_b32_sdwa v54,1,v54 dst_sel:DWORD dst_unused:UNUSED_PAD src0_sel:DWORD src1_sel:WORD_1
v_mfma_f32_16x16x32_bf16 v[120:123],a[232:235],v[28:31],v[120:123]
ds_read_u16_d16_hi v54,v54
v_lshlrev_b32_sdwa v53,1,v53 dst_sel:DWORD dst_unused:UNUSED_PAD src0_sel:DWORD src1_sel:WORD_1
v_mfma_f32_16x16x32_bf16 v[124:127],a[232:235],v[24:27],v[124:127]
s_waitcnt lgkmcnt(4)
ds_read_u16 v53,v53
v_or3_b32 v252,v14,v15,v252
v_accvgpr_write_b32 a31,v191
v_mfma_f32_16x16x32_bf16 v[92:95],a[236:239],v[24:27],v[92:95]
ds_read_b64_tr_b16 a[250:251],v3 offset:20144
v_lshlrev_b32_sdwa v55,1,v55 dst_sel:DWORD dst_unused:UNUSED_PAD src0_sel:DWORD src1_sel:WORD_1
v_mfma_f32_16x16x32_bf16 v[88:91],a[236:239],v[28:31],v[88:91]
v_rcp_f16_sdwa v9,v8 dst_sel:WORD_1 dst_unused:UNUSED_PRESERVE src0_sel:WORD_1
v_mfma_f32_16x16x32_bf16 v[128:131],a[240:243],v[28:31],v[128:131]
ds_read_u16_d16_hi v55,v55
v_lshlrev_b32_sdwa v56,1,v56 dst_sel:DWORD dst_unused:UNUSED_PAD src0_sel:DWORD src1_sel:WORD_1
v_mfma_f32_16x16x32_bf16 v[132:135],a[240:243],v[24:27],v[132:135]
ds_read_u16 v56,v56
v_lshlrev_b32_sdwa v58,1,v58 dst_sel:DWORD dst_unused:UNUSED_PAD src0_sel:DWORD src1_sel:WORD_1
v_mfma_f32_16x16x32_bf16 v[108:111],a[244:247],v[24:27],v[108:111]
ds_read_u16_d16_hi v58,v58
v_fma_mix_f32 v57,v57,v9,0 op_sel_hi:[0,1,0]
v_mfma_f32_16x16x32_bf16 v[104:107],a[244:247],v[28:31],v[104:107]
ds_read_b64_tr_b16 a[252:253],v3 offset:18616
v_mfma_f32_32x32x16_bf16 v[176:191],a[88:91],a[88:91],0
v_fma_mix_f32 v59,v59,v9,0 op_sel:[0,1,0] op_sel_hi:[0,1,0]
v_mfma_f32_16x16x32_bf16 v[24:27],a[72:75],a[0:3],v[16:19]
s_add_u32 m0,s57,38896
ds_read_b64_tr_b16 a[254:255],v3 offset:20152
v_lshlrev_b32_sdwa v60,1,v60 dst_sel:DWORD dst_unused:UNUSED_PAD src0_sel:DWORD src1_sel:WORD_1
v_mfma_f32_16x16x32_bf16 v[24:27],a[220:223],a[16:19],v[24:27]
s_add_u32 s89,s56,3538944
ds_read_u16 v60,v60
v_lshl_add_u64 v[44:45],v[44:45],0,v[46:47]
v_accvgpr_write_b32 a32,v192
v_mfma_f32_16x16x32_bf16 v[24:27],a[216:219],v[192:195],v[24:27]
s_waitcnt lgkmcnt(7)
buffer_load_dwordx4 v6,s[36:39],s89 offen lds
v_lshl_add_u64 v[46:47],v[48:49],0,v[50:51]
v_lshlrev_b32_sdwa v62,1,v62 dst_sel:DWORD dst_unused:UNUSED_PAD src0_sel:DWORD src1_sel:WORD_1
v_mfma_f32_16x16x32_bf16 v[28:31],a[192:195],v[192:195],v[20:23]
ds_read_u16_d16_hi v62,v62
v_accvgpr_write_b32 a33,v193
v_lshlrev_b32_sdwa v61,1,v61 dst_sel:DWORD dst_unused:UNUSED_PAD src0_sel:DWORD src1_sel:WORD_1
v_mfma_f32_16x16x32_bf16 v[28:31],a[196:199],a[16:19],v[28:31]
s_waitcnt lgkmcnt(6)
ds_read_u16 v61,v61
v_lshl_add_u64 v[48:49],v[52:53],0,v[54:55]
v_mfma_f32_16x16x32_bf16 v[28:31],a[200:203],a[0:3],v[28:31]
v_lshlrev_b32_sdwa v63,1,v63 dst_sel:DWORD dst_unused:UNUSED_PAD src0_sel:DWORD src1_sel:WORD_1
v_accvgpr_write_b32 a34,v194
v_mfma_f32_16x16x32_bf16 v[52:55],a[200:203],a[4:7],v[20:23]
s_waitcnt lgkmcnt(5)
ds_read_u16_d16_hi v63,v63
v_or_b32 v50,v56,v58
v_lshlrev_b32_sdwa v32,1,v32 dst_sel:DWORD dst_unused:UNUSED_PAD src0_sel:DWORD src1_sel:WORD_1
v_accvgpr_write_b32 a35,v195
v_mfma_f32_16x16x32_bf16 v[52:55],a[196:199],a[20:23],v[52:55]
s_waitcnt vmcnt(9)
ds_read_u16 v32,v32
v_cvt_pk_bf16_f32 v51,v57,v59
v_lshlrev_b32_sdwa v34,1,v34 dst_sel:DWORD dst_unused:UNUSED_PAD src0_sel:DWORD src1_sel:WORD_1
v_mfma_f32_16x16x32_bf16 v[52:55],a[192:195],v[196:199],v[52:55]
s_barrier
ds_read_u16_d16_hi v34,v34
v_accvgpr_write_b32 a36,v196
v_mfma_f32_16x16x32_bf16 v[56:59],a[216:219],v[196:199],v[16:19]
ds_read_b64_tr_b16 a[224:225],v3 offset:0
v_lshlrev_b32_sdwa v33,1,v33 dst_sel:DWORD dst_unused:UNUSED_PAD src0_sel:DWORD src1_sel:WORD_1
v_mfma_f32_16x16x32_bf16 v[56:59],a[220:223],a[20:23],v[56:59]
s_add_u32 m0,s58,12320
ds_read_u16 v33,v33
v_accvgpr_write_b32 a37,v197
v_lshlrev_b32_sdwa v35,1,v35 dst_sel:DWORD dst_unused:UNUSED_PAD src0_sel:DWORD src1_sel:WORD_1
v_mfma_f32_16x16x32_bf16 v[56:59],a[72:75],a[4:7],v[56:59]
s_add_u32 s69,s69,s59
ds_read_u16_d16_hi v35,v35
v_accvgpr_write_b32 a38,v198
v_mfma_f32_16x16x32_bf16 v[136:139],a[92:95],v[44:47],v[136:139]
buffer_load_dwordx4 v5,s[44:47],s69 offen offset:0 lds
v_lshlrev_b32_sdwa v36,1,v36 dst_sel:DWORD dst_unused:UNUSED_PAD src0_sel:DWORD src1_sel:WORD_1
v_mfma_f32_16x16x32_bf16 v[140:143],a[92:95],v[48:51],v[140:143]
ds_read_u16 v36,v36
v_lshlrev_b32_sdwa v38,1,v38 dst_sel:DWORD dst_unused:UNUSED_PAD src0_sel:DWORD src1_sel:WORD_1
v_accvgpr_write_b32 a39,v199
v_mfma_f32_16x16x32_bf16 v[68:71],a[48:51],v[48:51],v[68:71]
ds_read_u16_d16_hi v38,v38
v_lshlrev_b32_sdwa v37,1,v37 dst_sel:DWORD dst_unused:UNUSED_PAD src0_sel:DWORD src1_sel:WORD_1
v_mfma_f32_16x16x32_bf16 v[64:67],a[48:51],v[44:47],v[64:67]
s_add_u32 m0,s57,42432
ds_read_u16 v37,v37
v_accvgpr_write_b32 a40,v200
v_mfma_f32_16x16x32_bf16 v[144:147],a[56:59],v[44:47],v[144:147]
s_add_u32 s89,s56,3637248
ds_read_b64_tr_b16 a[226:227],v3 offset:1536
ds_read_b64_tr_b16 a[228:229],v3 offset:8
v_lshlrev_b32_sdwa v39,1,v39 dst_sel:DWORD dst_unused:UNUSED_PAD src0_sel:DWORD src1_sel:WORD_1
v_mfma_f32_16x16x32_bf16 v[148:151],a[56:59],v[48:51],v[148:151]
buffer_load_dwordx4 v6,s[36:39],s89 offen lds
ds_read_u16_d16_hi v39,v39
v_accvgpr_write_b32 a41,v201
v_lshlrev_b32_sdwa v40,1,v40 dst_sel:DWORD dst_unused:UNUSED_PAD src0_sel:DWORD src1_sel:WORD_1
v_mfma_f32_16x16x32_bf16 v[84:87],a[64:67],v[48:51],v[84:87]
s_add_u32 m0,s58,12320
ds_read_u16 v40,v40
ds_read_b64_tr_b16 a[230:231],v3 offset:1544
v_accvgpr_write_b32 a42,v202
v_mfma_f32_16x16x32_bf16 v[80:83],a[64:67],v[44:47],v[80:83]
s_add_u32 s69,s69,s53
ds_read_b64_tr_b16 a[232:233],v3 offset:64
v_lshlrev_b32_sdwa v42,1,v42 dst_sel:DWORD dst_unused:UNUSED_PAD src0_sel:DWORD src1_sel:WORD_1
v_mfma_f32_16x16x32_bf16 v[152:155],a[248:251],v[44:47],v[152:155]
buffer_load_dwordx4 v5,s[44:47],s69 offen offset:1024 lds
ds_read_u16_d16_hi v42,v42
v_lshlrev_b32_sdwa v41,1,v41 dst_sel:DWORD dst_unused:UNUSED_PAD src0_sel:DWORD src1_sel:WORD_1
v_accvgpr_write_b32 a43,v203
v_mfma_f32_16x16x32_bf16 v[156:159],a[248:251],v[48:51],v[156:159]
s_waitcnt lgkmcnt(10)
ds_read_u16 v41,v41
ds_read_b64_tr_b16 a[234:235],v3 offset:1600
v_lshlrev_b32_sdwa v43,1,v43 dst_sel:DWORD dst_unused:UNUSED_PAD src0_sel:DWORD src1_sel:WORD_1
v_mfma_f32_16x16x32_bf16 v[100:103],a[252:255],v[48:51],v[100:103]
s_add_u32 s69,s69,s53
ds_read_u16_d16_hi v43,v43
ds_read_b64_tr_b16 a[236:237],v3 offset:72
v_accvgpr_write_b32 a44,v204
v_mfma_f32_16x16x32_bf16 v[96:99],a[252:255],v[44:47],v[96:99]
buffer_load_dwordx4 v5,s[44:47],s69 offen offset:2048 lds
ds_read_b64_tr_b16 a[238:239],v3 offset:1608
v_accvgpr_write_b32 a45,v205
v_mfma_f32_16x16x32_bf16 v[48:51],a[72:75],a[8:11],v[16:19]
ds_read_b64 v[14:15],v7 offset:0
ds_read_b64_tr_b16 a[204:205],v1 offset:0
v_lshlrev_b32_sdwa v24,1,v24 dst_sel:DWORD dst_unused:UNUSED_PAD src0_sel:DWORD src1_sel:WORD_1
v_mfma_f32_16x16x32_bf16 v[48:51],a[220:223],a[24:27],v[48:51]
ds_read_b64_tr_b16 a[206:207],v1 offset:1024
ds_read_b64_tr_b16 a[208:209],v1 offset:2048
v_lshl_add_u64 v[60:61],v[60:61],0,v[62:63]
v_fma_mixlo_f16 v8,v53,s99,0
v_mfma_f32_16x16x32_bf16 v[48:51],a[216:219],a[40:43],v[48:51]
ds_read_b64_tr_b16 a[210:211],v1 offset:3072
ds_read_u16 v24,v24
v_lshl_add_u64 v[62:63],v[32:33],0,v[34:35]
v_lshlrev_b32_sdwa v26,1,v26 dst_sel:DWORD dst_unused:UNUSED_PAD src0_sel:DWORD src1_sel:WORD_1
v_mfma_f32_16x16x32_bf16 v[44:47],a[192:195],a[40:43],v[20:23]
ds_read_u16_d16_hi v26,v26
ds_read_b64_tr_b16 a[212:213],v1 offset:4096
v_fma_mixhi_f16 v8,v55,s99,0
v_mfma_f32_16x16x32_bf16 v[44:47],a[196:199],a[24:27],v[44:47]
s_waitcnt lgkmcnt(14)
ds_read_b64_tr_b16 a[214:215],v1 offset:5120
ds_read_b64_tr_b16 a[240:241],v3 offset:128
v_lshl_add_u64 v[32:33],v[36:37],0,v[38:39]
v_lshlrev_b32_sdwa v25,1,v25 dst_sel:DWORD dst_unused:UNUSED_PAD src0_sel:DWORD src1_sel:WORD_1
v_pk_fma_f16 v9,v8,v8,s54
v_mfma_f32_16x16x32_bf16 v[44:47],a[200:203],a[8:11],v[44:47]
s_waitcnt lgkmcnt(9)
ds_read_u16 v25,v25
v_or3_b32 v252,v14,v15,v252
v_lshlrev_b32_sdwa v27,1,v27 dst_sel:DWORD dst_unused:UNUSED_PAD src0_sel:DWORD src1_sel:WORD_1
v_mfma_f32_16x16x32_bf16 v[36:39],a[200:203],a[12:15],v[20:23]
s_add_u32 m0,s57,45976
ds_read_b64 v[14:15],v7 offset:512
ds_read_b128 v[20:23],v4 offset:640
v_pk_mul_f16 v9,v8,v9
v_mfma_f32_16x16x32_bf16 v[36:39],a[196:199],a[28:31],v[36:39]
s_add_u32 s89,s56,3735552
ds_read_u16_d16_hi v27,v27
ds_read_b64_tr_b16 a[200:201],v2 offset:4096
v_lshl_add_u64 v[34:35],v[40:41],0,v[42:43]
v_lshlrev_b32_sdwa v28,1,v28 dst_sel:DWORD dst_unused:UNUSED_PAD src0_sel:DWORD src1_sel:WORD_1
v_accvgpr_write_b32 a46,v206
v_mfma_f32_16x16x32_bf16 v[36:39],a[192:195],v[204:207],v[36:39]
ds_read_u16 v28,v28
v_exp_f16 v8,v9
v_mfma_f32_16x16x32_bf16 v[40:43],a[216:219],v[204:207],v[16:19]
buffer_load_dwordx4 v6,s[36:39],s89 offen lds
ds_read_b128 v[16:19],v4 offset:672
v_lshlrev_b32_sdwa v30,1,v30 dst_sel:DWORD dst_unused:UNUSED_PAD src0_sel:DWORD src1_sel:WORD_1
v_mfma_f32_16x16x32_bf16 v[40:43],a[220:223],a[28:31],v[40:43]
ds_read_u16_d16_hi v30,v30
ds_read_b64_tr_b16 a[202:203],v2 offset:5120
v_lshlrev_b32_sdwa v29,1,v29 dst_sel:DWORD dst_unused:UNUSED_PAD src0_sel:DWORD src1_sel:WORD_1
v_mfma_f32_16x16x32_bf16 v[40:43],a[72:75],a[12:15],v[40:43]
s_waitcnt lgkmcnt(7)
ds_read_u16 v29,v29
ds_read_b64_tr_b16 a[196:197],v2 offset:2048
v_or3_b32 v252,v14,v15,v252
v_mfma_f32_16x16x32_bf16 v[112:115],a[92:95],v[60:63],v[112:115]
ds_read_b64 v[14:15],v7 offset:1024
ds_read_b64_tr_b16 a[198:199],v2 offset:3072
v_exp_f16_sdwa v8,v9 dst_sel:WORD_1 dst_unused:UNUSED_PRESERVE src0_sel:WORD_1
v_lshlrev_b32_sdwa v31,1,v31 dst_sel:DWORD dst_unused:UNUSED_PAD src0_sel:DWORD src1_sel:WORD_1
v_mfma_f32_16x16x32_bf16 v[116:119],a[92:95],v[32:35],v[116:119]
ds_read_u16_d16_hi v31,v31
ds_read_b64_tr_b16 a[192:193],v2 offset:0
v_lshlrev_b32_sdwa v56,1,v56 dst_sel:DWORD dst_unused:UNUSED_PAD src0_sel:DWORD src1_sel:WORD_1
v_mfma_f32_16x16x32_bf16 v[76:79],a[48:51],v[32:35],v[76:79]
ds_read_u16 v56,v56
ds_read_b64_tr_b16 a[194:195],v2 offset:1024
v_pk_fma_f16 v8,v8,s55,s55
v_mfma_f32_16x16x32_bf16 v[72:75],a[48:51],v[60:63],v[72:75]
v_rcp_f16_e64 v9,v8
v_lshlrev_b32_sdwa v58,1,v58 dst_sel:DWORD dst_unused:UNUSED_PAD src0_sel:DWORD src1_sel:WORD_1
v_accvgpr_write_b32 a47,v207
v_mfma_f32_16x16x32_bf16 v[120:123],a[56:59],v[60:63],v[120:123]
s_add_u32 m0,s57,49512
ds_read_u16_d16_hi v58,v58
v_lshlrev_b32_sdwa v57,1,v57 dst_sel:DWORD dst_unused:UNUSED_PAD src0_sel:DWORD src1_sel:WORD_1
v_mfma_f32_16x16x32_bf16 v[124:127],a[56:59],v[32:35],v[124:127]
s_waitcnt lgkmcnt(6)
ds_read_u16 v57,v57
v_or3_b32 v252,v14,v15,v252
v_mfma_f32_16x16x32_bf16 v[92:95],a[64:67],v[32:35],v[92:95]
s_add_u32 s89,s56,3833856
ds_read_b64_tr_b16 a[242:243],v3 offset:1664
v_and_b32 v252,0x40004000,v252
v_lshlrev_b32_sdwa v59,1,v59 dst_sel:DWORD dst_unused:UNUSED_PAD src0_sel:DWORD src1_sel:WORD_1
v_mfma_f32_16x16x32_bf16 v[88:91],a[64:67],v[60:63],v[88:91]
buffer_load_dwordx4 v6,s[36:39],s89 offen lds
v_cmp_ne_u32_e32 vcc,0,v252
s_nop 0
s_or_b64 s[90:91],s[90:91],vcc
v_mfma_f32_16x16x32_bf16 v[128:131],a[248:251],v[60:63],v[128:131]
ds_read_u16_d16_hi v59,v59
v_rcp_f16_sdwa v9,v8 dst_sel:WORD_1 dst_unused:UNUSED_PRESERVE src0_sel:WORD_1
v_lshlrev_b32_sdwa v52,1,v52 dst_sel:DWORD dst_unused:UNUSED_PAD src0_sel:DWORD src1_sel:WORD_1
v_mfma_f32_16x16x32_bf16 v[132:135],a[248:251],v[32:35],v[132:135]
ds_read_u16 v52,v52
v_lshlrev_b32_sdwa v54,1,v54 dst_sel:DWORD dst_unused:UNUSED_PAD src0_sel:DWORD src1_sel:WORD_1
v_mfma_f32_16x16x32_bf16 v[108:111],a[252:255],v[32:35],v[108:111]
ds_read_u16_d16_hi v54,v54
v_fma_mix_f32 v53,v53,v9,0 op_sel_hi:[0,1,0]
v_mfma_f32_16x16x32_bf16 v[104:107],a[252:255],v[60:63],v[104:107]
ds_read_b64_tr_b16 a[244:245],v3 offset:136
v_mfma_f32_32x32x16_bf16 v[192:207],a[88:91],a[88:91],0
v_fma_mix_f32 v55,v55,v9,0 op_sel:[0,1,0] op_sel_hi:[0,1,0]
v_mfma_f32_16x16x32_bf16 v[32:35],a[204:207],a[0:3],v[20:23]
ds_read_b64_tr_b16 a[246:247],v3 offset:1672
v_lshlrev_b32_sdwa v48,1,v48 dst_sel:DWORD dst_unused:UNUSED_PAD src0_sel:DWORD src1_sel:WORD_1
v_mfma_f32_16x16x32_bf16 v[32:35],a[208:211],a[16:19],v[32:35]
ds_read_u16 v48,v48
v_lshl_add_u64 v[24:25],v[24:25],0,v[26:27]
v_mfma_f32_16x16x32_bf16 v[32:35],a[212:215],a[32:35],v[32:35]
s_waitcnt lgkmcnt(7)
v_lshl_add_u64 v[26:27],v[28:29],0,v[30:31]
v_lshlrev_b32_sdwa v50,1,v50 dst_sel:DWORD dst_unused:UNUSED_PAD src0_sel:DWORD src1_sel:WORD_1
v_mfma_f32_16x16x32_bf16 v[60:63],a[200:203],a[32:35],v[16:19]
ds_read_u16_d16_hi v50,v50
v_lshlrev_b32_sdwa v49,1,v49 dst_sel:DWORD dst_unused:UNUSED_PAD src0_sel:DWORD src1_sel:WORD_1
v_mfma_f32_16x16x32_bf16 v[60:63],a[196:199],a[16:19],v[60:63]
s_waitcnt lgkmcnt(6)
ds_read_u16 v49,v49
v_lshl_add_u64 v[28:29],v[56:57],0,v[58:59]
v_mfma_f32_16x16x32_bf16 v[60:63],a[192:195],a[0:3],v[60:63]
v_lshlrev_b32_sdwa v51,1,v51 dst_sel:DWORD dst_unused:UNUSED_PAD src0_sel:DWORD src1_sel:WORD_1
v_lshlrev_b32_sdwa v44,1,v44 dst_sel:DWORD dst_unused:UNUSED_PAD src0_sel:DWORD src1_sel:WORD_1
v_mfma_f32_16x16x32_bf16 v[56:59],a[192:195],a[4:7],v[16:19]
s_waitcnt lgkmcnt(5)
ds_read_u16_d16_hi v51,v51
v_or_b32 v30,v52,v54
v_mfma_f32_16x16x32_bf16 v[56:59],a[196:199],a[20:23],v[56:59]
s_waitcnt vmcnt(11)
ds_read_u16 v44,v44
v_cvt_pk_bf16_f32 v31,v53,v55
v_lshlrev_b32_sdwa v46,1,v46 dst_sel:DWORD dst_unused:UNUSED_PAD src0_sel:DWORD src1_sel:WORD_1
v_mfma_f32_16x16x32_bf16 v[56:59],a[200:203],a[36:39],v[56:59]
s_barrier
ds_read_u16_d16_hi v46,v46
v_lshlrev_b32_sdwa v45,1,v45 dst_sel:DWORD dst_unused:UNUSED_PAD src0_sel:DWORD src1_sel:WORD_1
v_mfma_f32_16x16x32_bf16 v[52:55],a[212:215],a[36:39],v[20:23]
s_add_u32 m0,s57,53040
ds_read_u16 v45,v45
v_lshlrev_b32_sdwa v47,1,v47 dst_sel:DWORD dst_unused:UNUSED_PAD src0_sel:DWORD src1_sel:WORD_1
v_mfma_f32_16x16x32_bf16 v[52:55],a[208:211],a[20:23],v[52:55]
s_add_u32 s89,s56,3932160
ds_read_u16_d16_hi v47,v47
v_mfma_f32_32x32x16_bf16 v[240:255],a[88:91],a[88:91],0
v_lshlrev_b32_sdwa v40,1,v40 dst_sel:DWORD dst_unused:UNUSED_PAD src0_sel:DWORD src1_sel:WORD_1
v_lshlrev_b32_sdwa v42,1,v42 dst_sel:DWORD dst_unused:UNUSED_PAD src0_sel:DWORD src1_sel:WORD_1
v_lshlrev_b32_sdwa v41,1,v41 dst_sel:DWORD dst_unused:UNUSED_PAD src0_sel:DWORD src1_sel:WORD_1
v_lshlrev_b32_sdwa v43,1,v43 dst_sel:DWORD dst_unused:UNUSED_PAD src0_sel:DWORD src1_sel:WORD_1
v_lshlrev_b32_sdwa v36,1,v36 dst_sel:DWORD dst_unused:UNUSED_PAD src0_sel:DWORD src1_sel:WORD_1
v_mfma_f32_16x16x32_bf16 v[52:55],a[204:207],a[4:7],v[52:55]
buffer_load_dwordx4 v6,s[36:39],s89 offen lds
ds_read_u16 v40,v40
v_mfma_f32_16x16x32_bf16 v[136:139],a[224:227],v[24:27],v[136:139]
s_add_u32 m0,s58,18480
ds_read_u16_d16_hi v42,v42
ds_read_u16 v41,v41
v_mfma_f32_16x16x32_bf16 v[140:143],a[224:227],v[28:31],v[140:143]
s_add_u32 s69,s69,s59
ds_read_u16_d16_hi v43,v43
v_lshlrev_b32_sdwa v38,1,v38 dst_sel:DWORD dst_unused:UNUSED_PAD src0_sel:DWORD src1_sel:WORD_1
v_mfma_f32_16x16x32_bf16 v[68:71],a[228:231],v[28:31],v[68:71]
buffer_load_dwordx4 v5,s[44:47],s69 offen offset:0 lds
ds_read_u16 v36,v36
v_mfma_f32_16x16x32_bf16 v[64:67],a[228:231],v[24:27],v[64:67]
s_add_u32 s69,s69,s53
ds_read_u16_d16_hi v38,v38
ds_read_b64_tr_b16 a[248:249],v3 offset:6160
v_mfma_f32_16x16x32_bf16 v[144:147],a[232:235],v[24:27],v[144:147]
buffer_load_dwordx4 v5,s[44:47],s69 offen offset:1024 lds
v_lshlrev_b32_sdwa v37,1,v37 dst_sel:DWORD dst_unused:UNUSED_PAD src0_sel:DWORD src1_sel:WORD_1
v_mfma_f32_16x16x32_bf16 v[148:151],a[232:235],v[28:31],v[148:151]
ds_read_u16 v37,v37
v_lshlrev_b32_sdwa v39,1,v39 dst_sel:DWORD dst_unused:UNUSED_PAD src0_sel:DWORD src1_sel:WORD_1
v_mfma_f32_16x16x32_bf16 v[84:87],a[236:239],v[28:31],v[84:87]
ds_read_u16_d16_hi v39,v39
v_mfma_f32_16x16x32_bf16 v[80:83],a[236:239],v[24:27],v[80:83]
ds_read_b64_tr_b16 a[250:251],v3 offset:7696
v_mfma_f32_16x16x32_bf16 v[152:155],a[240:243],v[24:27],v[152:155]
s_add_u32 s69,s69,s53
ds_read_b64_tr_b16 a[252:253],v3 offset:6168
v_mfma_f32_16x16x32_bf16 v[156:159],a[240:243],v[28:31],v[156:159]
s_waitcnt lgkmcnt(8)
buffer_load_dwordx4 v5,s[44:47],s69 offen offset:2048 lds
v_mfma_f32_16x16x32_bf16 v[100:103],a[244:247],v[28:31],v[100:103]
ds_read_b64_tr_b16 a[254:255],v3 offset:7704
v_mfma_f32_16x16x32_bf16 v[96:99],a[244:247],v[24:27],v[96:99]
s_waitcnt vmcnt(13)
s_barrier
v_add_u32_sdwa v24,s60,v10 dst_sel:DWORD dst_unused:UNUSED_PAD src0_sel:DWORD src1_sel:WORD_1
v_subrev_u32 v8,88400,v24
v_min_u32_e64 v24,v24,v8
v_add_u32 v25,7072,v24
v_subrev_u32 v8,88400,v25
v_min_u32_e64 v25,v25,v8
v_lshl_add_u64 v[48:49],v[48:49],0,v[50:51]
v_lshl_add_u64 v[50:51],v[44:45],0,v[46:47]
s_waitcnt lgkmcnt(8)
v_lshl_add_u64 v[44:45],v[40:41],0,v[42:43]
s_waitcnt lgkmcnt(3)
v_lshl_add_u64 v[46:47],v[36:37],0,v[38:39]
v_mfma_f32_16x16x32_bf16 v[112:115],a[224:227],v[48:51],v[112:115]
s_add_u32 m0,s58,0
s_add_u32 s69,s69,s59
buffer_load_dwordx4 v5,s[44:47],s69 offen offset:0 lds
v_xad_u32 v28,v11,0,v24
v_xad_u32 v29,v11,8,v25
v_mfma_f32_16x16x32_bf16 v[116:119],a[224:227],v[44:47],v[116:119]
TE03_%=:
s_cbranch_execz TP4345_%=
v_xad_u32 v30,v11,16,v24
v_xad_u32 v31,v11,24,v25
v_mfma_f32_16x16x32_bf16 v[76:79],a[228:231],v[44:47],v[76:79]
ds_read_b64 a[48:49],v28 offset:0
ds_read_b64 a[50:51],v28 offset:192
v_mfma_f32_16x16x32_bf16 v[72:75],a[228:231],v[48:51],v[72:75]
ds_read_b64 a[52:53],v29 offset:0
ds_read_b64 a[54:55],v29 offset:192
v_mfma_f32_16x16x32_bf16 v[120:123],a[232:235],v[48:51],v[120:123]
ds_read_b64 a[56:57],v28 offset:8
ds_read_b64 a[58:59],v28 offset:200
v_mfma_f32_16x16x32_bf16 v[124:127],a[232:235],v[44:47],v[124:127]
s_add_u32 s69,s69,s53
buffer_load_dwordx4 v5,s[44:47],s69 offen offset:1024 lds
ds_read_b64 a[60:61],v29 offset:8
ds_read_b64 a[62:63],v29 offset:200
v_mfma_f32_16x16x32_bf16 v[92:95],a[236:239],v[44:47],v[92:95]
v_mfma_f32_16x16x32_bf16 v[88:91],a[236:239],v[48:51],v[88:91]
v_mfma_f32_16x16x32_bf16 v[128:131],a[240:243],v[48:51],v[128:131]
v_mfma_f32_16x16x32_bf16 v[132:135],a[240:243],v[44:47],v[132:135]
v_mfma_f32_16x16x32_bf16 v[108:111],a[244:247],v[44:47],v[108:111]
v_mfma_f32_16x16x32_bf16 v[104:107],a[244:247],v[48:51],v[104:107]
s_nop 0
ds_read_b64 a[64:65],v30 offset:0
ds_read_b64 a[66:67],v30 offset:192
ds_read_b64 a[68:69],v31 offset:0
ds_read_b64 a[70:71],v31 offset:192
s_waitcnt lgkmcnt(8)
v_smfmac_f32_16x16x64_bf16 v[160:163],a[96:99],a[48:55],v10
s_waitcnt lgkmcnt(4)
s_add_u32 s69,s69,s53
buffer_load_dwordx4 v5,s[44:47],s69 offen offset:2048 lds
ds_read_b64 a[48:49],v30 offset:8
ds_read_b64 a[50:51],v30 offset:200
ds_read_b64 a[52:53],v31 offset:8
ds_read_b64 a[54:55],v31 offset:200
v_smfmac_f32_16x16x64_bf16 v[164:167],a[100:103],a[56:63],v10
s_waitcnt lgkmcnt(4)
ds_read_b64 a[56:57],v28 offset:64
ds_read_b64 a[58:59],v28 offset:256
ds_read_b64 a[60:61],v29 offset:64
ds_read_b64 a[62:63],v29 offset:256
v_smfmac_f32_16x16x64_bf16 v[168:171],a[104:107],a[64:71],v10
s_waitcnt lgkmcnt(4)
ds_read_b64 a[64:65],v28 offset:72
ds_read_b64 a[66:67],v28 offset:264
ds_read_b64 a[68:69],v29 offset:72
ds_read_b64 a[70:71],v29 offset:264
v_and_b32 v48,8,v11
v_pk_mul_f32 v[44:45],v[160:161],v[160:161]
v_pk_mul_f32 v[46:47],v[162:163],v[162:163]
v_smfmac_f32_16x16x64_bf16 v[172:175],a[108:111],a[48:55],v10
s_waitcnt lgkmcnt(4)
ds_read_b64 a[48:49],v30 offset:64
ds_read_b64 a[50:51],v30 offset:256
ds_read_b64 a[52:53],v31 offset:64
ds_read_b64 a[54:55],v31 offset:256
v_mul_u32_u24 v48,884,v48
s_add_u32 s64,s60,3536
v_pk_add_f32 v[36:37],v[160:161],v[164:165]
v_pk_fma_f32 v[44:45],v[164:165],v[164:165],v[44:45]
v_pk_add_f32 v[38:39],v[162:163],v[166:167]
v_pk_fma_f32 v[46:47],v[166:167],v[166:167],v[46:47]
v_smfmac_f32_16x16x64_bf16 v[176:179],a[128:131],a[56:63],v10
s_waitcnt lgkmcnt(4)
ds_read_b64 a[56:57],v30 offset:72
ds_read_b64 a[58:59],v30 offset:264
ds_read_b64 a[60:61],v31 offset:72
ds_read_b64 a[62:63],v31 offset:264
v_add_u32 v51,s64,v48
v_pk_add_f32 v[36:37],v[36:37],v[168:169]
v_pk_fma_f32 v[44:45],v[168:169],v[168:169],v[44:45]
v_pk_add_f32 v[38:39],v[38:39],v[170:171]
v_pk_fma_f32 v[46:47],v[170:171],v[170:171],v[46:47]
v_smfmac_f32_16x16x64_bf16 v[180:183],a[132:135],a[64:71],v10
s_waitcnt lgkmcnt(4)
ds_read_b64 a[64:65],v28 offset:128
ds_read_b64 a[66:67],v28 offset:320
ds_read_b64 a[68:69],v29 offset:128
ds_read_b64 a[70:71],v29 offset:320
v_subrev_u32 v8,88400,v51
v_pk_add_f32 v[36:37],v[36:37],v[172:173]
v_pk_fma_f32 v[44:45],v[172:173],v[172:173],v[44:45]
v_pk_add_f32 v[38:39],v[38:39],v[174:175]
v_pk_fma_f32 v[46:47],v[174:175],v[174:175],v[46:47]
v_smfmac_f32_16x16x64_bf16 v[184:187],a[136:139],a[48:55],v10
s_waitcnt lgkmcnt(4)
ds_read_b64 a[48:49],v28 offset:136
ds_read_b64 a[50:51],v28 offset:328
ds_read_b64 a[52:53],v29 offset:136
ds_read_b64 a[54:55],v29 offset:328
v_min_u32 v51,v51,v8
v_pk_add_f32 v[36:37],v[36:37],v[176:177]
v_pk_fma_f32 v[44:45],v[176:177],v[176:177],v[44:45]
v_pk_add_f32 v[38:39],v[38:39],v[178:179]
v_pk_fma_f32 v[46:47],v[178:179],v[178:179],v[46:47]
v_smfmac_f32_16x16x64_bf16 v[188:191],a[140:143],a[56:63],v10
s_waitcnt lgkmcnt(4)
ds_read_b64 a[56:57],v30 offset:128
ds_read_b64 a[58:59],v30 offset:320
ds_read_b64 a[60:61],v31 offset:128
ds_read_b64 a[62:63],v31 offset:320
v_xad_u32 v28,v11,32,v24
v_xad_u32 v29,v11,40,v25
v_pk_add_f32 v[36:37],v[36:37],v[180:181]
v_pk_fma_f32 v[44:45],v[180:181],v[180:181],v[44:45]
v_pk_add_f32 v[38:39],v[38:39],v[182:183]
v_pk_fma_f32 v[46:47],v[182:183],v[182:183],v[46:47]
v_smfmac_f32_16x16x64_bf16 v[192:195],a[160:163],a[64:71],v10
s_waitcnt lgkmcnt(4)
ds_read_b64 a[64:65],v30 offset:136
ds_read_b64 a[66:67],v30 offset:328
ds_read_b64 a[68:69],v31 offset:136
ds_read_b64 a[70:71],v31 offset:328
v_add_u32 v50,3536,v51
v_pk_add_f32 v[36:37],v[36:37],v[184:185]
v_pk_fma_f32 v[44:45],v[184:185],v[184:185],v[44:45]
v_pk_add_f32 v[38:39],v[38:39],v[186:187]
v_pk_fma_f32 v[46:47],v[186:187],v[186:187],v[46:47]
v_smfmac_f32_16x16x64_bf16 v[196:199],a[164:167],a[48:55],v10
s_waitcnt lgkmcnt(4)
ds_read_b64 a[48:49],v28 offset:0
ds_read_b64 a[50:51],v28 offset:192
ds_read_b64 a[52:53],v29 offset:0
ds_read_b64 a[54:55],v29 offset:192
v_xad_u32 v30,v11,48,v24
v_xad_u32 v31,v11,56,v25
v_pk_add_f32 v[36:37],v[36:37],v[188:189]
v_pk_fma_f32 v[44:45],v[188:189],v[188:189],v[44:45]
v_pk_add_f32 v[38:39],v[38:39],v[190:191]
v_pk_fma_f32 v[46:47],v[190:191],v[190:191],v[46:47]
v_smfmac_f32_16x16x64_bf16 v[200:203],a[168:171],a[56:63],v10
s_waitcnt lgkmcnt(4)
ds_read_b64 a[56:57],v28 offset:8
ds_read_b64 a[58:59],v28 offset:200
ds_read_b64 a[60:61],v29 offset:8
ds_read_b64 a[62:63],v29 offset:200
v_subrev_u32 v8,88400,v50
v_pk_add_f32 v[36:37],v[36:37],v[192:193]
v_pk_fma_f32 v[44:45],v[192:193],v[192:193],v[44:45]
v_pk_add_f32 v[38:39],v[38:39],v[194:195]
v_pk_fma_f32 v[46:47],v[194:195],v[194:195],v[46:47]
v_smfmac_f32_16x16x64_bf16 v[204:207],a[172:175],a[64:71],v10
s_waitcnt lgkmcnt(4)
ds_read_b64 a[64:65],v28 offset:64
ds_read_b64 a[66:67],v28 offset:256
ds_read_b64 a[68:69],v29 offset:64
ds_read_b64 a[70:71],v29 offset:256
v_min_u32 v50,v50,v8
v_pk_add_f32 v[36:37],v[36:37],v[196:197]
v_pk_fma_f32 v[44:45],v[196:197],v[196:197],v[44:45]
v_pk_add_f32 v[38:39],v[38:39],v[198:199]
v_pk_fma_f32 v[46:47],v[198:199],v[198:199],v[46:47]
v_smfmac_f32_16x16x64_bf16 v[208:211],a[112:115],a[48:55],v10
s_waitcnt lgkmcnt(4)
ds_read_b64 a[48:49],v28 offset:72
ds_read_b64 a[50:51],v28 offset:264
ds_read_b64 a[52:53],v29 offset:72
ds_read_b64 a[54:55],v29 offset:264
v_xor_b32 v9,0x80008,v12
v_pk_add_f32 v[36:37],v[36:37],v[200:201]
v_pk_fma_f32 v[44:45],v[200:201],v[200:201],v[44:45]
v_pk_add_f32 v[38:39],v[38:39],v[202:203]
v_pk_fma_f32 v[46:47],v[202:203],v[202:203],v[46:47]
v_smfmac_f32_16x16x64_bf16 v[212:215],a[116:119],a[56:63],v10
s_waitcnt lgkmcnt(4)
ds_read_b64 a[56:57],v28 offset:128
ds_read_b64 a[58:59],v28 offset:320
ds_read_b64 a[60:61],v29 offset:128
ds_read_b64 a[62:63],v29 offset:320
v_add_u32_sdwa v48,v12,v51 dst_sel:DWORD dst_unused:UNUSED_PAD src0_sel:WORD_0 src1_sel:DWORD
v_pk_add_f32 v[36:37],v[36:37],v[204:205]
v_pk_fma_f32 v[44:45],v[204:205],v[204:205],v[44:45]
v_pk_add_f32 v[38:39],v[38:39],v[206:207]
v_pk_fma_f32 v[46:47],v[206:207],v[206:207],v[46:47]
v_smfmac_f32_16x16x64_bf16 v[216:219],a[144:147],a[64:71],v10
s_waitcnt lgkmcnt(4)
ds_read_b64 a[64:65],v28 offset:136
ds_read_b64 a[66:67],v28 offset:328
ds_read_b64 a[68:69],v29 offset:136
ds_read_b64 a[70:71],v29 offset:328
v_add_u32_sdwa v49,v12,v51 dst_sel:DWORD dst_unused:UNUSED_PAD src0_sel:WORD_1 src1_sel:DWORD
v_pk_add_f32 v[36:37],v[36:37],v[208:209]
v_pk_fma_f32 v[44:45],v[208:209],v[208:209],v[44:45]
v_pk_add_f32 v[38:39],v[38:39],v[210:211]
v_pk_fma_f32 v[46:47],v[210:211],v[210:211],v[46:47]
v_smfmac_f32_16x16x64_bf16 v[220:223],a[148:151],a[48:55],v10
s_waitcnt lgkmcnt(4)
ds_read_b64 a[48:49],v30 offset:0
ds_read_b64 a[50:51],v30 offset:192
ds_read_b64 a[52:53],v31 offset:0
ds_read_b64 a[54:55],v31 offset:192
v_add_u32_sdwa v51,v9,v50 dst_sel:DWORD dst_unused:UNUSED_PAD src0_sel:WORD_1 src1_sel:DWORD
v_pk_add_f32 v[36:37],v[36:37],v[212:213]
v_pk_fma_f32 v[44:45],v[212:213],v[212:213],v[44:45]
v_pk_add_f32 v[38:39],v[38:39],v[214:215]
v_pk_fma_f32 v[46:47],v[214:215],v[214:215],v[46:47]
v_smfmac_f32_16x16x64_bf16 v[224:227],a[176:179],a[56:63],v10
s_waitcnt lgkmcnt(4)
ds_read_b64 a[56:57],v30 offset:8
ds_read_b64 a[58:59],v30 offset:200
ds_read_b64 a[60:61],v31 offset:8
ds_read_b64 a[62:63],v31 offset:200
v_add_u32_sdwa v50,v9,v50 dst_sel:DWORD dst_unused:UNUSED_PAD src0_sel:WORD_0 src1_sel:DWORD
v_pk_add_f32 v[36:37],v[36:37],v[216:217]
v_pk_fma_f32 v[44:45],v[216:217],v[216:217],v[44:45]
v_pk_add_f32 v[38:39],v[38:39],v[218:219]
v_pk_fma_f32 v[46:47],v[218:219],v[218:219],v[46:47]
v_smfmac_f32_16x16x64_bf16 v[228:231],a[180:183],a[64:71],v10
s_waitcnt lgkmcnt(4)
ds_read_b64 a[64:65],v30 offset:64
ds_read_b64 a[66:67],v30 offset:256
ds_read_b64 a[68:69],v31 offset:64
ds_read_b64 a[70:71],v31 offset:256
v_pk_add_f32 v[36:37],v[36:37],v[220:221]
v_pk_fma_f32 v[44:45],v[220:221],v[220:221],v[44:45]
v_pk_add_f32 v[38:39],v[38:39],v[222:223]
v_pk_fma_f32 v[46:47],v[222:223],v[222:223],v[46:47]
v_smfmac_f32_16x16x64_bf16 v[232:235],a[120:123],a[48:55],v10
s_waitcnt lgkmcnt(4)
ds_read_b64 a[48:49],v30 offset:72
ds_read_b64 a[50:51],v30 offset:264
ds_read_b64 a[52:53],v31 offset:72
ds_read_b64 a[54:55],v31 offset:264
v_pk_add_f32 v[36:37],v[36:37],v[224:225]
v_pk_fma_f32 v[44:45],v[224:225],v[224:225],v[44:45]
v_pk_add_f32 v[38:39],v[38:39],v[226:227]
v_pk_fma_f32 v[46:47],v[226:227],v[226:227],v[46:47]
v_smfmac_f32_16x16x64_bf16 v[236:239],a[124:127],a[56:63],v10
s_waitcnt lgkmcnt(4)
ds_read_b64 a[56:57],v30 offset:128
ds_read_b64 a[58:59],v30 offset:320
ds_read_b64 a[60:61],v31 offset:128
ds_read_b64 a[62:63],v31 offset:320
v_pk_add_f32 v[36:37],v[36:37],v[228:229]
v_pk_fma_f32 v[44:45],v[228:229],v[228:229],v[44:45]
v_pk_add_f32 v[38:39],v[38:39],v[230:231]
v_pk_fma_f32 v[46:47],v[230:231],v[230:231],v[46:47]
v_smfmac_f32_16x16x64_bf16 v[240:243],a[152:155],a[64:71],v10
s_waitcnt lgkmcnt(4)
ds_read_b64 a[64:65],v30 offset:136
ds_read_b64 a[66:67],v30 offset:328
ds_read_b64 a[68:69],v31 offset:136
ds_read_b64 a[70:71],v31 offset:328
v_pk_add_f32 v[36:37],v[36:37],v[232:233]
v_pk_fma_f32 v[44:45],v[232:233],v[232:233],v[44:45]
v_pk_add_f32 v[38:39],v[38:39],v[234:235]
v_pk_fma_f32 v[46:47],v[234:235],v[234:235],v[46:47]
v_smfmac_f32_16x16x64_bf16 v[244:247],a[156:159],a[48:55],v10
s_waitcnt lgkmcnt(4)
ds_read_b64 a[224:225],v48 offset:57344
ds_read_b64 a[226:227],v48 offset:57352
ds_read_b64 a[228:229],v49 offset:57344
ds_read_b64 a[230:231],v49 offset:57352
ds_read_b128 a[240:243],v13 offset:0
v_pk_add_f32 v[36:37],v[36:37],v[236:237]
v_pk_fma_f32 v[44:45],v[236:237],v[236:237],v[44:45]
v_pk_add_f32 v[38:39],v[38:39],v[238:239]
v_pk_fma_f32 v[46:47],v[238:239],v[238:239],v[46:47]
v_smfmac_f32_16x16x64_bf16 v[248:251],a[184:187],a[56:63],v10
s_waitcnt lgkmcnt(5)
ds_read_b64 a[232:233],v50 offset:57344
ds_read_b64 a[234:235],v50 offset:57352
ds_read_b64 a[236:237],v51 offset:57344
ds_read_b64 a[238:239],v51 offset:57352
ds_read_b128 a[244:247],v13 offset:256
v_pk_add_f32 v[36:37],v[36:37],v[240:241]
v_pk_fma_f32 v[44:45],v[240:241],v[240:241],v[44:45]
v_pk_add_f32 v[38:39],v[38:39],v[242:243]
v_pk_fma_f32 v[46:47],v[242:243],v[242:243],v[46:47]
v_smfmac_f32_16x16x64_bf16 v[252:255],a[188:191],a[64:71],v10
ds_read_b128 v[28:31],v4 offset:3584
ds_read_b128 v[24:27],v4 offset:3616
v_pk_add_f32 v[36:37],v[36:37],v[244:245]
v_pk_fma_f32 v[44:45],v[244:245],v[244:245],v[44:45]
v_pk_add_f32 v[38:39],v[38:39],v[246:247]
v_pk_fma_f32 v[46:47],v[246:247],v[246:247],v[46:47]
v_pk_add_f32 v[36:37],v[36:37],v[248:249]
v_pk_fma_f32 v[44:45],v[248:249],v[248:249],v[44:45]
v_pk_add_f32 v[38:39],v[38:39],v[250:251]
v_pk_fma_f32 v[46:47],v[250:251],v[250:251],v[46:47]
v_pk_add_f32 v[36:37],v[36:37],v[252:253]
v_pk_fma_f32 v[44:45],v[252:253],v[252:253],v[44:45]
v_pk_add_f32 v[38:39],v[38:39],v[254:255]
v_pk_fma_f32 v[46:47],v[254:255],v[254:255],v[46:47]
s_nop 0
v_permlane32_swap_b32 v36,v38
v_permlane32_swap_b32 v37,v39
v_permlane32_swap_b32 v44,v46
v_permlane32_swap_b32_e64 v45,v47
v_pk_add_f32 v[36:37],v[36:37],v[38:39]
v_pk_add_f32 v[44:45],v[44:45],v[46:47]
s_add_u32 s60,s60,56576
s_sub_u32 s64,s60,88400
s_min_u32 s60,s60,s64
v_permlane16_swap_b32 v36,v37
v_permlane16_swap_b32 v44,v45
v_add_f32 v8,v36,v37
v_add_f32_e64 v9,v44,v45
v_mul_f32 v8,0x3c2aaaab,v8
v_mul_f32 v9,0x3c2aaaab,v9
v_cmp_ngt_f32 vcc,0x5d800000,v9
v_fma_f32 v9,-v8,v8,v9
v_max_f32 v9,0,v9
s_or_b64 s[90:91],s[90:91],vcc
v_add_f32 v9,0x3727c5ac,v9
v_rsq_f32 v9,v9
s_nop 0
v_mul_f32 v36,s96,v9
v_mul_f32 v37,s96,v9
v_mul_f32 v44,-v8,v36
v_mul_f32 v45,-v8,v36
v_permlane16_swap_b32 v36,v37
s_nop 0
v_permlane16_swap_b32 v44,v45
v_mov_b64 v[38:39],v[36:37]
v_mov_b64 v[46:47],v[44:45]
s_nop 0
v_permlane32_swap_b32 v36,v38
v_permlane32_swap_b32 v37,v39
v_permlane32_swap_b32 v44,v46
v_permlane32_swap_b32 v45,v47
v_pk_fma_f32 v[160:161],v[160:161],v[36:37],v[44:45]
v_pk_fma_f32 v[162:163],v[162:163],v[38:39],v[46:47]
v_pk_fma_f32 v[164:165],v[164:165],v[36:37],v[44:45]
v_pk_fma_f32 v[166:167],v[166:167],v[38:39],v[46:47]
v_pk_fma_f32 v[168:169],v[168:169],v[36:37],v[44:45]
v_pk_fma_f32 v[170:171],v[170:171],v[38:39],v[46:47]
v_pk_fma_f32 v[172:173],v[172:173],v[36:37],v[44:45]
v_pk_fma_f32 v[174:175],v[174:175],v[38:39],v[46:47]
v_pk_fma_f32 v[208:209],v[208:209],v[36:37],v[44:45]
v_pk_fma_f32 v[210:211],v[210:211],v[38:39],v[46:47]
v_pk_fma_f32 v[212:213],v[212:213],v[36:37],v[44:45]
v_pk_fma_f32 v[214:215],v[214:215],v[38:39],v[46:47]
v_pk_fma_f32 v[232:233],v[232:233],v[36:37],v[44:45]
v_pk_fma_f32 v[234:235],v[234:235],v[38:39],v[46:47]
v_pk_fma_f32 v[236:237],v[236:237],v[36:37],v[44:45]
v_pk_fma_f32 v[238:239],v[238:239],v[38:39],v[46:47]
v_pk_fma_f32 v[176:177],v[176:177],v[36:37],v[44:45]
v_pk_fma_f32 v[178:179],v[178:179],v[38:39],v[46:47]
v_pk_fma_f32 v[180:181],v[180:181],v[36:37],v[44:45]
v_pk_fma_f32 v[182:183],v[182:183],v[38:39],v[46:47]
v_pk_fma_f32 v[184:185],v[184:185],v[36:37],v[44:45]
v_pk_fma_f32 v[186:187],v[186:187],v[38:39],v[46:47]
v_pk_fma_f32 v[188:189],v[188:189],v[36:37],v[44:45]
v_pk_fma_f32 v[190:191],v[190:191],v[38:39],v[46:47]
v_pk_fma_f32 v[216:217],v[216:217],v[36:37],v[44:45]
v_pk_fma_f32 v[218:219],v[218:219],v[38:39],v[46:47]
v_pk_fma_f32 v[220:221],v[220:221],v[36:37],v[44:45]
v_pk_fma_f32 v[222:223],v[222:223],v[38:39],v[46:47]
v_pk_fma_f32 v[240:241],v[240:241],v[36:37],v[44:45]
v_pk_fma_f32 v[242:243],v[242:243],v[38:39],v[46:47]
v_pk_fma_f32 v[244:245],v[244:245],v[36:37],v[44:45]
v_pk_fma_f32 v[246:247],v[246:247],v[38:39],v[46:47]
v_pk_fma_f32 v[192:193],v[192:193],v[36:37],v[44:45]
v_pk_fma_f32 v[194:195],v[194:195],v[38:39],v[46:47]
v_pk_fma_f32 v[196:197],v[196:197],v[36:37],v[44:45]
v_pk_fma_f32 v[198:199],v[198:199],v[38:39],v[46:47]
v_pk_fma_f32 v[200:201],v[200:201],v[36:37],v[44:45]
v_pk_fma_f32 v[202:203],v[202:203],v[38:39],v[46:47]
v_pk_fma_f32 v[204:205],v[204:205],v[36:37],v[44:45]
v_pk_fma_f32 v[206:207],v[206:207],v[38:39],v[46:47]
v_pk_fma_f32 v[224:225],v[224:225],v[36:37],v[44:45]
v_pk_fma_f32 v[226:227],v[226:227],v[38:39],v[46:47]
v_pk_fma_f32 v[228:229],v[228:229],v[36:37],v[44:45]
v_pk_fma_f32 v[230:231],v[230:231],v[38:39],v[46:47]
v_pk_fma_f32 v[248:249],v[248:249],v[36:37],v[44:45]
v_pk_fma_f32 v[250:251],v[250:251],v[38:39],v[46:47]
v_pk_fma_f32 v[252:253],v[252:253],v[36:37],v[44:45]
v_pk_fma_f32 v[254:255],v[254:255],v[38:39],v[46:47]
v_mfma_f32_16x16x32_bf16 v[40:43],a[204:207],a[8:11],v[20:23]
ds_read_b64_tr_b16 a[216:217],v1 offset:6160
v_cvt_pk_bf16_f32 v160,v160,v164
v_cvt_pk_bf16_f32 v168,v168,v172
v_cvt_pk_bf16_f32 v208,v208,v212
v_lshlrev_b32_sdwa v32,1,v32 dst_sel:DWORD dst_unused:UNUSED_PAD src0_sel:DWORD src1_sel:WORD_1
v_mfma_f32_16x16x32_bf16 v[40:43],a[208:211],a[24:27],v[40:43]
ds_read_u16 v32,v32
v_accvgpr_write_b32 a48,v160
v_accvgpr_write_b32 a49,v168
v_cvt_pk_bf16_f32 v232,v232,v236
v_accvgpr_write_b32 a50,v208
v_mfma_f32_16x16x32_bf16 v[40:43],a[212:215],a[40:43],v[40:43]
ds_read_b64_tr_b16 a[218:219],v1 offset:7184
v_cvt_pk_bf16_f32 v161,v161,v165
v_accvgpr_write_b32 a51,v232
v_cvt_pk_bf16_f32 v169,v169,v173
v_accvgpr_write_b32 a52,v161
v_mfma_f32_16x16x32_bf16 v[36:39],a[200:203],a[40:43],v[16:19]
ds_read_b64_tr_b16 a[220:221],v1 offset:8208
v_cvt_pk_bf16_f32 v209,v209,v213
v_accvgpr_write_b32 a53,v169
v_cvt_pk_bf16_f32 v233,v233,v237
v_lshlrev_b32_sdwa v34,1,v34 dst_sel:DWORD dst_unused:UNUSED_PAD src0_sel:DWORD src1_sel:WORD_1
v_accvgpr_write_b32 a54,v209
v_mfma_f32_16x16x32_bf16 v[36:39],a[196:199],a[24:27],v[36:39]
ds_read_u16_d16_hi v34,v34
v_cvt_pk_bf16_f32 v162,v162,v166
v_accvgpr_write_b32 a55,v233
v_cvt_pk_bf16_f32 v170,v170,v174
v_accvgpr_write_b32 a56,v162
v_mfma_f32_16x16x32_bf16 v[36:39],a[192:195],a[8:11],v[36:39]
ds_read_b64_tr_b16 a[222:223],v1 offset:9232
v_accvgpr_write_b32 a57,v170
v_cvt_pk_bf16_f32 v210,v210,v214
v_accvgpr_write_b32 a58,v210
v_lshlrev_b32_sdwa v33,1,v33 dst_sel:DWORD dst_unused:UNUSED_PAD src0_sel:DWORD src1_sel:WORD_1
v_cvt_pk_bf16_f32 v234,v234,v238
v_mfma_f32_16x16x32_bf16 v[44:47],a[192:195],a[12:15],v[16:19]
ds_read_u16 v33,v33
v_accvgpr_write_b32 a59,v234
v_cvt_pk_bf16_f32 v163,v163,v167
v_accvgpr_write_b32 a60,v163
v_mfma_f32_16x16x32_bf16 v[44:47],a[196:199],a[28:31],v[44:47]
v_cvt_pk_bf16_f32 v171,v171,v175
v_accvgpr_write_b32 a61,v171
v_cvt_pk_bf16_f32 v211,v211,v215
v_accvgpr_write_b32 a62,v211
v_lshlrev_b32_sdwa v35,1,v35 dst_sel:DWORD dst_unused:UNUSED_PAD src0_sel:DWORD src1_sel:WORD_1
v_mfma_f32_16x16x32_bf16 v[44:47],a[200:203],a[44:47],v[44:47]
s_waitcnt lgkmcnt(7)
ds_read_u16_d16_hi v35,v35
v_cvt_pk_bf16_f32 v235,v235,v239
v_accvgpr_write_b32 a63,v235
v_cvt_pk_bf16_f32 v176,v176,v180
v_cvt_pk_bf16_f32 v184,v184,v188
v_mfma_f32_16x16x32_bf16 v[232:235],a[240:243],a[224:227],v[28:31]
s_nop 0
v_cvt_pk_bf16_f32 v216,v216,v220
v_accvgpr_write_b32 a64,v176
v_accvgpr_write_b32 a65,v184
v_cvt_pk_bf16_f32 v240,v240,v244
v_lshlrev_b32_sdwa v60,1,v60 dst_sel:DWORD dst_unused:UNUSED_PAD src0_sel:DWORD src1_sel:WORD_1
v_mfma_f32_16x16x32_bf16 v[236:239],a[240:243],a[228:231],v[28:31]
ds_read_u16 v60,v60
v_accvgpr_write_b32 a66,v216
v_cvt_pk_bf16_f32 v177,v177,v181
v_accvgpr_write_b32 a67,v240
v_cvt_pk_bf16_f32 v185,v185,v189
v_mfma_f32_16x16x32_bf16 v[208:211],a[240:243],a[232:235],v[28:31]
v_accvgpr_write_b32 a68,v177
v_cvt_pk_bf16_f32 v217,v217,v221
v_accvgpr_write_b32 a69,v185
v_lshlrev_b32_sdwa v62,1,v62 dst_sel:DWORD dst_unused:UNUSED_PAD src0_sel:DWORD src1_sel:WORD_1
v_cvt_pk_bf16_f32 v241,v241,v245
v_mfma_f32_16x16x32_bf16 v[212:215],a[240:243],a[236:239],v[28:31]
ds_read_b128 a[240:243],v13 offset:512
ds_read_b128 v[28:31],v4 offset:3712
v_accvgpr_write_b32 a70,v217
v_cvt_pk_bf16_f32 v178,v178,v182
v_accvgpr_write_b32 a71,v241
v_mfma_f32_16x16x32_bf16 v[160:163],a[244:247],a[224:227],v[24:27]
ds_read_b64 a[224:225],v48 offset:57408
ds_read_b64 a[226:227],v48 offset:57416
v_cvt_pk_bf16_f32 v186,v186,v190
v_accvgpr_write_b32 a72,v178
v_accvgpr_write_b32 a73,v186
v_cvt_pk_bf16_f32 v218,v218,v222
v_lshlrev_b32_sdwa v61,1,v61 dst_sel:DWORD dst_unused:UNUSED_PAD src0_sel:DWORD src1_sel:WORD_1
v_mfma_f32_16x16x32_bf16 v[164:167],a[244:247],a[228:231],v[24:27]
ds_read_b64 a[228:229],v49 offset:57408
ds_read_b64 a[230:231],v49 offset:57416
v_accvgpr_write_b32 a74,v218
v_cvt_pk_bf16_f32 v242,v242,v246
v_accvgpr_write_b32 a75,v242
v_cvt_pk_bf16_f32 v179,v179,v183
v_mfma_f32_16x16x32_bf16 v[168:171],a[244:247],a[232:235],v[24:27]
ds_read_b64 a[232:233],v50 offset:57408
ds_read_b64 a[234:235],v50 offset:57416
v_accvgpr_write_b32 a76,v179
v_cvt_pk_bf16_f32 v187,v187,v191
v_accvgpr_write_b32 a77,v187
v_cvt_pk_bf16_f32 v219,v219,v223
v_lshlrev_b32_sdwa v63,1,v63 dst_sel:DWORD dst_unused:UNUSED_PAD src0_sel:DWORD src1_sel:WORD_1
v_mfma_f32_16x16x32_bf16 v[172:175],a[244:247],a[236:239],v[24:27]
s_waitcnt lgkmcnt(4)
ds_read_b64 a[236:237],v51 offset:57408
ds_read_b64 a[238:239],v51 offset:57416
v_accvgpr_write_b32 a78,v219
v_cvt_pk_bf16_f32 v243,v243,v247
v_accvgpr_write_b32 a79,v243
v_cvt_pk_bf16_f32 v192,v192,v196
v_mfma_f32_16x16x32_bf16 v[240:243],a[240:243],a[224:227],v[28:31]
s_waitcnt lgkmcnt(4)
ds_read_b128 a[244:247],v13 offset:768
ds_read_b128 v[24:27],v4 offset:3744
v_cvt_pk_bf16_f32 v200,v200,v204
v_cvt_pk_bf16_f32 v224,v224,v228
v_accvgpr_write_b32 a80,v192
v_accvgpr_write_b32 a81,v200
v_lshlrev_b32_sdwa v52,1,v52 dst_sel:DWORD dst_unused:UNUSED_PAD src0_sel:DWORD src1_sel:WORD_1
v_mfma_f32_16x16x32_bf16 v[244:247],a[240:243],a[228:231],v[28:31]
s_waitcnt lgkmcnt(4)
ds_read_u16_d16_hi v62,v62
v_cvt_pk_bf16_f32 v248,v248,v252
v_accvgpr_write_b32 a82,v224
v_cvt_pk_bf16_f32 v193,v193,v197
v_mfma_f32_16x16x32_bf16 v[216:219],a[240:243],a[232:235],v[28:31]
s_waitcnt lgkmcnt(3)
ds_read_u16 v61,v61
v_accvgpr_write_b32 a83,v248
v_cvt_pk_bf16_f32 v201,v201,v205
v_accvgpr_write_b32 a84,v193
v_cvt_pk_bf16_f32 v225,v225,v229
v_lshlrev_b32_sdwa v54,1,v54 dst_sel:DWORD dst_unused:UNUSED_PAD src0_sel:DWORD src1_sel:WORD_1
v_mfma_f32_16x16x32_bf16 v[220:223],a[240:243],a[236:239],v[28:31]
s_waitcnt lgkmcnt(2)
ds_read_b128 a[240:243],v13 offset:1024
ds_read_b128 v[28:31],v4 offset:3840
v_accvgpr_write_b32 a85,v201
v_cvt_pk_bf16_f32 v249,v249,v253
v_accvgpr_write_b32 a86,v225
v_cvt_pk_bf16_f32 v194,v194,v198
v_mfma_f32_16x16x32_bf16 v[176:179],a[244:247],a[224:227],v[24:27]
s_nop 0
ds_read_b64 a[224:225],v48 offset:57472
ds_read_b64 a[226:227],v48 offset:57480
v_accvgpr_write_b32 a87,v249
v_cvt_pk_bf16_f32 v202,v202,v206
v_accvgpr_write_b32 a88,v194
v_accvgpr_write_b32 a89,v202
v_lshlrev_b32_sdwa v53,1,v53 dst_sel:DWORD dst_unused:UNUSED_PAD src0_sel:DWORD src1_sel:WORD_1
v_mfma_f32_16x16x32_bf16 v[180:183],a[244:247],a[228:231],v[24:27]
ds_read_b64 a[228:229],v49 offset:57472
ds_read_b64 a[230:231],v49 offset:57480
v_cvt_pk_bf16_f32 v226,v226,v230
v_accvgpr_write_b32 a90,v226
v_cvt_pk_bf16_f32 v250,v250,v254
v_accvgpr_write_b32 a91,v250
v_mfma_f32_16x16x32_bf16 v[184:187],a[244:247],a[232:235],v[24:27]
s_nop 0
ds_read_b64 a[232:233],v50 offset:57472
ds_read_b64 a[234:235],v50 offset:57480
v_cvt_pk_bf16_f32 v195,v195,v199
v_accvgpr_write_b32 a92,v195
v_cvt_pk_bf16_f32 v203,v203,v207
v_accvgpr_write_b32 a93,v203
v_mfma_f32_16x16x32_bf16 v[188:191],a[244:247],a[236:239],v[24:27]
s_waitcnt lgkmcnt(4)
ds_read_b64 a[236:237],v51 offset:57472
ds_read_b64 a[238:239],v51 offset:57480
v_cvt_pk_bf16_f32 v227,v227,v231
v_accvgpr_write_b32 a94,v227
v_cvt_pk_bf16_f32 v251,v251,v255
TP2753_%=:
s_cbranch_execz TP4880_%=
v_lshlrev_b32_sdwa v55,1,v55 dst_sel:DWORD dst_unused:UNUSED_PAD src0_sel:DWORD src1_sel:WORD_1
v_accvgpr_write_b32 a95,v251
v_mfma_f32_16x16x32_bf16 v[248:251],a[240:243],a[224:227],v[28:31]
s_waitcnt lgkmcnt(4)
ds_read_b128 a[244:247],v13 offset:1280
ds_read_b128 v[24:27],v4 offset:3872
v_mfma_f32_16x16x32_bf16 v[252:255],a[240:243],a[228:231],v[28:31]
s_waitcnt lgkmcnt(4)
ds_read_u16_d16_hi v63,v63
ds_read_u16 v52,v52
v_lshlrev_b32_sdwa v56,1,v56 dst_sel:DWORD dst_unused:UNUSED_PAD src0_sel:DWORD src1_sel:WORD_1
v_mfma_f32_16x16x32_bf16 v[224:227],a[240:243],a[232:235],v[28:31]
s_waitcnt lgkmcnt(4)
ds_read_u16_d16_hi v54,v54
v_lshlrev_b32_sdwa v58,1,v58 dst_sel:DWORD dst_unused:UNUSED_PAD src0_sel:DWORD src1_sel:WORD_1
v_mfma_f32_16x16x32_bf16 v[228:231],a[240:243],a[236:239],v[28:31]
s_waitcnt lgkmcnt(3)
ds_read_b64_tr_b16 a[240:241],v3 offset:6224
ds_read_b64_tr_b16 a[242:243],v3 offset:7760
v_mfma_f32_16x16x32_bf16 v[192:195],a[244:247],a[224:227],v[24:27]
ds_read_b64_tr_b16 a[224:225],v3 offset:6232
ds_read_b64_tr_b16 a[226:227],v3 offset:7768
v_mfma_f32_16x16x32_bf16 v[196:199],a[244:247],a[228:231],v[24:27]
ds_read_b64_tr_b16 a[228:229],v3 offset:6288
ds_read_b64_tr_b16 a[230:231],v3 offset:7824
v_lshlrev_b32_sdwa v57,1,v57 dst_sel:DWORD dst_unused:UNUSED_PAD src0_sel:DWORD src1_sel:WORD_1
v_mfma_f32_16x16x32_bf16 v[200:203],a[244:247],a[232:235],v[24:27]
ds_read_b64_tr_b16 a[232:233],v3 offset:6296
ds_read_b64_tr_b16 a[234:235],v3 offset:7832
v_mfma_f32_16x16x32_bf16 v[204:207],a[244:247],a[236:239],v[24:27]
ds_read_u16 v53,v53
ds_read_u16_d16_hi v55,v55
v_lshlrev_b32_sdwa v59,1,v59 dst_sel:DWORD dst_unused:UNUSED_PAD src0_sel:DWORD src1_sel:WORD_1
v_mfma_f32_16x16x32_bf16 v[48:51],a[212:215],a[44:47],v[20:23]
ds_read_u16 v56,v56
v_mfma_f32_16x16x32_bf16 v[48:51],a[208:211],a[28:31],v[48:51]
s_waitcnt vmcnt(9)
ds_read_u16_d16_hi v58,v58
v_mfma_f32_16x16x32_bf16 v[48:51],a[204:207],a[12:15],v[48:51]
s_barrier
ds_read_u16 v57,v57
ds_read_u16_d16_hi v59,v59
v_mfma_f32_16x16x32_bf16 v[28:31],a[204:207],a[48:51],v[20:23]
s_add_u32 m0,s57,56576
ds_read_b64 v[14:15],v7 offset:6160
v_lshlrev_b32_sdwa v40,1,v40 dst_sel:DWORD dst_unused:UNUSED_PAD src0_sel:DWORD src1_sel:WORD_1
v_mfma_f32_16x16x32_bf16 v[28:31],a[208:211],a[64:67],v[28:31]
s_add_u32 s89,s56,4030464
ds_read_u16 v40,v40
v_lshl_add_u64 v[32:33],v[32:33],0,v[34:35]
v_mfma_f32_16x16x32_bf16 v[28:31],a[212:215],a[80:83],v[28:31]
s_waitcnt lgkmcnt(12)
buffer_load_dwordx4 v6,s[36:39],s89 offen lds
v_lshl_add_u64 v[34:35],v[60:61],0,v[62:63]
v_lshlrev_b32_sdwa v42,1,v42 dst_sel:DWORD dst_unused:UNUSED_PAD src0_sel:DWORD src1_sel:WORD_1
v_mfma_f32_16x16x32_bf16 v[24:27],a[200:203],a[80:83],v[16:19]
ds_read_u16_d16_hi v42,v42
v_lshlrev_b32_sdwa v41,1,v41 dst_sel:DWORD dst_unused:UNUSED_PAD src0_sel:DWORD src1_sel:WORD_1
v_mfma_f32_16x16x32_bf16 v[24:27],a[196:199],a[64:67],v[24:27]
s_waitcnt lgkmcnt(7)
ds_read_u16 v41,v41
v_lshl_add_u64 v[60:61],v[52:53],0,v[54:55]
v_mfma_f32_16x16x32_bf16 v[24:27],a[192:195],a[48:51],v[24:27]
s_waitcnt lgkmcnt(3)
v_or3_b32 v13,v14,v15,0
v_lshlrev_b32_sdwa v43,1,v43 dst_sel:DWORD dst_unused:UNUSED_PAD src0_sel:DWORD src1_sel:WORD_1
v_mfma_f32_16x16x32_bf16 v[52:55],a[192:195],a[52:55],v[16:19]
ds_read_b64 v[14:15],v7 offset:6672
v_lshlrev_b32_sdwa v36,1,v36 dst_sel:DWORD dst_unused:UNUSED_PAD src0_sel:DWORD src1_sel:WORD_1
v_mfma_f32_16x16x32_bf16 v[52:55],a[196:199],a[68:71],v[52:55]
ds_read_u16_d16_hi v43,v43
v_lshl_add_u64 v[62:63],v[56:57],0,v[58:59]
v_lshlrev_b32_sdwa v38,1,v38 dst_sel:DWORD dst_unused:UNUSED_PAD src0_sel:DWORD src1_sel:WORD_1
v_mfma_f32_16x16x32_bf16 v[52:55],a[200:203],a[84:87],v[52:55]
ds_read_u16 v36,v36
v_lshlrev_b32_sdwa v37,1,v37 dst_sel:DWORD dst_unused:UNUSED_PAD src0_sel:DWORD src1_sel:WORD_1
v_mfma_f32_16x16x32_bf16 v[56:59],a[212:215],a[84:87],v[20:23]
ds_read_u16_d16_hi v38,v38
v_lshlrev_b32_sdwa v39,1,v39 dst_sel:DWORD dst_unused:UNUSED_PAD src0_sel:DWORD src1_sel:WORD_1
v_mfma_f32_16x16x32_bf16 v[56:59],a[208:211],a[68:71],v[56:59]
ds_read_u16 v37,v37
v_lshlrev_b32_sdwa v48,1,v48 dst_sel:DWORD dst_unused:UNUSED_PAD src0_sel:DWORD src1_sel:WORD_1
v_mfma_f32_16x16x32_bf16 v[56:59],a[204:207],a[52:55],v[56:59]
s_waitcnt lgkmcnt(4)
ds_read_u16_d16_hi v39,v39
v_or3_b32 v13,v14,v15,v13
v_mfma_f32_16x16x32_bf16 v[136:139],a[248:251],v[32:35],v[136:139]
ds_read_b64 v[14:15],v7 offset:7184
v_lshlrev_b32_sdwa v50,1,v50 dst_sel:DWORD dst_unused:UNUSED_PAD src0_sel:DWORD src1_sel:WORD_1
v_mfma_f32_16x16x32_bf16 v[140:143],a[248:251],v[60:63],v[140:143]
ds_read_u16 v48,v48
v_lshlrev_b32_sdwa v49,1,v49 dst_sel:DWORD dst_unused:UNUSED_PAD src0_sel:DWORD src1_sel:WORD_1
v_mfma_f32_16x16x32_bf16 v[68:71],a[252:255],v[60:63],v[68:71]
ds_read_u16_d16_hi v50,v50
v_lshlrev_b32_sdwa v51,1,v51 dst_sel:DWORD dst_unused:UNUSED_PAD src0_sel:DWORD src1_sel:WORD_1
v_mfma_f32_16x16x32_bf16 v[64:67],a[252:255],v[32:35],v[64:67]
ds_read_u16 v49,v49
v_lshlrev_b32_sdwa v44,1,v44 dst_sel:DWORD dst_unused:UNUSED_PAD src0_sel:DWORD src1_sel:WORD_1
v_mfma_f32_16x16x32_bf16 v[144:147],a[240:243],v[32:35],v[144:147]
ds_read_u16_d16_hi v51,v51
v_lshlrev_b32_sdwa v46,1,v46 dst_sel:DWORD dst_unused:UNUSED_PAD src0_sel:DWORD src1_sel:WORD_1
v_mfma_f32_16x16x32_bf16 v[148:151],a[240:243],v[60:63],v[148:151]
s_waitcnt lgkmcnt(4)
ds_read_u16 v44,v44
v_or3_b32 v13,v14,v15,v13
v_mfma_f32_16x16x32_bf16 v[84:87],a[224:227],v[60:63],v[84:87]
ds_read_u16_d16_hi v46,v46
v_lshlrev_b32_sdwa v45,1,v45 dst_sel:DWORD dst_unused:UNUSED_PAD src0_sel:DWORD src1_sel:WORD_1
v_mfma_f32_16x16x32_bf16 v[80:83],a[224:227],v[32:35],v[80:83]
ds_read_u16 v45,v45
v_lshlrev_b32_sdwa v47,1,v47 dst_sel:DWORD dst_unused:UNUSED_PAD src0_sel:DWORD src1_sel:WORD_1
v_mfma_f32_16x16x32_bf16 v[152:155],a[228:231],v[32:35],v[152:155]
ds_read_u16_d16_hi v47,v47
v_mfma_f32_16x16x32_bf16 v[156:159],a[228:231],v[60:63],v[156:159]
v_mfma_f32_16x16x32_bf16 v[100:103],a[232:235],v[60:63],v[100:103]
v_mfma_f32_16x16x32_bf16 v[96:99],a[232:235],v[32:35],v[96:99]
s_add_u32 m0,s57,60120
v_mfma_f32_16x16x32_bf16 v[60:63],a[204:207],a[56:59],v[20:23]
s_add_u32 s89,s56,4128768
v_lshlrev_b32_sdwa v28,1,v28 dst_sel:DWORD dst_unused:UNUSED_PAD src0_sel:DWORD src1_sel:WORD_1
v_fma_mixlo_f16 v8,v53,s99,0
v_mfma_f32_16x16x32_bf16 v[60:63],a[208:211],a[72:75],v[60:63]
buffer_load_dwordx4 v6,s[36:39],s89 offen lds
v_lshl_add_u64 v[40:41],v[40:41],0,v[42:43]
v_mfma_f32_16x16x32_bf16 v[60:63],a[212:215],a[88:91],v[60:63]
ds_read_u16 v28,v28
v_lshl_add_u64 v[42:43],v[36:37],0,v[38:39]
v_lshlrev_b32_sdwa v30,1,v30 dst_sel:DWORD dst_unused:UNUSED_PAD src0_sel:DWORD src1_sel:WORD_1
v_mfma_f32_16x16x32_bf16 v[32:35],a[200:203],a[88:91],v[16:19]
ds_read_u16_d16_hi v30,v30
v_fma_mixhi_f16 v8,v55,s99,0
v_mfma_f32_16x16x32_bf16 v[32:35],a[196:199],a[72:75],v[32:35]
s_waitcnt lgkmcnt(3)
v_lshl_add_u64 v[36:37],v[48:49],0,v[50:51]
v_lshlrev_b32_sdwa v29,1,v29 dst_sel:DWORD dst_unused:UNUSED_PAD src0_sel:DWORD src1_sel:WORD_1
v_pk_fma_f16 v9,v8,v8,s54
v_mfma_f32_16x16x32_bf16 v[32:35],a[192:195],a[56:59],v[32:35]
ds_read_u16 v29,v29
v_lshlrev_b32_sdwa v31,1,v31 dst_sel:DWORD dst_unused:UNUSED_PAD src0_sel:DWORD src1_sel:WORD_1
v_mfma_f32_16x16x32_bf16 v[48:51],a[192:195],a[60:63],v[16:19]
ds_read_b128 v[16:19],v4 offset:768
ds_read_b64_tr_b16 a[192:193],v1 offset:10256
v_pk_mul_f16 v9,v8,v9
v_mfma_f32_16x16x32_bf16 v[48:51],a[196:199],a[76:79],v[48:51]
s_waitcnt lgkmcnt(5)
ds_read_b64_tr_b16 a[194:195],v1 offset:11280
ds_read_b64_tr_b16 a[196:197],v2 offset:10256
v_lshl_add_u64 v[38:39],v[44:45],0,v[46:47]
v_lshlrev_b32_sdwa v24,1,v24 dst_sel:DWORD dst_unused:UNUSED_PAD src0_sel:DWORD src1_sel:WORD_1
v_mfma_f32_16x16x32_bf16 v[48:51],a[200:203],a[92:95],v[48:51]
ds_read_b64_tr_b16 a[198:199],v2 offset:11280
ds_read_b64_tr_b16 a[200:201],v2 offset:8208
v_exp_f16 v8,v9
v_mfma_f32_16x16x32_bf16 v[44:47],a[212:215],a[92:95],v[20:23]
ds_read_b128 v[20:23],v4 offset:800
ds_read_b64_tr_b16 a[202:203],v2 offset:9232
v_lshlrev_b32_sdwa v26,1,v26 dst_sel:DWORD dst_unused:UNUSED_PAD src0_sel:DWORD src1_sel:WORD_1
v_mfma_f32_16x16x32_bf16 v[44:47],a[208:211],a[76:79],v[44:47]
ds_read_b64_tr_b16 a[212:213],v2 offset:6160
ds_read_b64_tr_b16 a[214:215],v2 offset:7184
v_lshlrev_b32_sdwa v25,1,v25 dst_sel:DWORD dst_unused:UNUSED_PAD src0_sel:DWORD src1_sel:WORD_1
v_mfma_f32_16x16x32_bf16 v[44:47],a[204:207],a[60:63],v[44:47]
ds_read_u16_d16_hi v31,v31
v_exp_f16_sdwa v8,v9 dst_sel:WORD_1 dst_unused:UNUSED_PRESERVE src0_sel:WORD_1
v_mfma_f32_16x16x32_bf16 v[112:115],a[248:251],v[40:43],v[112:115]
ds_read_u16 v24,v24
v_lshlrev_b32_sdwa v27,1,v27 dst_sel:DWORD dst_unused:UNUSED_PAD src0_sel:DWORD src1_sel:WORD_1
v_mfma_f32_16x16x32_bf16 v[116:119],a[248:251],v[36:39],v[116:119]
ds_read_u16_d16_hi v26,v26
v_lshlrev_b32_sdwa v56,1,v56 dst_sel:DWORD dst_unused:UNUSED_PAD src0_sel:DWORD src1_sel:WORD_1
v_mfma_f32_16x16x32_bf16 v[76:79],a[252:255],v[36:39],v[76:79]
ds_read_u16 v25,v25
v_pk_fma_f16 v8,v8,s55,s55
v_mfma_f32_16x16x32_bf16 v[72:75],a[252:255],v[40:43],v[72:75]
v_rcp_f16 v9,v8
v_lshlrev_b32_sdwa v58,1,v58 dst_sel:DWORD dst_unused:UNUSED_PAD src0_sel:DWORD src1_sel:WORD_1
v_mfma_f32_16x16x32_bf16 v[120:123],a[240:243],v[40:43],v[120:123]
ds_read_u16_d16_hi v27,v27
v_lshlrev_b32_sdwa v57,1,v57 dst_sel:DWORD dst_unused:UNUSED_PAD src0_sel:DWORD src1_sel:WORD_1
v_mfma_f32_16x16x32_bf16 v[124:127],a[240:243],v[36:39],v[124:127]
ds_read_u16 v56,v56
v_lshlrev_b32_sdwa v59,1,v59 dst_sel:DWORD dst_unused:UNUSED_PAD src0_sel:DWORD src1_sel:WORD_1
v_mfma_f32_16x16x32_bf16 v[92:95],a[224:227],v[36:39],v[92:95]
ds_read_u16_d16_hi v58,v58
v_lshlrev_b32_sdwa v52,1,v52 dst_sel:DWORD dst_unused:UNUSED_PAD src0_sel:DWORD src1_sel:WORD_1
v_mfma_f32_16x16x32_bf16 v[88:91],a[224:227],v[40:43],v[88:91]
v_rcp_f16_sdwa v9,v8 dst_sel:WORD_1 dst_unused:UNUSED_PRESERVE src0_sel:WORD_1
v_mfma_f32_16x16x32_bf16 v[128:131],a[228:231],v[40:43],v[128:131]
ds_read_u16 v57,v57
ds_read_u16_d16_hi v59,v59
v_mfma_f32_16x16x32_bf16 v[132:135],a[228:231],v[36:39],v[132:135]
ds_read_u16 v52,v52
v_lshlrev_b32_sdwa v54,1,v54 dst_sel:DWORD dst_unused:UNUSED_PAD src0_sel:DWORD src1_sel:WORD_1
v_mfma_f32_16x16x32_bf16 v[108:111],a[232:235],v[36:39],v[108:111]
ds_read_u16_d16_hi v54,v54
v_fma_mix_f32 v53,v53,v9,0 op_sel_hi:[0,1,0]
v_mfma_f32_16x16x32_bf16 v[104:107],a[232:235],v[40:43],v[104:107]
s_waitcnt lgkmcnt(7)
v_fma_mix_f32 v55,v55,v9,0 op_sel:[0,1,0] op_sel_hi:[0,1,0]
v_mfma_f32_16x16x32_bf16 v[36:39],a[216:219],a[0:3],v[16:19]
s_add_u32 m0,s57,63656
v_lshlrev_b32_sdwa v60,1,v60 dst_sel:DWORD dst_unused:UNUSED_PAD src0_sel:DWORD src1_sel:WORD_1
v_lshlrev_b32_sdwa v62,1,v62 dst_sel:DWORD dst_unused:UNUSED_PAD src0_sel:DWORD src1_sel:WORD_1
v_mfma_f32_16x16x32_bf16 v[36:39],a[220:223],a[16:19],v[36:39]
s_add_u32 s89,s56,4227072
ds_read_u16 v60,v60
v_lshl_add_u64 v[28:29],v[28:29],0,v[30:31]
v_mfma_f32_16x16x32_bf16 v[36:39],a[192:195],a[32:35],v[36:39]
s_waitcnt lgkmcnt(6)
buffer_load_dwordx4 v6,s[36:39],s89 offen lds
v_lshl_add_u64 v[30:31],v[24:25],0,v[26:27]
v_mfma_f32_16x16x32_bf16 v[40:43],a[196:199],a[32:35],v[20:23]
ds_read_u16_d16_hi v62,v62
v_lshlrev_b32_sdwa v61,1,v61 dst_sel:DWORD dst_unused:UNUSED_PAD src0_sel:DWORD src1_sel:WORD_1
v_mfma_f32_16x16x32_bf16 v[40:43],a[200:203],a[16:19],v[40:43]
s_waitcnt lgkmcnt(4)
ds_read_u16 v61,v61
v_lshl_add_u64 v[24:25],v[56:57],0,v[58:59]
v_mfma_f32_16x16x32_bf16 v[40:43],a[212:215],a[0:3],v[40:43]
v_lshlrev_b32_sdwa v63,1,v63 dst_sel:DWORD dst_unused:UNUSED_PAD src0_sel:DWORD src1_sel:WORD_1
v_lshlrev_b32_sdwa v32,1,v32 dst_sel:DWORD dst_unused:UNUSED_PAD src0_sel:DWORD src1_sel:WORD_1
v_mfma_f32_16x16x32_bf16 v[56:59],a[212:215],a[4:7],v[20:23]
s_waitcnt lgkmcnt(3)
ds_read_u16_d16_hi v63,v63
v_or_b32 v26,v52,v54
v_mfma_f32_16x16x32_bf16 v[56:59],a[200:203],a[20:23],v[56:59]
ds_read_u16 v32,v32
v_cvt_pk_bf16_f32 v27,v53,v55
v_lshlrev_b32_sdwa v34,1,v34 dst_sel:DWORD dst_unused:UNUSED_PAD src0_sel:DWORD src1_sel:WORD_1
v_mfma_f32_16x16x32_bf16 v[56:59],a[196:199],a[36:39],v[56:59]
ds_read_u16_d16_hi v34,v34
v_lshlrev_b32_sdwa v33,1,v33 dst_sel:DWORD dst_unused:UNUSED_PAD src0_sel:DWORD src1_sel:WORD_1
v_mfma_f32_16x16x32_bf16 v[52:55],a[192:195],a[36:39],v[16:19]
ds_read_u16 v33,v33
v_lshlrev_b32_sdwa v35,1,v35 dst_sel:DWORD dst_unused:UNUSED_PAD src0_sel:DWORD src1_sel:WORD_1
v_mfma_f32_16x16x32_bf16 v[52:55],a[220:223],a[20:23],v[52:55]
ds_read_u16_d16_hi v35,v35
v_lshlrev_b32_sdwa v44,1,v44 dst_sel:DWORD dst_unused:UNUSED_PAD src0_sel:DWORD src1_sel:WORD_1
v_mfma_f32_16x16x32_bf16 v[52:55],a[216:219],a[4:7],v[52:55]
ds_read_u16 v44,v44
v_lshlrev_b32_sdwa v46,1,v46 dst_sel:DWORD dst_unused:UNUSED_PAD src0_sel:DWORD src1_sel:WORD_1
v_mfma_f32_16x16x32_bf16 v[232:235],a[248:251],v[28:31],v[232:235]
ds_read_u16_d16_hi v46,v46
v_lshlrev_b32_sdwa v45,1,v45 dst_sel:DWORD dst_unused:UNUSED_PAD src0_sel:DWORD src1_sel:WORD_1
v_mfma_f32_16x16x32_bf16 v[236:239],a[248:251],v[24:27],v[236:239]
ds_read_u16 v45,v45
v_lshlrev_b32_sdwa v47,1,v47 dst_sel:DWORD dst_unused:UNUSED_PAD src0_sel:DWORD src1_sel:WORD_1
v_mfma_f32_16x16x32_bf16 v[164:167],a[252:255],v[24:27],v[164:167]
ds_read_u16_d16_hi v47,v47
ds_read_b64_tr_b16 a[236:237],v3 offset:12320
v_mfma_f32_16x16x32_bf16 v[160:163],a[252:255],v[28:31],v[160:163]
ds_read_b64_tr_b16 a[238:239],v3 offset:13856
v_lshlrev_b32_sdwa v48,1,v48 dst_sel:DWORD dst_unused:UNUSED_PAD src0_sel:DWORD src1_sel:WORD_1
v_mfma_f32_16x16x32_bf16 v[240:243],a[240:243],v[28:31],v[240:243]
ds_read_u16 v48,v48
v_lshlrev_b32_sdwa v50,1,v50 dst_sel:DWORD dst_unused:UNUSED_PAD src0_sel:DWORD src1_sel:WORD_1
v_mfma_f32_16x16x32_bf16 v[244:247],a[240:243],v[24:27],v[244:247]
ds_read_u16_d16_hi v50,v50
v_lshlrev_b32_sdwa v49,1,v49 dst_sel:DWORD dst_unused:UNUSED_PAD src0_sel:DWORD src1_sel:WORD_1
v_mfma_f32_16x16x32_bf16 v[180:183],a[224:227],v[24:27],v[180:183]
s_add_u32 m0,s57,67184
ds_read_u16 v49,v49
v_lshlrev_b32_sdwa v51,1,v51 dst_sel:DWORD dst_unused:UNUSED_PAD src0_sel:DWORD src1_sel:WORD_1
v_mfma_f32_16x16x32_bf16 v[176:179],a[224:227],v[28:31],v[176:179]
s_add_u32 s89,s56,4325376
ds_read_u16_d16_hi v51,v51
v_mfma_f32_16x16x32_bf16 v[248:251],a[228:231],v[28:31],v[248:251]
buffer_load_dwordx4 v6,s[36:39],s89 offen lds
v_mfma_f32_16x16x32_bf16 v[252:255],a[228:231],v[24:27],v[252:255]
ds_read_b64_tr_b16 a[244:245],v3 offset:12328
v_mfma_f32_16x16x32_bf16 v[196:199],a[232:235],v[24:27],v[196:199]
ds_read_b64_tr_b16 a[246:247],v3 offset:13864
v_mfma_f32_16x16x32_bf16 v[192:195],a[232:235],v[28:31],v[192:195]
v_mfma_f32_16x16x32_bf16 v[24:27],a[216:219],a[8:11],v[16:19]
ds_read_b64_tr_b16 a[208:209],v1 offset:12320
v_lshlrev_b32_sdwa v36,1,v36 dst_sel:DWORD dst_unused:UNUSED_PAD src0_sel:DWORD src1_sel:WORD_1
v_mfma_f32_16x16x32_bf16 v[24:27],a[220:223],a[24:27],v[24:27]
s_waitcnt lgkmcnt(6)
ds_read_u16 v36,v36
v_lshl_add_u64 v[60:61],v[60:61],0,v[62:63]
v_fma_mixlo_f16 v8,v57,s99,0
v_mfma_f32_16x16x32_bf16 v[24:27],a[192:195],a[40:43],v[24:27]
ds_read_b64_tr_b16 a[210:211],v1 offset:13344
v_lshl_add_u64 v[62:63],v[32:33],0,v[34:35]
v_lshlrev_b32_sdwa v38,1,v38 dst_sel:DWORD dst_unused:UNUSED_PAD src0_sel:DWORD src1_sel:WORD_1
v_mfma_f32_16x16x32_bf16 v[28:31],a[196:199],a[40:43],v[20:23]
ds_read_u16_d16_hi v38,v38
v_fma_mixhi_f16 v8,v59,s99,0
v_mfma_f32_16x16x32_bf16 v[28:31],a[200:203],a[24:27],v[28:31]
ds_read_b64_tr_b16 a[204:205],v1 offset:14368
v_lshl_add_u64 v[32:33],v[44:45],0,v[46:47]
v_lshlrev_b32_sdwa v37,1,v37 dst_sel:DWORD dst_unused:UNUSED_PAD src0_sel:DWORD src1_sel:WORD_1
v_pk_fma_f16 v9,v8,v8,s54
v_mfma_f32_16x16x32_bf16 v[28:31],a[212:215],a[8:11],v[28:31]
ds_read_u16 v37,v37
v_lshlrev_b32_sdwa v39,1,v39 dst_sel:DWORD dst_unused:UNUSED_PAD src0_sel:DWORD src1_sel:WORD_1
v_mfma_f32_16x16x32_bf16 v[44:47],a[212:215],a[12:15],v[20:23]
ds_read_u16_d16_hi v39,v39
v_pk_mul_f16 v9,v8,v9
v_mfma_f32_16x16x32_bf16 v[44:47],a[200:203],a[28:31],v[44:47]
s_waitcnt lgkmcnt(7)
ds_read_b64_tr_b16 a[206:207],v1 offset:15392
v_lshl_add_u64 v[34:35],v[48:49],0,v[50:51]
v_lshlrev_b32_sdwa v40,1,v40 dst_sel:DWORD dst_unused:UNUSED_PAD src0_sel:DWORD src1_sel:WORD_1
v_mfma_f32_16x16x32_bf16 v[44:47],a[196:199],a[44:47],v[44:47]
v_exp_f16 v8,v9
v_mfma_f32_16x16x32_bf16 v[48:51],a[192:195],a[44:47],v[16:19]
ds_read_u16 v40,v40
v_lshlrev_b32_sdwa v42,1,v42 dst_sel:DWORD dst_unused:UNUSED_PAD src0_sel:DWORD src1_sel:WORD_1
v_mfma_f32_16x16x32_bf16 v[48:51],a[220:223],a[28:31],v[48:51]
ds_read_u16_d16_hi v42,v42
v_lshlrev_b32_sdwa v41,1,v41 dst_sel:DWORD dst_unused:UNUSED_PAD src0_sel:DWORD src1_sel:WORD_1
v_mfma_f32_16x16x32_bf16 v[48:51],a[216:219],a[12:15],v[48:51]
v_exp_f16_sdwa v8,v9 dst_sel:WORD_1 dst_unused:UNUSED_PRESERVE src0_sel:WORD_1
v_mfma_f32_16x16x32_bf16 v[208:211],a[248:251],v[60:63],v[208:211]
ds_read_u16 v41,v41
v_lshlrev_b32_sdwa v43,1,v43 dst_sel:DWORD dst_unused:UNUSED_PAD src0_sel:DWORD src1_sel:WORD_1
v_mfma_f32_16x16x32_bf16 v[212:215],a[248:251],v[32:35],v[212:215]
ds_read_u16_d16_hi v43,v43
v_lshlrev_b32_sdwa v52,1,v52 dst_sel:DWORD dst_unused:UNUSED_PAD src0_sel:DWORD src1_sel:WORD_1
v_mfma_f32_16x16x32_bf16 v[172:175],a[252:255],v[32:35],v[172:175]
ds_read_u16 v52,v52
v_pk_fma_f16 v8,v8,s55,s55
v_mfma_f32_16x16x32_bf16 v[168:171],a[252:255],v[60:63],v[168:171]
ds_read_b64_tr_b16 a[248:249],v3 offset:12384
v_rcp_f16 v9,v8
v_lshlrev_b32_sdwa v54,1,v54 dst_sel:DWORD dst_unused:UNUSED_PAD src0_sel:DWORD src1_sel:WORD_1
v_mfma_f32_16x16x32_bf16 v[216:219],a[240:243],v[60:63],v[216:219]
ds_read_u16_d16_hi v54,v54
v_lshlrev_b32_sdwa v53,1,v53 dst_sel:DWORD dst_unused:UNUSED_PAD src0_sel:DWORD src1_sel:WORD_1
v_mfma_f32_16x16x32_bf16 v[220:223],a[240:243],v[32:35],v[220:223]
ds_read_u16 v53,v53
ds_read_b64_tr_b16 a[250:251],v3 offset:13920
v_mfma_f32_16x16x32_bf16 v[188:191],a[224:227],v[32:35],v[188:191]
ds_read_b64_tr_b16 a[252:253],v3 offset:12392
v_lshlrev_b32_sdwa v55,1,v55 dst_sel:DWORD dst_unused:UNUSED_PAD src0_sel:DWORD src1_sel:WORD_1
v_mfma_f32_16x16x32_bf16 v[184:187],a[224:227],v[60:63],v[184:187]
ds_read_u16_d16_hi v55,v55
v_rcp_f16_sdwa v9,v8 dst_sel:WORD_1 dst_unused:UNUSED_PRESERVE src0_sel:WORD_1
v_mfma_f32_16x16x32_bf16 v[224:227],a[228:231],v[60:63],v[224:227]
ds_read_b64_tr_b16 a[254:255],v3 offset:13928
ds_read_b64_tr_b16 a[240:241],v3 offset:12448
v_lshlrev_b32_sdwa v56,1,v56 dst_sel:DWORD dst_unused:UNUSED_PAD src0_sel:DWORD src1_sel:WORD_1
v_mfma_f32_16x16x32_bf16 v[228:231],a[228:231],v[32:35],v[228:231]
ds_read_u16 v56,v56
v_lshlrev_b32_sdwa v58,1,v58 dst_sel:DWORD dst_unused:UNUSED_PAD src0_sel:DWORD src1_sel:WORD_1
v_mfma_f32_16x16x32_bf16 v[204:207],a[232:235],v[32:35],v[204:207]
ds_read_u16_d16_hi v58,v58
v_fma_mix_f32 v57,v57,v9,0 op_sel_hi:[0,1,0]
v_mfma_f32_16x16x32_bf16 v[200:203],a[232:235],v[60:63],v[200:203]
ds_read_b64_tr_b16 a[242:243],v3 offset:13984
v_fma_mix_f32 v59,v59,v9,0 op_sel:[0,1,0] op_sel_hi:[0,1,0]
v_mfma_f32_16x16x32_bf16 v[32:35],a[216:219],a[48:51],v[16:19]
s_add_u32 m0,s57,70720
ds_read_b64 v[14:15],v7 offset:12320
v_lshlrev_b32_sdwa v24,1,v24 dst_sel:DWORD dst_unused:UNUSED_PAD src0_sel:DWORD src1_sel:WORD_1
v_mfma_f32_16x16x32_bf16 v[32:35],a[220:223],a[64:67],v[32:35]
s_waitcnt lgkmcnt(10)
ds_read_b64_tr_b16 a[224:225],v3 offset:12456
v_lshl_add_u64 v[36:37],v[36:37],0,v[38:39]
v_mfma_f32_16x16x32_bf16 v[32:35],a[192:195],a[80:83],v[32:35]
s_add_u32 s89,s56,4423680
ds_read_b64_tr_b16 a[226:227],v3 offset:13992
v_lshl_add_u64 v[38:39],v[40:41],0,v[42:43]
v_lshlrev_b32_sdwa v26,1,v26 dst_sel:DWORD dst_unused:UNUSED_PAD src0_sel:DWORD src1_sel:WORD_1
v_mfma_f32_16x16x32_bf16 v[60:63],a[196:199],a[80:83],v[20:23]
buffer_load_dwordx4 v6,s[36:39],s89 offen lds
v_lshlrev_b32_sdwa v25,1,v25 dst_sel:DWORD dst_unused:UNUSED_PAD src0_sel:DWORD src1_sel:WORD_1
v_mfma_f32_16x16x32_bf16 v[60:63],a[200:203],a[64:67],v[60:63]
s_waitcnt lgkmcnt(8)
ds_read_u16 v24,v24
v_lshl_add_u64 v[40:41],v[52:53],0,v[54:55]
v_mfma_f32_16x16x32_bf16 v[60:63],a[212:215],a[48:51],v[60:63]
s_waitcnt lgkmcnt(3)
ds_read_u16_d16_hi v26,v26
v_or3_b32 v13,v14,v15,v13
v_lshlrev_b32_sdwa v27,1,v27 dst_sel:DWORD dst_unused:UNUSED_PAD src0_sel:DWORD src1_sel:WORD_1
v_mfma_f32_16x16x32_bf16 v[52:55],a[212:215],a[52:55],v[20:23]
ds_read_b64 v[14:15],v7 offset:12832
v_or_b32 v42,v56,v58
v_lshlrev_b32_sdwa v28,1,v28 dst_sel:DWORD dst_unused:UNUSED_PAD src0_sel:DWORD src1_sel:WORD_1
v_mfma_f32_16x16x32_bf16 v[52:55],a[200:203],a[68:71],v[52:55]
ds_read_u16 v25,v25
v_cvt_pk_bf16_f32 v43,v57,v59
v_lshlrev_b32_sdwa v30,1,v30 dst_sel:DWORD dst_unused:UNUSED_PAD src0_sel:DWORD src1_sel:WORD_1
v_mfma_f32_16x16x32_bf16 v[52:55],a[196:199],a[84:87],v[52:55]
ds_read_u16_d16_hi v27,v27
v_lshlrev_b32_sdwa v29,1,v29 dst_sel:DWORD dst_unused:UNUSED_PAD src0_sel:DWORD src1_sel:WORD_1
v_mfma_f32_16x16x32_bf16 v[56:59],a[192:195],a[84:87],v[16:19]
ds_read_u16 v28,v28
v_lshlrev_b32_sdwa v31,1,v31 dst_sel:DWORD dst_unused:UNUSED_PAD src0_sel:DWORD src1_sel:WORD_1
v_mfma_f32_16x16x32_bf16 v[56:59],a[220:223],a[68:71],v[56:59]
ds_read_u16_d16_hi v30,v30
v_lshlrev_b32_sdwa v48,1,v48 dst_sel:DWORD dst_unused:UNUSED_PAD src0_sel:DWORD src1_sel:WORD_1
v_mfma_f32_16x16x32_bf16 v[56:59],a[216:219],a[52:55],v[56:59]
s_waitcnt lgkmcnt(4)
ds_read_u16 v29,v29
v_or3_b32 v13,v14,v15,v13
v_mfma_f32_16x16x32_bf16 v[136:139],a[236:239],v[36:39],v[136:139]
ds_read_b64 v[14:15],v7 offset:13344
v_lshlrev_b32_sdwa v50,1,v50 dst_sel:DWORD dst_unused:UNUSED_PAD src0_sel:DWORD src1_sel:WORD_1
v_mfma_f32_16x16x32_bf16 v[140:143],a[236:239],v[40:43],v[140:143]
s_add_u32 m0,s57,74264
ds_read_u16_d16_hi v31,v31
v_lshlrev_b32_sdwa v49,1,v49 dst_sel:DWORD dst_unused:UNUSED_PAD src0_sel:DWORD src1_sel:WORD_1
v_mfma_f32_16x16x32_bf16 v[68:71],a[244:247],v[40:43],v[68:71]
s_add_u32 s89,s56,4521984
ds_read_u16 v48,v48
v_lshlrev_b32_sdwa v51,1,v51 dst_sel:DWORD dst_unused:UNUSED_PAD src0_sel:DWORD src1_sel:WORD_1
v_mfma_f32_16x16x32_bf16 v[64:67],a[244:247],v[36:39],v[64:67]
buffer_load_dwordx4 v6,s[36:39],s89 offen lds
v_lshlrev_b32_sdwa v44,1,v44 dst_sel:DWORD dst_unused:UNUSED_PAD src0_sel:DWORD src1_sel:WORD_1
v_mfma_f32_16x16x32_bf16 v[144:147],a[248:251],v[36:39],v[144:147]
ds_read_u16_d16_hi v50,v50
v_lshlrev_b32_sdwa v46,1,v46 dst_sel:DWORD dst_unused:UNUSED_PAD src0_sel:DWORD src1_sel:WORD_1
v_mfma_f32_16x16x32_bf16 v[148:151],a[248:251],v[40:43],v[148:151]
s_waitcnt lgkmcnt(3)
ds_read_u16 v49,v49
v_or3_b32 v13,v14,v15,v13
v_mfma_f32_16x16x32_bf16 v[84:87],a[252:255],v[40:43],v[84:87]
ds_read_u16_d16_hi v51,v51
v_lshlrev_b32_sdwa v45,1,v45 dst_sel:DWORD dst_unused:UNUSED_PAD src0_sel:DWORD src1_sel:WORD_1
v_mfma_f32_16x16x32_bf16 v[80:83],a[252:255],v[36:39],v[80:83]
ds_read_u16 v44,v44
v_lshlrev_b32_sdwa v47,1,v47 dst_sel:DWORD dst_unused:UNUSED_PAD src0_sel:DWORD src1_sel:WORD_1
v_mfma_f32_16x16x32_bf16 v[152:155],a[240:243],v[36:39],v[152:155]
ds_read_u16_d16_hi v46,v46
v_mfma_f32_16x16x32_bf16 v[156:159],a[240:243],v[40:43],v[156:159]
ds_read_u16 v45,v45
v_mfma_f32_16x16x32_bf16 v[100:103],a[224:227],v[40:43],v[100:103]
ds_read_u16_d16_hi v47,v47
v_mfma_f32_16x16x32_bf16 v[96:99],a[224:227],v[36:39],v[96:99]
v_mfma_f32_16x16x32_bf16 v[40:43],a[216:219],a[56:59],v[16:19]
v_lshlrev_b32_sdwa v32,1,v32 dst_sel:DWORD dst_unused:UNUSED_PAD src0_sel:DWORD src1_sel:WORD_1
v_fma_mixlo_f16 v8,v53,s99,0
v_mfma_f32_16x16x32_bf16 v[40:43],a[220:223],a[72:75],v[40:43]
ds_read_u16 v32,v32
v_lshl_add_u64 v[24:25],v[24:25],0,v[26:27]
v_mfma_f32_16x16x32_bf16 v[40:43],a[192:195],a[88:91],v[40:43]
s_waitcnt lgkmcnt(6)
v_lshl_add_u64 v[26:27],v[28:29],0,v[30:31]
v_lshlrev_b32_sdwa v34,1,v34 dst_sel:DWORD dst_unused:UNUSED_PAD src0_sel:DWORD src1_sel:WORD_1
v_mfma_f32_16x16x32_bf16 v[36:39],a[196:199],a[88:91],v[20:23]
ds_read_u16_d16_hi v34,v34
v_fma_mixhi_f16 v8,v55,s99,0
v_mfma_f32_16x16x32_bf16 v[36:39],a[200:203],a[72:75],v[36:39]
s_waitcnt lgkmcnt(5)
v_lshl_add_u64 v[28:29],v[48:49],0,v[50:51]
v_lshlrev_b32_sdwa v33,1,v33 dst_sel:DWORD dst_unused:UNUSED_PAD src0_sel:DWORD src1_sel:WORD_1
v_pk_fma_f16 v9,v8,v8,s54
v_mfma_f32_16x16x32_bf16 v[36:39],a[212:215],a[56:59],v[36:39]
ds_read_u16 v33,v33
v_lshlrev_b32_sdwa v35,1,v35 dst_sel:DWORD dst_unused:UNUSED_PAD src0_sel:DWORD src1_sel:WORD_1
v_mfma_f32_16x16x32_bf16 v[48:51],a[212:215],a[60:63],v[20:23]
ds_read_b128 v[20:23],v4 offset:896
v_pk_mul_f16 v9,v8,v9
v_mfma_f32_16x16x32_bf16 v[48:51],a[200:203],a[76:79],v[48:51]
s_waitcnt lgkmcnt(4)
ds_read_u16_d16_hi v35,v35
ds_read_b64_tr_b16 a[212:213],v1 offset:16416
v_lshl_add_u64 v[30:31],v[44:45],0,v[46:47]
v_lshlrev_b32_sdwa v60,1,v60 dst_sel:DWORD dst_unused:UNUSED_PAD src0_sel:DWORD src1_sel:WORD_1
v_mfma_f32_16x16x32_bf16 v[48:51],a[196:199],a[92:95],v[48:51]
ds_read_b64_tr_b16 a[214:215],v1 offset:17440
v_exp_f16 v8,v9
v_mfma_f32_16x16x32_bf16 v[44:47],a[192:195],a[92:95],v[16:19]
ds_read_b128 v[16:19],v4 offset:928
ds_read_u16 v60,v60
v_lshlrev_b32_sdwa v62,1,v62 dst_sel:DWORD dst_unused:UNUSED_PAD src0_sel:DWORD src1_sel:WORD_1
v_mfma_f32_16x16x32_bf16 v[44:47],a[220:223],a[76:79],v[44:47]
ds_read_u16_d16_hi v62,v62
ds_read_b64_tr_b16 a[200:201],v2 offset:16416
v_lshlrev_b32_sdwa v61,1,v61 dst_sel:DWORD dst_unused:UNUSED_PAD src0_sel:DWORD src1_sel:WORD_1
v_mfma_f32_16x16x32_bf16 v[44:47],a[216:219],a[60:63],v[44:47]
ds_read_u16 v61,v61
v_exp_f16_sdwa v8,v9 dst_sel:WORD_1 dst_unused:UNUSED_PRESERVE src0_sel:WORD_1
v_mfma_f32_16x16x32_bf16 v[112:115],a[236:239],v[24:27],v[112:115]
ds_read_b64_tr_b16 a[202:203],v2 offset:17440
ds_read_b64_tr_b16 a[196:197],v2 offset:14368
v_lshlrev_b32_sdwa v63,1,v63 dst_sel:DWORD dst_unused:UNUSED_PAD src0_sel:DWORD src1_sel:WORD_1
v_mfma_f32_16x16x32_bf16 v[116:119],a[236:239],v[28:31],v[116:119]
ds_read_u16_d16_hi v63,v63
v_lshlrev_b32_sdwa v56,1,v56 dst_sel:DWORD dst_unused:UNUSED_PAD src0_sel:DWORD src1_sel:WORD_1
v_mfma_f32_16x16x32_bf16 v[76:79],a[244:247],v[28:31],v[76:79]
ds_read_b64_tr_b16 a[198:199],v2 offset:15392
v_pk_fma_f16 v8,v8,s55,s55
v_mfma_f32_16x16x32_bf16 v[72:75],a[244:247],v[24:27],v[72:75]
v_rcp_f16 v9,v8
v_lshlrev_b32_sdwa v58,1,v58 dst_sel:DWORD dst_unused:UNUSED_PAD src0_sel:DWORD src1_sel:WORD_1
v_mfma_f32_16x16x32_bf16 v[120:123],a[248:251],v[24:27],v[120:123]
ds_read_u16 v56,v56
v_lshlrev_b32_sdwa v57,1,v57 dst_sel:DWORD dst_unused:UNUSED_PAD src0_sel:DWORD src1_sel:WORD_1
v_mfma_f32_16x16x32_bf16 v[124:127],a[248:251],v[28:31],v[124:127]
ds_read_u16_d16_hi v58,v58
v_lshlrev_b32_sdwa v59,1,v59 dst_sel:DWORD dst_unused:UNUSED_PAD src0_sel:DWORD src1_sel:WORD_1
v_mfma_f32_16x16x32_bf16 v[92:95],a[252:255],v[28:31],v[92:95]
ds_read_u16 v57,v57
v_lshlrev_b32_sdwa v52,1,v52 dst_sel:DWORD dst_unused:UNUSED_PAD src0_sel:DWORD src1_sel:WORD_1
v_mfma_f32_16x16x32_bf16 v[88:91],a[252:255],v[24:27],v[88:91]
ds_read_u16_d16_hi v59,v59
v_rcp_f16_sdwa v9,v8 dst_sel:WORD_1 dst_unused:UNUSED_PRESERVE src0_sel:WORD_1
v_mfma_f32_16x16x32_bf16 v[128:131],a[240:243],v[24:27],v[128:131]
ds_read_b64_tr_b16 a[192:193],v2 offset:12320
v_lshlrev_b32_sdwa v54,1,v54 dst_sel:DWORD dst_unused:UNUSED_PAD src0_sel:DWORD src1_sel:WORD_1
v_mfma_f32_16x16x32_bf16 v[132:135],a[240:243],v[28:31],v[132:135]
ds_read_b64_tr_b16 a[194:195],v2 offset:13344
v_fma_mix_f32 v53,v53,v9,0 op_sel_hi:[0,1,0]
v_mfma_f32_16x16x32_bf16 v[108:111],a[224:227],v[28:31],v[108:111]
ds_read_u16 v52,v52
v_fma_mix_f32 v55,v55,v9,0 op_sel:[0,1,0] op_sel_hi:[0,1,0]
v_mfma_f32_16x16x32_bf16 v[104:107],a[224:227],v[24:27],v[104:107]
s_waitcnt lgkmcnt(7)
ds_read_u16_d16_hi v54,v54
v_mfma_f32_16x16x32_bf16 v[28:31],a[208:211],a[0:3],v[20:23]
s_add_u32 m0,s57,77800
v_lshlrev_b32_sdwa v40,1,v40 dst_sel:DWORD dst_unused:UNUSED_PAD src0_sel:DWORD src1_sel:WORD_1
v_lshlrev_b32_sdwa v42,1,v42 dst_sel:DWORD dst_unused:UNUSED_PAD src0_sel:DWORD src1_sel:WORD_1
v_mfma_f32_16x16x32_bf16 v[28:31],a[204:207],a[16:19],v[28:31]
s_add_u32 s89,s56,4620288
ds_read_u16 v40,v40
v_lshl_add_u64 v[32:33],v[32:33],0,v[34:35]
v_mfma_f32_16x16x32_bf16 v[28:31],a[212:215],a[32:35],v[28:31]
buffer_load_dwordx4 v6,s[36:39],s89 offen lds
v_lshl_add_u64 v[34:35],v[60:61],0,v[62:63]
v_mfma_f32_16x16x32_bf16 v[24:27],a[200:203],a[32:35],v[16:19]
ds_read_u16_d16_hi v42,v42
v_lshlrev_b32_sdwa v41,1,v41 dst_sel:DWORD dst_unused:UNUSED_PAD src0_sel:DWORD src1_sel:WORD_1
v_mfma_f32_16x16x32_bf16 v[24:27],a[196:199],a[16:19],v[24:27]
s_waitcnt lgkmcnt(4)
ds_read_u16 v41,v41
v_lshl_add_u64 v[60:61],v[56:57],0,v[58:59]
v_mfma_f32_16x16x32_bf16 v[24:27],a[192:195],a[0:3],v[24:27]
TP3285_%=:
s_cbranch_execz TP5419_%=
v_lshlrev_b32_sdwa v43,1,v43 dst_sel:DWORD dst_unused:UNUSED_PAD src0_sel:DWORD src1_sel:WORD_1
v_lshlrev_b32_sdwa v36,1,v36 dst_sel:DWORD dst_unused:UNUSED_PAD src0_sel:DWORD src1_sel:WORD_1
v_mfma_f32_16x16x32_bf16 v[56:59],a[192:195],a[4:7],v[16:19]
s_waitcnt lgkmcnt(3)
ds_read_u16_d16_hi v43,v43
v_or_b32_e64 v62,v52,v54
v_mfma_f32_16x16x32_bf16 v[56:59],a[196:199],a[20:23],v[56:59]
s_waitcnt vmcnt(7)
ds_read_u16 v36,v36
v_cvt_pk_bf16_f32 v63,v53,v55
v_lshlrev_b32_sdwa v38,1,v38 dst_sel:DWORD dst_unused:UNUSED_PAD src0_sel:DWORD src1_sel:WORD_1
v_mfma_f32_16x16x32_bf16 v[56:59],a[200:203],a[36:39],v[56:59]
s_barrier
ds_read_u16_d16_hi v38,v38
v_lshlrev_b32_sdwa v37,1,v37 dst_sel:DWORD dst_unused:UNUSED_PAD src0_sel:DWORD src1_sel:WORD_1
v_mfma_f32_16x16x32_bf16 v[52:55],a[212:215],a[36:39],v[20:23]
s_add_u32 m0,s57,81328
ds_read_u16 v37,v37
v_lshlrev_b32_sdwa v39,1,v39 dst_sel:DWORD dst_unused:UNUSED_PAD src0_sel:DWORD src1_sel:WORD_1
v_mfma_f32_16x16x32_bf16 v[52:55],a[204:207],a[20:23],v[52:55]
s_add_u32 s89,s56,4718592
ds_read_u16_d16_hi v39,v39
v_lshlrev_b32_sdwa v44,1,v44 dst_sel:DWORD dst_unused:UNUSED_PAD src0_sel:DWORD src1_sel:WORD_1
v_mfma_f32_16x16x32_bf16 v[52:55],a[208:211],a[4:7],v[52:55]
buffer_load_dwordx4 v6,s[36:39],s89 offen lds
v_lshlrev_b32_sdwa v46,1,v46 dst_sel:DWORD dst_unused:UNUSED_PAD src0_sel:DWORD src1_sel:WORD_1
v_mfma_f32_16x16x32_bf16 v[232:235],a[236:239],v[32:35],v[232:235]
s_add_u32 m0,s58,6160
ds_read_u16 v44,v44
v_lshlrev_b32_sdwa v45,1,v45 dst_sel:DWORD dst_unused:UNUSED_PAD src0_sel:DWORD src1_sel:WORD_1
v_mfma_f32_16x16x32_bf16 v[236:239],a[236:239],v[60:63],v[236:239]
s_add_u32 s69,s69,s59
ds_read_u16_d16_hi v46,v46
v_lshlrev_b32_sdwa v47,1,v47 dst_sel:DWORD dst_unused:UNUSED_PAD src0_sel:DWORD src1_sel:WORD_1
v_mfma_f32_16x16x32_bf16 v[164:167],a[244:247],v[60:63],v[164:167]
buffer_load_dwordx4 v5,s[44:47],s69 offen offset:0 lds
ds_read_u16 v45,v45
v_mfma_f32_16x16x32_bf16 v[160:163],a[244:247],v[32:35],v[160:163]
ds_read_u16_d16_hi v47,v47
ds_read_b64_tr_b16 a[228:229],v3 offset:18480
v_mfma_f32_16x16x32_bf16 v[240:243],a[248:251],v[32:35],v[240:243]
s_add_u32 s69,s69,s53
ds_read_b64_tr_b16 a[230:231],v3 offset:20016
v_lshlrev_b32_sdwa v48,1,v48 dst_sel:DWORD dst_unused:UNUSED_PAD src0_sel:DWORD src1_sel:WORD_1
v_mfma_f32_16x16x32_bf16 v[244:247],a[248:251],v[60:63],v[244:247]
buffer_load_dwordx4 v5,s[44:47],s69 offen offset:1024 lds
v_lshlrev_b32_sdwa v50,1,v50 dst_sel:DWORD dst_unused:UNUSED_PAD src0_sel:DWORD src1_sel:WORD_1
v_mfma_f32_16x16x32_bf16 v[180:183],a[252:255],v[60:63],v[180:183]
ds_read_u16 v48,v48
v_lshlrev_b32_sdwa v49,1,v49 dst_sel:DWORD dst_unused:UNUSED_PAD src0_sel:DWORD src1_sel:WORD_1
v_mfma_f32_16x16x32_bf16 v[176:179],a[252:255],v[32:35],v[176:179]
ds_read_u16_d16_hi v50,v50
v_lshlrev_b32_sdwa v51,1,v51 dst_sel:DWORD dst_unused:UNUSED_PAD src0_sel:DWORD src1_sel:WORD_1
v_mfma_f32_16x16x32_bf16 v[248:251],a[240:243],v[32:35],v[248:251]
ds_read_u16 v49,v49
v_mfma_f32_16x16x32_bf16 v[252:255],a[240:243],v[60:63],v[252:255]
s_add_u32 s69,s69,s53
ds_read_u16_d16_hi v51,v51
v_mfma_f32_16x16x32_bf16 v[196:199],a[224:227],v[60:63],v[196:199]
buffer_load_dwordx4 v5,s[44:47],s69 offen offset:2048 lds
v_mfma_f32_16x16x32_bf16 v[192:195],a[224:227],v[32:35],v[192:195]
ds_read_b64_tr_b16 a[232:233],v3 offset:18488
v_mfma_f32_16x16x32_bf16 v[60:63],a[208:211],a[8:11],v[20:23]
ds_read_b64_tr_b16 a[234:235],v3 offset:20024
v_lshlrev_b32_sdwa v28,1,v28 dst_sel:DWORD dst_unused:UNUSED_PAD src0_sel:DWORD src1_sel:WORD_1
v_mfma_f32_16x16x32_bf16 v[60:63],a[204:207],a[24:27],v[60:63]
s_waitcnt lgkmcnt(6)
ds_read_u16 v28,v28
v_lshl_add_u64 v[40:41],v[40:41],0,v[42:43]
v_fma_mixlo_f16 v8,v57,s99,0
v_mfma_f32_16x16x32_bf16 v[60:63],a[212:215],a[40:43],v[60:63]
ds_read_b64_tr_b16 a[220:221],v1 offset:18480
v_lshl_add_u64 v[42:43],v[36:37],0,v[38:39]
v_lshlrev_b32_sdwa v30,1,v30 dst_sel:DWORD dst_unused:UNUSED_PAD src0_sel:DWORD src1_sel:WORD_1
v_mfma_f32_16x16x32_bf16 v[32:35],a[200:203],a[40:43],v[16:19]
ds_read_u16_d16_hi v30,v30
v_fma_mixhi_f16 v8,v59,s99,0
v_mfma_f32_16x16x32_bf16 v[32:35],a[196:199],a[24:27],v[32:35]
ds_read_b64_tr_b16 a[222:223],v1 offset:19504
v_lshl_add_u64 v[36:37],v[44:45],0,v[46:47]
v_lshlrev_b32_sdwa v29,1,v29 dst_sel:DWORD dst_unused:UNUSED_PAD src0_sel:DWORD src1_sel:WORD_1
v_pk_fma_f16 v9,v8,v8,s54
v_mfma_f32_16x16x32_bf16 v[32:35],a[192:195],a[8:11],v[32:35]
ds_read_u16 v29,v29
v_lshlrev_b32_sdwa v31,1,v31 dst_sel:DWORD dst_unused:UNUSED_PAD src0_sel:DWORD src1_sel:WORD_1
v_mfma_f32_16x16x32_bf16 v[44:47],a[192:195],a[12:15],v[16:19]
ds_read_u16_d16_hi v31,v31
v_pk_mul_f16 v9,v8,v9
v_mfma_f32_16x16x32_bf16 v[44:47],a[196:199],a[28:31],v[44:47]
s_waitcnt lgkmcnt(8)
ds_read_b64_tr_b16 a[216:217],v1 offset:20528
v_lshl_add_u64 v[38:39],v[48:49],0,v[50:51]
v_lshlrev_b32_sdwa v24,1,v24 dst_sel:DWORD dst_unused:UNUSED_PAD src0_sel:DWORD src1_sel:WORD_1
v_mfma_f32_16x16x32_bf16 v[44:47],a[200:203],a[44:47],v[44:47]
v_exp_f16 v8,v9
v_mfma_f32_16x16x32_bf16 v[48:51],a[212:215],a[44:47],v[20:23]
ds_read_u16 v24,v24
v_lshlrev_b32_sdwa v26,1,v26 dst_sel:DWORD dst_unused:UNUSED_PAD src0_sel:DWORD src1_sel:WORD_1
v_mfma_f32_16x16x32_bf16 v[48:51],a[204:207],a[28:31],v[48:51]
ds_read_u16_d16_hi v26,v26
v_lshlrev_b32_sdwa v25,1,v25 dst_sel:DWORD dst_unused:UNUSED_PAD src0_sel:DWORD src1_sel:WORD_1
v_mfma_f32_16x16x32_bf16 v[48:51],a[208:211],a[12:15],v[48:51]
v_exp_f16_sdwa v8,v9 dst_sel:WORD_1 dst_unused:UNUSED_PRESERVE src0_sel:WORD_1
v_mfma_f32_16x16x32_bf16 v[208:211],a[236:239],v[40:43],v[208:211]
ds_read_u16 v25,v25
v_lshlrev_b32_sdwa v27,1,v27 dst_sel:DWORD dst_unused:UNUSED_PAD src0_sel:DWORD src1_sel:WORD_1
v_mfma_f32_16x16x32_bf16 v[212:215],a[236:239],v[36:39],v[212:215]
ds_read_u16_d16_hi v27,v27
v_lshlrev_b32_sdwa v52,1,v52 dst_sel:DWORD dst_unused:UNUSED_PAD src0_sel:DWORD src1_sel:WORD_1
v_mfma_f32_16x16x32_bf16 v[172:175],a[244:247],v[36:39],v[172:175]
ds_read_u16 v52,v52
v_pk_fma_f16 v8,v8,s55,s55
v_mfma_f32_16x16x32_bf16 v[168:171],a[244:247],v[40:43],v[168:171]
ds_read_b64_tr_b16 a[236:237],v3 offset:18544
v_rcp_f16 v9,v8
v_lshlrev_b32_sdwa v54,1,v54 dst_sel:DWORD dst_unused:UNUSED_PAD src0_sel:DWORD src1_sel:WORD_1
v_mfma_f32_16x16x32_bf16 v[216:219],a[248:251],v[40:43],v[216:219]
ds_read_u16_d16_hi v54,v54
v_lshlrev_b32_sdwa v53,1,v53 dst_sel:DWORD dst_unused:UNUSED_PAD src0_sel:DWORD src1_sel:WORD_1
v_mfma_f32_16x16x32_bf16 v[220:223],a[248:251],v[36:39],v[220:223]
ds_read_u16 v53,v53
ds_read_b64_tr_b16 a[238:239],v3 offset:20080
v_mfma_f32_16x16x32_bf16 v[188:191],a[252:255],v[36:39],v[188:191]
ds_read_b64_tr_b16 a[244:245],v3 offset:18552
v_lshlrev_b32_sdwa v55,1,v55 dst_sel:DWORD dst_unused:UNUSED_PAD src0_sel:DWORD src1_sel:WORD_1
v_mfma_f32_16x16x32_bf16 v[184:187],a[252:255],v[40:43],v[184:187]
ds_read_u16_d16_hi v55,v55
v_rcp_f16_sdwa v9,v8 dst_sel:WORD_1 dst_unused:UNUSED_PRESERVE src0_sel:WORD_1
v_mfma_f32_16x16x32_bf16 v[224:227],a[240:243],v[40:43],v[224:227]
ds_read_b64_tr_b16 a[246:247],v3 offset:20088
ds_read_b64_tr_b16 a[248:249],v3 offset:18608
v_lshlrev_b32_sdwa v56,1,v56 dst_sel:DWORD dst_unused:UNUSED_PAD src0_sel:DWORD src1_sel:WORD_1
v_mfma_f32_16x16x32_bf16 v[228:231],a[240:243],v[36:39],v[228:231]
ds_read_u16 v56,v56
v_lshlrev_b32_sdwa v58,1,v58 dst_sel:DWORD dst_unused:UNUSED_PAD src0_sel:DWORD src1_sel:WORD_1
v_mfma_f32_16x16x32_bf16 v[204:207],a[224:227],v[36:39],v[204:207]
ds_read_u16_d16_hi v58,v58
v_fma_mix_f32 v57,v57,v9,0 op_sel_hi:[0,1,0]
v_mfma_f32_16x16x32_bf16 v[200:203],a[224:227],v[40:43],v[200:203]
ds_read_b64_tr_b16 a[250:251],v3 offset:20144
v_fma_mix_f32 v59,v59,v9,0 op_sel:[0,1,0] op_sel_hi:[0,1,0]
v_mfma_f32_16x16x32_bf16 v[36:39],a[208:211],a[48:51],v[20:23]
s_add_u32 m0,s58,12320
ds_read_b64 v[14:15],v7 offset:18480
v_lshlrev_b32_sdwa v60,1,v60 dst_sel:DWORD dst_unused:UNUSED_PAD src0_sel:DWORD src1_sel:WORD_1
v_mfma_f32_16x16x32_bf16 v[36:39],a[204:207],a[64:67],v[36:39]
s_waitcnt lgkmcnt(10)
ds_read_b64_tr_b16 a[252:253],v3 offset:18616
v_lshl_add_u64 v[28:29],v[28:29],0,v[30:31]
v_mfma_f32_16x16x32_bf16 v[36:39],a[212:215],a[80:83],v[36:39]
s_add_u32 s69,s69,s59
ds_read_b64_tr_b16 a[254:255],v3 offset:20152
v_lshl_add_u64 v[30:31],v[24:25],0,v[26:27]
v_lshlrev_b32_sdwa v62,1,v62 dst_sel:DWORD dst_unused:UNUSED_PAD src0_sel:DWORD src1_sel:WORD_1
v_mfma_f32_16x16x32_bf16 v[40:43],a[200:203],a[80:83],v[16:19]
buffer_load_dwordx4 v5,s[44:47],s69 offen offset:0 lds
v_lshlrev_b32_sdwa v61,1,v61 dst_sel:DWORD dst_unused:UNUSED_PAD src0_sel:DWORD src1_sel:WORD_1
v_mfma_f32_16x16x32_bf16 v[40:43],a[196:199],a[64:67],v[40:43]
s_waitcnt lgkmcnt(8)
ds_read_u16 v60,v60
v_lshl_add_u64 v[24:25],v[52:53],0,v[54:55]
v_mfma_f32_16x16x32_bf16 v[40:43],a[192:195],a[48:51],v[40:43]
s_waitcnt lgkmcnt(3)
ds_read_u16_d16_hi v62,v62
v_or3_b32 v13,v14,v15,v13
v_lshlrev_b32_sdwa v63,1,v63 dst_sel:DWORD dst_unused:UNUSED_PAD src0_sel:DWORD src1_sel:WORD_1
v_mfma_f32_16x16x32_bf16 v[52:55],a[192:195],a[52:55],v[16:19]
s_add_u32 s69,s69,s53
ds_read_b64 v[14:15],v7 offset:18992
v_or_b32 v26,v56,v58
v_lshlrev_b32_sdwa v32,1,v32 dst_sel:DWORD dst_unused:UNUSED_PAD src0_sel:DWORD src1_sel:WORD_1
v_mfma_f32_16x16x32_bf16 v[52:55],a[196:199],a[68:71],v[52:55]
buffer_load_dwordx4 v5,s[44:47],s69 offen offset:1024 lds
ds_read_u16 v61,v61
v_cvt_pk_bf16_f32 v27,v57,v59
v_lshlrev_b32_sdwa v34,1,v34 dst_sel:DWORD dst_unused:UNUSED_PAD src0_sel:DWORD src1_sel:WORD_1
v_mfma_f32_16x16x32_bf16 v[52:55],a[200:203],a[84:87],v[52:55]
s_add_u32 m0,s57,84864
ds_read_u16_d16_hi v63,v63
v_lshlrev_b32_sdwa v33,1,v33 dst_sel:DWORD dst_unused:UNUSED_PAD src0_sel:DWORD src1_sel:WORD_1
v_mfma_f32_16x16x32_bf16 v[56:59],a[212:215],a[84:87],v[20:23]
s_add_u32 s89,s56,4816896
ds_read_u16 v32,v32
v_lshlrev_b32_sdwa v35,1,v35 dst_sel:DWORD dst_unused:UNUSED_PAD src0_sel:DWORD src1_sel:WORD_1
v_mfma_f32_16x16x32_bf16 v[56:59],a[204:207],a[68:71],v[56:59]
buffer_load_dwordx4 v6,s[36:39],s89 offen lds
ds_read_u16_d16_hi v34,v34
v_mfma_f32_16x16x32_bf16 v[56:59],a[208:211],a[52:55],v[56:59]
s_waitcnt lgkmcnt(4)
ds_read_u16 v33,v33
v_or3_b32 v13,v14,v15,v13
v_mfma_f32_16x16x32_bf16 v[136:139],a[228:231],v[28:31],v[136:139]
s_add_u32 m0,s58,12320
ds_read_b64 v[14:15],v7 offset:19504
v_lshlrev_b32_sdwa v48,1,v48 dst_sel:DWORD dst_unused:UNUSED_PAD src0_sel:DWORD src1_sel:WORD_1
v_mfma_f32_16x16x32_bf16 v[140:143],a[228:231],v[24:27],v[140:143]
s_add_u32 s69,s69,s53
ds_read_u16_d16_hi v35,v35
v_lshlrev_b32_sdwa v50,1,v50 dst_sel:DWORD dst_unused:UNUSED_PAD src0_sel:DWORD src1_sel:WORD_1
v_mfma_f32_16x16x32_bf16 v[68:71],a[232:235],v[24:27],v[68:71]
buffer_load_dwordx4 v5,s[44:47],s69 offen offset:2048 lds
v_lshlrev_b32_sdwa v49,1,v49 dst_sel:DWORD dst_unused:UNUSED_PAD src0_sel:DWORD src1_sel:WORD_1
v_mfma_f32_16x16x32_bf16 v[64:67],a[232:235],v[28:31],v[64:67]
s_add_u32 m0,s57,8
ds_read_u16 v48,v48
v_lshlrev_b32_sdwa v51,1,v51 dst_sel:DWORD dst_unused:UNUSED_PAD src0_sel:DWORD src1_sel:WORD_1
v_mfma_f32_16x16x32_bf16 v[144:147],a[236:239],v[28:31],v[144:147]
s_add_u32 s89,s56,4915200
ds_read_u16_d16_hi v50,v50
v_lshlrev_b32_sdwa v44,1,v44 dst_sel:DWORD dst_unused:UNUSED_PAD src0_sel:DWORD src1_sel:WORD_1
v_mfma_f32_16x16x32_bf16 v[148:151],a[236:239],v[24:27],v[148:151]
s_waitcnt lgkmcnt(3)
buffer_load_dwordx4 v6,s[36:39],s89 offen lds
ds_read_u16 v49,v49
v_or3_b32 v13,v14,v15,v13
v_mfma_f32_16x16x32_bf16 v[84:87],a[244:247],v[24:27],v[84:87]
ds_read_u16_d16_hi v51,v51
v_lshlrev_b32_sdwa v46,1,v46 dst_sel:DWORD dst_unused:UNUSED_PAD src0_sel:DWORD src1_sel:WORD_1
v_mfma_f32_16x16x32_bf16 v[80:83],a[244:247],v[28:31],v[80:83]
ds_read_u16 v44,v44
v_lshlrev_b32_sdwa v45,1,v45 dst_sel:DWORD dst_unused:UNUSED_PAD src0_sel:DWORD src1_sel:WORD_1
v_mfma_f32_16x16x32_bf16 v[152:155],a[248:251],v[28:31],v[152:155]
ds_read_u16_d16_hi v46,v46
v_lshlrev_b32_sdwa v47,1,v47 dst_sel:DWORD dst_unused:UNUSED_PAD src0_sel:DWORD src1_sel:WORD_1
v_mfma_f32_16x16x32_bf16 v[156:159],a[248:251],v[24:27],v[156:159]
ds_read_u16 v45,v45
v_mfma_f32_16x16x32_bf16 v[100:103],a[252:255],v[24:27],v[100:103]
ds_read_u16_d16_hi v47,v47
v_mfma_f32_16x16x32_bf16 v[96:99],a[252:255],v[28:31],v[96:99]
ds_read_b64_tr_b16 a[218:219],v1 offset:21552
v_mfma_f32_16x16x32_bf16 v[24:27],a[208:211],a[56:59],v[20:23]
v_lshlrev_b32_sdwa v36,1,v36 dst_sel:DWORD dst_unused:UNUSED_PAD src0_sel:DWORD src1_sel:WORD_1
v_fma_mixlo_f16 v8,v53,s99,0
v_mfma_f32_16x16x32_bf16 v[24:27],a[204:207],a[72:75],v[24:27]
ds_read_u16 v36,v36
v_lshl_add_u64 v[60:61],v[60:61],0,v[62:63]
v_mfma_f32_16x16x32_bf16 v[24:27],a[212:215],a[88:91],v[24:27]
s_waitcnt lgkmcnt(7)
v_lshl_add_u64 v[62:63],v[32:33],0,v[34:35]
v_lshlrev_b32_sdwa v38,1,v38 dst_sel:DWORD dst_unused:UNUSED_PAD src0_sel:DWORD src1_sel:WORD_1
v_mfma_f32_16x16x32_bf16 v[28:31],a[200:203],a[88:91],v[16:19]
ds_read_u16_d16_hi v38,v38
v_fma_mixhi_f16 v8,v55,s99,0
v_mfma_f32_16x16x32_bf16 v[28:31],a[196:199],a[72:75],v[28:31]
s_waitcnt lgkmcnt(6)
v_lshl_add_u64 v[32:33],v[48:49],0,v[50:51]
v_lshlrev_b32_sdwa v37,1,v37 dst_sel:DWORD dst_unused:UNUSED_PAD src0_sel:DWORD src1_sel:WORD_1
v_pk_fma_f16 v9,v8,v8,s54
v_mfma_f32_16x16x32_bf16 v[28:31],a[192:195],a[56:59],v[28:31]
ds_read_u16 v37,v37
v_lshlrev_b32_sdwa v39,1,v39 dst_sel:DWORD dst_unused:UNUSED_PAD src0_sel:DWORD src1_sel:WORD_1
v_mfma_f32_16x16x32_bf16 v[48:51],a[192:195],a[60:63],v[16:19]
ds_read_b128 v[16:19],v4 offset:1024
v_pk_mul_f16 v9,v8,v9
v_mfma_f32_16x16x32_bf16 v[48:51],a[196:199],a[76:79],v[48:51]
s_waitcnt lgkmcnt(5)
ds_read_u16_d16_hi v39,v39
ds_read_b64_tr_b16 a[192:193],v1 offset:22576
v_lshl_add_u64 v[34:35],v[44:45],0,v[46:47]
v_lshlrev_b32_sdwa v40,1,v40 dst_sel:DWORD dst_unused:UNUSED_PAD src0_sel:DWORD src1_sel:WORD_1
v_mfma_f32_16x16x32_bf16 v[48:51],a[200:203],a[92:95],v[48:51]
ds_read_b64_tr_b16 a[194:195],v1 offset:23600
v_exp_f16_e64 v8,v9
v_mfma_f32_16x16x32_bf16 v[44:47],a[212:215],a[92:95],v[20:23]
ds_read_b128 v[20:23],v4 offset:1056
ds_read_u16 v40,v40
v_lshlrev_b32_sdwa v42,1,v42 dst_sel:DWORD dst_unused:UNUSED_PAD src0_sel:DWORD src1_sel:WORD_1
v_mfma_f32_16x16x32_bf16 v[44:47],a[204:207],a[76:79],v[44:47]
ds_read_u16_d16_hi v42,v42
ds_read_b64_tr_b16 a[196:197],v2 offset:22576
v_lshlrev_b32_sdwa v41,1,v41 dst_sel:DWORD dst_unused:UNUSED_PAD src0_sel:DWORD src1_sel:WORD_1
v_mfma_f32_16x16x32_bf16 v[44:47],a[208:211],a[60:63],v[44:47]
ds_read_u16 v41,v41
v_exp_f16_sdwa v8,v9 dst_sel:WORD_1 dst_unused:UNUSED_PRESERVE src0_sel:WORD_1
v_mfma_f32_16x16x32_bf16 v[112:115],a[228:231],v[60:63],v[112:115]
ds_read_b64_tr_b16 a[198:199],v2 offset:23600
ds_read_b64_tr_b16 a[200:201],v2 offset:20528
v_lshlrev_b32_sdwa v43,1,v43 dst_sel:DWORD dst_unused:UNUSED_PAD src0_sel:DWORD src1_sel:WORD_1
v_mfma_f32_16x16x32_bf16 v[116:119],a[228:231],v[32:35],v[116:119]
ds_read_u16_d16_hi v43,v43
v_lshlrev_b32_sdwa v56,1,v56 dst_sel:DWORD dst_unused:UNUSED_PAD src0_sel:DWORD src1_sel:WORD_1
v_mfma_f32_16x16x32_bf16 v[76:79],a[232:235],v[32:35],v[76:79]
ds_read_b64_tr_b16 a[202:203],v2 offset:21552
v_pk_fma_f16 v8,v8,s55,s55
v_mfma_f32_16x16x32_bf16 v[72:75],a[232:235],v[60:63],v[72:75]
v_rcp_f16 v9,v8
v_lshlrev_b32_sdwa v58,1,v58 dst_sel:DWORD dst_unused:UNUSED_PAD src0_sel:DWORD src1_sel:WORD_1
v_mfma_f32_16x16x32_bf16 v[120:123],a[236:239],v[60:63],v[120:123]
ds_read_u16 v56,v56
v_lshlrev_b32_sdwa v57,1,v57 dst_sel:DWORD dst_unused:UNUSED_PAD src0_sel:DWORD src1_sel:WORD_1
v_mfma_f32_16x16x32_bf16 v[124:127],a[236:239],v[32:35],v[124:127]
ds_read_u16_d16_hi v58,v58
v_lshlrev_b32_sdwa v59,1,v59 dst_sel:DWORD dst_unused:UNUSED_PAD src0_sel:DWORD src1_sel:WORD_1
v_mfma_f32_16x16x32_bf16 v[92:95],a[244:247],v[32:35],v[92:95]
ds_read_u16 v57,v57
v_lshlrev_b32_sdwa v52,1,v52 dst_sel:DWORD dst_unused:UNUSED_PAD src0_sel:DWORD src1_sel:WORD_1
v_mfma_f32_16x16x32_bf16 v[88:91],a[244:247],v[60:63],v[88:91]
ds_read_u16_d16_hi v59,v59
v_rcp_f16_sdwa v9,v8 dst_sel:WORD_1 dst_unused:UNUSED_PRESERVE src0_sel:WORD_1
v_mfma_f32_16x16x32_bf16 v[128:131],a[248:251],v[60:63],v[128:131]
ds_read_b64_tr_b16 a[212:213],v2 offset:18480
v_lshlrev_b32_sdwa v54,1,v54 dst_sel:DWORD dst_unused:UNUSED_PAD src0_sel:DWORD src1_sel:WORD_1
v_mfma_f32_16x16x32_bf16 v[132:135],a[248:251],v[32:35],v[132:135]
ds_read_b64_tr_b16 a[214:215],v2 offset:19504
v_fma_mix_f32 v53,v53,v9,0 op_sel_hi:[0,1,0]
v_mfma_f32_16x16x32_bf16 v[108:111],a[252:255],v[32:35],v[108:111]
ds_read_u16 v52,v52
v_fma_mix_f32 v55,v55,v9,0 op_sel:[0,1,0] op_sel_hi:[0,1,0]
v_mfma_f32_16x16x32_bf16 v[104:107],a[252:255],v[60:63],v[104:107]
s_waitcnt lgkmcnt(7)
ds_read_u16_d16_hi v54,v54
v_mfma_f32_16x16x32_bf16 v[32:35],a[220:223],a[0:3],v[16:19]
s_add_u32 m0,s57,3544
v_lshlrev_b32_sdwa v24,1,v24 dst_sel:DWORD dst_unused:UNUSED_PAD src0_sel:DWORD src1_sel:WORD_1
v_lshlrev_b32_sdwa v26,1,v26 dst_sel:DWORD dst_unused:UNUSED_PAD src0_sel:DWORD src1_sel:WORD_1
v_mfma_f32_16x16x32_bf16 v[32:35],a[216:219],a[16:19],v[32:35]
s_add_u32 s89,s56,5013504
ds_read_u16 v24,v24
v_lshl_add_u64 v[36:37],v[36:37],0,v[38:39]
v_mfma_f32_16x16x32_bf16 v[32:35],a[192:195],a[32:35],v[32:35]
buffer_load_dwordx4 v6,s[36:39],s89 offen lds
v_lshl_add_u64 v[38:39],v[40:41],0,v[42:43]
v_mfma_f32_16x16x32_bf16 v[60:63],a[196:199],a[32:35],v[20:23]
s_add_u32 m0,s57,7072
ds_read_u16_d16_hi v26,v26
v_lshlrev_b32_sdwa v25,1,v25 dst_sel:DWORD dst_unused:UNUSED_PAD src0_sel:DWORD src1_sel:WORD_1
v_mfma_f32_16x16x32_bf16 v[60:63],a[200:203],a[16:19],v[60:63]
s_waitcnt lgkmcnt(4)
ds_read_u16 v25,v25
v_lshl_add_u64 v[40:41],v[56:57],0,v[58:59]
v_mfma_f32_16x16x32_bf16 v[60:63],a[212:215],a[0:3],v[60:63]
s_add_u32 s89,s56,5111808
v_lshlrev_b32_sdwa v27,1,v27 dst_sel:DWORD dst_unused:UNUSED_PAD src0_sel:DWORD src1_sel:WORD_1
v_lshlrev_b32_sdwa v28,1,v28 dst_sel:DWORD dst_unused:UNUSED_PAD src0_sel:DWORD src1_sel:WORD_1
v_mfma_f32_16x16x32_bf16 v[56:59],a[212:215],a[4:7],v[20:23]
s_waitcnt lgkmcnt(3)
buffer_load_dwordx4 v6,s[36:39],s89 offen lds
v_or_b32 v42,v52,v54
v_mfma_f32_16x16x32_bf16 v[56:59],a[200:203],a[20:23],v[56:59]
ds_read_u16_d16_hi v27,v27
v_cvt_pk_bf16_f32 v43,v53,v55
v_lshlrev_b32_sdwa v30,1,v30 dst_sel:DWORD dst_unused:UNUSED_PAD src0_sel:DWORD src1_sel:WORD_1
v_mfma_f32_16x16x32_bf16 v[56:59],a[196:199],a[36:39],v[56:59]
ds_read_u16 v28,v28
v_lshlrev_b32_sdwa v29,1,v29 dst_sel:DWORD dst_unused:UNUSED_PAD src0_sel:DWORD src1_sel:WORD_1
v_mfma_f32_16x16x32_bf16 v[52:55],a[192:195],a[36:39],v[16:19]
ds_read_u16_d16_hi v30,v30
v_lshlrev_b32_sdwa v31,1,v31 dst_sel:DWORD dst_unused:UNUSED_PAD src0_sel:DWORD src1_sel:WORD_1
v_mfma_f32_16x16x32_bf16 v[52:55],a[216:219],a[20:23],v[52:55]
ds_read_u16 v29,v29
v_lshlrev_b32_sdwa v44,1,v44 dst_sel:DWORD dst_unused:UNUSED_PAD src0_sel:DWORD src1_sel:WORD_1
v_mfma_f32_16x16x32_bf16 v[52:55],a[220:223],a[4:7],v[52:55]
ds_read_u16_d16_hi v31,v31
v_lshlrev_b32_sdwa v46,1,v46 dst_sel:DWORD dst_unused:UNUSED_PAD src0_sel:DWORD src1_sel:WORD_1
v_mfma_f32_16x16x32_bf16 v[232:235],a[228:231],v[36:39],v[232:235]
ds_read_u16 v44,v44
v_lshlrev_b32_sdwa v45,1,v45 dst_sel:DWORD dst_unused:UNUSED_PAD src0_sel:DWORD src1_sel:WORD_1
v_mfma_f32_16x16x32_bf16 v[236:239],a[228:231],v[40:43],v[236:239]
ds_read_u16_d16_hi v46,v46
v_lshlrev_b32_sdwa v47,1,v47 dst_sel:DWORD dst_unused:UNUSED_PAD src0_sel:DWORD src1_sel:WORD_1
v_mfma_f32_16x16x32_bf16 v[164:167],a[232:235],v[40:43],v[164:167]
ds_read_u16 v45,v45
ds_read_u16_d16_hi v47,v47
v_mfma_f32_16x16x32_bf16 v[160:163],a[232:235],v[36:39],v[160:163]
ds_read_b64_tr_b16 a[240:241],v3 offset:0
v_lshlrev_b32_sdwa v48,1,v48 dst_sel:DWORD dst_unused:UNUSED_PAD src0_sel:DWORD src1_sel:WORD_1
v_mfma_f32_16x16x32_bf16 v[240:243],a[236:239],v[36:39],v[240:243]
ds_read_u16 v48,v48
v_lshlrev_b32_sdwa v50,1,v50 dst_sel:DWORD dst_unused:UNUSED_PAD src0_sel:DWORD src1_sel:WORD_1
v_mfma_f32_16x16x32_bf16 v[244:247],a[236:239],v[40:43],v[244:247]
ds_read_u16_d16_hi v50,v50
v_lshlrev_b32_sdwa v49,1,v49 dst_sel:DWORD dst_unused:UNUSED_PAD src0_sel:DWORD src1_sel:WORD_1
v_mfma_f32_16x16x32_bf16 v[180:183],a[244:247],v[40:43],v[180:183]
ds_read_u16 v49,v49
v_lshlrev_b32_sdwa v51,1,v51 dst_sel:DWORD dst_unused:UNUSED_PAD src0_sel:DWORD src1_sel:WORD_1
v_mfma_f32_16x16x32_bf16 v[176:179],a[244:247],v[36:39],v[176:179]
ds_read_u16_d16_hi v51,v51
v_mfma_f32_16x16x32_bf16 v[248:251],a[248:251],v[36:39],v[248:251]
ds_read_b64_tr_b16 a[242:243],v3 offset:1536
v_mfma_f32_16x16x32_bf16 v[252:255],a[248:251],v[40:43],v[252:255]
ds_read_b64_tr_b16 a[224:225],v3 offset:8
v_mfma_f32_16x16x32_bf16 v[196:199],a[252:255],v[40:43],v[196:199]
ds_read_b64_tr_b16 a[226:227],v3 offset:1544
v_mfma_f32_16x16x32_bf16 v[192:195],a[252:255],v[36:39],v[192:195]
v_mfma_f32_16x16x32_bf16 v[40:43],a[220:223],a[8:11],v[16:19]
ds_read_b64_tr_b16 a[204:205],v1 offset:0
v_lshlrev_b32_sdwa v32,1,v32 dst_sel:DWORD dst_unused:UNUSED_PAD src0_sel:DWORD src1_sel:WORD_1
v_mfma_f32_16x16x32_bf16 v[40:43],a[216:219],a[24:27],v[40:43]
s_waitcnt lgkmcnt(7)
ds_read_u16 v32,v32
v_lshl_add_u64 v[24:25],v[24:25],0,v[26:27]
v_fma_mixlo_f16 v8,v57,s99,0
v_mfma_f32_16x16x32_bf16 v[40:43],a[192:195],a[40:43],v[40:43]
ds_read_b64_tr_b16 a[206:207],v1 offset:1024
v_lshl_add_u64 v[26:27],v[28:29],0,v[30:31]
v_lshlrev_b32_sdwa v34,1,v34 dst_sel:DWORD dst_unused:UNUSED_PAD src0_sel:DWORD src1_sel:WORD_1
v_mfma_f32_16x16x32_bf16 v[36:39],a[196:199],a[40:43],v[20:23]
ds_read_u16_d16_hi v34,v34
v_fma_mixhi_f16 v8,v59,s99,0
v_mfma_f32_16x16x32_bf16 v[36:39],a[200:203],a[24:27],v[36:39]
ds_read_b64_tr_b16 a[208:209],v1 offset:2048
v_lshl_add_u64 v[28:29],v[44:45],0,v[46:47]
v_lshlrev_b32_sdwa v33,1,v33 dst_sel:DWORD dst_unused:UNUSED_PAD src0_sel:DWORD src1_sel:WORD_1
v_pk_fma_f16 v9,v8,v8,s54
v_mfma_f32_16x16x32_bf16 v[36:39],a[212:215],a[8:11],v[36:39]
ds_read_u16 v33,v33
v_lshlrev_b32_sdwa v35,1,v35 dst_sel:DWORD dst_unused:UNUSED_PAD src0_sel:DWORD src1_sel:WORD_1
v_mfma_f32_16x16x32_bf16 v[44:47],a[212:215],a[12:15],v[20:23]
ds_read_u16_d16_hi v35,v35
v_pk_mul_f16 v9,v8,v9
v_mfma_f32_16x16x32_bf16 v[44:47],a[200:203],a[28:31],v[44:47]
s_waitcnt lgkmcnt(7)
ds_read_b64_tr_b16 a[210:211],v1 offset:3072
v_lshl_add_u64 v[30:31],v[48:49],0,v[50:51]
v_lshlrev_b32_sdwa v60,1,v60 dst_sel:DWORD dst_unused:UNUSED_PAD src0_sel:DWORD src1_sel:WORD_1
v_mfma_f32_16x16x32_bf16 v[44:47],a[196:199],a[44:47],v[44:47]
v_exp_f16 v8,v9
v_mfma_f32_16x16x32_bf16 v[48:51],a[192:195],a[44:47],v[16:19]
ds_read_u16 v60,v60
v_lshlrev_b32_sdwa v62,1,v62 dst_sel:DWORD dst_unused:UNUSED_PAD src0_sel:DWORD src1_sel:WORD_1
v_mfma_f32_16x16x32_bf16 v[48:51],a[216:219],a[28:31],v[48:51]
ds_read_u16_d16_hi v62,v62
v_lshlrev_b32_sdwa v61,1,v61 dst_sel:DWORD dst_unused:UNUSED_PAD src0_sel:DWORD src1_sel:WORD_1
v_mfma_f32_16x16x32_bf16 v[48:51],a[220:223],a[12:15],v[48:51]
v_exp_f16_sdwa v8,v9 dst_sel:WORD_1 dst_unused:UNUSED_PRESERVE src0_sel:WORD_1
v_mfma_f32_16x16x32_bf16 v[208:211],a[228:231],v[24:27],v[208:211]
ds_read_u16 v61,v61
v_lshlrev_b32_sdwa v63,1,v63 dst_sel:DWORD dst_unused:UNUSED_PAD src0_sel:DWORD src1_sel:WORD_1
v_mfma_f32_16x16x32_bf16 v[212:215],a[228:231],v[28:31],v[212:215]
ds_read_u16_d16_hi v63,v63
v_lshlrev_b32_sdwa v52,1,v52 dst_sel:DWORD dst_unused:UNUSED_PAD src0_sel:DWORD src1_sel:WORD_1
v_mfma_f32_16x16x32_bf16 v[172:175],a[232:235],v[28:31],v[172:175]
ds_read_u16 v52,v52
v_pk_fma_f16 v8,v8,s55,s55
v_mfma_f32_16x16x32_bf16 v[168:171],a[232:235],v[24:27],v[168:171]
ds_read_b64_tr_b16 a[228:229],v3 offset:64
v_rcp_f16 v9,v8
v_lshlrev_b32_sdwa v54,1,v54 dst_sel:DWORD dst_unused:UNUSED_PAD src0_sel:DWORD src1_sel:WORD_1
v_mfma_f32_16x16x32_bf16 v[216:219],a[236:239],v[24:27],v[216:219]
ds_read_u16_d16_hi v54,v54
v_lshlrev_b32_sdwa v53,1,v53 dst_sel:DWORD dst_unused:UNUSED_PAD src0_sel:DWORD src1_sel:WORD_1
v_mfma_f32_16x16x32_bf16 v[220:223],a[236:239],v[28:31],v[220:223]
ds_read_u16 v53,v53
ds_read_b64_tr_b16 a[230:231],v3 offset:1600
v_mfma_f32_16x16x32_bf16 v[188:191],a[244:247],v[28:31],v[188:191]
ds_read_b64_tr_b16 a[232:233],v3 offset:72
v_lshlrev_b32_sdwa v55,1,v55 dst_sel:DWORD dst_unused:UNUSED_PAD src0_sel:DWORD src1_sel:WORD_1
v_mfma_f32_16x16x32_bf16 v[184:187],a[244:247],v[24:27],v[184:187]
ds_read_u16_d16_hi v55,v55
v_rcp_f16_sdwa v9,v8 dst_sel:WORD_1 dst_unused:UNUSED_PRESERVE src0_sel:WORD_1
v_mfma_f32_16x16x32_bf16 v[224:227],a[248:251],v[24:27],v[224:227]
ds_read_b64_tr_b16 a[234:235],v3 offset:1608
ds_read_b64_tr_b16 a[236:237],v3 offset:128
v_lshlrev_b32_sdwa v56,1,v56 dst_sel:DWORD dst_unused:UNUSED_PAD src0_sel:DWORD src1_sel:WORD_1
v_mfma_f32_16x16x32_bf16 v[228:231],a[248:251],v[28:31],v[228:231]
ds_read_u16 v56,v56
v_lshlrev_b32_sdwa v58,1,v58 dst_sel:DWORD dst_unused:UNUSED_PAD src0_sel:DWORD src1_sel:WORD_1
v_mfma_f32_16x16x32_bf16 v[204:207],a[252:255],v[28:31],v[204:207]
ds_read_u16_d16_hi v58,v58
v_fma_mix_f32 v57,v57,v9,0 op_sel_hi:[0,1,0]
v_mfma_f32_16x16x32_bf16 v[200:203],a[252:255],v[24:27],v[200:203]
ds_read_b64_tr_b16 a[238:239],v3 offset:1664
v_fma_mix_f32 v59,v59,v9,0 op_sel:[0,1,0] op_sel_hi:[0,1,0]
v_mfma_f32_16x16x32_bf16 v[28:31],a[220:223],a[48:51],v[16:19]
s_add_u32 m0,s57,10608
ds_read_b64 v[14:15],v7 offset:0
v_lshlrev_b32_sdwa v40,1,v40 dst_sel:DWORD dst_unused:UNUSED_PAD src0_sel:DWORD src1_sel:WORD_1
v_mfma_f32_16x16x32_bf16 v[28:31],a[216:219],a[64:67],v[28:31]
s_waitcnt lgkmcnt(10)
ds_read_b64_tr_b16 a[244:245],v3 offset:136
v_lshl_add_u64 v[32:33],v[32:33],0,v[34:35]
v_mfma_f32_16x16x32_bf16 v[28:31],a[192:195],a[80:83],v[28:31]
s_add_u32 s89,s56,5210112
ds_read_b64_tr_b16 a[246:247],v3 offset:1672
v_lshl_add_u64 v[34:35],v[60:61],0,v[62:63]
v_lshlrev_b32_sdwa v42,1,v42 dst_sel:DWORD dst_unused:UNUSED_PAD src0_sel:DWORD src1_sel:WORD_1
v_mfma_f32_16x16x32_bf16 v[24:27],a[196:199],a[80:83],v[20:23]
buffer_load_dwordx4 v6,s[36:39],s89 offen lds
v_lshlrev_b32_sdwa v41,1,v41 dst_sel:DWORD dst_unused:UNUSED_PAD src0_sel:DWORD src1_sel:WORD_1
v_mfma_f32_16x16x32_bf16 v[24:27],a[200:203],a[64:67],v[24:27]
s_waitcnt lgkmcnt(8)
ds_read_u16 v40,v40
v_lshl_add_u64 v[60:61],v[52:53],0,v[54:55]
v_mfma_f32_16x16x32_bf16 v[24:27],a[212:215],a[48:51],v[24:27]
s_waitcnt lgkmcnt(3)
ds_read_u16_d16_hi v42,v42
v_or3_b32 v13,v14,v15,v13
v_lshlrev_b32_sdwa v43,1,v43 dst_sel:DWORD dst_unused:UNUSED_PAD src0_sel:DWORD src1_sel:WORD_1
v_mfma_f32_16x16x32_bf16 v[52:55],a[212:215],a[52:55],v[20:23]
s_add_u32 m0,s57,14152
ds_read_b64 v[14:15],v7 offset:512
v_or_b32 v62,v56,v58
v_lshlrev_b32_sdwa v36,1,v36 dst_sel:DWORD dst_unused:UNUSED_PAD src0_sel:DWORD src1_sel:WORD_1
v_mfma_f32_16x16x32_bf16 v[52:55],a[200:203],a[68:71],v[52:55]
s_add_u32 s89,s56,5308416
ds_read_u16 v41,v41
v_cvt_pk_bf16_f32 v63,v57,v59
v_lshlrev_b32_sdwa v38,1,v38 dst_sel:DWORD dst_unused:UNUSED_PAD src0_sel:DWORD src1_sel:WORD_1
v_mfma_f32_16x16x32_bf16 v[52:55],a[196:199],a[84:87],v[52:55]
buffer_load_dwordx4 v6,s[36:39],s89 offen lds
v_lshlrev_b32_sdwa v37,1,v37 dst_sel:DWORD dst_unused:UNUSED_PAD src0_sel:DWORD src1_sel:WORD_1
v_mfma_f32_16x16x32_bf16 v[56:59],a[192:195],a[84:87],v[16:19]
ds_read_u16_d16_hi v43,v43
v_lshlrev_b32_sdwa v39,1,v39 dst_sel:DWORD dst_unused:UNUSED_PAD src0_sel:DWORD src1_sel:WORD_1
v_mfma_f32_16x16x32_bf16 v[56:59],a[216:219],a[68:71],v[56:59]
ds_read_u16 v36,v36
v_lshlrev_b32_sdwa v48,1,v48 dst_sel:DWORD dst_unused:UNUSED_PAD src0_sel:DWORD src1_sel:WORD_1
v_mfma_f32_16x16x32_bf16 v[56:59],a[220:223],a[52:55],v[56:59]
s_waitcnt lgkmcnt(3)
ds_read_u16_d16_hi v38,v38
v_or3_b32 v13,v14,v15,v13
v_mfma_f32_16x16x32_bf16 v[136:139],a[240:243],v[32:35],v[136:139]
ds_read_b64 v[14:15],v7 offset:1024
v_lshlrev_b32_sdwa v50,1,v50 dst_sel:DWORD dst_unused:UNUSED_PAD src0_sel:DWORD src1_sel:WORD_1
v_mfma_f32_16x16x32_bf16 v[140:143],a[240:243],v[60:63],v[140:143]
ds_read_u16 v37,v37
v_lshlrev_b32_sdwa v49,1,v49 dst_sel:DWORD dst_unused:UNUSED_PAD src0_sel:DWORD src1_sel:WORD_1
v_mfma_f32_16x16x32_bf16 v[68:71],a[224:227],v[60:63],v[68:71]
ds_read_u16_d16_hi v39,v39
v_lshlrev_b32_sdwa v51,1,v51 dst_sel:DWORD dst_unused:UNUSED_PAD src0_sel:DWORD src1_sel:WORD_1
v_mfma_f32_16x16x32_bf16 v[64:67],a[224:227],v[32:35],v[64:67]
ds_read_u16 v48,v48
v_lshlrev_b32_sdwa v44,1,v44 dst_sel:DWORD dst_unused:UNUSED_PAD src0_sel:DWORD src1_sel:WORD_1
v_mfma_f32_16x16x32_bf16 v[144:147],a[228:231],v[32:35],v[144:147]
ds_read_u16_d16_hi v50,v50
v_lshlrev_b32_sdwa v46,1,v46 dst_sel:DWORD dst_unused:UNUSED_PAD src0_sel:DWORD src1_sel:WORD_1
v_mfma_f32_16x16x32_bf16 v[148:151],a[228:231],v[60:63],v[148:151]
s_waitcnt lgkmcnt(4)
ds_read_u16 v49,v49
TP3814_%=:
s_cbranch_execz TP5958_%=
v_or3_b32 v13,v14,v15,v13
v_mfma_f32_16x16x32_bf16 v[84:87],a[232:235],v[60:63],v[84:87]
s_add_u32 m0,s57,17688
ds_read_u16_d16_hi v51,v51
v_lshlrev_b32_sdwa v45,1,v45 dst_sel:DWORD dst_unused:UNUSED_PAD src0_sel:DWORD src1_sel:WORD_1
v_mfma_f32_16x16x32_bf16 v[80:83],a[232:235],v[32:35],v[80:83]
s_add_u32 s89,s56,5406720
ds_read_u16 v44,v44
v_lshlrev_b32_sdwa v47,1,v47 dst_sel:DWORD dst_unused:UNUSED_PAD src0_sel:DWORD src1_sel:WORD_1
v_mfma_f32_16x16x32_bf16 v[152:155],a[236:239],v[32:35],v[152:155]
buffer_load_dwordx4 v6,s[36:39],s89 offen lds
v_mfma_f32_16x16x32_bf16 v[156:159],a[236:239],v[60:63],v[156:159]
ds_read_u16_d16_hi v46,v46
v_mfma_f32_16x16x32_bf16 v[100:103],a[244:247],v[60:63],v[100:103]
ds_read_u16 v45,v45
v_mfma_f32_16x16x32_bf16 v[96:99],a[244:247],v[32:35],v[96:99]
ds_read_u16_d16_hi v47,v47
v_mfma_f32_16x16x32_bf16 v[60:63],a[220:223],a[56:59],v[16:19]
v_lshlrev_b32_sdwa v28,1,v28 dst_sel:DWORD dst_unused:UNUSED_PAD src0_sel:DWORD src1_sel:WORD_1
v_fma_mixlo_f16 v8,v53,s99,0
v_mfma_f32_16x16x32_bf16 v[60:63],a[216:219],a[72:75],v[60:63]
ds_read_u16 v28,v28
v_lshl_add_u64 v[40:41],v[40:41],0,v[42:43]
v_mfma_f32_16x16x32_bf16 v[60:63],a[192:195],a[88:91],v[60:63]
s_waitcnt lgkmcnt(6)
v_lshl_add_u64 v[42:43],v[36:37],0,v[38:39]
v_lshlrev_b32_sdwa v30,1,v30 dst_sel:DWORD dst_unused:UNUSED_PAD src0_sel:DWORD src1_sel:WORD_1
v_mfma_f32_16x16x32_bf16 v[32:35],a[196:199],a[88:91],v[20:23]
ds_read_u16_d16_hi v30,v30
v_fma_mixhi_f16 v8,v55,s99,0
v_mfma_f32_16x16x32_bf16 v[32:35],a[200:203],a[72:75],v[32:35]
s_waitcnt lgkmcnt(5)
v_lshl_add_u64 v[36:37],v[48:49],0,v[50:51]
v_lshlrev_b32_sdwa v29,1,v29 dst_sel:DWORD dst_unused:UNUSED_PAD src0_sel:DWORD src1_sel:WORD_1
v_pk_fma_f16 v9,v8,v8,s54
v_mfma_f32_16x16x32_bf16 v[32:35],a[212:215],a[56:59],v[32:35]
ds_read_u16 v29,v29
v_lshlrev_b32_sdwa v31,1,v31 dst_sel:DWORD dst_unused:UNUSED_PAD src0_sel:DWORD src1_sel:WORD_1
v_mfma_f32_16x16x32_bf16 v[48:51],a[212:215],a[60:63],v[20:23]
s_nop 0
ds_read_b128 v[20:23],v4 offset:1152
v_pk_mul_f16 v9,v8,v9
v_mfma_f32_16x16x32_bf16 v[48:51],a[200:203],a[76:79],v[48:51]
s_waitcnt lgkmcnt(4)
ds_read_u16_d16_hi v31,v31
ds_read_b64_tr_b16 a[212:213],v1 offset:4096
v_lshl_add_u64 v[38:39],v[44:45],0,v[46:47]
v_lshlrev_b32_sdwa v24,1,v24 dst_sel:DWORD dst_unused:UNUSED_PAD src0_sel:DWORD src1_sel:WORD_1
v_mfma_f32_16x16x32_bf16 v[48:51],a[196:199],a[92:95],v[48:51]
ds_read_b64_tr_b16 a[214:215],v1 offset:5120
v_exp_f16 v8,v9
v_mfma_f32_16x16x32_bf16 v[44:47],a[192:195],a[92:95],v[16:19]
ds_read_b128 v[16:19],v4 offset:1184
ds_read_u16 v24,v24
v_lshlrev_b32_sdwa v26,1,v26 dst_sel:DWORD dst_unused:UNUSED_PAD src0_sel:DWORD src1_sel:WORD_1
v_mfma_f32_16x16x32_bf16 v[44:47],a[216:219],a[76:79],v[44:47]
ds_read_u16_d16_hi v26,v26
ds_read_b64_tr_b16 a[200:201],v2 offset:4096
v_lshlrev_b32_sdwa v25,1,v25 dst_sel:DWORD dst_unused:UNUSED_PAD src0_sel:DWORD src1_sel:WORD_1
v_mfma_f32_16x16x32_bf16 v[44:47],a[220:223],a[60:63],v[44:47]
ds_read_u16 v25,v25
v_exp_f16_sdwa v8,v9 dst_sel:WORD_1 dst_unused:UNUSED_PRESERVE src0_sel:WORD_1
v_mfma_f32_16x16x32_bf16 v[112:115],a[240:243],v[40:43],v[112:115]
ds_read_b64_tr_b16 a[202:203],v2 offset:5120
ds_read_b64_tr_b16 a[196:197],v2 offset:2048
v_lshlrev_b32_sdwa v27,1,v27 dst_sel:DWORD dst_unused:UNUSED_PAD src0_sel:DWORD src1_sel:WORD_1
v_mfma_f32_16x16x32_bf16 v[116:119],a[240:243],v[36:39],v[116:119]
ds_read_u16_d16_hi v27,v27
v_lshlrev_b32_sdwa v56,1,v56 dst_sel:DWORD dst_unused:UNUSED_PAD src0_sel:DWORD src1_sel:WORD_1
v_mfma_f32_16x16x32_bf16 v[76:79],a[224:227],v[36:39],v[76:79]
ds_read_b64_tr_b16 a[198:199],v2 offset:3072
v_pk_fma_f16 v8,v8,s55,s55
v_mfma_f32_16x16x32_bf16 v[72:75],a[224:227],v[40:43],v[72:75]
v_rcp_f16 v9,v8
v_lshlrev_b32_sdwa v58,1,v58 dst_sel:DWORD dst_unused:UNUSED_PAD src0_sel:DWORD src1_sel:WORD_1
v_mfma_f32_16x16x32_bf16 v[120:123],a[228:231],v[40:43],v[120:123]
ds_read_u16 v56,v56
v_lshlrev_b32_sdwa v57,1,v57 dst_sel:DWORD dst_unused:UNUSED_PAD src0_sel:DWORD src1_sel:WORD_1
v_mfma_f32_16x16x32_bf16 v[124:127],a[228:231],v[36:39],v[124:127]
ds_read_u16_d16_hi v58,v58
v_lshlrev_b32_sdwa v59,1,v59 dst_sel:DWORD dst_unused:UNUSED_PAD src0_sel:DWORD src1_sel:WORD_1
v_mfma_f32_16x16x32_bf16 v[92:95],a[232:235],v[36:39],v[92:95]
ds_read_u16 v57,v57
v_lshlrev_b32_sdwa v52,1,v52 dst_sel:DWORD dst_unused:UNUSED_PAD src0_sel:DWORD src1_sel:WORD_1
v_mfma_f32_16x16x32_bf16 v[88:91],a[232:235],v[40:43],v[88:91]
ds_read_u16_d16_hi v59,v59
v_rcp_f16_sdwa v9,v8 dst_sel:WORD_1 dst_unused:UNUSED_PRESERVE src0_sel:WORD_1
v_mfma_f32_16x16x32_bf16 v[128:131],a[236:239],v[40:43],v[128:131]
ds_read_b64_tr_b16 a[192:193],v2 offset:0
v_lshlrev_b32_sdwa v54,1,v54 dst_sel:DWORD dst_unused:UNUSED_PAD src0_sel:DWORD src1_sel:WORD_1
v_mfma_f32_16x16x32_bf16 v[132:135],a[236:239],v[36:39],v[132:135]
ds_read_b64_tr_b16 a[194:195],v2 offset:1024
v_fma_mix_f32 v53,v53,v9,0 op_sel_hi:[0,1,0]
v_mfma_f32_16x16x32_bf16 v[108:111],a[244:247],v[36:39],v[108:111]
ds_read_u16 v52,v52
v_fma_mix_f32 v55,v55,v9,0 op_sel:[0,1,0] op_sel_hi:[0,1,0]
v_mfma_f32_16x16x32_bf16 v[104:107],a[244:247],v[40:43],v[104:107]
s_waitcnt lgkmcnt(7)
ds_read_u16_d16_hi v54,v54
v_mfma_f32_16x16x32_bf16 v[36:39],a[204:207],a[0:3],v[20:23]
s_add_u32 m0,s57,21216
v_lshlrev_b32_sdwa v60,1,v60 dst_sel:DWORD dst_unused:UNUSED_PAD src0_sel:DWORD src1_sel:WORD_1
v_lshlrev_b32_sdwa v62,1,v62 dst_sel:DWORD dst_unused:UNUSED_PAD src0_sel:DWORD src1_sel:WORD_1
v_mfma_f32_16x16x32_bf16 v[36:39],a[208:211],a[16:19],v[36:39]
s_add_u32 s89,s56,5505024
ds_read_u16 v60,v60
v_lshl_add_u64 v[28:29],v[28:29],0,v[30:31]
v_mfma_f32_16x16x32_bf16 v[36:39],a[212:215],a[32:35],v[36:39]
buffer_load_dwordx4 v6,s[36:39],s89 offen lds
v_lshl_add_u64 v[30:31],v[24:25],0,v[26:27]
v_mfma_f32_16x16x32_bf16 v[40:43],a[200:203],a[32:35],v[16:19]
ds_read_u16_d16_hi v62,v62
v_lshlrev_b32_sdwa v61,1,v61 dst_sel:DWORD dst_unused:UNUSED_PAD src0_sel:DWORD src1_sel:WORD_1
v_mfma_f32_16x16x32_bf16 v[40:43],a[196:199],a[16:19],v[40:43]
s_waitcnt lgkmcnt(4)
ds_read_u16 v61,v61
v_lshl_add_u64 v[24:25],v[56:57],0,v[58:59]
v_mfma_f32_16x16x32_bf16 v[40:43],a[192:195],a[0:3],v[40:43]
v_lshlrev_b32_sdwa v63,1,v63 dst_sel:DWORD dst_unused:UNUSED_PAD src0_sel:DWORD src1_sel:WORD_1
v_lshlrev_b32_sdwa v32,1,v32 dst_sel:DWORD dst_unused:UNUSED_PAD src0_sel:DWORD src1_sel:WORD_1
v_mfma_f32_16x16x32_bf16 v[56:59],a[192:195],a[4:7],v[16:19]
s_waitcnt lgkmcnt(3)
ds_read_u16_d16_hi v63,v63
v_or_b32 v26,v52,v54
v_mfma_f32_16x16x32_bf16 v[56:59],a[196:199],a[20:23],v[56:59]
s_waitcnt vmcnt(7)
ds_read_u16 v32,v32
v_cvt_pk_bf16_f32 v27,v53,v55
v_lshlrev_b32_sdwa v34,1,v34 dst_sel:DWORD dst_unused:UNUSED_PAD src0_sel:DWORD src1_sel:WORD_1
v_mfma_f32_16x16x32_bf16 v[56:59],a[200:203],a[36:39],v[56:59]
s_barrier
ds_read_u16_d16_hi v34,v34
v_lshlrev_b32_sdwa v33,1,v33 dst_sel:DWORD dst_unused:UNUSED_PAD src0_sel:DWORD src1_sel:WORD_1
v_mfma_f32_16x16x32_bf16 v[52:55],a[212:215],a[36:39],v[20:23]
ds_read_u16 v33,v33
v_lshlrev_b32_sdwa v35,1,v35 dst_sel:DWORD dst_unused:UNUSED_PAD src0_sel:DWORD src1_sel:WORD_1
v_mfma_f32_16x16x32_bf16 v[52:55],a[208:211],a[20:23],v[52:55]
s_add_u32 m0,s58,18480
ds_read_u16_d16_hi v35,v35
v_lshlrev_b32_sdwa v44,1,v44 dst_sel:DWORD dst_unused:UNUSED_PAD src0_sel:DWORD src1_sel:WORD_1
v_mfma_f32_16x16x32_bf16 v[52:55],a[204:207],a[4:7],v[52:55]
s_mov_b32 s69,s88
ds_read_u16 v44,v44
v_lshlrev_b32_sdwa v46,1,v46 dst_sel:DWORD dst_unused:UNUSED_PAD src0_sel:DWORD src1_sel:WORD_1
v_mfma_f32_16x16x32_bf16 v[232:235],a[240:243],v[28:31],v[232:235]
buffer_load_dwordx4 v5,s[44:47],s69 offen offset:0 lds
v_lshlrev_b32_sdwa v45,1,v45 dst_sel:DWORD dst_unused:UNUSED_PAD src0_sel:DWORD src1_sel:WORD_1
v_mfma_f32_16x16x32_bf16 v[236:239],a[240:243],v[24:27],v[236:239]
ds_read_u16_d16_hi v46,v46
v_lshlrev_b32_sdwa v47,1,v47 dst_sel:DWORD dst_unused:UNUSED_PAD src0_sel:DWORD src1_sel:WORD_1
v_mfma_f32_16x16x32_bf16 v[164:167],a[224:227],v[24:27],v[164:167]
ds_read_u16 v45,v45
ds_read_u16_d16_hi v47,v47
v_mfma_f32_16x16x32_bf16 v[160:163],a[224:227],v[28:31],v[160:163]
ds_read_b64_tr_b16 a[248:249],v3 offset:6160
v_lshlrev_b32_sdwa v48,1,v48 dst_sel:DWORD dst_unused:UNUSED_PAD src0_sel:DWORD src1_sel:WORD_1
v_mfma_f32_16x16x32_bf16 v[240:243],a[228:231],v[28:31],v[240:243]
s_add_u32 s69,s69,s53
ds_read_u16 v48,v48
v_lshlrev_b32_sdwa v50,1,v50 dst_sel:DWORD dst_unused:UNUSED_PAD src0_sel:DWORD src1_sel:WORD_1
v_mfma_f32_16x16x32_bf16 v[244:247],a[228:231],v[24:27],v[244:247]
buffer_load_dwordx4 v5,s[44:47],s69 offen offset:1024 lds
ds_read_u16_d16_hi v50,v50
v_mfma_f32_16x16x32_bf16 v[180:183],a[232:235],v[24:27],v[180:183]
ds_read_b64_tr_b16 a[250:251],v3 offset:7696
v_lshlrev_b32_sdwa v49,1,v49 dst_sel:DWORD dst_unused:UNUSED_PAD src0_sel:DWORD src1_sel:WORD_1
v_mfma_f32_16x16x32_bf16 v[176:179],a[232:235],v[28:31],v[176:179]
ds_read_u16 v49,v49
v_lshlrev_b32_sdwa v51,1,v51 dst_sel:DWORD dst_unused:UNUSED_PAD src0_sel:DWORD src1_sel:WORD_1
v_mfma_f32_16x16x32_bf16 v[248:251],a[236:239],v[28:31],v[248:251]
ds_read_u16_d16_hi v51,v51
v_mfma_f32_16x16x32_bf16 v[252:255],a[236:239],v[24:27],v[252:255]
s_add_u32 s69,s69,s53
ds_read_b64_tr_b16 a[252:253],v3 offset:6168
v_mfma_f32_16x16x32_bf16 v[196:199],a[244:247],v[24:27],v[196:199]
buffer_load_dwordx4 v5,s[44:47],s69 offen offset:2048 lds
v_mfma_f32_16x16x32_bf16 v[192:195],a[244:247],v[28:31],v[192:195]
ds_read_b64_tr_b16 a[254:255],v3 offset:7704
v_mfma_f32_16x16x32_bf16 v[24:27],a[204:207],a[8:11],v[20:23]
ds_read_b64_tr_b16 a[216:217],v1 offset:6160
v_lshlrev_b32_sdwa v36,1,v36 dst_sel:DWORD dst_unused:UNUSED_PAD src0_sel:DWORD src1_sel:WORD_1
v_mfma_f32_16x16x32_bf16 v[24:27],a[208:211],a[24:27],v[24:27]
s_waitcnt lgkmcnt(7)
ds_read_u16 v36,v36
v_lshl_add_u64 v[60:61],v[60:61],0,v[62:63]
v_fma_mixlo_f16 v8,v57,s99,0
v_mfma_f32_16x16x32_bf16 v[24:27],a[212:215],a[40:43],v[24:27]
ds_read_b64_tr_b16 a[218:219],v1 offset:7184
v_lshl_add_u64 v[62:63],v[32:33],0,v[34:35]
v_lshlrev_b32_sdwa v38,1,v38 dst_sel:DWORD dst_unused:UNUSED_PAD src0_sel:DWORD src1_sel:WORD_1
v_mfma_f32_16x16x32_bf16 v[28:31],a[200:203],a[40:43],v[16:19]
ds_read_u16_d16_hi v38,v38
v_fma_mixhi_f16 v8,v59,s99,0
v_mfma_f32_16x16x32_bf16 v[28:31],a[196:199],a[24:27],v[28:31]
ds_read_b64_tr_b16 a[220:221],v1 offset:8208
v_lshl_add_u64 v[32:33],v[44:45],0,v[46:47]
v_lshlrev_b32_sdwa v37,1,v37 dst_sel:DWORD dst_unused:UNUSED_PAD src0_sel:DWORD src1_sel:WORD_1
v_pk_fma_f16 v9,v8,v8,s54
v_mfma_f32_16x16x32_bf16 v[28:31],a[192:195],a[8:11],v[28:31]
ds_read_u16 v37,v37
v_lshlrev_b32_sdwa v39,1,v39 dst_sel:DWORD dst_unused:UNUSED_PAD src0_sel:DWORD src1_sel:WORD_1
v_mfma_f32_16x16x32_bf16 v[44:47],a[192:195],a[12:15],v[16:19]
ds_read_u16_d16_hi v39,v39
v_pk_mul_f16 v9,v8,v9
v_mfma_f32_16x16x32_bf16 v[44:47],a[196:199],a[28:31],v[44:47]
s_waitcnt lgkmcnt(8)
ds_read_b64_tr_b16 a[222:223],v1 offset:9232
v_lshl_add_u64 v[34:35],v[48:49],0,v[50:51]
v_lshlrev_b32_sdwa v40,1,v40 dst_sel:DWORD dst_unused:UNUSED_PAD src0_sel:DWORD src1_sel:WORD_1
v_mfma_f32_16x16x32_bf16 v[44:47],a[200:203],a[44:47],v[44:47]
v_exp_f16 v8,v9
v_mfma_f32_16x16x32_bf16 v[48:51],a[212:215],a[44:47],v[20:23]
ds_read_u16 v40,v40
v_lshlrev_b32_sdwa v42,1,v42 dst_sel:DWORD dst_unused:UNUSED_PAD src0_sel:DWORD src1_sel:WORD_1
v_mfma_f32_16x16x32_bf16 v[48:51],a[208:211],a[28:31],v[48:51]
ds_read_u16_d16_hi v42,v42
v_lshlrev_b32_sdwa v41,1,v41 dst_sel:DWORD dst_unused:UNUSED_PAD src0_sel:DWORD src1_sel:WORD_1
v_mfma_f32_16x16x32_bf16 v[48:51],a[204:207],a[12:15],v[48:51]
v_exp_f16_sdwa v8,v9 dst_sel:WORD_1 dst_unused:UNUSED_PRESERVE src0_sel:WORD_1
v_mfma_f32_16x16x32_bf16 v[208:211],a[240:243],v[60:63],v[208:211]
ds_read_u16 v41,v41
v_lshlrev_b32_sdwa v43,1,v43 dst_sel:DWORD dst_unused:UNUSED_PAD src0_sel:DWORD src1_sel:WORD_1
v_mfma_f32_16x16x32_bf16 v[212:215],a[240:243],v[32:35],v[212:215]
ds_read_u16_d16_hi v43,v43
v_lshlrev_b32_sdwa v52,1,v52 dst_sel:DWORD dst_unused:UNUSED_PAD src0_sel:DWORD src1_sel:WORD_1
v_mfma_f32_16x16x32_bf16 v[172:175],a[224:227],v[32:35],v[172:175]
ds_read_u16 v52,v52
v_pk_fma_f16 v8,v8,s55,s55
v_mfma_f32_16x16x32_bf16 v[168:171],a[224:227],v[60:63],v[168:171]
ds_read_b64_tr_b16 a[240:241],v3 offset:6224
v_rcp_f16 v9,v8
v_lshlrev_b32_sdwa v54,1,v54 dst_sel:DWORD dst_unused:UNUSED_PAD src0_sel:DWORD src1_sel:WORD_1
v_mfma_f32_16x16x32_bf16 v[216:219],a[228:231],v[60:63],v[216:219]
ds_read_u16_d16_hi v54,v54
v_lshlrev_b32_sdwa v53,1,v53 dst_sel:DWORD dst_unused:UNUSED_PAD src0_sel:DWORD src1_sel:WORD_1
v_mfma_f32_16x16x32_bf16 v[220:223],a[228:231],v[32:35],v[220:223]
ds_read_u16 v53,v53
ds_read_b64_tr_b16 a[242:243],v3 offset:7760
v_mfma_f32_16x16x32_bf16 v[188:191],a[232:235],v[32:35],v[188:191]
ds_read_b64_tr_b16 a[224:225],v3 offset:6232
v_lshlrev_b32_sdwa v55,1,v55 dst_sel:DWORD dst_unused:UNUSED_PAD src0_sel:DWORD src1_sel:WORD_1
v_mfma_f32_16x16x32_bf16 v[184:187],a[232:235],v[60:63],v[184:187]
ds_read_u16_d16_hi v55,v55
v_rcp_f16_sdwa v9,v8 dst_sel:WORD_1 dst_unused:UNUSED_PRESERVE src0_sel:WORD_1
v_mfma_f32_16x16x32_bf16 v[224:227],a[236:239],v[60:63],v[224:227]
ds_read_b64_tr_b16 a[226:227],v3 offset:7768
ds_read_b64_tr_b16 a[228:229],v3 offset:6288
v_lshlrev_b32_sdwa v56,1,v56 dst_sel:DWORD dst_unused:UNUSED_PAD src0_sel:DWORD src1_sel:WORD_1
v_mfma_f32_16x16x32_bf16 v[228:231],a[236:239],v[32:35],v[228:231]
ds_read_u16 v56,v56
v_lshlrev_b32_sdwa v58,1,v58 dst_sel:DWORD dst_unused:UNUSED_PAD src0_sel:DWORD src1_sel:WORD_1
v_mfma_f32_16x16x32_bf16 v[204:207],a[244:247],v[32:35],v[204:207]
ds_read_u16_d16_hi v58,v58
v_fma_mix_f32 v57,v57,v9,0 op_sel_hi:[0,1,0]
v_mfma_f32_16x16x32_bf16 v[200:203],a[244:247],v[60:63],v[200:203]
ds_read_b64_tr_b16 a[230:231],v3 offset:7824
v_fma_mix_f32 v59,v59,v9,0 op_sel:[0,1,0] op_sel_hi:[0,1,0]
v_mfma_f32_16x16x32_bf16 v[32:35],a[204:207],a[48:51],v[20:23]
s_add_u32 m0,s58,0
ds_read_b64 v[14:15],v7 offset:6160
v_lshlrev_b32_sdwa v24,1,v24 dst_sel:DWORD dst_unused:UNUSED_PAD src0_sel:DWORD src1_sel:WORD_1
v_mfma_f32_16x16x32_bf16 v[32:35],a[208:211],a[64:67],v[32:35]
s_waitcnt lgkmcnt(10)
ds_read_b64_tr_b16 a[232:233],v3 offset:6296
v_lshl_add_u64 v[36:37],v[36:37],0,v[38:39]
v_mfma_f32_16x16x32_bf16 v[32:35],a[212:215],a[80:83],v[32:35]
s_mov_b32 s69,s87
ds_read_b64_tr_b16 a[234:235],v3 offset:7832
v_lshl_add_u64 v[38:39],v[40:41],0,v[42:43]
v_lshlrev_b32_sdwa v26,1,v26 dst_sel:DWORD dst_unused:UNUSED_PAD src0_sel:DWORD src1_sel:WORD_1
v_mfma_f32_16x16x32_bf16 v[60:63],a[200:203],a[80:83],v[16:19]
buffer_load_dwordx4 v5,s[44:47],s69 offen offset:0 lds
v_lshlrev_b32_sdwa v25,1,v25 dst_sel:DWORD dst_unused:UNUSED_PAD src0_sel:DWORD src1_sel:WORD_1
v_mfma_f32_16x16x32_bf16 v[60:63],a[196:199],a[64:67],v[60:63]
s_waitcnt lgkmcnt(8)
ds_read_u16 v24,v24
v_lshl_add_u64 v[40:41],v[52:53],0,v[54:55]
v_mfma_f32_16x16x32_bf16 v[60:63],a[192:195],a[48:51],v[60:63]
s_waitcnt lgkmcnt(3)
ds_read_u16_d16_hi v26,v26
v_or3_b32 v13,v14,v15,v13
v_lshlrev_b32_sdwa v27,1,v27 dst_sel:DWORD dst_unused:UNUSED_PAD src0_sel:DWORD src1_sel:WORD_1
v_mfma_f32_16x16x32_bf16 v[52:55],a[192:195],a[52:55],v[16:19]
s_add_u32 s69,s69,s53
ds_read_b64 v[14:15],v7 offset:6672
v_or_b32 v42,v56,v58
v_lshlrev_b32_sdwa v28,1,v28 dst_sel:DWORD dst_unused:UNUSED_PAD src0_sel:DWORD src1_sel:WORD_1
v_mfma_f32_16x16x32_bf16 v[52:55],a[196:199],a[68:71],v[52:55]
buffer_load_dwordx4 v5,s[44:47],s69 offen offset:1024 lds
v_cvt_pk_bf16_f32 v43,v57,v59
v_lshlrev_b32_sdwa v30,1,v30 dst_sel:DWORD dst_unused:UNUSED_PAD src0_sel:DWORD src1_sel:WORD_1
v_mfma_f32_16x16x32_bf16 v[52:55],a[200:203],a[84:87],v[52:55]
s_add_u32 s69,s69,s53
ds_read_u16 v25,v25
v_lshlrev_b32_sdwa v29,1,v29 dst_sel:DWORD dst_unused:UNUSED_PAD src0_sel:DWORD src1_sel:WORD_1
v_mfma_f32_16x16x32_bf16 v[56:59],a[212:215],a[84:87],v[20:23]
buffer_load_dwordx4 v5,s[44:47],s69 offen offset:2048 lds
ds_read_u16_d16_hi v27,v27
v_mfma_f32_16x16x32_bf16 v[56:59],a[208:211],a[68:71],v[56:59]
ds_read_u16 v28,v28
v_lshlrev_b32_sdwa v31,1,v31 dst_sel:DWORD dst_unused:UNUSED_PAD src0_sel:DWORD src1_sel:WORD_1
v_mfma_f32_16x16x32_bf16 v[56:59],a[204:207],a[52:55],v[56:59]
s_waitcnt lgkmcnt(3)
ds_read_u16_d16_hi v30,v30
v_or3_b32 v13,v14,v15,v13
v_mfma_f32_16x16x32_bf16 v[136:139],a[248:251],v[36:39],v[136:139]
ds_read_b64 v[14:15],v7 offset:7184
v_lshlrev_b32_sdwa v48,1,v48 dst_sel:DWORD dst_unused:UNUSED_PAD src0_sel:DWORD src1_sel:WORD_1
v_mfma_f32_16x16x32_bf16 v[140:143],a[248:251],v[40:43],v[140:143]
ds_read_u16 v29,v29
v_lshlrev_b32_sdwa v50,1,v50 dst_sel:DWORD dst_unused:UNUSED_PAD src0_sel:DWORD src1_sel:WORD_1
v_mfma_f32_16x16x32_bf16 v[68:71],a[252:255],v[40:43],v[68:71]
ds_read_u16_d16_hi v31,v31
v_lshlrev_b32_sdwa v49,1,v49 dst_sel:DWORD dst_unused:UNUSED_PAD src0_sel:DWORD src1_sel:WORD_1
v_mfma_f32_16x16x32_bf16 v[64:67],a[252:255],v[36:39],v[64:67]
ds_read_u16 v48,v48
v_lshlrev_b32_sdwa v51,1,v51 dst_sel:DWORD dst_unused:UNUSED_PAD src0_sel:DWORD src1_sel:WORD_1
v_mfma_f32_16x16x32_bf16 v[144:147],a[240:243],v[36:39],v[144:147]
ds_read_u16_d16_hi v50,v50
v_lshlrev_b32_sdwa v44,1,v44 dst_sel:DWORD dst_unused:UNUSED_PAD src0_sel:DWORD src1_sel:WORD_1
v_mfma_f32_16x16x32_bf16 v[148:151],a[240:243],v[40:43],v[148:151]
s_waitcnt lgkmcnt(4)
ds_read_u16 v49,v49
v_or3_b32 v13,v14,v15,v13
v_mfma_f32_16x16x32_bf16 v[84:87],a[224:227],v[40:43],v[84:87]
ds_read_u16_d16_hi v51,v51
v_lshlrev_b32_sdwa v46,1,v46 dst_sel:DWORD dst_unused:UNUSED_PAD src0_sel:DWORD src1_sel:WORD_1
v_mfma_f32_16x16x32_bf16 v[80:83],a[224:227],v[36:39],v[80:83]
ds_read_u16 v44,v44
v_lshlrev_b32_sdwa v45,1,v45 dst_sel:DWORD dst_unused:UNUSED_PAD src0_sel:DWORD src1_sel:WORD_1
v_mfma_f32_16x16x32_bf16 v[152:155],a[228:231],v[36:39],v[152:155]
ds_read_u16_d16_hi v46,v46
v_lshlrev_b32_sdwa v47,1,v47 dst_sel:DWORD dst_unused:UNUSED_PAD src0_sel:DWORD src1_sel:WORD_1
v_mfma_f32_16x16x32_bf16 v[156:159],a[228:231],v[40:43],v[156:159]
ds_read_u16 v45,v45
v_mfma_f32_16x16x32_bf16 v[100:103],a[232:235],v[40:43],v[100:103]
ds_read_u16_d16_hi v47,v47
v_mfma_f32_16x16x32_bf16 v[96:99],a[232:235],v[36:39],v[96:99]
v_mfma_f32_16x16x32_bf16 v[40:43],a[204:207],a[56:59],v[20:23]
v_lshlrev_b32_sdwa v32,1,v32 dst_sel:DWORD dst_unused:UNUSED_PAD src0_sel:DWORD src1_sel:WORD_1
v_fma_mixlo_f16 v8,v53,s99,0
v_mfma_f32_16x16x32_bf16 v[40:43],a[208:211],a[72:75],v[40:43]
ds_read_u16 v32,v32
v_lshl_add_u64 v[24:25],v[24:25],0,v[26:27]
v_mfma_f32_16x16x32_bf16 v[40:43],a[212:215],a[88:91],v[40:43]
s_waitcnt lgkmcnt(6)
v_lshl_add_u64 v[26:27],v[28:29],0,v[30:31]
v_lshlrev_b32_sdwa v34,1,v34 dst_sel:DWORD dst_unused:UNUSED_PAD src0_sel:DWORD src1_sel:WORD_1
v_mfma_f32_16x16x32_bf16 v[36:39],a[200:203],a[88:91],v[16:19]
ds_read_u16_d16_hi v34,v34
v_fma_mixhi_f16 v8,v55,s99,0
v_mfma_f32_16x16x32_bf16 v[36:39],a[196:199],a[72:75],v[36:39]
s_waitcnt lgkmcnt(5)
v_lshl_add_u64 v[28:29],v[48:49],0,v[50:51]
v_lshlrev_b32_sdwa v33,1,v33 dst_sel:DWORD dst_unused:UNUSED_PAD src0_sel:DWORD src1_sel:WORD_1
v_pk_fma_f16 v9,v8,v8,s54
v_mfma_f32_16x16x32_bf16 v[36:39],a[192:195],a[56:59],v[36:39]
ds_read_u16 v33,v33
v_lshlrev_b32_sdwa v35,1,v35 dst_sel:DWORD dst_unused:UNUSED_PAD src0_sel:DWORD src1_sel:WORD_1
v_mfma_f32_16x16x32_bf16 v[48:51],a[192:195],a[60:63],v[16:19]
ds_read_b128 v[16:19],v4 offset:1280
v_pk_mul_f16 v9,v8,v9
v_mfma_f32_16x16x32_bf16 v[48:51],a[196:199],a[76:79],v[48:51]
s_waitcnt lgkmcnt(4)
ds_read_u16_d16_hi v35,v35
ds_read_b64_tr_b16 a[192:193],v1 offset:10256
v_lshl_add_u64 v[30:31],v[44:45],0,v[46:47]
v_lshlrev_b32_sdwa v60,1,v60 dst_sel:DWORD dst_unused:UNUSED_PAD src0_sel:DWORD src1_sel:WORD_1
v_mfma_f32_16x16x32_bf16 v[48:51],a[200:203],a[92:95],v[48:51]
ds_read_b64_tr_b16 a[194:195],v1 offset:11280
v_exp_f16_e64 v8,v9
v_mfma_f32_16x16x32_bf16 v[44:47],a[212:215],a[92:95],v[20:23]
ds_read_b128 v[20:23],v4 offset:1312
ds_read_u16 v60,v60
v_lshlrev_b32_sdwa v62,1,v62 dst_sel:DWORD dst_unused:UNUSED_PAD src0_sel:DWORD src1_sel:WORD_1
v_mfma_f32_16x16x32_bf16 v[44:47],a[208:211],a[76:79],v[44:47]
ds_read_u16_d16_hi v62,v62
ds_read_b64_tr_b16 a[196:197],v2 offset:10256
v_lshlrev_b32_sdwa v61,1,v61 dst_sel:DWORD dst_unused:UNUSED_PAD src0_sel:DWORD src1_sel:WORD_1
v_mfma_f32_16x16x32_bf16 v[44:47],a[204:207],a[60:63],v[44:47]
ds_read_u16 v61,v61
v_exp_f16_sdwa v8,v9 dst_sel:WORD_1 dst_unused:UNUSED_PRESERVE src0_sel:WORD_1
v_mfma_f32_16x16x32_bf16 v[112:115],a[248:251],v[24:27],v[112:115]
ds_read_b64_tr_b16 a[198:199],v2 offset:11280
ds_read_b64_tr_b16 a[200:201],v2 offset:8208
v_lshlrev_b32_sdwa v63,1,v63 dst_sel:DWORD dst_unused:UNUSED_PAD src0_sel:DWORD src1_sel:WORD_1
v_mfma_f32_16x16x32_bf16 v[116:119],a[248:251],v[28:31],v[116:119]
ds_read_u16_d16_hi v63,v63
v_lshlrev_b32_sdwa v56,1,v56 dst_sel:DWORD dst_unused:UNUSED_PAD src0_sel:DWORD src1_sel:WORD_1
v_mfma_f32_16x16x32_bf16 v[76:79],a[252:255],v[28:31],v[76:79]
ds_read_b64_tr_b16 a[202:203],v2 offset:9232
v_pk_fma_f16 v8,v8,s55,s55
v_mfma_f32_16x16x32_bf16 v[72:75],a[252:255],v[24:27],v[72:75]
v_rcp_f16 v9,v8
v_lshlrev_b32_sdwa v58,1,v58 dst_sel:DWORD dst_unused:UNUSED_PAD src0_sel:DWORD src1_sel:WORD_1
v_mfma_f32_16x16x32_bf16 v[120:123],a[240:243],v[24:27],v[120:123]
ds_read_u16 v56,v56
v_lshlrev_b32_sdwa v57,1,v57 dst_sel:DWORD dst_unused:UNUSED_PAD src0_sel:DWORD src1_sel:WORD_1
v_mfma_f32_16x16x32_bf16 v[124:127],a[240:243],v[28:31],v[124:127]
ds_read_u16_d16_hi v58,v58
v_lshlrev_b32_sdwa v59,1,v59 dst_sel:DWORD dst_unused:UNUSED_PAD src0_sel:DWORD src1_sel:WORD_1
v_mfma_f32_16x16x32_bf16 v[92:95],a[224:227],v[28:31],v[92:95]
ds_read_u16 v57,v57
v_lshlrev_b32_sdwa v52,1,v52 dst_sel:DWORD dst_unused:UNUSED_PAD src0_sel:DWORD src1_sel:WORD_1
v_mfma_f32_16x16x32_bf16 v[88:91],a[224:227],v[24:27],v[88:91]
ds_read_u16_d16_hi v59,v59
v_rcp_f16_sdwa v9,v8 dst_sel:WORD_1 dst_unused:UNUSED_PRESERVE src0_sel:WORD_1
v_mfma_f32_16x16x32_bf16 v[128:131],a[228:231],v[24:27],v[128:131]
ds_read_b64_tr_b16 a[212:213],v2 offset:6160
v_lshlrev_b32_sdwa v54,1,v54 dst_sel:DWORD dst_unused:UNUSED_PAD src0_sel:DWORD src1_sel:WORD_1
v_mfma_f32_16x16x32_bf16 v[132:135],a[228:231],v[28:31],v[132:135]
ds_read_b64_tr_b16 a[214:215],v2 offset:7184
v_fma_mix_f32 v53,v53,v9,0 op_sel_hi:[0,1,0]
v_mfma_f32_16x16x32_bf16 v[108:111],a[232:235],v[28:31],v[108:111]
ds_read_u16 v52,v52
v_fma_mix_f32 v55,v55,v9,0 op_sel:[0,1,0] op_sel_hi:[0,1,0]
v_mfma_f32_16x16x32_bf16 v[104:107],a[232:235],v[24:27],v[104:107]
s_waitcnt lgkmcnt(7)
ds_read_u16_d16_hi v54,v54
v_mfma_f32_16x16x32_bf16 v[28:31],a[216:219],a[0:3],v[16:19]
v_lshlrev_b32_sdwa v40,1,v40 dst_sel:DWORD dst_unused:UNUSED_PAD src0_sel:DWORD src1_sel:WORD_1
v_lshlrev_b32_sdwa v42,1,v42 dst_sel:DWORD dst_unused:UNUSED_PAD src0_sel:DWORD src1_sel:WORD_1
v_mfma_f32_16x16x32_bf16 v[28:31],a[220:223],a[16:19],v[28:31]
ds_read_u16 v40,v40
v_lshl_add_u64 v[32:33],v[32:33],0,v[34:35]
v_mfma_f32_16x16x32_bf16 v[28:31],a[192:195],a[32:35],v[28:31]
ds_read_u16_d16_hi v42,v42
v_lshl_add_u64 v[34:35],v[60:61],0,v[62:63]
v_mfma_f32_16x16x32_bf16 v[24:27],a[196:199],a[32:35],v[20:23]
v_lshlrev_b32_sdwa v41,1,v41 dst_sel:DWORD dst_unused:UNUSED_PAD src0_sel:DWORD src1_sel:WORD_1
v_lshlrev_b32_sdwa v43,1,v43 dst_sel:DWORD dst_unused:UNUSED_PAD src0_sel:DWORD src1_sel:WORD_1
v_mfma_f32_16x16x32_bf16 v[24:27],a[200:203],a[16:19],v[24:27]
s_waitcnt lgkmcnt(4)
ds_read_u16 v41,v41
v_lshl_add_u64 v[60:61],v[56:57],0,v[58:59]
v_mfma_f32_16x16x32_bf16 v[24:27],a[212:215],a[0:3],v[24:27]
ds_read_u16_d16_hi v43,v43
v_lshlrev_b32_sdwa v36,1,v36 dst_sel:DWORD dst_unused:UNUSED_PAD src0_sel:DWORD src1_sel:WORD_1
v_mfma_f32_16x16x32_bf16 v[56:59],a[212:215],a[4:7],v[20:23]
s_waitcnt lgkmcnt(4)
ds_read_u16 v36,v36
v_or_b32 v62,v52,v54
v_mfma_f32_16x16x32_bf16 v[56:59],a[200:203],a[20:23],v[56:59]
v_cvt_pk_bf16_f32 v63,v53,v55
v_lshlrev_b32_sdwa v38,1,v38 dst_sel:DWORD dst_unused:UNUSED_PAD src0_sel:DWORD src1_sel:WORD_1
v_mfma_f32_16x16x32_bf16 v[56:59],a[196:199],a[36:39],v[56:59]
ds_read_u16_d16_hi v38,v38
v_lshlrev_b32_sdwa v37,1,v37 dst_sel:DWORD dst_unused:UNUSED_PAD src0_sel:DWORD src1_sel:WORD_1
v_mfma_f32_16x16x32_bf16 v[52:55],a[192:195],a[36:39],v[16:19]
ds_read_u16 v37,v37
v_lshlrev_b32_sdwa v39,1,v39 dst_sel:DWORD dst_unused:UNUSED_PAD src0_sel:DWORD src1_sel:WORD_1
v_mfma_f32_16x16x32_bf16 v[52:55],a[220:223],a[20:23],v[52:55]
ds_read_u16_d16_hi v39,v39
v_lshlrev_b32_sdwa v44,1,v44 dst_sel:DWORD dst_unused:UNUSED_PAD src0_sel:DWORD src1_sel:WORD_1
v_mfma_f32_16x16x32_bf16 v[52:55],a[216:219],a[4:7],v[52:55]
ds_read_u16 v44,v44
v_lshlrev_b32_sdwa v46,1,v46 dst_sel:DWORD dst_unused:UNUSED_PAD src0_sel:DWORD src1_sel:WORD_1
v_mfma_f32_16x16x32_bf16 v[232:235],a[248:251],v[32:35],v[232:235]
ds_read_u16_d16_hi v46,v46
v_lshlrev_b32_sdwa v45,1,v45 dst_sel:DWORD dst_unused:UNUSED_PAD src0_sel:DWORD src1_sel:WORD_1
v_mfma_f32_16x16x32_bf16 v[236:239],a[248:251],v[60:63],v[236:239]
ds_read_u16 v45,v45
v_lshlrev_b32_sdwa v47,1,v47 dst_sel:DWORD dst_unused:UNUSED_PAD src0_sel:DWORD src1_sel:WORD_1
v_mfma_f32_16x16x32_bf16 v[164:167],a[252:255],v[60:63],v[164:167]
ds_read_u16_d16_hi v47,v47
ds_read_b64_tr_b16 a[236:237],v3 offset:12320
v_mfma_f32_16x16x32_bf16 v[160:163],a[252:255],v[32:35],v[160:163]
ds_read_b64_tr_b16 a[238:239],v3 offset:13856
v_lshlrev_b32_sdwa v48,1,v48 dst_sel:DWORD dst_unused:UNUSED_PAD src0_sel:DWORD src1_sel:WORD_1
v_mfma_f32_16x16x32_bf16 v[240:243],a[240:243],v[32:35],v[240:243]
ds_read_u16 v48,v48
v_lshlrev_b32_sdwa v50,1,v50 dst_sel:DWORD dst_unused:UNUSED_PAD src0_sel:DWORD src1_sel:WORD_1
v_mfma_f32_16x16x32_bf16 v[244:247],a[240:243],v[60:63],v[244:247]
ds_read_u16_d16_hi v50,v50
v_lshlrev_b32_sdwa v49,1,v49 dst_sel:DWORD dst_unused:UNUSED_PAD src0_sel:DWORD src1_sel:WORD_1
v_mfma_f32_16x16x32_bf16 v[180:183],a[224:227],v[60:63],v[180:183]
ds_read_u16 v49,v49
v_lshlrev_b32_sdwa v51,1,v51 dst_sel:DWORD dst_unused:UNUSED_PAD src0_sel:DWORD src1_sel:WORD_1
v_mfma_f32_16x16x32_bf16 v[176:179],a[224:227],v[32:35],v[176:179]
ds_read_u16_d16_hi v51,v51
v_mfma_f32_16x16x32_bf16 v[248:251],a[228:231],v[32:35],v[248:251]
ds_read_b64_tr_b16 a[244:245],v3 offset:12328
v_mfma_f32_16x16x32_bf16 v[252:255],a[228:231],v[60:63],v[252:255]
ds_read_b64_tr_b16 a[246:247],v3 offset:13864
v_mfma_f32_16x16x32_bf16 v[196:199],a[232:235],v[60:63],v[196:199]
v_mfma_f32_16x16x32_bf16 v[192:195],a[232:235],v[32:35],v[192:195]
v_mfma_f32_16x16x32_bf16 v[60:63],a[216:219],a[8:11],v[16:19]
ds_read_b64_tr_b16 a[208:209],v1 offset:12320
v_lshlrev_b32_sdwa v28,1,v28 dst_sel:DWORD dst_unused:UNUSED_PAD src0_sel:DWORD src1_sel:WORD_1
v_mfma_f32_16x16x32_bf16 v[60:63],a[220:223],a[24:27],v[60:63]
s_waitcnt lgkmcnt(6)
ds_read_u16 v28,v28
v_lshl_add_u64 v[40:41],v[40:41],0,v[42:43]
v_fma_mixlo_f16 v8,v57,s99,0
v_mfma_f32_16x16x32_bf16 v[60:63],a[192:195],a[40:43],v[60:63]
ds_read_b64_tr_b16 a[210:211],v1 offset:13344
v_lshl_add_u64 v[42:43],v[36:37],0,v[38:39]
v_lshlrev_b32_sdwa v30,1,v30 dst_sel:DWORD dst_unused:UNUSED_PAD src0_sel:DWORD src1_sel:WORD_1
v_mfma_f32_16x16x32_bf16 v[32:35],a[196:199],a[40:43],v[20:23]
ds_read_u16_d16_hi v30,v30
v_fma_mixhi_f16 v8,v59,s99,0
v_mfma_f32_16x16x32_bf16 v[32:35],a[200:203],a[24:27],v[32:35]
ds_read_b64_tr_b16 a[204:205],v1 offset:14368
v_lshl_add_u64 v[36:37],v[44:45],0,v[46:47]
v_lshlrev_b32_sdwa v29,1,v29 dst_sel:DWORD dst_unused:UNUSED_PAD src0_sel:DWORD src1_sel:WORD_1
v_pk_fma_f16 v9,v8,v8,s54
v_mfma_f32_16x16x32_bf16 v[32:35],a[212:215],a[8:11],v[32:35]
ds_read_u16 v29,v29
v_lshlrev_b32_sdwa v31,1,v31 dst_sel:DWORD dst_unused:UNUSED_PAD src0_sel:DWORD src1_sel:WORD_1
v_mfma_f32_16x16x32_bf16 v[44:47],a[212:215],a[12:15],v[20:23]
ds_read_u16_d16_hi v31,v31
v_pk_mul_f16 v9,v8,v9
v_mfma_f32_16x16x32_bf16 v[44:47],a[200:203],a[28:31],v[44:47]
s_waitcnt lgkmcnt(7)
ds_read_b64_tr_b16 a[206:207],v1 offset:15392
v_lshl_add_u64 v[38:39],v[48:49],0,v[50:51]
v_lshlrev_b32_sdwa v24,1,v24 dst_sel:DWORD dst_unused:UNUSED_PAD src0_sel:DWORD src1_sel:WORD_1
v_mfma_f32_16x16x32_bf16 v[44:47],a[196:199],a[44:47],v[44:47]
v_exp_f16 v8,v9
v_mfma_f32_16x16x32_bf16 v[48:51],a[192:195],a[44:47],v[16:19]
ds_read_u16 v24,v24
v_lshlrev_b32_sdwa v26,1,v26 dst_sel:DWORD dst_unused:UNUSED_PAD src0_sel:DWORD src1_sel:WORD_1
v_mfma_f32_16x16x32_bf16 v[48:51],a[220:223],a[28:31],v[48:51]
ds_read_u16_d16_hi v26,v26
v_lshlrev_b32_sdwa v25,1,v25 dst_sel:DWORD dst_unused:UNUSED_PAD src0_sel:DWORD src1_sel:WORD_1
v_mfma_f32_16x16x32_bf16 v[48:51],a[216:219],a[12:15],v[48:51]
v_exp_f16_sdwa v8,v9 dst_sel:WORD_1 dst_unused:UNUSED_PRESERVE src0_sel:WORD_1
v_mfma_f32_16x16x32_bf16 v[208:211],a[248:251],v[40:43],v[208:211]
ds_read_u16 v25,v25
v_lshlrev_b32_sdwa v27,1,v27 dst_sel:DWORD dst_unused:UNUSED_PAD src0_sel:DWORD src1_sel:WORD_1
v_mfma_f32_16x16x32_bf16 v[212:215],a[248:251],v[36:39],v[212:215]
TP4345_%=:
s_cbranch_execz TP6487_%=
ds_read_u16_d16_hi v27,v27
v_lshlrev_b32_sdwa v52,1,v52 dst_sel:DWORD dst_unused:UNUSED_PAD src0_sel:DWORD src1_sel:WORD_1
v_mfma_f32_16x16x32_bf16 v[172:175],a[252:255],v[36:39],v[172:175]
ds_read_u16 v52,v52
v_pk_fma_f16 v8,v8,s55,s55
v_mfma_f32_16x16x32_bf16 v[168:171],a[252:255],v[40:43],v[168:171]
ds_read_b64_tr_b16 a[248:249],v3 offset:12384
v_rcp_f16 v9,v8
v_lshlrev_b32_sdwa v54,1,v54 dst_sel:DWORD dst_unused:UNUSED_PAD src0_sel:DWORD src1_sel:WORD_1
v_mfma_f32_16x16x32_bf16 v[216:219],a[240:243],v[40:43],v[216:219]
ds_read_u16_d16_hi v54,v54
v_lshlrev_b32_sdwa v53,1,v53 dst_sel:DWORD dst_unused:UNUSED_PAD src0_sel:DWORD src1_sel:WORD_1
v_mfma_f32_16x16x32_bf16 v[220:223],a[240:243],v[36:39],v[220:223]
ds_read_u16 v53,v53
ds_read_b64_tr_b16 a[250:251],v3 offset:13920
v_mfma_f32_16x16x32_bf16 v[188:191],a[224:227],v[36:39],v[188:191]
ds_read_b64_tr_b16 a[252:253],v3 offset:12392
v_lshlrev_b32_sdwa v55,1,v55 dst_sel:DWORD dst_unused:UNUSED_PAD src0_sel:DWORD src1_sel:WORD_1
v_mfma_f32_16x16x32_bf16 v[184:187],a[224:227],v[40:43],v[184:187]
ds_read_u16_d16_hi v55,v55
v_rcp_f16_sdwa v9,v8 dst_sel:WORD_1 dst_unused:UNUSED_PRESERVE src0_sel:WORD_1
v_mfma_f32_16x16x32_bf16 v[224:227],a[228:231],v[40:43],v[224:227]
ds_read_b64_tr_b16 a[254:255],v3 offset:13928
ds_read_b64_tr_b16 a[240:241],v3 offset:12448
v_lshlrev_b32_sdwa v56,1,v56 dst_sel:DWORD dst_unused:UNUSED_PAD src0_sel:DWORD src1_sel:WORD_1
v_mfma_f32_16x16x32_bf16 v[228:231],a[228:231],v[36:39],v[228:231]
ds_read_u16 v56,v56
v_lshlrev_b32_sdwa v58,1,v58 dst_sel:DWORD dst_unused:UNUSED_PAD src0_sel:DWORD src1_sel:WORD_1
v_mfma_f32_16x16x32_bf16 v[204:207],a[232:235],v[36:39],v[204:207]
ds_read_u16_d16_hi v58,v58
v_fma_mix_f32 v57,v57,v9,0 op_sel_hi:[0,1,0]
v_mfma_f32_16x16x32_bf16 v[200:203],a[232:235],v[40:43],v[200:203]
ds_read_b64_tr_b16 a[242:243],v3 offset:13984
v_fma_mix_f32 v59,v59,v9,0 op_sel:[0,1,0] op_sel_hi:[0,1,0]
v_mbcnt_lo_u32_b32 v8,-1,0
N_qs10_%=:
s_add_u32 s64,s16,N_qs10_%=-N_pf_%=+0
v_and_b32_e64 v8,3,v8
s_mov_b32 m0,12288
v_lshl_add_u32 v8,v8,7,s64
v_min_u32 v8,N_mainend_%=-N_pf_%=+2048,v8
global_load_lds_dword v8,s[14:15]
v_mfma_f32_16x16x32_bf16 v[36:39],a[216:219],a[48:51],v[16:19]
ds_read_b64 v[14:15],v7 offset:12320
v_lshlrev_b32_sdwa v60,1,v60 dst_sel:DWORD dst_unused:UNUSED_PAD src0_sel:DWORD src1_sel:WORD_1
v_mfma_f32_16x16x32_bf16 v[36:39],a[220:223],a[64:67],v[36:39]
s_waitcnt lgkmcnt(10)
ds_read_b64_tr_b16 a[224:225],v3 offset:12456
v_lshl_add_u64 v[28:29],v[28:29],0,v[30:31]
v_mfma_f32_16x16x32_bf16 v[36:39],a[192:195],a[80:83],v[36:39]
ds_read_b64_tr_b16 a[226:227],v3 offset:13992
v_lshl_add_u64 v[30:31],v[24:25],0,v[26:27]
v_lshlrev_b32_sdwa v62,1,v62 dst_sel:DWORD dst_unused:UNUSED_PAD src0_sel:DWORD src1_sel:WORD_1
v_mfma_f32_16x16x32_bf16 v[40:43],a[196:199],a[80:83],v[20:23]
ds_read_u16 v60,v60
v_lshlrev_b32_sdwa v61,1,v61 dst_sel:DWORD dst_unused:UNUSED_PAD src0_sel:DWORD src1_sel:WORD_1
v_mfma_f32_16x16x32_bf16 v[40:43],a[200:203],a[64:67],v[40:43]
s_waitcnt lgkmcnt(9)
ds_read_u16_d16_hi v62,v62
v_lshl_add_u64 v[24:25],v[52:53],0,v[54:55]
v_mfma_f32_16x16x32_bf16 v[40:43],a[212:215],a[48:51],v[40:43]
s_waitcnt lgkmcnt(4)
ds_read_u16 v61,v61
v_or3_b32 v13,v14,v15,v13
v_lshlrev_b32_sdwa v63,1,v63 dst_sel:DWORD dst_unused:UNUSED_PAD src0_sel:DWORD src1_sel:WORD_1
v_mfma_f32_16x16x32_bf16 v[52:55],a[212:215],a[52:55],v[20:23]
ds_read_b64 v[14:15],v7 offset:12832
v_or_b32 v26,v56,v58
v_lshlrev_b32_sdwa v32,1,v32 dst_sel:DWORD dst_unused:UNUSED_PAD src0_sel:DWORD src1_sel:WORD_1
v_mfma_f32_16x16x32_bf16 v[52:55],a[200:203],a[68:71],v[52:55]
ds_read_u16_d16_hi v63,v63
v_cvt_pk_bf16_f32 v27,v57,v59
v_lshlrev_b32_sdwa v34,1,v34 dst_sel:DWORD dst_unused:UNUSED_PAD src0_sel:DWORD src1_sel:WORD_1
v_mfma_f32_16x16x32_bf16 v[52:55],a[196:199],a[84:87],v[52:55]
ds_read_u16 v32,v32
v_lshlrev_b32_sdwa v33,1,v33 dst_sel:DWORD dst_unused:UNUSED_PAD src0_sel:DWORD src1_sel:WORD_1
v_mfma_f32_16x16x32_bf16 v[56:59],a[192:195],a[84:87],v[16:19]
ds_read_u16_d16_hi v34,v34
v_lshlrev_b32_sdwa v35,1,v35 dst_sel:DWORD dst_unused:UNUSED_PAD src0_sel:DWORD src1_sel:WORD_1
v_mfma_f32_16x16x32_bf16 v[56:59],a[220:223],a[68:71],v[56:59]
ds_read_u16 v33,v33
v_lshlrev_b32_sdwa v48,1,v48 dst_sel:DWORD dst_unused:UNUSED_PAD src0_sel:DWORD src1_sel:WORD_1
v_mfma_f32_16x16x32_bf16 v[56:59],a[216:219],a[52:55],v[56:59]
s_waitcnt lgkmcnt(4)
ds_read_u16_d16_hi v35,v35
v_or3_b32 v13,v14,v15,v13
v_mfma_f32_16x16x32_bf16 v[136:139],a[236:239],v[28:31],v[136:139]
ds_read_b64 v[14:15],v7 offset:13344
v_lshlrev_b32_sdwa v50,1,v50 dst_sel:DWORD dst_unused:UNUSED_PAD src0_sel:DWORD src1_sel:WORD_1
v_mfma_f32_16x16x32_bf16 v[140:143],a[236:239],v[24:27],v[140:143]
ds_read_u16 v48,v48
v_lshlrev_b32_sdwa v49,1,v49 dst_sel:DWORD dst_unused:UNUSED_PAD src0_sel:DWORD src1_sel:WORD_1
v_mfma_f32_16x16x32_bf16 v[68:71],a[244:247],v[24:27],v[68:71]
ds_read_u16_d16_hi v50,v50
v_lshlrev_b32_sdwa v51,1,v51 dst_sel:DWORD dst_unused:UNUSED_PAD src0_sel:DWORD src1_sel:WORD_1
v_mfma_f32_16x16x32_bf16 v[64:67],a[244:247],v[28:31],v[64:67]
ds_read_u16 v49,v49
v_lshlrev_b32_sdwa v44,1,v44 dst_sel:DWORD dst_unused:UNUSED_PAD src0_sel:DWORD src1_sel:WORD_1
v_mfma_f32_16x16x32_bf16 v[144:147],a[248:251],v[28:31],v[144:147]
ds_read_u16_d16_hi v51,v51
v_lshlrev_b32_sdwa v46,1,v46 dst_sel:DWORD dst_unused:UNUSED_PAD src0_sel:DWORD src1_sel:WORD_1
v_mfma_f32_16x16x32_bf16 v[148:151],a[248:251],v[24:27],v[148:151]
s_waitcnt lgkmcnt(4)
ds_read_u16 v44,v44
v_or3_b32 v13,v14,v15,v13
v_mfma_f32_16x16x32_bf16 v[84:87],a[252:255],v[24:27],v[84:87]
ds_read_u16_d16_hi v46,v46
v_and_b32 v13,0x40004000,v13
v_mfma_f32_16x16x32_bf16 v[80:83],a[252:255],v[28:31],v[80:83]
v_cmp_ne_u32_e32 vcc,0,v13
s_nop 0
s_or_b64 s[90:91],s[90:91],vcc
v_lshlrev_b32_sdwa v45,1,v45 dst_sel:DWORD dst_unused:UNUSED_PAD src0_sel:DWORD src1_sel:WORD_1
v_mfma_f32_16x16x32_bf16 v[152:155],a[240:243],v[28:31],v[152:155]
ds_read_u16 v45,v45
v_mbcnt_lo_u32_b32 v15,-1,0
v_mfma_f32_16x16x32_bf16 v[156:159],a[240:243],v[24:27],v[156:159]
v_mbcnt_hi_u32_b32 v15,-1,v15
v_lshlrev_b32_sdwa v47,1,v47 dst_sel:DWORD dst_unused:UNUSED_PAD src0_sel:DWORD src1_sel:WORD_1
v_mfma_f32_16x16x32_bf16 v[100:103],a[224:227],v[24:27],v[100:103]
ds_read_u16_d16_hi v47,v47
v_lshlrev_b32 v15,2,v15
v_mfma_f32_16x16x32_bf16 v[96:99],a[224:227],v[28:31],v[96:99]
ds_read_b32 v7,v15 offset:14848
ds_read_b32 v13,v15 offset:15104
v_mfma_f32_16x16x32_bf16 v[24:27],a[216:219],a[56:59],v[16:19]
ds_read_b32 v14,v15 offset:14336
ds_read_b32 v15,v15 offset:14592
v_lshlrev_b32_sdwa v36,1,v36 dst_sel:DWORD dst_unused:UNUSED_PAD src0_sel:DWORD src1_sel:WORD_1
v_mfma_f32_16x16x32_bf16 v[24:27],a[220:223],a[72:75],v[24:27]
ds_read_u16 v36,v36
v_lshl_add_u64 v[60:61],v[60:61],0,v[62:63]
v_fma_mixlo_f16 v8,v53,s99,0
v_mfma_f32_16x16x32_bf16 v[24:27],a[192:195],a[88:91],v[24:27]
v_lshl_add_u64 v[62:63],v[32:33],0,v[34:35]
v_lshlrev_b32_sdwa v38,1,v38 dst_sel:DWORD dst_unused:UNUSED_PAD src0_sel:DWORD src1_sel:WORD_1
v_mfma_f32_16x16x32_bf16 v[28:31],a[196:199],a[88:91],v[20:23]
ds_read_u16_d16_hi v38,v38
v_fma_mixhi_f16 v8,v55,s99,0
v_mfma_f32_16x16x32_bf16 v[28:31],a[200:203],a[72:75],v[28:31]
s_waitcnt lgkmcnt(8)
v_lshl_add_u64 v[32:33],v[48:49],0,v[50:51]
v_lshlrev_b32_sdwa v37,1,v37 dst_sel:DWORD dst_unused:UNUSED_PAD src0_sel:DWORD src1_sel:WORD_1
v_pk_fma_f16 v9,v8,v8,s54
v_mfma_f32_16x16x32_bf16 v[28:31],a[212:215],a[56:59],v[28:31]
ds_read_u16 v37,v37
v_lshlrev_b32_sdwa v39,1,v39 dst_sel:DWORD dst_unused:UNUSED_PAD src0_sel:DWORD src1_sel:WORD_1
v_mfma_f32_16x16x32_bf16 v[48:51],a[212:215],a[60:63],v[20:23]
ds_read_b128 v[20:23],v4 offset:1408
v_pk_mul_f16 v9,v8,v9
v_mfma_f32_16x16x32_bf16 v[48:51],a[200:203],a[76:79],v[48:51]
s_waitcnt lgkmcnt(8)
ds_read_u16_d16_hi v39,v39
ds_read_b64_tr_b16 a[212:213],v1 offset:16416
v_lshl_add_u64 v[34:35],v[44:45],0,v[46:47]
v_lshlrev_b32_sdwa v40,1,v40 dst_sel:DWORD dst_unused:UNUSED_PAD src0_sel:DWORD src1_sel:WORD_1
v_mfma_f32_16x16x32_bf16 v[48:51],a[196:199],a[92:95],v[48:51]
ds_read_b64_tr_b16 a[214:215],v1 offset:17440
v_exp_f16 v8,v9
v_mfma_f32_16x16x32_bf16 v[44:47],a[192:195],a[92:95],v[16:19]
ds_read_b128 v[16:19],v4 offset:1440
ds_read_u16 v40,v40
v_lshlrev_b32_sdwa v42,1,v42 dst_sel:DWORD dst_unused:UNUSED_PAD src0_sel:DWORD src1_sel:WORD_1
v_mfma_f32_16x16x32_bf16 v[44:47],a[220:223],a[76:79],v[44:47]
ds_read_u16_d16_hi v42,v42
ds_read_b64_tr_b16 a[200:201],v2 offset:16416
v_lshlrev_b32_sdwa v41,1,v41 dst_sel:DWORD dst_unused:UNUSED_PAD src0_sel:DWORD src1_sel:WORD_1
v_mfma_f32_16x16x32_bf16 v[44:47],a[216:219],a[60:63],v[44:47]
ds_read_u16 v41,v41
v_exp_f16_sdwa v8,v9 dst_sel:WORD_1 dst_unused:UNUSED_PRESERVE src0_sel:WORD_1
v_mfma_f32_16x16x32_bf16 v[112:115],a[236:239],v[60:63],v[112:115]
ds_read_b64_tr_b16 a[202:203],v2 offset:17440
ds_read_b64_tr_b16 a[196:197],v2 offset:14368
v_lshlrev_b32_sdwa v43,1,v43 dst_sel:DWORD dst_unused:UNUSED_PAD src0_sel:DWORD src1_sel:WORD_1
v_mfma_f32_16x16x32_bf16 v[116:119],a[236:239],v[32:35],v[116:119]
ds_read_u16_d16_hi v43,v43
v_lshlrev_b32_sdwa v56,1,v56 dst_sel:DWORD dst_unused:UNUSED_PAD src0_sel:DWORD src1_sel:WORD_1
v_mfma_f32_16x16x32_bf16 v[76:79],a[244:247],v[32:35],v[76:79]
ds_read_b64_tr_b16 a[198:199],v2 offset:15392
v_pk_fma_f16 v8,v8,s55,s55
v_mfma_f32_16x16x32_bf16 v[72:75],a[244:247],v[60:63],v[72:75]
v_rcp_f16 v9,v8
v_lshlrev_b32_sdwa v58,1,v58 dst_sel:DWORD dst_unused:UNUSED_PAD src0_sel:DWORD src1_sel:WORD_1
v_mfma_f32_16x16x32_bf16 v[120:123],a[248:251],v[60:63],v[120:123]
ds_read_u16 v56,v56
v_lshlrev_b32_sdwa v57,1,v57 dst_sel:DWORD dst_unused:UNUSED_PAD src0_sel:DWORD src1_sel:WORD_1
v_mfma_f32_16x16x32_bf16 v[124:127],a[248:251],v[32:35],v[124:127]
ds_read_u16_d16_hi v58,v58
v_lshlrev_b32_sdwa v59,1,v59 dst_sel:DWORD dst_unused:UNUSED_PAD src0_sel:DWORD src1_sel:WORD_1
v_mfma_f32_16x16x32_bf16 v[92:95],a[252:255],v[32:35],v[92:95]
ds_read_u16 v57,v57
v_lshlrev_b32_sdwa v52,1,v52 dst_sel:DWORD dst_unused:UNUSED_PAD src0_sel:DWORD src1_sel:WORD_1
v_mfma_f32_16x16x32_bf16 v[88:91],a[252:255],v[60:63],v[88:91]
ds_read_u16_d16_hi v59,v59
v_rcp_f16_sdwa v9,v8 dst_sel:WORD_1 dst_unused:UNUSED_PRESERVE src0_sel:WORD_1
v_mfma_f32_16x16x32_bf16 v[128:131],a[240:243],v[60:63],v[128:131]
ds_read_b64_tr_b16 a[192:193],v2 offset:12320
v_lshlrev_b32_sdwa v54,1,v54 dst_sel:DWORD dst_unused:UNUSED_PAD src0_sel:DWORD src1_sel:WORD_1
v_mfma_f32_16x16x32_bf16 v[132:135],a[240:243],v[32:35],v[132:135]
ds_read_b64_tr_b16 a[194:195],v2 offset:13344
v_fma_mix_f32 v53,v53,v9,0 op_sel_hi:[0,1,0]
v_mfma_f32_16x16x32_bf16 v[108:111],a[224:227],v[32:35],v[108:111]
ds_read_u16 v52,v52
v_fma_mix_f32 v55,v55,v9,0 op_sel:[0,1,0] op_sel_hi:[0,1,0]
v_mfma_f32_16x16x32_bf16 v[104:107],a[224:227],v[60:63],v[104:107]
s_waitcnt lgkmcnt(7)
ds_read_u16_d16_hi v54,v54
v_mfma_f32_16x16x32_bf16 v[32:35],a[208:211],a[0:3],v[20:23]
v_lshlrev_b32_sdwa v24,1,v24 dst_sel:DWORD dst_unused:UNUSED_PAD src0_sel:DWORD src1_sel:WORD_1
v_lshlrev_b32_sdwa v26,1,v26 dst_sel:DWORD dst_unused:UNUSED_PAD src0_sel:DWORD src1_sel:WORD_1
v_mfma_f32_16x16x32_bf16 v[32:35],a[204:207],a[16:19],v[32:35]
ds_read_u16 v24,v24
v_lshl_add_u64 v[36:37],v[36:37],0,v[38:39]
v_mfma_f32_16x16x32_bf16 v[32:35],a[212:215],a[32:35],v[32:35]
ds_read_u16_d16_hi v26,v26
v_lshl_add_u64 v[38:39],v[40:41],0,v[42:43]
v_mfma_f32_16x16x32_bf16 v[60:63],a[200:203],a[32:35],v[16:19]
v_lshlrev_b32_sdwa v25,1,v25 dst_sel:DWORD dst_unused:UNUSED_PAD src0_sel:DWORD src1_sel:WORD_1
v_lshlrev_b32_sdwa v27,1,v27 dst_sel:DWORD dst_unused:UNUSED_PAD src0_sel:DWORD src1_sel:WORD_1
v_mfma_f32_16x16x32_bf16 v[60:63],a[196:199],a[16:19],v[60:63]
s_waitcnt lgkmcnt(4)
ds_read_u16 v25,v25
v_lshl_add_u64 v[40:41],v[56:57],0,v[58:59]
v_mfma_f32_16x16x32_bf16 v[60:63],a[192:195],a[0:3],v[60:63]
ds_read_u16_d16_hi v27,v27
v_lshlrev_b32_sdwa v28,1,v28 dst_sel:DWORD dst_unused:UNUSED_PAD src0_sel:DWORD src1_sel:WORD_1
v_mfma_f32_16x16x32_bf16 v[56:59],a[192:195],a[4:7],v[16:19]
s_waitcnt lgkmcnt(4)
ds_read_u16 v28,v28
v_or_b32 v42,v52,v54
v_mfma_f32_16x16x32_bf16 v[56:59],a[196:199],a[20:23],v[56:59]
s_waitcnt vmcnt(4)
v_cvt_pk_bf16_f32 v43,v53,v55
v_lshlrev_b32_sdwa v30,1,v30 dst_sel:DWORD dst_unused:UNUSED_PAD src0_sel:DWORD src1_sel:WORD_1
v_mfma_f32_16x16x32_bf16 v[56:59],a[200:203],a[36:39],v[56:59]
s_barrier
ds_read_u16_d16_hi v30,v30
v_lshlrev_b32_sdwa v29,1,v29 dst_sel:DWORD dst_unused:UNUSED_PAD src0_sel:DWORD src1_sel:WORD_1
v_mfma_f32_16x16x32_bf16 v[52:55],a[212:215],a[36:39],v[20:23]
ds_read_u16 v29,v29
v_lshlrev_b32_sdwa v31,1,v31 dst_sel:DWORD dst_unused:UNUSED_PAD src0_sel:DWORD src1_sel:WORD_1
v_mfma_f32_16x16x32_bf16 v[52:55],a[204:207],a[20:23],v[52:55]
s_add_u32 m0,s58,6160
ds_read_u16_d16_hi v31,v31
v_lshlrev_b32_sdwa v44,1,v44 dst_sel:DWORD dst_unused:UNUSED_PAD src0_sel:DWORD src1_sel:WORD_1
v_mfma_f32_16x16x32_bf16 v[52:55],a[208:211],a[4:7],v[52:55]
s_add_u32 s69,s69,s59
ds_read_u16 v44,v44
v_lshlrev_b32_sdwa v46,1,v46 dst_sel:DWORD dst_unused:UNUSED_PAD src0_sel:DWORD src1_sel:WORD_1
v_mfma_f32_16x16x32_bf16 v[232:235],a[236:239],v[36:39],v[232:235]
buffer_load_dwordx4 v5,s[44:47],s69 offen offset:0 lds
v_lshlrev_b32_sdwa v45,1,v45 dst_sel:DWORD dst_unused:UNUSED_PAD src0_sel:DWORD src1_sel:WORD_1
v_mfma_f32_16x16x32_bf16 v[236:239],a[236:239],v[40:43],v[236:239]
ds_read_u16_d16_hi v46,v46
v_lshlrev_b32_sdwa v47,1,v47 dst_sel:DWORD dst_unused:UNUSED_PAD src0_sel:DWORD src1_sel:WORD_1
v_mfma_f32_16x16x32_bf16 v[164:167],a[244:247],v[40:43],v[164:167]
ds_read_u16 v45,v45
ds_read_u16_d16_hi v47,v47
v_mfma_f32_16x16x32_bf16 v[160:163],a[244:247],v[36:39],v[160:163]
ds_read_b64_tr_b16 a[228:229],v3 offset:18480
v_lshlrev_b32_sdwa v48,1,v48 dst_sel:DWORD dst_unused:UNUSED_PAD src0_sel:DWORD src1_sel:WORD_1
v_mfma_f32_16x16x32_bf16 v[240:243],a[248:251],v[36:39],v[240:243]
s_add_u32 s69,s69,s53
ds_read_u16 v48,v48
v_lshlrev_b32_sdwa v50,1,v50 dst_sel:DWORD dst_unused:UNUSED_PAD src0_sel:DWORD src1_sel:WORD_1
v_mfma_f32_16x16x32_bf16 v[244:247],a[248:251],v[40:43],v[244:247]
buffer_load_dwordx4 v5,s[44:47],s69 offen offset:1024 lds
ds_read_u16_d16_hi v50,v50
v_mfma_f32_16x16x32_bf16 v[180:183],a[252:255],v[40:43],v[180:183]
ds_read_b64_tr_b16 a[230:231],v3 offset:20016
v_lshlrev_b32_sdwa v49,1,v49 dst_sel:DWORD dst_unused:UNUSED_PAD src0_sel:DWORD src1_sel:WORD_1
v_mfma_f32_16x16x32_bf16 v[176:179],a[252:255],v[36:39],v[176:179]
ds_read_u16 v49,v49
v_lshlrev_b32_sdwa v51,1,v51 dst_sel:DWORD dst_unused:UNUSED_PAD src0_sel:DWORD src1_sel:WORD_1
v_mfma_f32_16x16x32_bf16 v[248:251],a[240:243],v[36:39],v[248:251]
ds_read_u16_d16_hi v51,v51
v_mfma_f32_16x16x32_bf16 v[252:255],a[240:243],v[40:43],v[252:255]
s_add_u32 s69,s69,s53
ds_read_b64_tr_b16 a[232:233],v3 offset:18488
v_mfma_f32_16x16x32_bf16 v[196:199],a[224:227],v[40:43],v[196:199]
buffer_load_dwordx4 v5,s[44:47],s69 offen offset:2048 lds
v_mfma_f32_16x16x32_bf16 v[192:195],a[224:227],v[36:39],v[192:195]
ds_read_b64_tr_b16 a[234:235],v3 offset:20024
v_mfma_f32_16x16x32_bf16 v[40:43],a[208:211],a[8:11],v[20:23]
ds_read_b64_tr_b16 a[220:221],v1 offset:18480
v_lshlrev_b32_sdwa v32,1,v32 dst_sel:DWORD dst_unused:UNUSED_PAD src0_sel:DWORD src1_sel:WORD_1
v_mfma_f32_16x16x32_bf16 v[40:43],a[204:207],a[24:27],v[40:43]
s_waitcnt lgkmcnt(7)
ds_read_u16 v32,v32
v_lshl_add_u64 v[24:25],v[24:25],0,v[26:27]
v_fma_mixlo_f16 v8,v57,s99,0
v_mfma_f32_16x16x32_bf16 v[40:43],a[212:215],a[40:43],v[40:43]
ds_read_b64_tr_b16 a[222:223],v1 offset:19504
v_lshl_add_u64 v[26:27],v[28:29],0,v[30:31]
v_lshlrev_b32_sdwa v34,1,v34 dst_sel:DWORD dst_unused:UNUSED_PAD src0_sel:DWORD src1_sel:WORD_1
v_mfma_f32_16x16x32_bf16 v[36:39],a[200:203],a[40:43],v[16:19]
ds_read_u16_d16_hi v34,v34
v_fma_mixhi_f16 v8,v59,s99,0
v_mfma_f32_16x16x32_bf16 v[36:39],a[196:199],a[24:27],v[36:39]
ds_read_b64_tr_b16 a[216:217],v1 offset:20528
v_lshl_add_u64 v[28:29],v[44:45],0,v[46:47]
v_lshlrev_b32_sdwa v33,1,v33 dst_sel:DWORD dst_unused:UNUSED_PAD src0_sel:DWORD src1_sel:WORD_1
v_pk_fma_f16 v9,v8,v8,s54
v_mfma_f32_16x16x32_bf16 v[36:39],a[192:195],a[8:11],v[36:39]
ds_read_u16 v33,v33
v_lshlrev_b32_sdwa v35,1,v35 dst_sel:DWORD dst_unused:UNUSED_PAD src0_sel:DWORD src1_sel:WORD_1
v_mfma_f32_16x16x32_bf16 v[44:47],a[192:195],a[12:15],v[16:19]
ds_read_u16_d16_hi v35,v35
v_pk_mul_f16 v9,v8,v9
v_mfma_f32_16x16x32_bf16 v[44:47],a[196:199],a[28:31],v[44:47]
s_waitcnt lgkmcnt(8)
ds_read_b64_tr_b16 a[218:219],v1 offset:21552
v_lshl_add_u64 v[30:31],v[48:49],0,v[50:51]
v_lshlrev_b32_sdwa v60,1,v60 dst_sel:DWORD dst_unused:UNUSED_PAD src0_sel:DWORD src1_sel:WORD_1
v_mfma_f32_16x16x32_bf16 v[44:47],a[200:203],a[44:47],v[44:47]
v_exp_f16 v8,v9
v_mfma_f32_16x16x32_bf16 v[48:51],a[212:215],a[44:47],v[20:23]
ds_read_u16 v60,v60
v_lshlrev_b32_sdwa v62,1,v62 dst_sel:DWORD dst_unused:UNUSED_PAD src0_sel:DWORD src1_sel:WORD_1
v_mfma_f32_16x16x32_bf16 v[48:51],a[204:207],a[28:31],v[48:51]
ds_read_u16_d16_hi v62,v62
v_lshlrev_b32_sdwa v61,1,v61 dst_sel:DWORD dst_unused:UNUSED_PAD src0_sel:DWORD src1_sel:WORD_1
v_mfma_f32_16x16x32_bf16 v[48:51],a[208:211],a[12:15],v[48:51]
v_exp_f16_sdwa v8,v9 dst_sel:WORD_1 dst_unused:UNUSED_PRESERVE src0_sel:WORD_1
v_mfma_f32_16x16x32_bf16 v[208:211],a[236:239],v[24:27],v[208:211]
ds_read_u16 v61,v61
v_lshlrev_b32_sdwa v63,1,v63 dst_sel:DWORD dst_unused:UNUSED_PAD src0_sel:DWORD src1_sel:WORD_1
v_mfma_f32_16x16x32_bf16 v[212:215],a[236:239],v[28:31],v[212:215]
ds_read_u16_d16_hi v63,v63
v_lshlrev_b32_sdwa v52,1,v52 dst_sel:DWORD dst_unused:UNUSED_PAD src0_sel:DWORD src1_sel:WORD_1
v_mfma_f32_16x16x32_bf16 v[172:175],a[244:247],v[28:31],v[172:175]
ds_read_u16 v52,v52
v_pk_fma_f16 v8,v8,s55,s55
v_mfma_f32_16x16x32_bf16 v[168:171],a[244:247],v[24:27],v[168:171]
ds_read_b64_tr_b16 a[236:237],v3 offset:18544
v_rcp_f16 v9,v8
v_lshlrev_b32_sdwa v54,1,v54 dst_sel:DWORD dst_unused:UNUSED_PAD src0_sel:DWORD src1_sel:WORD_1
v_mfma_f32_16x16x32_bf16 v[216:219],a[248:251],v[24:27],v[216:219]
ds_read_u16_d16_hi v54,v54
v_lshlrev_b32_sdwa v53,1,v53 dst_sel:DWORD dst_unused:UNUSED_PAD src0_sel:DWORD src1_sel:WORD_1
v_mfma_f32_16x16x32_bf16 v[220:223],a[248:251],v[28:31],v[220:223]
ds_read_u16 v53,v53
ds_read_b64_tr_b16 a[238:239],v3 offset:20080
v_mfma_f32_16x16x32_bf16 v[188:191],a[252:255],v[28:31],v[188:191]
ds_read_b64_tr_b16 a[244:245],v3 offset:18552
v_lshlrev_b32_sdwa v55,1,v55 dst_sel:DWORD dst_unused:UNUSED_PAD src0_sel:DWORD src1_sel:WORD_1
v_mfma_f32_16x16x32_bf16 v[184:187],a[252:255],v[24:27],v[184:187]
ds_read_u16_d16_hi v55,v55
v_rcp_f16_sdwa v9,v8 dst_sel:WORD_1 dst_unused:UNUSED_PRESERVE src0_sel:WORD_1
v_mfma_f32_16x16x32_bf16 v[224:227],a[240:243],v[24:27],v[224:227]
ds_read_b64_tr_b16 a[246:247],v3 offset:20088
ds_read_b64_tr_b16 a[248:249],v3 offset:18608
v_lshlrev_b32_sdwa v56,1,v56 dst_sel:DWORD dst_unused:UNUSED_PAD src0_sel:DWORD src1_sel:WORD_1
v_mfma_f32_16x16x32_bf16 v[228:231],a[240:243],v[28:31],v[228:231]
ds_read_u16 v56,v56
v_lshlrev_b32_sdwa v58,1,v58 dst_sel:DWORD dst_unused:UNUSED_PAD src0_sel:DWORD src1_sel:WORD_1
v_mfma_f32_16x16x32_bf16 v[204:207],a[224:227],v[28:31],v[204:207]
ds_read_u16_d16_hi v58,v58
v_fma_mix_f32 v57,v57,v9,0 op_sel_hi:[0,1,0]
v_mfma_f32_16x16x32_bf16 v[200:203],a[224:227],v[24:27],v[200:203]
ds_read_b64_tr_b16 a[250:251],v3 offset:20144
v_fma_mix_f32 v59,v59,v9,0 op_sel:[0,1,0] op_sel_hi:[0,1,0]
v_mfma_f32_16x16x32_bf16 v[28:31],a[208:211],a[48:51],v[20:23]
s_add_u32 m0,s58,12320
ds_read_b64_tr_b16 a[252:253],v3 offset:18616
v_lshlrev_b32_sdwa v40,1,v40 dst_sel:DWORD dst_unused:UNUSED_PAD src0_sel:DWORD src1_sel:WORD_1
v_mfma_f32_16x16x32_bf16 v[28:31],a[204:207],a[64:67],v[28:31]
s_waitcnt lgkmcnt(10)
ds_read_b64_tr_b16 a[254:255],v3 offset:20152
v_lshl_add_u64 v[32:33],v[32:33],0,v[34:35]
v_mfma_f32_16x16x32_bf16 v[28:31],a[212:215],a[80:83],v[28:31]
s_add_u32 s69,s69,s59
ds_read_u16 v40,v40
v_lshl_add_u64 v[34:35],v[60:61],0,v[62:63]
v_lshlrev_b32_sdwa v42,1,v42 dst_sel:DWORD dst_unused:UNUSED_PAD src0_sel:DWORD src1_sel:WORD_1
v_mfma_f32_16x16x32_bf16 v[24:27],a[200:203],a[80:83],v[16:19]
buffer_load_dwordx4 v5,s[44:47],s69 offen offset:0 lds
v_lshlrev_b32_sdwa v41,1,v41 dst_sel:DWORD dst_unused:UNUSED_PAD src0_sel:DWORD src1_sel:WORD_1
v_mfma_f32_16x16x32_bf16 v[24:27],a[196:199],a[64:67],v[24:27]
s_waitcnt lgkmcnt(8)
ds_read_u16_d16_hi v42,v42
v_lshl_add_u64 v[60:61],v[52:53],0,v[54:55]
v_mfma_f32_16x16x32_bf16 v[24:27],a[192:195],a[48:51],v[24:27]
s_add_u32 s69,s69,s53
ds_read_u16 v41,v41
v_lshlrev_b32_sdwa v43,1,v43 dst_sel:DWORD dst_unused:UNUSED_PAD src0_sel:DWORD src1_sel:WORD_1
v_mfma_f32_16x16x32_bf16 v[52:55],a[192:195],a[52:55],v[16:19]
s_waitcnt lgkmcnt(6)
buffer_load_dwordx4 v5,s[44:47],s69 offen offset:1024 lds
v_or_b32 v62,v56,v58
v_lshlrev_b32_sdwa v36,1,v36 dst_sel:DWORD dst_unused:UNUSED_PAD src0_sel:DWORD src1_sel:WORD_1
v_mfma_f32_16x16x32_bf16 v[52:55],a[196:199],a[68:71],v[52:55]
s_add_u32 s69,s69,s53
ds_read_u16_d16_hi v43,v43
v_cvt_pk_bf16_f32 v63,v57,v59
v_lshlrev_b32_sdwa v38,1,v38 dst_sel:DWORD dst_unused:UNUSED_PAD src0_sel:DWORD src1_sel:WORD_1
v_mfma_f32_16x16x32_bf16 v[52:55],a[200:203],a[84:87],v[52:55]
buffer_load_dwordx4 v5,s[44:47],s69 offen offset:2048 lds
v_lshlrev_b32_sdwa v37,1,v37 dst_sel:DWORD dst_unused:UNUSED_PAD src0_sel:DWORD src1_sel:WORD_1
v_mfma_f32_16x16x32_bf16 v[56:59],a[212:215],a[84:87],v[20:23]
ds_read_u16 v36,v36
v_lshlrev_b32_sdwa v39,1,v39 dst_sel:DWORD dst_unused:UNUSED_PAD src0_sel:DWORD src1_sel:WORD_1
v_mfma_f32_16x16x32_bf16 v[56:59],a[204:207],a[68:71],v[56:59]
ds_read_u16_d16_hi v38,v38
v_lshlrev_b32_sdwa v48,1,v48 dst_sel:DWORD dst_unused:UNUSED_PAD src0_sel:DWORD src1_sel:WORD_1
v_mfma_f32_16x16x32_bf16 v[56:59],a[208:211],a[52:55],v[56:59]
ds_read_u16 v37,v37
v_lshlrev_b32_sdwa v50,1,v50 dst_sel:DWORD dst_unused:UNUSED_PAD src0_sel:DWORD src1_sel:WORD_1
v_mfma_f32_16x16x32_bf16 v[136:139],a[228:231],v[32:35],v[136:139]
ds_read_u16_d16_hi v39,v39
v_lshlrev_b32_sdwa v49,1,v49 dst_sel:DWORD dst_unused:UNUSED_PAD src0_sel:DWORD src1_sel:WORD_1
v_mfma_f32_16x16x32_bf16 v[140:143],a[228:231],v[60:63],v[140:143]
ds_read_u16 v48,v48
v_lshlrev_b32_sdwa v51,1,v51 dst_sel:DWORD dst_unused:UNUSED_PAD src0_sel:DWORD src1_sel:WORD_1
v_mfma_f32_16x16x32_bf16 v[68:71],a[232:235],v[60:63],v[68:71]
ds_read_u16_d16_hi v50,v50
v_lshlrev_b32_sdwa v44,1,v44 dst_sel:DWORD dst_unused:UNUSED_PAD src0_sel:DWORD src1_sel:WORD_1
v_mfma_f32_16x16x32_bf16 v[64:67],a[232:235],v[32:35],v[64:67]
ds_read_u16 v49,v49
v_lshlrev_b32_sdwa v46,1,v46 dst_sel:DWORD dst_unused:UNUSED_PAD src0_sel:DWORD src1_sel:WORD_1
v_mfma_f32_16x16x32_bf16 v[144:147],a[236:239],v[32:35],v[144:147]
ds_read_u16_d16_hi v51,v51
v_lshlrev_b32_sdwa v45,1,v45 dst_sel:DWORD dst_unused:UNUSED_PAD src0_sel:DWORD src1_sel:WORD_1
v_mfma_f32_16x16x32_bf16 v[148:151],a[236:239],v[60:63],v[148:151]
ds_read_u16 v44,v44
v_lshlrev_b32_sdwa v47,1,v47 dst_sel:DWORD dst_unused:UNUSED_PAD src0_sel:DWORD src1_sel:WORD_1
v_mfma_f32_16x16x32_bf16 v[84:87],a[244:247],v[60:63],v[84:87]
ds_read_u16_d16_hi v46,v46
v_mfma_f32_16x16x32_bf16 v[80:83],a[244:247],v[32:35],v[80:83]
s_waitcnt lgkmcnt(8)
ds_read_u16 v45,v45
v_mfma_f32_16x16x32_bf16 v[152:155],a[248:251],v[32:35],v[152:155]
ds_read_u16_d16_hi v47,v47
v_mfma_f32_16x16x32_bf16 v[156:159],a[248:251],v[60:63],v[156:159]
v_mfma_f32_16x16x32_bf16 v[100:103],a[252:255],v[60:63],v[100:103]
v_mfma_f32_16x16x32_bf16 v[96:99],a[252:255],v[32:35],v[96:99]
v_mfma_f32_16x16x32_bf16 v[60:63],a[208:211],a[56:59],v[20:23]
v_mul_f32_dpp v136,v14,v136 row_newbcast:0 row_mask:0xf bank_mask:0xf
s_add_u32 s8,s61,0
v_mul_f32_dpp v137,v14,v137 row_newbcast:1 row_mask:0xf bank_mask:0xf
v_mul_f32_dpp v138,v14,v138 row_newbcast:2 row_mask:0xf bank_mask:0xf
v_lshlrev_b32_sdwa v28,1,v28 dst_sel:DWORD dst_unused:UNUSED_PAD src0_sel:DWORD src1_sel:WORD_1
v_mfma_f32_16x16x32_bf16 v[60:63],a[204:207],a[72:75],v[60:63]
s_nop 0
ds_read_u16 v28,v28
v_lshl_add_u64 v[40:41],v[40:41],0,v[42:43]
v_mul_f32_dpp v139,v14,v139 row_newbcast:3 row_mask:0xf bank_mask:0xf
v_mul_f32_dpp v64,v14,v64 row_newbcast:4 row_mask:0xf bank_mask:0xf
v_mul_f32_dpp v65,v14,v65 row_newbcast:5 row_mask:0xf bank_mask:0xf
v_mfma_f32_16x16x32_bf16 v[60:63],a[212:215],a[88:91],v[60:63]
s_waitcnt lgkmcnt(4)
v_lshl_add_u64 v[42:43],v[36:37],0,v[38:39]
v_mul_f32_dpp v66,v14,v66 row_newbcast:6 row_mask:0xf bank_mask:0xf
v_mul_f32_dpp v67,v14,v67 row_newbcast:7 row_mask:0xf bank_mask:0xf
v_lshlrev_b32_sdwa v30,1,v30 dst_sel:DWORD dst_unused:UNUSED_PAD src0_sel:DWORD src1_sel:WORD_1
v_cvt_pk_bf16_f32 v136,v136,v137
v_mfma_f32_16x16x32_bf16 v[32:35],a[200:203],a[88:91],v[16:19]
ds_read_u16_d16_hi v30,v30
v_cvt_pk_bf16_f32 v137,v138,v139
v_cvt_pk_bf16_f32 v138,v64,v65
v_lshlrev_b32_sdwa v29,1,v29 dst_sel:DWORD dst_unused:UNUSED_PAD src0_sel:DWORD src1_sel:WORD_1
v_cvt_pk_bf16_f32 v139,v66,v67
v_mfma_f32_16x16x32_bf16 v[32:35],a[196:199],a[72:75],v[32:35]
buffer_store_dwordx4 v[136:139],v7,s[40:43],s8 offen offset:0 sc1
v_lshl_add_u64 v[36:37],v[48:49],0,v[50:51]
v_mul_f32_dpp v144,v14,v144 row_newbcast:8 row_mask:0xf bank_mask:0xf
v_mul_f32_dpp v145,v14,v145 row_newbcast:9 row_mask:0xf bank_mask:0xf
v_mul_f32_dpp v146,v14,v146 row_newbcast:10 row_mask:0xf bank_mask:0xf
v_mfma_f32_16x16x32_bf16 v[32:35],a[192:195],a[56:59],v[32:35]
ds_read_u16 v29,v29
v_mul_f32_dpp v147,v14,v147 row_newbcast:11 row_mask:0xf bank_mask:0xf
v_mul_f32_dpp v80,v14,v80 row_newbcast:12 row_mask:0xf bank_mask:0xf
v_lshlrev_b32_sdwa v31,1,v31 dst_sel:DWORD dst_unused:UNUSED_PAD src0_sel:DWORD src1_sel:WORD_1
v_mul_f32_dpp v81,v14,v81 row_newbcast:13 row_mask:0xf bank_mask:0xf
v_mfma_f32_16x16x32_bf16 v[48:51],a[192:195],a[60:63],v[16:19]
s_nop 0
ds_read_b128 v[16:19],v4 offset:0
v_mul_f32_dpp v82,v14,v82 row_newbcast:14 row_mask:0xf bank_mask:0xf
v_mul_f32_dpp v83,v14,v83 row_newbcast:15 row_mask:0xf bank_mask:0xf
v_lshlrev_b32_sdwa v24,1,v24 dst_sel:DWORD dst_unused:UNUSED_PAD src0_sel:DWORD src1_sel:WORD_1
v_cvt_pk_bf16_f32 v144,v144,v145
v_mfma_f32_16x16x32_bf16 v[48:51],a[196:199],a[76:79],v[48:51]
s_waitcnt lgkmcnt(4)
ds_read_u16_d16_hi v31,v31
ds_read_b64_tr_b16 a[192:193],v1 offset:22576
v_lshl_add_u64 v[38:39],v[44:45],0,v[46:47]
v_cvt_pk_bf16_f32 v145,v146,v147
v_cvt_pk_bf16_f32 v146,v80,v81
v_cvt_pk_bf16_f32 v147,v82,v83
v_lshlrev_b32_sdwa v26,1,v26 dst_sel:DWORD dst_unused:UNUSED_PAD src0_sel:DWORD src1_sel:WORD_1
v_mfma_f32_16x16x32_bf16 v[48:51],a[200:203],a[92:95],v[48:51]
ds_read_b64_tr_b16 a[194:195],v1 offset:23600
ds_read_u16 v24,v24
v_mul_f32_dpp v152,v15,v152 row_newbcast:0 row_mask:0xf bank_mask:0xf
v_mul_f32_dpp v153,v15,v153 row_newbcast:1 row_mask:0xf bank_mask:0xf
v_mul_f32_dpp v154,v15,v154 row_newbcast:2 row_mask:0xf bank_mask:0xf
v_mfma_f32_16x16x32_bf16 v[44:47],a[212:215],a[92:95],v[20:23]
ds_read_b128 v[20:23],v4 offset:32
ds_read_u16_d16_hi v26,v26
v_mul_f32_dpp v155,v15,v155 row_newbcast:3 row_mask:0xf bank_mask:0xf
v_mul_f32_dpp v96,v15,v96 row_newbcast:4 row_mask:0xf bank_mask:0xf
v_lshlrev_b32_sdwa v25,1,v25 dst_sel:DWORD dst_unused:UNUSED_PAD src0_sel:DWORD src1_sel:WORD_1
v_mul_f32_dpp v97,v15,v97 row_newbcast:5 row_mask:0xf bank_mask:0xf
v_mfma_f32_16x16x32_bf16 v[44:47],a[204:207],a[76:79],v[44:47]
ds_read_u16 v25,v25
ds_read_b64_tr_b16 a[196:197],v2 offset:22576
v_mul_f32_dpp v98,v15,v98 row_newbcast:6 row_mask:0xf bank_mask:0xf
v_mul_f32_dpp v99,v15,v99 row_newbcast:7 row_mask:0xf bank_mask:0xf
v_lshlrev_b32_sdwa v27,1,v27 dst_sel:DWORD dst_unused:UNUSED_PAD src0_sel:DWORD src1_sel:WORD_1
v_cvt_pk_bf16_f32 v152,v152,v153
v_mfma_f32_16x16x32_bf16 v[44:47],a[208:211],a[60:63],v[44:47]
ds_read_u16_d16_hi v27,v27
ds_read_b64_tr_b16 a[198:199],v2 offset:23600
v_cvt_pk_bf16_f32 v153,v154,v155
v_cvt_pk_bf16_f32 v154,v96,v97
v_cvt_pk_bf16_f32 v155,v98,v99
v_mfma_f32_16x16x32_bf16 v[112:115],a[228:231],v[40:43],v[112:115]
ds_read_b64_tr_b16 a[200:201],v2 offset:20528
v_mul_f32_dpp v140,v14,v140 row_newbcast:0 row_mask:0xf bank_mask:0xf
v_mul_f32_dpp v141,v14,v141 row_newbcast:1 row_mask:0xf bank_mask:0xf
v_lshlrev_b32_sdwa v56,1,v56 dst_sel:DWORD dst_unused:UNUSED_PAD src0_sel:DWORD src1_sel:WORD_1
v_mul_f32_dpp v142,v14,v142 row_newbcast:2 row_mask:0xf bank_mask:0xf
v_mfma_f32_16x16x32_bf16 v[116:119],a[228:231],v[36:39],v[116:119]
ds_read_b64_tr_b16 a[202:203],v2 offset:21552
v_mul_f32_dpp v143,v14,v143 row_newbcast:3 row_mask:0xf bank_mask:0xf
v_mul_f32_dpp v68,v14,v68 row_newbcast:4 row_mask:0xf bank_mask:0xf
v_lshlrev_b32_sdwa v58,1,v58 dst_sel:DWORD dst_unused:UNUSED_PAD src0_sel:DWORD src1_sel:WORD_1
v_mul_f32_dpp v69,v14,v69 row_newbcast:5 row_mask:0xf bank_mask:0xf
v_mfma_f32_16x16x32_bf16 v[76:79],a[232:235],v[36:39],v[76:79]
ds_read_u16 v56,v56
v_mul_f32_dpp v70,v14,v70 row_newbcast:6 row_mask:0xf bank_mask:0xf
v_mul_f32_dpp v71,v14,v71 row_newbcast:7 row_mask:0xf bank_mask:0xf
v_cvt_pk_bf16_f32 v140,v140,v141
v_lshlrev_b32_sdwa v57,1,v57 dst_sel:DWORD dst_unused:UNUSED_PAD src0_sel:DWORD src1_sel:WORD_1
v_mfma_f32_16x16x32_bf16 v[72:75],a[232:235],v[40:43],v[72:75]
TP4880_%=:
s_cbranch_execz TP7016_%=
buffer_store_dwordx4 v[144:147],v7,s[40:43],s8 offen offset:64 sc1
ds_read_u16_d16_hi v58,v58
v_cvt_pk_bf16_f32 v141,v142,v143
v_cvt_pk_bf16_f32 v142,v68,v69
v_cvt_pk_bf16_f32 v143,v70,v71
v_mfma_f32_16x16x32_bf16 v[120:123],a[236:239],v[40:43],v[120:123]
s_nop 0
ds_read_u16 v57,v57
v_mul_f32_dpp v148,v14,v148 row_newbcast:8 row_mask:0xf bank_mask:0xf
v_mul_f32_dpp v149,v14,v149 row_newbcast:9 row_mask:0xf bank_mask:0xf
v_lshlrev_b32_sdwa v59,1,v59 dst_sel:DWORD dst_unused:UNUSED_PAD src0_sel:DWORD src1_sel:WORD_1
v_mul_f32_dpp v150,v14,v150 row_newbcast:10 row_mask:0xf bank_mask:0xf
v_mfma_f32_16x16x32_bf16 v[124:127],a[236:239],v[36:39],v[124:127]
ds_read_u16_d16_hi v59,v59
v_mul_f32_dpp v151,v14,v151 row_newbcast:11 row_mask:0xf bank_mask:0xf
v_mul_f32_dpp v84,v14,v84 row_newbcast:12 row_mask:0xf bank_mask:0xf
v_lshlrev_b32_sdwa v52,1,v52 dst_sel:DWORD dst_unused:UNUSED_PAD src0_sel:DWORD src1_sel:WORD_1
v_mul_f32_dpp v85,v14,v85 row_newbcast:13 row_mask:0xf bank_mask:0xf
v_mfma_f32_16x16x32_bf16 v[92:95],a[244:247],v[36:39],v[92:95]
ds_read_b64_tr_b16 a[212:213],v2 offset:18480
v_mul_f32_dpp v86,v14,v86 row_newbcast:14 row_mask:0xf bank_mask:0xf
v_mul_f32_dpp v87,v14,v87 row_newbcast:15 row_mask:0xf bank_mask:0xf
v_cvt_pk_bf16_f32 v148,v148,v149
v_mfma_f32_16x16x32_bf16 v[88:91],a[244:247],v[40:43],v[88:91]
ds_read_b64_tr_b16 a[214:215],v2 offset:19504
v_cvt_pk_bf16_f32 v149,v150,v151
v_cvt_pk_bf16_f32 v150,v84,v85
v_lshlrev_b32_sdwa v54,1,v54 dst_sel:DWORD dst_unused:UNUSED_PAD src0_sel:DWORD src1_sel:WORD_1
v_cvt_pk_bf16_f32 v151,v86,v87
v_mfma_f32_16x16x32_bf16 v[128:131],a[248:251],v[40:43],v[128:131]
ds_read_u16 v52,v52
v_mul_f32_dpp v156,v15,v156 row_newbcast:0 row_mask:0xf bank_mask:0xf
v_mul_f32_dpp v157,v15,v157 row_newbcast:1 row_mask:0xf bank_mask:0xf
v_lshlrev_b32_sdwa v53,1,v53 dst_sel:DWORD dst_unused:UNUSED_PAD src0_sel:DWORD src1_sel:WORD_1
v_mul_f32_dpp v158,v15,v158 row_newbcast:2 row_mask:0xf bank_mask:0xf
v_mfma_f32_16x16x32_bf16 v[132:135],a[248:251],v[36:39],v[132:135]
ds_read_u16_d16_hi v54,v54
v_mul_f32_dpp v159,v15,v159 row_newbcast:3 row_mask:0xf bank_mask:0xf
v_mul_f32_dpp v100,v15,v100 row_newbcast:4 row_mask:0xf bank_mask:0xf
v_mul_f32_dpp v101,v15,v101 row_newbcast:5 row_mask:0xf bank_mask:0xf
v_lshlrev_b32_sdwa v55,1,v55 dst_sel:DWORD dst_unused:UNUSED_PAD src0_sel:DWORD src1_sel:WORD_1
v_mfma_f32_16x16x32_bf16 v[108:111],a[252:255],v[36:39],v[108:111]
ds_read_u16 v53,v53
v_mul_f32_dpp v102,v15,v102 row_newbcast:6 row_mask:0xf bank_mask:0xf
v_mul_f32_dpp v103,v15,v103 row_newbcast:7 row_mask:0xf bank_mask:0xf
v_cvt_pk_bf16_f32 v156,v156,v157
v_mfma_f32_16x16x32_bf16 v[104:107],a[252:255],v[40:43],v[104:107]
s_waitcnt lgkmcnt(8)
ds_read_u16_d16_hi v55,v55
v_cvt_pk_bf16_f32 v157,v158,v159
v_cvt_pk_bf16_f32 v158,v100,v101
v_cvt_pk_bf16_f32 v159,v102,v103
v_mfma_f32_16x16x32_bf16 v[36:39],a[220:223],a[48:51],v[16:19]
s_nop 0
v_mul_f32_dpp v112,v14,v112 row_newbcast:0 row_mask:0xf bank_mask:0xf
s_add_u32 s9,s61,98304
v_accvgpr_write_b32 a16,0
v_accvgpr_write_b32 a17,0
v_lshlrev_b32_sdwa v60,1,v60 dst_sel:DWORD dst_unused:UNUSED_PAD src0_sel:DWORD src1_sel:WORD_1
v_mfma_f32_16x16x32_bf16 v[36:39],a[216:219],a[64:67],v[36:39]
ds_read_u16 v60,v60
v_lshl_add_u64 v[28:29],v[28:29],0,v[30:31]
v_accvgpr_write_b32 a18,0
v_accvgpr_write_b32 a19,0
v_mul_f32_dpp v113,v14,v113 row_newbcast:1 row_mask:0xf bank_mask:0xf
v_mfma_f32_16x16x32_bf16 v[36:39],a[192:195],a[80:83],v[36:39]
v_lshl_add_u64 v[30:31],v[24:25],0,v[26:27]
v_mul_f32_dpp v114,v14,v114 row_newbcast:2 row_mask:0xf bank_mask:0xf
v_mul_f32_dpp v115,v14,v115 row_newbcast:3 row_mask:0xf bank_mask:0xf
v_lshlrev_b32_sdwa v62,1,v62 dst_sel:DWORD dst_unused:UNUSED_PAD src0_sel:DWORD src1_sel:WORD_1
v_mul_f32_dpp v72,v14,v72 row_newbcast:4 row_mask:0xf bank_mask:0xf
v_mfma_f32_16x16x32_bf16 v[40:43],a[196:199],a[80:83],v[20:23]
s_nop 0
buffer_store_dwordx4 v[152:155],v7,s[40:43],s8 offen offset:128 sc1
v_mul_f32_dpp v73,v14,v73 row_newbcast:5 row_mask:0xf bank_mask:0xf
v_mul_f32_dpp v74,v14,v74 row_newbcast:6 row_mask:0xf bank_mask:0xf
v_lshlrev_b32_sdwa v61,1,v61 dst_sel:DWORD dst_unused:UNUSED_PAD src0_sel:DWORD src1_sel:WORD_1
v_mul_f32_dpp v75,v14,v75 row_newbcast:7 row_mask:0xf bank_mask:0xf
v_mfma_f32_16x16x32_bf16 v[40:43],a[200:203],a[64:67],v[40:43]
s_waitcnt lgkmcnt(5)
ds_read_u16_d16_hi v62,v62
v_lshl_add_u64 v[24:25],v[56:57],0,v[58:59]
v_cvt_pk_bf16_f32 v112,v112,v113
v_cvt_pk_bf16_f32 v113,v114,v115
v_cvt_pk_bf16_f32 v114,v72,v73
v_mfma_f32_16x16x32_bf16 v[40:43],a[212:215],a[48:51],v[40:43]
ds_read_u16 v61,v61
v_cvt_pk_bf16_f32 v115,v74,v75
v_mul_f32_dpp v120,v14,v120 row_newbcast:8 row_mask:0xf bank_mask:0xf
v_lshlrev_b32_sdwa v63,1,v63 dst_sel:DWORD dst_unused:UNUSED_PAD src0_sel:DWORD src1_sel:WORD_1
v_accvgpr_write_b32 a24,v112
v_mfma_f32_16x16x32_bf16 v[56:59],a[212:215],a[52:55],v[20:23]
s_nop 0
ds_read_u16_d16_hi v63,v63
v_mul_f32_dpp v121,v14,v121 row_newbcast:9 row_mask:0xf bank_mask:0xf
v_mul_f32_dpp v122,v14,v122 row_newbcast:10 row_mask:0xf bank_mask:0xf
v_lshlrev_b32_sdwa v32,1,v32 dst_sel:DWORD dst_unused:UNUSED_PAD src0_sel:DWORD src1_sel:WORD_1
v_accvgpr_write_b32 a25,v113
v_mfma_f32_16x16x32_bf16 v[56:59],a[200:203],a[68:71],v[56:59]
s_waitcnt lgkmcnt(4)
ds_read_u16 v32,v32
v_lshl_add_u64 v[26:27],v[52:53],0,v[54:55]
v_mul_f32_dpp v123,v14,v123 row_newbcast:11 row_mask:0xf bank_mask:0xf
v_mul_f32_dpp v88,v14,v88 row_newbcast:12 row_mask:0xf bank_mask:0xf
v_accvgpr_write_b32 a26,v114
v_lshlrev_b32_sdwa v34,1,v34 dst_sel:DWORD dst_unused:UNUSED_PAD src0_sel:DWORD src1_sel:WORD_1
v_mfma_f32_16x16x32_bf16 v[56:59],a[196:199],a[84:87],v[56:59]
s_waitcnt vmcnt(10)
s_barrier
ds_read_u16_d16_hi v34,v34
v_mul_f32_dpp v89,v14,v89 row_newbcast:13 row_mask:0xf bank_mask:0xf
v_mul_f32_dpp v90,v14,v90 row_newbcast:14 row_mask:0xf bank_mask:0xf
v_accvgpr_write_b32 a27,v115
v_mfma_f32_16x16x32_bf16 v[52:55],a[192:195],a[84:87],v[16:19]
ds_read_b64_tr_b16 a[240:241],v3 offset:0
v_mul_f32_dpp v91,v14,v91 row_newbcast:15 row_mask:0xf bank_mask:0xf
v_cvt_pk_bf16_f32 v120,v120,v121
v_lshlrev_b32_sdwa v33,1,v33 dst_sel:DWORD dst_unused:UNUSED_PAD src0_sel:DWORD src1_sel:WORD_1
v_cvt_pk_bf16_f32 v121,v122,v123
v_mfma_f32_16x16x32_bf16 v[52:55],a[216:219],a[68:71],v[52:55]
s_add_u32 m0,s58,18480
ds_read_u16 v33,v33
v_cvt_pk_bf16_f32 v122,v88,v89
v_cvt_pk_bf16_f32 v123,v90,v91
v_lshlrev_b32_sdwa v35,1,v35 dst_sel:DWORD dst_unused:UNUSED_PAD src0_sel:DWORD src1_sel:WORD_1
v_mul_f32_dpp v128,v15,v128 row_newbcast:0 row_mask:0xf bank_mask:0xf
v_mfma_f32_16x16x32_bf16 v[52:55],a[220:223],a[52:55],v[52:55]
s_add_u32 s69,s69,s59
ds_read_u16_d16_hi v35,v35
v_accvgpr_write_b32 a28,v120
v_mul_f32_dpp v129,v15,v129 row_newbcast:1 row_mask:0xf bank_mask:0xf
v_mul_f32_dpp v130,v15,v130 row_newbcast:2 row_mask:0xf bank_mask:0xf
v_mfma_f32_16x16x32_bf16 v[232:235],a[228:231],v[28:31],v[232:235]
buffer_load_dwordx4 v5,s[44:47],s69 offen offset:0 lds
v_accvgpr_write_b32 a29,v121
v_mul_f32_dpp v131,v15,v131 row_newbcast:3 row_mask:0xf bank_mask:0xf
v_lshlrev_b32_sdwa v44,1,v44 dst_sel:DWORD dst_unused:UNUSED_PAD src0_sel:DWORD src1_sel:WORD_1
v_mul_f32_dpp v104,v15,v104 row_newbcast:4 row_mask:0xf bank_mask:0xf
v_mfma_f32_16x16x32_bf16 v[236:239],a[228:231],v[24:27],v[236:239]
s_add_u32 s69,s69,s53
ds_read_u16 v44,v44
v_accvgpr_write_b32 a30,v122
v_mul_f32_dpp v105,v15,v105 row_newbcast:5 row_mask:0xf bank_mask:0xf
v_lshlrev_b32_sdwa v46,1,v46 dst_sel:DWORD dst_unused:UNUSED_PAD src0_sel:DWORD src1_sel:WORD_1
v_mul_f32_dpp v106,v15,v106 row_newbcast:6 row_mask:0xf bank_mask:0xf
v_mfma_f32_16x16x32_bf16 v[164:167],a[232:235],v[24:27],v[164:167]
buffer_load_dwordx4 v5,s[44:47],s69 offen offset:1024 lds
ds_read_u16_d16_hi v46,v46
v_accvgpr_write_b32 a31,v123
v_mul_f32_dpp v107,v15,v107 row_newbcast:7 row_mask:0xf bank_mask:0xf
v_cvt_pk_bf16_f32 v128,v128,v129
v_lshlrev_b32_sdwa v45,1,v45 dst_sel:DWORD dst_unused:UNUSED_PAD src0_sel:DWORD src1_sel:WORD_1
v_mfma_f32_16x16x32_bf16 v[160:163],a[232:235],v[28:31],v[160:163]
s_nop 0
buffer_store_dwordx4 v[140:143],v7,s[40:43],s8 offen offset:192 sc1
ds_read_u16 v45,v45
v_cvt_pk_bf16_f32 v129,v130,v131
v_cvt_pk_bf16_f32 v130,v104,v105
v_cvt_pk_bf16_f32 v131,v106,v107
v_mfma_f32_16x16x32_bf16 v[240:243],a[236:239],v[28:31],v[240:243]
s_add_u32 s69,s69,s53
ds_read_b64_tr_b16 a[242:243],v3 offset:1536
v_mul_f32_dpp v116,v14,v116 row_newbcast:0 row_mask:0xf bank_mask:0xf
v_accvgpr_write_b32 a32,v128
v_lshlrev_b32_sdwa v47,1,v47 dst_sel:DWORD dst_unused:UNUSED_PAD src0_sel:DWORD src1_sel:WORD_1
v_mul_f32_dpp v117,v14,v117 row_newbcast:1 row_mask:0xf bank_mask:0xf
v_mfma_f32_16x16x32_bf16 v[244:247],a[236:239],v[24:27],v[244:247]
buffer_load_dwordx4 v5,s[44:47],s69 offen offset:2048 lds
v_mul_f32_dpp v118,v14,v118 row_newbcast:2 row_mask:0xf bank_mask:0xf
v_accvgpr_write_b32 a33,v129
v_lshlrev_b32_sdwa v48,1,v48 dst_sel:DWORD dst_unused:UNUSED_PAD src0_sel:DWORD src1_sel:WORD_1
v_mul_f32_dpp v119,v14,v119 row_newbcast:3 row_mask:0xf bank_mask:0xf
v_mfma_f32_16x16x32_bf16 v[180:183],a[244:247],v[24:27],v[180:183]
ds_read_u16_d16_hi v47,v47
v_mul_f32_dpp v76,v14,v76 row_newbcast:4 row_mask:0xf bank_mask:0xf
v_accvgpr_write_b32 a34,v130
v_mul_f32_dpp v77,v14,v77 row_newbcast:5 row_mask:0xf bank_mask:0xf
v_mfma_f32_16x16x32_bf16 v[176:179],a[244:247],v[28:31],v[176:179]
ds_read_u16 v48,v48
v_mul_f32_dpp v78,v14,v78 row_newbcast:6 row_mask:0xf bank_mask:0xf
v_accvgpr_write_b32 a35,v131
v_lshlrev_b32_sdwa v50,1,v50 dst_sel:DWORD dst_unused:UNUSED_PAD src0_sel:DWORD src1_sel:WORD_1
v_mul_f32_dpp v79,v14,v79 row_newbcast:7 row_mask:0xf bank_mask:0xf
v_mfma_f32_16x16x32_bf16 v[248:251],a[248:251],v[28:31],v[248:251]
ds_read_u16_d16_hi v50,v50
v_cvt_pk_bf16_f32 v116,v116,v117
v_cvt_pk_bf16_f32 v117,v118,v119
v_lshlrev_b32_sdwa v49,1,v49 dst_sel:DWORD dst_unused:UNUSED_PAD src0_sel:DWORD src1_sel:WORD_1
v_cvt_pk_bf16_f32 v118,v76,v77
v_mfma_f32_16x16x32_bf16 v[252:255],a[248:251],v[24:27],v[252:255]
ds_read_u16 v49,v49
v_cvt_pk_bf16_f32 v119,v78,v79
v_mfma_f32_32x32x16_bf16 v[64:79],a[16:19],a[16:19],0
v_mul_f32_dpp v124,v14,v124 row_newbcast:8 row_mask:0xf bank_mask:0xf
v_lshlrev_b32_sdwa v51,1,v51 dst_sel:DWORD dst_unused:UNUSED_PAD src0_sel:DWORD src1_sel:WORD_1
v_accvgpr_write_b32 a36,v116
v_mul_f32_dpp v125,v14,v125 row_newbcast:9 row_mask:0xf bank_mask:0xf
v_mul_f32_dpp v126,v14,v126 row_newbcast:10 row_mask:0xf bank_mask:0xf
v_mfma_f32_16x16x32_bf16 v[196:199],a[252:255],v[24:27],v[196:199]
ds_read_u16_d16_hi v51,v51
v_accvgpr_write_b32 a37,v117
v_mfma_f32_16x16x32_bf16 v[192:195],a[252:255],v[28:31],v[192:195]
s_nop 0
ds_read_b64_tr_b16 a[224:225],v3 offset:8
v_mul_f32_dpp v127,v14,v127 row_newbcast:11 row_mask:0xf bank_mask:0xf
v_mul_f32_dpp v92,v14,v92 row_newbcast:12 row_mask:0xf bank_mask:0xf
ds_read_b64_tr_b16 a[226:227],v3 offset:1544
s_mov_b64 exec,0
s_cmp_eq_u32 s52,0
s_cbranch_scc1 TE10_%=
s_cmp_eq_u32 s52,1
s_cbranch_scc1 TE11_%=
s_cmp_eq_u32 s52,2
s_cbranch_scc1 TE12_%=
s_branch TE13_%=
TR1_%=:
s_mov_b64 exec,-1
v_add_u32_sdwa v28,s60,v10 dst_sel:DWORD dst_unused:UNUSED_PAD src0_sel:DWORD src1_sel:WORD_1
v_subrev_u32 v8,88400,v28
v_min_u32_e64 v28,v28,v8
v_add_u32 v29,7072,v28
v_subrev_u32 v8,88400,v29
v_min_u32_e64 v29,v29,v8
s_waitcnt lgkmcnt(15)
v_lshl_add_u64 v[60:61],v[60:61],0,v[62:63]
s_waitcnt lgkmcnt(11)
v_lshl_add_u64 v[62:63],v[32:33],0,v[34:35]
s_waitcnt lgkmcnt(6)
v_lshl_add_u64 v[32:33],v[44:45],0,v[46:47]
s_waitcnt lgkmcnt(2)
v_lshl_add_u64 v[34:35],v[48:49],0,v[50:51]
v_mfma_f32_16x16x32_bf16 v[208:211],a[228:231],v[60:63],v[208:211]
v_xad_u32 v24,v11,0,v28
v_xad_u32 v25,v11,8,v29
v_xad_u32 v26,v11,16,v28
v_xad_u32 v27,v11,24,v29
v_mfma_f32_16x16x32_bf16 v[212:215],a[228:231],v[32:35],v[212:215]
ds_read_b64 a[0:1],v24 offset:0
v_accvgpr_write_b32 a38,v118
v_mul_f32_dpp v93,v14,v93 row_newbcast:13 row_mask:0xf bank_mask:0xf
v_mul_f32_dpp v94,v14,v94 row_newbcast:14 row_mask:0xf bank_mask:0xf
v_accvgpr_write_b32 a39,v119
v_mfma_f32_16x16x32_bf16 v[172:175],a[232:235],v[32:35],v[172:175]
buffer_store_dwordx4 v[148:151],v7,s[40:43],s8 offen offset:256 sc1
ds_read_b64 a[2:3],v24 offset:192
v_mul_f32_dpp v95,v14,v95 row_newbcast:15 row_mask:0xf bank_mask:0xf
v_cvt_pk_bf16_f32 v124,v124,v125
v_cvt_pk_bf16_f32 v125,v126,v127
v_mfma_f32_16x16x32_bf16 v[168:171],a[232:235],v[60:63],v[168:171]
ds_read_b64 a[4:5],v25 offset:0
v_cvt_pk_bf16_f32 v126,v92,v93
v_cvt_pk_bf16_f32 v127,v94,v95
v_mfma_f32_32x32x16_bf16 v[80:95],a[16:19],a[16:19],0
v_mfma_f32_16x16x32_bf16 v[216:219],a[236:239],v[60:63],v[216:219]
ds_read_b64 a[6:7],v25 offset:192
v_mul_f32_dpp v132,v15,v132 row_newbcast:0 row_mask:0xf bank_mask:0xf
v_accvgpr_write_b32 a40,v124
v_mul_f32_dpp v133,v15,v133 row_newbcast:1 row_mask:0xf bank_mask:0xf
v_mfma_f32_16x16x32_bf16 v[220:223],a[236:239],v[32:35],v[220:223]
ds_read_b64 a[8:9],v24 offset:8
v_mul_f32_dpp v134,v15,v134 row_newbcast:2 row_mask:0xf bank_mask:0xf
v_accvgpr_write_b32 a41,v125
v_mul_f32_dpp v135,v15,v135 row_newbcast:3 row_mask:0xf bank_mask:0xf
v_mfma_f32_16x16x32_bf16 v[188:191],a[244:247],v[32:35],v[188:191]
ds_read_b64 a[10:11],v24 offset:200
v_mul_f32_dpp v108,v15,v108 row_newbcast:4 row_mask:0xf bank_mask:0xf
v_accvgpr_write_b32 a42,v126
v_mul_f32_dpp v109,v15,v109 row_newbcast:5 row_mask:0xf bank_mask:0xf
v_mfma_f32_16x16x32_bf16 v[184:187],a[244:247],v[60:63],v[184:187]
ds_read_b64 a[12:13],v25 offset:8
v_mul_f32_dpp v110,v15,v110 row_newbcast:6 row_mask:0xf bank_mask:0xf
v_accvgpr_write_b32 a43,v127
v_mul_f32_dpp v111,v15,v111 row_newbcast:7 row_mask:0xf bank_mask:0xf
v_mfma_f32_16x16x32_bf16 v[224:227],a[248:251],v[60:63],v[224:227]
ds_read_b64 a[14:15],v25 offset:200
v_cvt_pk_bf16_f32 v132,v132,v133
v_cvt_pk_bf16_f32 v133,v134,v135
v_cvt_pk_bf16_f32 v134,v108,v109
v_mfma_f32_16x16x32_bf16 v[228:231],a[248:251],v[32:35],v[228:231]
v_cvt_pk_bf16_f32 v135,v110,v111
v_mfma_f32_32x32x16_bf16 v[96:111],a[16:19],a[16:19],0
v_accvgpr_write_b32 a44,v132
v_mfma_f32_16x16x32_bf16 v[204:207],a[252:255],v[32:35],v[204:207]
v_accvgpr_write_b32 a45,v133
v_accvgpr_write_b32 a46,v134
v_accvgpr_write_b32 a47,v135
v_mfma_f32_16x16x32_bf16 v[200:203],a[252:255],v[60:63],v[200:203]
v_mfma_f32_32x32x16_bf16 v[112:127],a[16:19],a[16:19],0
v_mov_b64 v[44:45],0
v_mov_b64 v[46:47],0
ds_read_b64 a[16:17],v26 offset:0
ds_read_b64 a[18:19],v26 offset:192
ds_read_b64 a[20:21],v27 offset:0
ds_read_b64 a[22:23],v27 offset:192
s_waitcnt lgkmcnt(8)
v_smfmac_f32_16x16x64_bf16 v[64:67],a[96:99],a[0:7],v10
s_waitcnt lgkmcnt(4)
ds_read_b64 a[0:1],v26 offset:8
ds_read_b64 a[2:3],v26 offset:200
ds_read_b64 a[4:5],v27 offset:8
ds_read_b64 a[6:7],v27 offset:200
v_smfmac_f32_16x16x64_bf16 v[68:71],a[100:103],a[8:15],v10
s_waitcnt lgkmcnt(4)
ds_read_b64 a[8:9],v24 offset:64
ds_read_b64 a[10:11],v24 offset:256
ds_read_b64 a[12:13],v25 offset:64
ds_read_b64 a[14:15],v25 offset:256
v_smfmac_f32_16x16x64_bf16 v[72:75],a[104:107],a[16:23],v10
s_waitcnt lgkmcnt(4)
buffer_store_dwordx4 v[156:159],v7,s[40:43],s8 offen offset:320 sc1
ds_read_b64 a[16:17],v24 offset:72
ds_read_b64 a[18:19],v24 offset:264
ds_read_b64 a[20:21],v25 offset:72
ds_read_b64 a[22:23],v25 offset:264
v_and_b32 v60,8,v11
v_pk_mul_f32 v[32:33],v[64:65],v[64:65]
v_pk_mul_f32 v[34:35],v[66:67],v[66:67]
v_smfmac_f32_16x16x64_bf16 v[76:79],a[108:111],a[0:7],v10
s_waitcnt lgkmcnt(4)
ds_read_b64 a[0:1],v26 offset:64
ds_read_b64 a[2:3],v26 offset:256
ds_read_b64 a[4:5],v27 offset:64
ds_read_b64 a[6:7],v27 offset:256
v_mul_u32_u24 v60,884,v60
s_add_u32 s64,s60,3536
v_pk_add_f32 v[48:49],v[64:65],v[68:69]
v_pk_fma_f32 v[32:33],v[68:69],v[68:69],v[32:33]
v_pk_add_f32 v[50:51],v[66:67],v[70:71]
v_pk_fma_f32 v[34:35],v[70:71],v[70:71],v[34:35]
v_smfmac_f32_16x16x64_bf16 v[80:83],a[128:131],a[8:15],v10
s_waitcnt lgkmcnt(4)
ds_read_b64 a[8:9],v26 offset:72
ds_read_b64 a[10:11],v26 offset:264
ds_read_b64 a[12:13],v27 offset:72
ds_read_b64 a[14:15],v27 offset:264
v_add_u32 v63,s64,v60
v_pk_add_f32 v[48:49],v[48:49],v[72:73]
v_pk_fma_f32 v[32:33],v[72:73],v[72:73],v[32:33]
v_pk_add_f32 v[50:51],v[50:51],v[74:75]
v_pk_fma_f32 v[34:35],v[74:75],v[74:75],v[34:35]
v_smfmac_f32_16x16x64_bf16 v[84:87],a[132:135],a[16:23],v10
s_waitcnt lgkmcnt(4)
ds_read_b64 a[16:17],v24 offset:128
ds_read_b64 a[18:19],v24 offset:320
ds_read_b64 a[20:21],v25 offset:128
ds_read_b64 a[22:23],v25 offset:320
v_subrev_u32 v8,88400,v63
v_pk_add_f32 v[48:49],v[48:49],v[76:77]
v_pk_fma_f32 v[32:33],v[76:77],v[76:77],v[32:33]
v_pk_add_f32 v[50:51],v[50:51],v[78:79]
v_pk_fma_f32 v[34:35],v[78:79],v[78:79],v[34:35]
v_smfmac_f32_16x16x64_bf16 v[88:91],a[136:139],a[0:7],v10
s_waitcnt lgkmcnt(4)
ds_read_b64 a[0:1],v24 offset:136
ds_read_b64 a[2:3],v24 offset:328
ds_read_b64 a[4:5],v25 offset:136
ds_read_b64 a[6:7],v25 offset:328
v_min_u32 v63,v63,v8
v_pk_add_f32 v[48:49],v[48:49],v[80:81]
v_pk_fma_f32 v[32:33],v[80:81],v[80:81],v[32:33]
v_pk_add_f32 v[50:51],v[50:51],v[82:83]
v_pk_fma_f32 v[34:35],v[82:83],v[82:83],v[34:35]
v_smfmac_f32_16x16x64_bf16 v[92:95],a[140:143],a[8:15],v10
s_waitcnt lgkmcnt(4)
ds_read_b64 a[8:9],v26 offset:128
ds_read_b64 a[10:11],v26 offset:320
ds_read_b64 a[12:13],v27 offset:128
ds_read_b64 a[14:15],v27 offset:320
v_xad_u32 v24,v11,32,v28
v_xad_u32 v25,v11,40,v29
v_pk_add_f32 v[48:49],v[48:49],v[84:85]
v_pk_fma_f32 v[32:33],v[84:85],v[84:85],v[32:33]
v_pk_add_f32 v[50:51],v[50:51],v[86:87]
v_pk_fma_f32 v[34:35],v[86:87],v[86:87],v[34:35]
v_smfmac_f32_16x16x64_bf16 v[96:99],a[160:163],a[16:23],v10
s_waitcnt lgkmcnt(4)
ds_read_b64 a[16:17],v26 offset:136
ds_read_b64 a[18:19],v26 offset:328
ds_read_b64 a[20:21],v27 offset:136
ds_read_b64 a[22:23],v27 offset:328
v_add_u32 v62,3536,v63
v_pk_add_f32 v[48:49],v[48:49],v[88:89]
v_pk_fma_f32 v[32:33],v[88:89],v[88:89],v[32:33]
v_pk_add_f32 v[50:51],v[50:51],v[90:91]
v_pk_fma_f32 v[34:35],v[90:91],v[90:91],v[34:35]
v_smfmac_f32_16x16x64_bf16 v[100:103],a[164:167],a[0:7],v10
s_waitcnt lgkmcnt(4)
buffer_store_dwordx4 a[24:27],v7,s[40:43],s9 offen offset:0 sc1
ds_read_b64 a[0:1],v24 offset:0
ds_read_b64 a[2:3],v24 offset:192
ds_read_b64 a[4:5],v25 offset:0
ds_read_b64 a[6:7],v25 offset:192
v_xad_u32 v26,v11,48,v28
v_xad_u32 v27,v11,56,v29
v_pk_add_f32 v[48:49],v[48:49],v[92:93]
v_pk_fma_f32 v[32:33],v[92:93],v[92:93],v[32:33]
v_pk_add_f32 v[50:51],v[50:51],v[94:95]
v_pk_fma_f32 v[34:35],v[94:95],v[94:95],v[34:35]
v_smfmac_f32_16x16x64_bf16 v[104:107],a[168:171],a[8:15],v10
s_waitcnt lgkmcnt(4)
ds_read_b64 a[8:9],v24 offset:8
ds_read_b64 a[10:11],v24 offset:200
ds_read_b64 a[12:13],v25 offset:8
ds_read_b64 a[14:15],v25 offset:200
v_subrev_u32 v8,88400,v62
v_pk_add_f32 v[48:49],v[48:49],v[96:97]
v_pk_fma_f32 v[32:33],v[96:97],v[96:97],v[32:33]
v_pk_add_f32 v[50:51],v[50:51],v[98:99]
v_pk_fma_f32 v[34:35],v[98:99],v[98:99],v[34:35]
v_smfmac_f32_16x16x64_bf16 v[108:111],a[172:175],a[16:23],v10
s_waitcnt lgkmcnt(4)
ds_read_b64 a[16:17],v24 offset:64
ds_read_b64 a[18:19],v24 offset:256
ds_read_b64 a[20:21],v25 offset:64
ds_read_b64 a[22:23],v25 offset:256
v_min_u32 v62,v62,v8
v_pk_add_f32 v[48:49],v[48:49],v[100:101]
v_pk_fma_f32 v[32:33],v[100:101],v[100:101],v[32:33]
v_pk_add_f32 v[50:51],v[50:51],v[102:103]
v_pk_fma_f32 v[34:35],v[102:103],v[102:103],v[34:35]
v_smfmac_f32_16x16x64_bf16 v[112:115],a[112:115],a[0:7],v10
s_waitcnt lgkmcnt(4)
ds_read_b64 a[0:1],v24 offset:72
ds_read_b64 a[2:3],v24 offset:264
ds_read_b64 a[4:5],v25 offset:72
ds_read_b64 a[6:7],v25 offset:264
v_xor_b32 v9,0x80008,v12
v_pk_add_f32 v[48:49],v[48:49],v[104:105]
v_pk_fma_f32 v[32:33],v[104:105],v[104:105],v[32:33]
v_pk_add_f32 v[50:51],v[50:51],v[106:107]
v_pk_fma_f32 v[34:35],v[106:107],v[106:107],v[34:35]
v_mfma_f32_32x32x16_bf16 v[128:143],v[44:47],v[44:47],0
v_smfmac_f32_16x16x64_bf16 v[116:119],a[116:119],a[8:15],v10
s_waitcnt lgkmcnt(4)
ds_read_b64 a[8:9],v24 offset:128
ds_read_b64 a[10:11],v24 offset:320
ds_read_b64 a[12:13],v25 offset:128
ds_read_b64 a[14:15],v25 offset:320
v_add_u32_sdwa v60,v12,v63 dst_sel:DWORD dst_unused:UNUSED_PAD src0_sel:WORD_0 src1_sel:DWORD
v_pk_add_f32 v[48:49],v[48:49],v[108:109]
v_pk_fma_f32 v[32:33],v[108:109],v[108:109],v[32:33]
v_pk_add_f32 v[50:51],v[50:51],v[110:111]
v_pk_fma_f32 v[34:35],v[110:111],v[110:111],v[34:35]
v_smfmac_f32_16x16x64_bf16 v[120:123],a[144:147],a[16:23],v10
s_waitcnt lgkmcnt(4)
ds_read_b64 a[16:17],v24 offset:136
ds_read_b64 a[18:19],v24 offset:328
ds_read_b64 a[20:21],v25 offset:136
ds_read_b64 a[22:23],v25 offset:328
v_add_u32_sdwa v61,v12,v63 dst_sel:DWORD dst_unused:UNUSED_PAD src0_sel:WORD_1 src1_sel:DWORD
v_pk_add_f32 v[48:49],v[48:49],v[112:113]
v_pk_fma_f32 v[32:33],v[112:113],v[112:113],v[32:33]
v_pk_add_f32 v[50:51],v[50:51],v[114:115]
v_pk_fma_f32 v[34:35],v[114:115],v[114:115],v[34:35]
v_smfmac_f32_16x16x64_bf16 v[124:127],a[148:151],a[0:7],v10
s_waitcnt lgkmcnt(4)
buffer_store_dwordx4 a[28:31],v7,s[40:43],s9 offen offset:64 sc1
ds_read_b64 a[0:1],v26 offset:0
ds_read_b64 a[2:3],v26 offset:192
ds_read_b64 a[4:5],v27 offset:0
ds_read_b64 a[6:7],v27 offset:192
v_add_u32_sdwa v63,v9,v62 dst_sel:DWORD dst_unused:UNUSED_PAD src0_sel:WORD_1 src1_sel:DWORD
v_pk_add_f32 v[48:49],v[48:49],v[116:117]
v_pk_fma_f32 v[32:33],v[116:117],v[116:117],v[32:33]
v_pk_add_f32 v[50:51],v[50:51],v[118:119]
v_pk_fma_f32 v[34:35],v[118:119],v[118:119],v[34:35]
v_smfmac_f32_16x16x64_bf16 v[128:131],a[176:179],a[8:15],v10
s_waitcnt lgkmcnt(4)
ds_read_b64 a[8:9],v26 offset:8
ds_read_b64 a[10:11],v26 offset:200
ds_read_b64 a[12:13],v27 offset:8
ds_read_b64 a[14:15],v27 offset:200
v_add_u32_sdwa v62,v9,v62 dst_sel:DWORD dst_unused:UNUSED_PAD src0_sel:WORD_0 src1_sel:DWORD
v_pk_add_f32 v[48:49],v[48:49],v[120:121]
v_pk_fma_f32 v[32:33],v[120:121],v[120:121],v[32:33]
v_pk_add_f32 v[50:51],v[50:51],v[122:123]
v_pk_fma_f32 v[34:35],v[122:123],v[122:123],v[34:35]
v_mfma_f32_32x32x16_bf16 v[144:159],v[44:47],v[44:47],0
v_smfmac_f32_16x16x64_bf16 v[132:135],a[180:183],a[16:23],v10
s_waitcnt lgkmcnt(4)
ds_read_b64 a[16:17],v26 offset:64
ds_read_b64 a[18:19],v26 offset:256
ds_read_b64 a[20:21],v27 offset:64
ds_read_b64 a[22:23],v27 offset:256
v_pk_add_f32 v[48:49],v[48:49],v[124:125]
v_pk_fma_f32 v[32:33],v[124:125],v[124:125],v[32:33]
v_pk_add_f32 v[50:51],v[50:51],v[126:127]
v_pk_fma_f32 v[34:35],v[126:127],v[126:127],v[34:35]
v_smfmac_f32_16x16x64_bf16 v[136:139],a[120:123],a[0:7],v10
s_waitcnt lgkmcnt(4)
ds_read_b64 a[0:1],v26 offset:72
ds_read_b64 a[2:3],v26 offset:264
ds_read_b64 a[4:5],v27 offset:72
ds_read_b64 a[6:7],v27 offset:264
v_pk_add_f32 v[48:49],v[48:49],v[128:129]
v_pk_fma_f32 v[32:33],v[128:129],v[128:129],v[32:33]
v_pk_add_f32 v[50:51],v[50:51],v[130:131]
v_pk_fma_f32 v[34:35],v[130:131],v[130:131],v[34:35]
v_smfmac_f32_16x16x64_bf16 v[140:143],a[124:127],a[8:15],v10
s_waitcnt lgkmcnt(4)
ds_read_b64 a[8:9],v26 offset:128
ds_read_b64 a[10:11],v26 offset:320
ds_read_b64 a[12:13],v27 offset:128
ds_read_b64 a[14:15],v27 offset:320
v_pk_add_f32 v[48:49],v[48:49],v[132:133]
v_pk_fma_f32 v[32:33],v[132:133],v[132:133],v[32:33]
v_pk_add_f32 v[50:51],v[50:51],v[134:135]
v_pk_fma_f32 v[34:35],v[134:135],v[134:135],v[34:35]
v_smfmac_f32_16x16x64_bf16 v[144:147],a[152:155],a[16:23],v10
s_waitcnt lgkmcnt(4)
ds_read_b64 a[16:17],v26 offset:136
ds_read_b64 a[18:19],v26 offset:328
ds_read_b64 a[20:21],v27 offset:136
ds_read_b64 a[22:23],v27 offset:328
v_pk_add_f32 v[48:49],v[48:49],v[136:137]
v_pk_fma_f32 v[32:33],v[136:137],v[136:137],v[32:33]
v_pk_add_f32 v[50:51],v[50:51],v[138:139]
v_pk_fma_f32 v[34:35],v[138:139],v[138:139],v[34:35]
v_smfmac_f32_16x16x64_bf16 v[148:151],a[156:159],a[0:7],v10
s_waitcnt lgkmcnt(4)
ds_read_b64 a[228:229],v60 offset:57344
ds_read_b64 a[230:231],v60 offset:57352
ds_read_b64 a[232:233],v61 offset:57344
ds_read_b64 a[234:235],v61 offset:57352
ds_read_b128 a[248:251],v13 offset:0
v_pk_add_f32 v[48:49],v[48:49],v[140:141]
v_pk_fma_f32 v[32:33],v[140:141],v[140:141],v[32:33]
v_pk_add_f32 v[50:51],v[50:51],v[142:143]
v_pk_fma_f32 v[34:35],v[142:143],v[142:143],v[34:35]
v_smfmac_f32_16x16x64_bf16 v[152:155],a[184:187],a[8:15],v10
s_waitcnt lgkmcnt(5)
buffer_store_dwordx4 a[32:35],v7,s[40:43],s9 offen offset:128 sc1
ds_read_b64 a[236:237],v62 offset:57344
ds_read_b64 a[238:239],v62 offset:57352
ds_read_b64 a[244:245],v63 offset:57344
ds_read_b64 a[246:247],v63 offset:57352
ds_read_b128 a[252:255],v13 offset:256
v_pk_add_f32 v[48:49],v[48:49],v[144:145]
v_pk_fma_f32 v[32:33],v[144:145],v[144:145],v[32:33]
TP5419_%=:
s_cbranch_execz TR0_%=
s_nop 0
v_pk_add_f32 v[50:51],v[50:51],v[146:147]
v_pk_fma_f32 v[34:35],v[146:147],v[146:147],v[34:35]
v_smfmac_f32_16x16x64_bf16 v[156:159],a[188:191],a[16:23],v10
ds_read_b128 v[24:27],v4 offset:3584
ds_read_b128 v[28:31],v4 offset:3616
v_pk_add_f32 v[48:49],v[48:49],v[148:149]
v_pk_fma_f32 v[32:33],v[148:149],v[148:149],v[32:33]
v_pk_add_f32 v[50:51],v[50:51],v[150:151]
v_pk_fma_f32 v[34:35],v[150:151],v[150:151],v[34:35]
v_pk_add_f32 v[48:49],v[48:49],v[152:153]
v_pk_fma_f32 v[32:33],v[152:153],v[152:153],v[32:33]
v_pk_add_f32 v[50:51],v[50:51],v[154:155]
v_pk_fma_f32 v[34:35],v[154:155],v[154:155],v[34:35]
v_pk_add_f32 v[48:49],v[48:49],v[156:157]
v_pk_fma_f32 v[32:33],v[156:157],v[156:157],v[32:33]
v_pk_add_f32 v[50:51],v[50:51],v[158:159]
v_pk_fma_f32 v[34:35],v[158:159],v[158:159],v[34:35]
s_nop 0
v_permlane32_swap_b32 v48,v50
v_permlane32_swap_b32 v49,v51
v_permlane32_swap_b32 v32,v34
v_permlane32_swap_b32_e64 v33,v35
v_pk_add_f32 v[48:49],v[48:49],v[50:51]
v_pk_add_f32 v[32:33],v[32:33],v[34:35]
s_add_u32 s60,s60,56576
s_sub_u32 s64,s60,88400
s_min_u32 s60,s60,s64
v_permlane16_swap_b32 v48,v49
v_permlane16_swap_b32 v32,v33
v_add_f32 v8,v48,v49
v_add_f32_e64 v9,v32,v33
v_mul_f32 v8,0x3c2aaaab,v8
v_mul_f32 v9,0x3c2aaaab,v9
v_cmp_ngt_f32 vcc,0x5d800000,v9
v_fma_f32 v9,-v8,v8,v9
v_max_f32 v9,0,v9
s_or_b64 s[90:91],s[90:91],vcc
v_add_f32 v9,0x3727c5ac,v9
v_rsq_f32 v9,v9
s_nop 0
v_mul_f32 v48,s96,v9
v_mul_f32 v49,s96,v9
v_mul_f32 v32,-v8,v48
v_mul_f32 v33,-v8,v48
v_permlane16_swap_b32 v48,v49
s_nop 0
v_permlane16_swap_b32 v32,v33
v_mov_b64 v[50:51],v[48:49]
v_mov_b64 v[34:35],v[32:33]
s_nop 0
v_permlane32_swap_b32 v48,v50
v_permlane32_swap_b32 v49,v51
v_permlane32_swap_b32 v32,v34
v_permlane32_swap_b32 v33,v35
v_pk_fma_f32 v[64:65],v[64:65],v[48:49],v[32:33]
v_pk_fma_f32 v[66:67],v[66:67],v[50:51],v[34:35]
v_pk_fma_f32 v[68:69],v[68:69],v[48:49],v[32:33]
v_pk_fma_f32 v[70:71],v[70:71],v[50:51],v[34:35]
v_pk_fma_f32 v[72:73],v[72:73],v[48:49],v[32:33]
v_pk_fma_f32 v[74:75],v[74:75],v[50:51],v[34:35]
v_pk_fma_f32 v[76:77],v[76:77],v[48:49],v[32:33]
v_pk_fma_f32 v[78:79],v[78:79],v[50:51],v[34:35]
v_pk_fma_f32 v[112:113],v[112:113],v[48:49],v[32:33]
v_pk_fma_f32 v[114:115],v[114:115],v[50:51],v[34:35]
v_pk_fma_f32 v[116:117],v[116:117],v[48:49],v[32:33]
v_pk_fma_f32 v[118:119],v[118:119],v[50:51],v[34:35]
v_pk_fma_f32 v[136:137],v[136:137],v[48:49],v[32:33]
v_pk_fma_f32 v[138:139],v[138:139],v[50:51],v[34:35]
v_pk_fma_f32 v[140:141],v[140:141],v[48:49],v[32:33]
v_pk_fma_f32 v[142:143],v[142:143],v[50:51],v[34:35]
v_pk_fma_f32 v[80:81],v[80:81],v[48:49],v[32:33]
v_pk_fma_f32 v[82:83],v[82:83],v[50:51],v[34:35]
v_pk_fma_f32 v[84:85],v[84:85],v[48:49],v[32:33]
v_pk_fma_f32 v[86:87],v[86:87],v[50:51],v[34:35]
v_pk_fma_f32 v[88:89],v[88:89],v[48:49],v[32:33]
v_pk_fma_f32 v[90:91],v[90:91],v[50:51],v[34:35]
v_pk_fma_f32 v[92:93],v[92:93],v[48:49],v[32:33]
v_pk_fma_f32 v[94:95],v[94:95],v[50:51],v[34:35]
v_pk_fma_f32 v[120:121],v[120:121],v[48:49],v[32:33]
v_pk_fma_f32 v[122:123],v[122:123],v[50:51],v[34:35]
v_pk_fma_f32 v[124:125],v[124:125],v[48:49],v[32:33]
v_pk_fma_f32 v[126:127],v[126:127],v[50:51],v[34:35]
v_pk_fma_f32 v[144:145],v[144:145],v[48:49],v[32:33]
v_pk_fma_f32 v[146:147],v[146:147],v[50:51],v[34:35]
v_pk_fma_f32 v[148:149],v[148:149],v[48:49],v[32:33]
v_pk_fma_f32 v[150:151],v[150:151],v[50:51],v[34:35]
v_pk_fma_f32 v[96:97],v[96:97],v[48:49],v[32:33]
v_pk_fma_f32 v[98:99],v[98:99],v[50:51],v[34:35]
v_pk_fma_f32 v[100:101],v[100:101],v[48:49],v[32:33]
v_pk_fma_f32 v[102:103],v[102:103],v[50:51],v[34:35]
v_pk_fma_f32 v[104:105],v[104:105],v[48:49],v[32:33]
v_pk_fma_f32 v[106:107],v[106:107],v[50:51],v[34:35]
v_pk_fma_f32 v[108:109],v[108:109],v[48:49],v[32:33]
v_pk_fma_f32 v[110:111],v[110:111],v[50:51],v[34:35]
v_pk_fma_f32 v[128:129],v[128:129],v[48:49],v[32:33]
v_pk_fma_f32 v[130:131],v[130:131],v[50:51],v[34:35]
v_pk_fma_f32 v[132:133],v[132:133],v[48:49],v[32:33]
v_pk_fma_f32 v[134:135],v[134:135],v[50:51],v[34:35]
v_pk_fma_f32 v[152:153],v[152:153],v[48:49],v[32:33]
v_pk_fma_f32 v[154:155],v[154:155],v[50:51],v[34:35]
v_pk_fma_f32 v[156:157],v[156:157],v[48:49],v[32:33]
v_pk_fma_f32 v[158:159],v[158:159],v[50:51],v[34:35]
v_mfma_f32_16x16x32_bf16 v[44:47],a[220:223],a[56:59],v[16:19]
buffer_store_dwordx4 a[36:39],v7,s[40:43],s9 offen offset:192 sc1
v_cvt_pk_bf16_f32 v64,v64,v68
v_cvt_pk_bf16_f32 v72,v72,v76
v_cvt_pk_bf16_f32 v112,v112,v116
v_lshlrev_b32_sdwa v36,1,v36 dst_sel:DWORD dst_unused:UNUSED_PAD src0_sel:DWORD src1_sel:WORD_1
v_mfma_f32_16x16x32_bf16 v[44:47],a[216:219],a[72:75],v[44:47]
ds_read_u16 v36,v36
v_accvgpr_write_b32 a0,v64
v_accvgpr_write_b32 a1,v72
v_cvt_pk_bf16_f32 v136,v136,v140
v_accvgpr_write_b32 a2,v112
v_mfma_f32_16x16x32_bf16 v[44:47],a[192:195],a[88:91],v[44:47]
ds_read_b64_tr_b16 a[204:205],v1 offset:0
v_cvt_pk_bf16_f32 v65,v65,v69
v_accvgpr_write_b32 a3,v136
v_cvt_pk_bf16_f32 v73,v73,v77
v_accvgpr_write_b32 a4,v65
v_mfma_f32_16x16x32_bf16 v[48:51],a[196:199],a[88:91],v[20:23]
ds_read_b64_tr_b16 a[206:207],v1 offset:1024
v_cvt_pk_bf16_f32 v113,v113,v117
v_accvgpr_write_b32 a5,v73
v_cvt_pk_bf16_f32 v137,v137,v141
v_lshlrev_b32_sdwa v38,1,v38 dst_sel:DWORD dst_unused:UNUSED_PAD src0_sel:DWORD src1_sel:WORD_1
v_accvgpr_write_b32 a6,v113
v_mfma_f32_16x16x32_bf16 v[48:51],a[200:203],a[72:75],v[48:51]
ds_read_u16_d16_hi v38,v38
v_cvt_pk_bf16_f32 v66,v66,v70
v_accvgpr_write_b32 a7,v137
v_cvt_pk_bf16_f32 v74,v74,v78
v_accvgpr_write_b32 a8,v66
v_mfma_f32_16x16x32_bf16 v[48:51],a[212:215],a[56:59],v[48:51]
ds_read_b64_tr_b16 a[208:209],v1 offset:2048
v_accvgpr_write_b32 a9,v74
v_cvt_pk_bf16_f32 v114,v114,v118
v_accvgpr_write_b32 a10,v114
v_lshlrev_b32_sdwa v37,1,v37 dst_sel:DWORD dst_unused:UNUSED_PAD src0_sel:DWORD src1_sel:WORD_1
v_cvt_pk_bf16_f32 v138,v138,v142
v_mfma_f32_16x16x32_bf16 v[32:35],a[212:215],a[60:63],v[20:23]
buffer_store_dwordx4 a[40:43],v7,s[40:43],s9 offen offset:256 sc1
v_accvgpr_write_b32 a11,v138
v_cvt_pk_bf16_f32 v67,v67,v71
v_accvgpr_write_b32 a12,v67
v_mfma_f32_16x16x32_bf16 v[32:35],a[200:203],a[76:79],v[32:35]
ds_read_u16 v37,v37
v_cvt_pk_bf16_f32 v75,v75,v79
v_accvgpr_write_b32 a13,v75
v_cvt_pk_bf16_f32 v115,v115,v119
v_accvgpr_write_b32 a14,v115
v_lshlrev_b32_sdwa v39,1,v39 dst_sel:DWORD dst_unused:UNUSED_PAD src0_sel:DWORD src1_sel:WORD_1
v_mfma_f32_16x16x32_bf16 v[32:35],a[196:199],a[92:95],v[32:35]
s_waitcnt lgkmcnt(6)
ds_read_u16_d16_hi v39,v39
v_cvt_pk_bf16_f32 v139,v139,v143
v_accvgpr_write_b32 a15,v139
v_cvt_pk_bf16_f32 v80,v80,v84
v_cvt_pk_bf16_f32 v88,v88,v92
v_mfma_f32_16x16x32_bf16 v[136:139],a[248:251],a[228:231],v[24:27]
s_nop 0
ds_read_b64_tr_b16 a[210:211],v1 offset:3072
v_cvt_pk_bf16_f32 v120,v120,v124
v_accvgpr_write_b32 a16,v80
v_accvgpr_write_b32 a17,v88
v_cvt_pk_bf16_f32 v144,v144,v148
v_lshlrev_b32_sdwa v40,1,v40 dst_sel:DWORD dst_unused:UNUSED_PAD src0_sel:DWORD src1_sel:WORD_1
v_mfma_f32_16x16x32_bf16 v[140:143],a[248:251],a[232:235],v[24:27]
ds_read_u16 v40,v40
v_accvgpr_write_b32 a18,v120
v_cvt_pk_bf16_f32 v81,v81,v85
v_accvgpr_write_b32 a19,v144
v_cvt_pk_bf16_f32 v89,v89,v93
v_mfma_f32_16x16x32_bf16 v[112:115],a[248:251],a[236:239],v[24:27]
v_accvgpr_write_b32 a20,v81
v_cvt_pk_bf16_f32 v121,v121,v125
v_accvgpr_write_b32 a21,v89
v_lshlrev_b32_sdwa v42,1,v42 dst_sel:DWORD dst_unused:UNUSED_PAD src0_sel:DWORD src1_sel:WORD_1
v_cvt_pk_bf16_f32 v145,v145,v149
v_mfma_f32_16x16x32_bf16 v[116:119],a[248:251],a[244:247],v[24:27]
ds_read_b128 a[248:251],v13 offset:512
ds_read_b128 v[24:27],v4 offset:3712
v_accvgpr_write_b32 a22,v121
v_cvt_pk_bf16_f32 v82,v82,v86
v_accvgpr_write_b32 a23,v145
v_mfma_f32_16x16x32_bf16 v[64:67],a[252:255],a[228:231],v[28:31]
ds_read_b64 a[228:229],v60 offset:57408
ds_read_b64 a[230:231],v60 offset:57416
v_cvt_pk_bf16_f32 v90,v90,v94
v_accvgpr_write_b32 a24,v82
v_accvgpr_write_b32 a25,v90
v_cvt_pk_bf16_f32 v122,v122,v126
v_lshlrev_b32_sdwa v41,1,v41 dst_sel:DWORD dst_unused:UNUSED_PAD src0_sel:DWORD src1_sel:WORD_1
v_mfma_f32_16x16x32_bf16 v[68:71],a[252:255],a[232:235],v[28:31]
ds_read_b64 a[232:233],v61 offset:57408
ds_read_b64 a[234:235],v61 offset:57416
v_accvgpr_write_b32 a26,v122
v_cvt_pk_bf16_f32 v146,v146,v150
v_accvgpr_write_b32 a27,v146
v_cvt_pk_bf16_f32 v83,v83,v87
v_mfma_f32_16x16x32_bf16 v[72:75],a[252:255],a[236:239],v[28:31]
ds_read_b64 a[236:237],v62 offset:57408
ds_read_b64 a[238:239],v62 offset:57416
v_accvgpr_write_b32 a28,v83
v_cvt_pk_bf16_f32 v91,v91,v95
v_accvgpr_write_b32 a29,v91
v_cvt_pk_bf16_f32 v123,v123,v127
v_lshlrev_b32_sdwa v43,1,v43 dst_sel:DWORD dst_unused:UNUSED_PAD src0_sel:DWORD src1_sel:WORD_1
v_mfma_f32_16x16x32_bf16 v[76:79],a[252:255],a[244:247],v[28:31]
s_waitcnt lgkmcnt(4)
buffer_store_dwordx4 a[44:47],v7,s[40:43],s9 offen offset:320 sc1
ds_read_b64 a[244:245],v63 offset:57408
v_accvgpr_write_b32 a30,v123
v_cvt_pk_bf16_f32 v147,v147,v151
v_accvgpr_write_b32 a31,v147
v_cvt_pk_bf16_f32 v96,v96,v100
v_mfma_f32_16x16x32_bf16 v[144:147],a[248:251],a[228:231],v[24:27]
s_waitcnt lgkmcnt(3)
ds_read_b64 a[246:247],v63 offset:57416
ds_read_b128 a[252:255],v13 offset:768
v_cvt_pk_bf16_f32 v104,v104,v108
v_cvt_pk_bf16_f32 v128,v128,v132
v_accvgpr_write_b32 a32,v96
v_accvgpr_write_b32 a33,v104
v_lshlrev_b32_sdwa v52,1,v52 dst_sel:DWORD dst_unused:UNUSED_PAD src0_sel:DWORD src1_sel:WORD_1
v_mfma_f32_16x16x32_bf16 v[148:151],a[248:251],a[232:235],v[24:27]
s_waitcnt lgkmcnt(3)
ds_read_b128 v[28:31],v4 offset:3744
ds_read_u16_d16_hi v42,v42
v_cvt_pk_bf16_f32 v152,v152,v156
v_accvgpr_write_b32 a34,v128
v_cvt_pk_bf16_f32 v97,v97,v101
v_mfma_f32_16x16x32_bf16 v[120:123],a[248:251],a[236:239],v[24:27]
s_waitcnt lgkmcnt(3)
ds_read_u16 v41,v41
v_accvgpr_write_b32 a35,v152
v_cvt_pk_bf16_f32 v105,v105,v109
v_accvgpr_write_b32 a36,v97
v_cvt_pk_bf16_f32 v129,v129,v133
v_lshlrev_b32_sdwa v54,1,v54 dst_sel:DWORD dst_unused:UNUSED_PAD src0_sel:DWORD src1_sel:WORD_1
v_mfma_f32_16x16x32_bf16 v[124:127],a[248:251],a[244:247],v[24:27]
s_waitcnt lgkmcnt(2)
ds_read_b128 a[248:251],v13 offset:1024
ds_read_b128 v[24:27],v4 offset:3840
v_accvgpr_write_b32 a37,v105
v_cvt_pk_bf16_f32 v153,v153,v157
v_accvgpr_write_b32 a38,v129
v_cvt_pk_bf16_f32 v98,v98,v102
v_mfma_f32_16x16x32_bf16 v[80:83],a[252:255],a[228:231],v[28:31]
s_nop 0
ds_read_b64 a[228:229],v60 offset:57472
ds_read_b64 a[230:231],v60 offset:57480
v_accvgpr_write_b32 a39,v153
v_cvt_pk_bf16_f32 v106,v106,v110
v_accvgpr_write_b32 a40,v98
v_accvgpr_write_b32 a41,v106
v_lshlrev_b32_sdwa v53,1,v53 dst_sel:DWORD dst_unused:UNUSED_PAD src0_sel:DWORD src1_sel:WORD_1
v_mfma_f32_16x16x32_bf16 v[84:87],a[252:255],a[232:235],v[28:31]
ds_read_b64 a[232:233],v61 offset:57472
ds_read_b64 a[234:235],v61 offset:57480
v_cvt_pk_bf16_f32 v130,v130,v134
v_accvgpr_write_b32 a42,v130
v_cvt_pk_bf16_f32 v154,v154,v158
v_accvgpr_write_b32 a43,v154
v_mfma_f32_16x16x32_bf16 v[88:91],a[252:255],a[236:239],v[28:31]
s_nop 0
ds_read_b64 a[236:237],v62 offset:57472
ds_read_b64 a[238:239],v62 offset:57480
v_cvt_pk_bf16_f32 v99,v99,v103
v_accvgpr_write_b32 a44,v99
v_cvt_pk_bf16_f32 v107,v107,v111
v_accvgpr_write_b32 a45,v107
v_mfma_f32_16x16x32_bf16 v[92:95],a[252:255],a[244:247],v[28:31]
s_waitcnt lgkmcnt(4)
ds_read_b64 a[244:245],v63 offset:57472
ds_read_b64 a[246:247],v63 offset:57480
v_cvt_pk_bf16_f32 v131,v131,v135
v_accvgpr_write_b32 a46,v131
v_cvt_pk_bf16_f32 v155,v155,v159
v_lshlrev_b32_sdwa v55,1,v55 dst_sel:DWORD dst_unused:UNUSED_PAD src0_sel:DWORD src1_sel:WORD_1
v_accvgpr_write_b32 a47,v155
v_mfma_f32_16x16x32_bf16 v[152:155],a[248:251],a[228:231],v[24:27]
s_waitcnt lgkmcnt(4)
ds_read_b128 a[252:255],v13 offset:1280
ds_read_b128 v[28:31],v4 offset:3872
v_mfma_f32_16x16x32_bf16 v[156:159],a[248:251],a[232:235],v[24:27]
s_waitcnt lgkmcnt(4)
ds_read_u16_d16_hi v43,v43
ds_read_u16 v52,v52
v_lshlrev_b32_sdwa v56,1,v56 dst_sel:DWORD dst_unused:UNUSED_PAD src0_sel:DWORD src1_sel:WORD_1
v_mfma_f32_16x16x32_bf16 v[128:131],a[248:251],a[236:239],v[24:27]
s_waitcnt lgkmcnt(4)
ds_read_u16_d16_hi v54,v54
v_lshlrev_b32_sdwa v58,1,v58 dst_sel:DWORD dst_unused:UNUSED_PAD src0_sel:DWORD src1_sel:WORD_1
v_mfma_f32_16x16x32_bf16 v[132:135],a[248:251],a[244:247],v[24:27]
s_waitcnt lgkmcnt(3)
ds_read_b64_tr_b16 a[248:249],v3 offset:64
ds_read_b64_tr_b16 a[250:251],v3 offset:1600
v_mfma_f32_16x16x32_bf16 v[96:99],a[252:255],a[228:231],v[28:31]
ds_read_b64_tr_b16 a[228:229],v3 offset:72
ds_read_b64_tr_b16 a[230:231],v3 offset:1608
v_mfma_f32_16x16x32_bf16 v[100:103],a[252:255],a[232:235],v[28:31]
ds_read_b64_tr_b16 a[232:233],v3 offset:128
ds_read_b64_tr_b16 a[234:235],v3 offset:1664
v_lshlrev_b32_sdwa v57,1,v57 dst_sel:DWORD dst_unused:UNUSED_PAD src0_sel:DWORD src1_sel:WORD_1
v_mfma_f32_16x16x32_bf16 v[104:107],a[252:255],a[236:239],v[28:31]
ds_read_b64_tr_b16 a[236:237],v3 offset:136
ds_read_b64_tr_b16 a[238:239],v3 offset:1672
v_mfma_f32_16x16x32_bf16 v[108:111],a[252:255],a[244:247],v[28:31]
ds_read_u16 v53,v53
ds_read_u16_d16_hi v55,v55
v_lshlrev_b32_sdwa v59,1,v59 dst_sel:DWORD dst_unused:UNUSED_PAD src0_sel:DWORD src1_sel:WORD_1
v_mfma_f32_16x16x32_bf16 v[60:63],a[192:195],a[92:95],v[16:19]
ds_read_u16 v56,v56
v_mfma_f32_16x16x32_bf16 v[60:63],a[216:219],a[76:79],v[60:63]
s_waitcnt vmcnt(18)
ds_read_u16_d16_hi v58,v58
v_mfma_f32_16x16x32_bf16 v[60:63],a[220:223],a[60:63],v[60:63]
s_barrier
ds_read_u16 v57,v57
ds_read_u16_d16_hi v59,v59
v_mfma_f32_16x16x32_bf16 v[24:27],a[220:223],a[0:3],v[16:19]
s_add_u32 m0,s57,24752
v_lshlrev_b32_sdwa v44,1,v44 dst_sel:DWORD dst_unused:UNUSED_PAD src0_sel:DWORD src1_sel:WORD_1
v_lshlrev_b32_sdwa v46,1,v46 dst_sel:DWORD dst_unused:UNUSED_PAD src0_sel:DWORD src1_sel:WORD_1
v_mfma_f32_16x16x32_bf16 v[24:27],a[216:219],a[16:19],v[24:27]
s_add_u32 s89,s56,5603328
ds_read_u16 v44,v44
v_lshl_add_u64 v[36:37],v[36:37],0,v[38:39]
v_mfma_f32_16x16x32_bf16 v[24:27],a[192:195],a[32:35],v[24:27]
s_waitcnt lgkmcnt(11)
buffer_load_dwordx4 v6,s[36:39],s89 offen lds
v_lshl_add_u64 v[38:39],v[40:41],0,v[42:43]
v_mfma_f32_16x16x32_bf16 v[28:31],a[196:199],a[32:35],v[20:23]
ds_read_u16_d16_hi v46,v46
v_lshlrev_b32_sdwa v45,1,v45 dst_sel:DWORD dst_unused:UNUSED_PAD src0_sel:DWORD src1_sel:WORD_1
v_mfma_f32_16x16x32_bf16 v[28:31],a[200:203],a[16:19],v[28:31]
s_waitcnt lgkmcnt(6)
ds_read_u16 v45,v45
v_lshl_add_u64 v[40:41],v[52:53],0,v[54:55]
v_mfma_f32_16x16x32_bf16 v[28:31],a[212:215],a[0:3],v[28:31]
v_lshlrev_b32_sdwa v47,1,v47 dst_sel:DWORD dst_unused:UNUSED_PAD src0_sel:DWORD src1_sel:WORD_1
v_lshlrev_b32_sdwa v48,1,v48 dst_sel:DWORD dst_unused:UNUSED_PAD src0_sel:DWORD src1_sel:WORD_1
v_mfma_f32_16x16x32_bf16 v[52:55],a[212:215],a[4:7],v[20:23]
s_nop 0
ds_read_u16_d16_hi v47,v47
v_lshlrev_b32_sdwa v50,1,v50 dst_sel:DWORD dst_unused:UNUSED_PAD src0_sel:DWORD src1_sel:WORD_1
v_mfma_f32_16x16x32_bf16 v[52:55],a[200:203],a[20:23],v[52:55]
s_waitcnt lgkmcnt(4)
ds_read_u16 v48,v48
v_lshl_add_u64 v[42:43],v[56:57],0,v[58:59]
v_mfma_f32_16x16x32_bf16 v[52:55],a[196:199],a[36:39],v[52:55]
ds_read_u16_d16_hi v50,v50
v_lshlrev_b32_sdwa v49,1,v49 dst_sel:DWORD dst_unused:UNUSED_PAD src0_sel:DWORD src1_sel:WORD_1
v_mfma_f32_16x16x32_bf16 v[56:59],a[192:195],a[36:39],v[16:19]
ds_read_u16 v49,v49
v_lshlrev_b32_sdwa v51,1,v51 dst_sel:DWORD dst_unused:UNUSED_PAD src0_sel:DWORD src1_sel:WORD_1
v_mfma_f32_16x16x32_bf16 v[56:59],a[216:219],a[20:23],v[56:59]
ds_read_u16_d16_hi v51,v51
v_lshlrev_b32_sdwa v60,1,v60 dst_sel:DWORD dst_unused:UNUSED_PAD src0_sel:DWORD src1_sel:WORD_1
v_mfma_f32_16x16x32_bf16 v[56:59],a[220:223],a[4:7],v[56:59]
ds_read_u16 v60,v60
v_lshlrev_b32_sdwa v62,1,v62 dst_sel:DWORD dst_unused:UNUSED_PAD src0_sel:DWORD src1_sel:WORD_1
v_mfma_f32_16x16x32_bf16 v[232:235],a[240:243],v[36:39],v[232:235]
ds_read_u16_d16_hi v62,v62
v_lshlrev_b32_sdwa v61,1,v61 dst_sel:DWORD dst_unused:UNUSED_PAD src0_sel:DWORD src1_sel:WORD_1
v_mfma_f32_16x16x32_bf16 v[236:239],a[240:243],v[40:43],v[236:239]
ds_read_u16 v61,v61
v_lshlrev_b32_sdwa v63,1,v63 dst_sel:DWORD dst_unused:UNUSED_PAD src0_sel:DWORD src1_sel:WORD_1
v_mfma_f32_16x16x32_bf16 v[164:167],a[224:227],v[40:43],v[164:167]
ds_read_u16_d16_hi v63,v63
v_lshlrev_b32_sdwa v32,1,v32 dst_sel:DWORD dst_unused:UNUSED_PAD src0_sel:DWORD src1_sel:WORD_1
v_mfma_f32_16x16x32_bf16 v[160:163],a[224:227],v[36:39],v[160:163]
ds_read_u16 v32,v32
v_lshlrev_b32_sdwa v34,1,v34 dst_sel:DWORD dst_unused:UNUSED_PAD src0_sel:DWORD src1_sel:WORD_1
v_mfma_f32_16x16x32_bf16 v[240:243],a[248:251],v[36:39],v[240:243]
ds_read_u16_d16_hi v34,v34
v_lshlrev_b32_sdwa v33,1,v33 dst_sel:DWORD dst_unused:UNUSED_PAD src0_sel:DWORD src1_sel:WORD_1
v_mfma_f32_16x16x32_bf16 v[244:247],a[248:251],v[40:43],v[244:247]
ds_read_u16 v33,v33
v_lshlrev_b32_sdwa v35,1,v35 dst_sel:DWORD dst_unused:UNUSED_PAD src0_sel:DWORD src1_sel:WORD_1
v_mfma_f32_16x16x32_bf16 v[180:183],a[228:231],v[40:43],v[180:183]
ds_read_u16_d16_hi v35,v35
v_mfma_f32_16x16x32_bf16 v[176:179],a[228:231],v[36:39],v[176:179]
v_mfma_f32_16x16x32_bf16 v[248:251],a[232:235],v[36:39],v[248:251]
v_mfma_f32_16x16x32_bf16 v[252:255],a[232:235],v[40:43],v[252:255]
v_mfma_f32_16x16x32_bf16 v[196:199],a[236:239],v[40:43],v[196:199]
v_mfma_f32_16x16x32_bf16 v[192:195],a[236:239],v[36:39],v[192:195]
v_mfma_f32_16x16x32_bf16 v[40:43],a[220:223],a[8:11],v[16:19]
v_lshlrev_b32_sdwa v24,1,v24 dst_sel:DWORD dst_unused:UNUSED_PAD src0_sel:DWORD src1_sel:WORD_1
v_fma_mixlo_f16 v8,v53,s99,0
v_mfma_f32_16x16x32_bf16 v[40:43],a[216:219],a[24:27],v[40:43]
s_waitcnt lgkmcnt(2)
ds_read_u16 v24,v24
v_lshl_add_u64 v[44:45],v[44:45],0,v[46:47]
v_mfma_f32_16x16x32_bf16 v[40:43],a[192:195],a[40:43],v[40:43]
v_lshl_add_u64 v[46:47],v[48:49],0,v[50:51]
v_lshlrev_b32_sdwa v26,1,v26 dst_sel:DWORD dst_unused:UNUSED_PAD src0_sel:DWORD src1_sel:WORD_1
v_mfma_f32_16x16x32_bf16 v[36:39],a[196:199],a[40:43],v[20:23]
ds_read_u16_d16_hi v26,v26
v_fma_mixhi_f16 v8,v55,s99,0
v_mfma_f32_16x16x32_bf16 v[36:39],a[200:203],a[24:27],v[36:39]
v_lshl_add_u64 v[48:49],v[60:61],0,v[62:63]
v_lshlrev_b32_sdwa v25,1,v25 dst_sel:DWORD dst_unused:UNUSED_PAD src0_sel:DWORD src1_sel:WORD_1
v_pk_fma_f16 v9,v8,v8,s54
v_mfma_f32_16x16x32_bf16 v[36:39],a[212:215],a[8:11],v[36:39]
ds_read_u16 v25,v25
v_lshlrev_b32_sdwa v27,1,v27 dst_sel:DWORD dst_unused:UNUSED_PAD src0_sel:DWORD src1_sel:WORD_1
v_mfma_f32_16x16x32_bf16 v[60:63],a[212:215],a[12:15],v[20:23]
ds_read_b128 v[20:23],v4 offset:128
ds_read_b64_tr_b16 a[212:213],v1 offset:4096
v_pk_mul_f16 v9,v8,v9
v_mfma_f32_16x16x32_bf16 v[60:63],a[200:203],a[28:31],v[60:63]
s_waitcnt lgkmcnt(5)
ds_read_b64_tr_b16 a[214:215],v1 offset:5120
ds_read_b64_tr_b16 a[200:201],v2 offset:4096
v_lshl_add_u64 v[50:51],v[32:33],0,v[34:35]
v_lshlrev_b32_sdwa v28,1,v28 dst_sel:DWORD dst_unused:UNUSED_PAD src0_sel:DWORD src1_sel:WORD_1
v_mfma_f32_16x16x32_bf16 v[60:63],a[196:199],a[44:47],v[60:63]
ds_read_b64_tr_b16 a[202:203],v2 offset:5120
ds_read_b64_tr_b16 a[196:197],v2 offset:2048
v_exp_f16 v8,v9
v_mfma_f32_16x16x32_bf16 v[32:35],a[192:195],a[44:47],v[16:19]
ds_read_b128 v[16:19],v4 offset:160
ds_read_b64_tr_b16 a[198:199],v2 offset:3072
v_lshlrev_b32_sdwa v30,1,v30 dst_sel:DWORD dst_unused:UNUSED_PAD src0_sel:DWORD src1_sel:WORD_1
v_mfma_f32_16x16x32_bf16 v[32:35],a[216:219],a[28:31],v[32:35]
ds_read_b64_tr_b16 a[192:193],v2 offset:0
ds_read_b64_tr_b16 a[194:195],v2 offset:1024
v_lshlrev_b32_sdwa v29,1,v29 dst_sel:DWORD dst_unused:UNUSED_PAD src0_sel:DWORD src1_sel:WORD_1
v_mfma_f32_16x16x32_bf16 v[32:35],a[220:223],a[12:15],v[32:35]
ds_read_u16_d16_hi v27,v27
v_exp_f16_sdwa v8,v9 dst_sel:WORD_1 dst_unused:UNUSED_PRESERVE src0_sel:WORD_1
v_mfma_f32_16x16x32_bf16 v[208:211],a[240:243],v[44:47],v[208:211]
ds_read_u16 v28,v28
v_lshlrev_b32_sdwa v31,1,v31 dst_sel:DWORD dst_unused:UNUSED_PAD src0_sel:DWORD src1_sel:WORD_1
v_mfma_f32_16x16x32_bf16 v[212:215],a[240:243],v[48:51],v[212:215]
ds_read_u16_d16_hi v30,v30
v_lshlrev_b32_sdwa v56,1,v56 dst_sel:DWORD dst_unused:UNUSED_PAD src0_sel:DWORD src1_sel:WORD_1
v_mfma_f32_16x16x32_bf16 v[172:175],a[224:227],v[48:51],v[172:175]
ds_read_u16 v29,v29
v_pk_fma_f16 v8,v8,s55,s55
v_mfma_f32_16x16x32_bf16 v[168:171],a[224:227],v[44:47],v[168:171]
v_rcp_f16 v9,v8
v_lshlrev_b32_sdwa v58,1,v58 dst_sel:DWORD dst_unused:UNUSED_PAD src0_sel:DWORD src1_sel:WORD_1
v_mfma_f32_16x16x32_bf16 v[216:219],a[248:251],v[44:47],v[216:219]
ds_read_u16_d16_hi v31,v31
v_lshlrev_b32_sdwa v57,1,v57 dst_sel:DWORD dst_unused:UNUSED_PAD src0_sel:DWORD src1_sel:WORD_1
v_mfma_f32_16x16x32_bf16 v[220:223],a[248:251],v[48:51],v[220:223]
ds_read_u16 v56,v56
v_lshlrev_b32_sdwa v59,1,v59 dst_sel:DWORD dst_unused:UNUSED_PAD src0_sel:DWORD src1_sel:WORD_1
v_mfma_f32_16x16x32_bf16 v[188:191],a[228:231],v[48:51],v[188:191]
ds_read_u16_d16_hi v58,v58
v_lshlrev_b32_sdwa v52,1,v52 dst_sel:DWORD dst_unused:UNUSED_PAD src0_sel:DWORD src1_sel:WORD_1
v_mfma_f32_16x16x32_bf16 v[184:187],a[228:231],v[44:47],v[184:187]
v_rcp_f16_sdwa v9,v8 dst_sel:WORD_1 dst_unused:UNUSED_PRESERVE src0_sel:WORD_1
v_mfma_f32_16x16x32_bf16 v[224:227],a[232:235],v[44:47],v[224:227]
ds_read_u16 v57,v57
ds_read_u16_d16_hi v59,v59
v_mfma_f32_16x16x32_bf16 v[228:231],a[232:235],v[48:51],v[228:231]
ds_read_u16 v52,v52
v_lshlrev_b32_sdwa v54,1,v54 dst_sel:DWORD dst_unused:UNUSED_PAD src0_sel:DWORD src1_sel:WORD_1
v_mfma_f32_16x16x32_bf16 v[204:207],a[236:239],v[48:51],v[204:207]
ds_read_u16_d16_hi v54,v54
v_fma_mix_f32 v53,v53,v9,0 op_sel_hi:[0,1,0]
v_mfma_f32_16x16x32_bf16 v[200:203],a[236:239],v[44:47],v[200:203]
s_waitcnt lgkmcnt(7)
v_fma_mix_f32 v55,v55,v9,0 op_sel:[0,1,0] op_sel_hi:[0,1,0]
v_mfma_f32_16x16x32_bf16 v[48:51],a[204:207],a[48:51],v[20:23]
s_add_u32 m0,s57,28296
v_lshlrev_b32_sdwa v40,1,v40 dst_sel:DWORD dst_unused:UNUSED_PAD src0_sel:DWORD src1_sel:WORD_1
v_lshlrev_b32_sdwa v42,1,v42 dst_sel:DWORD dst_unused:UNUSED_PAD src0_sel:DWORD src1_sel:WORD_1
v_mfma_f32_16x16x32_bf16 v[48:51],a[208:211],a[64:67],v[48:51]
s_add_u32 s89,s56,5701632
ds_read_u16 v40,v40
v_lshl_add_u64 v[24:25],v[24:25],0,v[26:27]
v_mfma_f32_16x16x32_bf16 v[48:51],a[212:215],a[80:83],v[48:51]
s_waitcnt lgkmcnt(6)
buffer_load_dwordx4 v6,s[36:39],s89 offen lds
v_lshl_add_u64 v[26:27],v[28:29],0,v[30:31]
v_mfma_f32_16x16x32_bf16 v[44:47],a[200:203],a[80:83],v[16:19]
ds_read_u16_d16_hi v42,v42
v_lshlrev_b32_sdwa v41,1,v41 dst_sel:DWORD dst_unused:UNUSED_PAD src0_sel:DWORD src1_sel:WORD_1
v_mfma_f32_16x16x32_bf16 v[44:47],a[196:199],a[64:67],v[44:47]
s_waitcnt lgkmcnt(4)
ds_read_u16 v41,v41
v_lshl_add_u64 v[28:29],v[56:57],0,v[58:59]
v_mfma_f32_16x16x32_bf16 v[44:47],a[192:195],a[48:51],v[44:47]
v_lshlrev_b32_sdwa v43,1,v43 dst_sel:DWORD dst_unused:UNUSED_PAD src0_sel:DWORD src1_sel:WORD_1
v_lshlrev_b32_sdwa v36,1,v36 dst_sel:DWORD dst_unused:UNUSED_PAD src0_sel:DWORD src1_sel:WORD_1
v_mfma_f32_16x16x32_bf16 v[56:59],a[192:195],a[52:55],v[16:19]
s_waitcnt lgkmcnt(3)
ds_read_u16_d16_hi v43,v43
v_or_b32 v30,v52,v54
v_mfma_f32_16x16x32_bf16 v[56:59],a[196:199],a[68:71],v[56:59]
ds_read_u16 v36,v36
v_cvt_pk_bf16_f32 v31,v53,v55
v_lshlrev_b32_sdwa v38,1,v38 dst_sel:DWORD dst_unused:UNUSED_PAD src0_sel:DWORD src1_sel:WORD_1
v_mfma_f32_16x16x32_bf16 v[56:59],a[200:203],a[84:87],v[56:59]
ds_read_u16_d16_hi v38,v38
v_lshlrev_b32_sdwa v37,1,v37 dst_sel:DWORD dst_unused:UNUSED_PAD src0_sel:DWORD src1_sel:WORD_1
v_mfma_f32_16x16x32_bf16 v[52:55],a[212:215],a[84:87],v[20:23]
ds_read_u16 v37,v37
v_lshlrev_b32_sdwa v39,1,v39 dst_sel:DWORD dst_unused:UNUSED_PAD src0_sel:DWORD src1_sel:WORD_1
v_mfma_f32_16x16x32_bf16 v[52:55],a[208:211],a[68:71],v[52:55]
ds_read_u16_d16_hi v39,v39
v_lshlrev_b32_sdwa v32,1,v32 dst_sel:DWORD dst_unused:UNUSED_PAD src0_sel:DWORD src1_sel:WORD_1
v_mfma_f32_16x16x32_bf16 v[52:55],a[204:207],a[52:55],v[52:55]
s_add_u32 m0,s57,31832
ds_read_u16 v32,v32
v_lshlrev_b32_sdwa v34,1,v34 dst_sel:DWORD dst_unused:UNUSED_PAD src0_sel:DWORD src1_sel:WORD_1
v_mfma_f32_16x16x32_bf16 v[136:139],a[240:243],v[24:27],v[136:139]
s_add_u32 s89,s56,5799936
ds_read_u16_d16_hi v34,v34
v_lshlrev_b32_sdwa v33,1,v33 dst_sel:DWORD dst_unused:UNUSED_PAD src0_sel:DWORD src1_sel:WORD_1
v_mfma_f32_16x16x32_bf16 v[140:143],a[240:243],v[28:31],v[140:143]
buffer_load_dwordx4 v6,s[36:39],s89 offen lds
v_lshlrev_b32_sdwa v35,1,v35 dst_sel:DWORD dst_unused:UNUSED_PAD src0_sel:DWORD src1_sel:WORD_1
v_mfma_f32_16x16x32_bf16 v[68:71],a[224:227],v[28:31],v[68:71]
ds_read_u16 v33,v33
ds_read_u16_d16_hi v35,v35
v_mfma_f32_16x16x32_bf16 v[64:67],a[224:227],v[24:27],v[64:67]
ds_read_b64_tr_b16 a[244:245],v3 offset:6160
v_lshlrev_b32_sdwa v60,1,v60 dst_sel:DWORD dst_unused:UNUSED_PAD src0_sel:DWORD src1_sel:WORD_1
v_mfma_f32_16x16x32_bf16 v[144:147],a[248:251],v[24:27],v[144:147]
ds_read_u16 v60,v60
v_lshlrev_b32_sdwa v62,1,v62 dst_sel:DWORD dst_unused:UNUSED_PAD src0_sel:DWORD src1_sel:WORD_1
v_mfma_f32_16x16x32_bf16 v[148:151],a[248:251],v[28:31],v[148:151]
ds_read_u16_d16_hi v62,v62
v_lshlrev_b32_sdwa v61,1,v61 dst_sel:DWORD dst_unused:UNUSED_PAD src0_sel:DWORD src1_sel:WORD_1
v_mfma_f32_16x16x32_bf16 v[84:87],a[228:231],v[28:31],v[84:87]
ds_read_u16 v61,v61
v_lshlrev_b32_sdwa v63,1,v63 dst_sel:DWORD dst_unused:UNUSED_PAD src0_sel:DWORD src1_sel:WORD_1
v_mfma_f32_16x16x32_bf16 v[80:83],a[228:231],v[24:27],v[80:83]
ds_read_u16_d16_hi v63,v63
v_mfma_f32_16x16x32_bf16 v[152:155],a[232:235],v[24:27],v[152:155]
ds_read_b64_tr_b16 a[246:247],v3 offset:7696
v_mfma_f32_16x16x32_bf16 v[156:159],a[232:235],v[28:31],v[156:159]
TP5958_%=:
s_cbranch_execz TR0_%=
ds_read_b64_tr_b16 a[252:253],v3 offset:6168
v_mfma_f32_16x16x32_bf16 v[100:103],a[236:239],v[28:31],v[100:103]
ds_read_b64_tr_b16 a[254:255],v3 offset:7704
v_mfma_f32_16x16x32_bf16 v[96:99],a[236:239],v[24:27],v[96:99]
v_mfma_f32_16x16x32_bf16 v[28:31],a[204:207],a[56:59],v[20:23]
s_nop 0
ds_read_b64_tr_b16 a[216:217],v1 offset:6160
v_lshlrev_b32_sdwa v48,1,v48 dst_sel:DWORD dst_unused:UNUSED_PAD src0_sel:DWORD src1_sel:WORD_1
v_mfma_f32_16x16x32_bf16 v[28:31],a[208:211],a[72:75],v[28:31]
s_waitcnt lgkmcnt(7)
ds_read_u16 v48,v48
v_lshl_add_u64 v[40:41],v[40:41],0,v[42:43]
v_fma_mixlo_f16 v8,v57,s99,0
v_mfma_f32_16x16x32_bf16 v[28:31],a[212:215],a[88:91],v[28:31]
ds_read_b64_tr_b16 a[218:219],v1 offset:7184
v_lshl_add_u64 v[42:43],v[36:37],0,v[38:39]
v_lshlrev_b32_sdwa v50,1,v50 dst_sel:DWORD dst_unused:UNUSED_PAD src0_sel:DWORD src1_sel:WORD_1
v_mfma_f32_16x16x32_bf16 v[24:27],a[200:203],a[88:91],v[16:19]
ds_read_u16_d16_hi v50,v50
v_fma_mixhi_f16 v8,v59,s99,0
v_mfma_f32_16x16x32_bf16 v[24:27],a[196:199],a[72:75],v[24:27]
ds_read_b64_tr_b16 a[220:221],v1 offset:8208
v_lshl_add_u64 v[36:37],v[32:33],0,v[34:35]
v_lshlrev_b32_sdwa v49,1,v49 dst_sel:DWORD dst_unused:UNUSED_PAD src0_sel:DWORD src1_sel:WORD_1
v_pk_fma_f16 v9,v8,v8,s54
v_mfma_f32_16x16x32_bf16 v[24:27],a[192:195],a[56:59],v[24:27]
ds_read_u16 v49,v49
v_lshlrev_b32_sdwa v51,1,v51 dst_sel:DWORD dst_unused:UNUSED_PAD src0_sel:DWORD src1_sel:WORD_1
v_mfma_f32_16x16x32_bf16 v[32:35],a[192:195],a[60:63],v[16:19]
ds_read_u16_d16_hi v51,v51
v_pk_mul_f16 v9,v8,v9
v_mfma_f32_16x16x32_bf16 v[32:35],a[196:199],a[76:79],v[32:35]
s_waitcnt lgkmcnt(7)
ds_read_b64_tr_b16 a[222:223],v1 offset:9232
v_lshl_add_u64 v[38:39],v[60:61],0,v[62:63]
v_lshlrev_b32_sdwa v44,1,v44 dst_sel:DWORD dst_unused:UNUSED_PAD src0_sel:DWORD src1_sel:WORD_1
v_mfma_f32_16x16x32_bf16 v[32:35],a[200:203],a[92:95],v[32:35]
v_exp_f16 v8,v9
v_mfma_f32_16x16x32_bf16 v[60:63],a[212:215],a[92:95],v[20:23]
ds_read_u16 v44,v44
v_lshlrev_b32_sdwa v46,1,v46 dst_sel:DWORD dst_unused:UNUSED_PAD src0_sel:DWORD src1_sel:WORD_1
v_mfma_f32_16x16x32_bf16 v[60:63],a[208:211],a[76:79],v[60:63]
ds_read_u16_d16_hi v46,v46
v_lshlrev_b32_sdwa v45,1,v45 dst_sel:DWORD dst_unused:UNUSED_PAD src0_sel:DWORD src1_sel:WORD_1
v_mfma_f32_16x16x32_bf16 v[60:63],a[204:207],a[60:63],v[60:63]
v_exp_f16_sdwa v8,v9 dst_sel:WORD_1 dst_unused:UNUSED_PRESERVE src0_sel:WORD_1
v_mfma_f32_16x16x32_bf16 v[112:115],a[240:243],v[40:43],v[112:115]
ds_read_u16 v45,v45
v_lshlrev_b32_sdwa v47,1,v47 dst_sel:DWORD dst_unused:UNUSED_PAD src0_sel:DWORD src1_sel:WORD_1
v_mfma_f32_16x16x32_bf16 v[116:119],a[240:243],v[36:39],v[116:119]
ds_read_u16_d16_hi v47,v47
v_lshlrev_b32_sdwa v52,1,v52 dst_sel:DWORD dst_unused:UNUSED_PAD src0_sel:DWORD src1_sel:WORD_1
v_mfma_f32_16x16x32_bf16 v[76:79],a[224:227],v[36:39],v[76:79]
ds_read_u16 v52,v52
v_pk_fma_f16 v8,v8,s55,s55
v_mfma_f32_16x16x32_bf16 v[72:75],a[224:227],v[40:43],v[72:75]
ds_read_b64_tr_b16 a[240:241],v3 offset:6224
v_rcp_f16_e64 v9,v8
v_lshlrev_b32_sdwa v54,1,v54 dst_sel:DWORD dst_unused:UNUSED_PAD src0_sel:DWORD src1_sel:WORD_1
v_mfma_f32_16x16x32_bf16 v[120:123],a[248:251],v[40:43],v[120:123]
ds_read_u16_d16_hi v54,v54
v_lshlrev_b32_sdwa v53,1,v53 dst_sel:DWORD dst_unused:UNUSED_PAD src0_sel:DWORD src1_sel:WORD_1
v_mfma_f32_16x16x32_bf16 v[124:127],a[248:251],v[36:39],v[124:127]
ds_read_u16 v53,v53
ds_read_b64_tr_b16 a[242:243],v3 offset:7760
v_mfma_f32_16x16x32_bf16 v[92:95],a[228:231],v[36:39],v[92:95]
ds_read_b64_tr_b16 a[224:225],v3 offset:6232
v_lshlrev_b32_sdwa v55,1,v55 dst_sel:DWORD dst_unused:UNUSED_PAD src0_sel:DWORD src1_sel:WORD_1
v_mfma_f32_16x16x32_bf16 v[88:91],a[228:231],v[40:43],v[88:91]
ds_read_u16_d16_hi v55,v55
v_rcp_f16_sdwa v9,v8 dst_sel:WORD_1 dst_unused:UNUSED_PRESERVE src0_sel:WORD_1
v_mfma_f32_16x16x32_bf16 v[128:131],a[232:235],v[40:43],v[128:131]
ds_read_b64_tr_b16 a[226:227],v3 offset:7768
ds_read_b64_tr_b16 a[248:249],v3 offset:6288
v_lshlrev_b32_sdwa v56,1,v56 dst_sel:DWORD dst_unused:UNUSED_PAD src0_sel:DWORD src1_sel:WORD_1
v_mfma_f32_16x16x32_bf16 v[132:135],a[232:235],v[36:39],v[132:135]
ds_read_u16 v56,v56
v_lshlrev_b32_sdwa v58,1,v58 dst_sel:DWORD dst_unused:UNUSED_PAD src0_sel:DWORD src1_sel:WORD_1
v_mfma_f32_16x16x32_bf16 v[108:111],a[236:239],v[36:39],v[108:111]
ds_read_u16_d16_hi v58,v58
v_fma_mix_f32 v57,v57,v9,0 op_sel_hi:[0,1,0]
v_mfma_f32_16x16x32_bf16 v[104:107],a[236:239],v[40:43],v[104:107]
ds_read_b64_tr_b16 a[250:251],v3 offset:7824
v_fma_mix_f32 v59,v59,v9,0 op_sel:[0,1,0] op_sel_hi:[0,1,0]
v_mfma_f32_16x16x32_bf16 v[36:39],a[204:207],a[0:3],v[20:23]
s_add_u32 m0,s57,35360
ds_read_b64_tr_b16 a[228:229],v3 offset:6296
v_lshlrev_b32_sdwa v28,1,v28 dst_sel:DWORD dst_unused:UNUSED_PAD src0_sel:DWORD src1_sel:WORD_1
v_mfma_f32_16x16x32_bf16 v[36:39],a[208:211],a[16:19],v[36:39]
s_waitcnt lgkmcnt(10)
ds_read_b64_tr_b16 a[230:231],v3 offset:7832
v_lshl_add_u64 v[48:49],v[48:49],0,v[50:51]
v_mfma_f32_16x16x32_bf16 v[36:39],a[212:215],a[32:35],v[36:39]
s_add_u32 s89,s56,5898240
ds_read_u16 v28,v28
v_lshl_add_u64 v[50:51],v[44:45],0,v[46:47]
v_lshlrev_b32_sdwa v30,1,v30 dst_sel:DWORD dst_unused:UNUSED_PAD src0_sel:DWORD src1_sel:WORD_1
v_mfma_f32_16x16x32_bf16 v[40:43],a[200:203],a[32:35],v[16:19]
buffer_load_dwordx4 v6,s[36:39],s89 offen lds
v_lshlrev_b32_sdwa v29,1,v29 dst_sel:DWORD dst_unused:UNUSED_PAD src0_sel:DWORD src1_sel:WORD_1
v_mfma_f32_16x16x32_bf16 v[40:43],a[196:199],a[16:19],v[40:43]
s_waitcnt lgkmcnt(8)
ds_read_u16_d16_hi v30,v30
v_lshl_add_u64 v[44:45],v[52:53],0,v[54:55]
v_mfma_f32_16x16x32_bf16 v[40:43],a[192:195],a[0:3],v[40:43]
ds_read_u16 v29,v29
v_lshlrev_b32_sdwa v31,1,v31 dst_sel:DWORD dst_unused:UNUSED_PAD src0_sel:DWORD src1_sel:WORD_1
v_mfma_f32_16x16x32_bf16 v[52:55],a[192:195],a[4:7],v[16:19]
s_waitcnt lgkmcnt(6)
ds_read_u16_d16_hi v31,v31
v_or_b32 v46,v56,v58
v_lshlrev_b32_sdwa v24,1,v24 dst_sel:DWORD dst_unused:UNUSED_PAD src0_sel:DWORD src1_sel:WORD_1
v_mfma_f32_16x16x32_bf16 v[52:55],a[196:199],a[20:23],v[52:55]
ds_read_u16 v24,v24
v_cvt_pk_bf16_f32 v47,v57,v59
v_lshlrev_b32_sdwa v26,1,v26 dst_sel:DWORD dst_unused:UNUSED_PAD src0_sel:DWORD src1_sel:WORD_1
v_mfma_f32_16x16x32_bf16 v[52:55],a[200:203],a[36:39],v[52:55]
ds_read_u16_d16_hi v26,v26
v_lshlrev_b32_sdwa v25,1,v25 dst_sel:DWORD dst_unused:UNUSED_PAD src0_sel:DWORD src1_sel:WORD_1
v_mfma_f32_16x16x32_bf16 v[56:59],a[212:215],a[36:39],v[20:23]
ds_read_u16 v25,v25
v_lshlrev_b32_sdwa v27,1,v27 dst_sel:DWORD dst_unused:UNUSED_PAD src0_sel:DWORD src1_sel:WORD_1
v_mfma_f32_16x16x32_bf16 v[56:59],a[208:211],a[20:23],v[56:59]
ds_read_u16_d16_hi v27,v27
v_lshlrev_b32_sdwa v60,1,v60 dst_sel:DWORD dst_unused:UNUSED_PAD src0_sel:DWORD src1_sel:WORD_1
v_mfma_f32_16x16x32_bf16 v[56:59],a[204:207],a[4:7],v[56:59]
ds_read_u16 v60,v60
v_lshlrev_b32_sdwa v62,1,v62 dst_sel:DWORD dst_unused:UNUSED_PAD src0_sel:DWORD src1_sel:WORD_1
v_mfma_f32_16x16x32_bf16 v[232:235],a[244:247],v[48:51],v[232:235]
ds_read_u16_d16_hi v62,v62
v_lshlrev_b32_sdwa v61,1,v61 dst_sel:DWORD dst_unused:UNUSED_PAD src0_sel:DWORD src1_sel:WORD_1
v_mfma_f32_16x16x32_bf16 v[236:239],a[244:247],v[44:47],v[236:239]
ds_read_u16 v61,v61
v_lshlrev_b32_sdwa v63,1,v63 dst_sel:DWORD dst_unused:UNUSED_PAD src0_sel:DWORD src1_sel:WORD_1
v_mfma_f32_16x16x32_bf16 v[164:167],a[252:255],v[44:47],v[164:167]
ds_read_u16_d16_hi v63,v63
v_lshlrev_b32_sdwa v32,1,v32 dst_sel:DWORD dst_unused:UNUSED_PAD src0_sel:DWORD src1_sel:WORD_1
v_mfma_f32_16x16x32_bf16 v[160:163],a[252:255],v[48:51],v[160:163]
ds_read_u16 v32,v32
v_lshlrev_b32_sdwa v34,1,v34 dst_sel:DWORD dst_unused:UNUSED_PAD src0_sel:DWORD src1_sel:WORD_1
v_mfma_f32_16x16x32_bf16 v[240:243],a[240:243],v[48:51],v[240:243]
ds_read_u16_d16_hi v34,v34
v_lshlrev_b32_sdwa v33,1,v33 dst_sel:DWORD dst_unused:UNUSED_PAD src0_sel:DWORD src1_sel:WORD_1
v_mfma_f32_16x16x32_bf16 v[244:247],a[240:243],v[44:47],v[244:247]
ds_read_u16 v33,v33
v_lshlrev_b32_sdwa v35,1,v35 dst_sel:DWORD dst_unused:UNUSED_PAD src0_sel:DWORD src1_sel:WORD_1
v_mfma_f32_16x16x32_bf16 v[180:183],a[224:227],v[44:47],v[180:183]
ds_read_u16_d16_hi v35,v35
v_mfma_f32_16x16x32_bf16 v[176:179],a[224:227],v[48:51],v[176:179]
s_waitcnt lgkmcnt(8)
v_mfma_f32_16x16x32_bf16 v[248:251],a[248:251],v[48:51],v[248:251]
s_add_u32 m0,s57,38896
v_mfma_f32_16x16x32_bf16 v[252:255],a[248:251],v[44:47],v[252:255]
s_add_u32 s89,s56,5996544
v_mfma_f32_16x16x32_bf16 v[196:199],a[228:231],v[44:47],v[196:199]
buffer_load_dwordx4 v6,s[36:39],s89 offen lds
v_mfma_f32_16x16x32_bf16 v[192:195],a[228:231],v[48:51],v[192:195]
v_mfma_f32_16x16x32_bf16 v[44:47],a[204:207],a[8:11],v[20:23]
v_lshlrev_b32_sdwa v36,1,v36 dst_sel:DWORD dst_unused:UNUSED_PAD src0_sel:DWORD src1_sel:WORD_1
v_fma_mixlo_f16 v8,v53,s99,0
v_mfma_f32_16x16x32_bf16 v[44:47],a[208:211],a[24:27],v[44:47]
ds_read_u16 v36,v36
v_lshl_add_u64 v[28:29],v[28:29],0,v[30:31]
v_mfma_f32_16x16x32_bf16 v[44:47],a[212:215],a[40:43],v[44:47]
v_lshl_add_u64 v[30:31],v[24:25],0,v[26:27]
v_lshlrev_b32_sdwa v38,1,v38 dst_sel:DWORD dst_unused:UNUSED_PAD src0_sel:DWORD src1_sel:WORD_1
v_mfma_f32_16x16x32_bf16 v[48:51],a[200:203],a[40:43],v[16:19]
ds_read_u16_d16_hi v38,v38
v_fma_mixhi_f16 v8,v55,s99,0
v_mfma_f32_16x16x32_bf16 v[48:51],a[196:199],a[24:27],v[48:51]
s_waitcnt lgkmcnt(2)
v_lshl_add_u64 v[24:25],v[60:61],0,v[62:63]
v_lshlrev_b32_sdwa v37,1,v37 dst_sel:DWORD dst_unused:UNUSED_PAD src0_sel:DWORD src1_sel:WORD_1
v_pk_fma_f16 v9,v8,v8,s54
v_mfma_f32_16x16x32_bf16 v[48:51],a[192:195],a[8:11],v[48:51]
ds_read_u16 v37,v37
v_lshlrev_b32_sdwa v39,1,v39 dst_sel:DWORD dst_unused:UNUSED_PAD src0_sel:DWORD src1_sel:WORD_1
v_mfma_f32_16x16x32_bf16 v[60:63],a[192:195],a[12:15],v[16:19]
ds_read_b128 v[16:19],v4 offset:256
v_pk_mul_f16 v9,v8,v9
v_mfma_f32_16x16x32_bf16 v[60:63],a[196:199],a[28:31],v[60:63]
ds_read_u16_d16_hi v39,v39
ds_read_b64_tr_b16 a[192:193],v1 offset:10256
v_lshl_add_u64 v[26:27],v[32:33],0,v[34:35]
v_lshlrev_b32_sdwa v40,1,v40 dst_sel:DWORD dst_unused:UNUSED_PAD src0_sel:DWORD src1_sel:WORD_1
v_mfma_f32_16x16x32_bf16 v[60:63],a[200:203],a[44:47],v[60:63]
ds_read_b64_tr_b16 a[194:195],v1 offset:11280
v_exp_f16_e64 v8,v9
v_mfma_f32_16x16x32_bf16 v[32:35],a[212:215],a[44:47],v[20:23]
ds_read_b128 v[20:23],v4 offset:288
ds_read_u16 v40,v40
v_lshlrev_b32_sdwa v42,1,v42 dst_sel:DWORD dst_unused:UNUSED_PAD src0_sel:DWORD src1_sel:WORD_1
v_mfma_f32_16x16x32_bf16 v[32:35],a[208:211],a[28:31],v[32:35]
ds_read_u16_d16_hi v42,v42
ds_read_b64_tr_b16 a[196:197],v2 offset:10256
v_lshlrev_b32_sdwa v41,1,v41 dst_sel:DWORD dst_unused:UNUSED_PAD src0_sel:DWORD src1_sel:WORD_1
v_mfma_f32_16x16x32_bf16 v[32:35],a[204:207],a[12:15],v[32:35]
ds_read_u16 v41,v41
v_exp_f16_sdwa v8,v9 dst_sel:WORD_1 dst_unused:UNUSED_PRESERVE src0_sel:WORD_1
v_mfma_f32_16x16x32_bf16 v[208:211],a[244:247],v[28:31],v[208:211]
ds_read_b64_tr_b16 a[198:199],v2 offset:11280
ds_read_b64_tr_b16 a[200:201],v2 offset:8208
v_lshlrev_b32_sdwa v43,1,v43 dst_sel:DWORD dst_unused:UNUSED_PAD src0_sel:DWORD src1_sel:WORD_1
v_mfma_f32_16x16x32_bf16 v[212:215],a[244:247],v[24:27],v[212:215]
ds_read_u16_d16_hi v43,v43
v_lshlrev_b32_sdwa v56,1,v56 dst_sel:DWORD dst_unused:UNUSED_PAD src0_sel:DWORD src1_sel:WORD_1
v_mfma_f32_16x16x32_bf16 v[172:175],a[252:255],v[24:27],v[172:175]
ds_read_b64_tr_b16 a[202:203],v2 offset:9232
v_pk_fma_f16 v8,v8,s55,s55
v_mfma_f32_16x16x32_bf16 v[168:171],a[252:255],v[28:31],v[168:171]
v_rcp_f16 v9,v8
v_lshlrev_b32_sdwa v58,1,v58 dst_sel:DWORD dst_unused:UNUSED_PAD src0_sel:DWORD src1_sel:WORD_1
v_mfma_f32_16x16x32_bf16 v[216:219],a[240:243],v[28:31],v[216:219]
ds_read_u16 v56,v56
v_lshlrev_b32_sdwa v57,1,v57 dst_sel:DWORD dst_unused:UNUSED_PAD src0_sel:DWORD src1_sel:WORD_1
v_mfma_f32_16x16x32_bf16 v[220:223],a[240:243],v[24:27],v[220:223]
ds_read_u16_d16_hi v58,v58
v_lshlrev_b32_sdwa v59,1,v59 dst_sel:DWORD dst_unused:UNUSED_PAD src0_sel:DWORD src1_sel:WORD_1
v_mfma_f32_16x16x32_bf16 v[188:191],a[224:227],v[24:27],v[188:191]
ds_read_u16 v57,v57
v_lshlrev_b32_sdwa v52,1,v52 dst_sel:DWORD dst_unused:UNUSED_PAD src0_sel:DWORD src1_sel:WORD_1
v_mfma_f32_16x16x32_bf16 v[184:187],a[224:227],v[28:31],v[184:187]
ds_read_u16_d16_hi v59,v59
v_rcp_f16_sdwa v9,v8 dst_sel:WORD_1 dst_unused:UNUSED_PRESERVE src0_sel:WORD_1
v_mfma_f32_16x16x32_bf16 v[224:227],a[248:251],v[28:31],v[224:227]
ds_read_b64_tr_b16 a[212:213],v2 offset:6160
v_lshlrev_b32_sdwa v54,1,v54 dst_sel:DWORD dst_unused:UNUSED_PAD src0_sel:DWORD src1_sel:WORD_1
v_mfma_f32_16x16x32_bf16 v[228:231],a[248:251],v[24:27],v[228:231]
ds_read_b64_tr_b16 a[214:215],v2 offset:7184
v_fma_mix_f32 v53,v53,v9,0 op_sel_hi:[0,1,0]
v_mfma_f32_16x16x32_bf16 v[204:207],a[228:231],v[24:27],v[204:207]
ds_read_u16 v52,v52
v_fma_mix_f32 v55,v55,v9,0 op_sel:[0,1,0] op_sel_hi:[0,1,0]
v_mfma_f32_16x16x32_bf16 v[200:203],a[228:231],v[28:31],v[200:203]
s_waitcnt lgkmcnt(7)
ds_read_u16_d16_hi v54,v54
v_mfma_f32_16x16x32_bf16 v[24:27],a[216:219],a[48:51],v[16:19]
v_lshlrev_b32_sdwa v44,1,v44 dst_sel:DWORD dst_unused:UNUSED_PAD src0_sel:DWORD src1_sel:WORD_1
v_lshlrev_b32_sdwa v46,1,v46 dst_sel:DWORD dst_unused:UNUSED_PAD src0_sel:DWORD src1_sel:WORD_1
v_mfma_f32_16x16x32_bf16 v[24:27],a[220:223],a[64:67],v[24:27]
s_add_u32 m0,s57,42440
ds_read_u16 v44,v44
v_lshl_add_u64 v[36:37],v[36:37],0,v[38:39]
v_mfma_f32_16x16x32_bf16 v[24:27],a[192:195],a[80:83],v[24:27]
s_add_u32 s89,s56,6094848
ds_read_u16_d16_hi v46,v46
v_lshl_add_u64 v[38:39],v[40:41],0,v[42:43]
v_mfma_f32_16x16x32_bf16 v[28:31],a[196:199],a[80:83],v[20:23]
buffer_load_dwordx4 v6,s[36:39],s89 offen lds
v_lshlrev_b32_sdwa v45,1,v45 dst_sel:DWORD dst_unused:UNUSED_PAD src0_sel:DWORD src1_sel:WORD_1
v_mfma_f32_16x16x32_bf16 v[28:31],a[200:203],a[64:67],v[28:31]
s_waitcnt lgkmcnt(4)
ds_read_u16 v45,v45
v_lshl_add_u64 v[40:41],v[56:57],0,v[58:59]
v_mfma_f32_16x16x32_bf16 v[28:31],a[212:215],a[48:51],v[28:31]
v_lshlrev_b32_sdwa v47,1,v47 dst_sel:DWORD dst_unused:UNUSED_PAD src0_sel:DWORD src1_sel:WORD_1
v_lshlrev_b32_sdwa v48,1,v48 dst_sel:DWORD dst_unused:UNUSED_PAD src0_sel:DWORD src1_sel:WORD_1
v_mfma_f32_16x16x32_bf16 v[56:59],a[212:215],a[52:55],v[20:23]
s_waitcnt lgkmcnt(3)
ds_read_u16_d16_hi v47,v47
v_or_b32 v42,v52,v54
v_mfma_f32_16x16x32_bf16 v[56:59],a[200:203],a[68:71],v[56:59]
s_waitcnt vmcnt(14)
ds_read_u16 v48,v48
v_cvt_pk_bf16_f32 v43,v53,v55
v_lshlrev_b32_sdwa v50,1,v50 dst_sel:DWORD dst_unused:UNUSED_PAD src0_sel:DWORD src1_sel:WORD_1
v_mfma_f32_16x16x32_bf16 v[56:59],a[196:199],a[84:87],v[56:59]
s_barrier
ds_read_u16_d16_hi v50,v50
v_lshlrev_b32_sdwa v49,1,v49 dst_sel:DWORD dst_unused:UNUSED_PAD src0_sel:DWORD src1_sel:WORD_1
v_mfma_f32_16x16x32_bf16 v[52:55],a[192:195],a[84:87],v[16:19]
ds_read_u16 v49,v49
v_lshlrev_b32_sdwa v51,1,v51 dst_sel:DWORD dst_unused:UNUSED_PAD src0_sel:DWORD src1_sel:WORD_1
v_mfma_f32_16x16x32_bf16 v[52:55],a[220:223],a[68:71],v[52:55]
s_add_u32 m0,s58,0
ds_read_u16_d16_hi v51,v51
v_lshlrev_b32_sdwa v32,1,v32 dst_sel:DWORD dst_unused:UNUSED_PAD src0_sel:DWORD src1_sel:WORD_1
v_mfma_f32_16x16x32_bf16 v[52:55],a[216:219],a[52:55],v[52:55]
s_add_u32 s69,s69,s59
ds_read_u16 v32,v32
v_lshlrev_b32_sdwa v34,1,v34 dst_sel:DWORD dst_unused:UNUSED_PAD src0_sel:DWORD src1_sel:WORD_1
v_mfma_f32_16x16x32_bf16 v[136:139],a[244:247],v[36:39],v[136:139]
buffer_load_dwordx4 v5,s[44:47],s69 offen offset:0 lds
v_lshlrev_b32_sdwa v33,1,v33 dst_sel:DWORD dst_unused:UNUSED_PAD src0_sel:DWORD src1_sel:WORD_1
v_mfma_f32_16x16x32_bf16 v[140:143],a[244:247],v[40:43],v[140:143]
ds_read_u16_d16_hi v34,v34
v_lshlrev_b32_sdwa v35,1,v35 dst_sel:DWORD dst_unused:UNUSED_PAD src0_sel:DWORD src1_sel:WORD_1
v_mfma_f32_16x16x32_bf16 v[68:71],a[252:255],v[40:43],v[68:71]
ds_read_u16 v33,v33
ds_read_u16_d16_hi v35,v35
v_mfma_f32_16x16x32_bf16 v[64:67],a[252:255],v[36:39],v[64:67]
ds_read_b64_tr_b16 a[232:233],v3 offset:12320
v_lshlrev_b32_sdwa v60,1,v60 dst_sel:DWORD dst_unused:UNUSED_PAD src0_sel:DWORD src1_sel:WORD_1
v_mfma_f32_16x16x32_bf16 v[144:147],a[240:243],v[36:39],v[144:147]
s_add_u32 s69,s69,s53
ds_read_u16 v60,v60
v_lshlrev_b32_sdwa v62,1,v62 dst_sel:DWORD dst_unused:UNUSED_PAD src0_sel:DWORD src1_sel:WORD_1
v_mfma_f32_16x16x32_bf16 v[148:151],a[240:243],v[40:43],v[148:151]
buffer_load_dwordx4 v5,s[44:47],s69 offen offset:1024 lds
ds_read_u16_d16_hi v62,v62
v_mfma_f32_16x16x32_bf16 v[84:87],a[224:227],v[40:43],v[84:87]
ds_read_b64_tr_b16 a[234:235],v3 offset:13856
v_lshlrev_b32_sdwa v61,1,v61 dst_sel:DWORD dst_unused:UNUSED_PAD src0_sel:DWORD src1_sel:WORD_1
v_mfma_f32_16x16x32_bf16 v[80:83],a[224:227],v[36:39],v[80:83]
ds_read_u16 v61,v61
v_lshlrev_b32_sdwa v63,1,v63 dst_sel:DWORD dst_unused:UNUSED_PAD src0_sel:DWORD src1_sel:WORD_1
v_mfma_f32_16x16x32_bf16 v[152:155],a[248:251],v[36:39],v[152:155]
ds_read_u16_d16_hi v63,v63
v_mfma_f32_16x16x32_bf16 v[156:159],a[248:251],v[40:43],v[156:159]
s_add_u32 s69,s69,s53
ds_read_b64_tr_b16 a[236:237],v3 offset:12328
v_mfma_f32_16x16x32_bf16 v[100:103],a[228:231],v[40:43],v[100:103]
buffer_load_dwordx4 v5,s[44:47],s69 offen offset:2048 lds
v_mfma_f32_16x16x32_bf16 v[96:99],a[228:231],v[36:39],v[96:99]
ds_read_b64_tr_b16 a[238:239],v3 offset:13864
v_mfma_f32_16x16x32_bf16 v[40:43],a[216:219],a[56:59],v[16:19]
ds_read_b64_tr_b16 a[208:209],v1 offset:12320
v_lshlrev_b32_sdwa v24,1,v24 dst_sel:DWORD dst_unused:UNUSED_PAD src0_sel:DWORD src1_sel:WORD_1
v_mfma_f32_16x16x32_bf16 v[40:43],a[220:223],a[72:75],v[40:43]
s_waitcnt lgkmcnt(7)
ds_read_u16 v24,v24
v_lshl_add_u64 v[44:45],v[44:45],0,v[46:47]
v_fma_mixlo_f16 v8,v57,s99,0
v_mfma_f32_16x16x32_bf16 v[40:43],a[192:195],a[88:91],v[40:43]
ds_read_b64_tr_b16 a[210:211],v1 offset:13344
v_lshl_add_u64 v[46:47],v[48:49],0,v[50:51]
v_lshlrev_b32_sdwa v26,1,v26 dst_sel:DWORD dst_unused:UNUSED_PAD src0_sel:DWORD src1_sel:WORD_1
v_mfma_f32_16x16x32_bf16 v[36:39],a[196:199],a[88:91],v[20:23]
ds_read_u16_d16_hi v26,v26
v_fma_mixhi_f16 v8,v59,s99,0
v_mfma_f32_16x16x32_bf16 v[36:39],a[200:203],a[72:75],v[36:39]
ds_read_b64_tr_b16 a[204:205],v1 offset:14368
v_lshl_add_u64 v[48:49],v[32:33],0,v[34:35]
v_lshlrev_b32_sdwa v25,1,v25 dst_sel:DWORD dst_unused:UNUSED_PAD src0_sel:DWORD src1_sel:WORD_1
v_pk_fma_f16 v9,v8,v8,s54
v_mfma_f32_16x16x32_bf16 v[36:39],a[212:215],a[56:59],v[36:39]
ds_read_u16 v25,v25
v_lshlrev_b32_sdwa v27,1,v27 dst_sel:DWORD dst_unused:UNUSED_PAD src0_sel:DWORD src1_sel:WORD_1
v_mfma_f32_16x16x32_bf16 v[32:35],a[212:215],a[60:63],v[20:23]
ds_read_u16_d16_hi v27,v27
v_pk_mul_f16 v9,v8,v9
v_mfma_f32_16x16x32_bf16 v[32:35],a[200:203],a[76:79],v[32:35]
s_waitcnt lgkmcnt(8)
ds_read_b64_tr_b16 a[206:207],v1 offset:15392
v_lshl_add_u64 v[50:51],v[60:61],0,v[62:63]
v_lshlrev_b32_sdwa v28,1,v28 dst_sel:DWORD dst_unused:UNUSED_PAD src0_sel:DWORD src1_sel:WORD_1
v_mfma_f32_16x16x32_bf16 v[32:35],a[196:199],a[92:95],v[32:35]
v_exp_f16 v8,v9
v_mfma_f32_16x16x32_bf16 v[60:63],a[192:195],a[92:95],v[16:19]
ds_read_u16 v28,v28
v_lshlrev_b32_sdwa v30,1,v30 dst_sel:DWORD dst_unused:UNUSED_PAD src0_sel:DWORD src1_sel:WORD_1
v_mfma_f32_16x16x32_bf16 v[60:63],a[220:223],a[76:79],v[60:63]
ds_read_u16_d16_hi v30,v30
v_lshlrev_b32_sdwa v29,1,v29 dst_sel:DWORD dst_unused:UNUSED_PAD src0_sel:DWORD src1_sel:WORD_1
v_mfma_f32_16x16x32_bf16 v[60:63],a[216:219],a[60:63],v[60:63]
v_exp_f16_sdwa v8,v9 dst_sel:WORD_1 dst_unused:UNUSED_PRESERVE src0_sel:WORD_1
v_mfma_f32_16x16x32_bf16 v[112:115],a[244:247],v[44:47],v[112:115]
ds_read_u16 v29,v29
v_lshlrev_b32_sdwa v31,1,v31 dst_sel:DWORD dst_unused:UNUSED_PAD src0_sel:DWORD src1_sel:WORD_1
v_mfma_f32_16x16x32_bf16 v[116:119],a[244:247],v[48:51],v[116:119]
ds_read_u16_d16_hi v31,v31
v_lshlrev_b32_sdwa v52,1,v52 dst_sel:DWORD dst_unused:UNUSED_PAD src0_sel:DWORD src1_sel:WORD_1
v_mfma_f32_16x16x32_bf16 v[76:79],a[252:255],v[48:51],v[76:79]
ds_read_u16 v52,v52
v_pk_fma_f16 v8,v8,s55,s55
v_mfma_f32_16x16x32_bf16 v[72:75],a[252:255],v[44:47],v[72:75]
ds_read_b64_tr_b16 a[244:245],v3 offset:12384
v_rcp_f16_e64 v9,v8
v_lshlrev_b32_sdwa v54,1,v54 dst_sel:DWORD dst_unused:UNUSED_PAD src0_sel:DWORD src1_sel:WORD_1
v_mfma_f32_16x16x32_bf16 v[120:123],a[240:243],v[44:47],v[120:123]
ds_read_u16_d16_hi v54,v54
v_lshlrev_b32_sdwa v53,1,v53 dst_sel:DWORD dst_unused:UNUSED_PAD src0_sel:DWORD src1_sel:WORD_1
v_mfma_f32_16x16x32_bf16 v[124:127],a[240:243],v[48:51],v[124:127]
ds_read_u16 v53,v53
ds_read_b64_tr_b16 a[246:247],v3 offset:13920
v_mfma_f32_16x16x32_bf16 v[92:95],a[224:227],v[48:51],v[92:95]
ds_read_b64_tr_b16 a[252:253],v3 offset:12392
v_lshlrev_b32_sdwa v55,1,v55 dst_sel:DWORD dst_unused:UNUSED_PAD src0_sel:DWORD src1_sel:WORD_1
v_mfma_f32_16x16x32_bf16 v[88:91],a[224:227],v[44:47],v[88:91]
ds_read_u16_d16_hi v55,v55
v_rcp_f16_sdwa v9,v8 dst_sel:WORD_1 dst_unused:UNUSED_PRESERVE src0_sel:WORD_1
v_mfma_f32_16x16x32_bf16 v[128:131],a[248:251],v[44:47],v[128:131]
ds_read_b64_tr_b16 a[254:255],v3 offset:13928
ds_read_b64_tr_b16 a[240:241],v3 offset:12448
v_lshlrev_b32_sdwa v56,1,v56 dst_sel:DWORD dst_unused:UNUSED_PAD src0_sel:DWORD src1_sel:WORD_1
v_mfma_f32_16x16x32_bf16 v[132:135],a[248:251],v[48:51],v[132:135]
ds_read_u16 v56,v56
v_lshlrev_b32_sdwa v58,1,v58 dst_sel:DWORD dst_unused:UNUSED_PAD src0_sel:DWORD src1_sel:WORD_1
v_mfma_f32_16x16x32_bf16 v[108:111],a[228:231],v[48:51],v[108:111]
ds_read_u16_d16_hi v58,v58
v_fma_mix_f32 v57,v57,v9,0 op_sel_hi:[0,1,0]
v_mfma_f32_16x16x32_bf16 v[104:107],a[228:231],v[44:47],v[104:107]
ds_read_b64_tr_b16 a[242:243],v3 offset:13984
v_fma_mix_f32 v59,v59,v9,0 op_sel:[0,1,0] op_sel_hi:[0,1,0]
v_mfma_f32_16x16x32_bf16 v[48:51],a[216:219],a[0:3],v[16:19]
s_add_u32 m0,s58,6160
ds_read_b64_tr_b16 a[224:225],v3 offset:12456
v_lshlrev_b32_sdwa v40,1,v40 dst_sel:DWORD dst_unused:UNUSED_PAD src0_sel:DWORD src1_sel:WORD_1
v_mfma_f32_16x16x32_bf16 v[48:51],a[220:223],a[16:19],v[48:51]
s_waitcnt lgkmcnt(10)
ds_read_b64_tr_b16 a[226:227],v3 offset:13992
v_lshl_add_u64 v[24:25],v[24:25],0,v[26:27]
v_mfma_f32_16x16x32_bf16 v[48:51],a[192:195],a[32:35],v[48:51]
s_add_u32 s69,s69,s59
ds_read_u16 v40,v40
v_lshl_add_u64 v[26:27],v[28:29],0,v[30:31]
v_lshlrev_b32_sdwa v42,1,v42 dst_sel:DWORD dst_unused:UNUSED_PAD src0_sel:DWORD src1_sel:WORD_1
v_mfma_f32_16x16x32_bf16 v[44:47],a[196:199],a[32:35],v[20:23]
buffer_load_dwordx4 v5,s[44:47],s69 offen offset:0 lds
v_lshlrev_b32_sdwa v41,1,v41 dst_sel:DWORD dst_unused:UNUSED_PAD src0_sel:DWORD src1_sel:WORD_1
v_mfma_f32_16x16x32_bf16 v[44:47],a[200:203],a[16:19],v[44:47]
s_waitcnt lgkmcnt(8)
ds_read_u16_d16_hi v42,v42
v_lshl_add_u64 v[28:29],v[52:53],0,v[54:55]
v_mfma_f32_16x16x32_bf16 v[44:47],a[212:215],a[0:3],v[44:47]
s_add_u32 m0,s57,45976
ds_read_u16 v41,v41
v_lshlrev_b32_sdwa v43,1,v43 dst_sel:DWORD dst_unused:UNUSED_PAD src0_sel:DWORD src1_sel:WORD_1
v_mfma_f32_16x16x32_bf16 v[52:55],a[212:215],a[4:7],v[20:23]
s_waitcnt lgkmcnt(6)
ds_read_u16_d16_hi v43,v43
v_or_b32 v30,v56,v58
v_lshlrev_b32_sdwa v36,1,v36 dst_sel:DWORD dst_unused:UNUSED_PAD src0_sel:DWORD src1_sel:WORD_1
v_mfma_f32_16x16x32_bf16 v[52:55],a[200:203],a[20:23],v[52:55]
s_add_u32 s89,s56,6193152
ds_read_u16 v36,v36
v_cvt_pk_bf16_f32 v31,v57,v59
v_lshlrev_b32_sdwa v38,1,v38 dst_sel:DWORD dst_unused:UNUSED_PAD src0_sel:DWORD src1_sel:WORD_1
v_mfma_f32_16x16x32_bf16 v[52:55],a[196:199],a[36:39],v[52:55]
buffer_load_dwordx4 v6,s[36:39],s89 offen lds
v_lshlrev_b32_sdwa v37,1,v37 dst_sel:DWORD dst_unused:UNUSED_PAD src0_sel:DWORD src1_sel:WORD_1
v_mfma_f32_16x16x32_bf16 v[56:59],a[192:195],a[36:39],v[16:19]
s_add_u32 m0,s58,6160
ds_read_u16_d16_hi v38,v38
v_lshlrev_b32_sdwa v39,1,v39 dst_sel:DWORD dst_unused:UNUSED_PAD src0_sel:DWORD src1_sel:WORD_1
v_mfma_f32_16x16x32_bf16 v[56:59],a[220:223],a[20:23],v[56:59]
s_add_u32 s69,s69,s53
ds_read_u16 v37,v37
v_lshlrev_b32_sdwa v60,1,v60 dst_sel:DWORD dst_unused:UNUSED_PAD src0_sel:DWORD src1_sel:WORD_1
v_mfma_f32_16x16x32_bf16 v[56:59],a[216:219],a[4:7],v[56:59]
buffer_load_dwordx4 v5,s[44:47],s69 offen offset:1024 lds
v_lshlrev_b32_sdwa v62,1,v62 dst_sel:DWORD dst_unused:UNUSED_PAD src0_sel:DWORD src1_sel:WORD_1
v_mfma_f32_16x16x32_bf16 v[232:235],a[232:235],v[24:27],v[232:235]
s_add_u32 s69,s69,s53
ds_read_u16_d16_hi v39,v39
v_lshlrev_b32_sdwa v61,1,v61 dst_sel:DWORD dst_unused:UNUSED_PAD src0_sel:DWORD src1_sel:WORD_1
v_mfma_f32_16x16x32_bf16 v[236:239],a[232:235],v[28:31],v[236:239]
buffer_load_dwordx4 v5,s[44:47],s69 offen offset:2048 lds
v_lshlrev_b32_sdwa v63,1,v63 dst_sel:DWORD dst_unused:UNUSED_PAD src0_sel:DWORD src1_sel:WORD_1
v_mfma_f32_16x16x32_bf16 v[164:167],a[236:239],v[28:31],v[164:167]
s_add_u32 m0,s57,49504
ds_read_u16 v60,v60
v_lshlrev_b32_sdwa v32,1,v32 dst_sel:DWORD dst_unused:UNUSED_PAD src0_sel:DWORD src1_sel:WORD_1
v_mfma_f32_16x16x32_bf16 v[160:163],a[236:239],v[24:27],v[160:163]
s_add_u32 s89,s56,6291456
ds_read_u16_d16_hi v62,v62
v_lshlrev_b32_sdwa v34,1,v34 dst_sel:DWORD dst_unused:UNUSED_PAD src0_sel:DWORD src1_sel:WORD_1
v_mfma_f32_16x16x32_bf16 v[240:243],a[244:247],v[24:27],v[240:243]
buffer_load_dwordx4 v6,s[36:39],s89 offen lds
ds_read_u16 v61,v61
v_mfma_f32_16x16x32_bf16 v[244:247],a[244:247],v[28:31],v[244:247]
ds_read_u16_d16_hi v63,v63
v_lshlrev_b32_sdwa v33,1,v33 dst_sel:DWORD dst_unused:UNUSED_PAD src0_sel:DWORD src1_sel:WORD_1
v_mfma_f32_16x16x32_bf16 v[180:183],a[252:255],v[28:31],v[180:183]
ds_read_u16 v32,v32
v_lshlrev_b32_sdwa v35,1,v35 dst_sel:DWORD dst_unused:UNUSED_PAD src0_sel:DWORD src1_sel:WORD_1
v_mfma_f32_16x16x32_bf16 v[176:179],a[252:255],v[24:27],v[176:179]
s_waitcnt lgkmcnt(6)
ds_read_u16_d16_hi v34,v34
v_mfma_f32_16x16x32_bf16 v[248:251],a[240:243],v[24:27],v[248:251]
ds_read_u16 v33,v33
v_mfma_f32_16x16x32_bf16 v[252:255],a[240:243],v[28:31],v[252:255]
ds_read_u16_d16_hi v35,v35
v_mfma_f32_16x16x32_bf16 v[196:199],a[224:227],v[28:31],v[196:199]
v_mfma_f32_16x16x32_bf16 v[192:195],a[224:227],v[24:27],v[192:195]
v_mfma_f32_16x16x32_bf16 v[28:31],a[216:219],a[8:11],v[16:19]
v_lshlrev_b32_sdwa v48,1,v48 dst_sel:DWORD dst_unused:UNUSED_PAD src0_sel:DWORD src1_sel:WORD_1
v_fma_mixlo_f16 v8,v53,s99,0
v_mfma_f32_16x16x32_bf16 v[28:31],a[220:223],a[24:27],v[28:31]
ds_read_u16 v48,v48
v_lshl_add_u64 v[40:41],v[40:41],0,v[42:43]
v_mfma_f32_16x16x32_bf16 v[28:31],a[192:195],a[40:43],v[28:31]
s_waitcnt lgkmcnt(5)
v_lshl_add_u64 v[42:43],v[36:37],0,v[38:39]
v_lshlrev_b32_sdwa v50,1,v50 dst_sel:DWORD dst_unused:UNUSED_PAD src0_sel:DWORD src1_sel:WORD_1
v_mfma_f32_16x16x32_bf16 v[24:27],a[196:199],a[40:43],v[20:23]
ds_read_u16_d16_hi v50,v50
v_fma_mixhi_f16 v8,v55,s99,0
v_mfma_f32_16x16x32_bf16 v[24:27],a[200:203],a[24:27],v[24:27]
v_lshl_add_u64 v[36:37],v[60:61],0,v[62:63]
v_lshlrev_b32_sdwa v49,1,v49 dst_sel:DWORD dst_unused:UNUSED_PAD src0_sel:DWORD src1_sel:WORD_1
v_pk_fma_f16 v9,v8,v8,s54
v_mfma_f32_16x16x32_bf16 v[24:27],a[212:215],a[8:11],v[24:27]
ds_read_u16 v49,v49
v_lshlrev_b32_sdwa v51,1,v51 dst_sel:DWORD dst_unused:UNUSED_PAD src0_sel:DWORD src1_sel:WORD_1
v_mfma_f32_16x16x32_bf16 v[60:63],a[212:215],a[12:15],v[20:23]
ds_read_b128 v[20:23],v4 offset:384
v_pk_mul_f16 v9,v8,v9
v_mfma_f32_16x16x32_bf16 v[60:63],a[200:203],a[28:31],v[60:63]
s_waitcnt lgkmcnt(4)
ds_read_u16_d16_hi v51,v51
ds_read_b64_tr_b16 a[212:213],v1 offset:16416
v_lshl_add_u64 v[38:39],v[32:33],0,v[34:35]
v_lshlrev_b32_sdwa v44,1,v44 dst_sel:DWORD dst_unused:UNUSED_PAD src0_sel:DWORD src1_sel:WORD_1
v_mfma_f32_16x16x32_bf16 v[60:63],a[196:199],a[44:47],v[60:63]
ds_read_b64_tr_b16 a[214:215],v1 offset:17440
v_exp_f16_e64 v8,v9
v_mfma_f32_16x16x32_bf16 v[32:35],a[192:195],a[44:47],v[16:19]
ds_read_b128 v[16:19],v4 offset:416
ds_read_u16 v44,v44
v_lshlrev_b32_sdwa v46,1,v46 dst_sel:DWORD dst_unused:UNUSED_PAD src0_sel:DWORD src1_sel:WORD_1
v_mfma_f32_16x16x32_bf16 v[32:35],a[220:223],a[28:31],v[32:35]
ds_read_u16_d16_hi v46,v46
ds_read_b64_tr_b16 a[200:201],v2 offset:16416
v_lshlrev_b32_sdwa v45,1,v45 dst_sel:DWORD dst_unused:UNUSED_PAD src0_sel:DWORD src1_sel:WORD_1
v_mfma_f32_16x16x32_bf16 v[32:35],a[216:219],a[12:15],v[32:35]
ds_read_u16 v45,v45
v_exp_f16_sdwa v8,v9 dst_sel:WORD_1 dst_unused:UNUSED_PRESERVE src0_sel:WORD_1
v_mfma_f32_16x16x32_bf16 v[208:211],a[232:235],v[40:43],v[208:211]
ds_read_b64_tr_b16 a[202:203],v2 offset:17440
ds_read_b64_tr_b16 a[196:197],v2 offset:14368
v_lshlrev_b32_sdwa v47,1,v47 dst_sel:DWORD dst_unused:UNUSED_PAD src0_sel:DWORD src1_sel:WORD_1
v_mfma_f32_16x16x32_bf16 v[212:215],a[232:235],v[36:39],v[212:215]
ds_read_u16_d16_hi v47,v47
v_lshlrev_b32_sdwa v56,1,v56 dst_sel:DWORD dst_unused:UNUSED_PAD src0_sel:DWORD src1_sel:WORD_1
v_mfma_f32_16x16x32_bf16 v[172:175],a[236:239],v[36:39],v[172:175]
ds_read_b64_tr_b16 a[198:199],v2 offset:15392
v_pk_fma_f16 v8,v8,s55,s55
v_mfma_f32_16x16x32_bf16 v[168:171],a[236:239],v[40:43],v[168:171]
TP6487_%=:
s_cbranch_execz TR0_%=
v_rcp_f16 v9,v8
v_lshlrev_b32_sdwa v58,1,v58 dst_sel:DWORD dst_unused:UNUSED_PAD src0_sel:DWORD src1_sel:WORD_1
v_mfma_f32_16x16x32_bf16 v[216:219],a[244:247],v[40:43],v[216:219]
ds_read_u16 v56,v56
v_lshlrev_b32_sdwa v57,1,v57 dst_sel:DWORD dst_unused:UNUSED_PAD src0_sel:DWORD src1_sel:WORD_1
v_mfma_f32_16x16x32_bf16 v[220:223],a[244:247],v[36:39],v[220:223]
ds_read_u16_d16_hi v58,v58
v_lshlrev_b32_sdwa v59,1,v59 dst_sel:DWORD dst_unused:UNUSED_PAD src0_sel:DWORD src1_sel:WORD_1
v_mfma_f32_16x16x32_bf16 v[188:191],a[252:255],v[36:39],v[188:191]
ds_read_u16 v57,v57
v_lshlrev_b32_sdwa v52,1,v52 dst_sel:DWORD dst_unused:UNUSED_PAD src0_sel:DWORD src1_sel:WORD_1
v_mfma_f32_16x16x32_bf16 v[184:187],a[252:255],v[40:43],v[184:187]
ds_read_u16_d16_hi v59,v59
v_rcp_f16_sdwa v9,v8 dst_sel:WORD_1 dst_unused:UNUSED_PRESERVE src0_sel:WORD_1
v_mfma_f32_16x16x32_bf16 v[224:227],a[240:243],v[40:43],v[224:227]
ds_read_b64_tr_b16 a[192:193],v2 offset:12320
v_lshlrev_b32_sdwa v54,1,v54 dst_sel:DWORD dst_unused:UNUSED_PAD src0_sel:DWORD src1_sel:WORD_1
v_mfma_f32_16x16x32_bf16 v[228:231],a[240:243],v[36:39],v[228:231]
ds_read_b64_tr_b16 a[194:195],v2 offset:13344
v_fma_mix_f32 v53,v53,v9,0 op_sel_hi:[0,1,0]
v_mfma_f32_16x16x32_bf16 v[204:207],a[224:227],v[36:39],v[204:207]
ds_read_u16 v52,v52
v_fma_mix_f32 v55,v55,v9,0 op_sel:[0,1,0] op_sel_hi:[0,1,0]
v_mfma_f32_16x16x32_bf16 v[200:203],a[224:227],v[40:43],v[200:203]
s_waitcnt lgkmcnt(7)
ds_read_u16_d16_hi v54,v54
v_mfma_f32_16x16x32_bf16 v[36:39],a[208:211],a[48:51],v[20:23]
s_add_u32 m0,s57,53040
v_lshlrev_b32_sdwa v28,1,v28 dst_sel:DWORD dst_unused:UNUSED_PAD src0_sel:DWORD src1_sel:WORD_1
v_lshlrev_b32_sdwa v30,1,v30 dst_sel:DWORD dst_unused:UNUSED_PAD src0_sel:DWORD src1_sel:WORD_1
v_mfma_f32_16x16x32_bf16 v[36:39],a[204:207],a[64:67],v[36:39]
s_add_u32 s89,s56,6389760
ds_read_u16 v28,v28
v_lshl_add_u64 v[48:49],v[48:49],0,v[50:51]
v_mfma_f32_16x16x32_bf16 v[36:39],a[212:215],a[80:83],v[36:39]
buffer_load_dwordx4 v6,s[36:39],s89 offen lds
v_lshl_add_u64 v[50:51],v[44:45],0,v[46:47]
v_mfma_f32_16x16x32_bf16 v[40:43],a[200:203],a[80:83],v[16:19]
ds_read_u16_d16_hi v30,v30
v_lshlrev_b32_sdwa v29,1,v29 dst_sel:DWORD dst_unused:UNUSED_PAD src0_sel:DWORD src1_sel:WORD_1
v_mfma_f32_16x16x32_bf16 v[40:43],a[196:199],a[64:67],v[40:43]
s_waitcnt lgkmcnt(4)
ds_read_u16 v29,v29
v_lshl_add_u64 v[44:45],v[56:57],0,v[58:59]
v_mfma_f32_16x16x32_bf16 v[40:43],a[192:195],a[48:51],v[40:43]
v_lshlrev_b32_sdwa v31,1,v31 dst_sel:DWORD dst_unused:UNUSED_PAD src0_sel:DWORD src1_sel:WORD_1
v_lshlrev_b32_sdwa v24,1,v24 dst_sel:DWORD dst_unused:UNUSED_PAD src0_sel:DWORD src1_sel:WORD_1
v_mfma_f32_16x16x32_bf16 v[56:59],a[192:195],a[52:55],v[16:19]
s_waitcnt lgkmcnt(3)
ds_read_u16_d16_hi v31,v31
v_or_b32_e64 v46,v52,v54
v_mfma_f32_16x16x32_bf16 v[56:59],a[196:199],a[68:71],v[56:59]
ds_read_u16 v24,v24
v_cvt_pk_bf16_f32 v47,v53,v55
v_lshlrev_b32_sdwa v26,1,v26 dst_sel:DWORD dst_unused:UNUSED_PAD src0_sel:DWORD src1_sel:WORD_1
v_mfma_f32_16x16x32_bf16 v[56:59],a[200:203],a[84:87],v[56:59]
ds_read_u16_d16_hi v26,v26
v_lshlrev_b32_sdwa v25,1,v25 dst_sel:DWORD dst_unused:UNUSED_PAD src0_sel:DWORD src1_sel:WORD_1
v_mfma_f32_16x16x32_bf16 v[52:55],a[212:215],a[84:87],v[20:23]
ds_read_u16 v25,v25
v_lshlrev_b32_sdwa v27,1,v27 dst_sel:DWORD dst_unused:UNUSED_PAD src0_sel:DWORD src1_sel:WORD_1
v_mfma_f32_16x16x32_bf16 v[52:55],a[204:207],a[68:71],v[52:55]
ds_read_u16_d16_hi v27,v27
v_lshlrev_b32_sdwa v32,1,v32 dst_sel:DWORD dst_unused:UNUSED_PAD src0_sel:DWORD src1_sel:WORD_1
v_mfma_f32_16x16x32_bf16 v[52:55],a[208:211],a[52:55],v[52:55]
ds_read_u16 v32,v32
v_lshlrev_b32_sdwa v34,1,v34 dst_sel:DWORD dst_unused:UNUSED_PAD src0_sel:DWORD src1_sel:WORD_1
v_mfma_f32_16x16x32_bf16 v[136:139],a[232:235],v[48:51],v[136:139]
ds_read_u16_d16_hi v34,v34
v_lshlrev_b32_sdwa v33,1,v33 dst_sel:DWORD dst_unused:UNUSED_PAD src0_sel:DWORD src1_sel:WORD_1
v_mfma_f32_16x16x32_bf16 v[140:143],a[232:235],v[44:47],v[140:143]
ds_read_u16 v33,v33
v_lshlrev_b32_sdwa v35,1,v35 dst_sel:DWORD dst_unused:UNUSED_PAD src0_sel:DWORD src1_sel:WORD_1
v_mfma_f32_16x16x32_bf16 v[68:71],a[236:239],v[44:47],v[68:71]
ds_read_u16_d16_hi v35,v35
ds_read_b64_tr_b16 a[248:249],v3 offset:18480
v_mfma_f32_16x16x32_bf16 v[64:67],a[236:239],v[48:51],v[64:67]
ds_read_b64_tr_b16 a[250:251],v3 offset:20016
v_lshlrev_b32_sdwa v60,1,v60 dst_sel:DWORD dst_unused:UNUSED_PAD src0_sel:DWORD src1_sel:WORD_1
v_mfma_f32_16x16x32_bf16 v[144:147],a[244:247],v[48:51],v[144:147]
ds_read_u16 v60,v60
v_lshlrev_b32_sdwa v62,1,v62 dst_sel:DWORD dst_unused:UNUSED_PAD src0_sel:DWORD src1_sel:WORD_1
v_mfma_f32_16x16x32_bf16 v[148:151],a[244:247],v[44:47],v[148:151]
ds_read_u16_d16_hi v62,v62
v_lshlrev_b32_sdwa v61,1,v61 dst_sel:DWORD dst_unused:UNUSED_PAD src0_sel:DWORD src1_sel:WORD_1
v_mfma_f32_16x16x32_bf16 v[84:87],a[252:255],v[44:47],v[84:87]
ds_read_u16 v61,v61
v_lshlrev_b32_sdwa v63,1,v63 dst_sel:DWORD dst_unused:UNUSED_PAD src0_sel:DWORD src1_sel:WORD_1
v_mfma_f32_16x16x32_bf16 v[80:83],a[252:255],v[48:51],v[80:83]
ds_read_u16_d16_hi v63,v63
v_mfma_f32_16x16x32_bf16 v[152:155],a[240:243],v[48:51],v[152:155]
ds_read_b64_tr_b16 a[228:229],v3 offset:18488
v_mfma_f32_16x16x32_bf16 v[156:159],a[240:243],v[44:47],v[156:159]
ds_read_b64_tr_b16 a[230:231],v3 offset:20024
v_mfma_f32_16x16x32_bf16 v[100:103],a[224:227],v[44:47],v[100:103]
v_mfma_f32_16x16x32_bf16 v[96:99],a[224:227],v[48:51],v[96:99]
v_mfma_f32_16x16x32_bf16 v[44:47],a[208:211],a[56:59],v[20:23]
ds_read_b64_tr_b16 a[220:221],v1 offset:18480
v_lshlrev_b32_sdwa v36,1,v36 dst_sel:DWORD dst_unused:UNUSED_PAD src0_sel:DWORD src1_sel:WORD_1
v_mfma_f32_16x16x32_bf16 v[44:47],a[204:207],a[72:75],v[44:47]
s_waitcnt lgkmcnt(6)
ds_read_u16 v36,v36
v_lshl_add_u64 v[28:29],v[28:29],0,v[30:31]
v_fma_mixlo_f16 v8,v57,s99,0
v_mfma_f32_16x16x32_bf16 v[44:47],a[212:215],a[88:91],v[44:47]
ds_read_b64_tr_b16 a[222:223],v1 offset:19504
v_lshl_add_u64 v[30:31],v[24:25],0,v[26:27]
v_lshlrev_b32_sdwa v38,1,v38 dst_sel:DWORD dst_unused:UNUSED_PAD src0_sel:DWORD src1_sel:WORD_1
v_mfma_f32_16x16x32_bf16 v[48:51],a[200:203],a[88:91],v[16:19]
ds_read_u16_d16_hi v38,v38
v_fma_mixhi_f16 v8,v59,s99,0
v_mfma_f32_16x16x32_bf16 v[48:51],a[196:199],a[72:75],v[48:51]
ds_read_b64_tr_b16 a[216:217],v1 offset:20528
v_lshl_add_u64 v[24:25],v[32:33],0,v[34:35]
v_lshlrev_b32_sdwa v37,1,v37 dst_sel:DWORD dst_unused:UNUSED_PAD src0_sel:DWORD src1_sel:WORD_1
v_pk_fma_f16 v9,v8,v8,s54
v_mfma_f32_16x16x32_bf16 v[48:51],a[192:195],a[56:59],v[48:51]
ds_read_u16 v37,v37
v_lshlrev_b32_sdwa v39,1,v39 dst_sel:DWORD dst_unused:UNUSED_PAD src0_sel:DWORD src1_sel:WORD_1
v_mfma_f32_16x16x32_bf16 v[32:35],a[192:195],a[60:63],v[16:19]
ds_read_u16_d16_hi v39,v39
v_pk_mul_f16 v9,v8,v9
v_mfma_f32_16x16x32_bf16 v[32:35],a[196:199],a[76:79],v[32:35]
s_waitcnt lgkmcnt(7)
ds_read_b64_tr_b16 a[218:219],v1 offset:21552
v_lshl_add_u64 v[26:27],v[60:61],0,v[62:63]
v_lshlrev_b32_sdwa v40,1,v40 dst_sel:DWORD dst_unused:UNUSED_PAD src0_sel:DWORD src1_sel:WORD_1
v_mfma_f32_16x16x32_bf16 v[32:35],a[200:203],a[92:95],v[32:35]
v_exp_f16 v8,v9
v_mfma_f32_16x16x32_bf16 v[60:63],a[212:215],a[92:95],v[20:23]
ds_read_u16 v40,v40
v_lshlrev_b32_sdwa v42,1,v42 dst_sel:DWORD dst_unused:UNUSED_PAD src0_sel:DWORD src1_sel:WORD_1
v_mfma_f32_16x16x32_bf16 v[60:63],a[204:207],a[76:79],v[60:63]
ds_read_u16_d16_hi v42,v42
v_lshlrev_b32_sdwa v41,1,v41 dst_sel:DWORD dst_unused:UNUSED_PAD src0_sel:DWORD src1_sel:WORD_1
v_mfma_f32_16x16x32_bf16 v[60:63],a[208:211],a[60:63],v[60:63]
v_exp_f16_sdwa v8,v9 dst_sel:WORD_1 dst_unused:UNUSED_PRESERVE src0_sel:WORD_1
v_mfma_f32_16x16x32_bf16 v[112:115],a[232:235],v[28:31],v[112:115]
ds_read_u16 v41,v41
v_lshlrev_b32_sdwa v43,1,v43 dst_sel:DWORD dst_unused:UNUSED_PAD src0_sel:DWORD src1_sel:WORD_1
v_mfma_f32_16x16x32_bf16 v[116:119],a[232:235],v[24:27],v[116:119]
ds_read_u16_d16_hi v43,v43
v_lshlrev_b32_sdwa v52,1,v52 dst_sel:DWORD dst_unused:UNUSED_PAD src0_sel:DWORD src1_sel:WORD_1
v_mfma_f32_16x16x32_bf16 v[76:79],a[236:239],v[24:27],v[76:79]
ds_read_u16 v52,v52
v_pk_fma_f16 v8,v8,s55,s55
v_mfma_f32_16x16x32_bf16 v[72:75],a[236:239],v[28:31],v[72:75]
ds_read_b64_tr_b16 a[232:233],v3 offset:18544
v_rcp_f16_e64 v9,v8
v_lshlrev_b32_sdwa v54,1,v54 dst_sel:DWORD dst_unused:UNUSED_PAD src0_sel:DWORD src1_sel:WORD_1
v_mfma_f32_16x16x32_bf16 v[120:123],a[244:247],v[28:31],v[120:123]
ds_read_u16_d16_hi v54,v54
v_lshlrev_b32_sdwa v53,1,v53 dst_sel:DWORD dst_unused:UNUSED_PAD src0_sel:DWORD src1_sel:WORD_1
v_mfma_f32_16x16x32_bf16 v[124:127],a[244:247],v[24:27],v[124:127]
ds_read_u16 v53,v53
ds_read_b64_tr_b16 a[234:235],v3 offset:20080
v_mfma_f32_16x16x32_bf16 v[92:95],a[252:255],v[24:27],v[92:95]
ds_read_b64_tr_b16 a[236:237],v3 offset:18552
v_lshlrev_b32_sdwa v55,1,v55 dst_sel:DWORD dst_unused:UNUSED_PAD src0_sel:DWORD src1_sel:WORD_1
v_mfma_f32_16x16x32_bf16 v[88:91],a[252:255],v[28:31],v[88:91]
ds_read_u16_d16_hi v55,v55
v_rcp_f16_sdwa v9,v8 dst_sel:WORD_1 dst_unused:UNUSED_PRESERVE src0_sel:WORD_1
v_mfma_f32_16x16x32_bf16 v[128:131],a[240:243],v[28:31],v[128:131]
ds_read_b64_tr_b16 a[238:239],v3 offset:20088
ds_read_b64_tr_b16 a[244:245],v3 offset:18608
v_lshlrev_b32_sdwa v56,1,v56 dst_sel:DWORD dst_unused:UNUSED_PAD src0_sel:DWORD src1_sel:WORD_1
v_mfma_f32_16x16x32_bf16 v[132:135],a[240:243],v[24:27],v[132:135]
ds_read_u16 v56,v56
v_lshlrev_b32_sdwa v58,1,v58 dst_sel:DWORD dst_unused:UNUSED_PAD src0_sel:DWORD src1_sel:WORD_1
v_mfma_f32_16x16x32_bf16 v[108:111],a[224:227],v[24:27],v[108:111]
ds_read_u16_d16_hi v58,v58
v_fma_mix_f32 v57,v57,v9,0 op_sel_hi:[0,1,0]
v_mfma_f32_16x16x32_bf16 v[104:107],a[224:227],v[28:31],v[104:107]
ds_read_b64_tr_b16 a[246:247],v3 offset:20144
v_fma_mix_f32 v59,v59,v9,0 op_sel:[0,1,0] op_sel_hi:[0,1,0]
v_mfma_f32_16x16x32_bf16 v[24:27],a[208:211],a[0:3],v[20:23]
ds_read_b64_tr_b16 a[252:253],v3 offset:18616
v_lshlrev_b32_sdwa v44,1,v44 dst_sel:DWORD dst_unused:UNUSED_PAD src0_sel:DWORD src1_sel:WORD_1
v_mfma_f32_16x16x32_bf16 v[24:27],a[204:207],a[16:19],v[24:27]
s_waitcnt lgkmcnt(10)
ds_read_b64_tr_b16 a[254:255],v3 offset:20152
v_lshl_add_u64 v[36:37],v[36:37],0,v[38:39]
v_mfma_f32_16x16x32_bf16 v[24:27],a[212:215],a[32:35],v[24:27]
ds_read_u16 v44,v44
v_lshl_add_u64 v[38:39],v[40:41],0,v[42:43]
v_lshlrev_b32_sdwa v46,1,v46 dst_sel:DWORD dst_unused:UNUSED_PAD src0_sel:DWORD src1_sel:WORD_1
v_mfma_f32_16x16x32_bf16 v[28:31],a[200:203],a[32:35],v[16:19]
ds_read_u16_d16_hi v46,v46
v_lshlrev_b32_sdwa v45,1,v45 dst_sel:DWORD dst_unused:UNUSED_PAD src0_sel:DWORD src1_sel:WORD_1
v_mfma_f32_16x16x32_bf16 v[28:31],a[196:199],a[16:19],v[28:31]
s_waitcnt lgkmcnt(9)
ds_read_u16 v45,v45
v_lshl_add_u64 v[40:41],v[52:53],0,v[54:55]
v_mfma_f32_16x16x32_bf16 v[28:31],a[192:195],a[0:3],v[28:31]
v_lshlrev_b32_sdwa v47,1,v47 dst_sel:DWORD dst_unused:UNUSED_PAD src0_sel:DWORD src1_sel:WORD_1
v_lshlrev_b32_sdwa v48,1,v48 dst_sel:DWORD dst_unused:UNUSED_PAD src0_sel:DWORD src1_sel:WORD_1
v_mfma_f32_16x16x32_bf16 v[52:55],a[192:195],a[4:7],v[16:19]
s_waitcnt lgkmcnt(6)
ds_read_u16_d16_hi v47,v47
v_or_b32 v42,v56,v58
v_mfma_f32_16x16x32_bf16 v[52:55],a[196:199],a[20:23],v[52:55]
ds_read_u16 v48,v48
v_cvt_pk_bf16_f32 v43,v57,v59
v_lshlrev_b32_sdwa v50,1,v50 dst_sel:DWORD dst_unused:UNUSED_PAD src0_sel:DWORD src1_sel:WORD_1
v_mfma_f32_16x16x32_bf16 v[52:55],a[200:203],a[36:39],v[52:55]
ds_read_u16_d16_hi v50,v50
v_lshlrev_b32_sdwa v49,1,v49 dst_sel:DWORD dst_unused:UNUSED_PAD src0_sel:DWORD src1_sel:WORD_1
v_mfma_f32_16x16x32_bf16 v[56:59],a[212:215],a[36:39],v[20:23]
ds_read_u16 v49,v49
v_lshlrev_b32_sdwa v51,1,v51 dst_sel:DWORD dst_unused:UNUSED_PAD src0_sel:DWORD src1_sel:WORD_1
v_mfma_f32_16x16x32_bf16 v[56:59],a[204:207],a[20:23],v[56:59]
ds_read_u16_d16_hi v51,v51
v_lshlrev_b32_sdwa v60,1,v60 dst_sel:DWORD dst_unused:UNUSED_PAD src0_sel:DWORD src1_sel:WORD_1
v_mfma_f32_16x16x32_bf16 v[56:59],a[208:211],a[4:7],v[56:59]
ds_read_u16 v60,v60
v_lshlrev_b32_sdwa v62,1,v62 dst_sel:DWORD dst_unused:UNUSED_PAD src0_sel:DWORD src1_sel:WORD_1
v_mfma_f32_16x16x32_bf16 v[232:235],a[248:251],v[36:39],v[232:235]
ds_read_u16_d16_hi v62,v62
v_lshlrev_b32_sdwa v61,1,v61 dst_sel:DWORD dst_unused:UNUSED_PAD src0_sel:DWORD src1_sel:WORD_1
v_mfma_f32_16x16x32_bf16 v[236:239],a[248:251],v[40:43],v[236:239]
ds_read_u16 v61,v61
v_lshlrev_b32_sdwa v63,1,v63 dst_sel:DWORD dst_unused:UNUSED_PAD src0_sel:DWORD src1_sel:WORD_1
v_mfma_f32_16x16x32_bf16 v[164:167],a[228:231],v[40:43],v[164:167]
ds_read_u16_d16_hi v63,v63
v_lshlrev_b32_sdwa v32,1,v32 dst_sel:DWORD dst_unused:UNUSED_PAD src0_sel:DWORD src1_sel:WORD_1
v_mfma_f32_16x16x32_bf16 v[160:163],a[228:231],v[36:39],v[160:163]
ds_read_u16 v32,v32
v_lshlrev_b32_sdwa v34,1,v34 dst_sel:DWORD dst_unused:UNUSED_PAD src0_sel:DWORD src1_sel:WORD_1
v_mfma_f32_16x16x32_bf16 v[240:243],a[232:235],v[36:39],v[240:243]
ds_read_u16_d16_hi v34,v34
v_lshlrev_b32_sdwa v33,1,v33 dst_sel:DWORD dst_unused:UNUSED_PAD src0_sel:DWORD src1_sel:WORD_1
v_mfma_f32_16x16x32_bf16 v[244:247],a[232:235],v[40:43],v[244:247]
ds_read_u16 v33,v33
v_lshlrev_b32_sdwa v35,1,v35 dst_sel:DWORD dst_unused:UNUSED_PAD src0_sel:DWORD src1_sel:WORD_1
v_mfma_f32_16x16x32_bf16 v[180:183],a[236:239],v[40:43],v[180:183]
ds_read_u16_d16_hi v35,v35
v_mfma_f32_16x16x32_bf16 v[176:179],a[236:239],v[36:39],v[176:179]
s_waitcnt lgkmcnt(8)
v_mfma_f32_16x16x32_bf16 v[248:251],a[244:247],v[36:39],v[248:251]
v_mfma_f32_16x16x32_bf16 v[252:255],a[244:247],v[40:43],v[252:255]
v_mfma_f32_16x16x32_bf16 v[196:199],a[252:255],v[40:43],v[196:199]
v_mfma_f32_16x16x32_bf16 v[192:195],a[252:255],v[36:39],v[192:195]
v_mfma_f32_16x16x32_bf16 v[40:43],a[208:211],a[8:11],v[20:23]
v_lshlrev_b32_sdwa v24,1,v24 dst_sel:DWORD dst_unused:UNUSED_PAD src0_sel:DWORD src1_sel:WORD_1
v_fma_mixlo_f16 v8,v53,s99,0
v_mfma_f32_16x16x32_bf16 v[40:43],a[204:207],a[24:27],v[40:43]
ds_read_u16 v24,v24
v_lshl_add_u64 v[44:45],v[44:45],0,v[46:47]
v_mfma_f32_16x16x32_bf16 v[40:43],a[212:215],a[40:43],v[40:43]
v_lshl_add_u64 v[46:47],v[48:49],0,v[50:51]
v_lshlrev_b32_sdwa v26,1,v26 dst_sel:DWORD dst_unused:UNUSED_PAD src0_sel:DWORD src1_sel:WORD_1
v_mfma_f32_16x16x32_bf16 v[36:39],a[200:203],a[40:43],v[16:19]
ds_read_u16_d16_hi v26,v26
v_fma_mixhi_f16 v8,v55,s99,0
v_mfma_f32_16x16x32_bf16 v[36:39],a[196:199],a[24:27],v[36:39]
s_waitcnt lgkmcnt(2)
v_lshl_add_u64 v[48:49],v[60:61],0,v[62:63]
v_lshlrev_b32_sdwa v25,1,v25 dst_sel:DWORD dst_unused:UNUSED_PAD src0_sel:DWORD src1_sel:WORD_1
v_pk_fma_f16 v9,v8,v8,s54
v_mfma_f32_16x16x32_bf16 v[36:39],a[192:195],a[8:11],v[36:39]
ds_read_u16 v25,v25
v_lshlrev_b32_sdwa v27,1,v27 dst_sel:DWORD dst_unused:UNUSED_PAD src0_sel:DWORD src1_sel:WORD_1
v_mfma_f32_16x16x32_bf16 v[60:63],a[192:195],a[12:15],v[16:19]
ds_read_b128 v[16:19],v4 offset:512
v_pk_mul_f16 v9,v8,v9
v_mfma_f32_16x16x32_bf16 v[60:63],a[196:199],a[28:31],v[60:63]
ds_read_u16_d16_hi v27,v27
ds_read_b64_tr_b16 a[192:193],v1 offset:22576
v_lshl_add_u64 v[50:51],v[32:33],0,v[34:35]
v_lshlrev_b32_sdwa v28,1,v28 dst_sel:DWORD dst_unused:UNUSED_PAD src0_sel:DWORD src1_sel:WORD_1
v_mfma_f32_16x16x32_bf16 v[60:63],a[200:203],a[44:47],v[60:63]
ds_read_b64_tr_b16 a[194:195],v1 offset:23600
v_exp_f16 v8,v9
v_mfma_f32_16x16x32_bf16 v[32:35],a[212:215],a[44:47],v[20:23]
ds_read_b128 v[20:23],v4 offset:544
ds_read_u16 v28,v28
v_lshlrev_b32_sdwa v30,1,v30 dst_sel:DWORD dst_unused:UNUSED_PAD src0_sel:DWORD src1_sel:WORD_1
v_mfma_f32_16x16x32_bf16 v[32:35],a[204:207],a[28:31],v[32:35]
ds_read_u16_d16_hi v30,v30
ds_read_b64_tr_b16 a[196:197],v2 offset:22576
v_lshlrev_b32_sdwa v29,1,v29 dst_sel:DWORD dst_unused:UNUSED_PAD src0_sel:DWORD src1_sel:WORD_1
v_mfma_f32_16x16x32_bf16 v[32:35],a[208:211],a[12:15],v[32:35]
ds_read_u16 v29,v29
v_exp_f16_sdwa v8,v9 dst_sel:WORD_1 dst_unused:UNUSED_PRESERVE src0_sel:WORD_1
v_mfma_f32_16x16x32_bf16 v[208:211],a[248:251],v[44:47],v[208:211]
ds_read_b64_tr_b16 a[198:199],v2 offset:23600
ds_read_b64_tr_b16 a[200:201],v2 offset:20528
v_lshlrev_b32_sdwa v31,1,v31 dst_sel:DWORD dst_unused:UNUSED_PAD src0_sel:DWORD src1_sel:WORD_1
v_mfma_f32_16x16x32_bf16 v[212:215],a[248:251],v[48:51],v[212:215]
ds_read_u16_d16_hi v31,v31
v_lshlrev_b32_sdwa v56,1,v56 dst_sel:DWORD dst_unused:UNUSED_PAD src0_sel:DWORD src1_sel:WORD_1
v_mfma_f32_16x16x32_bf16 v[172:175],a[228:231],v[48:51],v[172:175]
ds_read_b64_tr_b16 a[202:203],v2 offset:21552
v_pk_fma_f16 v8,v8,s55,s55
v_mfma_f32_16x16x32_bf16 v[168:171],a[228:231],v[44:47],v[168:171]
v_rcp_f16 v9,v8
v_lshlrev_b32_sdwa v58,1,v58 dst_sel:DWORD dst_unused:UNUSED_PAD src0_sel:DWORD src1_sel:WORD_1
v_mfma_f32_16x16x32_bf16 v[216:219],a[232:235],v[44:47],v[216:219]
ds_read_u16 v56,v56
v_lshlrev_b32_sdwa v57,1,v57 dst_sel:DWORD dst_unused:UNUSED_PAD src0_sel:DWORD src1_sel:WORD_1
v_mfma_f32_16x16x32_bf16 v[220:223],a[232:235],v[48:51],v[220:223]
ds_read_u16_d16_hi v58,v58
v_lshlrev_b32_sdwa v59,1,v59 dst_sel:DWORD dst_unused:UNUSED_PAD src0_sel:DWORD src1_sel:WORD_1
v_mfma_f32_16x16x32_bf16 v[188:191],a[236:239],v[48:51],v[188:191]
ds_read_u16 v57,v57
v_lshlrev_b32_sdwa v52,1,v52 dst_sel:DWORD dst_unused:UNUSED_PAD src0_sel:DWORD src1_sel:WORD_1
v_mfma_f32_16x16x32_bf16 v[184:187],a[236:239],v[44:47],v[184:187]
ds_read_u16_d16_hi v59,v59
v_rcp_f16_sdwa v9,v8 dst_sel:WORD_1 dst_unused:UNUSED_PRESERVE src0_sel:WORD_1
v_mfma_f32_16x16x32_bf16 v[224:227],a[244:247],v[44:47],v[224:227]
ds_read_b64_tr_b16 a[212:213],v2 offset:18480
v_lshlrev_b32_sdwa v54,1,v54 dst_sel:DWORD dst_unused:UNUSED_PAD src0_sel:DWORD src1_sel:WORD_1
v_mfma_f32_16x16x32_bf16 v[228:231],a[244:247],v[48:51],v[228:231]
ds_read_b64_tr_b16 a[214:215],v2 offset:19504
v_fma_mix_f32 v53,v53,v9,0 op_sel_hi:[0,1,0]
v_mfma_f32_16x16x32_bf16 v[204:207],a[252:255],v[48:51],v[204:207]
ds_read_u16 v52,v52
v_fma_mix_f32 v55,v55,v9,0 op_sel:[0,1,0] op_sel_hi:[0,1,0]
v_mfma_f32_16x16x32_bf16 v[200:203],a[252:255],v[44:47],v[200:203]
s_waitcnt lgkmcnt(7)
ds_read_u16_d16_hi v54,v54
v_mfma_f32_16x16x32_bf16 v[48:51],a[220:223],a[48:51],v[16:19]
v_lshlrev_b32_sdwa v40,1,v40 dst_sel:DWORD dst_unused:UNUSED_PAD src0_sel:DWORD src1_sel:WORD_1
v_lshlrev_b32_sdwa v42,1,v42 dst_sel:DWORD dst_unused:UNUSED_PAD src0_sel:DWORD src1_sel:WORD_1
v_mfma_f32_16x16x32_bf16 v[48:51],a[216:219],a[64:67],v[48:51]
ds_read_u16 v40,v40
v_lshl_add_u64 v[24:25],v[24:25],0,v[26:27]
v_mfma_f32_16x16x32_bf16 v[48:51],a[192:195],a[80:83],v[48:51]
ds_read_u16_d16_hi v42,v42
v_lshl_add_u64 v[26:27],v[28:29],0,v[30:31]
v_mfma_f32_16x16x32_bf16 v[44:47],a[196:199],a[80:83],v[20:23]
v_lshlrev_b32_sdwa v41,1,v41 dst_sel:DWORD dst_unused:UNUSED_PAD src0_sel:DWORD src1_sel:WORD_1
v_lshlrev_b32_sdwa v43,1,v43 dst_sel:DWORD dst_unused:UNUSED_PAD src0_sel:DWORD src1_sel:WORD_1
v_mfma_f32_16x16x32_bf16 v[44:47],a[200:203],a[64:67],v[44:47]
s_waitcnt lgkmcnt(4)
ds_read_u16 v41,v41
v_lshl_add_u64 v[28:29],v[56:57],0,v[58:59]
v_mfma_f32_16x16x32_bf16 v[44:47],a[212:215],a[48:51],v[44:47]
ds_read_u16_d16_hi v43,v43
v_lshlrev_b32_sdwa v36,1,v36 dst_sel:DWORD dst_unused:UNUSED_PAD src0_sel:DWORD src1_sel:WORD_1
v_mfma_f32_16x16x32_bf16 v[56:59],a[212:215],a[52:55],v[20:23]
s_waitcnt lgkmcnt(4)
ds_read_u16 v36,v36
v_or_b32 v30,v52,v54
v_mfma_f32_16x16x32_bf16 v[56:59],a[200:203],a[68:71],v[56:59]
s_waitcnt vmcnt(6)
v_cvt_pk_bf16_f32 v31,v53,v55
v_lshlrev_b32_sdwa v38,1,v38 dst_sel:DWORD dst_unused:UNUSED_PAD src0_sel:DWORD src1_sel:WORD_1
v_mfma_f32_16x16x32_bf16 v[56:59],a[196:199],a[84:87],v[56:59]
s_barrier
ds_read_u16_d16_hi v38,v38
v_lshlrev_b32_sdwa v37,1,v37 dst_sel:DWORD dst_unused:UNUSED_PAD src0_sel:DWORD src1_sel:WORD_1
v_mfma_f32_16x16x32_bf16 v[52:55],a[192:195],a[84:87],v[16:19]
ds_read_u16 v37,v37
v_lshlrev_b32_sdwa v39,1,v39 dst_sel:DWORD dst_unused:UNUSED_PAD src0_sel:DWORD src1_sel:WORD_1
v_mfma_f32_16x16x32_bf16 v[52:55],a[216:219],a[68:71],v[52:55]
s_add_u32 m0,s58,12320
ds_read_u16_d16_hi v39,v39
v_lshlrev_b32_sdwa v32,1,v32 dst_sel:DWORD dst_unused:UNUSED_PAD src0_sel:DWORD src1_sel:WORD_1
v_mfma_f32_16x16x32_bf16 v[52:55],a[220:223],a[52:55],v[52:55]
s_add_u32 s69,s69,s59
ds_read_u16 v32,v32
v_lshlrev_b32_sdwa v34,1,v34 dst_sel:DWORD dst_unused:UNUSED_PAD src0_sel:DWORD src1_sel:WORD_1
v_mfma_f32_16x16x32_bf16 v[136:139],a[248:251],v[24:27],v[136:139]
buffer_load_dwordx4 v5,s[44:47],s69 offen offset:0 lds
v_lshlrev_b32_sdwa v33,1,v33 dst_sel:DWORD dst_unused:UNUSED_PAD src0_sel:DWORD src1_sel:WORD_1
v_mfma_f32_16x16x32_bf16 v[140:143],a[248:251],v[28:31],v[140:143]
ds_read_u16_d16_hi v34,v34
v_lshlrev_b32_sdwa v35,1,v35 dst_sel:DWORD dst_unused:UNUSED_PAD src0_sel:DWORD src1_sel:WORD_1
v_mfma_f32_16x16x32_bf16 v[68:71],a[228:231],v[28:31],v[68:71]
ds_read_u16 v33,v33
ds_read_u16_d16_hi v35,v35
v_mfma_f32_16x16x32_bf16 v[64:67],a[228:231],v[24:27],v[64:67]
ds_read_b64_tr_b16 a[240:241],v3 offset:0
v_lshlrev_b32_sdwa v60,1,v60 dst_sel:DWORD dst_unused:UNUSED_PAD src0_sel:DWORD src1_sel:WORD_1
v_mfma_f32_16x16x32_bf16 v[144:147],a[232:235],v[24:27],v[144:147]
s_add_u32 s69,s69,s53
ds_read_u16 v60,v60
v_lshlrev_b32_sdwa v62,1,v62 dst_sel:DWORD dst_unused:UNUSED_PAD src0_sel:DWORD src1_sel:WORD_1
v_mfma_f32_16x16x32_bf16 v[148:151],a[232:235],v[28:31],v[148:151]
buffer_load_dwordx4 v5,s[44:47],s69 offen offset:1024 lds
ds_read_u16_d16_hi v62,v62
v_mfma_f32_16x16x32_bf16 v[84:87],a[236:239],v[28:31],v[84:87]
ds_read_b64_tr_b16 a[242:243],v3 offset:1536
v_lshlrev_b32_sdwa v61,1,v61 dst_sel:DWORD dst_unused:UNUSED_PAD src0_sel:DWORD src1_sel:WORD_1
v_mfma_f32_16x16x32_bf16 v[80:83],a[236:239],v[24:27],v[80:83]
ds_read_u16 v61,v61
v_lshlrev_b32_sdwa v63,1,v63 dst_sel:DWORD dst_unused:UNUSED_PAD src0_sel:DWORD src1_sel:WORD_1
v_mfma_f32_16x16x32_bf16 v[152:155],a[244:247],v[24:27],v[152:155]
ds_read_u16_d16_hi v63,v63
v_mfma_f32_16x16x32_bf16 v[156:159],a[244:247],v[28:31],v[156:159]
s_add_u32 s69,s69,s53
ds_read_b64_tr_b16 a[224:225],v3 offset:8
v_mfma_f32_16x16x32_bf16 v[100:103],a[252:255],v[28:31],v[100:103]
buffer_load_dwordx4 v5,s[44:47],s69 offen offset:2048 lds
v_mfma_f32_16x16x32_bf16 v[96:99],a[252:255],v[24:27],v[96:99]
ds_read_b64_tr_b16 a[226:227],v3 offset:1544
v_mfma_f32_16x16x32_bf16 v[28:31],a[220:223],a[56:59],v[16:19]
ds_read_b64_tr_b16 a[204:205],v1 offset:0
v_lshlrev_b32_sdwa v48,1,v48 dst_sel:DWORD dst_unused:UNUSED_PAD src0_sel:DWORD src1_sel:WORD_1
v_mfma_f32_16x16x32_bf16 v[28:31],a[216:219],a[72:75],v[28:31]
s_waitcnt lgkmcnt(7)
ds_read_u16 v48,v48
v_lshl_add_u64 v[40:41],v[40:41],0,v[42:43]
v_fma_mixlo_f16 v8,v57,s99,0
v_mfma_f32_16x16x32_bf16 v[28:31],a[192:195],a[88:91],v[28:31]
ds_read_b64_tr_b16 a[206:207],v1 offset:1024
v_lshl_add_u64 v[42:43],v[36:37],0,v[38:39]
v_lshlrev_b32_sdwa v50,1,v50 dst_sel:DWORD dst_unused:UNUSED_PAD src0_sel:DWORD src1_sel:WORD_1
v_mfma_f32_16x16x32_bf16 v[24:27],a[196:199],a[88:91],v[20:23]
ds_read_u16_d16_hi v50,v50
v_fma_mixhi_f16 v8,v59,s99,0
v_mfma_f32_16x16x32_bf16 v[24:27],a[200:203],a[72:75],v[24:27]
ds_read_b64_tr_b16 a[208:209],v1 offset:2048
v_lshl_add_u64 v[36:37],v[32:33],0,v[34:35]
v_lshlrev_b32_sdwa v49,1,v49 dst_sel:DWORD dst_unused:UNUSED_PAD src0_sel:DWORD src1_sel:WORD_1
v_pk_fma_f16 v9,v8,v8,s54
v_mfma_f32_16x16x32_bf16 v[24:27],a[212:215],a[56:59],v[24:27]
ds_read_u16 v49,v49
v_lshlrev_b32_sdwa v51,1,v51 dst_sel:DWORD dst_unused:UNUSED_PAD src0_sel:DWORD src1_sel:WORD_1
v_mfma_f32_16x16x32_bf16 v[32:35],a[212:215],a[60:63],v[20:23]
ds_read_u16_d16_hi v51,v51
v_pk_mul_f16 v9,v8,v9
v_mfma_f32_16x16x32_bf16 v[32:35],a[200:203],a[76:79],v[32:35]
s_waitcnt lgkmcnt(8)
ds_read_b64_tr_b16 a[210:211],v1 offset:3072
v_lshl_add_u64 v[38:39],v[60:61],0,v[62:63]
v_lshlrev_b32_sdwa v44,1,v44 dst_sel:DWORD dst_unused:UNUSED_PAD src0_sel:DWORD src1_sel:WORD_1
v_mfma_f32_16x16x32_bf16 v[32:35],a[196:199],a[92:95],v[32:35]
v_exp_f16 v8,v9
v_mfma_f32_16x16x32_bf16 v[60:63],a[192:195],a[92:95],v[16:19]
ds_read_u16 v44,v44
v_lshlrev_b32_sdwa v46,1,v46 dst_sel:DWORD dst_unused:UNUSED_PAD src0_sel:DWORD src1_sel:WORD_1
v_mfma_f32_16x16x32_bf16 v[60:63],a[216:219],a[76:79],v[60:63]
ds_read_u16_d16_hi v46,v46
v_lshlrev_b32_sdwa v45,1,v45 dst_sel:DWORD dst_unused:UNUSED_PAD src0_sel:DWORD src1_sel:WORD_1
v_mfma_f32_16x16x32_bf16 v[60:63],a[220:223],a[60:63],v[60:63]
v_exp_f16_sdwa v8,v9 dst_sel:WORD_1 dst_unused:UNUSED_PRESERVE src0_sel:WORD_1
v_mfma_f32_16x16x32_bf16 v[112:115],a[248:251],v[40:43],v[112:115]
ds_read_u16 v45,v45
v_lshlrev_b32_sdwa v47,1,v47 dst_sel:DWORD dst_unused:UNUSED_PAD src0_sel:DWORD src1_sel:WORD_1
v_mfma_f32_16x16x32_bf16 v[116:119],a[248:251],v[36:39],v[116:119]
ds_read_u16_d16_hi v47,v47
v_lshlrev_b32_sdwa v52,1,v52 dst_sel:DWORD dst_unused:UNUSED_PAD src0_sel:DWORD src1_sel:WORD_1
v_mfma_f32_16x16x32_bf16 v[76:79],a[228:231],v[36:39],v[76:79]
ds_read_u16 v52,v52
v_pk_fma_f16 v8,v8,s55,s55
v_mfma_f32_16x16x32_bf16 v[72:75],a[228:231],v[40:43],v[72:75]
ds_read_b64_tr_b16 a[248:249],v3 offset:64
v_rcp_f16_e64 v9,v8
v_lshlrev_b32_sdwa v54,1,v54 dst_sel:DWORD dst_unused:UNUSED_PAD src0_sel:DWORD src1_sel:WORD_1
v_mfma_f32_16x16x32_bf16 v[120:123],a[232:235],v[40:43],v[120:123]
ds_read_u16_d16_hi v54,v54
v_lshlrev_b32_sdwa v53,1,v53 dst_sel:DWORD dst_unused:UNUSED_PAD src0_sel:DWORD src1_sel:WORD_1
v_mfma_f32_16x16x32_bf16 v[124:127],a[232:235],v[36:39],v[124:127]
ds_read_u16 v53,v53
ds_read_b64_tr_b16 a[250:251],v3 offset:1600
v_mfma_f32_16x16x32_bf16 v[92:95],a[236:239],v[36:39],v[92:95]
ds_read_b64_tr_b16 a[228:229],v3 offset:72
v_lshlrev_b32_sdwa v55,1,v55 dst_sel:DWORD dst_unused:UNUSED_PAD src0_sel:DWORD src1_sel:WORD_1
v_mfma_f32_16x16x32_bf16 v[88:91],a[236:239],v[40:43],v[88:91]
ds_read_u16_d16_hi v55,v55
v_rcp_f16_sdwa v9,v8 dst_sel:WORD_1 dst_unused:UNUSED_PRESERVE src0_sel:WORD_1
v_mfma_f32_16x16x32_bf16 v[128:131],a[244:247],v[40:43],v[128:131]
ds_read_b64_tr_b16 a[230:231],v3 offset:1608
ds_read_b64_tr_b16 a[232:233],v3 offset:128
v_lshlrev_b32_sdwa v56,1,v56 dst_sel:DWORD dst_unused:UNUSED_PAD src0_sel:DWORD src1_sel:WORD_1
v_mfma_f32_16x16x32_bf16 v[132:135],a[244:247],v[36:39],v[132:135]
ds_read_u16 v56,v56
v_lshlrev_b32_sdwa v58,1,v58 dst_sel:DWORD dst_unused:UNUSED_PAD src0_sel:DWORD src1_sel:WORD_1
v_mfma_f32_16x16x32_bf16 v[108:111],a[252:255],v[36:39],v[108:111]
ds_read_u16_d16_hi v58,v58
v_fma_mix_f32 v57,v57,v9,0 op_sel_hi:[0,1,0]
v_mfma_f32_16x16x32_bf16 v[104:107],a[252:255],v[40:43],v[104:107]
ds_read_b64_tr_b16 a[234:235],v3 offset:1664
v_fma_mix_f32 v59,v59,v9,0 op_sel:[0,1,0] op_sel_hi:[0,1,0]
v_mfma_f32_16x16x32_bf16 v[36:39],a[220:223],a[0:3],v[16:19]
s_add_u32 m0,s58,18480
ds_read_b64_tr_b16 a[236:237],v3 offset:136
v_lshlrev_b32_sdwa v28,1,v28 dst_sel:DWORD dst_unused:UNUSED_PAD src0_sel:DWORD src1_sel:WORD_1
v_mfma_f32_16x16x32_bf16 v[36:39],a[216:219],a[16:19],v[36:39]
s_waitcnt lgkmcnt(10)
ds_read_b64_tr_b16 a[238:239],v3 offset:1672
v_lshl_add_u64 v[48:49],v[48:49],0,v[50:51]
v_mfma_f32_16x16x32_bf16 v[36:39],a[192:195],a[32:35],v[36:39]
s_add_u32 s69,s69,s59
ds_read_u16 v28,v28
v_lshl_add_u64 v[50:51],v[44:45],0,v[46:47]
v_lshlrev_b32_sdwa v30,1,v30 dst_sel:DWORD dst_unused:UNUSED_PAD src0_sel:DWORD src1_sel:WORD_1
v_mfma_f32_16x16x32_bf16 v[40:43],a[196:199],a[32:35],v[20:23]
buffer_load_dwordx4 v5,s[44:47],s69 offen offset:0 lds
v_lshlrev_b32_sdwa v29,1,v29 dst_sel:DWORD dst_unused:UNUSED_PAD src0_sel:DWORD src1_sel:WORD_1
v_mfma_f32_16x16x32_bf16 v[40:43],a[200:203],a[16:19],v[40:43]
s_waitcnt lgkmcnt(8)
ds_read_u16_d16_hi v30,v30
v_lshl_add_u64 v[44:45],v[52:53],0,v[54:55]
v_mfma_f32_16x16x32_bf16 v[40:43],a[212:215],a[0:3],v[40:43]
s_add_u32 s69,s69,s53
ds_read_u16 v29,v29
v_lshlrev_b32_sdwa v31,1,v31 dst_sel:DWORD dst_unused:UNUSED_PAD src0_sel:DWORD src1_sel:WORD_1
v_mfma_f32_16x16x32_bf16 v[52:55],a[212:215],a[4:7],v[20:23]
s_waitcnt lgkmcnt(6)
buffer_load_dwordx4 v5,s[44:47],s69 offen offset:1024 lds
v_or_b32 v46,v56,v58
v_lshlrev_b32_sdwa v24,1,v24 dst_sel:DWORD dst_unused:UNUSED_PAD src0_sel:DWORD src1_sel:WORD_1
v_mfma_f32_16x16x32_bf16 v[52:55],a[200:203],a[20:23],v[52:55]
s_add_u32 s69,s69,s53
ds_read_u16_d16_hi v31,v31
v_cvt_pk_bf16_f32 v47,v57,v59
v_lshlrev_b32_sdwa v26,1,v26 dst_sel:DWORD dst_unused:UNUSED_PAD src0_sel:DWORD src1_sel:WORD_1
v_mfma_f32_16x16x32_bf16 v[52:55],a[196:199],a[36:39],v[52:55]
buffer_load_dwordx4 v5,s[44:47],s69 offen offset:2048 lds
v_lshlrev_b32_sdwa v25,1,v25 dst_sel:DWORD dst_unused:UNUSED_PAD src0_sel:DWORD src1_sel:WORD_1
v_mfma_f32_16x16x32_bf16 v[56:59],a[192:195],a[36:39],v[16:19]
ds_read_u16 v24,v24
v_lshlrev_b32_sdwa v27,1,v27 dst_sel:DWORD dst_unused:UNUSED_PAD src0_sel:DWORD src1_sel:WORD_1
v_mfma_f32_16x16x32_bf16 v[56:59],a[216:219],a[20:23],v[56:59]
ds_read_u16_d16_hi v26,v26
v_lshlrev_b32_sdwa v60,1,v60 dst_sel:DWORD dst_unused:UNUSED_PAD src0_sel:DWORD src1_sel:WORD_1
v_mfma_f32_16x16x32_bf16 v[56:59],a[220:223],a[4:7],v[56:59]
ds_read_u16 v25,v25
v_lshlrev_b32_sdwa v62,1,v62 dst_sel:DWORD dst_unused:UNUSED_PAD src0_sel:DWORD src1_sel:WORD_1
v_mfma_f32_16x16x32_bf16 v[232:235],a[240:243],v[48:51],v[232:235]
ds_read_u16_d16_hi v27,v27
v_lshlrev_b32_sdwa v61,1,v61 dst_sel:DWORD dst_unused:UNUSED_PAD src0_sel:DWORD src1_sel:WORD_1
v_mfma_f32_16x16x32_bf16 v[236:239],a[240:243],v[44:47],v[236:239]
ds_read_u16 v60,v60
v_lshlrev_b32_sdwa v63,1,v63 dst_sel:DWORD dst_unused:UNUSED_PAD src0_sel:DWORD src1_sel:WORD_1
v_mfma_f32_16x16x32_bf16 v[164:167],a[224:227],v[44:47],v[164:167]
TP7016_%=:
s_cbranch_execz TR0_%=
ds_read_u16_d16_hi v62,v62
v_lshlrev_b32_sdwa v32,1,v32 dst_sel:DWORD dst_unused:UNUSED_PAD src0_sel:DWORD src1_sel:WORD_1
v_mfma_f32_16x16x32_bf16 v[160:163],a[224:227],v[48:51],v[160:163]
ds_read_u16 v61,v61
v_lshlrev_b32_sdwa v34,1,v34 dst_sel:DWORD dst_unused:UNUSED_PAD src0_sel:DWORD src1_sel:WORD_1
v_mfma_f32_16x16x32_bf16 v[240:243],a[248:251],v[48:51],v[240:243]
ds_read_u16_d16_hi v63,v63
v_lshlrev_b32_sdwa v33,1,v33 dst_sel:DWORD dst_unused:UNUSED_PAD src0_sel:DWORD src1_sel:WORD_1
v_mfma_f32_16x16x32_bf16 v[244:247],a[248:251],v[44:47],v[244:247]
ds_read_u16 v32,v32
v_lshlrev_b32_sdwa v35,1,v35 dst_sel:DWORD dst_unused:UNUSED_PAD src0_sel:DWORD src1_sel:WORD_1
v_mfma_f32_16x16x32_bf16 v[180:183],a[228:231],v[44:47],v[180:183]
s_nop 0
ds_read_u16_d16_hi v34,v34
v_mfma_f32_16x16x32_bf16 v[176:179],a[228:231],v[48:51],v[176:179]
s_waitcnt lgkmcnt(8)
ds_read_u16 v33,v33
v_mfma_f32_16x16x32_bf16 v[248:251],a[232:235],v[48:51],v[248:251]
ds_read_u16_d16_hi v35,v35
v_mfma_f32_16x16x32_bf16 v[252:255],a[232:235],v[44:47],v[252:255]
v_mfma_f32_16x16x32_bf16 v[196:199],a[236:239],v[44:47],v[196:199]
v_mfma_f32_16x16x32_bf16 v[192:195],a[236:239],v[48:51],v[192:195]
v_mfma_f32_16x16x32_bf16 v[44:47],a[220:223],a[8:11],v[16:19]
v_mul_f32_dpp v232,v14,v232 row_newbcast:0 row_mask:0xf bank_mask:0xf
s_add_u32 s10,s61,1572864
v_mul_f32_dpp v233,v14,v233 row_newbcast:1 row_mask:0xf bank_mask:0xf
v_mul_f32_dpp v234,v14,v234 row_newbcast:2 row_mask:0xf bank_mask:0xf
v_lshlrev_b32_sdwa v36,1,v36 dst_sel:DWORD dst_unused:UNUSED_PAD src0_sel:DWORD src1_sel:WORD_1
v_mfma_f32_16x16x32_bf16 v[44:47],a[216:219],a[24:27],v[44:47]
ds_read_u16 v36,v36
v_lshl_add_u64 v[28:29],v[28:29],0,v[30:31]
v_mul_f32_dpp v235,v14,v235 row_newbcast:3 row_mask:0xf bank_mask:0xf
v_mul_f32_dpp v160,v14,v160 row_newbcast:4 row_mask:0xf bank_mask:0xf
v_mul_f32_dpp v161,v14,v161 row_newbcast:5 row_mask:0xf bank_mask:0xf
v_mfma_f32_16x16x32_bf16 v[44:47],a[192:195],a[40:43],v[44:47]
s_waitcnt lgkmcnt(4)
v_lshl_add_u64 v[30:31],v[24:25],0,v[26:27]
v_mul_f32_dpp v162,v14,v162 row_newbcast:6 row_mask:0xf bank_mask:0xf
v_mul_f32_dpp v163,v14,v163 row_newbcast:7 row_mask:0xf bank_mask:0xf
v_lshlrev_b32_sdwa v38,1,v38 dst_sel:DWORD dst_unused:UNUSED_PAD src0_sel:DWORD src1_sel:WORD_1
v_cvt_pk_bf16_f32 v232,v232,v233
v_mfma_f32_16x16x32_bf16 v[48:51],a[196:199],a[40:43],v[20:23]
s_nop 0
ds_read_u16_d16_hi v38,v38
v_cvt_pk_bf16_f32 v233,v234,v235
v_cvt_pk_bf16_f32 v234,v160,v161
v_lshlrev_b32_sdwa v37,1,v37 dst_sel:DWORD dst_unused:UNUSED_PAD src0_sel:DWORD src1_sel:WORD_1
v_cvt_pk_bf16_f32 v235,v162,v163
v_mfma_f32_16x16x32_bf16 v[48:51],a[200:203],a[24:27],v[48:51]
buffer_store_dwordx4 v[232:235],v7,s[40:43],s10 offen offset:0 sc1
v_lshl_add_u64 v[24:25],v[60:61],0,v[62:63]
v_mul_f32_dpp v240,v14,v240 row_newbcast:8 row_mask:0xf bank_mask:0xf
v_mul_f32_dpp v241,v14,v241 row_newbcast:9 row_mask:0xf bank_mask:0xf
v_mul_f32_dpp v242,v14,v242 row_newbcast:10 row_mask:0xf bank_mask:0xf
v_mfma_f32_16x16x32_bf16 v[48:51],a[212:215],a[8:11],v[48:51]
ds_read_u16 v37,v37
v_mul_f32_dpp v243,v14,v243 row_newbcast:11 row_mask:0xf bank_mask:0xf
v_mul_f32_dpp v176,v14,v176 row_newbcast:12 row_mask:0xf bank_mask:0xf
v_lshlrev_b32_sdwa v39,1,v39 dst_sel:DWORD dst_unused:UNUSED_PAD src0_sel:DWORD src1_sel:WORD_1
v_mul_f32_dpp v177,v14,v177 row_newbcast:13 row_mask:0xf bank_mask:0xf
v_mfma_f32_16x16x32_bf16 v[60:63],a[212:215],a[12:15],v[20:23]
s_nop 0
ds_read_b128 v[20:23],v4 offset:640
v_mul_f32_dpp v178,v14,v178 row_newbcast:14 row_mask:0xf bank_mask:0xf
v_mul_f32_dpp v179,v14,v179 row_newbcast:15 row_mask:0xf bank_mask:0xf
v_lshlrev_b32_sdwa v40,1,v40 dst_sel:DWORD dst_unused:UNUSED_PAD src0_sel:DWORD src1_sel:WORD_1
v_cvt_pk_bf16_f32 v240,v240,v241
v_mfma_f32_16x16x32_bf16 v[60:63],a[200:203],a[28:31],v[60:63]
s_waitcnt lgkmcnt(4)
ds_read_u16_d16_hi v39,v39
ds_read_b64_tr_b16 a[212:213],v1 offset:4096
v_lshl_add_u64 v[26:27],v[32:33],0,v[34:35]
v_cvt_pk_bf16_f32 v241,v242,v243
v_cvt_pk_bf16_f32 v242,v176,v177
v_cvt_pk_bf16_f32 v243,v178,v179
v_lshlrev_b32_sdwa v42,1,v42 dst_sel:DWORD dst_unused:UNUSED_PAD src0_sel:DWORD src1_sel:WORD_1
v_mfma_f32_16x16x32_bf16 v[60:63],a[196:199],a[44:47],v[60:63]
ds_read_b64_tr_b16 a[214:215],v1 offset:5120
ds_read_u16 v40,v40
v_mul_f32_dpp v248,v15,v248 row_newbcast:0 row_mask:0xf bank_mask:0xf
v_mul_f32_dpp v249,v15,v249 row_newbcast:1 row_mask:0xf bank_mask:0xf
v_mul_f32_dpp v250,v15,v250 row_newbcast:2 row_mask:0xf bank_mask:0xf
v_mfma_f32_16x16x32_bf16 v[32:35],a[192:195],a[44:47],v[16:19]
ds_read_b128 v[16:19],v4 offset:672
ds_read_u16_d16_hi v42,v42
v_mul_f32_dpp v251,v15,v251 row_newbcast:3 row_mask:0xf bank_mask:0xf
v_mul_f32_dpp v192,v15,v192 row_newbcast:4 row_mask:0xf bank_mask:0xf
v_lshlrev_b32_sdwa v41,1,v41 dst_sel:DWORD dst_unused:UNUSED_PAD src0_sel:DWORD src1_sel:WORD_1
v_mul_f32_dpp v193,v15,v193 row_newbcast:5 row_mask:0xf bank_mask:0xf
v_mfma_f32_16x16x32_bf16 v[32:35],a[216:219],a[28:31],v[32:35]
ds_read_u16 v41,v41
ds_read_b64_tr_b16 a[200:201],v2 offset:4096
v_mul_f32_dpp v194,v15,v194 row_newbcast:6 row_mask:0xf bank_mask:0xf
v_mul_f32_dpp v195,v15,v195 row_newbcast:7 row_mask:0xf bank_mask:0xf
v_lshlrev_b32_sdwa v43,1,v43 dst_sel:DWORD dst_unused:UNUSED_PAD src0_sel:DWORD src1_sel:WORD_1
v_cvt_pk_bf16_f32 v248,v248,v249
v_mfma_f32_16x16x32_bf16 v[32:35],a[220:223],a[12:15],v[32:35]
ds_read_u16_d16_hi v43,v43
ds_read_b64_tr_b16 a[202:203],v2 offset:5120
v_cvt_pk_bf16_f32 v249,v250,v251
v_cvt_pk_bf16_f32 v250,v192,v193
v_cvt_pk_bf16_f32 v251,v194,v195
v_mfma_f32_16x16x32_bf16 v[208:211],a[240:243],v[28:31],v[208:211]
ds_read_b64_tr_b16 a[196:197],v2 offset:2048
v_mul_f32_dpp v236,v14,v236 row_newbcast:0 row_mask:0xf bank_mask:0xf
v_mul_f32_dpp v237,v14,v237 row_newbcast:1 row_mask:0xf bank_mask:0xf
v_lshlrev_b32_sdwa v56,1,v56 dst_sel:DWORD dst_unused:UNUSED_PAD src0_sel:DWORD src1_sel:WORD_1
v_mul_f32_dpp v238,v14,v238 row_newbcast:2 row_mask:0xf bank_mask:0xf
v_mfma_f32_16x16x32_bf16 v[212:215],a[240:243],v[24:27],v[212:215]
ds_read_b64_tr_b16 a[198:199],v2 offset:3072
v_mul_f32_dpp v239,v14,v239 row_newbcast:3 row_mask:0xf bank_mask:0xf
v_mul_f32_dpp v164,v14,v164 row_newbcast:4 row_mask:0xf bank_mask:0xf
v_lshlrev_b32_sdwa v58,1,v58 dst_sel:DWORD dst_unused:UNUSED_PAD src0_sel:DWORD src1_sel:WORD_1
v_mul_f32_dpp v165,v14,v165 row_newbcast:5 row_mask:0xf bank_mask:0xf
v_mfma_f32_16x16x32_bf16 v[172:175],a[224:227],v[24:27],v[172:175]
ds_read_u16 v56,v56
v_mul_f32_dpp v166,v14,v166 row_newbcast:6 row_mask:0xf bank_mask:0xf
v_mul_f32_dpp v167,v14,v167 row_newbcast:7 row_mask:0xf bank_mask:0xf
v_cvt_pk_bf16_f32 v236,v236,v237
v_lshlrev_b32_sdwa v57,1,v57 dst_sel:DWORD dst_unused:UNUSED_PAD src0_sel:DWORD src1_sel:WORD_1
v_mfma_f32_16x16x32_bf16 v[168:171],a[224:227],v[28:31],v[168:171]
buffer_store_dwordx4 v[240:243],v7,s[40:43],s10 offen offset:64 sc1
ds_read_u16_d16_hi v58,v58
v_cvt_pk_bf16_f32 v237,v238,v239
v_cvt_pk_bf16_f32 v238,v164,v165
v_cvt_pk_bf16_f32 v239,v166,v167
v_mfma_f32_16x16x32_bf16 v[216:219],a[248:251],v[28:31],v[216:219]
ds_read_u16 v57,v57
v_mul_f32_dpp v244,v14,v244 row_newbcast:8 row_mask:0xf bank_mask:0xf
v_mul_f32_dpp v245,v14,v245 row_newbcast:9 row_mask:0xf bank_mask:0xf
v_lshlrev_b32_sdwa v59,1,v59 dst_sel:DWORD dst_unused:UNUSED_PAD src0_sel:DWORD src1_sel:WORD_1
v_mul_f32_dpp v246,v14,v246 row_newbcast:10 row_mask:0xf bank_mask:0xf
v_mfma_f32_16x16x32_bf16 v[220:223],a[248:251],v[24:27],v[220:223]
ds_read_u16_d16_hi v59,v59
v_mul_f32_dpp v247,v14,v247 row_newbcast:11 row_mask:0xf bank_mask:0xf
v_mul_f32_dpp v180,v14,v180 row_newbcast:12 row_mask:0xf bank_mask:0xf
v_lshlrev_b32_sdwa v52,1,v52 dst_sel:DWORD dst_unused:UNUSED_PAD src0_sel:DWORD src1_sel:WORD_1
v_mul_f32_dpp v181,v14,v181 row_newbcast:13 row_mask:0xf bank_mask:0xf
v_mfma_f32_16x16x32_bf16 v[188:191],a[228:231],v[24:27],v[188:191]
ds_read_b64_tr_b16 a[192:193],v2 offset:0
v_mul_f32_dpp v182,v14,v182 row_newbcast:14 row_mask:0xf bank_mask:0xf
v_mul_f32_dpp v183,v14,v183 row_newbcast:15 row_mask:0xf bank_mask:0xf
v_cvt_pk_bf16_f32 v244,v244,v245
v_mfma_f32_16x16x32_bf16 v[184:187],a[228:231],v[28:31],v[184:187]
ds_read_b64_tr_b16 a[194:195],v2 offset:1024
v_cvt_pk_bf16_f32 v245,v246,v247
v_cvt_pk_bf16_f32 v246,v180,v181
v_lshlrev_b32_sdwa v54,1,v54 dst_sel:DWORD dst_unused:UNUSED_PAD src0_sel:DWORD src1_sel:WORD_1
v_cvt_pk_bf16_f32 v247,v182,v183
v_mfma_f32_16x16x32_bf16 v[224:227],a[232:235],v[28:31],v[224:227]
ds_read_u16 v52,v52
v_mul_f32_dpp v252,v15,v252 row_newbcast:0 row_mask:0xf bank_mask:0xf
v_mul_f32_dpp v253,v15,v253 row_newbcast:1 row_mask:0xf bank_mask:0xf
v_lshlrev_b32_sdwa v53,1,v53 dst_sel:DWORD dst_unused:UNUSED_PAD src0_sel:DWORD src1_sel:WORD_1
v_mul_f32_dpp v254,v15,v254 row_newbcast:2 row_mask:0xf bank_mask:0xf
v_mfma_f32_16x16x32_bf16 v[228:231],a[232:235],v[24:27],v[228:231]
ds_read_u16_d16_hi v54,v54
v_mul_f32_dpp v255,v15,v255 row_newbcast:3 row_mask:0xf bank_mask:0xf
v_mul_f32_dpp v196,v15,v196 row_newbcast:4 row_mask:0xf bank_mask:0xf
v_mul_f32_dpp v197,v15,v197 row_newbcast:5 row_mask:0xf bank_mask:0xf
v_lshlrev_b32_sdwa v55,1,v55 dst_sel:DWORD dst_unused:UNUSED_PAD src0_sel:DWORD src1_sel:WORD_1
v_mfma_f32_16x16x32_bf16 v[204:207],a[236:239],v[24:27],v[204:207]
s_nop 0
ds_read_u16 v53,v53
v_mul_f32_dpp v198,v15,v198 row_newbcast:6 row_mask:0xf bank_mask:0xf
v_mul_f32_dpp v199,v15,v199 row_newbcast:7 row_mask:0xf bank_mask:0xf
v_cvt_pk_bf16_f32 v252,v252,v253
v_mfma_f32_16x16x32_bf16 v[200:203],a[236:239],v[28:31],v[200:203]
s_waitcnt lgkmcnt(8)
ds_read_u16_d16_hi v55,v55
v_cvt_pk_bf16_f32 v253,v254,v255
v_cvt_pk_bf16_f32 v254,v196,v197
v_cvt_pk_bf16_f32 v255,v198,v199
v_mfma_f32_16x16x32_bf16 v[24:27],a[204:207],a[0:3],v[20:23]
v_mul_f32_dpp v208,v14,v208 row_newbcast:0 row_mask:0xf bank_mask:0xf
s_add_u32 s11,s61,1671168
v_accvgpr_write_b32 a64,0
v_accvgpr_write_b32 a65,0
v_lshlrev_b32_sdwa v44,1,v44 dst_sel:DWORD dst_unused:UNUSED_PAD src0_sel:DWORD src1_sel:WORD_1
v_mfma_f32_16x16x32_bf16 v[24:27],a[208:211],a[16:19],v[24:27]
ds_read_u16 v44,v44
v_lshl_add_u64 v[36:37],v[36:37],0,v[38:39]
v_accvgpr_write_b32 a66,0
v_accvgpr_write_b32 a67,0
v_mul_f32_dpp v209,v14,v209 row_newbcast:1 row_mask:0xf bank_mask:0xf
v_mfma_f32_16x16x32_bf16 v[24:27],a[212:215],a[32:35],v[24:27]
v_lshl_add_u64 v[38:39],v[40:41],0,v[42:43]
v_mul_f32_dpp v210,v14,v210 row_newbcast:2 row_mask:0xf bank_mask:0xf
v_mul_f32_dpp v211,v14,v211 row_newbcast:3 row_mask:0xf bank_mask:0xf
v_lshlrev_b32_sdwa v46,1,v46 dst_sel:DWORD dst_unused:UNUSED_PAD src0_sel:DWORD src1_sel:WORD_1
v_mul_f32_dpp v168,v14,v168 row_newbcast:4 row_mask:0xf bank_mask:0xf
v_mfma_f32_16x16x32_bf16 v[28:31],a[200:203],a[32:35],v[16:19]
s_nop 0
buffer_store_dwordx4 v[248:251],v7,s[40:43],s10 offen offset:128 sc1
v_mul_f32_dpp v169,v14,v169 row_newbcast:5 row_mask:0xf bank_mask:0xf
v_mul_f32_dpp v170,v14,v170 row_newbcast:6 row_mask:0xf bank_mask:0xf
v_lshlrev_b32_sdwa v45,1,v45 dst_sel:DWORD dst_unused:UNUSED_PAD src0_sel:DWORD src1_sel:WORD_1
v_mul_f32_dpp v171,v14,v171 row_newbcast:7 row_mask:0xf bank_mask:0xf
v_mfma_f32_16x16x32_bf16 v[28:31],a[196:199],a[16:19],v[28:31]
s_waitcnt lgkmcnt(5)
ds_read_u16_d16_hi v46,v46
v_lshl_add_u64 v[40:41],v[56:57],0,v[58:59]
v_cvt_pk_bf16_f32 v208,v208,v209
v_cvt_pk_bf16_f32 v209,v210,v211
v_cvt_pk_bf16_f32 v210,v168,v169
v_mfma_f32_16x16x32_bf16 v[28:31],a[192:195],a[0:3],v[28:31]
ds_read_u16 v45,v45
v_cvt_pk_bf16_f32 v211,v170,v171
v_mul_f32_dpp v216,v14,v216 row_newbcast:8 row_mask:0xf bank_mask:0xf
v_lshlrev_b32_sdwa v47,1,v47 dst_sel:DWORD dst_unused:UNUSED_PAD src0_sel:DWORD src1_sel:WORD_1
v_accvgpr_write_b32 a72,v208
v_mfma_f32_16x16x32_bf16 v[56:59],a[192:195],a[4:7],v[16:19]
s_nop 0
ds_read_u16_d16_hi v47,v47
v_mul_f32_dpp v217,v14,v217 row_newbcast:9 row_mask:0xf bank_mask:0xf
v_mul_f32_dpp v218,v14,v218 row_newbcast:10 row_mask:0xf bank_mask:0xf
v_lshlrev_b32_sdwa v48,1,v48 dst_sel:DWORD dst_unused:UNUSED_PAD src0_sel:DWORD src1_sel:WORD_1
v_accvgpr_write_b32 a73,v209
v_mfma_f32_16x16x32_bf16 v[56:59],a[196:199],a[20:23],v[56:59]
s_waitcnt lgkmcnt(4)
ds_read_u16 v48,v48
v_lshl_add_u64 v[42:43],v[52:53],0,v[54:55]
v_mul_f32_dpp v219,v14,v219 row_newbcast:11 row_mask:0xf bank_mask:0xf
v_mul_f32_dpp v184,v14,v184 row_newbcast:12 row_mask:0xf bank_mask:0xf
v_accvgpr_write_b32 a74,v210
v_lshlrev_b32_sdwa v50,1,v50 dst_sel:DWORD dst_unused:UNUSED_PAD src0_sel:DWORD src1_sel:WORD_1
v_mfma_f32_16x16x32_bf16 v[56:59],a[200:203],a[36:39],v[56:59]
s_waitcnt vmcnt(9)
s_barrier
ds_read_u16_d16_hi v50,v50
v_mul_f32_dpp v185,v14,v185 row_newbcast:13 row_mask:0xf bank_mask:0xf
v_mul_f32_dpp v186,v14,v186 row_newbcast:14 row_mask:0xf bank_mask:0xf
v_accvgpr_write_b32 a75,v211
v_mfma_f32_16x16x32_bf16 v[52:55],a[212:215],a[36:39],v[20:23]
ds_read_b64_tr_b16 a[244:245],v3 offset:6160
v_mul_f32_dpp v187,v14,v187 row_newbcast:15 row_mask:0xf bank_mask:0xf
v_cvt_pk_bf16_f32 v216,v216,v217
v_lshlrev_b32_sdwa v49,1,v49 dst_sel:DWORD dst_unused:UNUSED_PAD src0_sel:DWORD src1_sel:WORD_1
v_cvt_pk_bf16_f32 v217,v218,v219
v_mfma_f32_16x16x32_bf16 v[52:55],a[208:211],a[20:23],v[52:55]
s_add_u32 m0,s58,0
ds_read_u16 v49,v49
v_cvt_pk_bf16_f32 v218,v184,v185
v_cvt_pk_bf16_f32 v219,v186,v187
v_lshlrev_b32_sdwa v51,1,v51 dst_sel:DWORD dst_unused:UNUSED_PAD src0_sel:DWORD src1_sel:WORD_1
v_mul_f32_dpp v224,v15,v224 row_newbcast:0 row_mask:0xf bank_mask:0xf
v_mfma_f32_16x16x32_bf16 v[52:55],a[204:207],a[4:7],v[52:55]
s_add_u32 s69,s69,s59
ds_read_u16_d16_hi v51,v51
v_accvgpr_write_b32 a76,v216
v_mul_f32_dpp v225,v15,v225 row_newbcast:1 row_mask:0xf bank_mask:0xf
v_mul_f32_dpp v226,v15,v226 row_newbcast:2 row_mask:0xf bank_mask:0xf
v_mfma_f32_16x16x32_bf16 v[136:139],a[240:243],v[36:39],v[136:139]
s_nop 0
buffer_load_dwordx4 v5,s[44:47],s69 offen offset:0 lds
v_accvgpr_write_b32 a77,v217
v_mul_f32_dpp v227,v15,v227 row_newbcast:3 row_mask:0xf bank_mask:0xf
v_lshlrev_b32_sdwa v32,1,v32 dst_sel:DWORD dst_unused:UNUSED_PAD src0_sel:DWORD src1_sel:WORD_1
v_mul_f32_dpp v200,v15,v200 row_newbcast:4 row_mask:0xf bank_mask:0xf
v_mfma_f32_16x16x32_bf16 v[140:143],a[240:243],v[40:43],v[140:143]
s_add_u32 s69,s69,s53
ds_read_u16 v32,v32
v_accvgpr_write_b32 a78,v218
v_mul_f32_dpp v201,v15,v201 row_newbcast:5 row_mask:0xf bank_mask:0xf
v_lshlrev_b32_sdwa v34,1,v34 dst_sel:DWORD dst_unused:UNUSED_PAD src0_sel:DWORD src1_sel:WORD_1
v_mul_f32_dpp v202,v15,v202 row_newbcast:6 row_mask:0xf bank_mask:0xf
v_mfma_f32_16x16x32_bf16 v[68:71],a[224:227],v[40:43],v[68:71]
buffer_load_dwordx4 v5,s[44:47],s69 offen offset:1024 lds
ds_read_u16_d16_hi v34,v34
v_accvgpr_write_b32 a79,v219
v_mul_f32_dpp v203,v15,v203 row_newbcast:7 row_mask:0xf bank_mask:0xf
v_cvt_pk_bf16_f32 v224,v224,v225
v_lshlrev_b32_sdwa v33,1,v33 dst_sel:DWORD dst_unused:UNUSED_PAD src0_sel:DWORD src1_sel:WORD_1
v_mfma_f32_16x16x32_bf16 v[64:67],a[224:227],v[36:39],v[64:67]
s_nop 0
buffer_store_dwordx4 v[236:239],v7,s[40:43],s10 offen offset:192 sc1
ds_read_u16 v33,v33
v_cvt_pk_bf16_f32 v225,v226,v227
v_cvt_pk_bf16_f32 v226,v200,v201
v_cvt_pk_bf16_f32 v227,v202,v203
v_mfma_f32_16x16x32_bf16 v[144:147],a[248:251],v[36:39],v[144:147]
s_add_u32 s69,s69,s53
ds_read_b64_tr_b16 a[246:247],v3 offset:7696
v_mul_f32_dpp v212,v14,v212 row_newbcast:0 row_mask:0xf bank_mask:0xf
v_accvgpr_write_b32 a80,v224
v_lshlrev_b32_sdwa v35,1,v35 dst_sel:DWORD dst_unused:UNUSED_PAD src0_sel:DWORD src1_sel:WORD_1
v_mul_f32_dpp v213,v14,v213 row_newbcast:1 row_mask:0xf bank_mask:0xf
v_mfma_f32_16x16x32_bf16 v[148:151],a[248:251],v[40:43],v[148:151]
buffer_load_dwordx4 v5,s[44:47],s69 offen offset:2048 lds
v_mul_f32_dpp v214,v14,v214 row_newbcast:2 row_mask:0xf bank_mask:0xf
v_accvgpr_write_b32 a81,v225
v_lshlrev_b32_sdwa v60,1,v60 dst_sel:DWORD dst_unused:UNUSED_PAD src0_sel:DWORD src1_sel:WORD_1
v_mul_f32_dpp v215,v14,v215 row_newbcast:3 row_mask:0xf bank_mask:0xf
v_mfma_f32_16x16x32_bf16 v[84:87],a[228:231],v[40:43],v[84:87]
ds_read_u16_d16_hi v35,v35
v_mul_f32_dpp v172,v14,v172 row_newbcast:4 row_mask:0xf bank_mask:0xf
v_accvgpr_write_b32 a82,v226
v_mul_f32_dpp v173,v14,v173 row_newbcast:5 row_mask:0xf bank_mask:0xf
v_mfma_f32_16x16x32_bf16 v[80:83],a[228:231],v[36:39],v[80:83]
ds_read_u16 v60,v60
v_mul_f32_dpp v174,v14,v174 row_newbcast:6 row_mask:0xf bank_mask:0xf
v_accvgpr_write_b32 a83,v227
v_lshlrev_b32_sdwa v62,1,v62 dst_sel:DWORD dst_unused:UNUSED_PAD src0_sel:DWORD src1_sel:WORD_1
v_mul_f32_dpp v175,v14,v175 row_newbcast:7 row_mask:0xf bank_mask:0xf
v_mfma_f32_16x16x32_bf16 v[152:155],a[232:235],v[36:39],v[152:155]
ds_read_u16_d16_hi v62,v62
v_cvt_pk_bf16_f32 v212,v212,v213
v_cvt_pk_bf16_f32 v213,v214,v215
v_lshlrev_b32_sdwa v61,1,v61 dst_sel:DWORD dst_unused:UNUSED_PAD src0_sel:DWORD src1_sel:WORD_1
v_cvt_pk_bf16_f32 v214,v172,v173
v_mfma_f32_16x16x32_bf16 v[156:159],a[232:235],v[40:43],v[156:159]
ds_read_u16 v61,v61
v_cvt_pk_bf16_f32 v215,v174,v175
v_mfma_f32_32x32x16_bf16 v[160:175],a[64:67],a[64:67],0
v_mul_f32_dpp v220,v14,v220 row_newbcast:8 row_mask:0xf bank_mask:0xf
v_lshlrev_b32_sdwa v63,1,v63 dst_sel:DWORD dst_unused:UNUSED_PAD src0_sel:DWORD src1_sel:WORD_1
v_accvgpr_write_b32 a84,v212
v_mul_f32_dpp v221,v14,v221 row_newbcast:9 row_mask:0xf bank_mask:0xf
v_mul_f32_dpp v222,v14,v222 row_newbcast:10 row_mask:0xf bank_mask:0xf
v_mfma_f32_16x16x32_bf16 v[100:103],a[236:239],v[40:43],v[100:103]
ds_read_u16_d16_hi v63,v63
v_accvgpr_write_b32 a85,v213
v_mfma_f32_16x16x32_bf16 v[96:99],a[236:239],v[36:39],v[96:99]
ds_read_b64_tr_b16 a[252:253],v3 offset:6168
v_mul_f32_dpp v223,v14,v223 row_newbcast:11 row_mask:0xf bank_mask:0xf
v_mul_f32_dpp v188,v14,v188 row_newbcast:12 row_mask:0xf bank_mask:0xf
ds_read_b64_tr_b16 a[254:255],v3 offset:7704
v_add_u32_sdwa v36,s60,v10 dst_sel:DWORD dst_unused:UNUSED_PAD src0_sel:DWORD src1_sel:WORD_1
v_subrev_u32 v8,88400,v36
v_min_u32_e64 v36,v36,v8
v_add_u32 v37,7072,v36
v_subrev_u32 v8,88400,v37
v_min_u32_e64 v37,v37,v8
s_waitcnt lgkmcnt(15)
v_lshl_add_u64 v[44:45],v[44:45],0,v[46:47]
s_waitcnt lgkmcnt(11)
v_lshl_add_u64 v[46:47],v[48:49],0,v[50:51]
s_waitcnt lgkmcnt(6)
v_lshl_add_u64 v[48:49],v[32:33],0,v[34:35]
s_waitcnt lgkmcnt(2)
v_lshl_add_u64 v[50:51],v[60:61],0,v[62:63]
v_mfma_f32_16x16x32_bf16 v[112:115],a[240:243],v[44:47],v[112:115]
v_xad_u32 v40,v11,0,v36
v_xad_u32 v41,v11,8,v37
v_xad_u32 v42,v11,16,v36
v_xad_u32 v43,v11,24,v37
v_mfma_f32_16x16x32_bf16 v[116:119],a[240:243],v[48:51],v[116:119]
ds_read_b64 a[48:49],v40 offset:0
v_accvgpr_write_b32 a86,v214
v_mul_f32_dpp v189,v14,v189 row_newbcast:13 row_mask:0xf bank_mask:0xf
v_mul_f32_dpp v190,v14,v190 row_newbcast:14 row_mask:0xf bank_mask:0xf
v_accvgpr_write_b32 a87,v215
v_mfma_f32_16x16x32_bf16 v[76:79],a[224:227],v[48:51],v[76:79]
buffer_store_dwordx4 v[244:247],v7,s[40:43],s10 offen offset:256 sc1
ds_read_b64 a[50:51],v40 offset:192
v_mul_f32_dpp v191,v14,v191 row_newbcast:15 row_mask:0xf bank_mask:0xf
v_cvt_pk_bf16_f32 v220,v220,v221
v_cvt_pk_bf16_f32 v221,v222,v223
v_mfma_f32_16x16x32_bf16 v[72:75],a[224:227],v[44:47],v[72:75]
ds_read_b64 a[52:53],v41 offset:0
v_cvt_pk_bf16_f32 v222,v188,v189
v_cvt_pk_bf16_f32 v223,v190,v191
v_mfma_f32_32x32x16_bf16 v[176:191],a[64:67],a[64:67],0
v_mfma_f32_16x16x32_bf16 v[120:123],a[248:251],v[44:47],v[120:123]
ds_read_b64 a[54:55],v41 offset:192
v_mul_f32_dpp v228,v15,v228 row_newbcast:0 row_mask:0xf bank_mask:0xf
v_accvgpr_write_b32 a88,v220
v_mul_f32_dpp v229,v15,v229 row_newbcast:1 row_mask:0xf bank_mask:0xf
v_mfma_f32_16x16x32_bf16 v[124:127],a[248:251],v[48:51],v[124:127]
ds_read_b64 a[56:57],v40 offset:8
v_mul_f32_dpp v230,v15,v230 row_newbcast:2 row_mask:0xf bank_mask:0xf
v_accvgpr_write_b32 a89,v221
v_mul_f32_dpp v231,v15,v231 row_newbcast:3 row_mask:0xf bank_mask:0xf
v_mfma_f32_16x16x32_bf16 v[92:95],a[228:231],v[48:51],v[92:95]
ds_read_b64 a[58:59],v40 offset:200
v_mul_f32_dpp v204,v15,v204 row_newbcast:4 row_mask:0xf bank_mask:0xf
v_accvgpr_write_b32 a90,v222
v_mul_f32_dpp v205,v15,v205 row_newbcast:5 row_mask:0xf bank_mask:0xf
v_mfma_f32_16x16x32_bf16 v[88:91],a[228:231],v[44:47],v[88:91]
ds_read_b64 a[60:61],v41 offset:8
v_mul_f32_dpp v206,v15,v206 row_newbcast:6 row_mask:0xf bank_mask:0xf
v_accvgpr_write_b32 a91,v223
v_mul_f32_dpp v207,v15,v207 row_newbcast:7 row_mask:0xf bank_mask:0xf
v_mfma_f32_16x16x32_bf16 v[128:131],a[232:235],v[44:47],v[128:131]
ds_read_b64 a[62:63],v41 offset:200
v_cvt_pk_bf16_f32 v228,v228,v229
v_cvt_pk_bf16_f32 v229,v230,v231
v_cvt_pk_bf16_f32 v230,v204,v205
v_mfma_f32_16x16x32_bf16 v[132:135],a[232:235],v[48:51],v[132:135]
v_cvt_pk_bf16_f32 v231,v206,v207
v_mfma_f32_32x32x16_bf16 v[192:207],a[64:67],a[64:67],0
v_accvgpr_write_b32 a92,v228
v_mfma_f32_16x16x32_bf16 v[108:111],a[236:239],v[48:51],v[108:111]
v_accvgpr_write_b32 a93,v229
v_accvgpr_write_b32 a94,v230
v_accvgpr_write_b32 a95,v231
v_mfma_f32_16x16x32_bf16 v[104:107],a[236:239],v[44:47],v[104:107]
v_mfma_f32_32x32x16_bf16 v[208:223],a[64:67],a[64:67],0
v_mov_b64 v[32:33],0
v_mov_b64 v[34:35],0
ds_read_b64 a[64:65],v42 offset:0
ds_read_b64 a[66:67],v42 offset:192
ds_read_b64 a[68:69],v43 offset:0
ds_read_b64 a[70:71],v43 offset:192
s_waitcnt lgkmcnt(8)
v_smfmac_f32_16x16x64_bf16 v[160:163],a[96:99],a[48:55],v10
s_waitcnt lgkmcnt(4)
ds_read_b64 a[48:49],v42 offset:8
ds_read_b64 a[50:51],v42 offset:200
ds_read_b64 a[52:53],v43 offset:8
ds_read_b64 a[54:55],v43 offset:200
v_smfmac_f32_16x16x64_bf16 v[164:167],a[100:103],a[56:63],v10
s_waitcnt lgkmcnt(4)
ds_read_b64 a[56:57],v40 offset:64
ds_read_b64 a[58:59],v40 offset:256
ds_read_b64 a[60:61],v41 offset:64
ds_read_b64 a[62:63],v41 offset:256
v_smfmac_f32_16x16x64_bf16 v[168:171],a[104:107],a[64:71],v10
s_waitcnt lgkmcnt(4)
buffer_store_dwordx4 v[252:255],v7,s[40:43],s10 offen offset:320 sc1
ds_read_b64 a[64:65],v40 offset:72
ds_read_b64 a[66:67],v40 offset:264
ds_read_b64 a[68:69],v41 offset:72
ds_read_b64 a[70:71],v41 offset:264
v_and_b32 v44,8,v11
v_pk_mul_f32 v[48:49],v[160:161],v[160:161]
v_pk_mul_f32 v[50:51],v[162:163],v[162:163]
v_smfmac_f32_16x16x64_bf16 v[172:175],a[108:111],a[48:55],v10
s_waitcnt lgkmcnt(4)
ds_read_b64 a[48:49],v42 offset:64
ds_read_b64 a[50:51],v42 offset:256
ds_read_b64 a[52:53],v43 offset:64
ds_read_b64 a[54:55],v43 offset:256
v_mul_u32_u24 v44,884,v44
s_add_u32 s64,s60,3536
v_pk_add_f32 v[60:61],v[160:161],v[164:165]
v_pk_fma_f32 v[48:49],v[164:165],v[164:165],v[48:49]
v_pk_add_f32 v[62:63],v[162:163],v[166:167]
v_pk_fma_f32 v[50:51],v[166:167],v[166:167],v[50:51]
v_smfmac_f32_16x16x64_bf16 v[176:179],a[128:131],a[56:63],v10
s_waitcnt lgkmcnt(4)
ds_read_b64 a[56:57],v42 offset:72
ds_read_b64 a[58:59],v42 offset:264
ds_read_b64 a[60:61],v43 offset:72
ds_read_b64 a[62:63],v43 offset:264
v_add_u32 v47,s64,v44
v_pk_add_f32 v[60:61],v[60:61],v[168:169]
v_pk_fma_f32 v[48:49],v[168:169],v[168:169],v[48:49]
v_pk_add_f32 v[62:63],v[62:63],v[170:171]
v_pk_fma_f32 v[50:51],v[170:171],v[170:171],v[50:51]
v_smfmac_f32_16x16x64_bf16 v[180:183],a[132:135],a[64:71],v10
s_waitcnt lgkmcnt(4)
ds_read_b64 a[64:65],v40 offset:128
ds_read_b64 a[66:67],v40 offset:320
ds_read_b64 a[68:69],v41 offset:128
ds_read_b64 a[70:71],v41 offset:320
v_subrev_u32 v8,88400,v47
v_pk_add_f32 v[60:61],v[60:61],v[172:173]
v_pk_fma_f32 v[48:49],v[172:173],v[172:173],v[48:49]
v_pk_add_f32 v[62:63],v[62:63],v[174:175]
v_pk_fma_f32 v[50:51],v[174:175],v[174:175],v[50:51]
v_smfmac_f32_16x16x64_bf16 v[184:187],a[136:139],a[48:55],v10
s_waitcnt lgkmcnt(4)
ds_read_b64 a[48:49],v40 offset:136
ds_read_b64 a[50:51],v40 offset:328
ds_read_b64 a[52:53],v41 offset:136
ds_read_b64 a[54:55],v41 offset:328
v_min_u32 v47,v47,v8
v_pk_add_f32 v[60:61],v[60:61],v[176:177]
v_pk_fma_f32 v[48:49],v[176:177],v[176:177],v[48:49]
v_pk_add_f32 v[62:63],v[62:63],v[178:179]
v_pk_fma_f32 v[50:51],v[178:179],v[178:179],v[50:51]
v_smfmac_f32_16x16x64_bf16 v[188:191],a[140:143],a[56:63],v10
s_waitcnt lgkmcnt(4)
ds_read_b64 a[56:57],v42 offset:128
ds_read_b64 a[58:59],v42 offset:320
ds_read_b64 a[60:61],v43 offset:128
ds_read_b64 a[62:63],v43 offset:320
v_xad_u32 v40,v11,32,v36
v_xad_u32 v41,v11,40,v37
v_pk_add_f32 v[60:61],v[60:61],v[180:181]
v_pk_fma_f32 v[48:49],v[180:181],v[180:181],v[48:49]
v_pk_add_f32 v[62:63],v[62:63],v[182:183]
v_pk_fma_f32 v[50:51],v[182:183],v[182:183],v[50:51]
v_smfmac_f32_16x16x64_bf16 v[192:195],a[160:163],a[64:71],v10
s_waitcnt lgkmcnt(4)
ds_read_b64 a[64:65],v42 offset:136
ds_read_b64 a[66:67],v42 offset:328
ds_read_b64 a[68:69],v43 offset:136
ds_read_b64 a[70:71],v43 offset:328
v_add_u32 v46,3536,v47
v_pk_add_f32 v[60:61],v[60:61],v[184:185]
v_pk_fma_f32 v[48:49],v[184:185],v[184:185],v[48:49]
v_pk_add_f32 v[62:63],v[62:63],v[186:187]
v_pk_fma_f32 v[50:51],v[186:187],v[186:187],v[50:51]
v_smfmac_f32_16x16x64_bf16 v[196:199],a[164:167],a[48:55],v10
s_waitcnt lgkmcnt(4)
buffer_store_dwordx4 a[72:75],v7,s[40:43],s11 offen offset:0 sc1
ds_read_b64 a[48:49],v40 offset:0
ds_read_b64 a[50:51],v40 offset:192
ds_read_b64 a[52:53],v41 offset:0
ds_read_b64 a[54:55],v41 offset:192
v_xad_u32 v42,v11,48,v36
v_xad_u32 v43,v11,56,v37
v_pk_add_f32 v[60:61],v[60:61],v[188:189]
v_pk_fma_f32 v[48:49],v[188:189],v[188:189],v[48:49]
v_pk_add_f32 v[62:63],v[62:63],v[190:191]
v_pk_fma_f32 v[50:51],v[190:191],v[190:191],v[50:51]
v_smfmac_f32_16x16x64_bf16 v[200:203],a[168:171],a[56:63],v10
s_waitcnt lgkmcnt(4)
ds_read_b64 a[56:57],v40 offset:8
ds_read_b64 a[58:59],v40 offset:200
ds_read_b64 a[60:61],v41 offset:8
ds_read_b64 a[62:63],v41 offset:200
v_subrev_u32 v8,88400,v46
v_pk_add_f32 v[60:61],v[60:61],v[192:193]
v_pk_fma_f32 v[48:49],v[192:193],v[192:193],v[48:49]
v_pk_add_f32 v[62:63],v[62:63],v[194:195]
v_pk_fma_f32 v[50:51],v[194:195],v[194:195],v[50:51]
v_smfmac_f32_16x16x64_bf16 v[204:207],a[172:175],a[64:71],v10
s_waitcnt lgkmcnt(4)
ds_read_b64 a[64:65],v40 offset:64
ds_read_b64 a[66:67],v40 offset:256
ds_read_b64 a[68:69],v41 offset:64
ds_read_b64 a[70:71],v41 offset:256
v_min_u32 v46,v46,v8
v_pk_add_f32 v[60:61],v[60:61],v[196:197]
v_pk_fma_f32 v[48:49],v[196:197],v[196:197],v[48:49]
v_pk_add_f32 v[62:63],v[62:63],v[198:199]
v_pk_fma_f32 v[50:51],v[198:199],v[198:199],v[50:51]
v_smfmac_f32_16x16x64_bf16 v[208:211],a[112:115],a[48:55],v10
s_waitcnt lgkmcnt(4)
TE10_%=:
s_cbranch_execz TP9685_%=
s_nop 0
ds_read_b64 a[48:49],v40 offset:72
ds_read_b64 a[50:51],v40 offset:264
ds_read_b64 a[52:53],v41 offset:72
ds_read_b64 a[54:55],v41 offset:264
v_xor_b32 v9,0x80008,v12
v_pk_add_f32 v[60:61],v[60:61],v[200:201]
v_pk_fma_f32 v[48:49],v[200:201],v[200:201],v[48:49]
v_pk_add_f32 v[62:63],v[62:63],v[202:203]
v_pk_fma_f32 v[50:51],v[202:203],v[202:203],v[50:51]
v_mfma_f32_32x32x16_bf16 v[224:239],v[32:35],v[32:35],0
v_smfmac_f32_16x16x64_bf16 v[212:215],a[116:119],a[56:63],v10
s_waitcnt lgkmcnt(4)
ds_read_b64 a[56:57],v40 offset:128
ds_read_b64 a[58:59],v40 offset:320
ds_read_b64 a[60:61],v41 offset:128
ds_read_b64 a[62:63],v41 offset:320
v_add_u32_sdwa v44,v12,v47 dst_sel:DWORD dst_unused:UNUSED_PAD src0_sel:WORD_0 src1_sel:DWORD
v_pk_add_f32 v[60:61],v[60:61],v[204:205]
v_pk_fma_f32 v[48:49],v[204:205],v[204:205],v[48:49]
v_pk_add_f32 v[62:63],v[62:63],v[206:207]
v_pk_fma_f32 v[50:51],v[206:207],v[206:207],v[50:51]
v_smfmac_f32_16x16x64_bf16 v[216:219],a[144:147],a[64:71],v10
s_waitcnt lgkmcnt(4)
ds_read_b64 a[64:65],v40 offset:136
ds_read_b64 a[66:67],v40 offset:328
ds_read_b64 a[68:69],v41 offset:136
ds_read_b64 a[70:71],v41 offset:328
v_add_u32_sdwa v45,v12,v47 dst_sel:DWORD dst_unused:UNUSED_PAD src0_sel:WORD_1 src1_sel:DWORD
v_pk_add_f32 v[60:61],v[60:61],v[208:209]
v_pk_fma_f32 v[48:49],v[208:209],v[208:209],v[48:49]
v_pk_add_f32 v[62:63],v[62:63],v[210:211]
v_pk_fma_f32 v[50:51],v[210:211],v[210:211],v[50:51]
v_smfmac_f32_16x16x64_bf16 v[220:223],a[148:151],a[48:55],v10
s_waitcnt lgkmcnt(4)
buffer_store_dwordx4 a[76:79],v7,s[40:43],s11 offen offset:64 sc1
ds_read_b64 a[48:49],v42 offset:0
ds_read_b64 a[50:51],v42 offset:192
ds_read_b64 a[52:53],v43 offset:0
ds_read_b64 a[54:55],v43 offset:192
v_add_u32_sdwa v47,v9,v46 dst_sel:DWORD dst_unused:UNUSED_PAD src0_sel:WORD_1 src1_sel:DWORD
v_pk_add_f32 v[60:61],v[60:61],v[212:213]
v_pk_fma_f32 v[48:49],v[212:213],v[212:213],v[48:49]
v_pk_add_f32 v[62:63],v[62:63],v[214:215]
v_pk_fma_f32 v[50:51],v[214:215],v[214:215],v[50:51]
v_smfmac_f32_16x16x64_bf16 v[224:227],a[176:179],a[56:63],v10
s_waitcnt lgkmcnt(4)
ds_read_b64 a[56:57],v42 offset:8
ds_read_b64 a[58:59],v42 offset:200
ds_read_b64 a[60:61],v43 offset:8
ds_read_b64 a[62:63],v43 offset:200
v_add_u32_sdwa v46,v9,v46 dst_sel:DWORD dst_unused:UNUSED_PAD src0_sel:WORD_0 src1_sel:DWORD
v_pk_add_f32 v[60:61],v[60:61],v[216:217]
v_pk_fma_f32 v[48:49],v[216:217],v[216:217],v[48:49]
v_pk_add_f32 v[62:63],v[62:63],v[218:219]
v_pk_fma_f32 v[50:51],v[218:219],v[218:219],v[50:51]
v_mfma_f32_32x32x16_bf16 v[240:255],v[32:35],v[32:35],0
v_smfmac_f32_16x16x64_bf16 v[228:231],a[180:183],a[64:71],v10
s_waitcnt lgkmcnt(4)
ds_read_b64 a[64:65],v42 offset:64
ds_read_b64 a[66:67],v42 offset:256
ds_read_b64 a[68:69],v43 offset:64
ds_read_b64 a[70:71],v43 offset:256
v_pk_add_f32 v[60:61],v[60:61],v[220:221]
v_pk_fma_f32 v[48:49],v[220:221],v[220:221],v[48:49]
v_pk_add_f32 v[62:63],v[62:63],v[222:223]
v_pk_fma_f32 v[50:51],v[222:223],v[222:223],v[50:51]
v_smfmac_f32_16x16x64_bf16 v[232:235],a[120:123],a[48:55],v10
s_waitcnt lgkmcnt(4)
ds_read_b64 a[48:49],v42 offset:72
ds_read_b64 a[50:51],v42 offset:264
ds_read_b64 a[52:53],v43 offset:72
ds_read_b64 a[54:55],v43 offset:264
v_pk_add_f32 v[60:61],v[60:61],v[224:225]
v_pk_fma_f32 v[48:49],v[224:225],v[224:225],v[48:49]
v_pk_add_f32 v[62:63],v[62:63],v[226:227]
v_pk_fma_f32 v[50:51],v[226:227],v[226:227],v[50:51]
v_smfmac_f32_16x16x64_bf16 v[236:239],a[124:127],a[56:63],v10
s_waitcnt lgkmcnt(4)
ds_read_b64 a[56:57],v42 offset:128
ds_read_b64 a[58:59],v42 offset:320
ds_read_b64 a[60:61],v43 offset:128
ds_read_b64 a[62:63],v43 offset:320
v_pk_add_f32 v[60:61],v[60:61],v[228:229]
v_pk_fma_f32 v[48:49],v[228:229],v[228:229],v[48:49]
v_pk_add_f32 v[62:63],v[62:63],v[230:231]
v_pk_fma_f32 v[50:51],v[230:231],v[230:231],v[50:51]
v_smfmac_f32_16x16x64_bf16 v[240:243],a[152:155],a[64:71],v10
s_waitcnt lgkmcnt(4)
ds_read_b64 a[64:65],v42 offset:136
ds_read_b64 a[66:67],v42 offset:328
ds_read_b64 a[68:69],v43 offset:136
ds_read_b64 a[70:71],v43 offset:328
v_pk_add_f32 v[60:61],v[60:61],v[232:233]
v_pk_fma_f32 v[48:49],v[232:233],v[232:233],v[48:49]
v_pk_add_f32 v[62:63],v[62:63],v[234:235]
v_pk_fma_f32 v[50:51],v[234:235],v[234:235],v[50:51]
v_smfmac_f32_16x16x64_bf16 v[244:247],a[156:159],a[48:55],v10
s_waitcnt lgkmcnt(4)
ds_read_b64 a[240:241],v44 offset:57344
ds_read_b64 a[242:243],v44 offset:57352
ds_read_b64 a[224:225],v45 offset:57344
ds_read_b64 a[226:227],v45 offset:57352
ds_read_b128 a[232:235],v13 offset:0
v_pk_add_f32 v[60:61],v[60:61],v[236:237]
v_pk_fma_f32 v[48:49],v[236:237],v[236:237],v[48:49]
v_pk_add_f32 v[62:63],v[62:63],v[238:239]
v_pk_fma_f32 v[50:51],v[238:239],v[238:239],v[50:51]
v_smfmac_f32_16x16x64_bf16 v[248:251],a[184:187],a[56:63],v10
s_waitcnt lgkmcnt(5)
buffer_store_dwordx4 a[80:83],v7,s[40:43],s11 offen offset:128 sc1
ds_read_b64 a[248:249],v46 offset:57344
ds_read_b64 a[250:251],v46 offset:57352
ds_read_b64 a[228:229],v47 offset:57344
ds_read_b64 a[230:231],v47 offset:57352
ds_read_b128 a[236:239],v13 offset:256
v_pk_add_f32 v[60:61],v[60:61],v[240:241]
v_pk_fma_f32 v[48:49],v[240:241],v[240:241],v[48:49]
v_pk_add_f32 v[62:63],v[62:63],v[242:243]
v_pk_fma_f32 v[50:51],v[242:243],v[242:243],v[50:51]
v_smfmac_f32_16x16x64_bf16 v[252:255],a[188:191],a[64:71],v10
ds_read_b128 v[40:43],v4 offset:3584
ds_read_b128 v[36:39],v4 offset:3616
v_pk_add_f32 v[60:61],v[60:61],v[244:245]
v_pk_fma_f32 v[48:49],v[244:245],v[244:245],v[48:49]
v_pk_add_f32 v[62:63],v[62:63],v[246:247]
v_pk_fma_f32 v[50:51],v[246:247],v[246:247],v[50:51]
v_pk_add_f32 v[60:61],v[60:61],v[248:249]
v_pk_fma_f32 v[48:49],v[248:249],v[248:249],v[48:49]
v_pk_add_f32 v[62:63],v[62:63],v[250:251]
v_pk_fma_f32 v[50:51],v[250:251],v[250:251],v[50:51]
v_pk_add_f32 v[60:61],v[60:61],v[252:253]
v_pk_fma_f32 v[48:49],v[252:253],v[252:253],v[48:49]
v_pk_add_f32 v[62:63],v[62:63],v[254:255]
v_pk_fma_f32 v[50:51],v[254:255],v[254:255],v[50:51]
s_nop 0
v_permlane32_swap_b32 v60,v62
v_permlane32_swap_b32 v61,v63
v_permlane32_swap_b32 v48,v50
v_permlane32_swap_b32_e64 v49,v51
v_pk_add_f32 v[60:61],v[60:61],v[62:63]
v_pk_add_f32 v[48:49],v[48:49],v[50:51]
s_nop 0
v_permlane16_swap_b32 v60,v61
v_permlane16_swap_b32 v48,v49
v_add_f32 v8,v60,v61
v_add_f32_e64 v9,v48,v49
v_mul_f32 v8,0x3c2aaaab,v8
v_mul_f32 v9,0x3c2aaaab,v9
v_cmp_ngt_f32 vcc,0x5d800000,v9
v_fma_f32 v9,-v8,v8,v9
v_max_f32 v9,0,v9
s_or_b64 s[90:91],s[90:91],vcc
v_add_f32 v9,0x3727c5ac,v9
v_rsq_f32 v9,v9
s_nop 0
v_mul_f32 v60,s96,v9
v_mul_f32 v61,s96,v9
v_mul_f32 v48,-v8,v60
v_mul_f32 v49,-v8,v60
v_permlane16_swap_b32 v60,v61
s_nop 0
v_permlane16_swap_b32 v48,v49
v_mov_b64 v[62:63],v[60:61]
v_mov_b64 v[50:51],v[48:49]
s_nop 0
v_permlane32_swap_b32 v60,v62
v_permlane32_swap_b32 v61,v63
v_permlane32_swap_b32 v48,v50
v_permlane32_swap_b32 v49,v51
s_or_b32 s64,s90,s91
s_lshl_b32 s65,s52,2
v_mov_b32 v8,s64
v_mov_b32 v9,s65
ds_write_b32 v9,v8 offset:11648
v_pk_fma_f32 v[160:161],v[160:161],v[60:61],v[48:49]
v_pk_fma_f32 v[162:163],v[162:163],v[62:63],v[50:51]
v_pk_fma_f32 v[164:165],v[164:165],v[60:61],v[48:49]
v_pk_fma_f32 v[166:167],v[166:167],v[62:63],v[50:51]
v_pk_fma_f32 v[168:169],v[168:169],v[60:61],v[48:49]
v_pk_fma_f32 v[170:171],v[170:171],v[62:63],v[50:51]
v_pk_fma_f32 v[172:173],v[172:173],v[60:61],v[48:49]
v_pk_fma_f32 v[174:175],v[174:175],v[62:63],v[50:51]
v_pk_fma_f32 v[208:209],v[208:209],v[60:61],v[48:49]
v_pk_fma_f32 v[210:211],v[210:211],v[62:63],v[50:51]
v_pk_fma_f32 v[212:213],v[212:213],v[60:61],v[48:49]
v_pk_fma_f32 v[214:215],v[214:215],v[62:63],v[50:51]
v_pk_fma_f32 v[232:233],v[232:233],v[60:61],v[48:49]
v_pk_fma_f32 v[234:235],v[234:235],v[62:63],v[50:51]
v_pk_fma_f32 v[236:237],v[236:237],v[60:61],v[48:49]
v_pk_fma_f32 v[238:239],v[238:239],v[62:63],v[50:51]
v_pk_fma_f32 v[176:177],v[176:177],v[60:61],v[48:49]
v_pk_fma_f32 v[178:179],v[178:179],v[62:63],v[50:51]
v_pk_fma_f32 v[180:181],v[180:181],v[60:61],v[48:49]
v_pk_fma_f32 v[182:183],v[182:183],v[62:63],v[50:51]
v_pk_fma_f32 v[184:185],v[184:185],v[60:61],v[48:49]
v_pk_fma_f32 v[186:187],v[186:187],v[62:63],v[50:51]
v_pk_fma_f32 v[188:189],v[188:189],v[60:61],v[48:49]
v_pk_fma_f32 v[190:191],v[190:191],v[62:63],v[50:51]
v_pk_fma_f32 v[216:217],v[216:217],v[60:61],v[48:49]
v_pk_fma_f32 v[218:219],v[218:219],v[62:63],v[50:51]
v_pk_fma_f32 v[220:221],v[220:221],v[60:61],v[48:49]
v_pk_fma_f32 v[222:223],v[222:223],v[62:63],v[50:51]
v_pk_fma_f32 v[240:241],v[240:241],v[60:61],v[48:49]
v_pk_fma_f32 v[242:243],v[242:243],v[62:63],v[50:51]
v_pk_fma_f32 v[244:245],v[244:245],v[60:61],v[48:49]
v_pk_fma_f32 v[246:247],v[246:247],v[62:63],v[50:51]
v_pk_fma_f32 v[192:193],v[192:193],v[60:61],v[48:49]
v_pk_fma_f32 v[194:195],v[194:195],v[62:63],v[50:51]
v_pk_fma_f32 v[196:197],v[196:197],v[60:61],v[48:49]
v_pk_fma_f32 v[198:199],v[198:199],v[62:63],v[50:51]
v_pk_fma_f32 v[200:201],v[200:201],v[60:61],v[48:49]
v_pk_fma_f32 v[202:203],v[202:203],v[62:63],v[50:51]
v_pk_fma_f32 v[204:205],v[204:205],v[60:61],v[48:49]
v_pk_fma_f32 v[206:207],v[206:207],v[62:63],v[50:51]
v_pk_fma_f32 v[224:225],v[224:225],v[60:61],v[48:49]
v_pk_fma_f32 v[226:227],v[226:227],v[62:63],v[50:51]
v_pk_fma_f32 v[228:229],v[228:229],v[60:61],v[48:49]
v_pk_fma_f32 v[230:231],v[230:231],v[62:63],v[50:51]
v_pk_fma_f32 v[248:249],v[248:249],v[60:61],v[48:49]
v_pk_fma_f32 v[250:251],v[250:251],v[62:63],v[50:51]
v_pk_fma_f32 v[252:253],v[252:253],v[60:61],v[48:49]
v_pk_fma_f32 v[254:255],v[254:255],v[62:63],v[50:51]
v_mfma_f32_16x16x32_bf16 v[32:35],a[204:207],a[8:11],v[20:23]
buffer_store_dwordx4 a[84:87],v7,s[40:43],s11 offen offset:192 sc1
v_cvt_pk_bf16_f32 v160,v160,v164
v_cvt_pk_bf16_f32 v168,v168,v172
v_cvt_pk_bf16_f32 v208,v208,v212
v_lshlrev_b32_sdwa v24,1,v24 dst_sel:DWORD dst_unused:UNUSED_PAD src0_sel:DWORD src1_sel:WORD_1
v_mfma_f32_16x16x32_bf16 v[32:35],a[208:211],a[24:27],v[32:35]
ds_read_u16 v24,v24
v_accvgpr_write_b32 a48,v160
v_accvgpr_write_b32 a49,v168
v_cvt_pk_bf16_f32 v232,v232,v236
v_accvgpr_write_b32 a50,v208
v_mfma_f32_16x16x32_bf16 v[32:35],a[212:215],a[40:43],v[32:35]
ds_read_b64_tr_b16 a[216:217],v1 offset:6160
v_cvt_pk_bf16_f32 v161,v161,v165
v_accvgpr_write_b32 a51,v232
v_cvt_pk_bf16_f32 v169,v169,v173
v_accvgpr_write_b32 a52,v161
v_mfma_f32_16x16x32_bf16 v[60:63],a[200:203],a[40:43],v[16:19]
ds_read_b64_tr_b16 a[218:219],v1 offset:7184
v_cvt_pk_bf16_f32 v209,v209,v213
v_accvgpr_write_b32 a53,v169
v_cvt_pk_bf16_f32 v233,v233,v237
v_lshlrev_b32_sdwa v26,1,v26 dst_sel:DWORD dst_unused:UNUSED_PAD src0_sel:DWORD src1_sel:WORD_1
v_accvgpr_write_b32 a54,v209
v_mfma_f32_16x16x32_bf16 v[60:63],a[196:199],a[24:27],v[60:63]
ds_read_u16_d16_hi v26,v26
v_cvt_pk_bf16_f32 v162,v162,v166
v_accvgpr_write_b32 a55,v233
v_cvt_pk_bf16_f32 v170,v170,v174
v_accvgpr_write_b32 a56,v162
v_mfma_f32_16x16x32_bf16 v[60:63],a[192:195],a[8:11],v[60:63]
buffer_store_dwordx4 a[88:91],v7,s[40:43],s11 offen offset:256 sc1
v_accvgpr_write_b32 a57,v170
v_cvt_pk_bf16_f32 v210,v210,v214
v_accvgpr_write_b32 a58,v210
v_lshlrev_b32_sdwa v25,1,v25 dst_sel:DWORD dst_unused:UNUSED_PAD src0_sel:DWORD src1_sel:WORD_1
v_cvt_pk_bf16_f32 v234,v234,v238
v_mfma_f32_16x16x32_bf16 v[48:51],a[192:195],a[12:15],v[16:19]
ds_read_u16 v25,v25
v_accvgpr_write_b32 a59,v234
v_cvt_pk_bf16_f32 v163,v163,v167
v_accvgpr_write_b32 a60,v163
v_mfma_f32_16x16x32_bf16 v[48:51],a[196:199],a[28:31],v[48:51]
ds_read_b64_tr_b16 a[220:221],v1 offset:8208
v_cvt_pk_bf16_f32 v171,v171,v175
v_accvgpr_write_b32 a61,v171
v_cvt_pk_bf16_f32 v211,v211,v215
v_accvgpr_write_b32 a62,v211
v_lshlrev_b32_sdwa v27,1,v27 dst_sel:DWORD dst_unused:UNUSED_PAD src0_sel:DWORD src1_sel:WORD_1
v_mfma_f32_16x16x32_bf16 v[48:51],a[200:203],a[44:47],v[48:51]
s_waitcnt lgkmcnt(6)
ds_read_u16_d16_hi v27,v27
v_cvt_pk_bf16_f32 v235,v235,v239
v_accvgpr_write_b32 a63,v235
v_cvt_pk_bf16_f32 v176,v176,v180
v_cvt_pk_bf16_f32 v184,v184,v188
v_mfma_f32_16x16x32_bf16 v[232:235],a[232:235],a[240:243],v[40:43]
s_nop 0
ds_read_b64_tr_b16 a[222:223],v1 offset:9232
v_cvt_pk_bf16_f32 v216,v216,v220
v_accvgpr_write_b32 a64,v176
v_accvgpr_write_b32 a65,v184
v_cvt_pk_bf16_f32 v240,v240,v244
v_lshlrev_b32_sdwa v28,1,v28 dst_sel:DWORD dst_unused:UNUSED_PAD src0_sel:DWORD src1_sel:WORD_1
v_mfma_f32_16x16x32_bf16 v[236:239],a[232:235],a[224:227],v[40:43]
ds_read_u16 v28,v28
v_accvgpr_write_b32 a66,v216
v_cvt_pk_bf16_f32 v177,v177,v181
v_accvgpr_write_b32 a67,v240
v_cvt_pk_bf16_f32 v185,v185,v189
v_mfma_f32_16x16x32_bf16 v[208:211],a[232:235],a[248:251],v[40:43]
v_accvgpr_write_b32 a68,v177
v_cvt_pk_bf16_f32 v217,v217,v221
v_accvgpr_write_b32 a69,v185
v_lshlrev_b32_sdwa v30,1,v30 dst_sel:DWORD dst_unused:UNUSED_PAD src0_sel:DWORD src1_sel:WORD_1
v_cvt_pk_bf16_f32 v241,v241,v245
v_mfma_f32_16x16x32_bf16 v[212:215],a[232:235],a[228:231],v[40:43]
ds_read_b128 a[232:235],v13 offset:512
ds_read_b128 v[40:43],v4 offset:3712
v_accvgpr_write_b32 a70,v217
v_cvt_pk_bf16_f32 v178,v178,v182
v_accvgpr_write_b32 a71,v241
v_mfma_f32_16x16x32_bf16 v[160:163],a[236:239],a[240:243],v[36:39]
ds_read_b64 a[240:241],v44 offset:57408
ds_read_b64 a[242:243],v44 offset:57416
v_cvt_pk_bf16_f32 v186,v186,v190
v_accvgpr_write_b32 a72,v178
v_accvgpr_write_b32 a73,v186
v_cvt_pk_bf16_f32 v218,v218,v222
v_lshlrev_b32_sdwa v29,1,v29 dst_sel:DWORD dst_unused:UNUSED_PAD src0_sel:DWORD src1_sel:WORD_1
v_mfma_f32_16x16x32_bf16 v[164:167],a[236:239],a[224:227],v[36:39]
ds_read_b64 a[224:225],v45 offset:57408
ds_read_b64 a[226:227],v45 offset:57416
v_accvgpr_write_b32 a74,v218
v_cvt_pk_bf16_f32 v242,v242,v246
v_accvgpr_write_b32 a75,v242
v_cvt_pk_bf16_f32 v179,v179,v183
v_mfma_f32_16x16x32_bf16 v[168:171],a[236:239],a[248:251],v[36:39]
ds_read_b64 a[248:249],v46 offset:57408
ds_read_b64 a[250:251],v46 offset:57416
v_accvgpr_write_b32 a76,v179
v_cvt_pk_bf16_f32 v187,v187,v191
v_accvgpr_write_b32 a77,v187
v_cvt_pk_bf16_f32 v219,v219,v223
v_lshlrev_b32_sdwa v31,1,v31 dst_sel:DWORD dst_unused:UNUSED_PAD src0_sel:DWORD src1_sel:WORD_1
v_mfma_f32_16x16x32_bf16 v[172:175],a[236:239],a[228:231],v[36:39]
s_waitcnt lgkmcnt(4)
buffer_store_dwordx4 a[92:95],v7,s[40:43],s11 offen offset:320 sc1
ds_read_b64 a[228:229],v47 offset:57408
v_accvgpr_write_b32 a78,v219
v_cvt_pk_bf16_f32 v243,v243,v247
v_accvgpr_write_b32 a79,v243
v_cvt_pk_bf16_f32 v192,v192,v196
v_mfma_f32_16x16x32_bf16 v[240:243],a[232:235],a[240:243],v[40:43]
s_waitcnt lgkmcnt(3)
ds_read_b64 a[230:231],v47 offset:57416
ds_read_b128 a[236:239],v13 offset:768
v_cvt_pk_bf16_f32 v200,v200,v204
v_cvt_pk_bf16_f32 v224,v224,v228
v_accvgpr_write_b32 a80,v192
v_accvgpr_write_b32 a81,v200
v_lshlrev_b32_sdwa v52,1,v52 dst_sel:DWORD dst_unused:UNUSED_PAD src0_sel:DWORD src1_sel:WORD_1
v_mfma_f32_16x16x32_bf16 v[244:247],a[232:235],a[224:227],v[40:43]
s_waitcnt lgkmcnt(3)
ds_read_b128 v[36:39],v4 offset:3744
ds_read_u16_d16_hi v30,v30
v_cvt_pk_bf16_f32 v248,v248,v252
v_accvgpr_write_b32 a82,v224
v_cvt_pk_bf16_f32 v193,v193,v197
v_mfma_f32_16x16x32_bf16 v[216:219],a[232:235],a[248:251],v[40:43]
s_waitcnt lgkmcnt(3)
ds_read_u16 v29,v29
v_accvgpr_write_b32 a83,v248
v_cvt_pk_bf16_f32 v201,v201,v205
v_accvgpr_write_b32 a84,v193
v_cvt_pk_bf16_f32 v225,v225,v229
v_lshlrev_b32_sdwa v54,1,v54 dst_sel:DWORD dst_unused:UNUSED_PAD src0_sel:DWORD src1_sel:WORD_1
v_mfma_f32_16x16x32_bf16 v[220:223],a[232:235],a[228:231],v[40:43]
s_waitcnt lgkmcnt(2)
ds_read_b128 a[232:235],v13 offset:1024
ds_read_b128 v[40:43],v4 offset:3840
v_accvgpr_write_b32 a85,v201
v_cvt_pk_bf16_f32 v249,v249,v253
v_accvgpr_write_b32 a86,v225
v_cvt_pk_bf16_f32 v194,v194,v198
v_mfma_f32_16x16x32_bf16 v[176:179],a[236:239],a[240:243],v[36:39]
s_nop 0
ds_read_b64 a[240:241],v44 offset:57472
ds_read_b64 a[242:243],v44 offset:57480
v_accvgpr_write_b32 a87,v249
v_cvt_pk_bf16_f32 v202,v202,v206
v_accvgpr_write_b32 a88,v194
v_accvgpr_write_b32 a89,v202
v_lshlrev_b32_sdwa v53,1,v53 dst_sel:DWORD dst_unused:UNUSED_PAD src0_sel:DWORD src1_sel:WORD_1
v_mfma_f32_16x16x32_bf16 v[180:183],a[236:239],a[224:227],v[36:39]
ds_read_b64 a[224:225],v45 offset:57472
ds_read_b64 a[226:227],v45 offset:57480
v_cvt_pk_bf16_f32 v226,v226,v230
v_accvgpr_write_b32 a90,v226
v_cvt_pk_bf16_f32 v250,v250,v254
v_accvgpr_write_b32 a91,v250
v_mfma_f32_16x16x32_bf16 v[184:187],a[236:239],a[248:251],v[36:39]
s_nop 0
ds_read_b64 a[248:249],v46 offset:57472
ds_read_b64 a[250:251],v46 offset:57480
v_cvt_pk_bf16_f32 v195,v195,v199
v_accvgpr_write_b32 a92,v195
v_cvt_pk_bf16_f32 v203,v203,v207
v_accvgpr_write_b32 a93,v203
v_mfma_f32_16x16x32_bf16 v[188:191],a[236:239],a[228:231],v[36:39]
s_waitcnt lgkmcnt(4)
ds_read_b64 a[228:229],v47 offset:57472
ds_read_b64 a[230:231],v47 offset:57480
v_cvt_pk_bf16_f32 v227,v227,v231
v_accvgpr_write_b32 a94,v227
v_cvt_pk_bf16_f32 v251,v251,v255
v_lshlrev_b32_sdwa v55,1,v55 dst_sel:DWORD dst_unused:UNUSED_PAD src0_sel:DWORD src1_sel:WORD_1
v_accvgpr_write_b32 a95,v251
v_mfma_f32_16x16x32_bf16 v[248:251],a[232:235],a[240:243],v[40:43]
s_waitcnt lgkmcnt(4)
ds_read_b128 a[236:239],v13 offset:1280
ds_read_b128 v[36:39],v4 offset:3872
v_mfma_f32_16x16x32_bf16 v[252:255],a[232:235],a[224:227],v[40:43]
s_waitcnt lgkmcnt(4)
ds_read_u16_d16_hi v31,v31
ds_read_u16 v52,v52
v_lshlrev_b32_sdwa v56,1,v56 dst_sel:DWORD dst_unused:UNUSED_PAD src0_sel:DWORD src1_sel:WORD_1
v_mfma_f32_16x16x32_bf16 v[224:227],a[232:235],a[248:251],v[40:43]
s_waitcnt lgkmcnt(4)
ds_read_u16_d16_hi v54,v54
v_lshlrev_b32_sdwa v58,1,v58 dst_sel:DWORD dst_unused:UNUSED_PAD src0_sel:DWORD src1_sel:WORD_1
v_mfma_f32_16x16x32_bf16 v[228:231],a[232:235],a[228:231],v[40:43]
s_waitcnt lgkmcnt(3)
ds_read_b64_tr_b16 a[232:233],v3 offset:6224
ds_read_b64_tr_b16 a[234:235],v3 offset:7760
v_mfma_f32_16x16x32_bf16 v[192:195],a[236:239],a[240:243],v[36:39]
ds_read_b64_tr_b16 a[240:241],v3 offset:6232
ds_read_b64_tr_b16 a[242:243],v3 offset:7768
v_mfma_f32_16x16x32_bf16 v[196:199],a[236:239],a[224:227],v[36:39]
ds_read_b64_tr_b16 a[224:225],v3 offset:6288
ds_read_b64_tr_b16 a[226:227],v3 offset:7824
v_lshlrev_b32_sdwa v57,1,v57 dst_sel:DWORD dst_unused:UNUSED_PAD src0_sel:DWORD src1_sel:WORD_1
v_mfma_f32_16x16x32_bf16 v[200:203],a[236:239],a[248:251],v[36:39]
ds_read_b64_tr_b16 a[248:249],v3 offset:6296
ds_read_b64_tr_b16 a[250:251],v3 offset:7832
v_mfma_f32_16x16x32_bf16 v[204:207],a[236:239],a[228:231],v[36:39]
ds_read_u16 v53,v53
ds_read_u16_d16_hi v55,v55
v_lshlrev_b32_sdwa v59,1,v59 dst_sel:DWORD dst_unused:UNUSED_PAD src0_sel:DWORD src1_sel:WORD_1
v_mfma_f32_16x16x32_bf16 v[44:47],a[212:215],a[44:47],v[20:23]
ds_read_u16 v56,v56
v_mfma_f32_16x16x32_bf16 v[44:47],a[208:211],a[28:31],v[44:47]
s_waitcnt vmcnt(18)
ds_read_u16_d16_hi v58,v58
v_mfma_f32_16x16x32_bf16 v[44:47],a[204:207],a[12:15],v[44:47]
s_barrier
ds_read_u16 v57,v57
ds_read_u16_d16_hi v59,v59
v_mfma_f32_16x16x32_bf16 v[40:43],a[204:207],a[48:51],v[20:23]
v_lshlrev_b32_sdwa v32,1,v32 dst_sel:DWORD dst_unused:UNUSED_PAD src0_sel:DWORD src1_sel:WORD_1
v_lshlrev_b32_sdwa v34,1,v34 dst_sel:DWORD dst_unused:UNUSED_PAD src0_sel:DWORD src1_sel:WORD_1
v_mfma_f32_16x16x32_bf16 v[40:43],a[208:211],a[64:67],v[40:43]
ds_read_u16 v32,v32
v_lshl_add_u64 v[24:25],v[24:25],0,v[26:27]
v_mfma_f32_16x16x32_bf16 v[40:43],a[212:215],a[80:83],v[40:43]
s_waitcnt lgkmcnt(11)
ds_read_u16_d16_hi v34,v34
v_lshl_add_u64 v[26:27],v[28:29],0,v[30:31]
v_mfma_f32_16x16x32_bf16 v[36:39],a[200:203],a[80:83],v[16:19]
v_lshlrev_b32_sdwa v33,1,v33 dst_sel:DWORD dst_unused:UNUSED_PAD src0_sel:DWORD src1_sel:WORD_1
v_lshlrev_b32_sdwa v35,1,v35 dst_sel:DWORD dst_unused:UNUSED_PAD src0_sel:DWORD src1_sel:WORD_1
v_mfma_f32_16x16x32_bf16 v[36:39],a[196:199],a[64:67],v[36:39]
s_waitcnt lgkmcnt(6)
ds_read_u16 v33,v33
v_lshl_add_u64 v[28:29],v[52:53],0,v[54:55]
v_mfma_f32_16x16x32_bf16 v[36:39],a[192:195],a[48:51],v[36:39]
ds_read_u16_d16_hi v35,v35
v_lshlrev_b32_sdwa v60,1,v60 dst_sel:DWORD dst_unused:UNUSED_PAD src0_sel:DWORD src1_sel:WORD_1
v_mfma_f32_16x16x32_bf16 v[52:55],a[192:195],a[52:55],v[16:19]
ds_read_u16 v60,v60
v_lshlrev_b32_sdwa v62,1,v62 dst_sel:DWORD dst_unused:UNUSED_PAD src0_sel:DWORD src1_sel:WORD_1
v_mfma_f32_16x16x32_bf16 v[52:55],a[196:199],a[68:71],v[52:55]
s_waitcnt lgkmcnt(5)
ds_read_u16_d16_hi v62,v62
v_lshl_add_u64 v[30:31],v[56:57],0,v[58:59]
v_mfma_f32_16x16x32_bf16 v[52:55],a[200:203],a[84:87],v[52:55]
s_nop 0
v_lshlrev_b32_sdwa v61,1,v61 dst_sel:DWORD dst_unused:UNUSED_PAD src0_sel:DWORD src1_sel:WORD_1
v_lshlrev_b32_sdwa v63,1,v63 dst_sel:DWORD dst_unused:UNUSED_PAD src0_sel:DWORD src1_sel:WORD_1
v_mfma_f32_16x16x32_bf16 v[56:59],a[212:215],a[84:87],v[20:23]
ds_read_u16 v61,v61
v_lshlrev_b32_sdwa v44,1,v44 dst_sel:DWORD dst_unused:UNUSED_PAD src0_sel:DWORD src1_sel:WORD_1
v_mfma_f32_16x16x32_bf16 v[56:59],a[208:211],a[68:71],v[56:59]
ds_read_u16_d16_hi v63,v63
v_lshlrev_b32_sdwa v46,1,v46 dst_sel:DWORD dst_unused:UNUSED_PAD src0_sel:DWORD src1_sel:WORD_1
v_mfma_f32_16x16x32_bf16 v[56:59],a[204:207],a[52:55],v[56:59]
ds_read_u16 v44,v44
v_lshlrev_b32_sdwa v45,1,v45 dst_sel:DWORD dst_unused:UNUSED_PAD src0_sel:DWORD src1_sel:WORD_1
v_mfma_f32_16x16x32_bf16 v[136:139],a[244:247],v[24:27],v[136:139]
ds_read_u16_d16_hi v46,v46
v_lshlrev_b32_sdwa v47,1,v47 dst_sel:DWORD dst_unused:UNUSED_PAD src0_sel:DWORD src1_sel:WORD_1
v_mfma_f32_16x16x32_bf16 v[140:143],a[244:247],v[28:31],v[140:143]
ds_read_u16 v45,v45
v_lshlrev_b32_sdwa v48,1,v48 dst_sel:DWORD dst_unused:UNUSED_PAD src0_sel:DWORD src1_sel:WORD_1
v_mfma_f32_16x16x32_bf16 v[68:71],a[252:255],v[28:31],v[68:71]
ds_read_u16_d16_hi v47,v47
v_lshlrev_b32_sdwa v50,1,v50 dst_sel:DWORD dst_unused:UNUSED_PAD src0_sel:DWORD src1_sel:WORD_1
v_mfma_f32_16x16x32_bf16 v[64:67],a[252:255],v[24:27],v[64:67]
ds_read_u16 v48,v48
v_lshlrev_b32_sdwa v49,1,v49 dst_sel:DWORD dst_unused:UNUSED_PAD src0_sel:DWORD src1_sel:WORD_1
v_mfma_f32_16x16x32_bf16 v[144:147],a[232:235],v[24:27],v[144:147]
ds_read_u16_d16_hi v50,v50
v_lshlrev_b32_sdwa v51,1,v51 dst_sel:DWORD dst_unused:UNUSED_PAD src0_sel:DWORD src1_sel:WORD_1
v_mfma_f32_16x16x32_bf16 v[148:151],a[232:235],v[28:31],v[148:151]
ds_read_u16 v49,v49
v_mfma_f32_16x16x32_bf16 v[84:87],a[240:243],v[28:31],v[84:87]
ds_read_u16_d16_hi v51,v51
v_mfma_f32_16x16x32_bf16 v[80:83],a[240:243],v[24:27],v[80:83]
v_mfma_f32_16x16x32_bf16 v[152:155],a[224:227],v[24:27],v[152:155]
v_mfma_f32_16x16x32_bf16 v[156:159],a[224:227],v[28:31],v[156:159]
v_mfma_f32_16x16x32_bf16 v[100:103],a[248:251],v[28:31],v[100:103]
v_mfma_f32_16x16x32_bf16 v[96:99],a[248:251],v[24:27],v[96:99]
v_mfma_f32_16x16x32_bf16 v[28:31],a[204:207],a[56:59],v[20:23]
v_lshlrev_b32_sdwa v40,1,v40 dst_sel:DWORD dst_unused:UNUSED_PAD src0_sel:DWORD src1_sel:WORD_1
v_fma_mixlo_f16 v8,v53,s99,0
v_mfma_f32_16x16x32_bf16 v[28:31],a[208:211],a[72:75],v[28:31]
s_waitcnt lgkmcnt(2)
ds_read_u16 v40,v40
v_lshl_add_u64 v[32:33],v[32:33],0,v[34:35]
v_mfma_f32_16x16x32_bf16 v[28:31],a[212:215],a[88:91],v[28:31]
v_lshl_add_u64 v[34:35],v[60:61],0,v[62:63]
v_lshlrev_b32_sdwa v42,1,v42 dst_sel:DWORD dst_unused:UNUSED_PAD src0_sel:DWORD src1_sel:WORD_1
v_mfma_f32_16x16x32_bf16 v[24:27],a[200:203],a[88:91],v[16:19]
ds_read_u16_d16_hi v42,v42
v_fma_mixhi_f16 v8,v55,s99,0
v_mfma_f32_16x16x32_bf16 v[24:27],a[196:199],a[72:75],v[24:27]
v_lshl_add_u64 v[60:61],v[44:45],0,v[46:47]
v_lshlrev_b32_sdwa v41,1,v41 dst_sel:DWORD dst_unused:UNUSED_PAD src0_sel:DWORD src1_sel:WORD_1
v_pk_fma_f16 v9,v8,v8,s54
v_mfma_f32_16x16x32_bf16 v[24:27],a[192:195],a[56:59],v[24:27]
ds_read_u16 v41,v41
v_lshlrev_b32_sdwa v43,1,v43 dst_sel:DWORD dst_unused:UNUSED_PAD src0_sel:DWORD src1_sel:WORD_1
v_mfma_f32_16x16x32_bf16 v[44:47],a[192:195],a[60:63],v[16:19]
ds_read_b128 v[16:19],v4 offset:768
ds_read_b64_tr_b16 a[192:193],v1 offset:10256
v_pk_mul_f16 v9,v8,v9
v_mfma_f32_16x16x32_bf16 v[44:47],a[196:199],a[76:79],v[44:47]
s_waitcnt lgkmcnt(5)
ds_read_b64_tr_b16 a[194:195],v1 offset:11280
ds_read_b64_tr_b16 a[196:197],v2 offset:10256
v_lshl_add_u64 v[62:63],v[48:49],0,v[50:51]
v_lshlrev_b32_sdwa v36,1,v36 dst_sel:DWORD dst_unused:UNUSED_PAD src0_sel:DWORD src1_sel:WORD_1
v_mfma_f32_16x16x32_bf16 v[44:47],a[200:203],a[92:95],v[44:47]
ds_read_b64_tr_b16 a[198:199],v2 offset:11280
ds_read_b64_tr_b16 a[200:201],v2 offset:8208
v_exp_f16 v8,v9
v_mfma_f32_16x16x32_bf16 v[48:51],a[212:215],a[92:95],v[20:23]
ds_read_b128 v[20:23],v4 offset:800
ds_read_b64_tr_b16 a[202:203],v2 offset:9232
v_lshlrev_b32_sdwa v38,1,v38 dst_sel:DWORD dst_unused:UNUSED_PAD src0_sel:DWORD src1_sel:WORD_1
v_mfma_f32_16x16x32_bf16 v[48:51],a[208:211],a[76:79],v[48:51]
TE11_%=:
s_cbranch_execz TP10211_%=
ds_read_b64_tr_b16 a[212:213],v2 offset:6160
ds_read_b64_tr_b16 a[214:215],v2 offset:7184
v_lshlrev_b32_sdwa v37,1,v37 dst_sel:DWORD dst_unused:UNUSED_PAD src0_sel:DWORD src1_sel:WORD_1
v_mfma_f32_16x16x32_bf16 v[48:51],a[204:207],a[60:63],v[48:51]
ds_read_u16_d16_hi v43,v43
v_exp_f16_sdwa v8,v9 dst_sel:WORD_1 dst_unused:UNUSED_PRESERVE src0_sel:WORD_1
v_mfma_f32_16x16x32_bf16 v[112:115],a[244:247],v[32:35],v[112:115]
ds_read_u16 v36,v36
v_lshlrev_b32_sdwa v39,1,v39 dst_sel:DWORD dst_unused:UNUSED_PAD src0_sel:DWORD src1_sel:WORD_1
v_mfma_f32_16x16x32_bf16 v[116:119],a[244:247],v[60:63],v[116:119]
ds_read_u16_d16_hi v38,v38
v_lshlrev_b32_sdwa v56,1,v56 dst_sel:DWORD dst_unused:UNUSED_PAD src0_sel:DWORD src1_sel:WORD_1
v_mfma_f32_16x16x32_bf16 v[76:79],a[252:255],v[60:63],v[76:79]
ds_read_u16 v37,v37
v_pk_fma_f16 v8,v8,s55,s55
v_mfma_f32_16x16x32_bf16 v[72:75],a[252:255],v[32:35],v[72:75]
v_rcp_f16_e64 v9,v8
v_lshlrev_b32_sdwa v58,1,v58 dst_sel:DWORD dst_unused:UNUSED_PAD src0_sel:DWORD src1_sel:WORD_1
v_mfma_f32_16x16x32_bf16 v[120:123],a[232:235],v[32:35],v[120:123]
ds_read_u16_d16_hi v39,v39
v_lshlrev_b32_sdwa v57,1,v57 dst_sel:DWORD dst_unused:UNUSED_PAD src0_sel:DWORD src1_sel:WORD_1
v_mfma_f32_16x16x32_bf16 v[124:127],a[232:235],v[60:63],v[124:127]
ds_read_u16 v56,v56
v_lshlrev_b32_sdwa v59,1,v59 dst_sel:DWORD dst_unused:UNUSED_PAD src0_sel:DWORD src1_sel:WORD_1
v_mfma_f32_16x16x32_bf16 v[92:95],a[240:243],v[60:63],v[92:95]
ds_read_u16_d16_hi v58,v58
v_lshlrev_b32_sdwa v52,1,v52 dst_sel:DWORD dst_unused:UNUSED_PAD src0_sel:DWORD src1_sel:WORD_1
v_mfma_f32_16x16x32_bf16 v[88:91],a[240:243],v[32:35],v[88:91]
v_rcp_f16_sdwa v9,v8 dst_sel:WORD_1 dst_unused:UNUSED_PRESERVE src0_sel:WORD_1
v_mfma_f32_16x16x32_bf16 v[128:131],a[224:227],v[32:35],v[128:131]
ds_read_u16 v57,v57
ds_read_u16_d16_hi v59,v59
v_mfma_f32_16x16x32_bf16 v[132:135],a[224:227],v[60:63],v[132:135]
ds_read_u16 v52,v52
v_lshlrev_b32_sdwa v54,1,v54 dst_sel:DWORD dst_unused:UNUSED_PAD src0_sel:DWORD src1_sel:WORD_1
v_mfma_f32_16x16x32_bf16 v[108:111],a[248:251],v[60:63],v[108:111]
ds_read_u16_d16_hi v54,v54
v_fma_mix_f32 v53,v53,v9,0 op_sel_hi:[0,1,0]
v_mfma_f32_16x16x32_bf16 v[104:107],a[248:251],v[32:35],v[104:107]
s_waitcnt lgkmcnt(7)
v_fma_mix_f32 v55,v55,v9,0 op_sel:[0,1,0] op_sel_hi:[0,1,0]
v_mfma_f32_16x16x32_bf16 v[60:63],a[216:219],a[0:3],v[16:19]
s_add_u32 m0,s62,0
v_lshlrev_b32_sdwa v28,1,v28 dst_sel:DWORD dst_unused:UNUSED_PAD src0_sel:DWORD src1_sel:WORD_1
v_lshlrev_b32_sdwa v30,1,v30 dst_sel:DWORD dst_unused:UNUSED_PAD src0_sel:DWORD src1_sel:WORD_1
v_mfma_f32_16x16x32_bf16 v[60:63],a[220:223],a[16:19],v[60:63]
s_mov_b32 s68,s87
ds_read_u16 v28,v28
v_lshl_add_u64 v[40:41],v[40:41],0,v[42:43]
v_mfma_f32_16x16x32_bf16 v[60:63],a[192:195],a[32:35],v[60:63]
s_waitcnt lgkmcnt(6)
buffer_load_dwordx4 v5,s[44:47],s68 offen offset:0 lds
v_lshl_add_u64 v[42:43],v[36:37],0,v[38:39]
v_mfma_f32_16x16x32_bf16 v[32:35],a[196:199],a[32:35],v[20:23]
s_add_u32 s68,s68,s53
ds_read_u16_d16_hi v30,v30
v_lshlrev_b32_sdwa v29,1,v29 dst_sel:DWORD dst_unused:UNUSED_PAD src0_sel:DWORD src1_sel:WORD_1
v_mfma_f32_16x16x32_bf16 v[32:35],a[200:203],a[16:19],v[32:35]
s_waitcnt lgkmcnt(4)
buffer_load_dwordx4 v5,s[44:47],s68 offen offset:1024 lds
v_lshl_add_u64 v[36:37],v[56:57],0,v[58:59]
v_mfma_f32_16x16x32_bf16 v[32:35],a[212:215],a[0:3],v[32:35]
ds_read_u16 v29,v29
v_lshlrev_b32_sdwa v31,1,v31 dst_sel:DWORD dst_unused:UNUSED_PAD src0_sel:DWORD src1_sel:WORD_1
v_mfma_f32_16x16x32_bf16 v[56:59],a[212:215],a[4:7],v[20:23]
s_waitcnt lgkmcnt(3)
ds_read_u16_d16_hi v31,v31
v_or_b32 v38,v52,v54
v_lshlrev_b32_sdwa v24,1,v24 dst_sel:DWORD dst_unused:UNUSED_PAD src0_sel:DWORD src1_sel:WORD_1
v_mfma_f32_16x16x32_bf16 v[56:59],a[200:203],a[20:23],v[56:59]
ds_read_u16 v24,v24
v_cvt_pk_bf16_f32 v39,v53,v55
v_lshlrev_b32_sdwa v26,1,v26 dst_sel:DWORD dst_unused:UNUSED_PAD src0_sel:DWORD src1_sel:WORD_1
v_mfma_f32_16x16x32_bf16 v[56:59],a[196:199],a[36:39],v[56:59]
ds_read_u16_d16_hi v26,v26
v_lshlrev_b32_sdwa v25,1,v25 dst_sel:DWORD dst_unused:UNUSED_PAD src0_sel:DWORD src1_sel:WORD_1
v_mfma_f32_16x16x32_bf16 v[52:55],a[192:195],a[36:39],v[16:19]
ds_read_u16 v25,v25
v_lshlrev_b32_sdwa v27,1,v27 dst_sel:DWORD dst_unused:UNUSED_PAD src0_sel:DWORD src1_sel:WORD_1
v_mfma_f32_16x16x32_bf16 v[52:55],a[220:223],a[20:23],v[52:55]
ds_read_u16_d16_hi v27,v27
v_lshlrev_b32_sdwa v48,1,v48 dst_sel:DWORD dst_unused:UNUSED_PAD src0_sel:DWORD src1_sel:WORD_1
v_mfma_f32_16x16x32_bf16 v[52:55],a[216:219],a[4:7],v[52:55]
ds_read_u16 v48,v48
v_lshlrev_b32_sdwa v50,1,v50 dst_sel:DWORD dst_unused:UNUSED_PAD src0_sel:DWORD src1_sel:WORD_1
v_mfma_f32_16x16x32_bf16 v[232:235],a[244:247],v[40:43],v[232:235]
ds_read_u16_d16_hi v50,v50
v_lshlrev_b32_sdwa v49,1,v49 dst_sel:DWORD dst_unused:UNUSED_PAD src0_sel:DWORD src1_sel:WORD_1
v_mfma_f32_16x16x32_bf16 v[236:239],a[244:247],v[36:39],v[236:239]
ds_read_u16 v49,v49
v_lshlrev_b32_sdwa v51,1,v51 dst_sel:DWORD dst_unused:UNUSED_PAD src0_sel:DWORD src1_sel:WORD_1
v_mfma_f32_16x16x32_bf16 v[164:167],a[252:255],v[36:39],v[164:167]
ds_read_u16_d16_hi v51,v51
ds_read_b64_tr_b16 a[228:229],v3 offset:12320
v_mfma_f32_16x16x32_bf16 v[160:163],a[252:255],v[40:43],v[160:163]
s_add_u32 s68,s68,s53
ds_read_b64_tr_b16 a[230:231],v3 offset:13856
v_lshlrev_b32_sdwa v44,1,v44 dst_sel:DWORD dst_unused:UNUSED_PAD src0_sel:DWORD src1_sel:WORD_1
v_mfma_f32_16x16x32_bf16 v[240:243],a[232:235],v[40:43],v[240:243]
buffer_load_dwordx4 v5,s[44:47],s68 offen offset:2048 lds
v_lshlrev_b32_sdwa v46,1,v46 dst_sel:DWORD dst_unused:UNUSED_PAD src0_sel:DWORD src1_sel:WORD_1
v_mfma_f32_16x16x32_bf16 v[244:247],a[232:235],v[36:39],v[244:247]
ds_read_u16 v44,v44
v_lshlrev_b32_sdwa v45,1,v45 dst_sel:DWORD dst_unused:UNUSED_PAD src0_sel:DWORD src1_sel:WORD_1
v_mfma_f32_16x16x32_bf16 v[180:183],a[240:243],v[36:39],v[180:183]
ds_read_u16_d16_hi v46,v46
v_lshlrev_b32_sdwa v47,1,v47 dst_sel:DWORD dst_unused:UNUSED_PAD src0_sel:DWORD src1_sel:WORD_1
v_mfma_f32_16x16x32_bf16 v[176:179],a[240:243],v[40:43],v[176:179]
ds_read_u16 v45,v45
v_mfma_f32_16x16x32_bf16 v[248:251],a[224:227],v[40:43],v[248:251]
ds_read_u16_d16_hi v47,v47
v_mfma_f32_16x16x32_bf16 v[252:255],a[224:227],v[36:39],v[252:255]
ds_read_b64_tr_b16 a[236:237],v3 offset:12328
v_mfma_f32_16x16x32_bf16 v[196:199],a[248:251],v[36:39],v[196:199]
ds_read_b64_tr_b16 a[238:239],v3 offset:13864
v_mfma_f32_16x16x32_bf16 v[192:195],a[248:251],v[40:43],v[192:195]
v_mfma_f32_16x16x32_bf16 v[36:39],a[216:219],a[8:11],v[16:19]
ds_read_b64_tr_b16 a[208:209],v1 offset:12320
v_lshlrev_b32_sdwa v60,1,v60 dst_sel:DWORD dst_unused:UNUSED_PAD src0_sel:DWORD src1_sel:WORD_1
v_mfma_f32_16x16x32_bf16 v[36:39],a[220:223],a[24:27],v[36:39]
s_waitcnt lgkmcnt(7)
ds_read_u16 v60,v60
v_lshl_add_u64 v[28:29],v[28:29],0,v[30:31]
v_fma_mixlo_f16 v8,v57,s99,0
v_mfma_f32_16x16x32_bf16 v[36:39],a[192:195],a[40:43],v[36:39]
ds_read_b64_tr_b16 a[210:211],v1 offset:13344
v_lshl_add_u64 v[30:31],v[24:25],0,v[26:27]
v_lshlrev_b32_sdwa v62,1,v62 dst_sel:DWORD dst_unused:UNUSED_PAD src0_sel:DWORD src1_sel:WORD_1
v_mfma_f32_16x16x32_bf16 v[40:43],a[196:199],a[40:43],v[20:23]
ds_read_u16_d16_hi v62,v62
v_fma_mixhi_f16 v8,v59,s99,0
v_mfma_f32_16x16x32_bf16 v[40:43],a[200:203],a[24:27],v[40:43]
ds_read_b64_tr_b16 a[204:205],v1 offset:14368
v_lshl_add_u64 v[24:25],v[48:49],0,v[50:51]
v_lshlrev_b32_sdwa v61,1,v61 dst_sel:DWORD dst_unused:UNUSED_PAD src0_sel:DWORD src1_sel:WORD_1
v_pk_fma_f16 v9,v8,v8,s54
v_mfma_f32_16x16x32_bf16 v[40:43],a[212:215],a[8:11],v[40:43]
ds_read_u16 v61,v61
v_lshlrev_b32_sdwa v63,1,v63 dst_sel:DWORD dst_unused:UNUSED_PAD src0_sel:DWORD src1_sel:WORD_1
v_mfma_f32_16x16x32_bf16 v[48:51],a[212:215],a[12:15],v[20:23]
ds_read_u16_d16_hi v63,v63
v_pk_mul_f16 v9,v8,v9
v_mfma_f32_16x16x32_bf16 v[48:51],a[200:203],a[28:31],v[48:51]
s_waitcnt lgkmcnt(7)
ds_read_b64_tr_b16 a[206:207],v1 offset:15392
v_lshl_add_u64 v[26:27],v[44:45],0,v[46:47]
v_lshlrev_b32_sdwa v32,1,v32 dst_sel:DWORD dst_unused:UNUSED_PAD src0_sel:DWORD src1_sel:WORD_1
v_mfma_f32_16x16x32_bf16 v[48:51],a[196:199],a[44:47],v[48:51]
v_exp_f16 v8,v9
v_mfma_f32_16x16x32_bf16 v[44:47],a[192:195],a[44:47],v[16:19]
ds_read_u16 v32,v32
v_lshlrev_b32_sdwa v34,1,v34 dst_sel:DWORD dst_unused:UNUSED_PAD src0_sel:DWORD src1_sel:WORD_1
v_mfma_f32_16x16x32_bf16 v[44:47],a[220:223],a[28:31],v[44:47]
ds_read_u16_d16_hi v34,v34
v_lshlrev_b32_sdwa v33,1,v33 dst_sel:DWORD dst_unused:UNUSED_PAD src0_sel:DWORD src1_sel:WORD_1
v_mfma_f32_16x16x32_bf16 v[44:47],a[216:219],a[12:15],v[44:47]
v_exp_f16_sdwa v8,v9 dst_sel:WORD_1 dst_unused:UNUSED_PRESERVE src0_sel:WORD_1
v_mfma_f32_16x16x32_bf16 v[208:211],a[244:247],v[28:31],v[208:211]
ds_read_u16 v33,v33
v_lshlrev_b32_sdwa v35,1,v35 dst_sel:DWORD dst_unused:UNUSED_PAD src0_sel:DWORD src1_sel:WORD_1
v_mfma_f32_16x16x32_bf16 v[212:215],a[244:247],v[24:27],v[212:215]
ds_read_u16_d16_hi v35,v35
v_lshlrev_b32_sdwa v52,1,v52 dst_sel:DWORD dst_unused:UNUSED_PAD src0_sel:DWORD src1_sel:WORD_1
v_mfma_f32_16x16x32_bf16 v[172:175],a[252:255],v[24:27],v[172:175]
ds_read_u16 v52,v52
v_pk_fma_f16 v8,v8,s55,s55
v_mfma_f32_16x16x32_bf16 v[168:171],a[252:255],v[28:31],v[168:171]
ds_read_b64_tr_b16 a[244:245],v3 offset:12384
v_rcp_f16 v9,v8
v_lshlrev_b32_sdwa v54,1,v54 dst_sel:DWORD dst_unused:UNUSED_PAD src0_sel:DWORD src1_sel:WORD_1
v_mfma_f32_16x16x32_bf16 v[216:219],a[232:235],v[28:31],v[216:219]
ds_read_u16_d16_hi v54,v54
v_lshlrev_b32_sdwa v53,1,v53 dst_sel:DWORD dst_unused:UNUSED_PAD src0_sel:DWORD src1_sel:WORD_1
v_mfma_f32_16x16x32_bf16 v[220:223],a[232:235],v[24:27],v[220:223]
ds_read_u16 v53,v53
ds_read_b64_tr_b16 a[246:247],v3 offset:13920
v_mfma_f32_16x16x32_bf16 v[188:191],a[240:243],v[24:27],v[188:191]
ds_read_b64_tr_b16 a[252:253],v3 offset:12392
v_lshlrev_b32_sdwa v55,1,v55 dst_sel:DWORD dst_unused:UNUSED_PAD src0_sel:DWORD src1_sel:WORD_1
v_mfma_f32_16x16x32_bf16 v[184:187],a[240:243],v[28:31],v[184:187]
ds_read_u16_d16_hi v55,v55
v_rcp_f16_sdwa v9,v8 dst_sel:WORD_1 dst_unused:UNUSED_PRESERVE src0_sel:WORD_1
v_mfma_f32_16x16x32_bf16 v[224:227],a[224:227],v[28:31],v[224:227]
ds_read_b64_tr_b16 a[254:255],v3 offset:13928
ds_read_b64_tr_b16 a[232:233],v3 offset:12448
v_lshlrev_b32_sdwa v56,1,v56 dst_sel:DWORD dst_unused:UNUSED_PAD src0_sel:DWORD src1_sel:WORD_1
v_mfma_f32_16x16x32_bf16 v[228:231],a[224:227],v[24:27],v[228:231]
ds_read_u16 v56,v56
v_lshlrev_b32_sdwa v58,1,v58 dst_sel:DWORD dst_unused:UNUSED_PAD src0_sel:DWORD src1_sel:WORD_1
v_mfma_f32_16x16x32_bf16 v[204:207],a[248:251],v[24:27],v[204:207]
ds_read_u16_d16_hi v58,v58
v_fma_mix_f32 v57,v57,v9,0 op_sel_hi:[0,1,0]
v_mfma_f32_16x16x32_bf16 v[200:203],a[248:251],v[28:31],v[200:203]
ds_read_b64_tr_b16 a[234:235],v3 offset:13984
v_fma_mix_f32 v59,v59,v9,0 op_sel:[0,1,0] op_sel_hi:[0,1,0]
v_mfma_f32_16x16x32_bf16 v[24:27],a[216:219],a[48:51],v[16:19]
s_add_u32 m0,s62,6160
ds_read_b64_tr_b16 a[240:241],v3 offset:12456
v_lshlrev_b32_sdwa v36,1,v36 dst_sel:DWORD dst_unused:UNUSED_PAD src0_sel:DWORD src1_sel:WORD_1
v_mfma_f32_16x16x32_bf16 v[24:27],a[220:223],a[64:67],v[24:27]
s_waitcnt lgkmcnt(10)
ds_read_b64_tr_b16 a[242:243],v3 offset:13992
v_lshl_add_u64 v[60:61],v[60:61],0,v[62:63]
v_mfma_f32_16x16x32_bf16 v[24:27],a[192:195],a[80:83],v[24:27]
s_add_u32 s68,s68,s59
ds_read_u16 v36,v36
v_lshl_add_u64 v[62:63],v[32:33],0,v[34:35]
v_lshlrev_b32_sdwa v38,1,v38 dst_sel:DWORD dst_unused:UNUSED_PAD src0_sel:DWORD src1_sel:WORD_1
v_mfma_f32_16x16x32_bf16 v[28:31],a[196:199],a[80:83],v[20:23]
buffer_load_dwordx4 v5,s[44:47],s68 offen offset:0 lds
v_lshlrev_b32_sdwa v37,1,v37 dst_sel:DWORD dst_unused:UNUSED_PAD src0_sel:DWORD src1_sel:WORD_1
v_mfma_f32_16x16x32_bf16 v[28:31],a[200:203],a[64:67],v[28:31]
s_waitcnt lgkmcnt(8)
ds_read_u16_d16_hi v38,v38
v_lshl_add_u64 v[32:33],v[52:53],0,v[54:55]
v_mfma_f32_16x16x32_bf16 v[28:31],a[212:215],a[48:51],v[28:31]
s_add_u32 s68,s68,s53
ds_read_u16 v37,v37
v_lshlrev_b32_sdwa v39,1,v39 dst_sel:DWORD dst_unused:UNUSED_PAD src0_sel:DWORD src1_sel:WORD_1
v_mfma_f32_16x16x32_bf16 v[52:55],a[212:215],a[52:55],v[20:23]
s_waitcnt lgkmcnt(6)
buffer_load_dwordx4 v5,s[44:47],s68 offen offset:1024 lds
v_or_b32 v34,v56,v58
v_lshlrev_b32_sdwa v40,1,v40 dst_sel:DWORD dst_unused:UNUSED_PAD src0_sel:DWORD src1_sel:WORD_1
v_mfma_f32_16x16x32_bf16 v[52:55],a[200:203],a[68:71],v[52:55]
s_add_u32 s68,s68,s53
ds_read_u16_d16_hi v39,v39
v_cvt_pk_bf16_f32 v35,v57,v59
v_lshlrev_b32_sdwa v42,1,v42 dst_sel:DWORD dst_unused:UNUSED_PAD src0_sel:DWORD src1_sel:WORD_1
v_mfma_f32_16x16x32_bf16 v[52:55],a[196:199],a[84:87],v[52:55]
buffer_load_dwordx4 v5,s[44:47],s68 offen offset:2048 lds
v_lshlrev_b32_sdwa v41,1,v41 dst_sel:DWORD dst_unused:UNUSED_PAD src0_sel:DWORD src1_sel:WORD_1
v_mfma_f32_16x16x32_bf16 v[56:59],a[192:195],a[84:87],v[16:19]
ds_read_u16 v40,v40
v_lshlrev_b32_sdwa v43,1,v43 dst_sel:DWORD dst_unused:UNUSED_PAD src0_sel:DWORD src1_sel:WORD_1
v_mfma_f32_16x16x32_bf16 v[56:59],a[220:223],a[68:71],v[56:59]
ds_read_u16_d16_hi v42,v42
v_lshlrev_b32_sdwa v44,1,v44 dst_sel:DWORD dst_unused:UNUSED_PAD src0_sel:DWORD src1_sel:WORD_1
v_mfma_f32_16x16x32_bf16 v[56:59],a[216:219],a[52:55],v[56:59]
ds_read_u16 v41,v41
v_lshlrev_b32_sdwa v46,1,v46 dst_sel:DWORD dst_unused:UNUSED_PAD src0_sel:DWORD src1_sel:WORD_1
v_mfma_f32_16x16x32_bf16 v[136:139],a[228:231],v[60:63],v[136:139]
ds_read_u16_d16_hi v43,v43
v_lshlrev_b32_sdwa v45,1,v45 dst_sel:DWORD dst_unused:UNUSED_PAD src0_sel:DWORD src1_sel:WORD_1
v_mfma_f32_16x16x32_bf16 v[140:143],a[228:231],v[32:35],v[140:143]
ds_read_u16 v44,v44
v_lshlrev_b32_sdwa v47,1,v47 dst_sel:DWORD dst_unused:UNUSED_PAD src0_sel:DWORD src1_sel:WORD_1
v_mfma_f32_16x16x32_bf16 v[68:71],a[236:239],v[32:35],v[68:71]
ds_read_u16_d16_hi v46,v46
v_lshlrev_b32_sdwa v48,1,v48 dst_sel:DWORD dst_unused:UNUSED_PAD src0_sel:DWORD src1_sel:WORD_1
v_mfma_f32_16x16x32_bf16 v[64:67],a[236:239],v[60:63],v[64:67]
ds_read_u16 v45,v45
v_lshlrev_b32_sdwa v50,1,v50 dst_sel:DWORD dst_unused:UNUSED_PAD src0_sel:DWORD src1_sel:WORD_1
v_mfma_f32_16x16x32_bf16 v[144:147],a[244:247],v[60:63],v[144:147]
s_add_u32 m0,s62,12320
ds_read_u16_d16_hi v47,v47
v_lshlrev_b32_sdwa v49,1,v49 dst_sel:DWORD dst_unused:UNUSED_PAD src0_sel:DWORD src1_sel:WORD_1
v_mfma_f32_16x16x32_bf16 v[148:151],a[244:247],v[32:35],v[148:151]
s_add_u32 s68,s68,s59
ds_read_u16 v48,v48
v_lshlrev_b32_sdwa v51,1,v51 dst_sel:DWORD dst_unused:UNUSED_PAD src0_sel:DWORD src1_sel:WORD_1
v_mfma_f32_16x16x32_bf16 v[84:87],a[252:255],v[32:35],v[84:87]
buffer_load_dwordx4 v5,s[44:47],s68 offen offset:0 lds
v_mfma_f32_16x16x32_bf16 v[80:83],a[252:255],v[60:63],v[80:83]
s_waitcnt lgkmcnt(7)
ds_read_u16_d16_hi v50,v50
v_mfma_f32_16x16x32_bf16 v[152:155],a[232:235],v[60:63],v[152:155]
ds_read_u16 v49,v49
v_mfma_f32_16x16x32_bf16 v[156:159],a[232:235],v[32:35],v[156:159]
ds_read_u16_d16_hi v51,v51
v_mfma_f32_16x16x32_bf16 v[100:103],a[240:243],v[32:35],v[100:103]
v_mfma_f32_16x16x32_bf16 v[96:99],a[240:243],v[60:63],v[96:99]
v_mfma_f32_16x16x32_bf16 v[32:35],a[216:219],a[56:59],v[16:19]
v_lshlrev_b32_sdwa v24,1,v24 dst_sel:DWORD dst_unused:UNUSED_PAD src0_sel:DWORD src1_sel:WORD_1
v_fma_mixlo_f16 v8,v53,s99,0
v_mfma_f32_16x16x32_bf16 v[32:35],a[220:223],a[72:75],v[32:35]
ds_read_u16 v24,v24
v_lshl_add_u64 v[36:37],v[36:37],0,v[38:39]
v_mfma_f32_16x16x32_bf16 v[32:35],a[192:195],a[88:91],v[32:35]
s_waitcnt lgkmcnt(4)
v_lshl_add_u64 v[38:39],v[40:41],0,v[42:43]
v_lshlrev_b32_sdwa v26,1,v26 dst_sel:DWORD dst_unused:UNUSED_PAD src0_sel:DWORD src1_sel:WORD_1
v_mfma_f32_16x16x32_bf16 v[60:63],a[196:199],a[88:91],v[20:23]
ds_read_u16_d16_hi v26,v26
v_fma_mixhi_f16 v8,v55,s99,0
v_mfma_f32_16x16x32_bf16 v[60:63],a[200:203],a[72:75],v[60:63]
v_lshl_add_u64 v[40:41],v[44:45],0,v[46:47]
v_lshlrev_b32_sdwa v25,1,v25 dst_sel:DWORD dst_unused:UNUSED_PAD src0_sel:DWORD src1_sel:WORD_1
v_pk_fma_f16 v9,v8,v8,s54
v_mfma_f32_16x16x32_bf16 v[60:63],a[212:215],a[56:59],v[60:63]
ds_read_u16 v25,v25
v_lshlrev_b32_sdwa v27,1,v27 dst_sel:DWORD dst_unused:UNUSED_PAD src0_sel:DWORD src1_sel:WORD_1
v_mfma_f32_16x16x32_bf16 v[44:47],a[212:215],a[60:63],v[20:23]
ds_read_b128 v[20:23],v4 offset:896
v_pk_mul_f16 v9,v8,v9
v_mfma_f32_16x16x32_bf16 v[44:47],a[200:203],a[76:79],v[44:47]
s_waitcnt lgkmcnt(4)
ds_read_u16_d16_hi v27,v27
ds_read_b64_tr_b16 a[212:213],v1 offset:16416
v_lshl_add_u64 v[42:43],v[48:49],0,v[50:51]
v_lshlrev_b32_sdwa v28,1,v28 dst_sel:DWORD dst_unused:UNUSED_PAD src0_sel:DWORD src1_sel:WORD_1
v_mfma_f32_16x16x32_bf16 v[44:47],a[196:199],a[92:95],v[44:47]
ds_read_b64_tr_b16 a[214:215],v1 offset:17440
v_exp_f16 v8,v9
v_mfma_f32_16x16x32_bf16 v[48:51],a[192:195],a[92:95],v[16:19]
ds_read_b128 v[16:19],v4 offset:928
ds_read_u16 v28,v28
v_lshlrev_b32_sdwa v30,1,v30 dst_sel:DWORD dst_unused:UNUSED_PAD src0_sel:DWORD src1_sel:WORD_1
v_mfma_f32_16x16x32_bf16 v[48:51],a[220:223],a[76:79],v[48:51]
ds_read_u16_d16_hi v30,v30
ds_read_b64_tr_b16 a[200:201],v2 offset:16416
v_lshlrev_b32_sdwa v29,1,v29 dst_sel:DWORD dst_unused:UNUSED_PAD src0_sel:DWORD src1_sel:WORD_1
v_mfma_f32_16x16x32_bf16 v[48:51],a[216:219],a[60:63],v[48:51]
ds_read_u16 v29,v29
v_exp_f16_sdwa v8,v9 dst_sel:WORD_1 dst_unused:UNUSED_PRESERVE src0_sel:WORD_1
v_mfma_f32_16x16x32_bf16 v[112:115],a[228:231],v[36:39],v[112:115]
ds_read_b64_tr_b16 a[202:203],v2 offset:17440
ds_read_b64_tr_b16 a[196:197],v2 offset:14368
v_lshlrev_b32_sdwa v31,1,v31 dst_sel:DWORD dst_unused:UNUSED_PAD src0_sel:DWORD src1_sel:WORD_1
v_mfma_f32_16x16x32_bf16 v[116:119],a[228:231],v[40:43],v[116:119]
ds_read_u16_d16_hi v31,v31
v_lshlrev_b32_sdwa v56,1,v56 dst_sel:DWORD dst_unused:UNUSED_PAD src0_sel:DWORD src1_sel:WORD_1
v_mfma_f32_16x16x32_bf16 v[76:79],a[236:239],v[40:43],v[76:79]
ds_read_b64_tr_b16 a[198:199],v2 offset:15392
v_pk_fma_f16 v8,v8,s55,s55
v_mfma_f32_16x16x32_bf16 v[72:75],a[236:239],v[36:39],v[72:75]
v_rcp_f16 v9,v8
v_lshlrev_b32_sdwa v58,1,v58 dst_sel:DWORD dst_unused:UNUSED_PAD src0_sel:DWORD src1_sel:WORD_1
v_mfma_f32_16x16x32_bf16 v[120:123],a[244:247],v[36:39],v[120:123]
ds_read_u16 v56,v56
v_lshlrev_b32_sdwa v57,1,v57 dst_sel:DWORD dst_unused:UNUSED_PAD src0_sel:DWORD src1_sel:WORD_1
v_mfma_f32_16x16x32_bf16 v[124:127],a[244:247],v[40:43],v[124:127]
ds_read_u16_d16_hi v58,v58
v_lshlrev_b32_sdwa v59,1,v59 dst_sel:DWORD dst_unused:UNUSED_PAD src0_sel:DWORD src1_sel:WORD_1
v_mfma_f32_16x16x32_bf16 v[92:95],a[252:255],v[40:43],v[92:95]
ds_read_u16 v57,v57
v_lshlrev_b32_sdwa v52,1,v52 dst_sel:DWORD dst_unused:UNUSED_PAD src0_sel:DWORD src1_sel:WORD_1
v_mfma_f32_16x16x32_bf16 v[88:91],a[252:255],v[36:39],v[88:91]
ds_read_u16_d16_hi v59,v59
v_rcp_f16_sdwa v9,v8 dst_sel:WORD_1 dst_unused:UNUSED_PRESERVE src0_sel:WORD_1
v_mfma_f32_16x16x32_bf16 v[128:131],a[232:235],v[36:39],v[128:131]
ds_read_b64_tr_b16 a[192:193],v2 offset:12320
v_lshlrev_b32_sdwa v54,1,v54 dst_sel:DWORD dst_unused:UNUSED_PAD src0_sel:DWORD src1_sel:WORD_1
v_mfma_f32_16x16x32_bf16 v[132:135],a[232:235],v[40:43],v[132:135]
ds_read_b64_tr_b16 a[194:195],v2 offset:13344
v_fma_mix_f32 v53,v53,v9,0 op_sel_hi:[0,1,0]
v_mfma_f32_16x16x32_bf16 v[108:111],a[240:243],v[40:43],v[108:111]
ds_read_u16 v52,v52
v_fma_mix_f32 v55,v55,v9,0 op_sel:[0,1,0] op_sel_hi:[0,1,0]
v_mfma_f32_16x16x32_bf16 v[104:107],a[240:243],v[36:39],v[104:107]
s_waitcnt lgkmcnt(7)
ds_read_u16_d16_hi v54,v54
v_mfma_f32_16x16x32_bf16 v[40:43],a[208:211],a[0:3],v[20:23]
s_add_u32 s68,s68,s53
v_lshlrev_b32_sdwa v32,1,v32 dst_sel:DWORD dst_unused:UNUSED_PAD src0_sel:DWORD src1_sel:WORD_1
v_lshlrev_b32_sdwa v34,1,v34 dst_sel:DWORD dst_unused:UNUSED_PAD src0_sel:DWORD src1_sel:WORD_1
v_mfma_f32_16x16x32_bf16 v[40:43],a[204:207],a[16:19],v[40:43]
buffer_load_dwordx4 v5,s[44:47],s68 offen offset:1024 lds
v_lshl_add_u64 v[24:25],v[24:25],0,v[26:27]
v_mfma_f32_16x16x32_bf16 v[40:43],a[212:215],a[32:35],v[40:43]
s_add_u32 s68,s68,s53
ds_read_u16 v32,v32
v_lshl_add_u64 v[26:27],v[28:29],0,v[30:31]
v_mfma_f32_16x16x32_bf16 v[36:39],a[200:203],a[32:35],v[16:19]
buffer_load_dwordx4 v5,s[44:47],s68 offen offset:2048 lds
v_lshlrev_b32_sdwa v33,1,v33 dst_sel:DWORD dst_unused:UNUSED_PAD src0_sel:DWORD src1_sel:WORD_1
v_mfma_f32_16x16x32_bf16 v[36:39],a[196:199],a[16:19],v[36:39]
s_waitcnt lgkmcnt(3)
ds_read_u16_d16_hi v34,v34
v_lshl_add_u64 v[28:29],v[56:57],0,v[58:59]
v_mfma_f32_16x16x32_bf16 v[36:39],a[192:195],a[0:3],v[36:39]
ds_read_u16 v33,v33
v_lshlrev_b32_sdwa v35,1,v35 dst_sel:DWORD dst_unused:UNUSED_PAD src0_sel:DWORD src1_sel:WORD_1
v_mfma_f32_16x16x32_bf16 v[56:59],a[192:195],a[4:7],v[16:19]
s_waitcnt lgkmcnt(3)
ds_read_u16_d16_hi v35,v35
v_or_b32 v30,v52,v54
v_lshlrev_b32_sdwa v60,1,v60 dst_sel:DWORD dst_unused:UNUSED_PAD src0_sel:DWORD src1_sel:WORD_1
v_mfma_f32_16x16x32_bf16 v[56:59],a[196:199],a[20:23],v[56:59]
s_waitcnt vmcnt(17)
ds_read_u16 v60,v60
v_cvt_pk_bf16_f32 v31,v53,v55
v_lshlrev_b32_sdwa v62,1,v62 dst_sel:DWORD dst_unused:UNUSED_PAD src0_sel:DWORD src1_sel:WORD_1
v_mfma_f32_16x16x32_bf16 v[56:59],a[200:203],a[36:39],v[56:59]
s_barrier
ds_read_u16_d16_hi v62,v62
v_lshlrev_b32_sdwa v61,1,v61 dst_sel:DWORD dst_unused:UNUSED_PAD src0_sel:DWORD src1_sel:WORD_1
v_mfma_f32_16x16x32_bf16 v[52:55],a[212:215],a[36:39],v[20:23]
s_add_u32 m0,s62,18480
ds_read_u16 v61,v61
v_lshlrev_b32_sdwa v63,1,v63 dst_sel:DWORD dst_unused:UNUSED_PAD src0_sel:DWORD src1_sel:WORD_1
v_mfma_f32_16x16x32_bf16 v[52:55],a[204:207],a[20:23],v[52:55]
s_add_u32 s68,s68,s59
ds_read_u16_d16_hi v63,v63
v_lshlrev_b32_sdwa v48,1,v48 dst_sel:DWORD dst_unused:UNUSED_PAD src0_sel:DWORD src1_sel:WORD_1
v_mfma_f32_16x16x32_bf16 v[52:55],a[208:211],a[4:7],v[52:55]
buffer_load_dwordx4 v5,s[44:47],s68 offen offset:0 lds
v_lshlrev_b32_sdwa v50,1,v50 dst_sel:DWORD dst_unused:UNUSED_PAD src0_sel:DWORD src1_sel:WORD_1
v_mfma_f32_16x16x32_bf16 v[232:235],a[228:231],v[24:27],v[232:235]
s_add_u32 m0,s58,6160
ds_read_u16 v48,v48
v_lshlrev_b32_sdwa v49,1,v49 dst_sel:DWORD dst_unused:UNUSED_PAD src0_sel:DWORD src1_sel:WORD_1
v_mfma_f32_16x16x32_bf16 v[236:239],a[228:231],v[28:31],v[236:239]
s_add_u32 s69,s69,s59
ds_read_u16_d16_hi v50,v50
v_lshlrev_b32_sdwa v51,1,v51 dst_sel:DWORD dst_unused:UNUSED_PAD src0_sel:DWORD src1_sel:WORD_1
v_mfma_f32_16x16x32_bf16 v[164:167],a[236:239],v[28:31],v[164:167]
buffer_load_dwordx4 v5,s[44:47],s69 offen offset:0 lds
ds_read_u16 v49,v49
v_mfma_f32_16x16x32_bf16 v[160:163],a[236:239],v[24:27],v[160:163]
ds_read_u16_d16_hi v51,v51
ds_read_b64_tr_b16 a[224:225],v3 offset:18480
v_mfma_f32_16x16x32_bf16 v[240:243],a[244:247],v[24:27],v[240:243]
s_add_u32 s69,s69,s53
ds_read_b64_tr_b16 a[226:227],v3 offset:20016
v_lshlrev_b32_sdwa v44,1,v44 dst_sel:DWORD dst_unused:UNUSED_PAD src0_sel:DWORD src1_sel:WORD_1
v_mfma_f32_16x16x32_bf16 v[244:247],a[244:247],v[28:31],v[244:247]
buffer_load_dwordx4 v5,s[44:47],s69 offen offset:1024 lds
v_lshlrev_b32_sdwa v46,1,v46 dst_sel:DWORD dst_unused:UNUSED_PAD src0_sel:DWORD src1_sel:WORD_1
v_mfma_f32_16x16x32_bf16 v[180:183],a[252:255],v[28:31],v[180:183]
ds_read_u16 v44,v44
v_lshlrev_b32_sdwa v45,1,v45 dst_sel:DWORD dst_unused:UNUSED_PAD src0_sel:DWORD src1_sel:WORD_1
v_mfma_f32_16x16x32_bf16 v[176:179],a[252:255],v[24:27],v[176:179]
s_add_u32 m0,s62,18480
ds_read_u16_d16_hi v46,v46
v_lshlrev_b32_sdwa v47,1,v47 dst_sel:DWORD dst_unused:UNUSED_PAD src0_sel:DWORD src1_sel:WORD_1
v_mfma_f32_16x16x32_bf16 v[248:251],a[232:235],v[24:27],v[248:251]
s_add_u32 s68,s68,s53
ds_read_u16 v45,v45
v_mfma_f32_16x16x32_bf16 v[252:255],a[232:235],v[28:31],v[252:255]
buffer_load_dwordx4 v5,s[44:47],s68 offen offset:1024 lds
v_mfma_f32_16x16x32_bf16 v[196:199],a[240:243],v[28:31],v[196:199]
s_add_u32 m0,s58,6160
ds_read_u16_d16_hi v47,v47
v_mfma_f32_16x16x32_bf16 v[192:195],a[240:243],v[24:27],v[192:195]
s_add_u32 s69,s69,s53
ds_read_b64_tr_b16 a[248:249],v3 offset:18488
v_mfma_f32_16x16x32_bf16 v[28:31],a[208:211],a[8:11],v[20:23]
buffer_load_dwordx4 v5,s[44:47],s69 offen offset:2048 lds
v_lshlrev_b32_sdwa v40,1,v40 dst_sel:DWORD dst_unused:UNUSED_PAD src0_sel:DWORD src1_sel:WORD_1
v_mfma_f32_16x16x32_bf16 v[28:31],a[204:207],a[24:27],v[28:31]
s_waitcnt lgkmcnt(5)
ds_read_u16 v40,v40
v_lshl_add_u64 v[32:33],v[32:33],0,v[34:35]
v_fma_mixlo_f16 v8,v57,s99,0
v_mfma_f32_16x16x32_bf16 v[28:31],a[212:215],a[40:43],v[28:31]
ds_read_b64_tr_b16 a[250:251],v3 offset:20024
v_lshl_add_u64 v[34:35],v[60:61],0,v[62:63]
v_lshlrev_b32_sdwa v42,1,v42 dst_sel:DWORD dst_unused:UNUSED_PAD src0_sel:DWORD src1_sel:WORD_1
v_mfma_f32_16x16x32_bf16 v[24:27],a[200:203],a[40:43],v[16:19]
ds_read_u16_d16_hi v42,v42
v_fma_mixhi_f16 v8,v59,s99,0
v_mfma_f32_16x16x32_bf16 v[24:27],a[196:199],a[24:27],v[24:27]
ds_read_b64_tr_b16 a[220:221],v1 offset:18480
v_lshl_add_u64 v[60:61],v[48:49],0,v[50:51]
v_lshlrev_b32_sdwa v41,1,v41 dst_sel:DWORD dst_unused:UNUSED_PAD src0_sel:DWORD src1_sel:WORD_1
v_pk_fma_f16 v9,v8,v8,s54
v_mfma_f32_16x16x32_bf16 v[24:27],a[192:195],a[8:11],v[24:27]
ds_read_u16 v41,v41
v_lshlrev_b32_sdwa v43,1,v43 dst_sel:DWORD dst_unused:UNUSED_PAD src0_sel:DWORD src1_sel:WORD_1
v_mfma_f32_16x16x32_bf16 v[48:51],a[192:195],a[12:15],v[16:19]
ds_read_u16_d16_hi v43,v43
v_pk_mul_f16 v9,v8,v9
v_mfma_f32_16x16x32_bf16 v[48:51],a[196:199],a[28:31],v[48:51]
s_waitcnt lgkmcnt(7)
ds_read_b64_tr_b16 a[222:223],v1 offset:19504
v_lshl_add_u64 v[62:63],v[44:45],0,v[46:47]
v_lshlrev_b32_sdwa v36,1,v36 dst_sel:DWORD dst_unused:UNUSED_PAD src0_sel:DWORD src1_sel:WORD_1
v_mfma_f32_16x16x32_bf16 v[48:51],a[200:203],a[44:47],v[48:51]
v_exp_f16 v8,v9
v_mfma_f32_16x16x32_bf16 v[44:47],a[212:215],a[44:47],v[20:23]
ds_read_u16 v36,v36
v_lshlrev_b32_sdwa v38,1,v38 dst_sel:DWORD dst_unused:UNUSED_PAD src0_sel:DWORD src1_sel:WORD_1
v_mfma_f32_16x16x32_bf16 v[44:47],a[204:207],a[28:31],v[44:47]
ds_read_u16_d16_hi v38,v38
v_lshlrev_b32_sdwa v37,1,v37 dst_sel:DWORD dst_unused:UNUSED_PAD src0_sel:DWORD src1_sel:WORD_1
v_mfma_f32_16x16x32_bf16 v[44:47],a[208:211],a[12:15],v[44:47]
v_exp_f16_sdwa v8,v9 dst_sel:WORD_1 dst_unused:UNUSED_PRESERVE src0_sel:WORD_1
v_mfma_f32_16x16x32_bf16 v[208:211],a[228:231],v[32:35],v[208:211]
ds_read_u16 v37,v37
v_lshlrev_b32_sdwa v39,1,v39 dst_sel:DWORD dst_unused:UNUSED_PAD src0_sel:DWORD src1_sel:WORD_1
v_mfma_f32_16x16x32_bf16 v[212:215],a[228:231],v[60:63],v[212:215]
ds_read_u16_d16_hi v39,v39
v_lshlrev_b32_sdwa v52,1,v52 dst_sel:DWORD dst_unused:UNUSED_PAD src0_sel:DWORD src1_sel:WORD_1
v_mfma_f32_16x16x32_bf16 v[172:175],a[236:239],v[60:63],v[172:175]
ds_read_u16 v52,v52
v_pk_fma_f16 v8,v8,s55,s55
v_mfma_f32_16x16x32_bf16 v[168:171],a[236:239],v[32:35],v[168:171]
ds_read_b64_tr_b16 a[228:229],v3 offset:18544
v_rcp_f16_e64 v9,v8
v_lshlrev_b32_sdwa v54,1,v54 dst_sel:DWORD dst_unused:UNUSED_PAD src0_sel:DWORD src1_sel:WORD_1
v_mfma_f32_16x16x32_bf16 v[216:219],a[244:247],v[32:35],v[216:219]
ds_read_u16_d16_hi v54,v54
v_lshlrev_b32_sdwa v53,1,v53 dst_sel:DWORD dst_unused:UNUSED_PAD src0_sel:DWORD src1_sel:WORD_1
v_mfma_f32_16x16x32_bf16 v[220:223],a[244:247],v[60:63],v[220:223]
ds_read_u16 v53,v53
ds_read_b64_tr_b16 a[230:231],v3 offset:20080
v_mfma_f32_16x16x32_bf16 v[188:191],a[252:255],v[60:63],v[188:191]
ds_read_b64_tr_b16 a[236:237],v3 offset:18552
v_lshlrev_b32_sdwa v55,1,v55 dst_sel:DWORD dst_unused:UNUSED_PAD src0_sel:DWORD src1_sel:WORD_1
v_mfma_f32_16x16x32_bf16 v[184:187],a[252:255],v[32:35],v[184:187]
ds_read_u16_d16_hi v55,v55
v_rcp_f16_sdwa v9,v8 dst_sel:WORD_1 dst_unused:UNUSED_PRESERVE src0_sel:WORD_1
v_mfma_f32_16x16x32_bf16 v[224:227],a[232:235],v[32:35],v[224:227]
ds_read_b64_tr_b16 a[238:239],v3 offset:20088
ds_read_b64_tr_b16 a[244:245],v3 offset:18608
v_lshlrev_b32_sdwa v56,1,v56 dst_sel:DWORD dst_unused:UNUSED_PAD src0_sel:DWORD src1_sel:WORD_1
v_mfma_f32_16x16x32_bf16 v[228:231],a[232:235],v[60:63],v[228:231]
ds_read_u16 v56,v56
v_lshlrev_b32_sdwa v58,1,v58 dst_sel:DWORD dst_unused:UNUSED_PAD src0_sel:DWORD src1_sel:WORD_1
v_mfma_f32_16x16x32_bf16 v[204:207],a[240:243],v[60:63],v[204:207]
ds_read_u16_d16_hi v58,v58
v_fma_mix_f32 v57,v57,v9,0 op_sel_hi:[0,1,0]
v_mfma_f32_16x16x32_bf16 v[200:203],a[240:243],v[32:35],v[200:203]
ds_read_b64_tr_b16 a[246:247],v3 offset:20144
v_fma_mix_f32 v59,v59,v9,0 op_sel:[0,1,0] op_sel_hi:[0,1,0]
v_mfma_f32_16x16x32_bf16 v[60:63],a[208:211],a[48:51],v[20:23]
s_add_u32 m0,s58,12320
ds_read_b64_tr_b16 a[252:253],v3 offset:18616
v_lshlrev_b32_sdwa v28,1,v28 dst_sel:DWORD dst_unused:UNUSED_PAD src0_sel:DWORD src1_sel:WORD_1
v_mfma_f32_16x16x32_bf16 v[60:63],a[204:207],a[64:67],v[60:63]
s_waitcnt lgkmcnt(10)
ds_read_b64_tr_b16 a[254:255],v3 offset:20152
v_lshl_add_u64 v[40:41],v[40:41],0,v[42:43]
v_mfma_f32_16x16x32_bf16 v[60:63],a[212:215],a[80:83],v[60:63]
s_add_u32 s69,s69,s59
ds_read_u16 v28,v28
v_lshl_add_u64 v[42:43],v[36:37],0,v[38:39]
v_lshlrev_b32_sdwa v30,1,v30 dst_sel:DWORD dst_unused:UNUSED_PAD src0_sel:DWORD src1_sel:WORD_1
v_mfma_f32_16x16x32_bf16 v[32:35],a[200:203],a[80:83],v[16:19]
buffer_load_dwordx4 v5,s[44:47],s69 offen offset:0 lds
TE12_%=:
s_cbranch_execz TP10730_%=
v_lshlrev_b32_sdwa v29,1,v29 dst_sel:DWORD dst_unused:UNUSED_PAD src0_sel:DWORD src1_sel:WORD_1
v_mfma_f32_16x16x32_bf16 v[32:35],a[196:199],a[64:67],v[32:35]
s_waitcnt lgkmcnt(8)
ds_read_u16_d16_hi v30,v30
v_lshl_add_u64 v[36:37],v[52:53],0,v[54:55]
v_mfma_f32_16x16x32_bf16 v[32:35],a[192:195],a[48:51],v[32:35]
s_add_u32 s69,s69,s53
ds_read_u16 v29,v29
v_lshlrev_b32_sdwa v31,1,v31 dst_sel:DWORD dst_unused:UNUSED_PAD src0_sel:DWORD src1_sel:WORD_1
v_mfma_f32_16x16x32_bf16 v[52:55],a[192:195],a[52:55],v[16:19]
s_waitcnt lgkmcnt(6)
buffer_load_dwordx4 v5,s[44:47],s69 offen offset:1024 lds
v_or_b32_e64 v38,v56,v58
v_lshlrev_b32_sdwa v24,1,v24 dst_sel:DWORD dst_unused:UNUSED_PAD src0_sel:DWORD src1_sel:WORD_1
v_mfma_f32_16x16x32_bf16 v[52:55],a[196:199],a[68:71],v[52:55]
s_add_u32 m0,s62,18480
ds_read_u16_d16_hi v31,v31
v_cvt_pk_bf16_f32 v39,v57,v59
v_lshlrev_b32_sdwa v26,1,v26 dst_sel:DWORD dst_unused:UNUSED_PAD src0_sel:DWORD src1_sel:WORD_1
v_mfma_f32_16x16x32_bf16 v[52:55],a[200:203],a[84:87],v[52:55]
s_add_u32 s68,s68,s53
ds_read_u16 v24,v24
v_lshlrev_b32_sdwa v25,1,v25 dst_sel:DWORD dst_unused:UNUSED_PAD src0_sel:DWORD src1_sel:WORD_1
v_mfma_f32_16x16x32_bf16 v[56:59],a[212:215],a[84:87],v[20:23]
buffer_load_dwordx4 v5,s[44:47],s68 offen offset:2048 lds
ds_read_u16_d16_hi v26,v26
v_mfma_f32_16x16x32_bf16 v[56:59],a[204:207],a[68:71],v[56:59]
s_add_u32 m0,s58,12320
ds_read_u16 v25,v25
v_lshlrev_b32_sdwa v27,1,v27 dst_sel:DWORD dst_unused:UNUSED_PAD src0_sel:DWORD src1_sel:WORD_1
v_mfma_f32_16x16x32_bf16 v[56:59],a[208:211],a[52:55],v[56:59]
s_add_u32 s69,s69,s53
ds_read_u16_d16_hi v27,v27
v_lshlrev_b32_sdwa v44,1,v44 dst_sel:DWORD dst_unused:UNUSED_PAD src0_sel:DWORD src1_sel:WORD_1
v_mfma_f32_16x16x32_bf16 v[136:139],a[224:227],v[40:43],v[136:139]
buffer_load_dwordx4 v5,s[44:47],s69 offen offset:2048 lds
v_lshlrev_b32_sdwa v46,1,v46 dst_sel:DWORD dst_unused:UNUSED_PAD src0_sel:DWORD src1_sel:WORD_1
v_mfma_f32_16x16x32_bf16 v[140:143],a[224:227],v[36:39],v[140:143]
s_add_u32 m0,s62,24640
ds_read_u16 v44,v44
v_lshlrev_b32_sdwa v45,1,v45 dst_sel:DWORD dst_unused:UNUSED_PAD src0_sel:DWORD src1_sel:WORD_1
v_mfma_f32_16x16x32_bf16 v[68:71],a[248:251],v[36:39],v[68:71]
s_add_u32 s68,s68,s59
ds_read_u16_d16_hi v46,v46
v_lshlrev_b32_sdwa v47,1,v47 dst_sel:DWORD dst_unused:UNUSED_PAD src0_sel:DWORD src1_sel:WORD_1
v_mfma_f32_16x16x32_bf16 v[64:67],a[248:251],v[40:43],v[64:67]
buffer_load_dwordx4 v5,s[44:47],s68 offen offset:0 lds
ds_read_u16 v45,v45
v_mfma_f32_16x16x32_bf16 v[144:147],a[228:231],v[40:43],v[144:147]
s_add_u32 s68,s68,s53
ds_read_u16_d16_hi v47,v47
v_lshlrev_b32_sdwa v48,1,v48 dst_sel:DWORD dst_unused:UNUSED_PAD src0_sel:DWORD src1_sel:WORD_1
v_mfma_f32_16x16x32_bf16 v[148:151],a[228:231],v[36:39],v[148:151]
buffer_load_dwordx4 v5,s[44:47],s68 offen offset:1024 lds
v_lshlrev_b32_sdwa v50,1,v50 dst_sel:DWORD dst_unused:UNUSED_PAD src0_sel:DWORD src1_sel:WORD_1
v_mfma_f32_16x16x32_bf16 v[84:87],a[236:239],v[36:39],v[84:87]
ds_read_u16 v48,v48
v_lshlrev_b32_sdwa v49,1,v49 dst_sel:DWORD dst_unused:UNUSED_PAD src0_sel:DWORD src1_sel:WORD_1
v_mfma_f32_16x16x32_bf16 v[80:83],a[236:239],v[40:43],v[80:83]
s_waitcnt lgkmcnt(6)
ds_read_u16_d16_hi v50,v50
v_lshlrev_b32_sdwa v51,1,v51 dst_sel:DWORD dst_unused:UNUSED_PAD src0_sel:DWORD src1_sel:WORD_1
v_mfma_f32_16x16x32_bf16 v[152:155],a[244:247],v[40:43],v[152:155]
ds_read_u16 v49,v49
v_mfma_f32_16x16x32_bf16 v[156:159],a[244:247],v[36:39],v[156:159]
s_add_u32 s68,s68,s53
ds_read_u16_d16_hi v51,v51
v_mfma_f32_16x16x32_bf16 v[100:103],a[252:255],v[36:39],v[100:103]
buffer_load_dwordx4 v5,s[44:47],s68 offen offset:2048 lds
v_mfma_f32_16x16x32_bf16 v[96:99],a[252:255],v[40:43],v[96:99]
ds_read_b64_tr_b16 a[216:217],v1 offset:20528
v_mfma_f32_16x16x32_bf16 v[36:39],a[208:211],a[56:59],v[20:23]
ds_read_b64_tr_b16 a[218:219],v1 offset:21552
v_lshlrev_b32_sdwa v60,1,v60 dst_sel:DWORD dst_unused:UNUSED_PAD src0_sel:DWORD src1_sel:WORD_1
v_mfma_f32_16x16x32_bf16 v[36:39],a[204:207],a[72:75],v[36:39]
ds_read_u16 v60,v60
v_lshl_add_u64 v[28:29],v[28:29],0,v[30:31]
v_fma_mixlo_f16 v8,v53,s99,0
v_mfma_f32_16x16x32_bf16 v[36:39],a[212:215],a[88:91],v[36:39]
s_waitcnt lgkmcnt(7)
v_lshl_add_u64 v[30:31],v[24:25],0,v[26:27]
v_lshlrev_b32_sdwa v62,1,v62 dst_sel:DWORD dst_unused:UNUSED_PAD src0_sel:DWORD src1_sel:WORD_1
v_mfma_f32_16x16x32_bf16 v[40:43],a[200:203],a[88:91],v[16:19]
ds_read_u16_d16_hi v62,v62
v_fma_mixhi_f16 v8,v55,s99,0
v_mfma_f32_16x16x32_bf16 v[40:43],a[196:199],a[72:75],v[40:43]
v_lshl_add_u64 v[24:25],v[44:45],0,v[46:47]
v_lshlrev_b32_sdwa v61,1,v61 dst_sel:DWORD dst_unused:UNUSED_PAD src0_sel:DWORD src1_sel:WORD_1
v_pk_fma_f16 v9,v8,v8,s54
v_mfma_f32_16x16x32_bf16 v[40:43],a[192:195],a[56:59],v[40:43]
ds_read_u16 v61,v61
v_lshlrev_b32_sdwa v63,1,v63 dst_sel:DWORD dst_unused:UNUSED_PAD src0_sel:DWORD src1_sel:WORD_1
v_mfma_f32_16x16x32_bf16 v[44:47],a[192:195],a[60:63],v[16:19]
ds_read_b128 v[16:19],v4 offset:1024
v_pk_mul_f16 v9,v8,v9
v_mfma_f32_16x16x32_bf16 v[44:47],a[196:199],a[76:79],v[44:47]
s_waitcnt lgkmcnt(6)
ds_read_u16_d16_hi v63,v63
ds_read_b64_tr_b16 a[192:193],v1 offset:22576
v_lshl_add_u64 v[26:27],v[48:49],0,v[50:51]
v_lshlrev_b32_sdwa v32,1,v32 dst_sel:DWORD dst_unused:UNUSED_PAD src0_sel:DWORD src1_sel:WORD_1
v_mfma_f32_16x16x32_bf16 v[44:47],a[200:203],a[92:95],v[44:47]
ds_read_b64_tr_b16 a[194:195],v1 offset:23600
v_exp_f16_e64 v8,v9
v_mfma_f32_16x16x32_bf16 v[48:51],a[212:215],a[92:95],v[20:23]
ds_read_b128 v[20:23],v4 offset:1056
ds_read_u16 v32,v32
v_lshlrev_b32_sdwa v34,1,v34 dst_sel:DWORD dst_unused:UNUSED_PAD src0_sel:DWORD src1_sel:WORD_1
v_mfma_f32_16x16x32_bf16 v[48:51],a[204:207],a[76:79],v[48:51]
ds_read_u16_d16_hi v34,v34
ds_read_b64_tr_b16 a[196:197],v2 offset:22576
v_lshlrev_b32_sdwa v33,1,v33 dst_sel:DWORD dst_unused:UNUSED_PAD src0_sel:DWORD src1_sel:WORD_1
v_mfma_f32_16x16x32_bf16 v[48:51],a[208:211],a[60:63],v[48:51]
ds_read_u16 v33,v33
v_exp_f16_sdwa v8,v9 dst_sel:WORD_1 dst_unused:UNUSED_PRESERVE src0_sel:WORD_1
v_mfma_f32_16x16x32_bf16 v[112:115],a[224:227],v[28:31],v[112:115]
ds_read_b64_tr_b16 a[198:199],v2 offset:23600
ds_read_b64_tr_b16 a[200:201],v2 offset:20528
v_lshlrev_b32_sdwa v35,1,v35 dst_sel:DWORD dst_unused:UNUSED_PAD src0_sel:DWORD src1_sel:WORD_1
v_mfma_f32_16x16x32_bf16 v[116:119],a[224:227],v[24:27],v[116:119]
ds_read_u16_d16_hi v35,v35
v_lshlrev_b32_sdwa v56,1,v56 dst_sel:DWORD dst_unused:UNUSED_PAD src0_sel:DWORD src1_sel:WORD_1
v_mfma_f32_16x16x32_bf16 v[76:79],a[248:251],v[24:27],v[76:79]
ds_read_b64_tr_b16 a[202:203],v2 offset:21552
v_pk_fma_f16 v8,v8,s55,s55
v_mfma_f32_16x16x32_bf16 v[72:75],a[248:251],v[28:31],v[72:75]
v_rcp_f16 v9,v8
v_lshlrev_b32_sdwa v58,1,v58 dst_sel:DWORD dst_unused:UNUSED_PAD src0_sel:DWORD src1_sel:WORD_1
v_mfma_f32_16x16x32_bf16 v[120:123],a[228:231],v[28:31],v[120:123]
ds_read_u16 v56,v56
v_lshlrev_b32_sdwa v57,1,v57 dst_sel:DWORD dst_unused:UNUSED_PAD src0_sel:DWORD src1_sel:WORD_1
v_mfma_f32_16x16x32_bf16 v[124:127],a[228:231],v[24:27],v[124:127]
ds_read_u16_d16_hi v58,v58
v_lshlrev_b32_sdwa v59,1,v59 dst_sel:DWORD dst_unused:UNUSED_PAD src0_sel:DWORD src1_sel:WORD_1
v_mfma_f32_16x16x32_bf16 v[92:95],a[236:239],v[24:27],v[92:95]
ds_read_u16 v57,v57
v_lshlrev_b32_sdwa v52,1,v52 dst_sel:DWORD dst_unused:UNUSED_PAD src0_sel:DWORD src1_sel:WORD_1
v_mfma_f32_16x16x32_bf16 v[88:91],a[236:239],v[28:31],v[88:91]
ds_read_u16_d16_hi v59,v59
v_rcp_f16_sdwa v9,v8 dst_sel:WORD_1 dst_unused:UNUSED_PRESERVE src0_sel:WORD_1
v_mfma_f32_16x16x32_bf16 v[128:131],a[244:247],v[28:31],v[128:131]
ds_read_b64_tr_b16 a[212:213],v2 offset:18480
v_lshlrev_b32_sdwa v54,1,v54 dst_sel:DWORD dst_unused:UNUSED_PAD src0_sel:DWORD src1_sel:WORD_1
v_mfma_f32_16x16x32_bf16 v[132:135],a[244:247],v[24:27],v[132:135]
ds_read_b64_tr_b16 a[214:215],v2 offset:19504
v_fma_mix_f32 v53,v53,v9,0 op_sel_hi:[0,1,0]
v_mfma_f32_16x16x32_bf16 v[108:111],a[252:255],v[24:27],v[108:111]
ds_read_u16 v52,v52
v_fma_mix_f32 v55,v55,v9,0 op_sel:[0,1,0] op_sel_hi:[0,1,0]
v_mfma_f32_16x16x32_bf16 v[104:107],a[252:255],v[28:31],v[104:107]
s_waitcnt lgkmcnt(7)
ds_read_u16_d16_hi v54,v54
v_mfma_f32_16x16x32_bf16 v[24:27],a[220:223],a[0:3],v[16:19]
v_lshlrev_b32_sdwa v36,1,v36 dst_sel:DWORD dst_unused:UNUSED_PAD src0_sel:DWORD src1_sel:WORD_1
v_lshlrev_b32_sdwa v38,1,v38 dst_sel:DWORD dst_unused:UNUSED_PAD src0_sel:DWORD src1_sel:WORD_1
v_mfma_f32_16x16x32_bf16 v[24:27],a[216:219],a[16:19],v[24:27]
ds_read_u16 v36,v36
v_lshl_add_u64 v[60:61],v[60:61],0,v[62:63]
v_mfma_f32_16x16x32_bf16 v[24:27],a[192:195],a[32:35],v[24:27]
ds_read_u16_d16_hi v38,v38
v_lshl_add_u64 v[62:63],v[32:33],0,v[34:35]
v_mfma_f32_16x16x32_bf16 v[28:31],a[196:199],a[32:35],v[20:23]
v_lshlrev_b32_sdwa v37,1,v37 dst_sel:DWORD dst_unused:UNUSED_PAD src0_sel:DWORD src1_sel:WORD_1
v_lshlrev_b32_sdwa v39,1,v39 dst_sel:DWORD dst_unused:UNUSED_PAD src0_sel:DWORD src1_sel:WORD_1
v_mfma_f32_16x16x32_bf16 v[28:31],a[200:203],a[16:19],v[28:31]
s_waitcnt lgkmcnt(4)
ds_read_u16 v37,v37
v_lshl_add_u64 v[32:33],v[56:57],0,v[58:59]
v_mfma_f32_16x16x32_bf16 v[28:31],a[212:215],a[0:3],v[28:31]
ds_read_u16_d16_hi v39,v39
v_lshlrev_b32_sdwa v40,1,v40 dst_sel:DWORD dst_unused:UNUSED_PAD src0_sel:DWORD src1_sel:WORD_1
v_mfma_f32_16x16x32_bf16 v[56:59],a[212:215],a[4:7],v[20:23]
s_waitcnt lgkmcnt(4)
ds_read_u16 v40,v40
v_or_b32 v34,v52,v54
v_mfma_f32_16x16x32_bf16 v[56:59],a[200:203],a[20:23],v[56:59]
v_cvt_pk_bf16_f32 v35,v53,v55
v_lshlrev_b32_sdwa v42,1,v42 dst_sel:DWORD dst_unused:UNUSED_PAD src0_sel:DWORD src1_sel:WORD_1
v_mfma_f32_16x16x32_bf16 v[56:59],a[196:199],a[36:39],v[56:59]
ds_read_u16_d16_hi v42,v42
v_lshlrev_b32_sdwa v41,1,v41 dst_sel:DWORD dst_unused:UNUSED_PAD src0_sel:DWORD src1_sel:WORD_1
v_mfma_f32_16x16x32_bf16 v[52:55],a[192:195],a[36:39],v[16:19]
ds_read_u16 v41,v41
v_lshlrev_b32_sdwa v43,1,v43 dst_sel:DWORD dst_unused:UNUSED_PAD src0_sel:DWORD src1_sel:WORD_1
v_mfma_f32_16x16x32_bf16 v[52:55],a[216:219],a[20:23],v[52:55]
ds_read_u16_d16_hi v43,v43
v_lshlrev_b32_sdwa v48,1,v48 dst_sel:DWORD dst_unused:UNUSED_PAD src0_sel:DWORD src1_sel:WORD_1
v_mfma_f32_16x16x32_bf16 v[52:55],a[220:223],a[4:7],v[52:55]
ds_read_u16 v48,v48
v_lshlrev_b32_sdwa v50,1,v50 dst_sel:DWORD dst_unused:UNUSED_PAD src0_sel:DWORD src1_sel:WORD_1
v_mfma_f32_16x16x32_bf16 v[232:235],a[224:227],v[60:63],v[232:235]
ds_read_u16_d16_hi v50,v50
v_lshlrev_b32_sdwa v49,1,v49 dst_sel:DWORD dst_unused:UNUSED_PAD src0_sel:DWORD src1_sel:WORD_1
v_mfma_f32_16x16x32_bf16 v[236:239],a[224:227],v[32:35],v[236:239]
ds_read_u16 v49,v49
v_lshlrev_b32_sdwa v51,1,v51 dst_sel:DWORD dst_unused:UNUSED_PAD src0_sel:DWORD src1_sel:WORD_1
v_mfma_f32_16x16x32_bf16 v[164:167],a[248:251],v[32:35],v[164:167]
ds_read_u16_d16_hi v51,v51
ds_read_b64_tr_b16 a[232:233],v3 offset:0
v_mfma_f32_16x16x32_bf16 v[160:163],a[248:251],v[60:63],v[160:163]
ds_read_b64_tr_b16 a[234:235],v3 offset:1536
v_lshlrev_b32_sdwa v44,1,v44 dst_sel:DWORD dst_unused:UNUSED_PAD src0_sel:DWORD src1_sel:WORD_1
v_mfma_f32_16x16x32_bf16 v[240:243],a[228:231],v[60:63],v[240:243]
ds_read_u16 v44,v44
v_lshlrev_b32_sdwa v46,1,v46 dst_sel:DWORD dst_unused:UNUSED_PAD src0_sel:DWORD src1_sel:WORD_1
v_mfma_f32_16x16x32_bf16 v[244:247],a[228:231],v[32:35],v[244:247]
ds_read_u16_d16_hi v46,v46
v_lshlrev_b32_sdwa v45,1,v45 dst_sel:DWORD dst_unused:UNUSED_PAD src0_sel:DWORD src1_sel:WORD_1
v_mfma_f32_16x16x32_bf16 v[180:183],a[236:239],v[32:35],v[180:183]
ds_read_u16 v45,v45
v_lshlrev_b32_sdwa v47,1,v47 dst_sel:DWORD dst_unused:UNUSED_PAD src0_sel:DWORD src1_sel:WORD_1
v_mfma_f32_16x16x32_bf16 v[176:179],a[236:239],v[60:63],v[176:179]
ds_read_u16_d16_hi v47,v47
v_mfma_f32_16x16x32_bf16 v[248:251],a[244:247],v[60:63],v[248:251]
ds_read_b64_tr_b16 a[240:241],v3 offset:8
v_mfma_f32_16x16x32_bf16 v[252:255],a[244:247],v[32:35],v[252:255]
ds_read_b64_tr_b16 a[242:243],v3 offset:1544
v_mfma_f32_16x16x32_bf16 v[196:199],a[252:255],v[32:35],v[196:199]
v_mfma_f32_16x16x32_bf16 v[192:195],a[252:255],v[60:63],v[192:195]
v_mfma_f32_16x16x32_bf16 v[32:35],a[220:223],a[8:11],v[16:19]
ds_read_b64_tr_b16 a[204:205],v1 offset:0
v_lshlrev_b32_sdwa v24,1,v24 dst_sel:DWORD dst_unused:UNUSED_PAD src0_sel:DWORD src1_sel:WORD_1
v_mfma_f32_16x16x32_bf16 v[32:35],a[216:219],a[24:27],v[32:35]
s_waitcnt lgkmcnt(6)
ds_read_u16 v24,v24
v_lshl_add_u64 v[36:37],v[36:37],0,v[38:39]
v_fma_mixlo_f16 v8,v57,s99,0
v_mfma_f32_16x16x32_bf16 v[32:35],a[192:195],a[40:43],v[32:35]
ds_read_b64_tr_b16 a[206:207],v1 offset:1024
v_lshl_add_u64 v[38:39],v[40:41],0,v[42:43]
v_lshlrev_b32_sdwa v26,1,v26 dst_sel:DWORD dst_unused:UNUSED_PAD src0_sel:DWORD src1_sel:WORD_1
v_mfma_f32_16x16x32_bf16 v[60:63],a[196:199],a[40:43],v[20:23]
ds_read_u16_d16_hi v26,v26
v_fma_mixhi_f16 v8,v59,s99,0
v_mfma_f32_16x16x32_bf16 v[60:63],a[200:203],a[24:27],v[60:63]
ds_read_b64_tr_b16 a[208:209],v1 offset:2048
v_lshl_add_u64 v[40:41],v[48:49],0,v[50:51]
v_lshlrev_b32_sdwa v25,1,v25 dst_sel:DWORD dst_unused:UNUSED_PAD src0_sel:DWORD src1_sel:WORD_1
v_pk_fma_f16 v9,v8,v8,s54
v_mfma_f32_16x16x32_bf16 v[60:63],a[212:215],a[8:11],v[60:63]
ds_read_u16 v25,v25
v_lshlrev_b32_sdwa v27,1,v27 dst_sel:DWORD dst_unused:UNUSED_PAD src0_sel:DWORD src1_sel:WORD_1
v_mfma_f32_16x16x32_bf16 v[48:51],a[212:215],a[12:15],v[20:23]
ds_read_u16_d16_hi v27,v27
v_pk_mul_f16 v9,v8,v9
v_mfma_f32_16x16x32_bf16 v[48:51],a[200:203],a[28:31],v[48:51]
s_waitcnt lgkmcnt(7)
ds_read_b64_tr_b16 a[210:211],v1 offset:3072
v_lshl_add_u64 v[42:43],v[44:45],0,v[46:47]
v_lshlrev_b32_sdwa v28,1,v28 dst_sel:DWORD dst_unused:UNUSED_PAD src0_sel:DWORD src1_sel:WORD_1
v_mfma_f32_16x16x32_bf16 v[48:51],a[196:199],a[44:47],v[48:51]
v_exp_f16 v8,v9
v_mfma_f32_16x16x32_bf16 v[44:47],a[192:195],a[44:47],v[16:19]
ds_read_u16 v28,v28
v_lshlrev_b32_sdwa v30,1,v30 dst_sel:DWORD dst_unused:UNUSED_PAD src0_sel:DWORD src1_sel:WORD_1
v_mfma_f32_16x16x32_bf16 v[44:47],a[216:219],a[28:31],v[44:47]
ds_read_u16_d16_hi v30,v30
v_lshlrev_b32_sdwa v29,1,v29 dst_sel:DWORD dst_unused:UNUSED_PAD src0_sel:DWORD src1_sel:WORD_1
v_mfma_f32_16x16x32_bf16 v[44:47],a[220:223],a[12:15],v[44:47]
v_exp_f16_sdwa v8,v9 dst_sel:WORD_1 dst_unused:UNUSED_PRESERVE src0_sel:WORD_1
v_mfma_f32_16x16x32_bf16 v[208:211],a[224:227],v[36:39],v[208:211]
ds_read_u16 v29,v29
v_lshlrev_b32_sdwa v31,1,v31 dst_sel:DWORD dst_unused:UNUSED_PAD src0_sel:DWORD src1_sel:WORD_1
v_mfma_f32_16x16x32_bf16 v[212:215],a[224:227],v[40:43],v[212:215]
ds_read_u16_d16_hi v31,v31
v_lshlrev_b32_sdwa v52,1,v52 dst_sel:DWORD dst_unused:UNUSED_PAD src0_sel:DWORD src1_sel:WORD_1
v_mfma_f32_16x16x32_bf16 v[172:175],a[248:251],v[40:43],v[172:175]
ds_read_u16 v52,v52
v_pk_fma_f16 v8,v8,s55,s55
v_mfma_f32_16x16x32_bf16 v[168:171],a[248:251],v[36:39],v[168:171]
ds_read_b64_tr_b16 a[224:225],v3 offset:64
v_rcp_f16 v9,v8
v_lshlrev_b32_sdwa v54,1,v54 dst_sel:DWORD dst_unused:UNUSED_PAD src0_sel:DWORD src1_sel:WORD_1
v_mfma_f32_16x16x32_bf16 v[216:219],a[228:231],v[36:39],v[216:219]
ds_read_u16_d16_hi v54,v54
v_lshlrev_b32_sdwa v53,1,v53 dst_sel:DWORD dst_unused:UNUSED_PAD src0_sel:DWORD src1_sel:WORD_1
v_mfma_f32_16x16x32_bf16 v[220:223],a[228:231],v[40:43],v[220:223]
ds_read_u16 v53,v53
ds_read_b64_tr_b16 a[226:227],v3 offset:1600
v_mfma_f32_16x16x32_bf16 v[188:191],a[236:239],v[40:43],v[188:191]
ds_read_b64_tr_b16 a[248:249],v3 offset:72
v_lshlrev_b32_sdwa v55,1,v55 dst_sel:DWORD dst_unused:UNUSED_PAD src0_sel:DWORD src1_sel:WORD_1
v_mfma_f32_16x16x32_bf16 v[184:187],a[236:239],v[36:39],v[184:187]
ds_read_u16_d16_hi v55,v55
v_rcp_f16_sdwa v9,v8 dst_sel:WORD_1 dst_unused:UNUSED_PRESERVE src0_sel:WORD_1
v_mfma_f32_16x16x32_bf16 v[224:227],a[244:247],v[36:39],v[224:227]
ds_read_b64_tr_b16 a[250:251],v3 offset:1608
ds_read_b64_tr_b16 a[228:229],v3 offset:128
v_lshlrev_b32_sdwa v56,1,v56 dst_sel:DWORD dst_unused:UNUSED_PAD src0_sel:DWORD src1_sel:WORD_1
v_mfma_f32_16x16x32_bf16 v[228:231],a[244:247],v[40:43],v[228:231]
ds_read_u16 v56,v56
v_lshlrev_b32_sdwa v58,1,v58 dst_sel:DWORD dst_unused:UNUSED_PAD src0_sel:DWORD src1_sel:WORD_1
v_mfma_f32_16x16x32_bf16 v[204:207],a[252:255],v[40:43],v[204:207]
ds_read_u16_d16_hi v58,v58
v_fma_mix_f32 v57,v57,v9,0 op_sel_hi:[0,1,0]
v_mfma_f32_16x16x32_bf16 v[200:203],a[252:255],v[36:39],v[200:203]
ds_read_b64_tr_b16 a[230:231],v3 offset:1664
v_fma_mix_f32 v59,v59,v9,0 op_sel:[0,1,0] op_sel_hi:[0,1,0]
v_mbcnt_lo_u32_b32 v8,-1,0
N_qs20_%=:
s_add_u32 s64,s16,N_qs20_%=-N_pf_%=+0
v_and_b32 v8,3,v8
s_mov_b32 m0,12288
v_lshl_add_u32 v8,v8,7,s64
v_min_u32 v8,N_mainend_%=-N_pf_%=+2048,v8
global_load_lds_dword v8,s[14:15]
v_mfma_f32_16x16x32_bf16 v[40:43],a[220:223],a[48:51],v[16:19]
ds_read_b64_tr_b16 a[236:237],v3 offset:136
v_lshlrev_b32_sdwa v32,1,v32 dst_sel:DWORD dst_unused:UNUSED_PAD src0_sel:DWORD src1_sel:WORD_1
v_mfma_f32_16x16x32_bf16 v[40:43],a[216:219],a[64:67],v[40:43]
s_waitcnt lgkmcnt(10)
ds_read_b64_tr_b16 a[238:239],v3 offset:1672
v_lshl_add_u64 v[24:25],v[24:25],0,v[26:27]
v_mfma_f32_16x16x32_bf16 v[40:43],a[192:195],a[80:83],v[40:43]
ds_read_u16 v32,v32
v_lshl_add_u64 v[26:27],v[28:29],0,v[30:31]
v_lshlrev_b32_sdwa v34,1,v34 dst_sel:DWORD dst_unused:UNUSED_PAD src0_sel:DWORD src1_sel:WORD_1
v_mfma_f32_16x16x32_bf16 v[36:39],a[196:199],a[80:83],v[20:23]
ds_read_u16_d16_hi v34,v34
v_lshlrev_b32_sdwa v33,1,v33 dst_sel:DWORD dst_unused:UNUSED_PAD src0_sel:DWORD src1_sel:WORD_1
v_mfma_f32_16x16x32_bf16 v[36:39],a[200:203],a[64:67],v[36:39]
s_waitcnt lgkmcnt(9)
ds_read_u16 v33,v33
v_lshl_add_u64 v[28:29],v[52:53],0,v[54:55]
v_mfma_f32_16x16x32_bf16 v[36:39],a[212:215],a[48:51],v[36:39]
v_lshlrev_b32_sdwa v35,1,v35 dst_sel:DWORD dst_unused:UNUSED_PAD src0_sel:DWORD src1_sel:WORD_1
v_lshlrev_b32_sdwa v60,1,v60 dst_sel:DWORD dst_unused:UNUSED_PAD src0_sel:DWORD src1_sel:WORD_1
v_mfma_f32_16x16x32_bf16 v[52:55],a[212:215],a[52:55],v[20:23]
s_waitcnt lgkmcnt(6)
ds_read_u16_d16_hi v35,v35
v_or_b32 v30,v56,v58
v_mfma_f32_16x16x32_bf16 v[52:55],a[200:203],a[68:71],v[52:55]
ds_read_u16 v60,v60
v_cvt_pk_bf16_f32 v31,v57,v59
v_lshlrev_b32_sdwa v62,1,v62 dst_sel:DWORD dst_unused:UNUSED_PAD src0_sel:DWORD src1_sel:WORD_1
v_mfma_f32_16x16x32_bf16 v[52:55],a[196:199],a[84:87],v[52:55]
ds_read_u16_d16_hi v62,v62
v_lshlrev_b32_sdwa v61,1,v61 dst_sel:DWORD dst_unused:UNUSED_PAD src0_sel:DWORD src1_sel:WORD_1
v_mfma_f32_16x16x32_bf16 v[56:59],a[192:195],a[84:87],v[16:19]
ds_read_u16 v61,v61
v_lshlrev_b32_sdwa v63,1,v63 dst_sel:DWORD dst_unused:UNUSED_PAD src0_sel:DWORD src1_sel:WORD_1
v_mfma_f32_16x16x32_bf16 v[56:59],a[216:219],a[68:71],v[56:59]
ds_read_u16_d16_hi v63,v63
v_lshlrev_b32_sdwa v44,1,v44 dst_sel:DWORD dst_unused:UNUSED_PAD src0_sel:DWORD src1_sel:WORD_1
v_mfma_f32_16x16x32_bf16 v[56:59],a[220:223],a[52:55],v[56:59]
ds_read_u16 v44,v44
v_lshlrev_b32_sdwa v46,1,v46 dst_sel:DWORD dst_unused:UNUSED_PAD src0_sel:DWORD src1_sel:WORD_1
v_mfma_f32_16x16x32_bf16 v[136:139],a[232:235],v[24:27],v[136:139]
ds_read_u16_d16_hi v46,v46
v_lshlrev_b32_sdwa v45,1,v45 dst_sel:DWORD dst_unused:UNUSED_PAD src0_sel:DWORD src1_sel:WORD_1
v_mfma_f32_16x16x32_bf16 v[140:143],a[232:235],v[28:31],v[140:143]
ds_read_u16 v45,v45
v_lshlrev_b32_sdwa v47,1,v47 dst_sel:DWORD dst_unused:UNUSED_PAD src0_sel:DWORD src1_sel:WORD_1
v_mfma_f32_16x16x32_bf16 v[68:71],a[240:243],v[28:31],v[68:71]
ds_read_u16_d16_hi v47,v47
v_lshlrev_b32_sdwa v48,1,v48 dst_sel:DWORD dst_unused:UNUSED_PAD src0_sel:DWORD src1_sel:WORD_1
v_mfma_f32_16x16x32_bf16 v[64:67],a[240:243],v[24:27],v[64:67]
ds_read_u16 v48,v48
v_lshlrev_b32_sdwa v50,1,v50 dst_sel:DWORD dst_unused:UNUSED_PAD src0_sel:DWORD src1_sel:WORD_1
v_mfma_f32_16x16x32_bf16 v[144:147],a[224:227],v[24:27],v[144:147]
ds_read_u16_d16_hi v50,v50
v_lshlrev_b32_sdwa v49,1,v49 dst_sel:DWORD dst_unused:UNUSED_PAD src0_sel:DWORD src1_sel:WORD_1
v_mfma_f32_16x16x32_bf16 v[148:151],a[224:227],v[28:31],v[148:151]
ds_read_u16 v49,v49
v_lshlrev_b32_sdwa v51,1,v51 dst_sel:DWORD dst_unused:UNUSED_PAD src0_sel:DWORD src1_sel:WORD_1
v_mfma_f32_16x16x32_bf16 v[84:87],a[248:251],v[28:31],v[84:87]
ds_read_u16_d16_hi v51,v51
v_mfma_f32_16x16x32_bf16 v[80:83],a[248:251],v[24:27],v[80:83]
s_waitcnt lgkmcnt(8)
v_mfma_f32_16x16x32_bf16 v[152:155],a[228:231],v[24:27],v[152:155]
v_mfma_f32_16x16x32_bf16 v[156:159],a[228:231],v[28:31],v[156:159]
v_mfma_f32_16x16x32_bf16 v[100:103],a[236:239],v[28:31],v[100:103]
v_mfma_f32_16x16x32_bf16 v[96:99],a[236:239],v[24:27],v[96:99]
v_mfma_f32_16x16x32_bf16 v[28:31],a[220:223],a[56:59],v[16:19]
v_lshlrev_b32_sdwa v40,1,v40 dst_sel:DWORD dst_unused:UNUSED_PAD src0_sel:DWORD src1_sel:WORD_1
v_fma_mixlo_f16 v8,v53,s99,0
v_mfma_f32_16x16x32_bf16 v[28:31],a[216:219],a[72:75],v[28:31]
ds_read_u16 v40,v40
v_lshl_add_u64 v[32:33],v[32:33],0,v[34:35]
v_mfma_f32_16x16x32_bf16 v[28:31],a[192:195],a[88:91],v[28:31]
v_lshl_add_u64 v[34:35],v[60:61],0,v[62:63]
v_lshlrev_b32_sdwa v42,1,v42 dst_sel:DWORD dst_unused:UNUSED_PAD src0_sel:DWORD src1_sel:WORD_1
v_mfma_f32_16x16x32_bf16 v[24:27],a[196:199],a[88:91],v[20:23]
ds_read_u16_d16_hi v42,v42
v_fma_mixhi_f16 v8,v55,s99,0
v_mfma_f32_16x16x32_bf16 v[24:27],a[200:203],a[72:75],v[24:27]
s_waitcnt lgkmcnt(2)
v_lshl_add_u64 v[60:61],v[44:45],0,v[46:47]
v_lshlrev_b32_sdwa v41,1,v41 dst_sel:DWORD dst_unused:UNUSED_PAD src0_sel:DWORD src1_sel:WORD_1
v_pk_fma_f16 v9,v8,v8,s54
v_mfma_f32_16x16x32_bf16 v[24:27],a[212:215],a[56:59],v[24:27]
ds_read_u16 v41,v41
v_lshlrev_b32_sdwa v43,1,v43 dst_sel:DWORD dst_unused:UNUSED_PAD src0_sel:DWORD src1_sel:WORD_1
v_mfma_f32_16x16x32_bf16 v[44:47],a[212:215],a[60:63],v[20:23]
ds_read_b128 v[20:23],v4 offset:1152
v_pk_mul_f16 v9,v8,v9
v_mfma_f32_16x16x32_bf16 v[44:47],a[200:203],a[76:79],v[44:47]
ds_read_u16_d16_hi v43,v43
ds_read_b64_tr_b16 a[212:213],v1 offset:4096
v_lshl_add_u64 v[62:63],v[48:49],0,v[50:51]
v_lshlrev_b32_sdwa v36,1,v36 dst_sel:DWORD dst_unused:UNUSED_PAD src0_sel:DWORD src1_sel:WORD_1
v_mfma_f32_16x16x32_bf16 v[44:47],a[196:199],a[92:95],v[44:47]
ds_read_b64_tr_b16 a[214:215],v1 offset:5120
v_exp_f16 v8,v9
v_mfma_f32_16x16x32_bf16 v[48:51],a[192:195],a[92:95],v[16:19]
ds_read_b128 v[16:19],v4 offset:1184
ds_read_u16 v36,v36
v_lshlrev_b32_sdwa v38,1,v38 dst_sel:DWORD dst_unused:UNUSED_PAD src0_sel:DWORD src1_sel:WORD_1
v_mfma_f32_16x16x32_bf16 v[48:51],a[216:219],a[76:79],v[48:51]
ds_read_u16_d16_hi v38,v38
ds_read_b64_tr_b16 a[200:201],v2 offset:4096
v_lshlrev_b32_sdwa v37,1,v37 dst_sel:DWORD dst_unused:UNUSED_PAD src0_sel:DWORD src1_sel:WORD_1
v_mfma_f32_16x16x32_bf16 v[48:51],a[220:223],a[60:63],v[48:51]
ds_read_u16 v37,v37
v_exp_f16_sdwa v8,v9 dst_sel:WORD_1 dst_unused:UNUSED_PRESERVE src0_sel:WORD_1
v_mfma_f32_16x16x32_bf16 v[112:115],a[232:235],v[32:35],v[112:115]
ds_read_b64_tr_b16 a[202:203],v2 offset:5120
ds_read_b64_tr_b16 a[196:197],v2 offset:2048
v_lshlrev_b32_sdwa v39,1,v39 dst_sel:DWORD dst_unused:UNUSED_PAD src0_sel:DWORD src1_sel:WORD_1
v_mfma_f32_16x16x32_bf16 v[116:119],a[232:235],v[60:63],v[116:119]
ds_read_u16_d16_hi v39,v39
v_lshlrev_b32_sdwa v56,1,v56 dst_sel:DWORD dst_unused:UNUSED_PAD src0_sel:DWORD src1_sel:WORD_1
v_mfma_f32_16x16x32_bf16 v[76:79],a[240:243],v[60:63],v[76:79]
ds_read_b64_tr_b16 a[198:199],v2 offset:3072
v_pk_fma_f16 v8,v8,s55,s55
v_mfma_f32_16x16x32_bf16 v[72:75],a[240:243],v[32:35],v[72:75]
v_rcp_f16 v9,v8
v_lshlrev_b32_sdwa v58,1,v58 dst_sel:DWORD dst_unused:UNUSED_PAD src0_sel:DWORD src1_sel:WORD_1
v_mfma_f32_16x16x32_bf16 v[120:123],a[224:227],v[32:35],v[120:123]
ds_read_u16 v56,v56
v_lshlrev_b32_sdwa v57,1,v57 dst_sel:DWORD dst_unused:UNUSED_PAD src0_sel:DWORD src1_sel:WORD_1
v_mfma_f32_16x16x32_bf16 v[124:127],a[224:227],v[60:63],v[124:127]
ds_read_u16_d16_hi v58,v58
v_lshlrev_b32_sdwa v59,1,v59 dst_sel:DWORD dst_unused:UNUSED_PAD src0_sel:DWORD src1_sel:WORD_1
v_mfma_f32_16x16x32_bf16 v[92:95],a[248:251],v[60:63],v[92:95]
ds_read_u16 v57,v57
v_lshlrev_b32_sdwa v52,1,v52 dst_sel:DWORD dst_unused:UNUSED_PAD src0_sel:DWORD src1_sel:WORD_1
v_mfma_f32_16x16x32_bf16 v[88:91],a[248:251],v[32:35],v[88:91]
ds_read_u16_d16_hi v59,v59
v_rcp_f16_sdwa v9,v8 dst_sel:WORD_1 dst_unused:UNUSED_PRESERVE src0_sel:WORD_1
v_mfma_f32_16x16x32_bf16 v[128:131],a[228:231],v[32:35],v[128:131]
ds_read_b64_tr_b16 a[192:193],v2 offset:0
v_lshlrev_b32_sdwa v54,1,v54 dst_sel:DWORD dst_unused:UNUSED_PAD src0_sel:DWORD src1_sel:WORD_1
v_mfma_f32_16x16x32_bf16 v[132:135],a[228:231],v[60:63],v[132:135]
ds_read_b64_tr_b16 a[194:195],v2 offset:1024
v_fma_mix_f32 v53,v53,v9,0 op_sel_hi:[0,1,0]
v_mfma_f32_16x16x32_bf16 v[108:111],a[236:239],v[60:63],v[108:111]
ds_read_u16 v52,v52
v_fma_mix_f32 v55,v55,v9,0 op_sel:[0,1,0] op_sel_hi:[0,1,0]
v_mfma_f32_16x16x32_bf16 v[104:107],a[236:239],v[32:35],v[104:107]
s_waitcnt lgkmcnt(7)
ds_read_u16_d16_hi v54,v54
v_mfma_f32_16x16x32_bf16 v[60:63],a[204:207],a[0:3],v[20:23]
v_lshlrev_b32_sdwa v28,1,v28 dst_sel:DWORD dst_unused:UNUSED_PAD src0_sel:DWORD src1_sel:WORD_1
v_lshlrev_b32_sdwa v30,1,v30 dst_sel:DWORD dst_unused:UNUSED_PAD src0_sel:DWORD src1_sel:WORD_1
v_mfma_f32_16x16x32_bf16 v[60:63],a[208:211],a[16:19],v[60:63]
ds_read_u16 v28,v28
v_lshl_add_u64 v[40:41],v[40:41],0,v[42:43]
v_mfma_f32_16x16x32_bf16 v[60:63],a[212:215],a[32:35],v[60:63]
ds_read_u16_d16_hi v30,v30
v_lshl_add_u64 v[42:43],v[36:37],0,v[38:39]
v_mfma_f32_16x16x32_bf16 v[32:35],a[200:203],a[32:35],v[16:19]
v_lshlrev_b32_sdwa v29,1,v29 dst_sel:DWORD dst_unused:UNUSED_PAD src0_sel:DWORD src1_sel:WORD_1
v_lshlrev_b32_sdwa v31,1,v31 dst_sel:DWORD dst_unused:UNUSED_PAD src0_sel:DWORD src1_sel:WORD_1
v_mfma_f32_16x16x32_bf16 v[32:35],a[196:199],a[16:19],v[32:35]
s_waitcnt lgkmcnt(4)
ds_read_u16 v29,v29
v_lshl_add_u64 v[36:37],v[56:57],0,v[58:59]
v_mfma_f32_16x16x32_bf16 v[32:35],a[192:195],a[0:3],v[32:35]
ds_read_u16_d16_hi v31,v31
v_lshlrev_b32_sdwa v24,1,v24 dst_sel:DWORD dst_unused:UNUSED_PAD src0_sel:DWORD src1_sel:WORD_1
v_mfma_f32_16x16x32_bf16 v[56:59],a[192:195],a[4:7],v[16:19]
s_waitcnt lgkmcnt(4)
ds_read_u16 v24,v24
v_or_b32 v38,v52,v54
v_mfma_f32_16x16x32_bf16 v[56:59],a[196:199],a[20:23],v[56:59]
s_waitcnt vmcnt(4)
v_cvt_pk_bf16_f32 v39,v53,v55
v_lshlrev_b32_sdwa v26,1,v26 dst_sel:DWORD dst_unused:UNUSED_PAD src0_sel:DWORD src1_sel:WORD_1
v_mfma_f32_16x16x32_bf16 v[56:59],a[200:203],a[36:39],v[56:59]
s_barrier
ds_read_u16_d16_hi v26,v26
v_lshlrev_b32_sdwa v25,1,v25 dst_sel:DWORD dst_unused:UNUSED_PAD src0_sel:DWORD src1_sel:WORD_1
v_mfma_f32_16x16x32_bf16 v[52:55],a[212:215],a[36:39],v[20:23]
ds_read_u16 v25,v25
v_lshlrev_b32_sdwa v27,1,v27 dst_sel:DWORD dst_unused:UNUSED_PAD src0_sel:DWORD src1_sel:WORD_1
v_mfma_f32_16x16x32_bf16 v[52:55],a[208:211],a[20:23],v[52:55]
s_add_u32 m0,s58,18480
ds_read_u16_d16_hi v27,v27
v_lshlrev_b32_sdwa v48,1,v48 dst_sel:DWORD dst_unused:UNUSED_PAD src0_sel:DWORD src1_sel:WORD_1
v_mfma_f32_16x16x32_bf16 v[52:55],a[204:207],a[4:7],v[52:55]
s_mov_b32 s69,s88
ds_read_u16 v48,v48
v_lshlrev_b32_sdwa v50,1,v50 dst_sel:DWORD dst_unused:UNUSED_PAD src0_sel:DWORD src1_sel:WORD_1
v_mfma_f32_16x16x32_bf16 v[232:235],a[232:235],v[40:43],v[232:235]
buffer_load_dwordx4 v5,s[44:47],s69 offen offset:0 lds
v_lshlrev_b32_sdwa v49,1,v49 dst_sel:DWORD dst_unused:UNUSED_PAD src0_sel:DWORD src1_sel:WORD_1
v_mfma_f32_16x16x32_bf16 v[236:239],a[232:235],v[36:39],v[236:239]
ds_read_u16_d16_hi v50,v50
v_lshlrev_b32_sdwa v51,1,v51 dst_sel:DWORD dst_unused:UNUSED_PAD src0_sel:DWORD src1_sel:WORD_1
v_mfma_f32_16x16x32_bf16 v[164:167],a[240:243],v[36:39],v[164:167]
ds_read_u16 v49,v49
ds_read_u16_d16_hi v51,v51
v_mfma_f32_16x16x32_bf16 v[160:163],a[240:243],v[40:43],v[160:163]
ds_read_b64_tr_b16 a[244:245],v3 offset:6160
v_lshlrev_b32_sdwa v44,1,v44 dst_sel:DWORD dst_unused:UNUSED_PAD src0_sel:DWORD src1_sel:WORD_1
v_mfma_f32_16x16x32_bf16 v[240:243],a[224:227],v[40:43],v[240:243]
s_add_u32 s69,s69,s53
ds_read_u16 v44,v44
v_lshlrev_b32_sdwa v46,1,v46 dst_sel:DWORD dst_unused:UNUSED_PAD src0_sel:DWORD src1_sel:WORD_1
v_mfma_f32_16x16x32_bf16 v[244:247],a[224:227],v[36:39],v[244:247]
buffer_load_dwordx4 v5,s[44:47],s69 offen offset:1024 lds
ds_read_u16_d16_hi v46,v46
v_mfma_f32_16x16x32_bf16 v[180:183],a[248:251],v[36:39],v[180:183]
ds_read_b64_tr_b16 a[246:247],v3 offset:7696
v_lshlrev_b32_sdwa v45,1,v45 dst_sel:DWORD dst_unused:UNUSED_PAD src0_sel:DWORD src1_sel:WORD_1
v_mfma_f32_16x16x32_bf16 v[176:179],a[248:251],v[40:43],v[176:179]
ds_read_u16 v45,v45
v_lshlrev_b32_sdwa v47,1,v47 dst_sel:DWORD dst_unused:UNUSED_PAD src0_sel:DWORD src1_sel:WORD_1
v_mfma_f32_16x16x32_bf16 v[248:251],a[228:231],v[40:43],v[248:251]
ds_read_u16_d16_hi v47,v47
v_mfma_f32_16x16x32_bf16 v[252:255],a[228:231],v[36:39],v[252:255]
s_add_u32 s69,s69,s53
ds_read_b64_tr_b16 a[252:253],v3 offset:6168
v_mfma_f32_16x16x32_bf16 v[196:199],a[236:239],v[36:39],v[196:199]
buffer_load_dwordx4 v5,s[44:47],s69 offen offset:2048 lds
v_mfma_f32_16x16x32_bf16 v[192:195],a[236:239],v[40:43],v[192:195]
ds_read_b64_tr_b16 a[254:255],v3 offset:7704
v_add_u32 v11,88272,v1
v_add_u32 v12,88272,v2
v_mfma_f32_16x16x32_bf16 v[36:39],a[204:207],a[8:11],v[20:23]
ds_read_b64_tr_b16 a[96:97],v11 offset:0
TE13_%=:
s_cbranch_execz TR1_%=
ds_read_b64_tr_b16 a[98:99],v11 offset:1024
v_lshlrev_b32_sdwa v60,1,v60 dst_sel:DWORD dst_unused:UNUSED_PAD src0_sel:DWORD src1_sel:WORD_1
v_mfma_f32_16x16x32_bf16 v[36:39],a[208:211],a[24:27],v[36:39]
s_waitcnt lgkmcnt(8)
ds_read_b64_tr_b16 a[100:101],v11 offset:2048
ds_read_b64_tr_b16 a[102:103],v11 offset:3072
v_lshl_add_u64 v[28:29],v[28:29],0,v[30:31]
v_fma_mixlo_f16 v8,v57,s99,0
v_mfma_f32_16x16x32_bf16 v[36:39],a[212:215],a[40:43],v[36:39]
s_nop 0
ds_read_u16 v60,v60
ds_read_b64_tr_b16 a[104:105],v11 offset:4096
v_lshl_add_u64 v[30:31],v[24:25],0,v[26:27]
v_lshlrev_b32_sdwa v62,1,v62 dst_sel:DWORD dst_unused:UNUSED_PAD src0_sel:DWORD src1_sel:WORD_1
v_mfma_f32_16x16x32_bf16 v[40:43],a[200:203],a[40:43],v[16:19]
ds_read_u16_d16_hi v62,v62
ds_read_b64_tr_b16 a[106:107],v11 offset:5120
v_fma_mixhi_f16 v8,v59,s99,0
v_mfma_f32_16x16x32_bf16 v[40:43],a[196:199],a[24:27],v[40:43]
ds_read_b64_tr_b16 a[108:109],v12 offset:4096
v_lshl_add_u64 v[24:25],v[48:49],0,v[50:51]
v_lshlrev_b32_sdwa v61,1,v61 dst_sel:DWORD dst_unused:UNUSED_PAD src0_sel:DWORD src1_sel:WORD_1
v_pk_fma_f16 v9,v8,v8,s54
v_mfma_f32_16x16x32_bf16 v[40:43],a[192:195],a[8:11],v[40:43]
ds_read_u16 v61,v61
v_lshlrev_b32_sdwa v63,1,v63 dst_sel:DWORD dst_unused:UNUSED_PAD src0_sel:DWORD src1_sel:WORD_1
v_mfma_f32_16x16x32_bf16 v[48:51],a[192:195],a[12:15],v[16:19]
s_nop 0
ds_read_u16_d16_hi v63,v63
v_pk_mul_f16 v9,v8,v9
v_mfma_f32_16x16x32_bf16 v[48:51],a[196:199],a[28:31],v[48:51]
s_waitcnt lgkmcnt(12)
ds_read_b64_tr_b16 a[110:111],v12 offset:5120
ds_read_b64_tr_b16 a[112:113],v12 offset:2048
v_lshl_add_u64 v[26:27],v[44:45],0,v[46:47]
v_lshlrev_b32_sdwa v32,1,v32 dst_sel:DWORD dst_unused:UNUSED_PAD src0_sel:DWORD src1_sel:WORD_1
v_mfma_f32_16x16x32_bf16 v[48:51],a[200:203],a[44:47],v[48:51]
ds_read_u16 v32,v32
v_exp_f16 v8,v9
v_mfma_f32_16x16x32_bf16 v[44:47],a[212:215],a[44:47],v[20:23]
ds_read_b64_tr_b16 a[114:115],v12 offset:3072
v_lshlrev_b32_sdwa v34,1,v34 dst_sel:DWORD dst_unused:UNUSED_PAD src0_sel:DWORD src1_sel:WORD_1
v_mfma_f32_16x16x32_bf16 v[44:47],a[208:211],a[28:31],v[44:47]
ds_read_u16_d16_hi v34,v34
v_lshlrev_b32_sdwa v33,1,v33 dst_sel:DWORD dst_unused:UNUSED_PAD src0_sel:DWORD src1_sel:WORD_1
v_mfma_f32_16x16x32_bf16 v[44:47],a[204:207],a[12:15],v[44:47]
v_exp_f16_sdwa v8,v9 dst_sel:WORD_1 dst_unused:UNUSED_PRESERVE src0_sel:WORD_1
v_mfma_f32_16x16x32_bf16 v[208:211],a[232:235],v[28:31],v[208:211]
ds_read_u16 v33,v33
v_lshlrev_b32_sdwa v35,1,v35 dst_sel:DWORD dst_unused:UNUSED_PAD src0_sel:DWORD src1_sel:WORD_1
v_mfma_f32_16x16x32_bf16 v[212:215],a[232:235],v[24:27],v[212:215]
ds_read_u16_d16_hi v35,v35
ds_read_b64_tr_b16 a[232:233],v3 offset:6224
v_lshlrev_b32_sdwa v52,1,v52 dst_sel:DWORD dst_unused:UNUSED_PAD src0_sel:DWORD src1_sel:WORD_1
v_mfma_f32_16x16x32_bf16 v[172:175],a[240:243],v[24:27],v[172:175]
ds_read_u16 v52,v52
v_pk_fma_f16 v8,v8,s55,s55
v_mfma_f32_16x16x32_bf16 v[168:171],a[240:243],v[28:31],v[168:171]
ds_read_b64_tr_b16 a[234:235],v3 offset:7760
v_rcp_f16 v9,v8
v_lshlrev_b32_sdwa v54,1,v54 dst_sel:DWORD dst_unused:UNUSED_PAD src0_sel:DWORD src1_sel:WORD_1
v_mfma_f32_16x16x32_bf16 v[216:219],a[224:227],v[28:31],v[216:219]
ds_read_u16_d16_hi v54,v54
ds_read_b64_tr_b16 a[240:241],v3 offset:6232
v_mfma_f32_16x16x32_bf16 v[220:223],a[224:227],v[24:27],v[220:223]
ds_read_b64_tr_b16 a[242:243],v3 offset:7768
ds_read_b64_tr_b16 a[224:225],v3 offset:6288
v_lshlrev_b32_sdwa v53,1,v53 dst_sel:DWORD dst_unused:UNUSED_PAD src0_sel:DWORD src1_sel:WORD_1
v_mfma_f32_16x16x32_bf16 v[188:191],a[248:251],v[24:27],v[188:191]
ds_read_u16 v53,v53
ds_read_b64_tr_b16 a[226:227],v3 offset:7824
v_lshlrev_b32_sdwa v55,1,v55 dst_sel:DWORD dst_unused:UNUSED_PAD src0_sel:DWORD src1_sel:WORD_1
v_mfma_f32_16x16x32_bf16 v[184:187],a[248:251],v[28:31],v[184:187]
ds_read_u16_d16_hi v55,v55
v_rcp_f16_sdwa v9,v8 dst_sel:WORD_1 dst_unused:UNUSED_PRESERVE src0_sel:WORD_1
v_mfma_f32_16x16x32_bf16 v[224:227],a[228:231],v[28:31],v[224:227]
ds_read_b64_tr_b16 a[248:249],v3 offset:6296
ds_read_b64_tr_b16 a[250:251],v3 offset:7832
v_lshlrev_b32_sdwa v56,1,v56 dst_sel:DWORD dst_unused:UNUSED_PAD src0_sel:DWORD src1_sel:WORD_1
v_mfma_f32_16x16x32_bf16 v[228:231],a[228:231],v[24:27],v[228:231]
ds_read_u16 v56,v56
ds_read_b64_tr_b16 a[116:117],v12 offset:0
v_lshlrev_b32_sdwa v58,1,v58 dst_sel:DWORD dst_unused:UNUSED_PAD src0_sel:DWORD src1_sel:WORD_1
v_mfma_f32_16x16x32_bf16 v[204:207],a[236:239],v[24:27],v[204:207]
ds_read_u16_d16_hi v58,v58
v_fma_mix_f32 v57,v57,v9,0 op_sel_hi:[0,1,0]
v_mfma_f32_16x16x32_bf16 v[200:203],a[236:239],v[28:31],v[200:203]
ds_read_b64_tr_b16 a[118:119],v12 offset:1024
ds_read_b64_tr_b16 a[120:121],v12 offset:6160
v_fma_mix_f32 v59,v59,v9,0 op_sel:[0,1,0] op_sel_hi:[0,1,0]
v_mfma_f32_16x16x32_bf16 v[24:27],a[204:207],a[48:51],v[20:23]
ds_read_b64_tr_b16 a[122:123],v12 offset:7184
ds_read_b64_tr_b16 a[216:217],v1 offset:6160
v_lshlrev_b32_sdwa v36,1,v36 dst_sel:DWORD dst_unused:UNUSED_PAD src0_sel:DWORD src1_sel:WORD_1
v_mfma_f32_16x16x32_bf16 v[24:27],a[208:211],a[64:67],v[24:27]
s_waitcnt lgkmcnt(14)
ds_read_u16 v36,v36
ds_read_b64_tr_b16 a[218:219],v1 offset:7184
v_lshl_add_u64 v[60:61],v[60:61],0,v[62:63]
v_mfma_f32_16x16x32_bf16 v[24:27],a[212:215],a[80:83],v[24:27]
ds_read_b64_tr_b16 a[124:125],v12 offset:8208
v_lshl_add_u64 v[62:63],v[32:33],0,v[34:35]
v_lshlrev_b32_sdwa v38,1,v38 dst_sel:DWORD dst_unused:UNUSED_PAD src0_sel:DWORD src1_sel:WORD_1
v_mfma_f32_16x16x32_bf16 v[28:31],a[200:203],a[80:83],v[16:19]
ds_read_u16_d16_hi v38,v38
v_lshlrev_b32_sdwa v37,1,v37 dst_sel:DWORD dst_unused:UNUSED_PAD src0_sel:DWORD src1_sel:WORD_1
v_mfma_f32_16x16x32_bf16 v[28:31],a[196:199],a[64:67],v[28:31]
s_waitcnt lgkmcnt(13)
ds_read_u16 v37,v37
v_lshl_add_u64 v[32:33],v[52:53],0,v[54:55]
v_mfma_f32_16x16x32_bf16 v[28:31],a[192:195],a[48:51],v[28:31]
ds_read_b64_tr_b16 a[126:127],v12 offset:9232
v_lshlrev_b32_sdwa v39,1,v39 dst_sel:DWORD dst_unused:UNUSED_PAD src0_sel:DWORD src1_sel:WORD_1
v_mfma_f32_16x16x32_bf16 v[52:55],a[192:195],a[52:55],v[16:19]
s_waitcnt lgkmcnt(10)
ds_read_u16_d16_hi v39,v39
v_or_b32 v34,v56,v58
v_lshlrev_b32_sdwa v40,1,v40 dst_sel:DWORD dst_unused:UNUSED_PAD src0_sel:DWORD src1_sel:WORD_1
v_mfma_f32_16x16x32_bf16 v[52:55],a[196:199],a[68:71],v[52:55]
ds_read_u16 v40,v40
ds_read_b64_tr_b16 a[220:221],v1 offset:8208
v_cvt_pk_bf16_f32 v35,v57,v59
v_lshlrev_b32_sdwa v42,1,v42 dst_sel:DWORD dst_unused:UNUSED_PAD src0_sel:DWORD src1_sel:WORD_1
v_mfma_f32_16x16x32_bf16 v[52:55],a[200:203],a[84:87],v[52:55]
ds_read_u16_d16_hi v42,v42
v_lshlrev_b32_sdwa v41,1,v41 dst_sel:DWORD dst_unused:UNUSED_PAD src0_sel:DWORD src1_sel:WORD_1
v_mfma_f32_16x16x32_bf16 v[56:59],a[212:215],a[84:87],v[20:23]
ds_read_u16 v41,v41
v_lshlrev_b32_sdwa v43,1,v43 dst_sel:DWORD dst_unused:UNUSED_PAD src0_sel:DWORD src1_sel:WORD_1
v_mfma_f32_16x16x32_bf16 v[56:59],a[208:211],a[68:71],v[56:59]
ds_read_u16_d16_hi v43,v43
v_lshlrev_b32_sdwa v44,1,v44 dst_sel:DWORD dst_unused:UNUSED_PAD src0_sel:DWORD src1_sel:WORD_1
v_mfma_f32_16x16x32_bf16 v[56:59],a[204:207],a[52:55],v[56:59]
ds_read_u16 v44,v44
v_lshlrev_b32_sdwa v46,1,v46 dst_sel:DWORD dst_unused:UNUSED_PAD src0_sel:DWORD src1_sel:WORD_1
v_mfma_f32_16x16x32_bf16 v[136:139],a[244:247],v[60:63],v[136:139]
ds_read_u16_d16_hi v46,v46
v_lshlrev_b32_sdwa v45,1,v45 dst_sel:DWORD dst_unused:UNUSED_PAD src0_sel:DWORD src1_sel:WORD_1
v_mfma_f32_16x16x32_bf16 v[140:143],a[244:247],v[32:35],v[140:143]
ds_read_u16 v45,v45
v_lshlrev_b32_sdwa v47,1,v47 dst_sel:DWORD dst_unused:UNUSED_PAD src0_sel:DWORD src1_sel:WORD_1
v_mfma_f32_16x16x32_bf16 v[68:71],a[252:255],v[32:35],v[68:71]
ds_read_u16_d16_hi v47,v47
v_lshlrev_b32_sdwa v48,1,v48 dst_sel:DWORD dst_unused:UNUSED_PAD src0_sel:DWORD src1_sel:WORD_1
v_mfma_f32_16x16x32_bf16 v[64:67],a[252:255],v[60:63],v[64:67]
ds_read_u16 v48,v48
ds_read_b64_tr_b16 a[222:223],v1 offset:9232
v_mfma_f32_16x16x32_bf16 v[144:147],a[232:235],v[60:63],v[144:147]
ds_read_b64_tr_b16 a[128:129],v12 offset:10256
v_lshlrev_b32_sdwa v50,1,v50 dst_sel:DWORD dst_unused:UNUSED_PAD src0_sel:DWORD src1_sel:WORD_1
v_mfma_f32_16x16x32_bf16 v[148:151],a[232:235],v[32:35],v[148:151]
ds_read_u16_d16_hi v50,v50
v_lshlrev_b32_sdwa v49,1,v49 dst_sel:DWORD dst_unused:UNUSED_PAD src0_sel:DWORD src1_sel:WORD_1
v_mfma_f32_16x16x32_bf16 v[84:87],a[240:243],v[32:35],v[84:87]
ds_read_u16 v49,v49
v_lshlrev_b32_sdwa v51,1,v51 dst_sel:DWORD dst_unused:UNUSED_PAD src0_sel:DWORD src1_sel:WORD_1
v_mfma_f32_16x16x32_bf16 v[80:83],a[240:243],v[60:63],v[80:83]
ds_read_u16_d16_hi v51,v51
v_mfma_f32_16x16x32_bf16 v[152:155],a[224:227],v[60:63],v[152:155]
ds_read_b64_tr_b16 a[130:131],v12 offset:11280
v_mfma_f32_16x16x32_bf16 v[156:159],a[224:227],v[32:35],v[156:159]
ds_read_b64_tr_b16 a[132:133],v11 offset:10256
v_mfma_f32_16x16x32_bf16 v[100:103],a[248:251],v[32:35],v[100:103]
ds_read_b64_tr_b16 a[134:135],v11 offset:11280
v_mfma_f32_16x16x32_bf16 v[96:99],a[248:251],v[60:63],v[96:99]
ds_read_b64_tr_b16 a[136:137],v11 offset:8208
v_mfma_f32_16x16x32_bf16 v[32:35],a[204:207],a[56:59],v[20:23]
s_nop 0
ds_read_b64_tr_b16 a[138:139],v11 offset:9232
v_lshlrev_b32_sdwa v24,1,v24 dst_sel:DWORD dst_unused:UNUSED_PAD src0_sel:DWORD src1_sel:WORD_1
v_mfma_f32_16x16x32_bf16 v[32:35],a[208:211],a[72:75],v[32:35]
s_waitcnt lgkmcnt(8)
ds_read_u16 v24,v24
v_lshl_add_u64 v[36:37],v[36:37],0,v[38:39]
v_fma_mixlo_f16 v8,v53,s99,0
v_mfma_f32_16x16x32_bf16 v[32:35],a[212:215],a[88:91],v[32:35]
ds_read_b64_tr_b16 a[140:141],v11 offset:6160
v_lshl_add_u64 v[38:39],v[40:41],0,v[42:43]
v_lshlrev_b32_sdwa v26,1,v26 dst_sel:DWORD dst_unused:UNUSED_PAD src0_sel:DWORD src1_sel:WORD_1
v_mfma_f32_16x16x32_bf16 v[60:63],a[200:203],a[88:91],v[16:19]
ds_read_u16_d16_hi v26,v26
v_fma_mixhi_f16 v8,v55,s99,0
v_mfma_f32_16x16x32_bf16 v[60:63],a[196:199],a[72:75],v[60:63]
ds_read_b64_tr_b16 a[142:143],v11 offset:7184
v_lshl_add_u64 v[40:41],v[44:45],0,v[46:47]
v_lshlrev_b32_sdwa v25,1,v25 dst_sel:DWORD dst_unused:UNUSED_PAD src0_sel:DWORD src1_sel:WORD_1
v_pk_fma_f16 v9,v8,v8,s54
v_mfma_f32_16x16x32_bf16 v[60:63],a[192:195],a[56:59],v[60:63]
ds_read_u16 v25,v25
v_lshlrev_b32_sdwa v27,1,v27 dst_sel:DWORD dst_unused:UNUSED_PAD src0_sel:DWORD src1_sel:WORD_1
v_mfma_f32_16x16x32_bf16 v[44:47],a[192:195],a[60:63],v[16:19]
s_nop 0
ds_read_b128 v[16:19],v4 offset:1280
v_pk_mul_f16 v9,v8,v9
v_mfma_f32_16x16x32_bf16 v[44:47],a[196:199],a[76:79],v[44:47]
s_waitcnt lgkmcnt(8)
ds_read_u16_d16_hi v27,v27
ds_read_b64_tr_b16 a[192:193],v1 offset:10256
v_lshl_add_u64 v[42:43],v[48:49],0,v[50:51]
v_lshlrev_b32_sdwa v28,1,v28 dst_sel:DWORD dst_unused:UNUSED_PAD src0_sel:DWORD src1_sel:WORD_1
v_mfma_f32_16x16x32_bf16 v[44:47],a[200:203],a[92:95],v[44:47]
ds_read_b64_tr_b16 a[194:195],v1 offset:11280
v_exp_f16 v8,v9
v_mfma_f32_16x16x32_bf16 v[48:51],a[212:215],a[92:95],v[20:23]
ds_read_b128 v[20:23],v4 offset:1312
ds_read_u16 v28,v28
v_lshlrev_b32_sdwa v30,1,v30 dst_sel:DWORD dst_unused:UNUSED_PAD src0_sel:DWORD src1_sel:WORD_1
v_mfma_f32_16x16x32_bf16 v[48:51],a[208:211],a[76:79],v[48:51]
ds_read_u16_d16_hi v30,v30
ds_read_b64_tr_b16 a[196:197],v2 offset:10256
v_lshlrev_b32_sdwa v29,1,v29 dst_sel:DWORD dst_unused:UNUSED_PAD src0_sel:DWORD src1_sel:WORD_1
v_mfma_f32_16x16x32_bf16 v[48:51],a[204:207],a[60:63],v[48:51]
ds_read_u16 v29,v29
v_exp_f16_sdwa v8,v9 dst_sel:WORD_1 dst_unused:UNUSED_PRESERVE src0_sel:WORD_1
v_mfma_f32_16x16x32_bf16 v[112:115],a[244:247],v[36:39],v[112:115]
ds_read_b64_tr_b16 a[198:199],v2 offset:11280
ds_read_b64_tr_b16 a[200:201],v2 offset:8208
v_lshlrev_b32_sdwa v31,1,v31 dst_sel:DWORD dst_unused:UNUSED_PAD src0_sel:DWORD src1_sel:WORD_1
v_mfma_f32_16x16x32_bf16 v[116:119],a[244:247],v[40:43],v[116:119]
ds_read_u16_d16_hi v31,v31
v_lshlrev_b32_sdwa v56,1,v56 dst_sel:DWORD dst_unused:UNUSED_PAD src0_sel:DWORD src1_sel:WORD_1
v_mfma_f32_16x16x32_bf16 v[76:79],a[252:255],v[40:43],v[76:79]
ds_read_b64_tr_b16 a[202:203],v2 offset:9232
v_pk_fma_f16 v8,v8,s55,s55
v_mfma_f32_16x16x32_bf16 v[72:75],a[252:255],v[36:39],v[72:75]
ds_read_u16 v56,v56
v_rcp_f16 v9,v8
v_lshlrev_b32_sdwa v58,1,v58 dst_sel:DWORD dst_unused:UNUSED_PAD src0_sel:DWORD src1_sel:WORD_1
v_mfma_f32_16x16x32_bf16 v[120:123],a[232:235],v[36:39],v[120:123]
ds_read_u16_d16_hi v58,v58
ds_read_b64_tr_b16 a[212:213],v2 offset:6160
v_mfma_f32_16x16x32_bf16 v[124:127],a[232:235],v[40:43],v[124:127]
ds_read_b64_tr_b16 a[214:215],v2 offset:7184
v_lshlrev_b32_sdwa v57,1,v57 dst_sel:DWORD dst_unused:UNUSED_PAD src0_sel:DWORD src1_sel:WORD_1
v_mfma_f32_16x16x32_bf16 v[92:95],a[240:243],v[40:43],v[92:95]
ds_read_u16 v57,v57
v_lshlrev_b32_sdwa v59,1,v59 dst_sel:DWORD dst_unused:UNUSED_PAD src0_sel:DWORD src1_sel:WORD_1
v_mfma_f32_16x16x32_bf16 v[88:91],a[240:243],v[36:39],v[88:91]
v_rcp_f16_sdwa v9,v8 dst_sel:WORD_1 dst_unused:UNUSED_PRESERVE src0_sel:WORD_1
v_mfma_f32_16x16x32_bf16 v[128:131],a[224:227],v[36:39],v[128:131]
ds_read_u16_d16_hi v59,v59
v_lshlrev_b32_sdwa v52,1,v52 dst_sel:DWORD dst_unused:UNUSED_PAD src0_sel:DWORD src1_sel:WORD_1
v_mfma_f32_16x16x32_bf16 v[132:135],a[224:227],v[40:43],v[132:135]
ds_read_u16 v52,v52
v_lshlrev_b32_sdwa v54,1,v54 dst_sel:DWORD dst_unused:UNUSED_PAD src0_sel:DWORD src1_sel:WORD_1
v_mfma_f32_16x16x32_bf16 v[108:111],a[248:251],v[40:43],v[108:111]
ds_read_u16_d16_hi v54,v54
v_fma_mix_f32 v53,v53,v9,0 op_sel_hi:[0,1,0]
v_mfma_f32_16x16x32_bf16 v[104:107],a[248:251],v[36:39],v[104:107]
s_waitcnt lgkmcnt(8)
ds_read_b64_tr_b16 a[144:145],v11 offset:12320
v_fma_mix_f32 v55,v55,v9,0 op_sel:[0,1,0] op_sel_hi:[0,1,0]
v_mfma_f32_16x16x32_bf16 v[40:43],a[216:219],a[0:3],v[16:19]
ds_read_b64_tr_b16 a[146:147],v11 offset:13344
v_lshlrev_b32_sdwa v32,1,v32 dst_sel:DWORD dst_unused:UNUSED_PAD src0_sel:DWORD src1_sel:WORD_1
v_mfma_f32_16x16x32_bf16 v[40:43],a[220:223],a[16:19],v[40:43]
ds_read_u16 v32,v32
v_lshl_add_u64 v[24:25],v[24:25],0,v[26:27]
v_mfma_f32_16x16x32_bf16 v[40:43],a[192:195],a[32:35],v[40:43]
ds_read_b64_tr_b16 a[148:149],v11 offset:14368
v_lshl_add_u64 v[26:27],v[28:29],0,v[30:31]
v_lshlrev_b32_sdwa v34,1,v34 dst_sel:DWORD dst_unused:UNUSED_PAD src0_sel:DWORD src1_sel:WORD_1
v_mfma_f32_16x16x32_bf16 v[36:39],a[196:199],a[32:35],v[20:23]
ds_read_u16_d16_hi v34,v34
v_lshlrev_b32_sdwa v33,1,v33 dst_sel:DWORD dst_unused:UNUSED_PAD src0_sel:DWORD src1_sel:WORD_1
v_mfma_f32_16x16x32_bf16 v[36:39],a[200:203],a[16:19],v[36:39]
s_waitcnt lgkmcnt(7)
ds_read_u16 v33,v33
v_lshl_add_u64 v[28:29],v[56:57],0,v[58:59]
v_mfma_f32_16x16x32_bf16 v[36:39],a[212:215],a[0:3],v[36:39]
ds_read_b64_tr_b16 a[150:151],v11 offset:15392
v_lshlrev_b32_sdwa v35,1,v35 dst_sel:DWORD dst_unused:UNUSED_PAD src0_sel:DWORD src1_sel:WORD_1
v_mfma_f32_16x16x32_bf16 v[56:59],a[212:215],a[4:7],v[20:23]
s_waitcnt lgkmcnt(7)
ds_read_u16_d16_hi v35,v35
v_or_b32 v30,v52,v54
v_lshlrev_b32_sdwa v60,1,v60 dst_sel:DWORD dst_unused:UNUSED_PAD src0_sel:DWORD src1_sel:WORD_1
v_mfma_f32_16x16x32_bf16 v[56:59],a[200:203],a[20:23],v[56:59]
ds_read_u16 v60,v60
v_cvt_pk_bf16_f32 v31,v53,v55
v_lshlrev_b32_sdwa v62,1,v62 dst_sel:DWORD dst_unused:UNUSED_PAD src0_sel:DWORD src1_sel:WORD_1
v_mfma_f32_16x16x32_bf16 v[56:59],a[196:199],a[36:39],v[56:59]
ds_read_u16_d16_hi v62,v62
v_lshlrev_b32_sdwa v61,1,v61 dst_sel:DWORD dst_unused:UNUSED_PAD src0_sel:DWORD src1_sel:WORD_1
v_mfma_f32_16x16x32_bf16 v[52:55],a[192:195],a[36:39],v[16:19]
ds_read_u16 v61,v61
v_lshlrev_b32_sdwa v63,1,v63 dst_sel:DWORD dst_unused:UNUSED_PAD src0_sel:DWORD src1_sel:WORD_1
v_mfma_f32_16x16x32_bf16 v[52:55],a[220:223],a[20:23],v[52:55]
ds_read_u16_d16_hi v63,v63
v_lshlrev_b32_sdwa v48,1,v48 dst_sel:DWORD dst_unused:UNUSED_PAD src0_sel:DWORD src1_sel:WORD_1
v_mfma_f32_16x16x32_bf16 v[52:55],a[216:219],a[4:7],v[52:55]
ds_read_u16 v48,v48
v_lshlrev_b32_sdwa v50,1,v50 dst_sel:DWORD dst_unused:UNUSED_PAD src0_sel:DWORD src1_sel:WORD_1
v_mfma_f32_16x16x32_bf16 v[232:235],a[244:247],v[24:27],v[232:235]
ds_read_u16_d16_hi v50,v50
ds_read_b64_tr_b16 a[152:153],v11 offset:16416
v_mfma_f32_16x16x32_bf16 v[236:239],a[244:247],v[28:31],v[236:239]
ds_read_b64_tr_b16 a[154:155],v11 offset:17440
v_lshlrev_b32_sdwa v49,1,v49 dst_sel:DWORD dst_unused:UNUSED_PAD src0_sel:DWORD src1_sel:WORD_1
v_mfma_f32_16x16x32_bf16 v[164:167],a[252:255],v[28:31],v[164:167]
ds_read_u16 v49,v49
ds_read_b64_tr_b16 a[228:229],v3 offset:12320
v_mfma_f32_16x16x32_bf16 v[160:163],a[252:255],v[24:27],v[160:163]
ds_read_b64_tr_b16 a[230:231],v3 offset:13856
ds_read_b64_tr_b16 a[236:237],v3 offset:12328
v_mfma_f32_16x16x32_bf16 v[240:243],a[232:235],v[24:27],v[240:243]
ds_read_b64_tr_b16 a[238:239],v3 offset:13864
v_lshlrev_b32_sdwa v51,1,v51 dst_sel:DWORD dst_unused:UNUSED_PAD src0_sel:DWORD src1_sel:WORD_1
v_mfma_f32_16x16x32_bf16 v[244:247],a[232:235],v[28:31],v[244:247]
ds_read_u16_d16_hi v51,v51
v_lshlrev_b32_sdwa v44,1,v44 dst_sel:DWORD dst_unused:UNUSED_PAD src0_sel:DWORD src1_sel:WORD_1
v_mfma_f32_16x16x32_bf16 v[180:183],a[240:243],v[28:31],v[180:183]
ds_read_u16 v44,v44
v_lshlrev_b32_sdwa v46,1,v46 dst_sel:DWORD dst_unused:UNUSED_PAD src0_sel:DWORD src1_sel:WORD_1
v_mfma_f32_16x16x32_bf16 v[176:179],a[240:243],v[24:27],v[176:179]
ds_read_u16_d16_hi v46,v46
v_lshlrev_b32_sdwa v45,1,v45 dst_sel:DWORD dst_unused:UNUSED_PAD src0_sel:DWORD src1_sel:WORD_1
v_mfma_f32_16x16x32_bf16 v[248:251],a[224:227],v[24:27],v[248:251]
ds_read_u16 v45,v45
v_lshlrev_b32_sdwa v47,1,v47 dst_sel:DWORD dst_unused:UNUSED_PAD src0_sel:DWORD src1_sel:WORD_1
v_mfma_f32_16x16x32_bf16 v[252:255],a[224:227],v[28:31],v[252:255]
ds_read_u16_d16_hi v47,v47
v_mfma_f32_16x16x32_bf16 v[196:199],a[248:251],v[28:31],v[196:199]
ds_read_b64_tr_b16 a[156:157],v12 offset:16416
v_mfma_f32_16x16x32_bf16 v[192:195],a[248:251],v[24:27],v[192:195]
ds_read_b64_tr_b16 a[158:159],v12 offset:17440
v_mfma_f32_16x16x32_bf16 v[28:31],a[216:219],a[8:11],v[16:19]
ds_read_b64_tr_b16 a[160:161],v12 offset:14368
v_lshlrev_b32_sdwa v40,1,v40 dst_sel:DWORD dst_unused:UNUSED_PAD src0_sel:DWORD src1_sel:WORD_1
v_mfma_f32_16x16x32_bf16 v[28:31],a[220:223],a[24:27],v[28:31]
s_waitcnt lgkmcnt(8)
ds_read_u16 v40,v40
v_lshl_add_u64 v[32:33],v[32:33],0,v[34:35]
v_fma_mixlo_f16 v8,v57,s99,0
v_mfma_f32_16x16x32_bf16 v[28:31],a[192:195],a[40:43],v[28:31]
ds_read_b64_tr_b16 a[162:163],v12 offset:15392
v_lshl_add_u64 v[34:35],v[60:61],0,v[62:63]
v_lshlrev_b32_sdwa v42,1,v42 dst_sel:DWORD dst_unused:UNUSED_PAD src0_sel:DWORD src1_sel:WORD_1
v_mfma_f32_16x16x32_bf16 v[24:27],a[196:199],a[40:43],v[20:23]
ds_read_u16_d16_hi v42,v42
v_fma_mixhi_f16 v8,v59,s99,0
v_mfma_f32_16x16x32_bf16 v[24:27],a[200:203],a[24:27],v[24:27]
s_waitcnt lgkmcnt(8)
ds_read_b64_tr_b16 a[164:165],v12 offset:12320
v_lshl_add_u64 v[60:61],v[48:49],0,v[50:51]
v_lshlrev_b32_sdwa v41,1,v41 dst_sel:DWORD dst_unused:UNUSED_PAD src0_sel:DWORD src1_sel:WORD_1
v_pk_fma_f16 v9,v8,v8,s54
v_mfma_f32_16x16x32_bf16 v[24:27],a[212:215],a[8:11],v[24:27]
ds_read_u16 v41,v41
v_lshlrev_b32_sdwa v43,1,v43 dst_sel:DWORD dst_unused:UNUSED_PAD src0_sel:DWORD src1_sel:WORD_1
v_mfma_f32_16x16x32_bf16 v[48:51],a[212:215],a[12:15],v[20:23]
ds_read_u16_d16_hi v43,v43
v_pk_mul_f16 v9,v8,v9
v_mfma_f32_16x16x32_bf16 v[48:51],a[200:203],a[28:31],v[48:51]
s_waitcnt lgkmcnt(8)
ds_read_b64_tr_b16 a[166:167],v12 offset:13344
v_lshl_add_u64 v[62:63],v[44:45],0,v[46:47]
v_lshlrev_b32_sdwa v36,1,v36 dst_sel:DWORD dst_unused:UNUSED_PAD src0_sel:DWORD src1_sel:WORD_1
v_mfma_f32_16x16x32_bf16 v[48:51],a[196:199],a[44:47],v[48:51]
v_exp_f16 v8,v9
v_mfma_f32_16x16x32_bf16 v[44:47],a[192:195],a[44:47],v[16:19]
ds_read_u16 v36,v36
v_lshlrev_b32_sdwa v38,1,v38 dst_sel:DWORD dst_unused:UNUSED_PAD src0_sel:DWORD src1_sel:WORD_1
v_mfma_f32_16x16x32_bf16 v[44:47],a[220:223],a[28:31],v[44:47]
ds_read_u16_d16_hi v38,v38
v_lshlrev_b32_sdwa v37,1,v37 dst_sel:DWORD dst_unused:UNUSED_PAD src0_sel:DWORD src1_sel:WORD_1
v_mfma_f32_16x16x32_bf16 v[44:47],a[216:219],a[12:15],v[44:47]
v_exp_f16_sdwa v8,v9 dst_sel:WORD_1 dst_unused:UNUSED_PRESERVE src0_sel:WORD_1
v_mfma_f32_16x16x32_bf16 v[208:211],a[244:247],v[32:35],v[208:211]
ds_read_u16 v37,v37
v_lshlrev_b32_sdwa v39,1,v39 dst_sel:DWORD dst_unused:UNUSED_PAD src0_sel:DWORD src1_sel:WORD_1
v_mfma_f32_16x16x32_bf16 v[212:215],a[244:247],v[60:63],v[212:215]
ds_read_u16_d16_hi v39,v39
v_lshlrev_b32_sdwa v52,1,v52 dst_sel:DWORD dst_unused:UNUSED_PAD src0_sel:DWORD src1_sel:WORD_1
v_mfma_f32_16x16x32_bf16 v[172:175],a[252:255],v[60:63],v[172:175]
ds_read_u16 v52,v52
v_pk_fma_f16 v8,v8,s55,s55
v_mfma_f32_16x16x32_bf16 v[168:171],a[252:255],v[32:35],v[168:171]
ds_read_b64_tr_b16 a[244:245],v3 offset:12384
v_rcp_f16_e64 v9,v8
v_lshlrev_b32_sdwa v54,1,v54 dst_sel:DWORD dst_unused:UNUSED_PAD src0_sel:DWORD src1_sel:WORD_1
v_mfma_f32_16x16x32_bf16 v[216:219],a[232:235],v[32:35],v[216:219]
ds_read_u16_d16_hi v54,v54
ds_read_b64_tr_b16 a[246:247],v3 offset:13920
v_mfma_f32_16x16x32_bf16 v[220:223],a[232:235],v[60:63],v[220:223]
ds_read_b64_tr_b16 a[252:253],v3 offset:12392
ds_read_b64_tr_b16 a[254:255],v3 offset:13928
v_lshlrev_b32_sdwa v53,1,v53 dst_sel:DWORD dst_unused:UNUSED_PAD src0_sel:DWORD src1_sel:WORD_1
v_mfma_f32_16x16x32_bf16 v[188:191],a[240:243],v[60:63],v[188:191]
ds_read_u16 v53,v53
ds_read_b64_tr_b16 a[232:233],v3 offset:12448
v_lshlrev_b32_sdwa v55,1,v55 dst_sel:DWORD dst_unused:UNUSED_PAD src0_sel:DWORD src1_sel:WORD_1
v_mfma_f32_16x16x32_bf16 v[184:187],a[240:243],v[32:35],v[184:187]
ds_read_u16_d16_hi v55,v55
v_rcp_f16_sdwa v9,v8 dst_sel:WORD_1 dst_unused:UNUSED_PRESERVE src0_sel:WORD_1
v_mfma_f32_16x16x32_bf16 v[224:227],a[224:227],v[32:35],v[224:227]
ds_read_b64_tr_b16 a[234:235],v3 offset:13984
ds_read_b64_tr_b16 a[240:241],v3 offset:12456
v_lshlrev_b32_sdwa v56,1,v56 dst_sel:DWORD dst_unused:UNUSED_PAD src0_sel:DWORD src1_sel:WORD_1
v_mfma_f32_16x16x32_bf16 v[228:231],a[224:227],v[60:63],v[228:231]
ds_read_u16 v56,v56
v_lshlrev_b32_sdwa v58,1,v58 dst_sel:DWORD dst_unused:UNUSED_PAD src0_sel:DWORD src1_sel:WORD_1
v_mfma_f32_16x16x32_bf16 v[204:207],a[248:251],v[60:63],v[204:207]
ds_read_u16_d16_hi v58,v58
v_fma_mix_f32 v57,v57,v9,0 op_sel_hi:[0,1,0]
v_mfma_f32_16x16x32_bf16 v[200:203],a[248:251],v[32:35],v[200:203]
ds_read_b64_tr_b16 a[242:243],v3 offset:13992
ds_read_b64_tr_b16 a[168:169],v12 offset:18480
v_fma_mix_f32 v59,v59,v9,0 op_sel:[0,1,0] op_sel_hi:[0,1,0]
v_mfma_f32_16x16x32_bf16 v[60:63],a[216:219],a[48:51],v[16:19]
ds_read_b64_tr_b16 a[170:171],v12 offset:19504
ds_read_b64_tr_b16 a[172:173],v12 offset:20528
v_lshlrev_b32_sdwa v28,1,v28 dst_sel:DWORD dst_unused:UNUSED_PAD src0_sel:DWORD src1_sel:WORD_1
v_mfma_f32_16x16x32_bf16 v[60:63],a[220:223],a[64:67],v[60:63]
s_waitcnt lgkmcnt(13)
ds_read_u16 v28,v28
ds_read_b64_tr_b16 a[174:175],v12 offset:21552
v_lshl_add_u64 v[40:41],v[40:41],0,v[42:43]
v_mfma_f32_16x16x32_bf16 v[60:63],a[192:195],a[80:83],v[60:63]
ds_read_b64_tr_b16 a[208:209],v1 offset:12320
v_lshl_add_u64 v[42:43],v[36:37],0,v[38:39]
v_lshlrev_b32_sdwa v30,1,v30 dst_sel:DWORD dst_unused:UNUSED_PAD src0_sel:DWORD src1_sel:WORD_1
v_mfma_f32_16x16x32_bf16 v[32:35],a[196:199],a[80:83],v[20:23]
ds_read_u16_d16_hi v30,v30
v_lshlrev_b32_sdwa v29,1,v29 dst_sel:DWORD dst_unused:UNUSED_PAD src0_sel:DWORD src1_sel:WORD_1
v_mfma_f32_16x16x32_bf16 v[32:35],a[200:203],a[64:67],v[32:35]
s_waitcnt lgkmcnt(12)
ds_read_u16 v29,v29
v_lshl_add_u64 v[36:37],v[52:53],0,v[54:55]
v_mfma_f32_16x16x32_bf16 v[32:35],a[212:215],a[48:51],v[32:35]
ds_read_b64_tr_b16 a[210:211],v1 offset:13344
v_lshlrev_b32_sdwa v31,1,v31 dst_sel:DWORD dst_unused:UNUSED_PAD src0_sel:DWORD src1_sel:WORD_1
v_mfma_f32_16x16x32_bf16 v[52:55],a[212:215],a[52:55],v[20:23]
s_waitcnt lgkmcnt(10)
ds_read_u16_d16_hi v31,v31
v_or_b32 v38,v56,v58
v_lshlrev_b32_sdwa v24,1,v24 dst_sel:DWORD dst_unused:UNUSED_PAD src0_sel:DWORD src1_sel:WORD_1
v_mfma_f32_16x16x32_bf16 v[52:55],a[200:203],a[68:71],v[52:55]
ds_read_u16 v24,v24
ds_read_b64_tr_b16 a[204:205],v1 offset:14368
v_cvt_pk_bf16_f32 v39,v57,v59
v_lshlrev_b32_sdwa v26,1,v26 dst_sel:DWORD dst_unused:UNUSED_PAD src0_sel:DWORD src1_sel:WORD_1
v_mfma_f32_16x16x32_bf16 v[52:55],a[196:199],a[84:87],v[52:55]
ds_read_u16_d16_hi v26,v26
v_lshlrev_b32_sdwa v25,1,v25 dst_sel:DWORD dst_unused:UNUSED_PAD src0_sel:DWORD src1_sel:WORD_1
v_mfma_f32_16x16x32_bf16 v[56:59],a[192:195],a[84:87],v[16:19]
ds_read_u16 v25,v25
v_lshlrev_b32_sdwa v27,1,v27 dst_sel:DWORD dst_unused:UNUSED_PAD src0_sel:DWORD src1_sel:WORD_1
v_mfma_f32_16x16x32_bf16 v[56:59],a[220:223],a[68:71],v[56:59]
ds_read_u16_d16_hi v27,v27
v_lshlrev_b32_sdwa v44,1,v44 dst_sel:DWORD dst_unused:UNUSED_PAD src0_sel:DWORD src1_sel:WORD_1
v_mfma_f32_16x16x32_bf16 v[56:59],a[216:219],a[52:55],v[56:59]
ds_read_u16 v44,v44
v_lshlrev_b32_sdwa v46,1,v46 dst_sel:DWORD dst_unused:UNUSED_PAD src0_sel:DWORD src1_sel:WORD_1
v_mfma_f32_16x16x32_bf16 v[136:139],a[228:231],v[40:43],v[136:139]
ds_read_u16_d16_hi v46,v46
v_lshlrev_b32_sdwa v45,1,v45 dst_sel:DWORD dst_unused:UNUSED_PAD src0_sel:DWORD src1_sel:WORD_1
v_mfma_f32_16x16x32_bf16 v[140:143],a[228:231],v[36:39],v[140:143]
ds_read_u16 v45,v45
v_lshlrev_b32_sdwa v47,1,v47 dst_sel:DWORD dst_unused:UNUSED_PAD src0_sel:DWORD src1_sel:WORD_1
v_mfma_f32_16x16x32_bf16 v[68:71],a[236:239],v[36:39],v[68:71]
ds_read_u16_d16_hi v47,v47
v_lshlrev_b32_sdwa v48,1,v48 dst_sel:DWORD dst_unused:UNUSED_PAD src0_sel:DWORD src1_sel:WORD_1
v_mfma_f32_16x16x32_bf16 v[64:67],a[236:239],v[40:43],v[64:67]
ds_read_u16 v48,v48
ds_read_b64_tr_b16 a[206:207],v1 offset:15392
v_mfma_f32_16x16x32_bf16 v[144:147],a[244:247],v[40:43],v[144:147]
ds_read_b64_tr_b16 a[176:177],v12 offset:22576
v_lshlrev_b32_sdwa v50,1,v50 dst_sel:DWORD dst_unused:UNUSED_PAD src0_sel:DWORD src1_sel:WORD_1
v_mfma_f32_16x16x32_bf16 v[148:151],a[244:247],v[36:39],v[148:151]
ds_read_u16_d16_hi v50,v50
v_lshlrev_b32_sdwa v49,1,v49 dst_sel:DWORD dst_unused:UNUSED_PAD src0_sel:DWORD src1_sel:WORD_1
v_mfma_f32_16x16x32_bf16 v[84:87],a[252:255],v[36:39],v[84:87]
ds_read_u16 v49,v49
v_lshlrev_b32_sdwa v51,1,v51 dst_sel:DWORD dst_unused:UNUSED_PAD src0_sel:DWORD src1_sel:WORD_1
v_mfma_f32_16x16x32_bf16 v[80:83],a[252:255],v[40:43],v[80:83]
ds_read_u16_d16_hi v51,v51
v_mfma_f32_16x16x32_bf16 v[152:155],a[232:235],v[40:43],v[152:155]
ds_read_b64_tr_b16 a[178:179],v12 offset:23600
v_mfma_f32_16x16x32_bf16 v[156:159],a[232:235],v[36:39],v[156:159]
s_waitcnt lgkmcnt(9)
ds_read_b64_tr_b16 a[180:181],v11 offset:22576
v_mfma_f32_16x16x32_bf16 v[100:103],a[240:243],v[36:39],v[100:103]
ds_read_b64_tr_b16 a[182:183],v11 offset:23600
v_mfma_f32_16x16x32_bf16 v[96:99],a[240:243],v[40:43],v[96:99]
ds_read_b64_tr_b16 a[184:185],v11 offset:20528
v_mfma_f32_16x16x32_bf16 v[36:39],a[216:219],a[56:59],v[16:19]
ds_read_b64_tr_b16 a[186:187],v11 offset:21552
v_lshlrev_b32_sdwa v60,1,v60 dst_sel:DWORD dst_unused:UNUSED_PAD src0_sel:DWORD src1_sel:WORD_1
v_mfma_f32_16x16x32_bf16 v[36:39],a[220:223],a[72:75],v[36:39]
ds_read_u16 v60,v60
v_lshl_add_u64 v[28:29],v[28:29],0,v[30:31]
v_fma_mixlo_f16 v8,v53,s99,0
v_mfma_f32_16x16x32_bf16 v[36:39],a[192:195],a[88:91],v[36:39]
ds_read_b64_tr_b16 a[188:189],v11 offset:18480
v_lshl_add_u64 v[30:31],v[24:25],0,v[26:27]
v_lshlrev_b32_sdwa v62,1,v62 dst_sel:DWORD dst_unused:UNUSED_PAD src0_sel:DWORD src1_sel:WORD_1
v_mfma_f32_16x16x32_bf16 v[40:43],a[196:199],a[88:91],v[20:23]
ds_read_u16_d16_hi v62,v62
v_fma_mixhi_f16 v8,v55,s99,0
v_mfma_f32_16x16x32_bf16 v[40:43],a[200:203],a[72:75],v[40:43]
s_waitcnt lgkmcnt(8)
ds_read_b64_tr_b16 a[190:191],v11 offset:19504
v_lshl_add_u64 v[24:25],v[44:45],0,v[46:47]
v_lshlrev_b32_sdwa v61,1,v61 dst_sel:DWORD dst_unused:UNUSED_PAD src0_sel:DWORD src1_sel:WORD_1
v_pk_fma_f16 v9,v8,v8,s54
v_mfma_f32_16x16x32_bf16 v[40:43],a[212:215],a[56:59],v[40:43]
ds_read_u16 v61,v61
v_lshlrev_b32_sdwa v63,1,v63 dst_sel:DWORD dst_unused:UNUSED_PAD src0_sel:DWORD src1_sel:WORD_1
v_mfma_f32_16x16x32_bf16 v[44:47],a[212:215],a[60:63],v[20:23]
ds_read_b128 v[20:23],v4 offset:1408
v_pk_mul_f16 v9,v8,v9
v_mfma_f32_16x16x32_bf16 v[44:47],a[200:203],a[76:79],v[44:47]
ds_read_u16_d16_hi v63,v63
ds_read_b64_tr_b16 a[212:213],v1 offset:16416
v_lshl_add_u64 v[26:27],v[48:49],0,v[50:51]
v_lshlrev_b32_sdwa v32,1,v32 dst_sel:DWORD dst_unused:UNUSED_PAD src0_sel:DWORD src1_sel:WORD_1
v_mfma_f32_16x16x32_bf16 v[44:47],a[196:199],a[92:95],v[44:47]
ds_read_b64_tr_b16 a[214:215],v1 offset:17440
v_exp_f16_e64 v8,v9
v_mfma_f32_16x16x32_bf16 v[48:51],a[192:195],a[92:95],v[16:19]
ds_read_b128 v[16:19],v4 offset:1440
ds_read_u16 v32,v32
v_lshlrev_b32_sdwa v34,1,v34 dst_sel:DWORD dst_unused:UNUSED_PAD src0_sel:DWORD src1_sel:WORD_1
v_mfma_f32_16x16x32_bf16 v[48:51],a[220:223],a[76:79],v[48:51]
ds_read_u16_d16_hi v34,v34
ds_read_b64_tr_b16 a[200:201],v2 offset:16416
v_lshlrev_b32_sdwa v33,1,v33 dst_sel:DWORD dst_unused:UNUSED_PAD src0_sel:DWORD src1_sel:WORD_1
v_mfma_f32_16x16x32_bf16 v[48:51],a[216:219],a[60:63],v[48:51]
TP9685_%=:
s_cbranch_execz TR1_%=
ds_read_u16 v33,v33
v_exp_f16_sdwa v8,v9 dst_sel:WORD_1 dst_unused:UNUSED_PRESERVE src0_sel:WORD_1
v_mfma_f32_16x16x32_bf16 v[112:115],a[228:231],v[28:31],v[112:115]
ds_read_b64_tr_b16 a[202:203],v2 offset:17440
ds_read_b64_tr_b16 a[196:197],v2 offset:14368
v_lshlrev_b32_sdwa v35,1,v35 dst_sel:DWORD dst_unused:UNUSED_PAD src0_sel:DWORD src1_sel:WORD_1
v_mfma_f32_16x16x32_bf16 v[116:119],a[228:231],v[24:27],v[116:119]
ds_read_u16_d16_hi v35,v35
v_lshlrev_b32_sdwa v56,1,v56 dst_sel:DWORD dst_unused:UNUSED_PAD src0_sel:DWORD src1_sel:WORD_1
v_mfma_f32_16x16x32_bf16 v[76:79],a[236:239],v[24:27],v[76:79]
ds_read_b64_tr_b16 a[198:199],v2 offset:15392
v_pk_fma_f16 v8,v8,s55,s55
v_mfma_f32_16x16x32_bf16 v[72:75],a[236:239],v[28:31],v[72:75]
v_rcp_f16 v9,v8
v_lshlrev_b32_sdwa v58,1,v58 dst_sel:DWORD dst_unused:UNUSED_PAD src0_sel:DWORD src1_sel:WORD_1
v_mfma_f32_16x16x32_bf16 v[120:123],a[244:247],v[28:31],v[120:123]
ds_read_u16 v56,v56
v_lshlrev_b32_sdwa v57,1,v57 dst_sel:DWORD dst_unused:UNUSED_PAD src0_sel:DWORD src1_sel:WORD_1
v_mfma_f32_16x16x32_bf16 v[124:127],a[244:247],v[24:27],v[124:127]
ds_read_u16_d16_hi v58,v58
v_lshlrev_b32_sdwa v59,1,v59 dst_sel:DWORD dst_unused:UNUSED_PAD src0_sel:DWORD src1_sel:WORD_1
v_mfma_f32_16x16x32_bf16 v[92:95],a[252:255],v[24:27],v[92:95]
ds_read_u16 v57,v57
v_lshlrev_b32_sdwa v52,1,v52 dst_sel:DWORD dst_unused:UNUSED_PAD src0_sel:DWORD src1_sel:WORD_1
v_mfma_f32_16x16x32_bf16 v[88:91],a[252:255],v[28:31],v[88:91]
ds_read_u16_d16_hi v59,v59
v_rcp_f16_sdwa v9,v8 dst_sel:WORD_1 dst_unused:UNUSED_PRESERVE src0_sel:WORD_1
v_mfma_f32_16x16x32_bf16 v[128:131],a[232:235],v[28:31],v[128:131]
ds_read_b64_tr_b16 a[192:193],v2 offset:12320
v_lshlrev_b32_sdwa v54,1,v54 dst_sel:DWORD dst_unused:UNUSED_PAD src0_sel:DWORD src1_sel:WORD_1
v_mfma_f32_16x16x32_bf16 v[132:135],a[232:235],v[24:27],v[132:135]
ds_read_b64_tr_b16 a[194:195],v2 offset:13344
v_fma_mix_f32 v53,v53,v9,0 op_sel_hi:[0,1,0]
v_mfma_f32_16x16x32_bf16 v[108:111],a[240:243],v[24:27],v[108:111]
ds_read_u16 v52,v52
v_fma_mix_f32 v55,v55,v9,0 op_sel:[0,1,0] op_sel_hi:[0,1,0]
v_mfma_f32_16x16x32_bf16 v[104:107],a[240:243],v[28:31],v[104:107]
s_waitcnt lgkmcnt(7)
ds_read_u16_d16_hi v54,v54
v_mfma_f32_16x16x32_bf16 v[24:27],a[208:211],a[0:3],v[20:23]
v_lshlrev_b32_sdwa v36,1,v36 dst_sel:DWORD dst_unused:UNUSED_PAD src0_sel:DWORD src1_sel:WORD_1
v_lshlrev_b32_sdwa v38,1,v38 dst_sel:DWORD dst_unused:UNUSED_PAD src0_sel:DWORD src1_sel:WORD_1
v_mfma_f32_16x16x32_bf16 v[24:27],a[204:207],a[16:19],v[24:27]
ds_read_u16 v36,v36
v_lshl_add_u64 v[60:61],v[60:61],0,v[62:63]
v_mfma_f32_16x16x32_bf16 v[24:27],a[212:215],a[32:35],v[24:27]
ds_read_u16_d16_hi v38,v38
v_lshl_add_u64 v[62:63],v[32:33],0,v[34:35]
v_mfma_f32_16x16x32_bf16 v[28:31],a[200:203],a[32:35],v[16:19]
v_lshlrev_b32_sdwa v37,1,v37 dst_sel:DWORD dst_unused:UNUSED_PAD src0_sel:DWORD src1_sel:WORD_1
v_lshlrev_b32_sdwa v39,1,v39 dst_sel:DWORD dst_unused:UNUSED_PAD src0_sel:DWORD src1_sel:WORD_1
v_mfma_f32_16x16x32_bf16 v[28:31],a[196:199],a[16:19],v[28:31]
s_waitcnt lgkmcnt(4)
ds_read_u16 v37,v37
v_lshl_add_u64 v[32:33],v[56:57],0,v[58:59]
v_mfma_f32_16x16x32_bf16 v[28:31],a[192:195],a[0:3],v[28:31]
ds_read_u16_d16_hi v39,v39
v_lshlrev_b32_sdwa v40,1,v40 dst_sel:DWORD dst_unused:UNUSED_PAD src0_sel:DWORD src1_sel:WORD_1
v_mfma_f32_16x16x32_bf16 v[56:59],a[192:195],a[4:7],v[16:19]
s_waitcnt lgkmcnt(4)
ds_read_u16 v40,v40
v_or_b32_e64 v34,v52,v54
v_mfma_f32_16x16x32_bf16 v[56:59],a[196:199],a[20:23],v[56:59]
s_waitcnt vmcnt(0)
v_cvt_pk_bf16_f32 v35,v53,v55
v_lshlrev_b32_sdwa v42,1,v42 dst_sel:DWORD dst_unused:UNUSED_PAD src0_sel:DWORD src1_sel:WORD_1
v_mfma_f32_16x16x32_bf16 v[56:59],a[200:203],a[36:39],v[56:59]
s_barrier
ds_read_u16_d16_hi v42,v42
v_lshlrev_b32_sdwa v41,1,v41 dst_sel:DWORD dst_unused:UNUSED_PAD src0_sel:DWORD src1_sel:WORD_1
v_mfma_f32_16x16x32_bf16 v[52:55],a[212:215],a[36:39],v[20:23]
ds_read_u16 v41,v41
v_lshlrev_b32_sdwa v43,1,v43 dst_sel:DWORD dst_unused:UNUSED_PAD src0_sel:DWORD src1_sel:WORD_1
v_mfma_f32_16x16x32_bf16 v[52:55],a[204:207],a[20:23],v[52:55]
ds_read_u16_d16_hi v43,v43
v_lshlrev_b32_sdwa v48,1,v48 dst_sel:DWORD dst_unused:UNUSED_PAD src0_sel:DWORD src1_sel:WORD_1
v_mfma_f32_16x16x32_bf16 v[52:55],a[208:211],a[4:7],v[52:55]
ds_read_u16 v48,v48
v_lshlrev_b32_sdwa v50,1,v50 dst_sel:DWORD dst_unused:UNUSED_PAD src0_sel:DWORD src1_sel:WORD_1
v_mfma_f32_16x16x32_bf16 v[232:235],a[228:231],v[60:63],v[232:235]
ds_read_u16_d16_hi v50,v50
v_lshlrev_b32_sdwa v49,1,v49 dst_sel:DWORD dst_unused:UNUSED_PAD src0_sel:DWORD src1_sel:WORD_1
v_mfma_f32_16x16x32_bf16 v[236:239],a[228:231],v[32:35],v[236:239]
ds_read_u16 v49,v49
v_lshlrev_b32_sdwa v51,1,v51 dst_sel:DWORD dst_unused:UNUSED_PAD src0_sel:DWORD src1_sel:WORD_1
v_mfma_f32_16x16x32_bf16 v[164:167],a[236:239],v[32:35],v[164:167]
ds_read_u16_d16_hi v51,v51
ds_read_b64_tr_b16 a[224:225],v3 offset:18480
v_mfma_f32_16x16x32_bf16 v[160:163],a[236:239],v[60:63],v[160:163]
ds_read_b64_tr_b16 a[226:227],v3 offset:20016
v_lshlrev_b32_sdwa v44,1,v44 dst_sel:DWORD dst_unused:UNUSED_PAD src0_sel:DWORD src1_sel:WORD_1
v_mfma_f32_16x16x32_bf16 v[240:243],a[244:247],v[60:63],v[240:243]
ds_read_u16 v44,v44
v_lshlrev_b32_sdwa v46,1,v46 dst_sel:DWORD dst_unused:UNUSED_PAD src0_sel:DWORD src1_sel:WORD_1
v_mfma_f32_16x16x32_bf16 v[244:247],a[244:247],v[32:35],v[244:247]
ds_read_u16_d16_hi v46,v46
v_lshlrev_b32_sdwa v45,1,v45 dst_sel:DWORD dst_unused:UNUSED_PAD src0_sel:DWORD src1_sel:WORD_1
v_mfma_f32_16x16x32_bf16 v[180:183],a[252:255],v[32:35],v[180:183]
ds_read_u16 v45,v45
v_lshlrev_b32_sdwa v47,1,v47 dst_sel:DWORD dst_unused:UNUSED_PAD src0_sel:DWORD src1_sel:WORD_1
v_mfma_f32_16x16x32_bf16 v[176:179],a[252:255],v[60:63],v[176:179]
ds_read_u16_d16_hi v47,v47
v_mfma_f32_16x16x32_bf16 v[248:251],a[232:235],v[60:63],v[248:251]
ds_read_b64_tr_b16 a[248:249],v3 offset:18488
v_mfma_f32_16x16x32_bf16 v[252:255],a[232:235],v[32:35],v[252:255]
ds_read_b64_tr_b16 a[250:251],v3 offset:20024
v_mfma_f32_16x16x32_bf16 v[196:199],a[240:243],v[32:35],v[196:199]
v_mfma_f32_16x16x32_bf16 v[192:195],a[240:243],v[60:63],v[192:195]
v_mfma_f32_16x16x32_bf16 v[32:35],a[208:211],a[8:11],v[20:23]
ds_read_b64_tr_b16 a[220:221],v1 offset:18480
v_lshlrev_b32_sdwa v24,1,v24 dst_sel:DWORD dst_unused:UNUSED_PAD src0_sel:DWORD src1_sel:WORD_1
v_mfma_f32_16x16x32_bf16 v[32:35],a[204:207],a[24:27],v[32:35]
s_waitcnt lgkmcnt(6)
ds_read_u16 v24,v24
v_lshl_add_u64 v[36:37],v[36:37],0,v[38:39]
v_fma_mixlo_f16 v8,v57,s99,0
v_mfma_f32_16x16x32_bf16 v[32:35],a[212:215],a[40:43],v[32:35]
ds_read_b64_tr_b16 a[222:223],v1 offset:19504
v_lshl_add_u64 v[38:39],v[40:41],0,v[42:43]
v_lshlrev_b32_sdwa v26,1,v26 dst_sel:DWORD dst_unused:UNUSED_PAD src0_sel:DWORD src1_sel:WORD_1
v_mfma_f32_16x16x32_bf16 v[60:63],a[200:203],a[40:43],v[16:19]
ds_read_u16_d16_hi v26,v26
v_fma_mixhi_f16 v8,v59,s99,0
v_mfma_f32_16x16x32_bf16 v[60:63],a[196:199],a[24:27],v[60:63]
ds_read_b64_tr_b16 a[216:217],v1 offset:20528
v_lshl_add_u64 v[40:41],v[48:49],0,v[50:51]
v_lshlrev_b32_sdwa v25,1,v25 dst_sel:DWORD dst_unused:UNUSED_PAD src0_sel:DWORD src1_sel:WORD_1
v_pk_fma_f16 v9,v8,v8,s54
v_mfma_f32_16x16x32_bf16 v[60:63],a[192:195],a[8:11],v[60:63]
ds_read_u16 v25,v25
v_lshlrev_b32_sdwa v27,1,v27 dst_sel:DWORD dst_unused:UNUSED_PAD src0_sel:DWORD src1_sel:WORD_1
v_mfma_f32_16x16x32_bf16 v[48:51],a[192:195],a[12:15],v[16:19]
ds_read_u16_d16_hi v27,v27
v_pk_mul_f16 v9,v8,v9
v_mfma_f32_16x16x32_bf16 v[48:51],a[196:199],a[28:31],v[48:51]
s_waitcnt lgkmcnt(7)
ds_read_b64_tr_b16 a[218:219],v1 offset:21552
v_lshl_add_u64 v[42:43],v[44:45],0,v[46:47]
v_lshlrev_b32_sdwa v28,1,v28 dst_sel:DWORD dst_unused:UNUSED_PAD src0_sel:DWORD src1_sel:WORD_1
v_mfma_f32_16x16x32_bf16 v[48:51],a[200:203],a[44:47],v[48:51]
v_exp_f16 v8,v9
v_mfma_f32_16x16x32_bf16 v[44:47],a[212:215],a[44:47],v[20:23]
ds_read_u16 v28,v28
v_lshlrev_b32_sdwa v30,1,v30 dst_sel:DWORD dst_unused:UNUSED_PAD src0_sel:DWORD src1_sel:WORD_1
v_mfma_f32_16x16x32_bf16 v[44:47],a[204:207],a[28:31],v[44:47]
ds_read_u16_d16_hi v30,v30
v_lshlrev_b32_sdwa v29,1,v29 dst_sel:DWORD dst_unused:UNUSED_PAD src0_sel:DWORD src1_sel:WORD_1
v_mfma_f32_16x16x32_bf16 v[44:47],a[208:211],a[12:15],v[44:47]
ds_read_b64_tr_b16 a[0:1],v3 offset:38992
ds_read_b64_tr_b16 a[2:3],v3 offset:40528
v_exp_f16_sdwa v8,v9 dst_sel:WORD_1 dst_unused:UNUSED_PRESERVE src0_sel:WORD_1
v_mfma_f32_16x16x32_bf16 v[208:211],a[228:231],v[36:39],v[208:211]
ds_read_b64_tr_b16 a[4:5],v3 offset:45152
ds_read_b64_tr_b16 a[6:7],v3 offset:46688
v_lshlrev_b32_sdwa v31,1,v31 dst_sel:DWORD dst_unused:UNUSED_PAD src0_sel:DWORD src1_sel:WORD_1
v_mfma_f32_16x16x32_bf16 v[212:215],a[228:231],v[40:43],v[212:215]
ds_read_u16 v29,v29
ds_read_u16_d16_hi v31,v31
v_lshlrev_b32_sdwa v52,1,v52 dst_sel:DWORD dst_unused:UNUSED_PAD src0_sel:DWORD src1_sel:WORD_1
v_mfma_f32_16x16x32_bf16 v[172:175],a[236:239],v[40:43],v[172:175]
ds_read_u16 v52,v52
ds_read_b64_tr_b16 a[8:9],v3 offset:45160
v_pk_fma_f16 v8,v8,s55,s55
v_mfma_f32_16x16x32_bf16 v[168:171],a[236:239],v[36:39],v[168:171]
ds_read_b64_tr_b16 a[10:11],v3 offset:46696
v_rcp_f16 v9,v8
v_lshlrev_b32_sdwa v54,1,v54 dst_sel:DWORD dst_unused:UNUSED_PAD src0_sel:DWORD src1_sel:WORD_1
v_mfma_f32_16x16x32_bf16 v[216:219],a[244:247],v[36:39],v[216:219]
ds_read_u16_d16_hi v54,v54
ds_read_b64_tr_b16 a[12:13],v3 offset:39000
v_mfma_f32_16x16x32_bf16 v[220:223],a[244:247],v[40:43],v[220:223]
ds_read_b64_tr_b16 a[14:15],v3 offset:40536
ds_read_b64_tr_b16 a[228:229],v3 offset:18544
v_lshlrev_b32_sdwa v53,1,v53 dst_sel:DWORD dst_unused:UNUSED_PAD src0_sel:DWORD src1_sel:WORD_1
v_mfma_f32_16x16x32_bf16 v[188:191],a[252:255],v[40:43],v[188:191]
ds_read_u16 v53,v53
ds_read_b64_tr_b16 a[230:231],v3 offset:20080
v_lshlrev_b32_sdwa v55,1,v55 dst_sel:DWORD dst_unused:UNUSED_PAD src0_sel:DWORD src1_sel:WORD_1
v_mfma_f32_16x16x32_bf16 v[184:187],a[252:255],v[36:39],v[184:187]
ds_read_u16_d16_hi v55,v55
v_rcp_f16_sdwa v9,v8 dst_sel:WORD_1 dst_unused:UNUSED_PRESERVE src0_sel:WORD_1
v_mfma_f32_16x16x32_bf16 v[224:227],a[232:235],v[36:39],v[224:227]
ds_read_b64_tr_b16 a[236:237],v3 offset:18552
ds_read_b64_tr_b16 a[238:239],v3 offset:20088
v_lshlrev_b32_sdwa v56,1,v56 dst_sel:DWORD dst_unused:UNUSED_PAD src0_sel:DWORD src1_sel:WORD_1
v_mfma_f32_16x16x32_bf16 v[228:231],a[232:235],v[40:43],v[228:231]
ds_read_u16 v56,v56
ds_read_b64_tr_b16 a[244:245],v3 offset:18608
v_lshlrev_b32_sdwa v58,1,v58 dst_sel:DWORD dst_unused:UNUSED_PAD src0_sel:DWORD src1_sel:WORD_1
v_mfma_f32_16x16x32_bf16 v[204:207],a[240:243],v[40:43],v[204:207]
ds_read_u16_d16_hi v58,v58
ds_read_b64_tr_b16 a[246:247],v3 offset:20144
v_fma_mix_f32 v57,v57,v9,0 op_sel_hi:[0,1,0]
v_mfma_f32_16x16x32_bf16 v[200:203],a[240:243],v[36:39],v[200:203]
ds_read_b64_tr_b16 a[252:253],v3 offset:18616
ds_read_b64_tr_b16 a[254:255],v3 offset:20152
v_fma_mix_f32 v59,v59,v9,0 op_sel:[0,1,0] op_sel_hi:[0,1,0]
v_mfma_f32_16x16x32_bf16 v[40:43],a[208:211],a[48:51],v[20:23]
ds_read_b64_tr_b16 a[16:17],v3 offset:39056
ds_read_b64_tr_b16 a[18:19],v3 offset:40592
v_lshlrev_b32_sdwa v32,1,v32 dst_sel:DWORD dst_unused:UNUSED_PAD src0_sel:DWORD src1_sel:WORD_1
v_mfma_f32_16x16x32_bf16 v[40:43],a[204:207],a[64:67],v[40:43]
s_waitcnt lgkmcnt(15)
ds_read_u16 v32,v32
ds_read_b64_tr_b16 a[20:21],v3 offset:45216
v_lshl_add_u64 v[24:25],v[24:25],0,v[26:27]
v_mfma_f32_16x16x32_bf16 v[40:43],a[212:215],a[80:83],v[40:43]
ds_read_b64_tr_b16 a[22:23],v3 offset:46752
ds_read_b64_tr_b16 a[24:25],v3 offset:45224
v_lshl_add_u64 v[26:27],v[28:29],0,v[30:31]
v_lshlrev_b32_sdwa v34,1,v34 dst_sel:DWORD dst_unused:UNUSED_PAD src0_sel:DWORD src1_sel:WORD_1
v_mfma_f32_16x16x32_bf16 v[36:39],a[200:203],a[80:83],v[16:19]
ds_read_u16_d16_hi v34,v34
v_lshlrev_b32_sdwa v33,1,v33 dst_sel:DWORD dst_unused:UNUSED_PAD src0_sel:DWORD src1_sel:WORD_1
v_mfma_f32_16x16x32_bf16 v[36:39],a[196:199],a[64:67],v[36:39]
s_waitcnt lgkmcnt(15)
ds_read_u16 v33,v33
v_lshl_add_u64 v[28:29],v[52:53],0,v[54:55]
v_mfma_f32_16x16x32_bf16 v[36:39],a[192:195],a[48:51],v[36:39]
ds_read_b64_tr_b16 a[26:27],v3 offset:46760
v_lshlrev_b32_sdwa v35,1,v35 dst_sel:DWORD dst_unused:UNUSED_PAD src0_sel:DWORD src1_sel:WORD_1
v_mfma_f32_16x16x32_bf16 v[52:55],a[192:195],a[52:55],v[16:19]
s_waitcnt lgkmcnt(12)
ds_read_u16_d16_hi v35,v35
v_or_b32_e64 v30,v56,v58
v_lshlrev_b32_sdwa v60,1,v60 dst_sel:DWORD dst_unused:UNUSED_PAD src0_sel:DWORD src1_sel:WORD_1
v_mfma_f32_16x16x32_bf16 v[52:55],a[196:199],a[68:71],v[52:55]
ds_read_u16 v60,v60
ds_read_b64_tr_b16 a[28:29],v3 offset:39064
v_cvt_pk_bf16_f32 v31,v57,v59
v_lshlrev_b32_sdwa v62,1,v62 dst_sel:DWORD dst_unused:UNUSED_PAD src0_sel:DWORD src1_sel:WORD_1
v_mfma_f32_16x16x32_bf16 v[52:55],a[200:203],a[84:87],v[52:55]
ds_read_u16_d16_hi v62,v62
v_lshlrev_b32_sdwa v61,1,v61 dst_sel:DWORD dst_unused:UNUSED_PAD src0_sel:DWORD src1_sel:WORD_1
v_mfma_f32_16x16x32_bf16 v[56:59],a[212:215],a[84:87],v[20:23]
ds_read_u16 v61,v61
v_lshlrev_b32_sdwa v63,1,v63 dst_sel:DWORD dst_unused:UNUSED_PAD src0_sel:DWORD src1_sel:WORD_1
v_mfma_f32_16x16x32_bf16 v[56:59],a[204:207],a[68:71],v[56:59]
ds_read_u16_d16_hi v63,v63
v_lshlrev_b32_sdwa v44,1,v44 dst_sel:DWORD dst_unused:UNUSED_PAD src0_sel:DWORD src1_sel:WORD_1
v_mfma_f32_16x16x32_bf16 v[56:59],a[208:211],a[52:55],v[56:59]
ds_read_u16 v44,v44
v_lshlrev_b32_sdwa v46,1,v46 dst_sel:DWORD dst_unused:UNUSED_PAD src0_sel:DWORD src1_sel:WORD_1
v_mfma_f32_16x16x32_bf16 v[136:139],a[224:227],v[24:27],v[136:139]
ds_read_u16_d16_hi v46,v46
v_lshlrev_b32_sdwa v45,1,v45 dst_sel:DWORD dst_unused:UNUSED_PAD src0_sel:DWORD src1_sel:WORD_1
v_mfma_f32_16x16x32_bf16 v[140:143],a[224:227],v[28:31],v[140:143]
ds_read_u16 v45,v45
v_lshlrev_b32_sdwa v47,1,v47 dst_sel:DWORD dst_unused:UNUSED_PAD src0_sel:DWORD src1_sel:WORD_1
v_mfma_f32_16x16x32_bf16 v[68:71],a[248:251],v[28:31],v[68:71]
ds_read_u16_d16_hi v47,v47
v_lshlrev_b32_sdwa v48,1,v48 dst_sel:DWORD dst_unused:UNUSED_PAD src0_sel:DWORD src1_sel:WORD_1
v_mfma_f32_16x16x32_bf16 v[64:67],a[248:251],v[24:27],v[64:67]
ds_read_u16 v48,v48
ds_read_b64_tr_b16 a[30:31],v3 offset:40600
v_mfma_f32_16x16x32_bf16 v[144:147],a[228:231],v[24:27],v[144:147]
ds_read_b64_tr_b16 a[32:33],v3 offset:39120
v_lshlrev_b32_sdwa v50,1,v50 dst_sel:DWORD dst_unused:UNUSED_PAD src0_sel:DWORD src1_sel:WORD_1
v_mfma_f32_16x16x32_bf16 v[148:151],a[228:231],v[28:31],v[148:151]
ds_read_u16_d16_hi v50,v50
v_lshlrev_b32_sdwa v49,1,v49 dst_sel:DWORD dst_unused:UNUSED_PAD src0_sel:DWORD src1_sel:WORD_1
v_mfma_f32_16x16x32_bf16 v[84:87],a[236:239],v[28:31],v[84:87]
ds_read_u16 v49,v49
v_lshlrev_b32_sdwa v51,1,v51 dst_sel:DWORD dst_unused:UNUSED_PAD src0_sel:DWORD src1_sel:WORD_1
v_mfma_f32_16x16x32_bf16 v[80:83],a[236:239],v[24:27],v[80:83]
s_waitcnt lgkmcnt(9)
ds_read_u16_d16_hi v51,v51
v_mfma_f32_16x16x32_bf16 v[152:155],a[244:247],v[24:27],v[152:155]
s_nop 0
ds_read_b64_tr_b16 a[34:35],v3 offset:40656
v_mfma_f32_16x16x32_bf16 v[156:159],a[244:247],v[28:31],v[156:159]
ds_read_b64_tr_b16 a[36:37],v3 offset:45280
v_mfma_f32_16x16x32_bf16 v[100:103],a[252:255],v[28:31],v[100:103]
ds_read_b64_tr_b16 a[38:39],v3 offset:46816
v_mfma_f32_16x16x32_bf16 v[96:99],a[252:255],v[24:27],v[96:99]
ds_read_b64_tr_b16 a[40:41],v3 offset:45288
v_mfma_f32_16x16x32_bf16 v[28:31],a[208:211],a[56:59],v[20:23]
ds_read_b64_tr_b16 a[42:43],v3 offset:46824
v_mul_f32_dpp v136,v14,v136 row_newbcast:0 row_mask:0xf bank_mask:0xf
s_add_u32 s8,s61,3145728
v_lshlrev_b32_sdwa v40,1,v40 dst_sel:DWORD dst_unused:UNUSED_PAD src0_sel:DWORD src1_sel:WORD_1
v_mfma_f32_16x16x32_bf16 v[28:31],a[204:207],a[72:75],v[28:31]
ds_read_u16 v40,v40
v_lshl_add_u64 v[32:33],v[32:33],0,v[34:35]
v_mul_f32_dpp v137,v14,v137 row_newbcast:1 row_mask:0xf bank_mask:0xf
v_mul_f32_dpp v138,v14,v138 row_newbcast:2 row_mask:0xf bank_mask:0xf
v_mfma_f32_16x16x32_bf16 v[28:31],a[212:215],a[88:91],v[28:31]
ds_read_b64_tr_b16 a[44:45],v3 offset:39128
v_lshl_add_u64 v[34:35],v[60:61],0,v[62:63]
v_mul_f32_dpp v139,v14,v139 row_newbcast:3 row_mask:0xf bank_mask:0xf
v_lshlrev_b32_sdwa v42,1,v42 dst_sel:DWORD dst_unused:UNUSED_PAD src0_sel:DWORD src1_sel:WORD_1
v_mfma_f32_16x16x32_bf16 v[24:27],a[200:203],a[88:91],v[16:19]
ds_read_u16_d16_hi v42,v42
v_mul_f32_dpp v64,v14,v64 row_newbcast:4 row_mask:0xf bank_mask:0xf
v_lshlrev_b32_sdwa v41,1,v41 dst_sel:DWORD dst_unused:UNUSED_PAD src0_sel:DWORD src1_sel:WORD_1
v_mul_f32_dpp v65,v14,v65 row_newbcast:5 row_mask:0xf bank_mask:0xf
v_mfma_f32_16x16x32_bf16 v[24:27],a[196:199],a[72:75],v[24:27]
s_waitcnt lgkmcnt(8)
ds_read_u16 v41,v41
v_lshl_add_u64 v[60:61],v[44:45],0,v[46:47]
v_mul_f32_dpp v66,v14,v66 row_newbcast:6 row_mask:0xf bank_mask:0xf
v_mfma_f32_16x16x32_bf16 v[24:27],a[192:195],a[56:59],v[24:27]
s_nop 0
ds_read_b64_tr_b16 a[46:47],v3 offset:40664
v_mul_f32_dpp v67,v14,v67 row_newbcast:7 row_mask:0xf bank_mask:0xf
v_lshlrev_b32_sdwa v43,1,v43 dst_sel:DWORD dst_unused:UNUSED_PAD src0_sel:DWORD src1_sel:WORD_1
v_cvt_pk_bf16_f32 v136,v136,v137
v_mfma_f32_16x16x32_bf16 v[44:47],a[192:195],a[60:63],v[16:19]
ds_read_b128 v[16:19],v4 offset:0
v_cvt_pk_bf16_f32 v137,v138,v139
v_lshlrev_b32_sdwa v36,1,v36 dst_sel:DWORD dst_unused:UNUSED_PAD src0_sel:DWORD src1_sel:WORD_1
v_mfma_f32_16x16x32_bf16 v[44:47],a[196:199],a[76:79],v[44:47]
ds_read_u16_d16_hi v43,v43
ds_read_b64_tr_b16 a[192:193],v1 offset:22576
v_lshl_add_u64 v[62:63],v[48:49],0,v[50:51]
v_cvt_pk_bf16_f32 v138,v64,v65
v_cvt_pk_bf16_f32 v139,v66,v67
v_lshlrev_b32_sdwa v38,1,v38 dst_sel:DWORD dst_unused:UNUSED_PAD src0_sel:DWORD src1_sel:WORD_1
v_mfma_f32_16x16x32_bf16 v[44:47],a[200:203],a[92:95],v[44:47]
buffer_store_dwordx4 v[136:139],v7,s[40:43],s8 offen offset:0 sc1
ds_read_b64_tr_b16 a[194:195],v1 offset:23600
v_mul_f32_dpp v144,v14,v144 row_newbcast:8 row_mask:0xf bank_mask:0xf
v_mfma_f32_16x16x32_bf16 v[48:51],a[212:215],a[92:95],v[20:23]
ds_read_b128 v[20:23],v4 offset:32
ds_read_u16 v36,v36
v_mul_f32_dpp v145,v14,v145 row_newbcast:9 row_mask:0xf bank_mask:0xf
v_lshlrev_b32_sdwa v37,1,v37 dst_sel:DWORD dst_unused:UNUSED_PAD src0_sel:DWORD src1_sel:WORD_1
v_mul_f32_dpp v146,v14,v146 row_newbcast:10 row_mask:0xf bank_mask:0xf
v_mfma_f32_16x16x32_bf16 v[48:51],a[204:207],a[76:79],v[48:51]
ds_read_u16_d16_hi v38,v38
ds_read_u16 v37,v37
v_mul_f32_dpp v147,v14,v147 row_newbcast:11 row_mask:0xf bank_mask:0xf
v_lshlrev_b32_sdwa v39,1,v39 dst_sel:DWORD dst_unused:UNUSED_PAD src0_sel:DWORD src1_sel:WORD_1
v_mfma_f32_16x16x32_bf16 v[48:51],a[208:211],a[60:63],v[48:51]
ds_read_u16_d16_hi v39,v39
ds_read_b64_tr_b16 a[196:197],v2 offset:22576
v_mul_f32_dpp v80,v14,v80 row_newbcast:12 row_mask:0xf bank_mask:0xf
v_mul_f32_dpp v81,v14,v81 row_newbcast:13 row_mask:0xf bank_mask:0xf
v_mfma_f32_16x16x32_bf16 v[112:115],a[224:227],v[32:35],v[112:115]
ds_read_b64_tr_b16 a[198:199],v2 offset:23600
ds_read_b64_tr_b16 a[200:201],v2 offset:20528
v_lshlrev_b32_sdwa v56,1,v56 dst_sel:DWORD dst_unused:UNUSED_PAD src0_sel:DWORD src1_sel:WORD_1
v_mul_f32_dpp v82,v14,v82 row_newbcast:14 row_mask:0xf bank_mask:0xf
v_mfma_f32_16x16x32_bf16 v[116:119],a[224:227],v[60:63],v[116:119]
ds_read_b64_tr_b16 a[202:203],v2 offset:21552
v_mul_f32_dpp v83,v14,v83 row_newbcast:15 row_mask:0xf bank_mask:0xf
v_lshlrev_b32_sdwa v58,1,v58 dst_sel:DWORD dst_unused:UNUSED_PAD src0_sel:DWORD src1_sel:WORD_1
v_cvt_pk_bf16_f32 v144,v144,v145
v_mfma_f32_16x16x32_bf16 v[76:79],a[248:251],v[60:63],v[76:79]
ds_read_u16 v56,v56
v_cvt_pk_bf16_f32 v145,v146,v147
v_lshlrev_b32_sdwa v57,1,v57 dst_sel:DWORD dst_unused:UNUSED_PAD src0_sel:DWORD src1_sel:WORD_1
v_mfma_f32_16x16x32_bf16 v[72:75],a[248:251],v[32:35],v[72:75]
ds_read_u16_d16_hi v58,v58
v_cvt_pk_bf16_f32 v146,v80,v81
v_cvt_pk_bf16_f32 v147,v82,v83
v_mfma_f32_16x16x32_bf16 v[120:123],a[228:231],v[32:35],v[120:123]
ds_read_u16 v57,v57
v_mul_f32_dpp v152,v15,v152 row_newbcast:0 row_mask:0xf bank_mask:0xf
v_lshlrev_b32_sdwa v59,1,v59 dst_sel:DWORD dst_unused:UNUSED_PAD src0_sel:DWORD src1_sel:WORD_1
v_mfma_f32_16x16x32_bf16 v[124:127],a[228:231],v[60:63],v[124:127]
ds_read_u16_d16_hi v59,v59
v_mul_f32_dpp v153,v15,v153 row_newbcast:1 row_mask:0xf bank_mask:0xf
v_lshlrev_b32_sdwa v52,1,v52 dst_sel:DWORD dst_unused:UNUSED_PAD src0_sel:DWORD src1_sel:WORD_1
v_mul_f32_dpp v154,v15,v154 row_newbcast:2 row_mask:0xf bank_mask:0xf
v_mfma_f32_16x16x32_bf16 v[92:95],a[236:239],v[60:63],v[92:95]
buffer_store_dwordx4 v[144:147],v7,s[40:43],s8 offen offset:64 sc1
v_mul_f32_dpp v155,v15,v155 row_newbcast:3 row_mask:0xf bank_mask:0xf
v_mfma_f32_16x16x32_bf16 v[88:91],a[236:239],v[32:35],v[88:91]
ds_read_b64_tr_b16 a[212:213],v2 offset:18480
v_mul_f32_dpp v96,v15,v96 row_newbcast:4 row_mask:0xf bank_mask:0xf
v_lshlrev_b32_sdwa v54,1,v54 dst_sel:DWORD dst_unused:UNUSED_PAD src0_sel:DWORD src1_sel:WORD_1
v_mul_f32_dpp v97,v15,v97 row_newbcast:5 row_mask:0xf bank_mask:0xf
v_mfma_f32_16x16x32_bf16 v[128:131],a[244:247],v[32:35],v[128:131]
ds_read_b64_tr_b16 a[214:215],v2 offset:19504
v_mul_f32_dpp v98,v15,v98 row_newbcast:6 row_mask:0xf bank_mask:0xf
v_lshlrev_b32_sdwa v53,1,v53 dst_sel:DWORD dst_unused:UNUSED_PAD src0_sel:DWORD src1_sel:WORD_1
v_mfma_f32_16x16x32_bf16 v[132:135],a[244:247],v[60:63],v[132:135]
ds_read_u16 v52,v52
v_mul_f32_dpp v99,v15,v99 row_newbcast:7 row_mask:0xf bank_mask:0xf
v_cvt_pk_bf16_f32 v152,v152,v153
v_lshlrev_b32_sdwa v55,1,v55 dst_sel:DWORD dst_unused:UNUSED_PAD src0_sel:DWORD src1_sel:WORD_1
v_mfma_f32_16x16x32_bf16 v[108:111],a[252:255],v[60:63],v[108:111]
s_nop 0
ds_read_u16_d16_hi v54,v54
v_cvt_pk_bf16_f32 v153,v154,v155
v_mfma_f32_16x16x32_bf16 v[104:107],a[252:255],v[32:35],v[104:107]
s_waitcnt lgkmcnt(7)
ds_read_u16 v53,v53
ds_read_u16_d16_hi v55,v55
v_cvt_pk_bf16_f32 v154,v96,v97
v_cvt_pk_bf16_f32 v155,v98,v99
v_mfma_f32_16x16x32_bf16 v[60:63],a[220:223],a[48:51],v[16:19]
v_mul_f32_dpp v140,v14,v140 row_newbcast:0 row_mask:0xf bank_mask:0xf
v_lshlrev_b32_sdwa v28,1,v28 dst_sel:DWORD dst_unused:UNUSED_PAD src0_sel:DWORD src1_sel:WORD_1
v_mfma_f32_16x16x32_bf16 v[60:63],a[216:219],a[64:67],v[60:63]
ds_read_u16 v28,v28
v_lshl_add_u64 v[40:41],v[40:41],0,v[42:43]
v_mul_f32_dpp v141,v14,v141 row_newbcast:1 row_mask:0xf bank_mask:0xf
v_mul_f32_dpp v142,v14,v142 row_newbcast:2 row_mask:0xf bank_mask:0xf
v_mfma_f32_16x16x32_bf16 v[60:63],a[192:195],a[80:83],v[60:63]
v_lshl_add_u64 v[42:43],v[36:37],0,v[38:39]
v_mul_f32_dpp v143,v14,v143 row_newbcast:3 row_mask:0xf bank_mask:0xf
v_lshlrev_b32_sdwa v30,1,v30 dst_sel:DWORD dst_unused:UNUSED_PAD src0_sel:DWORD src1_sel:WORD_1
v_mfma_f32_16x16x32_bf16 v[32:35],a[196:199],a[80:83],v[20:23]
ds_read_u16_d16_hi v30,v30
v_mul_f32_dpp v68,v14,v68 row_newbcast:4 row_mask:0xf bank_mask:0xf
v_lshlrev_b32_sdwa v29,1,v29 dst_sel:DWORD dst_unused:UNUSED_PAD src0_sel:DWORD src1_sel:WORD_1
v_mul_f32_dpp v69,v14,v69 row_newbcast:5 row_mask:0xf bank_mask:0xf
v_mfma_f32_16x16x32_bf16 v[32:35],a[200:203],a[64:67],v[32:35]
s_waitcnt lgkmcnt(6)
ds_read_u16 v29,v29
v_lshl_add_u64 v[36:37],v[56:57],0,v[58:59]
v_mul_f32_dpp v70,v14,v70 row_newbcast:6 row_mask:0xf bank_mask:0xf
v_mfma_f32_16x16x32_bf16 v[32:35],a[212:215],a[48:51],v[32:35]
buffer_store_dwordx4 v[152:155],v7,s[40:43],s8 offen offset:128 sc1
v_mul_f32_dpp v71,v14,v71 row_newbcast:7 row_mask:0xf bank_mask:0xf
v_lshlrev_b32_sdwa v31,1,v31 dst_sel:DWORD dst_unused:UNUSED_PAD src0_sel:DWORD src1_sel:WORD_1
v_cvt_pk_bf16_f32 v140,v140,v141
v_mfma_f32_16x16x32_bf16 v[56:59],a[212:215],a[52:55],v[20:23]
ds_read_u16_d16_hi v31,v31
v_cvt_pk_bf16_f32 v141,v142,v143
v_lshlrev_b32_sdwa v24,1,v24 dst_sel:DWORD dst_unused:UNUSED_PAD src0_sel:DWORD src1_sel:WORD_1
v_mfma_f32_16x16x32_bf16 v[56:59],a[200:203],a[68:71],v[56:59]
s_waitcnt lgkmcnt(4)
ds_read_u16 v24,v24
v_lshl_add_u64 v[38:39],v[52:53],0,v[54:55]
v_cvt_pk_bf16_f32 v142,v68,v69
v_cvt_pk_bf16_f32 v143,v70,v71
v_lshlrev_b32_sdwa v26,1,v26 dst_sel:DWORD dst_unused:UNUSED_PAD src0_sel:DWORD src1_sel:WORD_1
v_mfma_f32_16x16x32_bf16 v[56:59],a[196:199],a[84:87],v[56:59]
ds_read_u16_d16_hi v26,v26
v_mul_f32_dpp v148,v14,v148 row_newbcast:8 row_mask:0xf bank_mask:0xf
v_mfma_f32_16x16x32_bf16 v[52:55],a[192:195],a[84:87],v[16:19]
ds_read_b64_tr_b16 a[232:233],v3 offset:32832
v_mul_f32_dpp v149,v14,v149 row_newbcast:9 row_mask:0xf bank_mask:0xf
v_lshlrev_b32_sdwa v25,1,v25 dst_sel:DWORD dst_unused:UNUSED_PAD src0_sel:DWORD src1_sel:WORD_1
v_mul_f32_dpp v150,v14,v150 row_newbcast:10 row_mask:0xf bank_mask:0xf
v_mfma_f32_16x16x32_bf16 v[52:55],a[216:219],a[68:71],v[52:55]
ds_read_u16 v25,v25
v_mul_f32_dpp v151,v14,v151 row_newbcast:11 row_mask:0xf bank_mask:0xf
v_lshlrev_b32_sdwa v27,1,v27 dst_sel:DWORD dst_unused:UNUSED_PAD src0_sel:DWORD src1_sel:WORD_1
v_mfma_f32_16x16x32_bf16 v[52:55],a[220:223],a[52:55],v[52:55]
ds_read_u16_d16_hi v27,v27
v_mul_f32_dpp v84,v14,v84 row_newbcast:12 row_mask:0xf bank_mask:0xf
v_mul_f32_dpp v85,v14,v85 row_newbcast:13 row_mask:0xf bank_mask:0xf
v_mfma_f32_16x16x32_bf16 v[232:235],a[224:227],v[40:43],v[232:235]
ds_read_b64_tr_b16 a[234:235],v3 offset:34368
v_lshlrev_b32_sdwa v48,1,v48 dst_sel:DWORD dst_unused:UNUSED_PAD src0_sel:DWORD src1_sel:WORD_1
v_mul_f32_dpp v86,v14,v86 row_newbcast:14 row_mask:0xf bank_mask:0xf
v_mfma_f32_16x16x32_bf16 v[236:239],a[224:227],v[36:39],v[236:239]
ds_read_u16 v48,v48
v_mul_f32_dpp v87,v14,v87 row_newbcast:15 row_mask:0xf bank_mask:0xf
v_lshlrev_b32_sdwa v50,1,v50 dst_sel:DWORD dst_unused:UNUSED_PAD src0_sel:DWORD src1_sel:WORD_1
v_cvt_pk_bf16_f32 v148,v148,v149
v_mfma_f32_16x16x32_bf16 v[164:167],a[248:251],v[36:39],v[164:167]
ds_read_u16_d16_hi v50,v50
v_cvt_pk_bf16_f32 v149,v150,v151
v_lshlrev_b32_sdwa v49,1,v49 dst_sel:DWORD dst_unused:UNUSED_PAD src0_sel:DWORD src1_sel:WORD_1
v_mfma_f32_16x16x32_bf16 v[160:163],a[248:251],v[40:43],v[160:163]
ds_read_u16 v49,v49
v_cvt_pk_bf16_f32 v150,v84,v85
v_cvt_pk_bf16_f32 v151,v86,v87
v_mfma_f32_16x16x32_bf16 v[240:243],a[228:231],v[40:43],v[240:243]
buffer_store_dwordx4 v[140:143],v7,s[40:43],s8 offen offset:192 sc1
v_mul_f32_dpp v156,v15,v156 row_newbcast:0 row_mask:0xf bank_mask:0xf
v_lshlrev_b32_sdwa v51,1,v51 dst_sel:DWORD dst_unused:UNUSED_PAD src0_sel:DWORD src1_sel:WORD_1
v_mfma_f32_16x16x32_bf16 v[244:247],a[228:231],v[36:39],v[244:247]
ds_read_u16_d16_hi v51,v51
v_mul_f32_dpp v157,v15,v157 row_newbcast:1 row_mask:0xf bank_mask:0xf
v_lshlrev_b32_sdwa v44,1,v44 dst_sel:DWORD dst_unused:UNUSED_PAD src0_sel:DWORD src1_sel:WORD_1
v_mul_f32_dpp v158,v15,v158 row_newbcast:2 row_mask:0xf bank_mask:0xf
v_mfma_f32_16x16x32_bf16 v[180:183],a[236:239],v[36:39],v[180:183]
ds_read_u16 v44,v44
v_mul_f32_dpp v159,v15,v159 row_newbcast:3 row_mask:0xf bank_mask:0xf
v_mfma_f32_16x16x32_bf16 v[176:179],a[236:239],v[40:43],v[176:179]
ds_read_b64_tr_b16 a[240:241],v3 offset:32840
v_mul_f32_dpp v100,v15,v100 row_newbcast:4 row_mask:0xf bank_mask:0xf
v_lshlrev_b32_sdwa v46,1,v46 dst_sel:DWORD dst_unused:UNUSED_PAD src0_sel:DWORD src1_sel:WORD_1
v_mul_f32_dpp v101,v15,v101 row_newbcast:5 row_mask:0xf bank_mask:0xf
v_mfma_f32_16x16x32_bf16 v[248:251],a[244:247],v[40:43],v[248:251]
ds_read_u16_d16_hi v46,v46
v_mul_f32_dpp v102,v15,v102 row_newbcast:6 row_mask:0xf bank_mask:0xf
v_lshlrev_b32_sdwa v45,1,v45 dst_sel:DWORD dst_unused:UNUSED_PAD src0_sel:DWORD src1_sel:WORD_1
v_mfma_f32_16x16x32_bf16 v[252:255],a[244:247],v[36:39],v[252:255]
ds_read_u16 v45,v45
v_mul_f32_dpp v103,v15,v103 row_newbcast:7 row_mask:0xf bank_mask:0xf
v_cvt_pk_bf16_f32 v156,v156,v157
v_lshlrev_b32_sdwa v47,1,v47 dst_sel:DWORD dst_unused:UNUSED_PAD src0_sel:DWORD src1_sel:WORD_1
v_mfma_f32_16x16x32_bf16 v[196:199],a[252:255],v[36:39],v[196:199]
ds_read_u16_d16_hi v47,v47
v_cvt_pk_bf16_f32 v157,v158,v159
v_mfma_f32_16x16x32_bf16 v[192:195],a[252:255],v[40:43],v[192:195]
ds_read_b64_tr_b16 a[242:243],v3 offset:34376
v_cvt_pk_bf16_f32 v158,v100,v101
v_cvt_pk_bf16_f32 v159,v102,v103
v_mfma_f32_16x16x32_bf16 v[36:39],a[220:223],a[56:59],v[16:19]
s_nop 0
ds_read_b128 v[64:67],v4 offset:128
ds_read_b128 v[68:71],v4 offset:160
v_mul_f32_dpp v112,v14,v112 row_newbcast:0 row_mask:0xf bank_mask:0xf
s_add_u32 s9,s61,3244032
v_lshlrev_b32_sdwa v60,1,v60 dst_sel:DWORD dst_unused:UNUSED_PAD src0_sel:DWORD src1_sel:WORD_1
v_mfma_f32_16x16x32_bf16 v[36:39],a[216:219],a[72:75],v[36:39]
s_waitcnt lgkmcnt(9)
ds_read_u16 v60,v60
ds_read_b128 v[80:83],v4 offset:256
v_lshl_add_u64 v[28:29],v[28:29],0,v[30:31]
v_mul_f32_dpp v113,v14,v113 row_newbcast:1 row_mask:0xf bank_mask:0xf
v_mul_f32_dpp v114,v14,v114 row_newbcast:2 row_mask:0xf bank_mask:0xf
v_mfma_f32_16x16x32_bf16 v[36:39],a[192:195],a[88:91],v[36:39]
ds_read_b128 v[84:87],v4 offset:288
v_lshl_add_u64 v[30:31],v[24:25],0,v[26:27]
v_mul_f32_dpp v115,v14,v115 row_newbcast:3 row_mask:0xf bank_mask:0xf
v_lshlrev_b32_sdwa v62,1,v62 dst_sel:DWORD dst_unused:UNUSED_PAD src0_sel:DWORD src1_sel:WORD_1
v_mfma_f32_16x16x32_bf16 v[40:43],a[196:199],a[88:91],v[20:23]
buffer_store_dwordx4 v[148:151],v7,s[40:43],s8 offen offset:256 sc1
v_mul_f32_dpp v72,v14,v72 row_newbcast:4 row_mask:0xf bank_mask:0xf
v_lshlrev_b32_sdwa v61,1,v61 dst_sel:DWORD dst_unused:UNUSED_PAD src0_sel:DWORD src1_sel:WORD_1
v_mul_f32_dpp v73,v14,v73 row_newbcast:5 row_mask:0xf bank_mask:0xf
v_mfma_f32_16x16x32_bf16 v[40:43],a[200:203],a[72:75],v[40:43]
s_waitcnt lgkmcnt(9)
ds_read_u16_d16_hi v62,v62
v_lshl_add_u64 v[24:25],v[48:49],0,v[50:51]
v_mul_f32_dpp v74,v14,v74 row_newbcast:6 row_mask:0xf bank_mask:0xf
v_mfma_f32_16x16x32_bf16 v[40:43],a[212:215],a[56:59],v[40:43]
TP10211_%=:
s_cbranch_execz TR1_%=
ds_read_u16 v61,v61
v_mul_f32_dpp v75,v14,v75 row_newbcast:7 row_mask:0xf bank_mask:0xf
v_lshlrev_b32_sdwa v63,1,v63 dst_sel:DWORD dst_unused:UNUSED_PAD src0_sel:DWORD src1_sel:WORD_1
v_cvt_pk_bf16_f32 v112,v112,v113
v_mfma_f32_16x16x32_bf16 v[48:51],a[212:215],a[60:63],v[20:23]
s_nop 0
ds_read_b64_tr_b16 a[212:213],v3 offset:51312
ds_read_b64_tr_b16 a[214:215],v3 offset:52848
v_cvt_pk_bf16_f32 v113,v114,v115
v_lshlrev_b32_sdwa v32,1,v32 dst_sel:DWORD dst_unused:UNUSED_PAD src0_sel:DWORD src1_sel:WORD_1
v_mfma_f32_16x16x32_bf16 v[48:51],a[200:203],a[76:79],v[48:51]
s_waitcnt lgkmcnt(10)
ds_read_b64_tr_b16 a[200:201],v3 offset:57472
ds_read_b64_tr_b16 a[202:203],v3 offset:59008
v_lshl_add_u64 v[26:27],v[44:45],0,v[46:47]
v_cvt_pk_bf16_f32 v114,v72,v73
v_cvt_pk_bf16_f32 v115,v74,v75
v_lshlrev_b32_sdwa v34,1,v34 dst_sel:DWORD dst_unused:UNUSED_PAD src0_sel:DWORD src1_sel:WORD_1
v_mfma_f32_16x16x32_bf16 v[48:51],a[196:199],a[92:95],v[48:51]
ds_read_u16_d16_hi v63,v63
ds_read_u16 v32,v32
v_mul_f32_dpp v120,v14,v120 row_newbcast:8 row_mask:0xf bank_mask:0xf
v_mfma_f32_16x16x32_bf16 v[44:47],a[192:195],a[92:95],v[16:19]
ds_read_u16_d16_hi v34,v34
ds_read_b64_tr_b16 a[196:197],v3 offset:57480
v_mul_f32_dpp v121,v14,v121 row_newbcast:9 row_mask:0xf bank_mask:0xf
v_lshlrev_b32_sdwa v33,1,v33 dst_sel:DWORD dst_unused:UNUSED_PAD src0_sel:DWORD src1_sel:WORD_1
v_mul_f32_dpp v122,v14,v122 row_newbcast:10 row_mask:0xf bank_mask:0xf
v_mfma_f32_16x16x32_bf16 v[44:47],a[216:219],a[76:79],v[44:47]
ds_read_u16 v33,v33
ds_read_b64_tr_b16 a[198:199],v3 offset:59016
v_mul_f32_dpp v123,v14,v123 row_newbcast:11 row_mask:0xf bank_mask:0xf
v_lshlrev_b32_sdwa v35,1,v35 dst_sel:DWORD dst_unused:UNUSED_PAD src0_sel:DWORD src1_sel:WORD_1
v_mfma_f32_16x16x32_bf16 v[44:47],a[220:223],a[60:63],v[44:47]
ds_read_u16_d16_hi v35,v35
ds_read_b64_tr_b16 a[192:193],v3 offset:51320
v_mul_f32_dpp v88,v14,v88 row_newbcast:12 row_mask:0xf bank_mask:0xf
v_mul_f32_dpp v89,v14,v89 row_newbcast:13 row_mask:0xf bank_mask:0xf
v_mfma_f32_16x16x32_bf16 v[208:211],a[224:227],v[28:31],v[208:211]
ds_read_b64_tr_b16 a[194:195],v3 offset:52856
ds_read_b128 v[96:99],v4 offset:384
v_lshlrev_b32_sdwa v52,1,v52 dst_sel:DWORD dst_unused:UNUSED_PAD src0_sel:DWORD src1_sel:WORD_1
v_mul_f32_dpp v90,v14,v90 row_newbcast:14 row_mask:0xf bank_mask:0xf
v_mfma_f32_16x16x32_bf16 v[212:215],a[224:227],v[24:27],v[212:215]
buffer_store_dwordx4 v[156:159],v7,s[40:43],s8 offen offset:320 sc1
ds_read_u16 v52,v52
v_mul_f32_dpp v91,v14,v91 row_newbcast:15 row_mask:0xf bank_mask:0xf
v_lshlrev_b32_sdwa v54,1,v54 dst_sel:DWORD dst_unused:UNUSED_PAD src0_sel:DWORD src1_sel:WORD_1
v_cvt_pk_bf16_f32 v120,v120,v121
v_mfma_f32_16x16x32_bf16 v[172:175],a[248:251],v[24:27],v[172:175]
ds_read_u16_d16_hi v54,v54
ds_read_b64_tr_b16 a[224:225],v3 offset:32896
v_cvt_pk_bf16_f32 v121,v122,v123
v_lshlrev_b32_sdwa v53,1,v53 dst_sel:DWORD dst_unused:UNUSED_PAD src0_sel:DWORD src1_sel:WORD_1
v_mfma_f32_16x16x32_bf16 v[168:171],a[248:251],v[28:31],v[168:171]
ds_read_u16 v53,v53
ds_read_b64_tr_b16 a[226:227],v3 offset:34432
v_cvt_pk_bf16_f32 v122,v88,v89
v_cvt_pk_bf16_f32 v123,v90,v91
v_mfma_f32_16x16x32_bf16 v[216:219],a[228:231],v[28:31],v[216:219]
ds_read_b64_tr_b16 a[248:249],v3 offset:32904
ds_read_b64_tr_b16 a[250:251],v3 offset:34440
v_mul_f32_dpp v128,v15,v128 row_newbcast:0 row_mask:0xf bank_mask:0xf
v_lshlrev_b32_sdwa v55,1,v55 dst_sel:DWORD dst_unused:UNUSED_PAD src0_sel:DWORD src1_sel:WORD_1
v_mfma_f32_16x16x32_bf16 v[220:223],a[228:231],v[24:27],v[220:223]
ds_read_u16_d16_hi v55,v55
ds_read_b128 v[100:103],v4 offset:416
v_mul_f32_dpp v129,v15,v129 row_newbcast:1 row_mask:0xf bank_mask:0xf
v_lshlrev_b32_sdwa v56,1,v56 dst_sel:DWORD dst_unused:UNUSED_PAD src0_sel:DWORD src1_sel:WORD_1
v_mul_f32_dpp v130,v15,v130 row_newbcast:2 row_mask:0xf bank_mask:0xf
v_mfma_f32_16x16x32_bf16 v[188:191],a[236:239],v[24:27],v[188:191]
ds_read_u16 v56,v56
ds_read_b64_tr_b16 a[228:229],v3 offset:32960
v_mul_f32_dpp v131,v15,v131 row_newbcast:3 row_mask:0xf bank_mask:0xf
v_mfma_f32_16x16x32_bf16 v[184:187],a[236:239],v[28:31],v[184:187]
ds_read_b64_tr_b16 a[230:231],v3 offset:34496
ds_read_b64_tr_b16 a[236:237],v3 offset:32968
v_mul_f32_dpp v104,v15,v104 row_newbcast:4 row_mask:0xf bank_mask:0xf
v_lshlrev_b32_sdwa v58,1,v58 dst_sel:DWORD dst_unused:UNUSED_PAD src0_sel:DWORD src1_sel:WORD_1
v_mul_f32_dpp v105,v15,v105 row_newbcast:5 row_mask:0xf bank_mask:0xf
v_mfma_f32_16x16x32_bf16 v[224:227],a[244:247],v[28:31],v[224:227]
ds_read_u16_d16_hi v58,v58
ds_read_b64_tr_b16 a[238:239],v3 offset:34504
v_mul_f32_dpp v106,v15,v106 row_newbcast:6 row_mask:0xf bank_mask:0xf
v_lshlrev_b32_sdwa v57,1,v57 dst_sel:DWORD dst_unused:UNUSED_PAD src0_sel:DWORD src1_sel:WORD_1
v_mfma_f32_16x16x32_bf16 v[228:231],a[244:247],v[24:27],v[228:231]
ds_read_u16 v57,v57
ds_read_b64_tr_b16 a[216:217],v3 offset:51376
v_mul_f32_dpp v107,v15,v107 row_newbcast:7 row_mask:0xf bank_mask:0xf
v_cvt_pk_bf16_f32 v128,v128,v129
v_lshlrev_b32_sdwa v59,1,v59 dst_sel:DWORD dst_unused:UNUSED_PAD src0_sel:DWORD src1_sel:WORD_1
v_mfma_f32_16x16x32_bf16 v[204:207],a[252:255],v[24:27],v[204:207]
s_nop 0
buffer_store_dwordx4 v[112:115],v7,s[40:43],s9 offen offset:0 sc1
ds_read_u16_d16_hi v59,v59
v_cvt_pk_bf16_f32 v129,v130,v131
v_mfma_f32_16x16x32_bf16 v[200:203],a[252:255],v[28:31],v[200:203]
s_waitcnt lgkmcnt(15)
ds_read_b64_tr_b16 a[218:219],v3 offset:52912
ds_read_b128 v[20:23],v4 offset:512
v_cvt_pk_bf16_f32 v130,v104,v105
v_cvt_pk_bf16_f32 v131,v106,v107
v_mfma_f32_16x16x32_bf16 v[24:27],a[96:99],a[48:51],v[64:67]
ds_read_b128 v[16:19],v4 offset:544
ds_read_b64_tr_b16 a[220:221],v3 offset:57536
v_mul_f32_dpp v116,v14,v116 row_newbcast:0 row_mask:0xf bank_mask:0xf
v_lshlrev_b32_sdwa v36,1,v36 dst_sel:DWORD dst_unused:UNUSED_PAD src0_sel:DWORD src1_sel:WORD_1
v_mfma_f32_16x16x32_bf16 v[24:27],a[100:103],a[64:67],v[24:27]
ds_read_u16 v36,v36
ds_read_b64_tr_b16 a[222:223],v3 offset:59072
v_lshl_add_u64 v[60:61],v[60:61],0,v[62:63]
v_mul_f32_dpp v117,v14,v117 row_newbcast:1 row_mask:0xf bank_mask:0xf
v_mul_f32_dpp v118,v14,v118 row_newbcast:2 row_mask:0xf bank_mask:0xf
v_mfma_f32_16x16x32_bf16 v[24:27],a[104:107],a[80:83],v[24:27]
ds_read_b64_tr_b16 a[204:205],v3 offset:57544
v_lshl_add_u64 v[62:63],v[32:33],0,v[34:35]
v_mul_f32_dpp v119,v14,v119 row_newbcast:3 row_mask:0xf bank_mask:0xf
v_lshlrev_b32_sdwa v38,1,v38 dst_sel:DWORD dst_unused:UNUSED_PAD src0_sel:DWORD src1_sel:WORD_1
v_mfma_f32_16x16x32_bf16 v[28:31],a[108:111],a[80:83],v[68:71]
ds_read_u16_d16_hi v38,v38
v_mul_f32_dpp v76,v14,v76 row_newbcast:4 row_mask:0xf bank_mask:0xf
v_lshlrev_b32_sdwa v37,1,v37 dst_sel:DWORD dst_unused:UNUSED_PAD src0_sel:DWORD src1_sel:WORD_1
v_mul_f32_dpp v77,v14,v77 row_newbcast:5 row_mask:0xf bank_mask:0xf
v_mfma_f32_16x16x32_bf16 v[28:31],a[112:115],a[64:67],v[28:31]
s_waitcnt lgkmcnt(13)
ds_read_u16 v37,v37
v_lshl_add_u64 v[32:33],v[52:53],0,v[54:55]
v_mul_f32_dpp v78,v14,v78 row_newbcast:6 row_mask:0xf bank_mask:0xf
v_mfma_f32_16x16x32_bf16 v[28:31],a[116:119],a[48:51],v[28:31]
s_nop 0
ds_read_b64_tr_b16 a[206:207],v3 offset:59080
v_mul_f32_dpp v79,v14,v79 row_newbcast:7 row_mask:0xf bank_mask:0xf
v_lshlrev_b32_sdwa v39,1,v39 dst_sel:DWORD dst_unused:UNUSED_PAD src0_sel:DWORD src1_sel:WORD_1
v_cvt_pk_bf16_f32 v116,v116,v117
v_mfma_f32_16x16x32_bf16 v[52:55],a[120:123],a[48:51],v[84:87]
s_nop 0
ds_read_u16_d16_hi v39,v39
v_cvt_pk_bf16_f32 v117,v118,v119
v_lshlrev_b32_sdwa v40,1,v40 dst_sel:DWORD dst_unused:UNUSED_PAD src0_sel:DWORD src1_sel:WORD_1
v_mfma_f32_16x16x32_bf16 v[52:55],a[124:127],a[64:67],v[52:55]
s_waitcnt lgkmcnt(11)
ds_read_u16 v40,v40
ds_read_b64_tr_b16 a[208:209],v3 offset:51384
v_lshl_add_u64 v[34:35],v[56:57],0,v[58:59]
v_cvt_pk_bf16_f32 v118,v76,v77
v_cvt_pk_bf16_f32 v119,v78,v79
v_lshlrev_b32_sdwa v42,1,v42 dst_sel:DWORD dst_unused:UNUSED_PAD src0_sel:DWORD src1_sel:WORD_1
v_mfma_f32_16x16x32_bf16 v[52:55],a[128:131],a[80:83],v[52:55]
buffer_store_dwordx4 v[120:123],v7,s[40:43],s9 offen offset:64 sc1
ds_read_u16_d16_hi v42,v42
v_mul_f32_dpp v124,v14,v124 row_newbcast:8 row_mask:0xf bank_mask:0xf
v_mfma_f32_16x16x32_bf16 v[56:59],a[132:135],a[80:83],v[80:83]
ds_read_b64_tr_b16 a[210:211],v3 offset:52920
v_mul_f32_dpp v125,v14,v125 row_newbcast:9 row_mask:0xf bank_mask:0xf
v_lshlrev_b32_sdwa v41,1,v41 dst_sel:DWORD dst_unused:UNUSED_PAD src0_sel:DWORD src1_sel:WORD_1
v_mul_f32_dpp v126,v14,v126 row_newbcast:10 row_mask:0xf bank_mask:0xf
v_mfma_f32_16x16x32_bf16 v[56:59],a[136:139],a[64:67],v[56:59]
ds_read_u16 v41,v41
v_mul_f32_dpp v127,v14,v127 row_newbcast:11 row_mask:0xf bank_mask:0xf
v_lshlrev_b32_sdwa v43,1,v43 dst_sel:DWORD dst_unused:UNUSED_PAD src0_sel:DWORD src1_sel:WORD_1
v_mfma_f32_16x16x32_bf16 v[56:59],a[140:143],a[48:51],v[56:59]
ds_read_u16_d16_hi v43,v43
v_mul_f32_dpp v92,v14,v92 row_newbcast:12 row_mask:0xf bank_mask:0xf
v_mul_f32_dpp v93,v14,v93 row_newbcast:13 row_mask:0xf bank_mask:0xf
v_mfma_f32_16x16x32_bf16 v[232:235],a[232:235],v[60:63],v[232:235]
ds_read_b64_tr_b16 a[244:245],v3 offset:51440
v_lshlrev_b32_sdwa v44,1,v44 dst_sel:DWORD dst_unused:UNUSED_PAD src0_sel:DWORD src1_sel:WORD_1
v_mul_f32_dpp v94,v14,v94 row_newbcast:14 row_mask:0xf bank_mask:0xf
v_mfma_f32_16x16x32_bf16 v[236:239],a[232:235],v[32:35],v[236:239]
ds_read_u16 v44,v44
v_mul_f32_dpp v95,v14,v95 row_newbcast:15 row_mask:0xf bank_mask:0xf
v_lshlrev_b32_sdwa v46,1,v46 dst_sel:DWORD dst_unused:UNUSED_PAD src0_sel:DWORD src1_sel:WORD_1
v_cvt_pk_bf16_f32 v124,v124,v125
v_mfma_f32_16x16x32_bf16 v[164:167],a[240:243],v[32:35],v[164:167]
ds_read_u16_d16_hi v46,v46
v_cvt_pk_bf16_f32 v125,v126,v127
v_lshlrev_b32_sdwa v45,1,v45 dst_sel:DWORD dst_unused:UNUSED_PAD src0_sel:DWORD src1_sel:WORD_1
v_mfma_f32_16x16x32_bf16 v[160:163],a[240:243],v[60:63],v[160:163]
ds_read_u16 v45,v45
v_cvt_pk_bf16_f32 v126,v92,v93
v_cvt_pk_bf16_f32 v127,v94,v95
v_mfma_f32_16x16x32_bf16 v[240:243],a[224:227],v[60:63],v[240:243]
ds_read_b64_tr_b16 a[246:247],v3 offset:52976
v_mul_f32_dpp v132,v15,v132 row_newbcast:0 row_mask:0xf bank_mask:0xf
v_lshlrev_b32_sdwa v47,1,v47 dst_sel:DWORD dst_unused:UNUSED_PAD src0_sel:DWORD src1_sel:WORD_1
v_mfma_f32_16x16x32_bf16 v[244:247],a[224:227],v[32:35],v[244:247]
ds_read_u16_d16_hi v47,v47
v_mul_f32_dpp v133,v15,v133 row_newbcast:1 row_mask:0xf bank_mask:0xf
v_lshlrev_b32_sdwa v48,1,v48 dst_sel:DWORD dst_unused:UNUSED_PAD src0_sel:DWORD src1_sel:WORD_1
v_mul_f32_dpp v134,v15,v134 row_newbcast:2 row_mask:0xf bank_mask:0xf
v_mfma_f32_16x16x32_bf16 v[180:183],a[248:251],v[32:35],v[180:183]
ds_read_u16 v48,v48
v_mul_f32_dpp v135,v15,v135 row_newbcast:3 row_mask:0xf bank_mask:0xf
v_mfma_f32_16x16x32_bf16 v[176:179],a[248:251],v[60:63],v[176:179]
buffer_store_dwordx4 v[128:131],v7,s[40:43],s9 offen offset:128 sc1
v_mul_f32_dpp v108,v15,v108 row_newbcast:4 row_mask:0xf bank_mask:0xf
v_lshlrev_b32_sdwa v50,1,v50 dst_sel:DWORD dst_unused:UNUSED_PAD src0_sel:DWORD src1_sel:WORD_1
v_mul_f32_dpp v109,v15,v109 row_newbcast:5 row_mask:0xf bank_mask:0xf
v_mfma_f32_16x16x32_bf16 v[248:251],a[228:231],v[60:63],v[248:251]
ds_read_u16_d16_hi v50,v50
v_mul_f32_dpp v110,v15,v110 row_newbcast:6 row_mask:0xf bank_mask:0xf
v_lshlrev_b32_sdwa v49,1,v49 dst_sel:DWORD dst_unused:UNUSED_PAD src0_sel:DWORD src1_sel:WORD_1
v_mfma_f32_16x16x32_bf16 v[252:255],a[228:231],v[32:35],v[252:255]
ds_read_u16 v49,v49
v_mul_f32_dpp v111,v15,v111 row_newbcast:7 row_mask:0xf bank_mask:0xf
v_cvt_pk_bf16_f32 v132,v132,v133
v_lshlrev_b32_sdwa v51,1,v51 dst_sel:DWORD dst_unused:UNUSED_PAD src0_sel:DWORD src1_sel:WORD_1
v_mfma_f32_16x16x32_bf16 v[196:199],a[236:239],v[32:35],v[196:199]
ds_read_u16_d16_hi v51,v51
v_cvt_pk_bf16_f32 v133,v134,v135
v_mfma_f32_16x16x32_bf16 v[192:195],a[236:239],v[60:63],v[192:195]
ds_read_b64_tr_b16 a[252:253],v3 offset:57600
v_cvt_pk_bf16_f32 v134,v108,v109
v_cvt_pk_bf16_f32 v135,v110,v111
v_mfma_f32_16x16x32_bf16 v[32:35],a[144:147],a[48:51],v[96:99]
ds_read_b64_tr_b16 a[254:255],v3 offset:59136
v_lshlrev_b32_sdwa v24,1,v24 dst_sel:DWORD dst_unused:UNUSED_PAD src0_sel:DWORD src1_sel:WORD_1
v_mfma_f32_16x16x32_bf16 v[32:35],a[148:151],a[64:67],v[32:35]
s_waitcnt lgkmcnt(7)
ds_read_u16 v24,v24
v_lshl_add_u64 v[36:37],v[36:37],0,v[38:39]
v_mfma_f32_16x16x32_bf16 v[32:35],a[152:155],a[80:83],v[32:35]
s_nop 0
v_lshl_add_u64 v[38:39],v[40:41],0,v[42:43]
v_lshlrev_b32_sdwa v26,1,v26 dst_sel:DWORD dst_unused:UNUSED_PAD src0_sel:DWORD src1_sel:WORD_1
v_mfma_f32_16x16x32_bf16 v[60:63],a[156:159],a[80:83],v[100:103]
ds_read_u16_d16_hi v26,v26
v_lshlrev_b32_sdwa v25,1,v25 dst_sel:DWORD dst_unused:UNUSED_PAD src0_sel:DWORD src1_sel:WORD_1
v_mfma_f32_16x16x32_bf16 v[60:63],a[160:163],a[64:67],v[60:63]
s_waitcnt lgkmcnt(7)
ds_read_u16 v25,v25
v_lshl_add_u64 v[40:41],v[44:45],0,v[46:47]
v_mfma_f32_16x16x32_bf16 v[60:63],a[164:167],a[48:51],v[60:63]
v_lshlrev_b32_sdwa v27,1,v27 dst_sel:DWORD dst_unused:UNUSED_PAD src0_sel:DWORD src1_sel:WORD_1
v_lshlrev_b32_sdwa v28,1,v28 dst_sel:DWORD dst_unused:UNUSED_PAD src0_sel:DWORD src1_sel:WORD_1
v_mfma_f32_16x16x32_bf16 v[44:47],a[168:171],a[48:51],v[16:19]
ds_read_u16_d16_hi v27,v27
v_lshlrev_b32_sdwa v30,1,v30 dst_sel:DWORD dst_unused:UNUSED_PAD src0_sel:DWORD src1_sel:WORD_1
v_mfma_f32_16x16x32_bf16 v[44:47],a[172:175],a[64:67],v[44:47]
s_waitcnt lgkmcnt(6)
ds_read_u16 v28,v28
v_lshl_add_u64 v[42:43],v[48:49],0,v[50:51]
v_mfma_f32_16x16x32_bf16 v[44:47],a[176:179],a[80:83],v[44:47]
buffer_store_dwordx4 v[116:119],v7,s[40:43],s9 offen offset:192 sc1
v_lshlrev_b32_sdwa v29,1,v29 dst_sel:DWORD dst_unused:UNUSED_PAD src0_sel:DWORD src1_sel:WORD_1
v_mfma_f32_16x16x32_bf16 v[48:51],a[180:183],a[80:83],v[20:23]
ds_read_u16_d16_hi v30,v30
v_lshlrev_b32_sdwa v31,1,v31 dst_sel:DWORD dst_unused:UNUSED_PAD src0_sel:DWORD src1_sel:WORD_1
v_mfma_f32_16x16x32_bf16 v[48:51],a[184:187],a[64:67],v[48:51]
ds_read_u16 v29,v29
v_lshlrev_b32_sdwa v56,1,v56 dst_sel:DWORD dst_unused:UNUSED_PAD src0_sel:DWORD src1_sel:WORD_1
v_mfma_f32_16x16x32_bf16 v[48:51],a[188:191],a[48:51],v[48:51]
ds_read_u16_d16_hi v31,v31
v_lshlrev_b32_sdwa v58,1,v58 dst_sel:DWORD dst_unused:UNUSED_PAD src0_sel:DWORD src1_sel:WORD_1
v_mfma_f32_16x16x32_bf16 v[208:211],a[232:235],v[36:39],v[208:211]
ds_read_u16 v56,v56
v_lshlrev_b32_sdwa v57,1,v57 dst_sel:DWORD dst_unused:UNUSED_PAD src0_sel:DWORD src1_sel:WORD_1
v_mfma_f32_16x16x32_bf16 v[212:215],a[232:235],v[40:43],v[212:215]
ds_read_u16_d16_hi v58,v58
v_lshlrev_b32_sdwa v59,1,v59 dst_sel:DWORD dst_unused:UNUSED_PAD src0_sel:DWORD src1_sel:WORD_1
v_mfma_f32_16x16x32_bf16 v[172:175],a[240:243],v[40:43],v[172:175]
ds_read_u16 v57,v57
v_lshlrev_b32_sdwa v52,1,v52 dst_sel:DWORD dst_unused:UNUSED_PAD src0_sel:DWORD src1_sel:WORD_1
v_mfma_f32_16x16x32_bf16 v[168:171],a[240:243],v[36:39],v[168:171]
ds_read_u16_d16_hi v59,v59
ds_read_u16 v52,v52
v_mfma_f32_16x16x32_bf16 v[216:219],a[224:227],v[36:39],v[216:219]
ds_read_b64_tr_b16 a[232:233],v3 offset:57608
v_lshlrev_b32_sdwa v54,1,v54 dst_sel:DWORD dst_unused:UNUSED_PAD src0_sel:DWORD src1_sel:WORD_1
v_mfma_f32_16x16x32_bf16 v[220:223],a[224:227],v[40:43],v[220:223]
ds_read_u16_d16_hi v54,v54
v_lshlrev_b32_sdwa v53,1,v53 dst_sel:DWORD dst_unused:UNUSED_PAD src0_sel:DWORD src1_sel:WORD_1
v_mfma_f32_16x16x32_bf16 v[188:191],a[248:251],v[40:43],v[188:191]
ds_read_u16 v53,v53
v_lshlrev_b32_sdwa v55,1,v55 dst_sel:DWORD dst_unused:UNUSED_PAD src0_sel:DWORD src1_sel:WORD_1
v_mfma_f32_16x16x32_bf16 v[184:187],a[248:251],v[36:39],v[184:187]
ds_read_u16_d16_hi v55,v55
v_mfma_f32_16x16x32_bf16 v[224:227],a[228:231],v[36:39],v[224:227]
ds_read_b64_tr_b16 a[234:235],v3 offset:59144
v_mfma_f32_16x16x32_bf16 v[228:231],a[228:231],v[40:43],v[228:231]
ds_read_b64_tr_b16 a[240:241],v3 offset:51448
v_mfma_f32_16x16x32_bf16 v[204:207],a[236:239],v[40:43],v[204:207]
ds_read_b64_tr_b16 a[242:243],v3 offset:52984
v_mfma_f32_16x16x32_bf16 v[200:203],a[236:239],v[36:39],v[200:203]
buffer_store_dwordx4 v[124:127],v7,s[40:43],s9 offen offset:256 sc1
v_mfma_f32_16x16x32_bf16 v[40:43],a[96:99],a[52:55],v[64:67]
v_lshlrev_b32_sdwa v32,1,v32 dst_sel:DWORD dst_unused:UNUSED_PAD src0_sel:DWORD src1_sel:WORD_1
v_lshlrev_b32_sdwa v34,1,v34 dst_sel:DWORD dst_unused:UNUSED_PAD src0_sel:DWORD src1_sel:WORD_1
v_mfma_f32_16x16x32_bf16 v[40:43],a[100:103],a[68:71],v[40:43]
s_waitcnt lgkmcnt(6)
ds_read_u16 v32,v32
v_lshl_add_u64 v[24:25],v[24:25],0,v[26:27]
v_mfma_f32_16x16x32_bf16 v[40:43],a[104:107],a[84:87],v[40:43]
ds_read_u16_d16_hi v34,v34
v_lshl_add_u64 v[26:27],v[28:29],0,v[30:31]
v_mfma_f32_16x16x32_bf16 v[36:39],a[108:111],a[84:87],v[68:71]
v_lshlrev_b32_sdwa v33,1,v33 dst_sel:DWORD dst_unused:UNUSED_PAD src0_sel:DWORD src1_sel:WORD_1
v_lshlrev_b32_sdwa v35,1,v35 dst_sel:DWORD dst_unused:UNUSED_PAD src0_sel:DWORD src1_sel:WORD_1
v_mfma_f32_16x16x32_bf16 v[36:39],a[112:115],a[68:71],v[36:39]
ds_read_u16 v33,v33
v_lshl_add_u64 v[28:29],v[56:57],0,v[58:59]
v_mfma_f32_16x16x32_bf16 v[36:39],a[116:119],a[52:55],v[36:39]
ds_read_u16_d16_hi v35,v35
v_lshlrev_b32_sdwa v60,1,v60 dst_sel:DWORD dst_unused:UNUSED_PAD src0_sel:DWORD src1_sel:WORD_1
v_mfma_f32_16x16x32_bf16 v[56:59],a[120:123],a[52:55],v[84:87]
ds_read_u16 v60,v60
v_lshlrev_b32_sdwa v62,1,v62 dst_sel:DWORD dst_unused:UNUSED_PAD src0_sel:DWORD src1_sel:WORD_1
v_mfma_f32_16x16x32_bf16 v[56:59],a[124:127],a[68:71],v[56:59]
s_waitcnt lgkmcnt(5)
ds_read_u16_d16_hi v62,v62
v_lshl_add_u64 v[30:31],v[52:53],0,v[54:55]
v_mfma_f32_16x16x32_bf16 v[56:59],a[128:131],a[84:87],v[56:59]
v_lshlrev_b32_sdwa v61,1,v61 dst_sel:DWORD dst_unused:UNUSED_PAD src0_sel:DWORD src1_sel:WORD_1
v_lshlrev_b32_sdwa v63,1,v63 dst_sel:DWORD dst_unused:UNUSED_PAD src0_sel:DWORD src1_sel:WORD_1
v_mfma_f32_16x16x32_bf16 v[52:55],a[132:135],a[84:87],v[80:83]
ds_read_u16 v61,v61
v_lshlrev_b32_sdwa v48,1,v48 dst_sel:DWORD dst_unused:UNUSED_PAD src0_sel:DWORD src1_sel:WORD_1
v_mfma_f32_16x16x32_bf16 v[52:55],a[136:139],a[68:71],v[52:55]
ds_read_u16_d16_hi v63,v63
v_lshlrev_b32_sdwa v50,1,v50 dst_sel:DWORD dst_unused:UNUSED_PAD src0_sel:DWORD src1_sel:WORD_1
v_mfma_f32_16x16x32_bf16 v[52:55],a[140:143],a[52:55],v[52:55]
ds_read_u16 v48,v48
v_lshlrev_b32_sdwa v49,1,v49 dst_sel:DWORD dst_unused:UNUSED_PAD src0_sel:DWORD src1_sel:WORD_1
v_mfma_f32_16x16x32_bf16 v[232:235],a[0:3],v[24:27],v[232:235]
ds_read_u16_d16_hi v50,v50
v_lshlrev_b32_sdwa v51,1,v51 dst_sel:DWORD dst_unused:UNUSED_PAD src0_sel:DWORD src1_sel:WORD_1
v_mfma_f32_16x16x32_bf16 v[232:235],a[4:7],v[28:31],v[232:235]
ds_read_u16 v49,v49
v_lshlrev_b32_sdwa v44,1,v44 dst_sel:DWORD dst_unused:UNUSED_PAD src0_sel:DWORD src1_sel:WORD_1
v_mfma_f32_16x16x32_bf16 v[160:163],a[8:11],v[28:31],v[160:163]
buffer_store_dwordx4 v[132:135],v7,s[40:43],s9 offen offset:320 sc1
v_lshlrev_b32_sdwa v46,1,v46 dst_sel:DWORD dst_unused:UNUSED_PAD src0_sel:DWORD src1_sel:WORD_1
v_mfma_f32_16x16x32_bf16 v[160:163],a[12:15],v[24:27],v[160:163]
ds_read_u16_d16_hi v51,v51
v_lshlrev_b32_sdwa v45,1,v45 dst_sel:DWORD dst_unused:UNUSED_PAD src0_sel:DWORD src1_sel:WORD_1
v_mfma_f32_16x16x32_bf16 v[240:243],a[16:19],v[24:27],v[240:243]
ds_read_u16 v44,v44
v_lshlrev_b32_sdwa v47,1,v47 dst_sel:DWORD dst_unused:UNUSED_PAD src0_sel:DWORD src1_sel:WORD_1
v_mfma_f32_16x16x32_bf16 v[240:243],a[20:23],v[28:31],v[240:243]
ds_read_u16_d16_hi v46,v46
v_mfma_f32_16x16x32_bf16 v[176:179],a[24:27],v[28:31],v[176:179]
ds_read_u16 v45,v45
v_mfma_f32_16x16x32_bf16 v[176:179],a[28:31],v[24:27],v[176:179]
ds_read_u16_d16_hi v47,v47
v_mfma_f32_16x16x32_bf16 v[248:251],a[32:35],v[24:27],v[248:251]
v_mfma_f32_16x16x32_bf16 v[248:251],a[36:39],v[28:31],v[248:251]
v_mfma_f32_16x16x32_bf16 v[192:195],a[40:43],v[28:31],v[192:195]
v_mfma_f32_16x16x32_bf16 v[192:195],a[44:47],v[24:27],v[192:195]
v_mfma_f32_16x16x32_bf16 v[28:31],a[144:147],a[52:55],v[96:99]
v_lshlrev_b32_sdwa v40,1,v40 dst_sel:DWORD dst_unused:UNUSED_PAD src0_sel:DWORD src1_sel:WORD_1
v_lshlrev_b32_sdwa v42,1,v42 dst_sel:DWORD dst_unused:UNUSED_PAD src0_sel:DWORD src1_sel:WORD_1
v_mfma_f32_16x16x32_bf16 v[28:31],a[148:151],a[68:71],v[28:31]
s_waitcnt lgkmcnt(3)
ds_read_u16 v40,v40
v_lshl_add_u64 v[32:33],v[32:33],0,v[34:35]
v_mfma_f32_16x16x32_bf16 v[28:31],a[152:155],a[84:87],v[28:31]
ds_read_u16_d16_hi v42,v42
v_lshl_add_u64 v[34:35],v[60:61],0,v[62:63]
v_mfma_f32_16x16x32_bf16 v[24:27],a[156:159],a[84:87],v[100:103]
v_lshl_add_u64 v[60:61],v[48:49],0,v[50:51]
v_lshlrev_b32_sdwa v41,1,v41 dst_sel:DWORD dst_unused:UNUSED_PAD src0_sel:DWORD src1_sel:WORD_1
v_mfma_f32_16x16x32_bf16 v[24:27],a[160:163],a[68:71],v[24:27]
s_waitcnt lgkmcnt(2)
ds_read_u16 v41,v41
v_lshl_add_u64 v[62:63],v[44:45],0,v[46:47]
v_mfma_f32_16x16x32_bf16 v[24:27],a[164:167],a[52:55],v[24:27]
v_lshlrev_b32_sdwa v43,1,v43 dst_sel:DWORD dst_unused:UNUSED_PAD src0_sel:DWORD src1_sel:WORD_1
v_lshlrev_b32_sdwa v36,1,v36 dst_sel:DWORD dst_unused:UNUSED_PAD src0_sel:DWORD src1_sel:WORD_1
v_mfma_f32_16x16x32_bf16 v[232:235],a[212:215],v[32:35],v[232:235]
ds_read_u16_d16_hi v43,v43
v_lshlrev_b32_sdwa v38,1,v38 dst_sel:DWORD dst_unused:UNUSED_PAD src0_sel:DWORD src1_sel:WORD_1
v_mfma_f32_16x16x32_bf16 v[232:235],a[200:203],v[60:63],v[232:235]
ds_read_u16 v36,v36
v_lshlrev_b32_sdwa v37,1,v37 dst_sel:DWORD dst_unused:UNUSED_PAD src0_sel:DWORD src1_sel:WORD_1
v_mfma_f32_16x16x32_bf16 v[160:163],a[196:199],v[60:63],v[160:163]
ds_read_u16_d16_hi v38,v38
v_lshlrev_b32_sdwa v39,1,v39 dst_sel:DWORD dst_unused:UNUSED_PAD src0_sel:DWORD src1_sel:WORD_1
v_mfma_f32_16x16x32_bf16 v[160:163],a[192:195],v[32:35],v[160:163]
ds_read_u16 v37,v37
v_lshlrev_b32_sdwa v52,1,v52 dst_sel:DWORD dst_unused:UNUSED_PAD src0_sel:DWORD src1_sel:WORD_1
v_mfma_f32_16x16x32_bf16 v[240:243],a[216:219],v[32:35],v[240:243]
ds_read_u16_d16_hi v39,v39
v_lshlrev_b32_sdwa v54,1,v54 dst_sel:DWORD dst_unused:UNUSED_PAD src0_sel:DWORD src1_sel:WORD_1
v_mfma_f32_16x16x32_bf16 v[240:243],a[220:223],v[60:63],v[240:243]
ds_read_u16 v52,v52
v_mul_f32_dpp v232,v14,v232 row_newbcast:0 row_mask:0xf bank_mask:0xf
s_add_u32 s10,s61,4718592
v_mfma_f32_16x16x32_bf16 v[176:179],a[204:207],v[60:63],v[176:179]
ds_read_u16_d16_hi v54,v54
v_mul_f32_dpp v233,v14,v233 row_newbcast:1 row_mask:0xf bank_mask:0xf
v_mfma_f32_16x16x32_bf16 v[176:179],a[208:211],v[32:35],v[176:179]
v_mul_f32_dpp v234,v14,v234 row_newbcast:2 row_mask:0xf bank_mask:0xf
v_lshlrev_b32_sdwa v53,1,v53 dst_sel:DWORD dst_unused:UNUSED_PAD src0_sel:DWORD src1_sel:WORD_1
v_mfma_f32_16x16x32_bf16 v[248:251],a[244:247],v[32:35],v[248:251]
ds_read_u16 v53,v53
v_mul_f32_dpp v235,v14,v235 row_newbcast:3 row_mask:0xf bank_mask:0xf
v_mfma_f32_16x16x32_bf16 v[248:251],a[252:255],v[60:63],v[248:251]
v_mul_f32_dpp v160,v14,v160 row_newbcast:4 row_mask:0xf bank_mask:0xf
v_lshlrev_b32_sdwa v55,1,v55 dst_sel:DWORD dst_unused:UNUSED_PAD src0_sel:DWORD src1_sel:WORD_1
v_mfma_f32_16x16x32_bf16 v[192:195],a[232:235],v[60:63],v[192:195]
ds_read_u16_d16_hi v55,v55
v_mul_f32_dpp v161,v14,v161 row_newbcast:5 row_mask:0xf bank_mask:0xf
v_mfma_f32_16x16x32_bf16 v[192:195],a[240:243],v[32:35],v[192:195]
v_mul_f32_dpp v162,v14,v162 row_newbcast:6 row_mask:0xf bank_mask:0xf
v_lshlrev_b32_sdwa v56,1,v56 dst_sel:DWORD dst_unused:UNUSED_PAD src0_sel:DWORD src1_sel:WORD_1
v_mfma_f32_16x16x32_bf16 v[48:51],a[168:171],a[52:55],v[16:19]
ds_read_u16 v56,v56
v_mul_f32_dpp v163,v14,v163 row_newbcast:7 row_mask:0xf bank_mask:0xf
v_mfma_f32_16x16x32_bf16 v[48:51],a[172:175],a[68:71],v[48:51]
v_cvt_pk_bf16_f32 v232,v232,v233
v_lshlrev_b32_sdwa v58,1,v58 dst_sel:DWORD dst_unused:UNUSED_PAD src0_sel:DWORD src1_sel:WORD_1
v_mfma_f32_16x16x32_bf16 v[48:51],a[176:179],a[84:87],v[48:51]
ds_read_u16_d16_hi v58,v58
v_cvt_pk_bf16_f32 v233,v234,v235
v_lshlrev_b32_sdwa v57,1,v57 dst_sel:DWORD dst_unused:UNUSED_PAD src0_sel:DWORD src1_sel:WORD_1
v_mfma_f32_16x16x32_bf16 v[44:47],a[180:183],a[84:87],v[20:23]
ds_read_u16 v57,v57
v_cvt_pk_bf16_f32 v234,v160,v161
v_lshlrev_b32_sdwa v59,1,v59 dst_sel:DWORD dst_unused:UNUSED_PAD src0_sel:DWORD src1_sel:WORD_1
v_mfma_f32_16x16x32_bf16 v[44:47],a[184:187],a[68:71],v[44:47]
ds_read_u16_d16_hi v59,v59
v_cvt_pk_bf16_f32 v235,v162,v163
v_mfma_f32_16x16x32_bf16 v[44:47],a[188:191],a[52:55],v[44:47]
buffer_store_dwordx4 v[232:235],v7,s[40:43],s10 offen offset:0 sc1
v_mul_f32_dpp v240,v14,v240 row_newbcast:8 row_mask:0xf bank_mask:0xf
v_mfma_f32_16x16x32_bf16 v[60:63],a[96:99],a[56:59],v[64:67]
v_lshlrev_b32_sdwa v28,1,v28 dst_sel:DWORD dst_unused:UNUSED_PAD src0_sel:DWORD src1_sel:WORD_1
v_mul_f32_dpp v241,v14,v241 row_newbcast:9 row_mask:0xf bank_mask:0xf
v_mfma_f32_16x16x32_bf16 v[60:63],a[100:103],a[72:75],v[60:63]
s_waitcnt lgkmcnt(4)
ds_read_u16 v28,v28
v_lshl_add_u64 v[40:41],v[40:41],0,v[42:43]
v_mfma_f32_16x16x32_bf16 v[60:63],a[104:107],a[88:91],v[60:63]
v_lshl_add_u64 v[42:43],v[36:37],0,v[38:39]
v_mul_f32_dpp v242,v14,v242 row_newbcast:10 row_mask:0xf bank_mask:0xf
v_lshlrev_b32_sdwa v30,1,v30 dst_sel:DWORD dst_unused:UNUSED_PAD src0_sel:DWORD src1_sel:WORD_1
v_mfma_f32_16x16x32_bf16 v[32:35],a[108:111],a[88:91],v[68:71]
ds_read_u16_d16_hi v30,v30
v_mul_f32_dpp v243,v14,v243 row_newbcast:11 row_mask:0xf bank_mask:0xf
v_lshlrev_b32_sdwa v29,1,v29 dst_sel:DWORD dst_unused:UNUSED_PAD src0_sel:DWORD src1_sel:WORD_1
v_mfma_f32_16x16x32_bf16 v[32:35],a[112:115],a[72:75],v[32:35]
ds_read_u16 v29,v29
v_lshl_add_u64 v[36:37],v[52:53],0,v[54:55]
v_mul_f32_dpp v176,v14,v176 row_newbcast:12 row_mask:0xf bank_mask:0xf
v_mfma_f32_16x16x32_bf16 v[32:35],a[116:119],a[56:59],v[32:35]
v_mul_f32_dpp v177,v14,v177 row_newbcast:13 row_mask:0xf bank_mask:0xf
v_lshlrev_b32_sdwa v31,1,v31 dst_sel:DWORD dst_unused:UNUSED_PAD src0_sel:DWORD src1_sel:WORD_1
v_mfma_f32_16x16x32_bf16 v[52:55],a[120:123],a[56:59],v[84:87]
ds_read_u16_d16_hi v31,v31
v_mul_f32_dpp v178,v14,v178 row_newbcast:14 row_mask:0xf bank_mask:0xf
v_lshlrev_b32_sdwa v24,1,v24 dst_sel:DWORD dst_unused:UNUSED_PAD src0_sel:DWORD src1_sel:WORD_1
v_mfma_f32_16x16x32_bf16 v[52:55],a[124:127],a[72:75],v[52:55]
s_waitcnt lgkmcnt(4)
ds_read_u16 v24,v24
v_lshl_add_u64 v[38:39],v[56:57],0,v[58:59]
v_mul_f32_dpp v179,v14,v179 row_newbcast:15 row_mask:0xf bank_mask:0xf
v_lshlrev_b32_sdwa v26,1,v26 dst_sel:DWORD dst_unused:UNUSED_PAD src0_sel:DWORD src1_sel:WORD_1
v_mfma_f32_16x16x32_bf16 v[52:55],a[128:131],a[88:91],v[52:55]
ds_read_u16_d16_hi v26,v26
v_cvt_pk_bf16_f32 v240,v240,v241
v_mfma_f32_16x16x32_bf16 v[56:59],a[132:135],a[88:91],v[80:83]
v_cvt_pk_bf16_f32 v241,v242,v243
v_lshlrev_b32_sdwa v25,1,v25 dst_sel:DWORD dst_unused:UNUSED_PAD src0_sel:DWORD src1_sel:WORD_1
v_mfma_f32_16x16x32_bf16 v[56:59],a[136:139],a[72:75],v[56:59]
ds_read_u16 v25,v25
v_cvt_pk_bf16_f32 v242,v176,v177
v_lshlrev_b32_sdwa v27,1,v27 dst_sel:DWORD dst_unused:UNUSED_PAD src0_sel:DWORD src1_sel:WORD_1
v_mfma_f32_16x16x32_bf16 v[56:59],a[140:143],a[56:59],v[56:59]
ds_read_u16_d16_hi v27,v27
v_cvt_pk_bf16_f32 v243,v178,v179
v_mfma_f32_16x16x32_bf16 v[236:239],a[0:3],v[40:43],v[236:239]
buffer_store_dwordx4 v[240:243],v7,s[40:43],s10 offen offset:64 sc1
v_lshlrev_b32_sdwa v44,1,v44 dst_sel:DWORD dst_unused:UNUSED_PAD src0_sel:DWORD src1_sel:WORD_1
v_mul_f32_dpp v248,v15,v248 row_newbcast:0 row_mask:0xf bank_mask:0xf
v_mfma_f32_16x16x32_bf16 v[236:239],a[4:7],v[36:39],v[236:239]
ds_read_u16 v44,v44
v_mul_f32_dpp v249,v15,v249 row_newbcast:1 row_mask:0xf bank_mask:0xf
v_lshlrev_b32_sdwa v46,1,v46 dst_sel:DWORD dst_unused:UNUSED_PAD src0_sel:DWORD src1_sel:WORD_1
v_mfma_f32_16x16x32_bf16 v[164:167],a[8:11],v[36:39],v[164:167]
ds_read_u16_d16_hi v46,v46
v_mul_f32_dpp v250,v15,v250 row_newbcast:2 row_mask:0xf bank_mask:0xf
v_lshlrev_b32_sdwa v45,1,v45 dst_sel:DWORD dst_unused:UNUSED_PAD src0_sel:DWORD src1_sel:WORD_1
v_mfma_f32_16x16x32_bf16 v[164:167],a[12:15],v[40:43],v[164:167]
ds_read_u16 v45,v45
v_mul_f32_dpp v251,v15,v251 row_newbcast:3 row_mask:0xf bank_mask:0xf
v_mfma_f32_16x16x32_bf16 v[244:247],a[16:19],v[40:43],v[244:247]
v_lshlrev_b32_sdwa v47,1,v47 dst_sel:DWORD dst_unused:UNUSED_PAD src0_sel:DWORD src1_sel:WORD_1
v_mul_f32_dpp v192,v15,v192 row_newbcast:4 row_mask:0xf bank_mask:0xf
v_mfma_f32_16x16x32_bf16 v[244:247],a[20:23],v[36:39],v[244:247]
ds_read_u16_d16_hi v47,v47
v_mul_f32_dpp v193,v15,v193 row_newbcast:5 row_mask:0xf bank_mask:0xf
v_lshlrev_b32_sdwa v48,1,v48 dst_sel:DWORD dst_unused:UNUSED_PAD src0_sel:DWORD src1_sel:WORD_1
v_mfma_f32_16x16x32_bf16 v[180:183],a[24:27],v[36:39],v[180:183]
ds_read_u16 v48,v48
v_mul_f32_dpp v194,v15,v194 row_newbcast:6 row_mask:0xf bank_mask:0xf
v_mfma_f32_16x16x32_bf16 v[180:183],a[28:31],v[40:43],v[180:183]
v_lshlrev_b32_sdwa v50,1,v50 dst_sel:DWORD dst_unused:UNUSED_PAD src0_sel:DWORD src1_sel:WORD_1
v_mul_f32_dpp v195,v15,v195 row_newbcast:7 row_mask:0xf bank_mask:0xf
v_mfma_f32_16x16x32_bf16 v[252:255],a[32:35],v[40:43],v[252:255]
ds_read_u16_d16_hi v50,v50
v_lshlrev_b32_sdwa v49,1,v49 dst_sel:DWORD dst_unused:UNUSED_PAD src0_sel:DWORD src1_sel:WORD_1
v_cvt_pk_bf16_f32 v248,v248,v249
v_mfma_f32_16x16x32_bf16 v[252:255],a[36:39],v[36:39],v[252:255]
ds_read_u16 v49,v49
v_cvt_pk_bf16_f32 v249,v250,v251
v_lshlrev_b32_sdwa v51,1,v51 dst_sel:DWORD dst_unused:UNUSED_PAD src0_sel:DWORD src1_sel:WORD_1
v_mfma_f32_16x16x32_bf16 v[196:199],a[40:43],v[36:39],v[196:199]
ds_read_u16_d16_hi v51,v51
v_cvt_pk_bf16_f32 v250,v192,v193
v_mfma_f32_16x16x32_bf16 v[196:199],a[44:47],v[40:43],v[196:199]
v_cvt_pk_bf16_f32 v251,v194,v195
v_mfma_f32_16x16x32_bf16 v[36:39],a[144:147],a[56:59],v[96:99]
v_lshlrev_b32_sdwa v60,1,v60 dst_sel:DWORD dst_unused:UNUSED_PAD src0_sel:DWORD src1_sel:WORD_1
v_lshlrev_b32_sdwa v62,1,v62 dst_sel:DWORD dst_unused:UNUSED_PAD src0_sel:DWORD src1_sel:WORD_1
v_mfma_f32_16x16x32_bf16 v[36:39],a[148:151],a[72:75],v[36:39]
s_waitcnt lgkmcnt(5)
buffer_store_dwordx4 v[248:251],v7,s[40:43],s10 offen offset:128 sc1
v_lshl_add_u64 v[28:29],v[28:29],0,v[30:31]
v_mfma_f32_16x16x32_bf16 v[36:39],a[152:155],a[88:91],v[36:39]
TP10730_%=:
s_cbranch_execz TR1_%=
ds_read_u16 v60,v60
v_lshl_add_u64 v[30:31],v[24:25],0,v[26:27]
v_mfma_f32_16x16x32_bf16 v[40:43],a[156:159],a[88:91],v[100:103]
s_waitcnt lgkmcnt(4)
ds_read_u16_d16_hi v62,v62
v_lshl_add_u64 v[24:25],v[44:45],0,v[46:47]
v_lshlrev_b32_sdwa v61,1,v61 dst_sel:DWORD dst_unused:UNUSED_PAD src0_sel:DWORD src1_sel:WORD_1
v_mfma_f32_16x16x32_bf16 v[40:43],a[160:163],a[72:75],v[40:43]
s_waitcnt lgkmcnt(2)
ds_read_u16 v61,v61
v_lshl_add_u64 v[26:27],v[48:49],0,v[50:51]
v_mfma_f32_16x16x32_bf16 v[40:43],a[164:167],a[56:59],v[40:43]
v_lshlrev_b32_sdwa v63,1,v63 dst_sel:DWORD dst_unused:UNUSED_PAD src0_sel:DWORD src1_sel:WORD_1
v_lshlrev_b32_sdwa v32,1,v32 dst_sel:DWORD dst_unused:UNUSED_PAD src0_sel:DWORD src1_sel:WORD_1
v_mfma_f32_16x16x32_bf16 v[236:239],a[212:215],v[28:31],v[236:239]
ds_read_u16_d16_hi v63,v63
v_lshlrev_b32_sdwa v34,1,v34 dst_sel:DWORD dst_unused:UNUSED_PAD src0_sel:DWORD src1_sel:WORD_1
v_mfma_f32_16x16x32_bf16 v[236:239],a[200:203],v[24:27],v[236:239]
ds_read_u16 v32,v32
v_lshlrev_b32_sdwa v33,1,v33 dst_sel:DWORD dst_unused:UNUSED_PAD src0_sel:DWORD src1_sel:WORD_1
v_mfma_f32_16x16x32_bf16 v[164:167],a[196:199],v[24:27],v[164:167]
ds_read_u16_d16_hi v34,v34
v_lshlrev_b32_sdwa v35,1,v35 dst_sel:DWORD dst_unused:UNUSED_PAD src0_sel:DWORD src1_sel:WORD_1
v_mfma_f32_16x16x32_bf16 v[164:167],a[192:195],v[28:31],v[164:167]
ds_read_u16 v33,v33
v_lshlrev_b32_sdwa v56,1,v56 dst_sel:DWORD dst_unused:UNUSED_PAD src0_sel:DWORD src1_sel:WORD_1
v_mfma_f32_16x16x32_bf16 v[244:247],a[216:219],v[28:31],v[244:247]
ds_read_u16_d16_hi v35,v35
v_lshlrev_b32_sdwa v58,1,v58 dst_sel:DWORD dst_unused:UNUSED_PAD src0_sel:DWORD src1_sel:WORD_1
v_mfma_f32_16x16x32_bf16 v[244:247],a[220:223],v[24:27],v[244:247]
ds_read_u16 v56,v56
v_mul_f32_dpp v236,v14,v236 row_newbcast:0 row_mask:0xf bank_mask:0xf
v_mfma_f32_16x16x32_bf16 v[180:183],a[204:207],v[24:27],v[180:183]
ds_read_u16_d16_hi v58,v58
v_mul_f32_dpp v237,v14,v237 row_newbcast:1 row_mask:0xf bank_mask:0xf
v_mfma_f32_16x16x32_bf16 v[180:183],a[208:211],v[28:31],v[180:183]
v_mul_f32_dpp v238,v14,v238 row_newbcast:2 row_mask:0xf bank_mask:0xf
v_lshlrev_b32_sdwa v57,1,v57 dst_sel:DWORD dst_unused:UNUSED_PAD src0_sel:DWORD src1_sel:WORD_1
v_mfma_f32_16x16x32_bf16 v[252:255],a[244:247],v[28:31],v[252:255]
ds_read_u16 v57,v57
v_mul_f32_dpp v239,v14,v239 row_newbcast:3 row_mask:0xf bank_mask:0xf
v_mfma_f32_16x16x32_bf16 v[252:255],a[252:255],v[24:27],v[252:255]
v_mul_f32_dpp v164,v14,v164 row_newbcast:4 row_mask:0xf bank_mask:0xf
v_lshlrev_b32_sdwa v59,1,v59 dst_sel:DWORD dst_unused:UNUSED_PAD src0_sel:DWORD src1_sel:WORD_1
v_mfma_f32_16x16x32_bf16 v[196:199],a[232:235],v[24:27],v[196:199]
ds_read_u16_d16_hi v59,v59
v_mul_f32_dpp v165,v14,v165 row_newbcast:5 row_mask:0xf bank_mask:0xf
v_mfma_f32_16x16x32_bf16 v[196:199],a[240:243],v[28:31],v[196:199]
v_mul_f32_dpp v166,v14,v166 row_newbcast:6 row_mask:0xf bank_mask:0xf
v_lshlrev_b32_sdwa v52,1,v52 dst_sel:DWORD dst_unused:UNUSED_PAD src0_sel:DWORD src1_sel:WORD_1
v_mfma_f32_16x16x32_bf16 v[44:47],a[168:171],a[56:59],v[16:19]
ds_read_u16 v52,v52
v_mul_f32_dpp v167,v14,v167 row_newbcast:7 row_mask:0xf bank_mask:0xf
v_mfma_f32_16x16x32_bf16 v[44:47],a[172:175],a[72:75],v[44:47]
v_cvt_pk_bf16_f32 v236,v236,v237
v_lshlrev_b32_sdwa v54,1,v54 dst_sel:DWORD dst_unused:UNUSED_PAD src0_sel:DWORD src1_sel:WORD_1
v_mfma_f32_16x16x32_bf16 v[44:47],a[176:179],a[88:91],v[44:47]
ds_read_u16_d16_hi v54,v54
v_cvt_pk_bf16_f32 v237,v238,v239
v_lshlrev_b32_sdwa v53,1,v53 dst_sel:DWORD dst_unused:UNUSED_PAD src0_sel:DWORD src1_sel:WORD_1
v_mfma_f32_16x16x32_bf16 v[48:51],a[180:183],a[88:91],v[20:23]
ds_read_u16 v53,v53
v_cvt_pk_bf16_f32 v238,v164,v165
v_lshlrev_b32_sdwa v55,1,v55 dst_sel:DWORD dst_unused:UNUSED_PAD src0_sel:DWORD src1_sel:WORD_1
v_mfma_f32_16x16x32_bf16 v[48:51],a[184:187],a[72:75],v[48:51]
ds_read_u16_d16_hi v55,v55
v_cvt_pk_bf16_f32 v239,v166,v167
v_mfma_f32_16x16x32_bf16 v[48:51],a[188:191],a[56:59],v[48:51]
buffer_store_dwordx4 v[236:239],v7,s[40:43],s10 offen offset:192 sc1
v_mul_f32_dpp v244,v14,v244 row_newbcast:8 row_mask:0xf bank_mask:0xf
v_mfma_f32_16x16x32_bf16 v[24:27],a[96:99],a[60:63],v[64:67]
v_lshlrev_b32_sdwa v36,1,v36 dst_sel:DWORD dst_unused:UNUSED_PAD src0_sel:DWORD src1_sel:WORD_1
v_mul_f32_dpp v245,v14,v245 row_newbcast:9 row_mask:0xf bank_mask:0xf
v_mfma_f32_16x16x32_bf16 v[24:27],a[100:103],a[76:79],v[24:27]
s_waitcnt lgkmcnt(4)
ds_read_u16 v36,v36
v_lshl_add_u64 v[60:61],v[60:61],0,v[62:63]
v_mfma_f32_16x16x32_bf16 v[24:27],a[104:107],a[92:95],v[24:27]
v_lshl_add_u64 v[62:63],v[32:33],0,v[34:35]
v_mul_f32_dpp v246,v14,v246 row_newbcast:10 row_mask:0xf bank_mask:0xf
v_lshlrev_b32_sdwa v38,1,v38 dst_sel:DWORD dst_unused:UNUSED_PAD src0_sel:DWORD src1_sel:WORD_1
v_mfma_f32_16x16x32_bf16 v[28:31],a[108:111],a[92:95],v[68:71]
ds_read_u16_d16_hi v38,v38
v_mul_f32_dpp v247,v14,v247 row_newbcast:11 row_mask:0xf bank_mask:0xf
v_lshlrev_b32_sdwa v37,1,v37 dst_sel:DWORD dst_unused:UNUSED_PAD src0_sel:DWORD src1_sel:WORD_1
v_mfma_f32_16x16x32_bf16 v[28:31],a[112:115],a[76:79],v[28:31]
ds_read_u16 v37,v37
v_lshl_add_u64 v[32:33],v[56:57],0,v[58:59]
v_mul_f32_dpp v180,v14,v180 row_newbcast:12 row_mask:0xf bank_mask:0xf
v_mfma_f32_16x16x32_bf16 v[28:31],a[116:119],a[60:63],v[28:31]
v_mul_f32_dpp v181,v14,v181 row_newbcast:13 row_mask:0xf bank_mask:0xf
v_lshlrev_b32_sdwa v39,1,v39 dst_sel:DWORD dst_unused:UNUSED_PAD src0_sel:DWORD src1_sel:WORD_1
v_mfma_f32_16x16x32_bf16 v[56:59],a[120:123],a[60:63],v[84:87]
ds_read_u16_d16_hi v39,v39
v_mul_f32_dpp v182,v14,v182 row_newbcast:14 row_mask:0xf bank_mask:0xf
v_lshlrev_b32_sdwa v40,1,v40 dst_sel:DWORD dst_unused:UNUSED_PAD src0_sel:DWORD src1_sel:WORD_1
v_mfma_f32_16x16x32_bf16 v[56:59],a[124:127],a[76:79],v[56:59]
s_waitcnt lgkmcnt(4)
ds_read_u16 v40,v40
v_lshl_add_u64 v[34:35],v[52:53],0,v[54:55]
v_mul_f32_dpp v183,v14,v183 row_newbcast:15 row_mask:0xf bank_mask:0xf
v_lshlrev_b32_sdwa v42,1,v42 dst_sel:DWORD dst_unused:UNUSED_PAD src0_sel:DWORD src1_sel:WORD_1
v_mfma_f32_16x16x32_bf16 v[56:59],a[128:131],a[92:95],v[56:59]
ds_read_u16_d16_hi v42,v42
v_cvt_pk_bf16_f32 v244,v244,v245
v_mfma_f32_16x16x32_bf16 v[52:55],a[132:135],a[92:95],v[80:83]
v_cvt_pk_bf16_f32 v245,v246,v247
v_lshlrev_b32_sdwa v41,1,v41 dst_sel:DWORD dst_unused:UNUSED_PAD src0_sel:DWORD src1_sel:WORD_1
v_mfma_f32_16x16x32_bf16 v[52:55],a[136:139],a[76:79],v[52:55]
ds_read_u16 v41,v41
v_cvt_pk_bf16_f32 v246,v180,v181
v_lshlrev_b32_sdwa v43,1,v43 dst_sel:DWORD dst_unused:UNUSED_PAD src0_sel:DWORD src1_sel:WORD_1
v_mfma_f32_16x16x32_bf16 v[52:55],a[140:143],a[60:63],v[52:55]
ds_read_u16_d16_hi v43,v43
v_cvt_pk_bf16_f32 v247,v182,v183
v_mfma_f32_16x16x32_bf16 v[208:211],a[0:3],v[60:63],v[208:211]
buffer_store_dwordx4 v[244:247],v7,s[40:43],s10 offen offset:256 sc1
v_lshlrev_b32_sdwa v48,1,v48 dst_sel:DWORD dst_unused:UNUSED_PAD src0_sel:DWORD src1_sel:WORD_1
v_mul_f32_dpp v252,v15,v252 row_newbcast:0 row_mask:0xf bank_mask:0xf
v_mfma_f32_16x16x32_bf16 v[208:211],a[4:7],v[32:35],v[208:211]
ds_read_u16 v48,v48
v_mul_f32_dpp v253,v15,v253 row_newbcast:1 row_mask:0xf bank_mask:0xf
v_lshlrev_b32_sdwa v50,1,v50 dst_sel:DWORD dst_unused:UNUSED_PAD src0_sel:DWORD src1_sel:WORD_1
v_mfma_f32_16x16x32_bf16 v[168:171],a[8:11],v[32:35],v[168:171]
ds_read_u16_d16_hi v50,v50
v_mul_f32_dpp v254,v15,v254 row_newbcast:2 row_mask:0xf bank_mask:0xf
v_lshlrev_b32_sdwa v49,1,v49 dst_sel:DWORD dst_unused:UNUSED_PAD src0_sel:DWORD src1_sel:WORD_1
v_mfma_f32_16x16x32_bf16 v[168:171],a[12:15],v[60:63],v[168:171]
ds_read_u16 v49,v49
v_mul_f32_dpp v255,v15,v255 row_newbcast:3 row_mask:0xf bank_mask:0xf
v_mfma_f32_16x16x32_bf16 v[216:219],a[16:19],v[60:63],v[216:219]
v_lshlrev_b32_sdwa v51,1,v51 dst_sel:DWORD dst_unused:UNUSED_PAD src0_sel:DWORD src1_sel:WORD_1
v_mul_f32_dpp v196,v15,v196 row_newbcast:4 row_mask:0xf bank_mask:0xf
v_mfma_f32_16x16x32_bf16 v[216:219],a[20:23],v[32:35],v[216:219]
ds_read_u16_d16_hi v51,v51
v_mul_f32_dpp v197,v15,v197 row_newbcast:5 row_mask:0xf bank_mask:0xf
v_lshlrev_b32_sdwa v44,1,v44 dst_sel:DWORD dst_unused:UNUSED_PAD src0_sel:DWORD src1_sel:WORD_1
v_mfma_f32_16x16x32_bf16 v[184:187],a[24:27],v[32:35],v[184:187]
ds_read_u16 v44,v44
v_mul_f32_dpp v198,v15,v198 row_newbcast:6 row_mask:0xf bank_mask:0xf
v_mfma_f32_16x16x32_bf16 v[184:187],a[28:31],v[60:63],v[184:187]
v_lshlrev_b32_sdwa v46,1,v46 dst_sel:DWORD dst_unused:UNUSED_PAD src0_sel:DWORD src1_sel:WORD_1
v_mul_f32_dpp v199,v15,v199 row_newbcast:7 row_mask:0xf bank_mask:0xf
v_mfma_f32_16x16x32_bf16 v[224:227],a[32:35],v[60:63],v[224:227]
ds_read_u16_d16_hi v46,v46
v_lshlrev_b32_sdwa v45,1,v45 dst_sel:DWORD dst_unused:UNUSED_PAD src0_sel:DWORD src1_sel:WORD_1
v_cvt_pk_bf16_f32 v252,v252,v253
v_mfma_f32_16x16x32_bf16 v[224:227],a[36:39],v[32:35],v[224:227]
ds_read_u16 v45,v45
v_cvt_pk_bf16_f32 v253,v254,v255
v_lshlrev_b32_sdwa v47,1,v47 dst_sel:DWORD dst_unused:UNUSED_PAD src0_sel:DWORD src1_sel:WORD_1
v_mfma_f32_16x16x32_bf16 v[200:203],a[40:43],v[32:35],v[200:203]
ds_read_u16_d16_hi v47,v47
v_cvt_pk_bf16_f32 v254,v196,v197
v_mfma_f32_16x16x32_bf16 v[200:203],a[44:47],v[60:63],v[200:203]
v_cvt_pk_bf16_f32 v255,v198,v199
v_mov_b32 v10,0
v_mfma_f32_16x16x32_bf16 v[32:35],a[144:147],a[60:63],v[96:99]
ds_read_b128 v[72:75],v10 offset:11648
v_lshlrev_b32_sdwa v24,1,v24 dst_sel:DWORD dst_unused:UNUSED_PAD src0_sel:DWORD src1_sel:WORD_1
v_mfma_f32_16x16x32_bf16 v[32:35],a[148:151],a[76:79],v[32:35]
s_waitcnt lgkmcnt(6)
buffer_store_dwordx4 v[252:255],v7,s[40:43],s10 offen offset:320 sc1
v_lshl_add_u64 v[36:37],v[36:37],0,v[38:39]
v_mfma_f32_16x16x32_bf16 v[32:35],a[152:155],a[92:95],v[32:35]
ds_read_u16 v24,v24
v_lshl_add_u64 v[38:39],v[40:41],0,v[42:43]
v_lshlrev_b32_sdwa v26,1,v26 dst_sel:DWORD dst_unused:UNUSED_PAD src0_sel:DWORD src1_sel:WORD_1
v_mfma_f32_16x16x32_bf16 v[60:63],a[156:159],a[92:95],v[100:103]
s_waitcnt lgkmcnt(5)
ds_read_u16_d16_hi v26,v26
v_lshl_add_u64 v[40:41],v[48:49],0,v[50:51]
v_lshlrev_b32_sdwa v25,1,v25 dst_sel:DWORD dst_unused:UNUSED_PAD src0_sel:DWORD src1_sel:WORD_1
v_mfma_f32_16x16x32_bf16 v[60:63],a[160:163],a[76:79],v[60:63]
s_waitcnt lgkmcnt(3)
ds_read_u16 v25,v25
v_lshl_add_u64 v[42:43],v[44:45],0,v[46:47]
v_mfma_f32_16x16x32_bf16 v[60:63],a[164:167],a[60:63],v[60:63]
v_lshlrev_b32_sdwa v27,1,v27 dst_sel:DWORD dst_unused:UNUSED_PAD src0_sel:DWORD src1_sel:WORD_1
v_lshlrev_b32_sdwa v28,1,v28 dst_sel:DWORD dst_unused:UNUSED_PAD src0_sel:DWORD src1_sel:WORD_1
v_mfma_f32_16x16x32_bf16 v[208:211],a[212:215],v[36:39],v[208:211]
ds_read_u16_d16_hi v27,v27
v_lshlrev_b32_sdwa v30,1,v30 dst_sel:DWORD dst_unused:UNUSED_PAD src0_sel:DWORD src1_sel:WORD_1
v_mfma_f32_16x16x32_bf16 v[208:211],a[200:203],v[40:43],v[208:211]
ds_read_u16 v28,v28
v_lshlrev_b32_sdwa v29,1,v29 dst_sel:DWORD dst_unused:UNUSED_PAD src0_sel:DWORD src1_sel:WORD_1
v_mfma_f32_16x16x32_bf16 v[168:171],a[196:199],v[40:43],v[168:171]
ds_read_u16_d16_hi v30,v30
v_lshlrev_b32_sdwa v31,1,v31 dst_sel:DWORD dst_unused:UNUSED_PAD src0_sel:DWORD src1_sel:WORD_1
v_mfma_f32_16x16x32_bf16 v[168:171],a[192:195],v[36:39],v[168:171]
ds_read_u16 v29,v29
v_lshlrev_b32_sdwa v52,1,v52 dst_sel:DWORD dst_unused:UNUSED_PAD src0_sel:DWORD src1_sel:WORD_1
v_mfma_f32_16x16x32_bf16 v[216:219],a[216:219],v[36:39],v[216:219]
ds_read_u16_d16_hi v31,v31
v_lshlrev_b32_sdwa v54,1,v54 dst_sel:DWORD dst_unused:UNUSED_PAD src0_sel:DWORD src1_sel:WORD_1
v_mfma_f32_16x16x32_bf16 v[216:219],a[220:223],v[40:43],v[216:219]
ds_read_u16 v52,v52
v_mul_f32_dpp v208,v14,v208 row_newbcast:0 row_mask:0xf bank_mask:0xf
s_add_u32 s11,s61,4816896
v_mul_f32_dpp v209,v14,v209 row_newbcast:1 row_mask:0xf bank_mask:0xf
v_mul_f32_dpp v210,v14,v210 row_newbcast:2 row_mask:0xf bank_mask:0xf
v_mfma_f32_16x16x32_bf16 v[184:187],a[204:207],v[40:43],v[184:187]
ds_read_u16_d16_hi v54,v54
v_mul_f32_dpp v211,v14,v211 row_newbcast:3 row_mask:0xf bank_mask:0xf
v_mul_f32_dpp v168,v14,v168 row_newbcast:4 row_mask:0xf bank_mask:0xf
v_mul_f32_dpp v169,v14,v169 row_newbcast:5 row_mask:0xf bank_mask:0xf
v_mfma_f32_16x16x32_bf16 v[184:187],a[208:211],v[36:39],v[184:187]
v_mul_f32_dpp v170,v14,v170 row_newbcast:6 row_mask:0xf bank_mask:0xf
v_mul_f32_dpp v171,v14,v171 row_newbcast:7 row_mask:0xf bank_mask:0xf
v_cvt_pk_bf16_f32 v208,v208,v209
v_mfma_f32_16x16x32_bf16 v[224:227],a[244:247],v[36:39],v[224:227]
v_cvt_pk_bf16_f32 v209,v210,v211
v_cvt_pk_bf16_f32 v210,v168,v169
v_cvt_pk_bf16_f32 v211,v170,v171
v_lshlrev_b32_sdwa v53,1,v53 dst_sel:DWORD dst_unused:UNUSED_PAD src0_sel:DWORD src1_sel:WORD_1
v_mfma_f32_16x16x32_bf16 v[224:227],a[252:255],v[40:43],v[224:227]
buffer_store_dwordx4 v[208:211],v7,s[40:43],s11 offen offset:0 sc1
v_mul_f32_dpp v216,v14,v216 row_newbcast:8 row_mask:0xf bank_mask:0xf
v_mul_f32_dpp v217,v14,v217 row_newbcast:9 row_mask:0xf bank_mask:0xf
v_mul_f32_dpp v218,v14,v218 row_newbcast:10 row_mask:0xf bank_mask:0xf
v_mfma_f32_16x16x32_bf16 v[200:203],a[232:235],v[40:43],v[200:203]
ds_read_u16 v53,v53
v_mul_f32_dpp v219,v14,v219 row_newbcast:11 row_mask:0xf bank_mask:0xf
v_mul_f32_dpp v184,v14,v184 row_newbcast:12 row_mask:0xf bank_mask:0xf
v_mul_f32_dpp v185,v14,v185 row_newbcast:13 row_mask:0xf bank_mask:0xf
v_lshlrev_b32_sdwa v55,1,v55 dst_sel:DWORD dst_unused:UNUSED_PAD src0_sel:DWORD src1_sel:WORD_1
v_mfma_f32_16x16x32_bf16 v[200:203],a[240:243],v[36:39],v[200:203]
ds_read_u16_d16_hi v55,v55
v_mul_f32_dpp v186,v14,v186 row_newbcast:14 row_mask:0xf bank_mask:0xf
v_mul_f32_dpp v187,v14,v187 row_newbcast:15 row_mask:0xf bank_mask:0xf
v_cvt_pk_bf16_f32 v216,v216,v217
v_lshlrev_b32_sdwa v56,1,v56 dst_sel:DWORD dst_unused:UNUSED_PAD src0_sel:DWORD src1_sel:WORD_1
v_mfma_f32_16x16x32_bf16 v[48:51],a[168:171],a[60:63],v[16:19]
ds_read_u16 v56,v56
v_cvt_pk_bf16_f32 v217,v218,v219
v_cvt_pk_bf16_f32 v218,v184,v185
v_cvt_pk_bf16_f32 v219,v186,v187
v_mfma_f32_16x16x32_bf16 v[48:51],a[172:175],a[76:79],v[48:51]
v_mul_f32_dpp v224,v15,v224 row_newbcast:0 row_mask:0xf bank_mask:0xf
v_mul_f32_dpp v225,v15,v225 row_newbcast:1 row_mask:0xf bank_mask:0xf
v_mul_f32_dpp v226,v15,v226 row_newbcast:2 row_mask:0xf bank_mask:0xf
v_mul_f32_dpp v227,v15,v227 row_newbcast:3 row_mask:0xf bank_mask:0xf
v_lshlrev_b32_sdwa v58,1,v58 dst_sel:DWORD dst_unused:UNUSED_PAD src0_sel:DWORD src1_sel:WORD_1
v_mfma_f32_16x16x32_bf16 v[48:51],a[176:179],a[92:95],v[48:51]
ds_read_u16_d16_hi v58,v58
v_mul_f32_dpp v200,v15,v200 row_newbcast:4 row_mask:0xf bank_mask:0xf
v_mul_f32_dpp v201,v15,v201 row_newbcast:5 row_mask:0xf bank_mask:0xf
v_mul_f32_dpp v202,v15,v202 row_newbcast:6 row_mask:0xf bank_mask:0xf
v_lshlrev_b32_sdwa v57,1,v57 dst_sel:DWORD dst_unused:UNUSED_PAD src0_sel:DWORD src1_sel:WORD_1
v_mfma_f32_16x16x32_bf16 v[44:47],a[180:183],a[92:95],v[20:23]
ds_read_u16 v57,v57
v_mul_f32_dpp v203,v15,v203 row_newbcast:7 row_mask:0xf bank_mask:0xf
v_cvt_pk_bf16_f32 v224,v224,v225
v_cvt_pk_bf16_f32 v225,v226,v227
v_lshlrev_b32_sdwa v59,1,v59 dst_sel:DWORD dst_unused:UNUSED_PAD src0_sel:DWORD src1_sel:WORD_1
v_mfma_f32_16x16x32_bf16 v[44:47],a[184:187],a[76:79],v[44:47]
ds_read_u16_d16_hi v59,v59
v_cvt_pk_bf16_f32 v226,v200,v201
v_cvt_pk_bf16_f32 v227,v202,v203
v_mfma_f32_16x16x32_bf16 v[44:47],a[188:191],a[60:63],v[44:47]
buffer_store_dwordx4 v[216:219],v7,s[40:43],s11 offen offset:64 sc1
s_waitcnt lgkmcnt(12)
v_lshl_add_u64 v[24:25],v[24:25],0,v[26:27]
s_waitcnt lgkmcnt(8)
v_lshl_add_u64 v[26:27],v[28:29],0,v[30:31]
s_waitcnt lgkmcnt(4)
v_lshl_add_u64 v[28:29],v[52:53],0,v[54:55]
s_waitcnt lgkmcnt(0)
v_lshl_add_u64 v[30:31],v[56:57],0,v[58:59]
v_mfma_f32_16x16x32_bf16 v[212:215],a[0:3],v[24:27],v[212:215]
v_lshlrev_b32_sdwa v32,1,v32 dst_sel:DWORD dst_unused:UNUSED_PAD src0_sel:DWORD src1_sel:WORD_1
v_lshlrev_b32_sdwa v34,1,v34 dst_sel:DWORD dst_unused:UNUSED_PAD src0_sel:DWORD src1_sel:WORD_1
v_mfma_f32_16x16x32_bf16 v[212:215],a[4:7],v[28:31],v[212:215]
ds_read_u16 v32,v32
v_lshlrev_b32_sdwa v33,1,v33 dst_sel:DWORD dst_unused:UNUSED_PAD src0_sel:DWORD src1_sel:WORD_1
v_mfma_f32_16x16x32_bf16 v[172:175],a[8:11],v[28:31],v[172:175]
ds_read_u16_d16_hi v34,v34
v_lshlrev_b32_sdwa v35,1,v35 dst_sel:DWORD dst_unused:UNUSED_PAD src0_sel:DWORD src1_sel:WORD_1
v_mfma_f32_16x16x32_bf16 v[172:175],a[12:15],v[24:27],v[172:175]
ds_read_u16 v33,v33
v_lshlrev_b32_sdwa v60,1,v60 dst_sel:DWORD dst_unused:UNUSED_PAD src0_sel:DWORD src1_sel:WORD_1
v_mfma_f32_16x16x32_bf16 v[220:223],a[16:19],v[24:27],v[220:223]
ds_read_u16_d16_hi v35,v35
v_lshlrev_b32_sdwa v62,1,v62 dst_sel:DWORD dst_unused:UNUSED_PAD src0_sel:DWORD src1_sel:WORD_1
v_mfma_f32_16x16x32_bf16 v[220:223],a[20:23],v[28:31],v[220:223]
ds_read_u16 v60,v60
v_lshlrev_b32_sdwa v61,1,v61 dst_sel:DWORD dst_unused:UNUSED_PAD src0_sel:DWORD src1_sel:WORD_1
v_lshlrev_b32_sdwa v63,1,v63 dst_sel:DWORD dst_unused:UNUSED_PAD src0_sel:DWORD src1_sel:WORD_1
v_mfma_f32_16x16x32_bf16 v[188:191],a[24:27],v[28:31],v[188:191]
ds_read_u16_d16_hi v62,v62
ds_read_u16 v61,v61
v_lshlrev_b32_sdwa v44,1,v44 dst_sel:DWORD dst_unused:UNUSED_PAD src0_sel:DWORD src1_sel:WORD_1
v_mfma_f32_16x16x32_bf16 v[188:191],a[28:31],v[24:27],v[188:191]
ds_read_u16_d16_hi v63,v63
v_lshlrev_b32_sdwa v46,1,v46 dst_sel:DWORD dst_unused:UNUSED_PAD src0_sel:DWORD src1_sel:WORD_1
v_lshlrev_b32_sdwa v45,1,v45 dst_sel:DWORD dst_unused:UNUSED_PAD src0_sel:DWORD src1_sel:WORD_1
v_mfma_f32_16x16x32_bf16 v[228:231],a[32:35],v[24:27],v[228:231]
ds_read_u16 v44,v44
ds_read_u16_d16_hi v46,v46
v_lshlrev_b32_sdwa v47,1,v47 dst_sel:DWORD dst_unused:UNUSED_PAD src0_sel:DWORD src1_sel:WORD_1
v_mfma_f32_16x16x32_bf16 v[228:231],a[36:39],v[28:31],v[228:231]
buffer_store_dwordx4 v[224:227],v7,s[40:43],s11 offen offset:128 sc1
ds_read_u16 v45,v45
v_lshlrev_b32_sdwa v48,1,v48 dst_sel:DWORD dst_unused:UNUSED_PAD src0_sel:DWORD src1_sel:WORD_1
v_mfma_f32_16x16x32_bf16 v[204:207],a[40:43],v[28:31],v[204:207]
ds_read_u16_d16_hi v47,v47
v_lshlrev_b32_sdwa v50,1,v50 dst_sel:DWORD dst_unused:UNUSED_PAD src0_sel:DWORD src1_sel:WORD_1
v_lshlrev_b32_sdwa v49,1,v49 dst_sel:DWORD dst_unused:UNUSED_PAD src0_sel:DWORD src1_sel:WORD_1
v_mfma_f32_16x16x32_bf16 v[204:207],a[44:47],v[24:27],v[204:207]
ds_read_u16 v48,v48
ds_read_u16_d16_hi v50,v50
v_lshlrev_b32_sdwa v51,1,v51 dst_sel:DWORD dst_unused:UNUSED_PAD src0_sel:DWORD src1_sel:WORD_1
ds_read_u16 v49,v49
ds_read_u16_d16_hi v51,v51
s_waitcnt lgkmcnt(12)
v_lshl_add_u64 v[32:33],v[32:33],0,v[34:35]
s_waitcnt lgkmcnt(8)
v_lshl_add_u64 v[34:35],v[60:61],0,v[62:63]
s_waitcnt lgkmcnt(4)
v_lshl_add_u64 v[60:61],v[44:45],0,v[46:47]
s_waitcnt lgkmcnt(0)
v_lshl_add_u64 v[62:63],v[48:49],0,v[50:51]
v_mfma_f32_16x16x32_bf16 v[212:215],a[212:215],v[32:35],v[212:215]
s_nop 0
v_mfma_f32_16x16x32_bf16 v[212:215],a[200:203],v[60:63],v[212:215]
v_mfma_f32_16x16x32_bf16 v[172:175],a[196:199],v[60:63],v[172:175]
v_mfma_f32_16x16x32_bf16 v[172:175],a[192:195],v[32:35],v[172:175]
v_mfma_f32_16x16x32_bf16 v[220:223],a[216:219],v[32:35],v[220:223]
v_mfma_f32_16x16x32_bf16 v[220:223],a[220:223],v[60:63],v[220:223]
v_mfma_f32_16x16x32_bf16 v[188:191],a[204:207],v[60:63],v[188:191]
s_nop 2
v_mul_f32_dpp v212,v14,v212 row_newbcast:0 row_mask:0xf bank_mask:0xf
v_mul_f32_dpp v213,v14,v213 row_newbcast:1 row_mask:0xf bank_mask:0xf
v_mul_f32_dpp v214,v14,v214 row_newbcast:2 row_mask:0xf bank_mask:0xf
v_mfma_f32_16x16x32_bf16 v[188:191],a[208:211],v[32:35],v[188:191]
v_mul_f32_dpp v215,v14,v215 row_newbcast:3 row_mask:0xf bank_mask:0xf
v_mul_f32_dpp v172,v14,v172 row_newbcast:4 row_mask:0xf bank_mask:0xf
v_mul_f32_dpp v173,v14,v173 row_newbcast:5 row_mask:0xf bank_mask:0xf
v_mfma_f32_16x16x32_bf16 v[228:231],a[244:247],v[32:35],v[228:231]
v_mul_f32_dpp v174,v14,v174 row_newbcast:6 row_mask:0xf bank_mask:0xf
v_mul_f32_dpp v175,v14,v175 row_newbcast:7 row_mask:0xf bank_mask:0xf
v_cvt_pk_bf16_f32 v212,v212,v213
v_mfma_f32_16x16x32_bf16 v[228:231],a[252:255],v[60:63],v[228:231]
v_cvt_pk_bf16_f32 v213,v214,v215
v_cvt_pk_bf16_f32 v214,v172,v173
v_cvt_pk_bf16_f32 v215,v174,v175
buffer_store_dwordx4 v[212:215],v7,s[40:43],s11 offen offset:192 sc1
v_mfma_f32_16x16x32_bf16 v[204:207],a[232:235],v[60:63],v[204:207]
v_mul_f32_dpp v220,v14,v220 row_newbcast:8 row_mask:0xf bank_mask:0xf
v_mul_f32_dpp v221,v14,v221 row_newbcast:9 row_mask:0xf bank_mask:0xf
v_mul_f32_dpp v222,v14,v222 row_newbcast:10 row_mask:0xf bank_mask:0xf
v_mfma_f32_16x16x32_bf16 v[204:207],a[240:243],v[32:35],v[204:207]
v_mul_f32_dpp v223,v14,v223 row_newbcast:11 row_mask:0xf bank_mask:0xf
v_mul_f32_dpp v188,v14,v188 row_newbcast:12 row_mask:0xf bank_mask:0xf
v_mul_f32_dpp v189,v14,v189 row_newbcast:13 row_mask:0xf bank_mask:0xf
v_mul_f32_dpp v190,v14,v190 row_newbcast:14 row_mask:0xf bank_mask:0xf
v_mul_f32_dpp v191,v14,v191 row_newbcast:15 row_mask:0xf bank_mask:0xf
v_cvt_pk_bf16_f32 v220,v220,v221
v_cvt_pk_bf16_f32 v221,v222,v223
v_cvt_pk_bf16_f32 v222,v188,v189
v_cvt_pk_bf16_f32 v223,v190,v191
buffer_store_dwordx4 v[220:223],v7,s[40:43],s11 offen offset:256 sc1
v_mul_f32_dpp v228,v15,v228 row_newbcast:0 row_mask:0xf bank_mask:0xf
v_mul_f32_dpp v229,v15,v229 row_newbcast:1 row_mask:0xf bank_mask:0xf
v_mul_f32_dpp v230,v15,v230 row_newbcast:2 row_mask:0xf bank_mask:0xf
v_mul_f32_dpp v231,v15,v231 row_newbcast:3 row_mask:0xf bank_mask:0xf
v_mul_f32_dpp v204,v15,v204 row_newbcast:4 row_mask:0xf bank_mask:0xf
v_mul_f32_dpp v205,v15,v205 row_newbcast:5 row_mask:0xf bank_mask:0xf
v_mul_f32_dpp v206,v15,v206 row_newbcast:6 row_mask:0xf bank_mask:0xf
v_mul_f32_dpp v207,v15,v207 row_newbcast:7 row_mask:0xf bank_mask:0xf
v_cvt_pk_bf16_f32 v228,v228,v229
v_cvt_pk_bf16_f32 v229,v230,v231
v_cvt_pk_bf16_f32 v230,v204,v205
v_cvt_pk_bf16_f32 v231,v206,v207
buffer_store_dwordx4 v[228:231],v7,s[40:43],s11 offen offset:320 sc1
v_or3_b32 v72,v72,v73,v74
v_or_b32 v72,v72,v75
s_nop 0
v_readfirstlane_b32 s64,v72
s_cmp_lg_u32 s64,0
s_cbranch_scc1 N_fallback_%=
s_endpgm
N_mainend_%=:
.fill 512,4,0xbf800000
N_fallback_%=:
s_waitcnt vmcnt(0) lgkmcnt(0)
s_barrier
)ASM"
    :: "s"(blockIdx.x), "v"(tid), "s"(ka)
    : "v1", "v2", "v3", "v4", "v5", "v6", "v7", "v8", "v9", "v10", "v11", "v12", "v13", "v14", "v15", "v16", "v17", "v18", "v19", "v20", "v21", "v22", "v23", "v24", "v25", "v26", "v27", "v28", "v29", "v30", "v31", "v32", "v33", "v34", "v35", "v36", "v37", "v38", "v39", "v40", "v41", "v42", "v43", "v44", "v45", "v46", "v47", "v48", "v49", "v50", "v51", "v52", "v53", "v54", "v55", "v56", "v57", "v58", "v59", "v60", "v61", "v62", "v63", "v64", "v65", "v66", "v67", "v68", "v69", "v70", "v71", "v72", "v73", "v74", "v75", "v76", "v77", "v78", "v79", "v80", "v81", "v82", "v83", "v84", "v85", "v86", "v87", "v88", "v89", "v90", "v91", "v92", "v93", "v94", "v95", "v96", "v97", "v98", "v99", "v100", "v101", "v102", "v103", "v104", "v105", "v106", "v107", "v108", "v109", "v110", "v111", "v112", "v113", "v114", "v115", "v116", "v117", "v118", "v119", "v120", "v121", "v122", "v123", "v124", "v125", "v126", "v127", "v128", "v129", "v130", "v131", "v132", "v133", "v134", "v135", "v136", "v137", "v138", "v139", "v140", "v141", "v142", "v143", "v144", "v145", "v146", "v147", "v148", "v149", "v150", "v151", "v152", "v153", "v154", "v155", "v156", "v157", "v158", "v159", "v160", "v161", "v162", "v163", "v164", "v165", "v166", "v167", "v168", "v169", "v170", "v171", "v172", "v173", "v174", "v175", "v176", "v177", "v178", "v179", "v180", "v181", "v182", "v183", "v184", "v185", "v186", "v187", "v188", "v189", "v190", "v191", "v192", "v193", "v194", "v195", "v196", "v197", "v198", "v199", "v200", "v201", "v202", "v203", "v204", "v205", "v206", "v207", "v208", "v209", "v210", "v211", "v212", "v213", "v214", "v215", "v216", "v217", "v218", "v219", "v220", "v221", "v222", "v223", "v224", "v225", "v226", "v227", "v228", "v229", "v230", "v231", "v232", "v233", "v234", "v235", "v236", "v237", "v238", "v239", "v240", "v241", "v242", "v243", "v244", "v245", "v246", "v247", "v248", "v249", "v250", "v251", "v252", "v253", "v254", "v255", "a0", "a1", "a2", "a3", "a4", "a5", "a6", "a7", "a8", "a9", "a10", "a11", "a12", "a13", "a14", "a15", "a16", "a17", "a18", "a19", "a20", "a21", "a22", "a23", "a24", "a25", "a26", "a27", "a28", "a29", "a30", "a31", "a32", "a33", "a34", "a35", "a36", "a37", "a38", "a39", "a40", "a41", "a42", "a43", "a44", "a45", "a46", "a47", "a48", "a49", "a50", "a51", "a52", "a53", "a54", "a55", "a56", "a57", "a58", "a59", "a60", "a61", "a62", "a63", "a64", "a65", "a66", "a67", "a68", "a69", "a70", "a71", "a72", "a73", "a74", "a75", "a76", "a77", "a78", "a79", "a80", "a81", "a82", "a83", "a84", "a85", "a86", "a87", "a88", "a89", "a90", "a91", "a92", "a93", "a94", "a95", "a96", "a97", "a98", "a99", "a100", "a101", "a102", "a103", "a104", "a105", "a106", "a107", "a108", "a109", "a110", "a111", "a112", "a113", "a114", "a115", "a116", "a117", "a118", "a119", "a120", "a121", "a122", "a123", "a124", "a125", "a126", "a127", "a128", "a129", "a130", "a131", "a132", "a133", "a134", "a135", "a136", "a137", "a138", "a139", "a140", "a141", "a142", "a143", "a144", "a145", "a146", "a147", "a148", "a149", "a150", "a151", "a152", "a153", "a154", "a155", "a156", "a157", "a158", "a159", "a160", "a161", "a162", "a163", "a164", "a165", "a166", "a167", "a168", "a169", "a170", "a171", "a172", "a173", "a174", "a175", "a176", "a177", "a178", "a179", "a180", "a181", "a182", "a183", "a184", "a185", "a186", "a187", "a188", "a189", "a190", "a191", "a192", "a193", "a194", "a195", "a196", "a197", "a198", "a199", "a200", "a201", "a202", "a203", "a204", "a205", "a206", "a207", "a208", "a209", "a210", "a211", "a212", "a213", "a214", "a215", "a216", "a217", "a218", "a219", "a220", "a221", "a222", "a223", "a224", "a225", "a226", "a227", "a228", "a229", "a230", "a231", "a232", "a233", "a234", "a235", "a236", "a237", "a238", "a239", "a240", "a241", "a242", "a243", "a244", "a245", "a246", "a247", "a248", "a249", "a250", "a251", "a252", "a253", "a254", "a255", "s4", "s5", "s6", "s7", "s8", "s9", "s10", "s11", "s12", "s13", "s14", "s15", "s16", "s17", "s18", "s19", "s20", "s21", "s22", "s23", "s24", "s25", "s26", "s27", "s28", "s29", "s30", "s31", "s32", "s33", "s34", "s35", "s36", "s37", "s38", "s39", "s40", "s41", "s42", "s43", "s44", "s45", "s46", "s47", "s48", "s49", "s50", "s51", "s52", "s53", "s54", "s55", "s56", "s57", "s58", "s59", "s60", "s61", "s62", "s63", "s64", "s65", "s66", "s67", "s68", "s69", "s70", "s71", "s72", "s73", "s74", "s75", "s76", "s77", "s78", "s79", "s80", "s81", "s82", "s83", "s84", "s85", "s86", "s87", "s88", "s89", "s90", "s91", "s92", "s93", "s94", "s95", "s96", "s97", "s98", "s99", "vcc", "scc", "memory");
    asm volatile("" : "+s"(ka));
    typedef const unsigned short* __attribute__((address_space(1))) gp;
    unsigned long long const __attribute__((address_space(4)))* kp = (unsigned long long const __attribute__((address_space(4)))*)ka;
    const unsigned short* x = (const unsigned short*)kp[0]; const unsigned short* dw = (const unsigned short*)kp[1];
    const unsigned short* w1 = (const unsigned short*)kp[2]; const unsigned short* b1 = (const unsigned short*)kp[3];
    const unsigned short* w2 = (const unsigned short*)kp[4]; const unsigned short* b2 = (const unsigned short*)kp[5];
    const unsigned short* gamma = (const unsigned short*)kp[6]; unsigned short* out = (unsigned short*)kp[7];
    extern __shared__ float sm[];
    float* cv = sm; float* hid = sm + 16 * 96; float* stt = hid + 16 * 384;
    const int x0 = (blockIdx.x >> 3) * 16, y0 = (blockIdx.x & 7) * 64;
    for (int y = y0; y < y0 + 64; ++y) {
        for (int i = tid; i < 16 * 96; i += 256) {
            int p = i / 96, c = i - p * 96; float s = 0.f;
            for (int ky = 0; ky < 3; ++ky) for (int kx = 0; kx < 3; ++kx) {
                int yy = y + ky - 1, xx = x0 + p + kx - 1;
                float v = (yy >= 0 && yy < 512 && xx >= 0 && xx < 512) ? bf2f(x[((size_t)yy * 512 + xx) * 96 + c]) : 0.f;
                s = fmaf(v, bf2f(dw[(ky * 3 + kx) * 96 + c]), s);
            }
            cv[i] = s;
        }
        __syncthreads();
        if (tid < 16) {
            float m = 0.f; for (int c = 0; c < 96; ++c) m += cv[tid * 96 + c];
            m *= (1.f / 96); float q = 0.f;
            for (int c = 0; c < 96; ++c) { float d = cv[tid * 96 + c] - m; q = fmaf(d, d, q); }
            stt[tid] = m; stt[16 + tid] = 1.f / sqrtf(q * (1.f / 96) + 1e-5f);
        }
        __syncthreads();
        for (int i = tid; i < 16 * 96; i += 256) { int p = i / 96; cv[i] = rbf((cv[i] - stt[p]) * stt[16 + p]); }
        __syncthreads();
        for (int e = tid; e < 384; e += 256) {
            float a[16];
#pragma unroll
            for (int p = 0; p < 16; ++p) a[p] = 0.f;
            for (int c = 0; c < 96; ++c) {
                float w = bf2f(w1[c * 384 + e]);
#pragma unroll
                for (int p = 0; p < 16; ++p) a[p] = fmaf(cv[p * 96 + c], w, a[p]);
            }
            float bb = bf2f(b1[e]);
#pragma unroll
            for (int p = 0; p < 16; ++p) {
                float h = a[p] + bb;
                hid[p * 384 + e] = rbf(0.5f * h * (1.f + tanhf(0.7978845608028654f * (h + 0.044715f * h * h * h))));
            }
        }
        __syncthreads();
        for (int i = tid; i < 16 * 96; i += 256) {
            int p = i / 96, c = i - p * 96; float s = 0.f;
            for (int e = 0; e < 384; ++e) s = fmaf(hid[p * 384 + e], bf2f(w2[e * 96 + c]), s);
            size_t o = ((size_t)y * 512 + x0 + p) * 96 + c;
            out[o] = f2bf(bf2f(x[o]) + bf2f(gamma[c]) * (s + bf2f(b2[c])));
        }
        __syncthreads();
    }

}

extern "C" void kernel_launch(void* const* d_in, const int* in_sizes, int n_in,
                              void* d_out, int out_size, void* d_ws, size_t ws_size,
                              hipStream_t stream) {
    (void)hipFuncSetAttribute((const void*)convnext_kernel, hipFuncAttributeMaxDynamicSharedMemorySize, LDS_BYTES);
    convnext_kernel<<<256, 256, LDS_BYTES, stream>>>(
        (const unsigned short*)d_in[0], (const unsigned short*)d_in[1], (const unsigned short*)d_in[2], (const unsigned short*)d_in[3],
        (const unsigned short*)d_in[4], (const unsigned short*)d_in[5], (const unsigned short*)d_in[6], (unsigned short*)d_out);
}
```

```cpp
#include <hip/hip_runtime.h>
#include <stdint.h>

constexpr int LDS_BYTES = 163584;

__device__ __forceinline__ float bf2f(unsigned short b) { return __uint_as_float((unsigned)b << 16); }
__device__ __forceinline__ unsigned short f2bf(float f) { unsigned r; asm("v_cvt_pk_bf16_f32 %0, %1, %1" : "=v"(r) : "v"(f)); return (unsigned short)r; }
__device__ __forceinline__ float rbf(float f) { return bf2f(f2bf(f)); }

__global__ __launch_bounds__(256, 1) __attribute__((aligned(4096))) void convnext_kernel(
    const unsigned short* __restrict__ x_, const unsigned short* __restrict__ dw_, const unsigned short* __restrict__ w1_,
    const unsigned short* __restrict__ b1_, const unsigned short* __restrict__ w2_, const unsigned short* __restrict__ b2_,
    const unsigned short* __restrict__ gamma_, unsigned short* __restrict__ out_) {
    const unsigned tid = threadIdx.x;
    unsigned long long ka = (unsigned long long)__builtin_amdgcn_kernarg_segment_ptr();
    asm volatile(
R"ASM(
s_load_dwordx16 s[20:35],%2,0
v_lshrrev_b32 v27,6,%1
v_and_b32 v24,63,%1
v_readfirstlane_b32 s52,v27
s_lshr_b32 s70,%0,3
v_and_b32_e64 v61,8,v24
v_lshl_add_u32 v62,s70,2,v27
v_mul_u32_u24 v61,15,v61
s_getpc_b64 s[14:15]
N_pf_%=:
v_or_b32 v61,7,v61
v_and_b32 v62,v61,v62
v_and_b32_e64 v61,7,v24
v_lshl_add_u32 v62,v62,3,v61
v_lshlrev_b32_e64 v62,7,v62
v_min_u32 v62,N_mainend_%=-N_pf_%=+2048,v62
global_load_dword v255,v62,s[14:15]
v_mul_u32_u24 v30,52,v27
v_add_u32 v30,v30,v24
v_subrev_u32_e64 v29,4,v30
v_max_i32 v29,0xc0,v29
v_min_u32_e64 v30,v30,v29
v_mul_u32_u24 v28,0xaaab,v30
s_and_b32 s64,%0,7
s_lshr_b32 s70,%0,3
v_lshrrev_b32 v28,19,v28
s_lshl_b32 s71,s64,6
s_mul_i32 s68,s70,3072
v_bfe_u32 v27,v28,2,2
s_sub_u32 s65,s71,1
s_mul_i32 s56,s65,98304
v_xor_b32 v27,v30,v27
s_add_u32 s56,s56,s68
s_cmp_lt_i32 s65,0
s_cselect_b32 s89,0x70000000,s56
v_lshlrev_b32 v27,4,v27
s_cmp_eq_u32 s70,0
s_cselect_b32 s67,0,99
s_cmp_eq_u32 s70,31
s_cselect_b32 s67,17,s67
v_mov_b32 v29,0x7fff0000
v_cmp_eq_u32_e32 vcc,s67,v28
s_mul_i32 s57,s52,832
s_add_u32 s57,s57,73728
v_cndmask_b32_e32 v6,v27,v29,vcc
s_mov_b32 s38,50331840
s_mov_b32 s39,0x00020000
s_mov_b32 s50,1728
s_mov_b32 s51,0x00020000
v_lshlrev_b32_e64 v8,4,v24
v_mul_u32_u24 v44,79,v24
v_add_u32 v45,5056,v44
v_lshrrev_b32 v44,10,v44
v_lshrrev_b32 v45,10,v45
v_sub_u32 v44,v24,v44
v_sub_u32 v45,v24,v45
v_lshlrev_b32_e64 v44,4,v44
v_add_lshl_u32 v45,v45,64,4
v_min_u32 v28,191,%1
v_lshlrev_b32_e64 v32,2,v28
v_min_u32 v34,95,%1
v_lshlrev_b32 v33,1,v34
v_and_b32 v27,15,v24
v_lshrrev_b32_e64 v28,4,v24
s_mul_i32 s66,s52,192
v_min_u32_e64 v27,11,v27
v_mul_u32_u24 v28,18432,v28
v_lshl_add_u32 v27,v27,4,s66
s_mul_i32 s66,s52,18432
v_add_u32 v172,v27,v28
v_add_u32 v173,s66,v8
s_and_b32 s64,s52,1
s_mul_i32 s65,s64,48
v_bfe_u32 v27,v24,3,2
v_and_b32 v28,4,v24
v_lshrrev_b32 v29,5,v24
v_add3_u32 v27,v27,v28,s65
v_lshl_add_u32 v27,v29,3,v27
v_mul_u32_u24 v27,768,v27
v_and_b32 v28,3,v24
v_xor_b32 v28,v28,v29
v_lshl_add_u32 v27,v28,4,v27
s_mul_i32 s65,s64,3072
v_add_u32 v28,s65,v8
s_mul_i32 s67,s64,3080
s_add_u32 s67,s67,40896
s_add_u32 s65,s65,16256
s_cmp_lt_u32 s52,2
s_cselect_b32 s58,s65,s67
s_cselect_b64 vcc,-1,0
s_movk_i32 s59,6144
s_cselect_b32 s59,-22464,s59
s_cselect_b32 s53,11264,0
s_cselect_b32 s88,0,67584
s_cselect_b32 s87,64,0
s_cselect_b32 s69,0,0x70000000
s_mov_b32 s62,32832
s_cselect_b32 s62,88272,s62
s_add_u32 s62,s62,s58
v_cndmask_b32_e32 v5,v28,v27,vcc
s_mov_b32 s46,73728
s_mov_b32 s47,0x00020000
s_waitcnt lgkmcnt(0)
s_sub_u32 s36,s20,192
s_subb_u32 s37,s21,0
s_and_b32 s37,s37,0xffff
s_add_u32 m0,s57,0
s_nop 0
buffer_load_dwordx4 v6,s[36:39],s89 offen lds
s_add_u32 m0,s57,3536
s_add_u32 s89,s56,98304
buffer_load_dwordx4 v6,s[36:39],s89 offen lds
s_add_u32 m0,s57,7080
s_add_u32 s89,s56,196608
s_nop 0
buffer_load_dwordx4 v6,s[36:39],s89 offen lds
s_mov_b32 s48,s22
s_and_b32 s49,s23,0xffff
s_mov_b32 m0,12288
global_load_ushort v37,v33,s[32:33]
buffer_load_dwordx4 v44,s[48:51],0 offen lds
s_mov_b32 m0,13312
global_load_dword v35,v32,s[26:27]
buffer_load_dwordx4 v45,s[48:51],0 offen lds
global_load_ushort v36,v33,s[30:31]
global_load_dwordx4 v[160:163],v172,s[24:25]
global_load_dwordx4 v[164:167],v172,s[24:25] offset:3840
global_load_dwordx4 v[168:171],v173,s[28:29]
s_cmp_lt_u32 s52,2
s_cselect_b64 s[44:45],s[24:25],s[28:29]
s_and_b32 s45,s45,0xffff
s_mov_b32 s99,0x72800000
s_mov_b32 s98,0xffff0000
s_mov_b64 s[90:91],0
v_mov_b32 v96,0x409d3ef8
v_lshlrev_b32 v102,4,%1
v_lshlrev_b32 v104,19,%1
v_cmp_eq_u32_e64 s[94:95],0,%1
v_add_u32 v103,0x8000,v102
s_mov_b32 s4,0x3f000000
s_mov_b32 s5,0x3fc00000
s_mov_b32 s6,0x40200000
s_mov_b32 s7,0x40600000
s_mov_b32 s8,0x40900000
s_mov_b32 s9,0x40b00000
s_mov_b32 s10,0x40d00000
s_mov_b32 s11,0x40f00000
s_mov_b32 s12,0x3f80222e
s_mov_b32 s13,0x13089abf
s_mov_b32 s96,0xebefe000
v_add_u32 v28,0x10000,v104
s_add_u32 m0,s57,10616
s_add_u32 s89,s56,294912
buffer_load_dwordx4 v6,s[36:39],s89 offen lds
v_mul_f32_e64 v98,s99,v104
v_fma_f32 v100,v28,s99,-v98
v_pk_fma_f32 v[64:65],v[100:101],s[4:5],v[98:99] op_sel_hi:[0,1,0]
v_pk_fma_f32 v[66:67],v[100:101],s[6:7],v[98:99] op_sel_hi:[0,1,0]
v_pk_fma_f32 v[68:69],v[100:101],s[8:9],v[98:99] op_sel_hi:[0,1,0]
v_pk_fma_f32 v[70:71],v[100:101],s[10:11],v[98:99] op_sel_hi:[0,1,0]
v_cndmask_b32_e64 v64,v64,0,s[94:95]
v_pk_mul_f32 v[72:73],v[64:65],v[64:65]
v_pk_mul_f32 v[74:75],v[66:67],v[66:67]
v_pk_mul_f32 v[76:77],v[68:69],v[68:69]
v_pk_mul_f32 v[78:79],v[70:71],v[70:71]
v_pk_fma_f32 v[72:73],v[72:73],s[12:13],v[96:97] op_sel_hi:[1,0,0]
v_pk_fma_f32 v[74:75],v[74:75],s[12:13],v[96:97] op_sel_hi:[1,0,0]
v_pk_fma_f32 v[76:77],v[76:77],s[12:13],v[96:97] op_sel_hi:[1,0,0]
v_pk_fma_f32 v[78:79],v[78:79],s[12:13],v[96:97] op_sel_hi:[1,0,0]
s_add_u32 m0,s57,14144
s_add_u32 s89,s56,393216
buffer_load_dwordx4 v6,s[36:39],s89 offen lds
v_pk_mul_f32 v[72:73],v[72:73],v[64:65]
v_pk_mul_f32 v[74:75],v[74:75],v[66:67]
v_pk_mul_f32 v[76:77],v[76:77],v[68:69]
v_pk_mul_f32 v[78:79],v[78:79],v[70:71]
v_exp_f32 v72,v72
v_exp_f32 v73,v73
v_exp_f32 v74,v74
v_exp_f32 v75,v75
v_exp_f32 v76,v76
v_exp_f32 v77,v77
s_add_u32 m0,s57,17680
s_add_u32 s89,s56,491520
buffer_load_dwordx4 v6,s[36:39],s89 offen lds
v_exp_f32 v78,v78
v_exp_f32 v79,v79
v_pk_fma_f32 v[72:73],v[72:73],s[96:97],s[96:97] op_sel_hi:[1,0,0]
v_pk_fma_f32 v[74:75],v[74:75],s[96:97],s[96:97] op_sel_hi:[1,0,0]
v_pk_fma_f32 v[76:77],v[76:77],s[96:97],s[96:97] op_sel_hi:[1,0,0]
v_pk_fma_f32 v[78:79],v[78:79],s[96:97],s[96:97] op_sel_hi:[1,0,0]
v_rcp_f32 v72,v72
v_rcp_f32 v73,v73
v_rcp_f32 v74,v74
v_rcp_f32 v75,v75
s_add_u32 m0,s57,21224
s_add_u32 s89,s56,589824
buffer_load_dwordx4 v6,s[36:39],s89 offen lds
v_rcp_f32 v76,v76
v_rcp_f32 v77,v77
v_rcp_f32 v78,v78
v_rcp_f32 v79,v79
v_pk_mul_f32 v[72:73],v[72:73],v[64:65]
v_pk_mul_f32 v[74:75],v[74:75],v[66:67]
v_pk_mul_f32 v[76:77],v[76:77],v[68:69]
v_pk_mul_f32 v[78:79],v[78:79],v[70:71]
v_pk_fma_f32 v[64:65],v[64:65],s[12:13],v[72:73] op_sel:[0,1,0] op_sel_hi:[1,1,1]
v_pk_fma_f32 v[66:67],v[66:67],s[12:13],v[74:75] op_sel:[0,1,0] op_sel_hi:[1,1,1]
v_pk_fma_f32 v[68:69],v[68:69],s[12:13],v[76:77] op_sel:[0,1,0] op_sel_hi:[1,1,1]
s_add_u32 m0,s57,24760
s_add_u32 s89,s56,688128
buffer_load_dwordx4 v6,s[36:39],s89 offen lds
v_pk_fma_f32 v[70:71],v[70:71],s[12:13],v[78:79] op_sel:[0,1,0] op_sel_hi:[1,1,1]
v_cvt_pk_bf16_f32 v80,v72,v73
v_cvt_pk_bf16_f32 v81,v74,v75
v_cvt_pk_bf16_f32 v82,v76,v77
v_cvt_pk_bf16_f32 v83,v78,v79
v_cvt_pk_bf16_f32 v84,v64,v65
v_cvt_pk_bf16_f32 v85,v66,v67
v_cvt_pk_bf16_f32 v86,v68,v69
v_cvt_pk_bf16_f32 v87,v70,v71
ds_write_b128 v102,v[80:83] offset:0
ds_write_b128 v103,v[84:87] offset:32768
v_add_u32 v104,0x8000000,v104
v_add_u32 v28,0x10000,v104
v_mul_f32_e64 v98,s99,v104
v_fma_f32 v100,v28,s99,-v98
s_add_u32 m0,s57,28288
s_add_u32 s89,s56,786432
buffer_load_dwordx4 v6,s[36:39],s89 offen lds
v_pk_fma_f32 v[64:65],v[100:101],s[4:5],v[98:99] op_sel_hi:[0,1,0]
v_pk_fma_f32 v[66:67],v[100:101],s[6:7],v[98:99] op_sel_hi:[0,1,0]
v_pk_fma_f32 v[68:69],v[100:101],s[8:9],v[98:99] op_sel_hi:[0,1,0]
v_pk_fma_f32 v[70:71],v[100:101],s[10:11],v[98:99] op_sel_hi:[0,1,0]
v_pk_mul_f32 v[72:73],v[64:65],v[64:65]
v_pk_mul_f32 v[74:75],v[66:67],v[66:67]
v_pk_mul_f32 v[76:77],v[68:69],v[68:69]
v_pk_mul_f32 v[78:79],v[70:71],v[70:71]
v_pk_fma_f32 v[72:73],v[72:73],s[12:13],v[96:97] op_sel_hi:[1,0,0]
v_pk_fma_f32 v[74:75],v[74:75],s[12:13],v[96:97] op_sel_hi:[1,0,0]
v_pk_fma_f32 v[76:77],v[76:77],s[12:13],v[96:97] op_sel_hi:[1,0,0]
v_pk_fma_f32 v[78:79],v[78:79],s[12:13],v[96:97] op_sel_hi:[1,0,0]
v_pk_mul_f32 v[72:73],v[72:73],v[64:65]
v_pk_mul_f32 v[74:75],v[74:75],v[66:67]
v_pk_mul_f32 v[76:77],v[76:77],v[68:69]
s_add_u32 m0,s57,31824
s_add_u32 s89,s56,884736
buffer_load_dwordx4 v6,s[36:39],s89 offen lds
v_pk_mul_f32 v[78:79],v[78:79],v[70:71]
v_exp_f32 v72,v72
v_exp_f32 v73,v73
v_exp_f32 v74,v74
v_exp_f32 v75,v75
v_exp_f32 v76,v76
v_exp_f32 v77,v77
v_exp_f32_e64 v78,v78
s_add_u32 m0,s57,35368
s_add_u32 s89,s56,983040
buffer_load_dwordx4 v6,s[36:39],s89 offen lds
v_exp_f32_e64 v79,v79
v_pk_fma_f32 v[72:73],v[72:73],s[96:97],s[96:97] op_sel_hi:[1,0,0]
v_pk_fma_f32 v[74:75],v[74:75],s[96:97],s[96:97] op_sel_hi:[1,0,0]
v_pk_fma_f32 v[76:77],v[76:77],s[96:97],s[96:97] op_sel_hi:[1,0,0]
v_pk_fma_f32 v[78:79],v[78:79],s[96:97],s[96:97] op_sel_hi:[1,0,0]
v_rcp_f32 v72,v72
v_rcp_f32 v73,v73
v_rcp_f32 v74,v74
v_rcp_f32 v75,v75
v_rcp_f32_e64 v76,v76
s_add_u32 m0,s57,38904
s_add_u32 s89,s56,1081344
buffer_load_dwordx4 v6,s[36:39],s89 offen lds
v_rcp_f32 v77,v77
v_rcp_f32 v78,v78
v_rcp_f32_e64 v79,v79
v_pk_mul_f32 v[72:73],v[72:73],v[64:65]
v_pk_mul_f32 v[74:75],v[74:75],v[66:67]
v_pk_mul_f32 v[76:77],v[76:77],v[68:69]
v_pk_mul_f32 v[78:79],v[78:79],v[70:71]
v_pk_fma_f32 v[64:65],v[64:65],s[12:13],v[72:73] op_sel:[0,1,0] op_sel_hi:[1,1,1]
v_pk_fma_f32 v[66:67],v[66:67],s[12:13],v[74:75] op_sel:[0,1,0] op_sel_hi:[1,1,1]
v_pk_fma_f32 v[68:69],v[68:69],s[12:13],v[76:77] op_sel:[0,1,0] op_sel_hi:[1,1,1]
v_pk_fma_f32 v[70:71],v[70:71],s[12:13],v[78:79] op_sel:[0,1,0] op_sel_hi:[1,1,1]
v_cvt_pk_bf16_f32 v88,v72,v73
v_cvt_pk_bf16_f32 v89,v74,v75
v_cvt_pk_bf16_f32 v90,v76,v77
v_cvt_pk_bf16_f32 v91,v78,v79
v_cvt_pk_bf16_f32 v92,v64,v65
v_cvt_pk_bf16_f32 v93,v66,v67
v_cvt_pk_bf16_f32 v94,v68,v69
v_cvt_pk_bf16_f32 v95,v70,v71
ds_write_b128 v102,v[88:91] offset:4096
ds_write_b128 v103,v[92:95] offset:36864
s_mov_b32 s96,0x8befe000
s_lshl_b32 s66,s52,2
s_mul_i32 s60,s66,3536
s_add_u32 s66,s66,s71
s_lshl2_add_u32 s16,s70,s52
s_mul_i32 s16,s16,512
v_and_b32 v25,15,v24
v_lshrrev_b32_e64 v26,4,v24
s_mul_i32 s61,s66,98304
s_add_u32 s61,s61,s68
s_mov_b32 s54,0x44ea44ea
s_mov_b32 s55,0x877f877f
s_mov_b64 s[40:41],s[34:35]
s_and_b32 s41,s41,0xffff
s_mov_b32 s42,50331648
s_mov_b32 s43,0x00020000
v_mov_b32 v41,0x70000
s_add_u32 m0,s57,42432
s_add_u32 s89,s56,1179648
buffer_load_dwordx4 v6,s[36:39],s89 offen lds
v_and_b32 v39,8,v24
v_and_b32 v40,7,v24
v_bfe_u32 v27,v24,1,1
v_sub_u32_e64 v27,v26,v27
v_cmp_gt_u32_e64 s[92:93],3,v27
v_and_b32 v28,1,v24
v_mul_u32_u24 v27,3,v27
v_sub_u32_e64 v27,v27,v28
v_cmp_eq_u32_e64 s[94:95],0,v28
v_lshrrev_b32_e64 v29,2,v25
v_mul_i32_i24 v27,208,v27
v_lshl_add_u32 v27,v29,1,v27
v_mul_u32_u24 v10,0x5555,v29
v_add_u32 v27,12288,v27
v_cndmask_b32_e64 v45,v41,v27,s[92:93]
v_cndmask_b32_e64 v44,v41,v45,s[94:95]
v_cndmask_b32_e64 v47,v45,v41,s[94:95]
s_waitcnt vmcnt(13)
s_nop 0
ds_read_u16 a96,v44 offset:0
ds_read_u16 a97,v45 offset:208
ds_read_u16 a98,v45 offset:416
ds_read_u16 a99,v47 offset:624
ds_read_u16 a100,v44 offset:8
ds_read_u16 a101,v45 offset:216
ds_read_u16 a102,v45 offset:424
ds_read_u16 a103,v47 offset:632
v_mov_b64 v[20:21],0
v_mov_b64 v[22:23],0
v_cmp_eq_u32_e64 s[92:93],v26,v29
v_lshlrev_b32_e64 v27,4,v25
v_add_u32 v27,10112,v27
v_cndmask_b32_e64 v13,v41,v27,s[92:93]
v_lshrrev_b32 v27,2,v39
v_lshrrev_b32 v28,5,v24
v_add_u32_e64 v27,v27,v28
s_add_u32 m0,s57,45968
s_add_u32 s89,s56,1277952
buffer_load_dwordx4 v6,s[36:39],s89 offen lds
v_mul_u32_u24 v27,3536,v27
v_lshl_or_b32 v10,v27,16,v10
v_and_b32 v27,1,v26
v_add_u32 v27,v27,v40
v_mul_u32_u24 v28,384,v27
v_lshrrev_b32_e64 v27,1,v27
v_lshl_add_u32 v28,v27,4,v28
v_mfma_f32_32x32x16_bf16 v[64:79],v[20:23],v[20:23],0
ds_read_u16 a104,v44 offset:16
ds_read_u16 a105,v45 offset:224
ds_read_u16 a106,v45 offset:432
ds_read_u16 a107,v47 offset:640
ds_read_u16 a108,v44 offset:24
ds_read_u16 a109,v45 offset:232
ds_read_u16 a110,v45 offset:440
ds_read_u16 a111,v47 offset:648
v_add_u32 v30,73728,v39
v_add_u32 v11,v28,v30
s_movk_i32 s66,16384
v_lshl_add_u32 v27,v40,1,1
v_lshrrev_b32 v28,2,v27
v_xor_b32 v28,v28,v26
v_mul_u32_u24 v27,192,v27
v_lshl_add_u32 v27,v28,4,v27
v_add3_u32 v12,v27,v39,s66
v_lshl_add_u32 v27,v40,1,2
v_lshrrev_b32 v28,2,v27
v_xor_b32 v28,v28,v26
v_mul_u32_u24 v27,192,v27
v_lshl_add_u32 v27,v28,4,v27
v_add3_u32 v27,v27,v39,s66
v_lshl_or_b32 v12,v27,16,v12
v_mfma_f32_32x32x16_bf16 v[80:95],v[20:23],v[20:23],0
ds_read_u16 a112,v44 offset:32
ds_read_u16 a113,v45 offset:240
ds_read_u16 a114,v45 offset:448
ds_read_u16 a115,v47 offset:656
ds_read_u16 a116,v44 offset:40
ds_read_u16 a117,v45 offset:248
ds_read_u16 a118,v45 offset:456
ds_read_u16 a119,v47 offset:664
v_mul_u32_u24 v27,24576,v39
s_add_u32 m0,s57,49512
s_add_u32 s89,s56,1376256
buffer_load_dwordx4 v6,s[36:39],s89 offen lds
v_mul_u32_u24 v28,384,v40
v_lshl_add_u32 v27,v26,4,v27
v_add_u32 v7,v27,v28
v_lshrrev_b32 v27,3,v25
v_bfe_u32 v28,v25,2,1
v_lshl_add_u32 v28,v27,3,v28
v_lshl_add_u32 v28,v26,1,v28
v_lshlrev_b32_e64 v28,6,v28
v_lshl_add_u32 v28,v27,4,v28
v_and_b32_e64 v29,1,v25
v_lshl_add_u32 v28,v29,5,v28
v_bfe_u32 v29,v25,1,1
v_lshl_add_u32 v28,v29,3,v28
v_xor_b32_e64 v29,16,v28
v_add_u32 v1,16256,v28
v_mfma_f32_32x32x16_bf16 v[96:111],v[20:23],v[20:23],0
ds_read_u16 a120,v44 offset:48
ds_read_u16 a121,v45 offset:256
ds_read_u16 a122,v45 offset:464
ds_read_u16 a123,v47 offset:672
ds_read_u16 a124,v44 offset:56
ds_read_u16 a125,v45 offset:264
ds_read_u16 a126,v45 offset:472
ds_read_u16 a127,v47 offset:680
v_add_u32 v2,16256,v29
v_lshrrev_b32_e64 v27,3,v25
v_bfe_u32 v28,v25,2,1
v_lshl_add_u32 v27,v28,1,v27
v_lshrrev_b32_e64 v28,1,v26
v_lshl_add_u32 v27,v28,2,v27
v_mul_u32_u24 v27,192,v27
v_and_b32_e64 v29,1,v26
v_mul_u32_u24 v30,3080,v29
v_add_u32 v27,v27,v30
v_and_b32 v30,3,v25
s_add_u32 m0,s57,53048
s_add_u32 s89,s56,1474560
buffer_load_dwordx4 v6,s[36:39],s89 offen lds
v_lshl_add_u32 v27,v30,4,v27
v_add_u32 v3,40896,v27
v_lshlrev_b32_e64 v29,6,v29
v_lshl_add_u32 v29,v28,4,v29
v_add_u32 v4,8192,v29
v_mfma_f32_32x32x16_bf16 v[112:127],v[20:23],v[20:23],0
ds_read_u16 a128,v44 offset:64
ds_read_u16 a129,v45 offset:272
ds_read_u16 a130,v45 offset:480
ds_read_u16 a131,v47 offset:688
ds_read_u16 a132,v44 offset:72
ds_read_u16 a133,v45 offset:280
ds_read_u16 a134,v45 offset:488
ds_read_u16 a135,v47 offset:696
v_lshrrev_b32_e64 v27,5,v34
v_bfe_u32 v28,v34,2,1
v_and_b32 v29,3,v34
v_lshlrev_b32 v49,7,v27
v_lshl_add_u32 v49,v28,5,v49
v_lshl_add_u32 v49,v29,2,v49
v_bfe_u32 v30,v34,3,1
v_lshl_add_u32 v49,v30,6,v49
v_bfe_u32 v30,v34,4,1
v_lshl_add_u32 v49,v30,4,v49
v_add_u32 v49,11776,v49
v_lshrrev_b32_e64 v55,6,v34
v_bfe_u32 v30,v34,3,2
v_lshl_add_u32 v55,v55,2,v30
v_and_b32_e64 v30,1,v27
v_lshl_add_u32 v55,v55,1,v30
v_mfma_f32_32x32x16_bf16 v[128:143],v[20:23],v[20:23],0
ds_read_u16 a136,v44 offset:80
ds_read_u16 a137,v45 offset:288
ds_read_u16 a138,v45 offset:496
ds_read_u16 a139,v47 offset:704
ds_read_u16 a140,v44 offset:88
ds_read_u16 a141,v45 offset:296
ds_read_u16 a142,v45 offset:504
ds_read_u16 a143,v47 offset:712
v_and_b32_e64 v30,7,v34
s_add_u32 m0,s57,56576
s_add_u32 s89,s56,1572864
buffer_load_dwordx4 v6,s[36:39],s89 offen lds
v_lshl_add_u32 v55,v55,3,v30
v_lshlrev_b32 v55,2,v55
v_lshlrev_b32 v15,2,v24
v_lshl_add_u32 v50,v27,1,v28
v_lshlrev_b32_e64 v50,4,v50
v_bfe_u32 v30,v34,3,2
v_lshl_add_u32 v50,v30,2,v50
v_add_u32 v50,v50,v29
v_lshlrev_b32 v50,4,v50
v_add_u32 v50,10112,v50
v_lshlrev_b32_e64 v51,4,v29
v_cmp_eq_u32_e64 s[94:95],1,v28
v_lshlrev_b32_e64 v48,1,v32
v_add_u32 v48,8192,v48
v_mov_b32_e64 v53,0
v_mfma_f32_32x32x16_bf16 v[144:159],v[20:23],v[20:23],0
ds_read_u16 a144,v44 offset:96
ds_read_u16 a145,v45 offset:304
ds_read_u16 a146,v45 offset:512
ds_read_u16 a147,v47 offset:720
ds_read_u16 a148,v44 offset:104
ds_read_u16 a149,v45 offset:312
ds_read_u16 a150,v45 offset:520
ds_read_u16 a151,v47 offset:728
v_lshlrev_b32 v37,16,v37
v_lshlrev_b32 v36,16,v36
v_mov_b32 v27,0x45800000
v_mov_b32 v28,0x53800000
v_cmp_nlt_f32_e64 s[90:91],|v37|,v27
v_cmp_nlt_f32_e64 vcc,|v36|,v28
v_mov_b32 v27,0x42800000
v_mul_f32 v37,0x6c800000,v37
v_mul_f32 v36,0x12800000,v36
v_rcp_f32_e64 v56,v37
s_add_u32 m0,s57,60112
s_add_u32 s89,s56,1671168
buffer_load_dwordx4 v6,s[36:39],s89 offen lds
v_lshlrev_b32_e64 v42,16,v35
v_and_b32 v43,s98,v35
s_or_b64 s[90:91],s[90:91],vcc
v_cmp_nlt_f32_e64 vcc,|v42|,v27
v_cmp_nlt_f32_e64 s[92:93],|v43|,v27
v_med3_f32 v56,v56,1.0,-1.0
ds_read_u16 a152,v44 offset:112
ds_read_u16 a153,v45 offset:320
ds_read_u16 a154,v45 offset:528
ds_read_u16 a155,v47 offset:736
ds_read_u16 a156,v44 offset:120
ds_read_u16 a157,v45 offset:328
ds_read_u16 a158,v45 offset:536
ds_read_u16 a159,v47 offset:744
v_pk_mul_f32 v[42:43],v[42:43],s[96:97] op_sel_hi:[1,0]
v_cvt_pk_bf16_f32 v52,v56,v53
ds_write_b64 v48,v[42:43]
ds_write_b32 v49,v36
v_lshlrev_b32 v54,16,v52
v_rcp_f32 v54,v54
v_lshlrev_b64 v[52:53],v51,v[52:53]
s_or_b64 s[90:91],s[90:91],vcc
s_or_b64 s[90:91],s[90:91],s[92:93]
v_cndmask_b32_e64 v56,v52,0,s[94:95]
v_cndmask_b32_e64 v58,0,v52,s[94:95]
v_cndmask_b32_e64 v57,v53,0,s[94:95]
v_cndmask_b32_e64 v59,0,v53,s[94:95]
ds_write_b32 v15,v7 offset:14848
ds_write_b32 v15,v13 offset:15104
s_mul_i32 s66,s52,1536
ds_read_u16 a160,v44 offset:128
ds_read_u16 a161,v45 offset:336
ds_read_u16 a162,v45 offset:544
ds_read_u16 a163,v47 offset:752
ds_read_u16 a164,v44 offset:136
ds_read_u16 a165,v45 offset:344
ds_read_u16 a166,v45 offset:552
ds_read_u16 a167,v47 offset:760
s_add_u32 m0,s58,18480
s_nop 0
buffer_load_dwordx4 v5,s[44:47],s69 offen offset:0 lds
s_add_u32 s69,s69,s53
buffer_load_dwordx4 v5,s[44:47],s69 offen offset:1024 lds
s_mov_b64 exec,0
s_cmp_eq_u32 s52,0
s_cbranch_scc1 TE00_%=
s_cmp_eq_u32 s52,1
s_cbranch_scc1 TE01_%=
s_cmp_eq_u32 s52,2
s_cbranch_scc1 TE02_%=
s_branch TE03_%=
TR0_%=:
s_mov_b64 exec,-1
s_add_u32 s66,s66,16256
v_lshl_add_u32 v7,v24,3,s66
ds_write_b128 v50,v[56:59]
ds_write_b32 v55,v54 offset:14336
s_waitcnt vmcnt(17)
v_and_b32 v160,0x7fff7fff,v160
v_and_b32 v161,0x7fff7fff,v161
v_and_b32 v162,0x7fff7fff,v162
v_and_b32 v163,0x7fff7fff,v163
v_and_b32 v164,0x7fff7fff,v164
v_and_b32 v165,0x7fff7fff,v165
v_and_b32 v166,0x7fff7fff,v166
v_and_b32 v167,0x7fff7fff,v167
v_and_b32 v168,0x7fff7fff,v168
v_and_b32 v169,0x7fff7fff,v169
v_and_b32 v170,0x7fff7fff,v170
TE00_%=:
s_cbranch_execz TP2751_%=
ds_read_u16 a168,v44 offset:144
ds_read_u16 a169,v45 offset:352
ds_read_u16 a170,v45 offset:560
ds_read_u16 a171,v47 offset:768
ds_read_u16 a172,v44 offset:152
ds_read_u16 a173,v45 offset:360
ds_read_u16 a174,v45 offset:568
ds_read_u16 a175,v47 offset:776
v_and_b32 v171,0x7fff7fff,v171
s_add_u32 s69,s69,s53
buffer_load_dwordx4 v5,s[44:47],s69 offen offset:2048 lds
v_pk_max_u16 v160,v160,v164
v_pk_max_u16 v161,v161,v165
v_pk_max_u16 v162,v162,v166
v_pk_max_u16 v163,v163,v167
v_mov_b64 v[164:165],v[160:161]
v_mov_b64 v[166:167],v[162:163]
s_nop 0
v_permlane32_swap_b32 v160,v164
v_permlane32_swap_b32 v161,v165
v_permlane32_swap_b32 v162,v166
v_permlane32_swap_b32_e64 v163,v167
v_pk_max_u16 v160,v160,v164
v_pk_max_u16 v161,v161,v165
v_pk_max_u16 v162,v162,v166
v_pk_max_u16 v163,v163,v167
v_mov_b64_e64 v[164:165],v[160:161]
ds_read_u16 a176,v44 offset:160
ds_read_u16 a177,v45 offset:368
ds_read_u16 a178,v45 offset:576
ds_read_u16 a179,v47 offset:784
ds_read_u16 a180,v44 offset:168
ds_read_u16 a181,v45 offset:376
ds_read_u16 a182,v45 offset:584
ds_read_u16 a183,v47 offset:792
v_mov_b64 v[166:167],v[162:163]
v_permlane16_swap_b32 v160,v164
v_permlane16_swap_b32 v161,v165
v_permlane16_swap_b32 v162,v166
v_permlane16_swap_b32_e64 v163,v167
v_pk_max_u16 v160,v160,v164
v_pk_max_u16 v161,v161,v165
v_pk_max_u16 v162,v162,v166
v_pk_max_u16 v163,v163,v167
v_pk_min_u16 v160,v160,v161
v_pk_min_u16 v162,v162,v163
v_pk_min_u16 v160,v160,v162
v_pk_min_u16 v160,v160,v160 op_sel:[0,1] op_sel_hi:[1,0]
v_pk_max_u16 v168,v168,v169
v_pk_max_u16 v170,v170,v171
v_pk_max_u16 v168,v168,v170
ds_read_u16 a184,v44 offset:176
ds_read_u16 a185,v45 offset:384
ds_read_u16 a186,v45 offset:592
ds_read_u16 a187,v47 offset:800
ds_read_u16 a188,v44 offset:184
ds_read_u16 a189,v45 offset:392
ds_read_u16 a190,v45 offset:600
ds_read_u16 a191,v47 offset:808
v_pk_max_u16 v168,v168,v168 op_sel:[0,1] op_sel_hi:[1,0]
s_add_u32 m0,s58,0
s_mov_b32 s69,s87
buffer_load_dwordx4 v5,s[44:47],s69 offen offset:0 lds
v_cmp_gt_u16_e32 vcc,0x3880,v160
s_nop 0
s_or_b64 s[90:91],s[90:91],vcc
v_cmp_le_u16_e32 vcc,0x5380,v168
s_nop 0
s_or_b64 s[90:91],s[90:91],vcc
v_accvgpr_write_b32 a88,0
v_accvgpr_write_b32 a89,0
v_accvgpr_write_b32 a90,0
v_accvgpr_write_b32 a91,0
s_add_u32 s69,s69,s53
buffer_load_dwordx4 v5,s[44:47],s69 offen offset:1024 lds
s_add_u32 s69,s69,s53
buffer_load_dwordx4 v5,s[44:47],s69 offen offset:2048 lds
s_waitcnt lgkmcnt(0)
s_waitcnt vmcnt(6)
s_barrier
v_add_u32_sdwa v28,s60,v10 dst_sel:DWORD dst_unused:UNUSED_PAD src0_sel:DWORD src1_sel:WORD_1
v_subrev_u32 v8,88400,v28
v_min_u32 v28,v28,v8
v_add_u32 v29,7072,v28
v_subrev_u32 v8,88400,v29
v_min_u32_e64 v29,v29,v8
v_xad_u32 v24,v11,0,v28
v_xad_u32 v25,v11,8,v29
v_xad_u32 v26,v11,16,v28
v_xad_u32 v27,v11,24,v29
ds_read_b64 a[0:1],v24 offset:0
ds_read_b64 a[2:3],v24 offset:192
ds_read_b64 a[4:5],v25 offset:0
ds_read_b64 a[6:7],v25 offset:192
ds_read_b64 a[8:9],v24 offset:8
ds_read_b64 a[10:11],v24 offset:200
ds_read_b64 a[12:13],v25 offset:8
ds_read_b64 a[14:15],v25 offset:200
ds_read_b64 a[16:17],v26 offset:0
ds_read_b64 a[18:19],v26 offset:192
ds_read_b64 a[20:21],v27 offset:0
ds_read_b64 a[22:23],v27 offset:192
s_waitcnt lgkmcnt(8)
v_smfmac_f32_16x16x64_bf16 v[64:67],a[96:99],a[0:7],v10
s_waitcnt lgkmcnt(4)
s_add_u32 m0,s58,6160
s_add_u32 s69,s69,s59
buffer_load_dwordx4 v5,s[44:47],s69 offen offset:0 lds
ds_read_b64 a[0:1],v26 offset:8
ds_read_b64 a[2:3],v26 offset:200
ds_read_b64 a[4:5],v27 offset:8
ds_read_b64 a[6:7],v27 offset:200
v_smfmac_f32_16x16x64_bf16 v[68:71],a[100:103],a[8:15],v10
s_waitcnt lgkmcnt(4)
s_add_u32 m0,s57,63656
s_add_u32 s89,s56,1769472
buffer_load_dwordx4 v6,s[36:39],s89 offen lds
ds_read_b64 a[8:9],v24 offset:64
ds_read_b64 a[10:11],v24 offset:256
ds_read_b64 a[12:13],v25 offset:64
ds_read_b64 a[14:15],v25 offset:256
v_smfmac_f32_16x16x64_bf16 v[72:75],a[104:107],a[16:23],v10
s_waitcnt lgkmcnt(4)
ds_read_b64 a[16:17],v24 offset:72
ds_read_b64 a[18:19],v24 offset:264
ds_read_b64 a[20:21],v25 offset:72
ds_read_b64 a[22:23],v25 offset:264
v_and_b32_e64 v248,8,v11
v_pk_mul_f32 v[40:41],v[64:65],v[64:65]
v_pk_mul_f32 v[42:43],v[66:67],v[66:67]
v_smfmac_f32_16x16x64_bf16 v[76:79],a[108:111],a[0:7],v10
s_waitcnt lgkmcnt(4)
ds_read_b64 a[0:1],v26 offset:64
ds_read_b64 a[2:3],v26 offset:256
ds_read_b64 a[4:5],v27 offset:64
ds_read_b64 a[6:7],v27 offset:256
v_mul_u32_u24 v248,884,v248
s_add_u32 s64,s60,3536
v_pk_add_f32 v[36:37],v[64:65],v[68:69]
v_pk_fma_f32 v[40:41],v[68:69],v[68:69],v[40:41]
v_pk_add_f32 v[38:39],v[66:67],v[70:71]
v_pk_fma_f32 v[42:43],v[70:71],v[70:71],v[42:43]
v_smfmac_f32_16x16x64_bf16 v[80:83],a[128:131],a[8:15],v10
s_waitcnt lgkmcnt(4)
s_add_u32 m0,s57,67192
s_add_u32 s89,s56,1867776
buffer_load_dwordx4 v6,s[36:39],s89 offen lds
ds_read_b64 a[8:9],v26 offset:72
ds_read_b64 a[10:11],v26 offset:264
ds_read_b64 a[12:13],v27 offset:72
ds_read_b64 a[14:15],v27 offset:264
v_add_u32 v251,s64,v248
v_pk_add_f32 v[36:37],v[36:37],v[72:73]
v_pk_fma_f32 v[40:41],v[72:73],v[72:73],v[40:41]
v_pk_add_f32 v[38:39],v[38:39],v[74:75]
v_pk_fma_f32 v[42:43],v[74:75],v[74:75],v[42:43]
v_smfmac_f32_16x16x64_bf16 v[84:87],a[132:135],a[16:23],v10
s_waitcnt lgkmcnt(4)
s_nop 0
ds_read_b64 a[16:17],v24 offset:128
ds_read_b64 a[18:19],v24 offset:320
ds_read_b64 a[20:21],v25 offset:128
ds_read_b64 a[22:23],v25 offset:320
v_subrev_u32 v8,88400,v251
v_pk_add_f32 v[36:37],v[36:37],v[76:77]
v_pk_fma_f32 v[40:41],v[76:77],v[76:77],v[40:41]
v_pk_add_f32 v[38:39],v[38:39],v[78:79]
v_pk_fma_f32 v[42:43],v[78:79],v[78:79],v[42:43]
v_smfmac_f32_16x16x64_bf16 v[88:91],a[136:139],a[0:7],v10
s_waitcnt lgkmcnt(4)
ds_read_b64 a[0:1],v24 offset:136
ds_read_b64 a[2:3],v24 offset:328
ds_read_b64 a[4:5],v25 offset:136
ds_read_b64 a[6:7],v25 offset:328
v_min_u32 v251,v251,v8
v_pk_add_f32 v[36:37],v[36:37],v[80:81]
v_pk_fma_f32 v[40:41],v[80:81],v[80:81],v[40:41]
v_pk_add_f32 v[38:39],v[38:39],v[82:83]
v_pk_fma_f32 v[42:43],v[82:83],v[82:83],v[42:43]
v_smfmac_f32_16x16x64_bf16 v[92:95],a[140:143],a[8:15],v10
s_waitcnt lgkmcnt(4)
s_add_u32 m0,s58,6160
s_add_u32 s69,s69,s53
buffer_load_dwordx4 v5,s[44:47],s69 offen offset:1024 lds
ds_read_b64 a[8:9],v26 offset:128
ds_read_b64 a[10:11],v26 offset:320
ds_read_b64 a[12:13],v27 offset:128
ds_read_b64 a[14:15],v27 offset:320
v_xad_u32 v24,v11,32,v28
v_xad_u32 v25,v11,40,v29
v_pk_add_f32 v[36:37],v[36:37],v[84:85]
v_pk_fma_f32 v[40:41],v[84:85],v[84:85],v[40:41]
v_pk_add_f32 v[38:39],v[38:39],v[86:87]
v_pk_fma_f32 v[42:43],v[86:87],v[86:87],v[42:43]
v_smfmac_f32_16x16x64_bf16 v[96:99],a[160:163],a[16:23],v10
s_waitcnt lgkmcnt(4)
ds_read_b64 a[16:17],v26 offset:136
ds_read_b64 a[18:19],v26 offset:328
ds_read_b64 a[20:21],v27 offset:136
ds_read_b64 a[22:23],v27 offset:328
v_add_u32 v250,3536,v251
v_pk_add_f32 v[36:37],v[36:37],v[88:89]
v_pk_fma_f32 v[40:41],v[88:89],v[88:89],v[40:41]
v_pk_add_f32 v[38:39],v[38:39],v[90:91]
v_pk_fma_f32 v[42:43],v[90:91],v[90:91],v[42:43]
v_smfmac_f32_16x16x64_bf16 v[100:103],a[164:167],a[0:7],v10
s_waitcnt lgkmcnt(4)
s_add_u32 m0,s57,70720
s_add_u32 s89,s56,1966080
buffer_load_dwordx4 v6,s[36:39],s89 offen lds
ds_read_b64 a[0:1],v24 offset:0
ds_read_b64 a[2:3],v24 offset:192
ds_read_b64 a[4:5],v25 offset:0
ds_read_b64 a[6:7],v25 offset:192
v_xad_u32 v26,v11,48,v28
v_xad_u32 v27,v11,56,v29
v_pk_add_f32 v[36:37],v[36:37],v[92:93]
v_pk_fma_f32 v[40:41],v[92:93],v[92:93],v[40:41]
v_pk_add_f32 v[38:39],v[38:39],v[94:95]
v_pk_fma_f32 v[42:43],v[94:95],v[94:95],v[42:43]
v_smfmac_f32_16x16x64_bf16 v[104:107],a[168:171],a[8:15],v10
s_waitcnt lgkmcnt(4)
s_nop 0
ds_read_b64 a[8:9],v24 offset:8
ds_read_b64 a[10:11],v24 offset:200
ds_read_b64 a[12:13],v25 offset:8
ds_read_b64 a[14:15],v25 offset:200
v_subrev_u32 v8,88400,v250
v_pk_add_f32 v[36:37],v[36:37],v[96:97]
v_pk_fma_f32 v[40:41],v[96:97],v[96:97],v[40:41]
v_pk_add_f32 v[38:39],v[38:39],v[98:99]
v_pk_fma_f32 v[42:43],v[98:99],v[98:99],v[42:43]
v_smfmac_f32_16x16x64_bf16 v[108:111],a[172:175],a[16:23],v10
s_waitcnt lgkmcnt(4)
ds_read_b64 a[16:17],v24 offset:64
ds_read_b64 a[18:19],v24 offset:256
ds_read_b64 a[20:21],v25 offset:64
ds_read_b64 a[22:23],v25 offset:256
v_min_u32 v250,v250,v8
v_pk_add_f32 v[36:37],v[36:37],v[100:101]
v_pk_fma_f32 v[40:41],v[100:101],v[100:101],v[40:41]
v_pk_add_f32 v[38:39],v[38:39],v[102:103]
v_pk_fma_f32 v[42:43],v[102:103],v[102:103],v[42:43]
v_smfmac_f32_16x16x64_bf16 v[112:115],a[112:115],a[0:7],v10
s_waitcnt lgkmcnt(4)
s_add_u32 m0,s58,6160
s_add_u32 s69,s69,s53
buffer_load_dwordx4 v5,s[44:47],s69 offen offset:2048 lds
ds_read_b64 a[0:1],v24 offset:72
ds_read_b64 a[2:3],v24 offset:264
ds_read_b64 a[4:5],v25 offset:72
ds_read_b64 a[6:7],v25 offset:264
v_xor_b32 v9,0x80008,v12
v_pk_add_f32 v[36:37],v[36:37],v[104:105]
v_pk_fma_f32 v[40:41],v[104:105],v[104:105],v[40:41]
v_pk_add_f32 v[38:39],v[38:39],v[106:107]
v_pk_fma_f32 v[42:43],v[106:107],v[106:107],v[42:43]
v_smfmac_f32_16x16x64_bf16 v[116:119],a[116:119],a[8:15],v10
s_waitcnt lgkmcnt(4)
ds_read_b64 a[8:9],v24 offset:128
ds_read_b64 a[10:11],v24 offset:320
ds_read_b64 a[12:13],v25 offset:128
ds_read_b64 a[14:15],v25 offset:320
v_add_u32_sdwa v248,v12,v251 dst_sel:DWORD dst_unused:UNUSED_PAD src0_sel:WORD_0 src1_sel:DWORD
v_pk_add_f32 v[36:37],v[36:37],v[108:109]
v_pk_fma_f32 v[40:41],v[108:109],v[108:109],v[40:41]
v_pk_add_f32 v[38:39],v[38:39],v[110:111]
v_pk_fma_f32 v[42:43],v[110:111],v[110:111],v[42:43]
v_smfmac_f32_16x16x64_bf16 v[120:123],a[144:147],a[16:23],v10
s_waitcnt lgkmcnt(4)
s_add_u32 m0,s57,74256
s_add_u32 s89,s56,2064384
buffer_load_dwordx4 v6,s[36:39],s89 offen lds
ds_read_b64 a[16:17],v24 offset:136
ds_read_b64 a[18:19],v24 offset:328
ds_read_b64 a[20:21],v25 offset:136
ds_read_b64 a[22:23],v25 offset:328
v_add_u32_sdwa v249,v12,v251 dst_sel:DWORD dst_unused:UNUSED_PAD src0_sel:WORD_1 src1_sel:DWORD
v_pk_add_f32 v[36:37],v[36:37],v[112:113]
v_pk_fma_f32 v[40:41],v[112:113],v[112:113],v[40:41]
v_pk_add_f32 v[38:39],v[38:39],v[114:115]
v_pk_fma_f32 v[42:43],v[114:115],v[114:115],v[42:43]
v_smfmac_f32_16x16x64_bf16 v[124:127],a[148:151],a[0:7],v10
s_waitcnt lgkmcnt(4)
ds_read_b64 a[0:1],v26 offset:0
ds_read_b64 a[2:3],v26 offset:192
ds_read_b64 a[4:5],v27 offset:0
ds_read_b64 a[6:7],v27 offset:192
v_add_u32_sdwa v251,v9,v250 dst_sel:DWORD dst_unused:UNUSED_PAD src0_sel:WORD_1 src1_sel:DWORD
v_pk_add_f32 v[36:37],v[36:37],v[116:117]
v_pk_fma_f32 v[40:41],v[116:117],v[116:117],v[40:41]
v_pk_add_f32 v[38:39],v[38:39],v[118:119]
v_pk_fma_f32 v[42:43],v[118:119],v[118:119],v[42:43]
v_smfmac_f32_16x16x64_bf16 v[128:131],a[176:179],a[8:15],v10
s_waitcnt lgkmcnt(4)
ds_read_b64 a[8:9],v26 offset:8
ds_read_b64 a[10:11],v26 offset:200
ds_read_b64 a[12:13],v27 offset:8
ds_read_b64 a[14:15],v27 offset:200
v_add_u32_sdwa v250,v9,v250 dst_sel:DWORD dst_unused:UNUSED_PAD src0_sel:WORD_0 src1_sel:DWORD
v_pk_add_f32 v[36:37],v[36:37],v[120:121]
v_pk_fma_f32 v[40:41],v[120:121],v[120:121],v[40:41]
v_pk_add_f32 v[38:39],v[38:39],v[122:123]
v_pk_fma_f32 v[42:43],v[122:123],v[122:123],v[42:43]
v_smfmac_f32_16x16x64_bf16 v[132:135],a[180:183],a[16:23],v10
s_waitcnt lgkmcnt(4)
s_add_u32 m0,s58,12320
s_add_u32 s69,s69,s59
buffer_load_dwordx4 v5,s[44:47],s69 offen offset:0 lds
ds_read_b64 a[16:17],v26 offset:64
ds_read_b64 a[18:19],v26 offset:256
ds_read_b64 a[20:21],v27 offset:64
ds_read_b64 a[22:23],v27 offset:256
v_pk_add_f32 v[36:37],v[36:37],v[124:125]
v_pk_fma_f32 v[40:41],v[124:125],v[124:125],v[40:41]
v_pk_add_f32 v[38:39],v[38:39],v[126:127]
v_pk_fma_f32 v[42:43],v[126:127],v[126:127],v[42:43]
v_smfmac_f32_16x16x64_bf16 v[136:139],a[120:123],a[0:7],v10
s_waitcnt lgkmcnt(4)
ds_read_b64 a[0:1],v26 offset:72
ds_read_b64 a[2:3],v26 offset:264
ds_read_b64 a[4:5],v27 offset:72
ds_read_b64 a[6:7],v27 offset:264
v_pk_add_f32 v[36:37],v[36:37],v[128:129]
v_pk_fma_f32 v[40:41],v[128:129],v[128:129],v[40:41]
v_pk_add_f32 v[38:39],v[38:39],v[130:131]
v_pk_fma_f32 v[42:43],v[130:131],v[130:131],v[42:43]
v_smfmac_f32_16x16x64_bf16 v[140:143],a[124:127],a[8:15],v10
s_waitcnt lgkmcnt(4)
s_add_u32 m0,s57,77800
s_add_u32 s89,s56,2162688
buffer_load_dwordx4 v6,s[36:39],s89 offen lds
ds_read_b64 a[8:9],v26 offset:128
ds_read_b64 a[10:11],v26 offset:320
ds_read_b64 a[12:13],v27 offset:128
ds_read_b64 a[14:15],v27 offset:320
v_pk_add_f32 v[36:37],v[36:37],v[132:133]
v_pk_fma_f32 v[40:41],v[132:133],v[132:133],v[40:41]
v_pk_add_f32 v[38:39],v[38:39],v[134:135]
v_pk_fma_f32 v[42:43],v[134:135],v[134:135],v[42:43]
v_smfmac_f32_16x16x64_bf16 v[144:147],a[152:155],a[16:23],v10
s_waitcnt lgkmcnt(4)
ds_read_b64 a[16:17],v26 offset:136
ds_read_b64 a[18:19],v26 offset:328
ds_read_b64 a[20:21],v27 offset:136
ds_read_b64 a[22:23],v27 offset:328
v_pk_add_f32 v[36:37],v[36:37],v[136:137]
v_pk_fma_f32 v[40:41],v[136:137],v[136:137],v[40:41]
v_pk_add_f32 v[38:39],v[38:39],v[138:139]
v_pk_fma_f32 v[42:43],v[138:139],v[138:139],v[42:43]
v_smfmac_f32_16x16x64_bf16 v[148:151],a[156:159],a[0:7],v10
s_waitcnt lgkmcnt(4)
ds_read_b64 a[24:25],v248 offset:57344
ds_read_b64 a[26:27],v248 offset:57352
ds_read_b64 a[36:37],v249 offset:57344
ds_read_b64 a[38:39],v249 offset:57352
v_pk_add_f32 v[36:37],v[36:37],v[140:141]
v_pk_fma_f32 v[40:41],v[140:141],v[140:141],v[40:41]
v_pk_add_f32 v[38:39],v[38:39],v[142:143]
v_pk_fma_f32 v[42:43],v[142:143],v[142:143],v[42:43]
v_smfmac_f32_16x16x64_bf16 v[152:155],a[184:187],a[8:15],v10
s_waitcnt lgkmcnt(4)
s_add_u32 m0,s58,12320
s_add_u32 s69,s69,s53
buffer_load_dwordx4 v5,s[44:47],s69 offen offset:1024 lds
ds_read_b64 a[28:29],v248 offset:57408
ds_read_b64 a[30:31],v248 offset:57416
ds_read_b64 a[40:41],v249 offset:57408
ds_read_b64 a[42:43],v249 offset:57416
v_pk_add_f32 v[36:37],v[36:37],v[144:145]
v_pk_fma_f32 v[40:41],v[144:145],v[144:145],v[40:41]
v_pk_add_f32 v[38:39],v[38:39],v[146:147]
v_pk_fma_f32 v[42:43],v[146:147],v[146:147],v[42:43]
v_smfmac_f32_16x16x64_bf16 v[156:159],a[188:191],a[16:23],v10
ds_read_b64 a[32:33],v248 offset:57472
ds_read_b64 a[34:35],v248 offset:57480
ds_read_b64 a[44:45],v249 offset:57472
ds_read_b64 a[46:47],v249 offset:57480
v_pk_add_f32 v[36:37],v[36:37],v[148:149]
v_pk_fma_f32 v[40:41],v[148:149],v[148:149],v[40:41]
v_pk_add_f32 v[38:39],v[38:39],v[150:151]
v_pk_fma_f32 v[42:43],v[150:151],v[150:151],v[42:43]
v_pk_add_f32 v[36:37],v[36:37],v[152:153]
v_pk_fma_f32 v[40:41],v[152:153],v[152:153],v[40:41]
v_pk_add_f32 v[38:39],v[38:39],v[154:155]
v_pk_fma_f32 v[42:43],v[154:155],v[154:155],v[42:43]
v_pk_add_f32 v[36:37],v[36:37],v[156:157]
v_pk_fma_f32 v[40:41],v[156:157],v[156:157],v[40:41]
v_pk_add_f32 v[38:39],v[38:39],v[158:159]
v_pk_fma_f32 v[42:43],v[158:159],v[158:159],v[42:43]
s_nop 0
v_permlane32_swap_b32 v36,v38
v_permlane32_swap_b32 v37,v39
v_permlane32_swap_b32 v40,v42
v_permlane32_swap_b32_e64 v41,v43
v_pk_add_f32 v[36:37],v[36:37],v[38:39]
v_pk_add_f32 v[40:41],v[40:41],v[42:43]
s_add_u32 s60,s60,56576
s_sub_u32 s64,s60,88400
s_min_u32 s60,s60,s64
v_permlane16_swap_b32 v36,v37
v_permlane16_swap_b32 v40,v41
v_add_f32 v8,v36,v37
v_add_f32_e64 v9,v40,v41
v_mul_f32 v8,0x3c2aaaab,v8
v_mul_f32 v9,0x3c2aaaab,v9
v_cmp_ngt_f32 vcc,0x5d800000,v9
v_fma_f32 v9,-v8,v8,v9
v_max_f32 v9,0,v9
s_or_b64 s[90:91],s[90:91],vcc
v_add_f32 v9,0x3727c5ac,v9
v_rsq_f32 v9,v9
s_nop 0
v_mul_f32 v36,s96,v9
v_mul_f32 v37,s96,v9
v_mul_f32 v40,-v8,v36
v_mul_f32 v41,-v8,v36
v_permlane16_swap_b32 v36,v37
s_nop 0
v_permlane16_swap_b32 v40,v41
v_mov_b64 v[38:39],v[36:37]
v_mov_b64 v[42:43],v[40:41]
s_nop 0
v_permlane32_swap_b32 v36,v38
v_permlane32_swap_b32 v37,v39
v_permlane32_swap_b32 v40,v42
v_permlane32_swap_b32 v41,v43
s_waitcnt vmcnt(10)
s_barrier
ds_read_b64_tr_b16 a[192:193],v1 offset:18480
ds_read_b64_tr_b16 a[194:195],v1 offset:19504
ds_read_b64_tr_b16 a[196:197],v1 offset:20528
ds_read_b64_tr_b16 a[198:199],v1 offset:21552
v_pk_fma_f32 v[64:65],v[64:65],v[36:37],v[40:41]
v_pk_fma_f32 v[66:67],v[66:67],v[38:39],v[42:43]
v_pk_fma_f32 v[68:69],v[68:69],v[36:37],v[40:41]
v_pk_fma_f32 v[70:71],v[70:71],v[38:39],v[42:43]
v_pk_fma_f32 v[72:73],v[72:73],v[36:37],v[40:41]
v_pk_fma_f32 v[74:75],v[74:75],v[38:39],v[42:43]
v_pk_fma_f32 v[76:77],v[76:77],v[36:37],v[40:41]
v_pk_fma_f32 v[78:79],v[78:79],v[38:39],v[42:43]
v_pk_fma_f32 v[112:113],v[112:113],v[36:37],v[40:41]
v_pk_fma_f32 v[114:115],v[114:115],v[38:39],v[42:43]
ds_read_b64_tr_b16 a[200:201],v1 offset:22576
ds_read_b64_tr_b16 a[202:203],v1 offset:23600
ds_read_b64_tr_b16 a[204:205],v2 offset:22576
ds_read_b64_tr_b16 a[206:207],v2 offset:23600
v_pk_fma_f32 v[116:117],v[116:117],v[36:37],v[40:41]
v_pk_fma_f32 v[118:119],v[118:119],v[38:39],v[42:43]
s_add_u32 m0,s57,81336
s_add_u32 s89,s56,2260992
buffer_load_dwordx4 v6,s[36:39],s89 offen lds
v_pk_fma_f32 v[136:137],v[136:137],v[36:37],v[40:41]
v_pk_fma_f32 v[138:139],v[138:139],v[38:39],v[42:43]
v_pk_fma_f32 v[140:141],v[140:141],v[36:37],v[40:41]
v_pk_fma_f32 v[142:143],v[142:143],v[38:39],v[42:43]
v_pk_fma_f32 v[80:81],v[80:81],v[36:37],v[40:41]
v_pk_fma_f32 v[82:83],v[82:83],v[38:39],v[42:43]
v_pk_fma_f32 v[84:85],v[84:85],v[36:37],v[40:41]
v_pk_fma_f32 v[86:87],v[86:87],v[38:39],v[42:43]
ds_read_b64_tr_b16 a[208:209],v2 offset:20528
ds_read_b64_tr_b16 a[210:211],v2 offset:21552
ds_read_b64_tr_b16 a[212:213],v2 offset:18480
ds_read_b64_tr_b16 a[214:215],v2 offset:19504
v_pk_fma_f32 v[88:89],v[88:89],v[36:37],v[40:41]
v_pk_fma_f32 v[90:91],v[90:91],v[38:39],v[42:43]
v_pk_fma_f32 v[92:93],v[92:93],v[36:37],v[40:41]
v_pk_fma_f32 v[94:95],v[94:95],v[38:39],v[42:43]
s_add_u32 m0,s58,12320
s_add_u32 s69,s69,s53
s_nop 0
buffer_load_dwordx4 v5,s[44:47],s69 offen offset:2048 lds
v_pk_fma_f32 v[120:121],v[120:121],v[36:37],v[40:41]
v_pk_fma_f32 v[122:123],v[122:123],v[38:39],v[42:43]
v_pk_fma_f32 v[124:125],v[124:125],v[36:37],v[40:41]
v_pk_fma_f32 v[126:127],v[126:127],v[38:39],v[42:43]
v_pk_fma_f32 v[144:145],v[144:145],v[36:37],v[40:41]
v_pk_fma_f32 v[146:147],v[146:147],v[38:39],v[42:43]
ds_read_b128 v[16:19],v4 offset:0
ds_read_b128 v[20:23],v4 offset:32
ds_read_b64 v[232:233],v7 offset:18480
ds_read_b64 v[234:235],v7 offset:18992
v_pk_fma_f32 v[148:149],v[148:149],v[36:37],v[40:41]
v_pk_fma_f32 v[150:151],v[150:151],v[38:39],v[42:43]
v_pk_fma_f32 v[96:97],v[96:97],v[36:37],v[40:41]
v_pk_fma_f32 v[98:99],v[98:99],v[38:39],v[42:43]
v_pk_fma_f32 v[100:101],v[100:101],v[36:37],v[40:41]
v_pk_fma_f32 v[102:103],v[102:103],v[38:39],v[42:43]
s_add_u32 m0,s57,84864
s_add_u32 s89,s56,2359296
buffer_load_dwordx4 v6,s[36:39],s89 offen lds
v_pk_fma_f32 v[104:105],v[104:105],v[36:37],v[40:41]
v_pk_fma_f32 v[106:107],v[106:107],v[38:39],v[42:43]
v_pk_fma_f32 v[108:109],v[108:109],v[36:37],v[40:41]
v_pk_fma_f32 v[110:111],v[110:111],v[38:39],v[42:43]
ds_read_b64 v[236:237],v7 offset:19504
v_pk_fma_f32 v[128:129],v[128:129],v[36:37],v[40:41]
v_pk_fma_f32 v[130:131],v[130:131],v[38:39],v[42:43]
v_pk_fma_f32 v[132:133],v[132:133],v[36:37],v[40:41]
v_pk_fma_f32 v[134:135],v[134:135],v[38:39],v[42:43]
v_pk_fma_f32 v[152:153],v[152:153],v[36:37],v[40:41]
v_pk_fma_f32 v[154:155],v[154:155],v[38:39],v[42:43]
v_pk_fma_f32 v[156:157],v[156:157],v[36:37],v[40:41]
v_pk_fma_f32 v[158:159],v[158:159],v[38:39],v[42:43]
v_cvt_pk_bf16_f32 v160,v64,v68
v_cvt_pk_bf16_f32 v161,v72,v76
v_cvt_pk_bf16_f32 v162,v112,v116
v_cvt_pk_bf16_f32 v163,v136,v140
v_cvt_pk_bf16_f32 v164,v65,v69
v_cvt_pk_bf16_f32 v165,v73,v77
v_cvt_pk_bf16_f32 v166,v113,v117
v_cvt_pk_bf16_f32 v167,v137,v141
v_cvt_pk_bf16_f32 v176,v80,v84
v_cvt_pk_bf16_f32 v177,v88,v92
v_cvt_pk_bf16_f32 v178,v120,v124
v_cvt_pk_bf16_f32 v179,v144,v148
v_cvt_pk_bf16_f32 v180,v81,v85
v_cvt_pk_bf16_f32 v181,v89,v93
v_cvt_pk_bf16_f32 v182,v121,v125
v_cvt_pk_bf16_f32 v183,v145,v149
v_cvt_pk_bf16_f32 v192,v96,v100
v_cvt_pk_bf16_f32 v193,v104,v108
v_cvt_pk_bf16_f32 v194,v128,v132
v_cvt_pk_bf16_f32 v195,v152,v156
v_cvt_pk_bf16_f32 v196,v97,v101
v_cvt_pk_bf16_f32 v197,v105,v109
v_cvt_pk_bf16_f32 v198,v129,v133
v_cvt_pk_bf16_f32 v199,v153,v157
s_waitcnt lgkmcnt(0)
v_or3_b32 v252,v232,v233,0
v_or3_b32 v252,v234,v235,v252
v_or3_b32 v252,v236,v237,v252
v_mfma_f32_16x16x32_bf16 v[44:47],a[192:195],v[160:163],v[16:19]
ds_read_b128 a[48:51],v13 offset:0
v_cvt_pk_bf16_f32 v168,v66,v70
v_cvt_pk_bf16_f32 v169,v74,v78
v_mfma_f32_16x16x32_bf16 v[44:47],a[196:199],v[176:179],v[44:47]
ds_read_b128 v[208:211],v4 offset:3584
v_cvt_pk_bf16_f32 v170,v114,v118
v_cvt_pk_bf16_f32 v171,v138,v142
v_mfma_f32_16x16x32_bf16 v[44:47],a[200:203],v[192:195],v[44:47]
ds_read_b128 a[52:55],v13 offset:256
v_cvt_pk_bf16_f32 v172,v67,v71
v_cvt_pk_bf16_f32 v173,v75,v79
v_mfma_f32_16x16x32_bf16 v[48:51],a[204:207],v[192:195],v[20:23]
ds_read_b128 v[212:215],v4 offset:3616
v_cvt_pk_bf16_f32 v174,v115,v119
v_cvt_pk_bf16_f32 v175,v139,v143
v_mfma_f32_16x16x32_bf16 v[48:51],a[208:211],v[176:179],v[48:51]
ds_read_b128 a[56:59],v13 offset:512
v_cvt_pk_bf16_f32 v184,v82,v86
v_cvt_pk_bf16_f32 v185,v90,v94
v_mfma_f32_16x16x32_bf16 v[48:51],a[212:215],v[160:163],v[48:51]
ds_read_b128 v[216:219],v4 offset:3712
v_cvt_pk_bf16_f32 v186,v122,v126
v_cvt_pk_bf16_f32 v187,v146,v150
v_mfma_f32_16x16x32_bf16 v[52:55],a[212:215],v[164:167],v[20:23]
TE01_%=:
s_cbranch_execz TP3284_%=
ds_read_b128 a[60:63],v13 offset:768
v_cvt_pk_bf16_f32 v188,v83,v87
v_cvt_pk_bf16_f32 v189,v91,v95
v_mfma_f32_16x16x32_bf16 v[52:55],a[208:211],v[180:183],v[52:55]
ds_read_b128 v[220:223],v4 offset:3744
v_cvt_pk_bf16_f32 v190,v123,v127
v_cvt_pk_bf16_f32 v191,v147,v151
v_mfma_f32_16x16x32_bf16 v[52:55],a[204:207],v[196:199],v[52:55]
ds_read_b128 a[64:67],v13 offset:1024
v_cvt_pk_bf16_f32 v200,v98,v102
v_cvt_pk_bf16_f32 v201,v106,v110
v_mfma_f32_16x16x32_bf16 v[56:59],a[200:203],v[196:199],v[16:19]
ds_read_b128 v[224:227],v4 offset:3840
v_cvt_pk_bf16_f32 v202,v130,v134
v_cvt_pk_bf16_f32 v203,v154,v158
v_mfma_f32_16x16x32_bf16 v[56:59],a[196:199],v[180:183],v[56:59]
ds_read_b128 a[68:71],v13 offset:1280
v_cvt_pk_bf16_f32 v204,v99,v103
v_cvt_pk_bf16_f32 v205,v107,v111
v_mfma_f32_16x16x32_bf16 v[56:59],a[192:195],v[164:167],v[56:59]
ds_read_b128 v[228:231],v4 offset:3872
v_cvt_pk_bf16_f32 v206,v131,v135
v_cvt_pk_bf16_f32 v207,v155,v159
v_mfma_f32_16x16x32_bf16 v[60:63],a[192:195],v[168:171],v[16:19]
ds_read_b64 v[14:15],v7 offset:0
ds_read_b64 a[0:1],v250 offset:57344
v_lshlrev_b32_sdwa v44,1,v44 dst_sel:DWORD dst_unused:UNUSED_PAD src0_sel:DWORD src1_sel:WORD_1
v_mfma_f32_16x16x32_bf16 v[60:63],a[196:199],v[184:187],v[60:63]
ds_read_b64 a[2:3],v250 offset:57352
ds_read_b64 a[12:13],v251 offset:57344
v_fma_mixlo_f16 v8,v53,s99,0
v_mfma_f32_16x16x32_bf16 v[60:63],a[200:203],v[200:203],v[60:63]
ds_read_b64 a[14:15],v251 offset:57352
ds_read_b64 a[4:5],v250 offset:57408
v_lshlrev_b32_sdwa v46,1,v46 dst_sel:DWORD dst_unused:UNUSED_PAD src0_sel:DWORD src1_sel:WORD_1
v_mfma_f32_16x16x32_bf16 v[32:35],a[204:207],v[200:203],v[20:23]
ds_read_b64 a[6:7],v250 offset:57416
ds_read_b64 a[16:17],v251 offset:57408
v_fma_mixhi_f16 v8,v55,s99,0
v_mfma_f32_16x16x32_bf16 v[32:35],a[208:211],v[184:187],v[32:35]
ds_read_b64 a[18:19],v251 offset:57416
ds_read_b64 a[8:9],v250 offset:57472
v_lshlrev_b32_sdwa v45,1,v45 dst_sel:DWORD dst_unused:UNUSED_PAD src0_sel:DWORD src1_sel:WORD_1
v_pk_fma_f16 v9,v8,v8,s54
v_mfma_f32_16x16x32_bf16 v[32:35],a[212:215],v[168:171],v[32:35]
s_waitcnt lgkmcnt(9)
ds_read_b64 a[10:11],v250 offset:57480
ds_read_b64 a[20:21],v251 offset:57472
v_or3_b32 v252,v14,v15,v252
v_lshlrev_b32_sdwa v47,1,v47 dst_sel:DWORD dst_unused:UNUSED_PAD src0_sel:DWORD src1_sel:WORD_1
v_mfma_f32_16x16x32_bf16 v[36:39],a[212:215],v[172:175],v[20:23]
ds_read_b64 v[14:15],v7 offset:512
ds_read_b128 v[20:23],v4 offset:128
v_pk_mul_f16 v9,v8,v9
v_mfma_f32_16x16x32_bf16 v[36:39],a[208:211],v[188:191],v[36:39]
ds_read_b64 a[22:23],v251 offset:57480
v_exp_f16_e64 v8,v9
v_lshlrev_b32_sdwa v48,1,v48 dst_sel:DWORD dst_unused:UNUSED_PAD src0_sel:DWORD src1_sel:WORD_1
v_mfma_f32_16x16x32_bf16 v[36:39],a[204:207],v[204:207],v[36:39]
ds_read_b64_tr_b16 a[216:217],v1 offset:0
ds_read_b64_tr_b16 a[218:219],v1 offset:1024
v_mfma_f32_16x16x32_bf16 v[40:43],a[200:203],v[204:207],v[16:19]
ds_read_b128 v[16:19],v4 offset:160
ds_read_b64_tr_b16 a[220:221],v1 offset:2048
v_lshlrev_b32_sdwa v50,1,v50 dst_sel:DWORD dst_unused:UNUSED_PAD src0_sel:DWORD src1_sel:WORD_1
v_mfma_f32_16x16x32_bf16 v[40:43],a[196:199],v[188:191],v[40:43]
ds_read_b64_tr_b16 a[222:223],v1 offset:3072
ds_read_u16 v44,v44
v_lshlrev_b32_sdwa v49,1,v49 dst_sel:DWORD dst_unused:UNUSED_PAD src0_sel:DWORD src1_sel:WORD_1
v_mfma_f32_16x16x32_bf16 v[40:43],a[192:195],v[172:175],v[40:43]
s_waitcnt lgkmcnt(8)
ds_read_u16_d16_hi v46,v46
ds_read_u16 v45,v45
v_or3_b32 v252,v14,v15,v252
v_mfma_f32_16x16x32_bf16 v[136:139],a[48:51],a[24:27],v[208:211]
ds_read_b64 v[14:15],v7 offset:1024
ds_read_u16_d16_hi v47,v47
v_exp_f16_sdwa v8,v9 dst_sel:WORD_1 dst_unused:UNUSED_PRESERVE src0_sel:WORD_1
v_lshlrev_b32_sdwa v51,1,v51 dst_sel:DWORD dst_unused:UNUSED_PAD src0_sel:DWORD src1_sel:WORD_1
v_mfma_f32_16x16x32_bf16 v[140:143],a[48:51],a[36:39],v[208:211]
ds_read_b64_tr_b16 a[72:73],v1 offset:4096
ds_read_b64_tr_b16 a[74:75],v1 offset:5120
v_lshlrev_b32_sdwa v56,1,v56 dst_sel:DWORD dst_unused:UNUSED_PAD src0_sel:DWORD src1_sel:WORD_1
v_mfma_f32_16x16x32_bf16 v[68:71],a[52:55],a[36:39],v[212:215]
ds_read_u16 v48,v48
ds_read_u16_d16_hi v50,v50
v_pk_fma_f16 v8,v8,s55,s55
v_mfma_f32_16x16x32_bf16 v[64:67],a[52:55],a[24:27],v[212:215]
ds_read_u16 v49,v49
v_rcp_f16 v9,v8
v_lshlrev_b32_sdwa v58,1,v58 dst_sel:DWORD dst_unused:UNUSED_PAD src0_sel:DWORD src1_sel:WORD_1
v_mfma_f32_16x16x32_bf16 v[144:147],a[56:59],a[28:31],v[216:219]
ds_read_u16_d16_hi v51,v51
ds_read_b64_tr_b16 a[76:77],v2 offset:4096
v_mfma_f32_16x16x32_bf16 v[148:151],a[56:59],a[40:43],v[216:219]
s_waitcnt lgkmcnt(8)
ds_read_b64_tr_b16 a[78:79],v2 offset:5120
ds_read_b64_tr_b16 a[80:81],v2 offset:2048
v_or3_b32 v252,v14,v15,v252
v_lshlrev_b32_sdwa v57,1,v57 dst_sel:DWORD dst_unused:UNUSED_PAD src0_sel:DWORD src1_sel:WORD_1
v_mfma_f32_16x16x32_bf16 v[84:87],a[60:63],a[40:43],v[220:223]
ds_read_b64_tr_b16 a[82:83],v2 offset:3072
ds_read_u16 v56,v56
v_lshlrev_b32_sdwa v59,1,v59 dst_sel:DWORD dst_unused:UNUSED_PAD src0_sel:DWORD src1_sel:WORD_1
v_mfma_f32_16x16x32_bf16 v[80:83],a[60:63],a[28:31],v[220:223]
ds_read_u16_d16_hi v58,v58
v_rcp_f16_sdwa v9,v8 dst_sel:WORD_1 dst_unused:UNUSED_PRESERVE src0_sel:WORD_1
v_mfma_f32_16x16x32_bf16 v[152:155],a[64:67],a[32:35],v[224:227]
ds_read_u16 v57,v57
ds_read_u16_d16_hi v59,v59
v_lshlrev_b32_sdwa v52,1,v52 dst_sel:DWORD dst_unused:UNUSED_PAD src0_sel:DWORD src1_sel:WORD_1
v_mfma_f32_16x16x32_bf16 v[156:159],a[64:67],a[44:47],v[224:227]
ds_read_b64_tr_b16 a[84:85],v2 offset:0
ds_read_b64_tr_b16 a[86:87],v2 offset:1024
v_lshlrev_b32_sdwa v54,1,v54 dst_sel:DWORD dst_unused:UNUSED_PAD src0_sel:DWORD src1_sel:WORD_1
v_mfma_f32_16x16x32_bf16 v[100:103],a[68:71],a[44:47],v[228:231]
s_waitcnt vmcnt(7)
ds_read_u16 v52,v52
ds_read_u16_d16_hi v54,v54
v_fma_mix_f32 v53,v53,v9,0 op_sel_hi:[0,1,0]
v_mfma_f32_16x16x32_bf16 v[96:99],a[68:71],a[32:35],v[228:231]
s_barrier
ds_read_b64_tr_b16 a[224:225],v3 offset:0
ds_read_b64_tr_b16 a[226:227],v3 offset:1536
v_fma_mix_f32 v55,v55,v9,0 op_sel:[0,1,0] op_sel_hi:[0,1,0]
v_mfma_f32_16x16x32_bf16 v[24:27],a[216:219],v[160:163],v[20:23]
s_add_u32 m0,s57,0
ds_read_b64_tr_b16 a[228:229],v3 offset:8
ds_read_b64_tr_b16 a[230:231],v3 offset:1544
v_lshlrev_b32_sdwa v60,1,v60 dst_sel:DWORD dst_unused:UNUSED_PAD src0_sel:DWORD src1_sel:WORD_1
v_mfma_f32_16x16x32_bf16 v[24:27],a[220:223],v[176:179],v[24:27]
s_waitcnt lgkmcnt(15)
ds_read_b64_tr_b16 a[232:233],v3 offset:64
ds_read_b64_tr_b16 a[234:235],v3 offset:1600
v_lshl_add_u64 v[44:45],v[44:45],0,v[46:47]
v_mfma_f32_16x16x32_bf16 v[24:27],a[72:75],v[192:195],v[24:27]
s_waitcnt lgkmcnt(15)
ds_read_b64_tr_b16 a[236:237],v3 offset:72
ds_read_b64_tr_b16 a[238:239],v3 offset:1608
v_lshl_add_u64 v[46:47],v[48:49],0,v[50:51]
v_mfma_f32_16x16x32_bf16 v[28:31],a[76:79],v[192:195],v[16:19]
s_waitcnt lgkmcnt(15)
ds_read_b64_tr_b16 a[240:241],v3 offset:128
ds_read_b64_tr_b16 a[242:243],v3 offset:1664
v_lshlrev_b32_sdwa v62,1,v62 dst_sel:DWORD dst_unused:UNUSED_PAD src0_sel:DWORD src1_sel:WORD_1
v_mfma_f32_16x16x32_bf16 v[28:31],a[80:83],v[176:179],v[28:31]
s_waitcnt lgkmcnt(12)
ds_read_b64_tr_b16 a[244:245],v3 offset:136
v_lshl_add_u64 v[48:49],v[56:57],0,v[58:59]
v_mfma_f32_16x16x32_bf16 v[28:31],a[84:87],v[160:163],v[28:31]
s_add_u32 s89,s56,2457600
ds_read_b64_tr_b16 a[246:247],v3 offset:1672
v_lshlrev_b32_sdwa v61,1,v61 dst_sel:DWORD dst_unused:UNUSED_PAD src0_sel:DWORD src1_sel:WORD_1
v_mfma_f32_16x16x32_bf16 v[56:59],a[84:87],v[164:167],v[16:19]
s_waitcnt lgkmcnt(12)
buffer_load_dwordx4 v6,s[36:39],s89 offen lds
v_or_b32_e64 v50,v52,v54
v_mfma_f32_16x16x32_bf16 v[56:59],a[80:83],v[180:183],v[56:59]
s_add_u32 m0,s58,18480
ds_read_u16 v60,v60
v_cvt_pk_bf16_f32 v51,v53,v55
v_lshlrev_b32_sdwa v63,1,v63 dst_sel:DWORD dst_unused:UNUSED_PAD src0_sel:DWORD src1_sel:WORD_1
v_mfma_f32_16x16x32_bf16 v[56:59],a[76:79],v[196:199],v[56:59]
s_add_u32 s69,s69,s59
ds_read_u16_d16_hi v62,v62
v_lshlrev_b32_sdwa v32,1,v32 dst_sel:DWORD dst_unused:UNUSED_PAD src0_sel:DWORD src1_sel:WORD_1
v_mfma_f32_16x16x32_bf16 v[52:55],a[72:75],v[196:199],v[20:23]
buffer_load_dwordx4 v5,s[44:47],s69 offen offset:0 lds
v_lshlrev_b32_sdwa v34,1,v34 dst_sel:DWORD dst_unused:UNUSED_PAD src0_sel:DWORD src1_sel:WORD_1
v_mfma_f32_16x16x32_bf16 v[52:55],a[220:223],v[180:183],v[52:55]
s_add_u32 s69,s69,s53
ds_read_u16 v61,v61
v_lshlrev_b32_sdwa v33,1,v33 dst_sel:DWORD dst_unused:UNUSED_PAD src0_sel:DWORD src1_sel:WORD_1
v_mfma_f32_16x16x32_bf16 v[52:55],a[216:219],v[164:167],v[52:55]
s_waitcnt lgkmcnt(7)
buffer_load_dwordx4 v5,s[44:47],s69 offen offset:1024 lds
ds_read_u16_d16_hi v63,v63
v_mfma_f32_16x16x32_bf16 v[136:139],a[224:227],v[44:47],v[136:139]
s_add_u32 s69,s69,s53
ds_read_u16 v32,v32
ds_read_u16_d16_hi v34,v34
v_mfma_f32_16x16x32_bf16 v[140:143],a[224:227],v[48:51],v[140:143]
buffer_load_dwordx4 v5,s[44:47],s69 offen offset:2048 lds
v_lshlrev_b32_sdwa v35,1,v35 dst_sel:DWORD dst_unused:UNUSED_PAD src0_sel:DWORD src1_sel:WORD_1
v_mfma_f32_16x16x32_bf16 v[68:71],a[228:231],v[48:51],v[68:71]
ds_read_u16 v33,v33
ds_read_u16_d16_hi v35,v35
v_mfma_f32_16x16x32_bf16 v[64:67],a[228:231],v[44:47],v[64:67]
ds_read_b64_tr_b16 a[248:249],v3 offset:6160
v_lshlrev_b32_sdwa v40,1,v40 dst_sel:DWORD dst_unused:UNUSED_PAD src0_sel:DWORD src1_sel:WORD_1
v_mfma_f32_16x16x32_bf16 v[144:147],a[232:235],v[44:47],v[144:147]
s_add_u32 m0,s57,3544
ds_read_u16 v40,v40
ds_read_b64_tr_b16 a[250:251],v3 offset:7696
v_mfma_f32_16x16x32_bf16 v[148:151],a[232:235],v[48:51],v[148:151]
s_add_u32 s89,s56,2555904
ds_read_b64_tr_b16 a[252:253],v3 offset:6168
v_lshlrev_b32_sdwa v42,1,v42 dst_sel:DWORD dst_unused:UNUSED_PAD src0_sel:DWORD src1_sel:WORD_1
v_mfma_f32_16x16x32_bf16 v[84:87],a[236:239],v[48:51],v[84:87]
buffer_load_dwordx4 v6,s[36:39],s89 offen lds
ds_read_u16_d16_hi v42,v42
v_mfma_f32_16x16x32_bf16 v[80:83],a[236:239],v[44:47],v[80:83]
s_waitcnt lgkmcnt(10)
ds_read_b64_tr_b16 a[254:255],v3 offset:7704
v_lshlrev_b32_sdwa v41,1,v41 dst_sel:DWORD dst_unused:UNUSED_PAD src0_sel:DWORD src1_sel:WORD_1
v_mfma_f32_16x16x32_bf16 v[152:155],a[240:243],v[44:47],v[152:155]
ds_read_u16 v41,v41
v_lshlrev_b32_sdwa v43,1,v43 dst_sel:DWORD dst_unused:UNUSED_PAD src0_sel:DWORD src1_sel:WORD_1
v_mfma_f32_16x16x32_bf16 v[156:159],a[240:243],v[48:51],v[156:159]
ds_read_u16_d16_hi v43,v43
v_lshlrev_b32_sdwa v36,1,v36 dst_sel:DWORD dst_unused:UNUSED_PAD src0_sel:DWORD src1_sel:WORD_1
v_mfma_f32_16x16x32_bf16 v[100:103],a[244:247],v[48:51],v[100:103]
ds_read_u16 v36,v36
v_lshlrev_b32_sdwa v38,1,v38 dst_sel:DWORD dst_unused:UNUSED_PAD src0_sel:DWORD src1_sel:WORD_1
v_mfma_f32_16x16x32_bf16 v[96:99],a[244:247],v[44:47],v[96:99]
ds_read_u16_d16_hi v38,v38
v_lshlrev_b32_sdwa v37,1,v37 dst_sel:DWORD dst_unused:UNUSED_PAD src0_sel:DWORD src1_sel:WORD_1
v_mfma_f32_16x16x32_bf16 v[112:115],a[48:51],a[0:3],v[208:211]
ds_read_u16 v37,v37
v_lshlrev_b32_sdwa v39,1,v39 dst_sel:DWORD dst_unused:UNUSED_PAD src0_sel:DWORD src1_sel:WORD_1
v_mfma_f32_16x16x32_bf16 v[116:119],a[48:51],a[12:15],v[208:211]
ds_read_u16_d16_hi v39,v39
ds_read_b64_tr_b16 a[92:93],v3 offset:6224
v_mfma_f32_16x16x32_bf16 v[76:79],a[52:55],a[12:15],v[212:215]
ds_read_b64_tr_b16 a[94:95],v3 offset:7760
v_mfma_f32_16x16x32_bf16 v[72:75],a[52:55],a[0:3],v[212:215]
ds_read_b64_tr_b16 a[48:49],v3 offset:6232
v_mfma_f32_16x16x32_bf16 v[120:123],a[56:59],a[4:7],v[216:219]
ds_read_b64_tr_b16 a[50:51],v3 offset:7768
v_mfma_f32_16x16x32_bf16 v[124:127],a[56:59],a[16:19],v[216:219]
ds_read_b64_tr_b16 a[56:57],v3 offset:6288
v_mfma_f32_16x16x32_bf16 v[92:95],a[60:63],a[16:19],v[220:223]
ds_read_b64_tr_b16 a[58:59],v3 offset:7824
v_mfma_f32_16x16x32_bf16 v[88:91],a[60:63],a[4:7],v[220:223]
v_mfma_f32_16x16x32_bf16 v[128:131],a[64:67],a[8:11],v[224:227]
v_mfma_f32_16x16x32_bf16 v[132:135],a[64:67],a[20:23],v[224:227]
ds_read_b64_tr_b16 a[64:65],v3 offset:6296
v_mfma_f32_16x16x32_bf16 v[108:111],a[68:71],a[20:23],v[228:231]
ds_read_b64_tr_b16 a[66:67],v3 offset:7832
v_mfma_f32_16x16x32_bf16 v[104:107],a[68:71],a[8:11],v[228:231]
s_add_u32 m0,s57,7080
v_mfma_f32_16x16x32_bf16 v[48:51],a[216:219],v[168:171],v[20:23]
s_add_u32 s89,s56,2654208
ds_read_b64 v[14:15],v7 offset:6160
ds_read_b64_tr_b16 a[212:213],v1 offset:6160
v_lshlrev_b32_sdwa v24,1,v24 dst_sel:DWORD dst_unused:UNUSED_PAD src0_sel:DWORD src1_sel:WORD_1
v_mfma_f32_16x16x32_bf16 v[48:51],a[220:223],v[184:187],v[48:51]
s_waitcnt lgkmcnt(6)
buffer_load_dwordx4 v6,s[36:39],s89 offen lds
ds_read_b64_tr_b16 a[214:215],v1 offset:7184
v_lshl_add_u64 v[60:61],v[60:61],0,v[62:63]
v_fma_mixlo_f16 v8,v57,s99,0
v_mfma_f32_16x16x32_bf16 v[48:51],a[72:75],v[200:203],v[48:51]
ds_read_b64_tr_b16 a[208:209],v1 offset:8208
ds_read_b64_tr_b16 a[210:211],v1 offset:9232
v_lshl_add_u64 v[62:63],v[32:33],0,v[34:35]
v_lshlrev_b32_sdwa v26,1,v26 dst_sel:DWORD dst_unused:UNUSED_PAD src0_sel:DWORD src1_sel:WORD_1
v_mfma_f32_16x16x32_bf16 v[44:47],a[76:79],v[200:203],v[16:19]
ds_read_u16 v24,v24
ds_read_u16_d16_hi v26,v26
v_fma_mixhi_f16 v8,v59,s99,0
v_mfma_f32_16x16x32_bf16 v[44:47],a[80:83],v[184:187],v[44:47]
ds_read_b64_tr_b16 a[204:205],v1 offset:10256
ds_read_b64_tr_b16 a[206:207],v1 offset:11280
v_lshl_add_u64 v[32:33],v[40:41],0,v[42:43]
v_lshlrev_b32_sdwa v25,1,v25 dst_sel:DWORD dst_unused:UNUSED_PAD src0_sel:DWORD src1_sel:WORD_1
v_pk_fma_f16 v9,v8,v8,s54
v_mfma_f32_16x16x32_bf16 v[44:47],a[84:87],v[168:171],v[44:47]
s_waitcnt lgkmcnt(8)
ds_read_u16 v25,v25
ds_read_b64_tr_b16 a[200:201],v2 offset:10256
v_or3_b32 v252,v14,v15,v252
v_lshlrev_b32_sdwa v27,1,v27 dst_sel:DWORD dst_unused:UNUSED_PAD src0_sel:DWORD src1_sel:WORD_1
v_mfma_f32_16x16x32_bf16 v[40:43],a[84:87],v[172:175],v[16:19]
s_nop 0
ds_read_b64 v[14:15],v7 offset:6672
ds_read_b128 v[16:19],v4 offset:256
v_pk_mul_f16 v9,v8,v9
v_mfma_f32_16x16x32_bf16 v[40:43],a[80:83],v[188:191],v[40:43]
ds_read_u16_d16_hi v27,v27
ds_read_b64_tr_b16 a[202:203],v2 offset:11280
v_lshl_add_u64 v[34:35],v[36:37],0,v[38:39]
v_lshlrev_b32_sdwa v28,1,v28 dst_sel:DWORD dst_unused:UNUSED_PAD src0_sel:DWORD src1_sel:WORD_1
v_mfma_f32_16x16x32_bf16 v[40:43],a[76:79],v[204:207],v[40:43]
ds_read_u16 v28,v28
v_exp_f16 v8,v9
v_mfma_f32_16x16x32_bf16 v[36:39],a[72:75],v[204:207],v[20:23]
ds_read_b128 v[20:23],v4 offset:288
ds_read_b64_tr_b16 a[196:197],v2 offset:8208
v_lshlrev_b32_sdwa v30,1,v30 dst_sel:DWORD dst_unused:UNUSED_PAD src0_sel:DWORD src1_sel:WORD_1
v_mfma_f32_16x16x32_bf16 v[36:39],a[220:223],v[188:191],v[36:39]
ds_read_u16_d16_hi v30,v30
v_lshlrev_b32_sdwa v29,1,v29 dst_sel:DWORD dst_unused:UNUSED_PAD src0_sel:DWORD src1_sel:WORD_1
v_mfma_f32_16x16x32_bf16 v[36:39],a[216:219],v[172:175],v[36:39]
s_waitcnt lgkmcnt(7)
ds_read_u16 v29,v29
v_or3_b32 v252,v14,v15,v252
v_mfma_f32_16x16x32_bf16 v[112:115],a[224:227],v[60:63],v[112:115]
s_add_u32 m0,s57,10608
ds_read_b64 v[14:15],v7 offset:7184
v_exp_f16_sdwa v8,v9 dst_sel:WORD_1 dst_unused:UNUSED_PRESERVE src0_sel:WORD_1
v_lshlrev_b32_sdwa v31,1,v31 dst_sel:DWORD dst_unused:UNUSED_PAD src0_sel:DWORD src1_sel:WORD_1
v_mfma_f32_16x16x32_bf16 v[116:119],a[224:227],v[32:35],v[116:119]
s_add_u32 s89,s56,2752512
ds_read_u16_d16_hi v31,v31
v_lshlrev_b32_sdwa v52,1,v52 dst_sel:DWORD dst_unused:UNUSED_PAD src0_sel:DWORD src1_sel:WORD_1
v_mfma_f32_16x16x32_bf16 v[76:79],a[228:231],v[32:35],v[76:79]
buffer_load_dwordx4 v6,s[36:39],s89 offen lds
ds_read_b64_tr_b16 a[198:199],v2 offset:9232
v_pk_fma_f16 v8,v8,s55,s55
v_mfma_f32_16x16x32_bf16 v[72:75],a[228:231],v[60:63],v[72:75]
v_rcp_f16 v9,v8
v_lshlrev_b32_sdwa v54,1,v54 dst_sel:DWORD dst_unused:UNUSED_PAD src0_sel:DWORD src1_sel:WORD_1
v_mfma_f32_16x16x32_bf16 v[120:123],a[232:235],v[60:63],v[120:123]
ds_read_u16 v52,v52
v_lshlrev_b32_sdwa v53,1,v53 dst_sel:DWORD dst_unused:UNUSED_PAD src0_sel:DWORD src1_sel:WORD_1
v_mfma_f32_16x16x32_bf16 v[124:127],a[232:235],v[32:35],v[124:127]
s_waitcnt lgkmcnt(3)
ds_read_u16_d16_hi v54,v54
v_or3_b32 v252,v14,v15,v252
v_mfma_f32_16x16x32_bf16 v[92:95],a[236:239],v[32:35],v[92:95]
ds_read_u16 v53,v53
v_lshlrev_b32_sdwa v55,1,v55 dst_sel:DWORD dst_unused:UNUSED_PAD src0_sel:DWORD src1_sel:WORD_1
v_mfma_f32_16x16x32_bf16 v[88:91],a[236:239],v[60:63],v[88:91]
v_rcp_f16_sdwa v9,v8 dst_sel:WORD_1 dst_unused:UNUSED_PRESERVE src0_sel:WORD_1
v_mfma_f32_16x16x32_bf16 v[128:131],a[240:243],v[60:63],v[128:131]
ds_read_u16_d16_hi v55,v55
v_lshlrev_b32_sdwa v56,1,v56 dst_sel:DWORD dst_unused:UNUSED_PAD src0_sel:DWORD src1_sel:WORD_1
v_mfma_f32_16x16x32_bf16 v[132:135],a[240:243],v[32:35],v[132:135]
ds_read_b64_tr_b16 a[192:193],v2 offset:6160
ds_read_b64_tr_b16 a[194:195],v2 offset:7184
v_lshlrev_b32_sdwa v58,1,v58 dst_sel:DWORD dst_unused:UNUSED_PAD src0_sel:DWORD src1_sel:WORD_1
v_mfma_f32_16x16x32_bf16 v[108:111],a[244:247],v[32:35],v[108:111]
ds_read_u16 v56,v56
v_fma_mix_f32 v57,v57,v9,0 op_sel_hi:[0,1,0]
v_mfma_f32_16x16x32_bf16 v[104:107],a[244:247],v[60:63],v[104:107]
ds_read_u16_d16_hi v58,v58
v_fma_mix_f32 v59,v59,v9,0 op_sel:[0,1,0] op_sel_hi:[0,1,0]
v_mfma_f32_16x16x32_bf16 v[32:35],a[212:215],v[160:163],v[16:19]
v_lshlrev_b32_sdwa v48,1,v48 dst_sel:DWORD dst_unused:UNUSED_PAD src0_sel:DWORD src1_sel:WORD_1
v_mfma_f32_32x32x16_bf16 v[208:223],a[88:91],a[88:91],0
v_lshlrev_b32_sdwa v50,1,v50 dst_sel:DWORD dst_unused:UNUSED_PAD src0_sel:DWORD src1_sel:WORD_1
v_lshlrev_b32_sdwa v49,1,v49 dst_sel:DWORD dst_unused:UNUSED_PAD src0_sel:DWORD src1_sel:WORD_1
v_lshlrev_b32_sdwa v51,1,v51 dst_sel:DWORD dst_unused:UNUSED_PAD src0_sel:DWORD src1_sel:WORD_1
v_lshlrev_b32_sdwa v44,1,v44 dst_sel:DWORD dst_unused:UNUSED_PAD src0_sel:DWORD src1_sel:WORD_1
v_lshlrev_b32_sdwa v46,1,v46 dst_sel:DWORD dst_unused:UNUSED_PAD src0_sel:DWORD src1_sel:WORD_1
v_mfma_f32_16x16x32_bf16 v[32:35],a[208:211],v[176:179],v[32:35]
s_add_u32 m0,s57,14144
ds_read_u16 v48,v48
ds_read_u16_d16_hi v50,v50
v_lshl_add_u64 v[24:25],v[24:25],0,v[26:27]
v_mfma_f32_16x16x32_bf16 v[32:35],a[204:207],v[192:195],v[32:35]
s_waitcnt lgkmcnt(8)
ds_read_u16 v49,v49
v_lshl_add_u64 v[26:27],v[28:29],0,v[30:31]
v_mfma_f32_16x16x32_bf16 v[60:63],a[200:203],v[192:195],v[20:23]
s_add_u32 s89,s56,2850816
ds_read_u16_d16_hi v51,v51
v_mfma_f32_32x32x16_bf16 v[224:239],a[88:91],a[88:91],0
v_accvgpr_write_b32 a0,v160
v_accvgpr_write_b32 a1,v161
v_accvgpr_write_b32 a2,v162
v_accvgpr_write_b32 a3,v163
v_lshlrev_b32_sdwa v45,1,v45 dst_sel:DWORD dst_unused:UNUSED_PAD src0_sel:DWORD src1_sel:WORD_1
v_mfma_f32_16x16x32_bf16 v[60:63],a[196:199],v[176:179],v[60:63]
s_waitcnt lgkmcnt(6)
buffer_load_dwordx4 v6,s[36:39],s89 offen lds
v_lshl_add_u64 v[28:29],v[52:53],0,v[54:55]
v_mfma_f32_16x16x32_bf16 v[60:63],a[192:195],a[0:3],v[60:63]
ds_read_u16 v44,v44
v_lshlrev_b32_sdwa v47,1,v47 dst_sel:DWORD dst_unused:UNUSED_PAD src0_sel:DWORD src1_sel:WORD_1
v_mfma_f32_16x16x32_bf16 v[52:55],a[192:195],v[164:167],v[20:23]
s_waitcnt lgkmcnt(5)
ds_read_u16_d16_hi v46,v46
v_or_b32_e64 v30,v56,v58
v_mfma_f32_16x16x32_bf16 v[52:55],a[196:199],v[180:183],v[52:55]
s_waitcnt vmcnt(9)
ds_read_u16 v45,v45
v_cvt_pk_bf16_f32 v31,v57,v59
v_mfma_f32_16x16x32_bf16 v[52:55],a[200:203],v[196:199],v[52:55]
s_barrier
ds_read_u16_d16_hi v47,v47
v_accvgpr_write_b32 a4,v164
v_mfma_f32_16x16x32_bf16 v[56:59],a[204:207],v[196:199],v[16:19]
ds_read_b64_tr_b16 a[224:225],v3 offset:12320
v_lshlrev_b32_sdwa v36,1,v36 dst_sel:DWORD dst_unused:UNUSED_PAD src0_sel:DWORD src1_sel:WORD_1
v_mfma_f32_16x16x32_bf16 v[56:59],a[208:211],v[180:183],v[56:59]
ds_read_u16 v36,v36
v_accvgpr_write_b32 a5,v165
v_mfma_f32_16x16x32_bf16 v[56:59],a[212:215],v[164:167],v[56:59]
s_add_u32 m0,s58,0
ds_read_b64_tr_b16 a[226:227],v3 offset:13856
v_lshlrev_b32_sdwa v38,1,v38 dst_sel:DWORD dst_unused:UNUSED_PAD src0_sel:DWORD src1_sel:WORD_1
v_mfma_f32_16x16x32_bf16 v[136:139],a[248:251],v[24:27],v[136:139]
s_add_u32 s69,s69,s59
ds_read_u16_d16_hi v38,v38
v_accvgpr_write_b32 a6,v166
v_mfma_f32_16x16x32_bf16 v[140:143],a[248:251],v[28:31],v[140:143]
buffer_load_dwordx4 v5,s[44:47],s69 offen offset:0 lds
v_lshlrev_b32_sdwa v37,1,v37 dst_sel:DWORD dst_unused:UNUSED_PAD src0_sel:DWORD src1_sel:WORD_1
v_mfma_f32_16x16x32_bf16 v[68:71],a[252:255],v[28:31],v[68:71]
ds_read_u16 v37,v37
v_lshlrev_b32_sdwa v39,1,v39 dst_sel:DWORD dst_unused:UNUSED_PAD src0_sel:DWORD src1_sel:WORD_1
v_mfma_f32_16x16x32_bf16 v[64:67],a[252:255],v[24:27],v[64:67]
ds_read_u16_d16_hi v39,v39
v_accvgpr_write_b32 a7,v167
v_mfma_f32_16x16x32_bf16 v[144:147],a[92:95],v[24:27],v[144:147]
s_add_u32 s69,s69,s53
ds_read_b64_tr_b16 a[228:229],v3 offset:12328
v_lshlrev_b32_sdwa v40,1,v40 dst_sel:DWORD dst_unused:UNUSED_PAD src0_sel:DWORD src1_sel:WORD_1
v_mfma_f32_16x16x32_bf16 v[148:151],a[92:95],v[28:31],v[148:151]
buffer_load_dwordx4 v5,s[44:47],s69 offen offset:1024 lds
ds_read_u16 v40,v40
v_mfma_f32_16x16x32_bf16 v[84:87],a[48:51],v[28:31],v[84:87]
s_add_u32 m0,s57,17688
ds_read_b64_tr_b16 a[230:231],v3 offset:13864
ds_read_b64_tr_b16 a[232:233],v3 offset:12384
v_accvgpr_write_b32 a8,v168
v_mfma_f32_16x16x32_bf16 v[80:83],a[48:51],v[24:27],v[80:83]
s_add_u32 s89,s56,2949120
ds_read_b64_tr_b16 a[234:235],v3 offset:13920
v_lshlrev_b32_sdwa v42,1,v42 dst_sel:DWORD dst_unused:UNUSED_PAD src0_sel:DWORD src1_sel:WORD_1
v_mfma_f32_16x16x32_bf16 v[152:155],a[56:59],v[24:27],v[152:155]
buffer_load_dwordx4 v6,s[36:39],s89 offen lds
ds_read_u16_d16_hi v42,v42
v_lshlrev_b32_sdwa v41,1,v41 dst_sel:DWORD dst_unused:UNUSED_PAD src0_sel:DWORD src1_sel:WORD_1
v_accvgpr_write_b32 a9,v169
v_mfma_f32_16x16x32_bf16 v[156:159],a[56:59],v[28:31],v[156:159]
s_add_u32 m0,s58,0
ds_read_u16 v41,v41
ds_read_b64_tr_b16 a[236:237],v3 offset:12392
v_lshlrev_b32_sdwa v43,1,v43 dst_sel:DWORD dst_unused:UNUSED_PAD src0_sel:DWORD src1_sel:WORD_1
v_mfma_f32_16x16x32_bf16 v[100:103],a[64:67],v[28:31],v[100:103]
s_add_u32 s69,s69,s53
ds_read_u16_d16_hi v43,v43
ds_read_b64_tr_b16 a[238:239],v3 offset:13928
v_accvgpr_write_b32 a10,v170
v_mfma_f32_16x16x32_bf16 v[96:99],a[64:67],v[24:27],v[96:99]
buffer_load_dwordx4 v5,s[44:47],s69 offen offset:2048 lds
ds_read_b64_tr_b16 a[240:241],v3 offset:12448
v_accvgpr_write_b32 a11,v171
s_nop 1
v_mfma_f32_16x16x32_bf16 v[28:31],a[212:215],a[8:11],v[16:19]
ds_read_b64 v[14:15],v7 offset:12320
ds_read_b64_tr_b16 a[52:53],v1 offset:12320
v_lshlrev_b32_sdwa v32,1,v32 dst_sel:DWORD dst_unused:UNUSED_PAD src0_sel:DWORD src1_sel:WORD_1
v_mfma_f32_16x16x32_bf16 v[28:31],a[208:211],v[184:187],v[28:31]
s_waitcnt lgkmcnt(12)
ds_read_b64_tr_b16 a[54:55],v1 offset:13344
ds_read_b64_tr_b16 a[60:61],v1 offset:14368
v_lshl_add_u64 v[48:49],v[48:49],0,v[50:51]
v_fma_mixlo_f16 v8,v53,s99,0
v_mfma_f32_16x16x32_bf16 v[28:31],a[204:207],v[200:203],v[28:31]
s_add_u32 m0,s58,6160
ds_read_b64_tr_b16 a[62:63],v1 offset:15392
ds_read_u16 v32,v32
v_lshl_add_u64 v[50:51],v[44:45],0,v[46:47]
v_lshlrev_b32_sdwa v34,1,v34 dst_sel:DWORD dst_unused:UNUSED_PAD src0_sel:DWORD src1_sel:WORD_1
v_mfma_f32_16x16x32_bf16 v[24:27],a[200:203],v[200:203],v[20:23]
s_add_u32 s69,s69,s59
ds_read_u16_d16_hi v34,v34
ds_read_b64_tr_b16 a[68:69],v1 offset:16416
v_fma_mixhi_f16 v8,v55,s99,0
v_mfma_f32_16x16x32_bf16 v[24:27],a[196:199],v[184:187],v[24:27]
buffer_load_dwordx4 v5,s[44:47],s69 offen offset:0 lds
ds_read_b64_tr_b16 a[70:71],v1 offset:17440
v_lshl_add_u64 v[44:45],v[36:37],0,v[38:39]
v_lshlrev_b32_sdwa v33,1,v33 dst_sel:DWORD dst_unused:UNUSED_PAD src0_sel:DWORD src1_sel:WORD_1
v_pk_fma_f16 v9,v8,v8,s54
v_mfma_f32_16x16x32_bf16 v[24:27],a[192:195],a[8:11],v[24:27]
s_waitcnt lgkmcnt(8)
ds_read_u16 v33,v33
ds_read_b64_tr_b16 a[84:85],v2 offset:16416
v_or3_b32 v252,v14,v15,v252
v_lshlrev_b32_sdwa v35,1,v35 dst_sel:DWORD dst_unused:UNUSED_PAD src0_sel:DWORD src1_sel:WORD_1
v_accvgpr_write_b32 a12,v172
v_mfma_f32_16x16x32_bf16 v[36:39],a[192:195],v[172:175],v[20:23]
s_add_u32 s69,s69,s53
ds_read_b64 v[14:15],v7 offset:12832
ds_read_b128 v[20:23],v4 offset:384
v_pk_mul_f16 v9,v8,v9
v_mfma_f32_16x16x32_bf16 v[36:39],a[196:199],v[188:191],v[36:39]
buffer_load_dwordx4 v5,s[44:47],s69 offen offset:1024 lds
ds_read_u16_d16_hi v35,v35
v_lshl_add_u64 v[46:47],v[40:41],0,v[42:43]
v_lshlrev_b32_sdwa v60,1,v60 dst_sel:DWORD dst_unused:UNUSED_PAD src0_sel:DWORD src1_sel:WORD_1
v_mfma_f32_16x16x32_bf16 v[36:39],a[200:203],v[204:207],v[36:39]
s_add_u32 m0,s57,21224
ds_read_u16 v60,v60
v_exp_f16_e64 v8,v9
v_mfma_f32_16x16x32_bf16 v[40:43],a[204:207],v[204:207],v[16:19]
s_add_u32 s89,s56,3047424
ds_read_b128 v[16:19],v4 offset:416
ds_read_b64_tr_b16 a[86:87],v2 offset:17440
v_lshlrev_b32_sdwa v62,1,v62 dst_sel:DWORD dst_unused:UNUSED_PAD src0_sel:DWORD src1_sel:WORD_1
v_mfma_f32_16x16x32_bf16 v[40:43],a[208:211],v[188:191],v[40:43]
buffer_load_dwordx4 v6,s[36:39],s89 offen lds
ds_read_u16_d16_hi v62,v62
v_lshlrev_b32_sdwa v61,1,v61 dst_sel:DWORD dst_unused:UNUSED_PAD src0_sel:DWORD src1_sel:WORD_1
v_mfma_f32_16x16x32_bf16 v[40:43],a[212:215],v[172:175],v[40:43]
s_waitcnt lgkmcnt(6)
ds_read_u16 v61,v61
ds_read_b64_tr_b16 a[80:81],v2 offset:14368
v_or3_b32 v252,v14,v15,v252
v_accvgpr_write_b32 a13,v173
v_mfma_f32_16x16x32_bf16 v[112:115],a[248:251],v[48:51],v[112:115]
s_add_u32 m0,s58,6160
ds_read_b64 v[14:15],v7 offset:13344
ds_read_b64_tr_b16 a[82:83],v2 offset:15392
v_exp_f16_sdwa v8,v9 dst_sel:WORD_1 dst_unused:UNUSED_PRESERVE src0_sel:WORD_1
v_lshlrev_b32_sdwa v63,1,v63 dst_sel:DWORD dst_unused:UNUSED_PAD src0_sel:DWORD src1_sel:WORD_1
v_mfma_f32_16x16x32_bf16 v[116:119],a[248:251],v[44:47],v[116:119]
s_add_u32 s69,s69,s53
ds_read_u16_d16_hi v63,v63
ds_read_b64_tr_b16 a[76:77],v2 offset:12320
v_lshlrev_b32_sdwa v56,1,v56 dst_sel:DWORD dst_unused:UNUSED_PAD src0_sel:DWORD src1_sel:WORD_1
v_mfma_f32_16x16x32_bf16 v[76:79],a[252:255],v[44:47],v[76:79]
buffer_load_dwordx4 v5,s[44:47],s69 offen offset:2048 lds
ds_read_u16 v56,v56
v_pk_fma_f16 v8,v8,s55,s55
v_mfma_f32_16x16x32_bf16 v[72:75],a[252:255],v[48:51],v[72:75]
ds_read_b64_tr_b16 a[78:79],v2 offset:13344
v_rcp_f16_e64 v9,v8
v_lshlrev_b32_sdwa v58,1,v58 dst_sel:DWORD dst_unused:UNUSED_PAD src0_sel:DWORD src1_sel:WORD_1
v_mfma_f32_16x16x32_bf16 v[120:123],a[92:95],v[48:51],v[120:123]
ds_read_u16_d16_hi v58,v58
v_lshlrev_b32_sdwa v57,1,v57 dst_sel:DWORD dst_unused:UNUSED_PAD src0_sel:DWORD src1_sel:WORD_1
v_mfma_f32_16x16x32_bf16 v[124:127],a[92:95],v[44:47],v[124:127]
s_waitcnt lgkmcnt(6)
ds_read_u16 v57,v57
TE02_%=:
s_cbranch_execz TP3811_%=
v_or3_b32 v252,v14,v15,v252
v_accvgpr_write_b32 a14,v174
v_mfma_f32_16x16x32_bf16 v[92:95],a[48:51],v[44:47],v[92:95]
s_add_u32 m0,s57,24752
ds_read_b64_tr_b16 a[242:243],v3 offset:13984
v_lshlrev_b32_sdwa v59,1,v59 dst_sel:DWORD dst_unused:UNUSED_PAD src0_sel:DWORD src1_sel:WORD_1
v_mfma_f32_16x16x32_bf16 v[88:91],a[48:51],v[48:51],v[88:91]
s_add_u32 s89,s56,3145728
v_rcp_f16_sdwa v9,v8 dst_sel:WORD_1 dst_unused:UNUSED_PRESERVE src0_sel:WORD_1
v_mfma_f32_16x16x32_bf16 v[128:131],a[56:59],v[48:51],v[128:131]
s_nop 0
buffer_load_dwordx4 v6,s[36:39],s89 offen lds
ds_read_u16_d16_hi v59,v59
v_lshlrev_b32_sdwa v52,1,v52 dst_sel:DWORD dst_unused:UNUSED_PAD src0_sel:DWORD src1_sel:WORD_1
v_mfma_f32_16x16x32_bf16 v[132:135],a[56:59],v[44:47],v[132:135]
ds_read_u16 v52,v52
v_lshlrev_b32_sdwa v54,1,v54 dst_sel:DWORD dst_unused:UNUSED_PAD src0_sel:DWORD src1_sel:WORD_1
v_mfma_f32_16x16x32_bf16 v[108:111],a[64:67],v[44:47],v[108:111]
ds_read_u16_d16_hi v54,v54
v_fma_mix_f32 v53,v53,v9,0 op_sel_hi:[0,1,0]
v_mfma_f32_16x16x32_bf16 v[104:107],a[64:67],v[48:51],v[104:107]
ds_read_b64_tr_b16 a[244:245],v3 offset:12456
v_accvgpr_write_b32 a15,v175
v_fma_mix_f32 v55,v55,v9,0 op_sel:[0,1,0] op_sel_hi:[0,1,0]
v_mfma_f32_16x16x32_bf16 v[44:47],a[52:55],a[0:3],v[20:23]
ds_read_b64_tr_b16 a[246:247],v3 offset:13992
v_lshlrev_b32_sdwa v28,1,v28 dst_sel:DWORD dst_unused:UNUSED_PAD src0_sel:DWORD src1_sel:WORD_1
v_mfma_f32_16x16x32_bf16 v[44:47],a[60:63],v[176:179],v[44:47]
ds_read_u16 v28,v28
v_lshl_add_u64 v[32:33],v[32:33],0,v[34:35]
v_mfma_f32_32x32x16_bf16 v[160:175],a[88:91],a[88:91],0
v_lshlrev_b32_sdwa v30,1,v30 dst_sel:DWORD dst_unused:UNUSED_PAD src0_sel:DWORD src1_sel:WORD_1
v_accvgpr_write_b32 a16,v176
v_lshlrev_b32_sdwa v29,1,v29 dst_sel:DWORD dst_unused:UNUSED_PAD src0_sel:DWORD src1_sel:WORD_1
v_accvgpr_write_b32 a17,v177
v_lshlrev_b32_sdwa v31,1,v31 dst_sel:DWORD dst_unused:UNUSED_PAD src0_sel:DWORD src1_sel:WORD_1
v_mfma_f32_16x16x32_bf16 v[44:47],a[68:71],v[192:195],v[44:47]
s_waitcnt lgkmcnt(7)
ds_read_u16_d16_hi v30,v30
v_lshl_add_u64 v[34:35],v[60:61],0,v[62:63]
v_mfma_f32_16x16x32_bf16 v[48:51],a[84:87],v[192:195],v[16:19]
ds_read_u16 v29,v29
v_accvgpr_write_b32 a18,v178
v_mfma_f32_16x16x32_bf16 v[48:51],a[80:83],v[176:179],v[48:51]
s_waitcnt lgkmcnt(7)
ds_read_u16_d16_hi v31,v31
v_lshl_add_u64 v[60:61],v[56:57],0,v[58:59]
v_mfma_f32_16x16x32_bf16 v[48:51],a[76:79],a[0:3],v[48:51]
v_lshlrev_b32_sdwa v24,1,v24 dst_sel:DWORD dst_unused:UNUSED_PAD src0_sel:DWORD src1_sel:WORD_1
v_lshlrev_b32_sdwa v26,1,v26 dst_sel:DWORD dst_unused:UNUSED_PAD src0_sel:DWORD src1_sel:WORD_1
v_mfma_f32_16x16x32_bf16 v[56:59],a[76:79],a[4:7],v[16:19]
s_waitcnt lgkmcnt(6)
ds_read_u16 v24,v24
v_or_b32 v62,v52,v54
v_mfma_f32_16x16x32_bf16 v[56:59],a[80:83],v[180:183],v[56:59]
s_waitcnt vmcnt(13)
ds_read_u16_d16_hi v26,v26
v_cvt_pk_bf16_f32 v63,v53,v55
v_mfma_f32_16x16x32_bf16 v[56:59],a[84:87],v[196:199],v[56:59]
s_barrier
v_accvgpr_write_b32 a19,v179
v_lshlrev_b32_sdwa v25,1,v25 dst_sel:DWORD dst_unused:UNUSED_PAD src0_sel:DWORD src1_sel:WORD_1
v_mfma_f32_16x16x32_bf16 v[52:55],a[68:71],v[196:199],v[20:23]
s_add_u32 m0,s57,28288
ds_read_u16 v25,v25
v_accvgpr_write_b32 a20,v180
v_mfma_f32_16x16x32_bf16 v[52:55],a[60:63],v[180:183],v[52:55]
s_add_u32 s89,s56,3244032
ds_read_b64_tr_b16 a[92:93],v3 offset:18480
v_lshlrev_b32_sdwa v27,1,v27 dst_sel:DWORD dst_unused:UNUSED_PAD src0_sel:DWORD src1_sel:WORD_1
v_mfma_f32_16x16x32_bf16 v[52:55],a[52:55],a[4:7],v[52:55]
buffer_load_dwordx4 v6,s[36:39],s89 offen lds
v_accvgpr_write_b32 a21,v181
v_mfma_f32_16x16x32_bf16 v[136:139],a[224:227],v[32:35],v[136:139]
ds_read_u16_d16_hi v27,v27
v_lshlrev_b32_sdwa v40,1,v40 dst_sel:DWORD dst_unused:UNUSED_PAD src0_sel:DWORD src1_sel:WORD_1
v_mfma_f32_16x16x32_bf16 v[140:143],a[224:227],v[60:63],v[140:143]
ds_read_u16 v40,v40
v_lshlrev_b32_sdwa v42,1,v42 dst_sel:DWORD dst_unused:UNUSED_PAD src0_sel:DWORD src1_sel:WORD_1
v_accvgpr_write_b32 a22,v182
v_mfma_f32_16x16x32_bf16 v[68:71],a[228:231],v[60:63],v[68:71]
ds_read_u16_d16_hi v42,v42
v_lshlrev_b32_sdwa v41,1,v41 dst_sel:DWORD dst_unused:UNUSED_PAD src0_sel:DWORD src1_sel:WORD_1
v_mfma_f32_16x16x32_bf16 v[64:67],a[228:231],v[32:35],v[64:67]
ds_read_u16 v41,v41
v_accvgpr_write_b32 a23,v183
v_mfma_f32_16x16x32_bf16 v[144:147],a[232:235],v[32:35],v[144:147]
ds_read_b64_tr_b16 a[94:95],v3 offset:20016
ds_read_b64_tr_b16 a[48:49],v3 offset:18488
v_lshlrev_b32_sdwa v43,1,v43 dst_sel:DWORD dst_unused:UNUSED_PAD src0_sel:DWORD src1_sel:WORD_1
v_mfma_f32_16x16x32_bf16 v[148:151],a[232:235],v[60:63],v[148:151]
ds_read_u16_d16_hi v43,v43
v_accvgpr_write_b32 a24,v184
v_lshlrev_b32_sdwa v36,1,v36 dst_sel:DWORD dst_unused:UNUSED_PAD src0_sel:DWORD src1_sel:WORD_1
v_mfma_f32_16x16x32_bf16 v[84:87],a[236:239],v[60:63],v[84:87]
ds_read_u16 v36,v36
ds_read_b64_tr_b16 a[50:51],v3 offset:20024
v_accvgpr_write_b32 a25,v185
v_mfma_f32_16x16x32_bf16 v[80:83],a[236:239],v[32:35],v[80:83]
ds_read_b64_tr_b16 a[56:57],v3 offset:18544
v_lshlrev_b32_sdwa v38,1,v38 dst_sel:DWORD dst_unused:UNUSED_PAD src0_sel:DWORD src1_sel:WORD_1
v_mfma_f32_16x16x32_bf16 v[152:155],a[240:243],v[32:35],v[152:155]
ds_read_u16_d16_hi v38,v38
ds_read_b64_tr_b16 a[58:59],v3 offset:20080
v_lshlrev_b32_sdwa v37,1,v37 dst_sel:DWORD dst_unused:UNUSED_PAD src0_sel:DWORD src1_sel:WORD_1
v_accvgpr_write_b32 a26,v186
v_mfma_f32_16x16x32_bf16 v[156:159],a[240:243],v[60:63],v[156:159]
s_waitcnt lgkmcnt(11)
ds_read_u16 v37,v37
ds_read_b64_tr_b16 a[64:65],v3 offset:18552
v_lshlrev_b32_sdwa v39,1,v39 dst_sel:DWORD dst_unused:UNUSED_PAD src0_sel:DWORD src1_sel:WORD_1
v_mfma_f32_16x16x32_bf16 v[100:103],a[244:247],v[60:63],v[100:103]
s_nop 0
ds_read_u16_d16_hi v39,v39
ds_read_b64_tr_b16 a[66:67],v3 offset:20088
v_accvgpr_write_b32 a27,v187
v_mfma_f32_16x16x32_bf16 v[96:99],a[244:247],v[32:35],v[96:99]
s_add_u32 m0,s57,31832
ds_read_b64_tr_b16 a[248:249],v3 offset:18608
v_accvgpr_write_b32 a28,v188
v_mfma_f32_16x16x32_bf16 v[60:63],a[52:55],a[8:11],v[20:23]
s_add_u32 s89,s56,3342336
ds_read_b64 v[14:15],v7 offset:18480
ds_read_b64_tr_b16 a[72:73],v1 offset:18480
v_lshlrev_b32_sdwa v44,1,v44 dst_sel:DWORD dst_unused:UNUSED_PAD src0_sel:DWORD src1_sel:WORD_1
v_mfma_f32_16x16x32_bf16 v[60:63],a[60:63],a[24:27],v[60:63]
buffer_load_dwordx4 v6,s[36:39],s89 offen lds
ds_read_b64_tr_b16 a[74:75],v1 offset:19504
v_lshl_add_u64 v[28:29],v[28:29],0,v[30:31]
v_fma_mixlo_f16 v8,v57,s99,0
v_mfma_f32_16x16x32_bf16 v[60:63],a[68:71],v[200:203],v[60:63]
ds_read_b64_tr_b16 a[220:221],v1 offset:20528
ds_read_b64_tr_b16 a[222:223],v1 offset:21552
v_lshl_add_u64 v[30:31],v[24:25],0,v[26:27]
v_lshlrev_b32_sdwa v46,1,v46 dst_sel:DWORD dst_unused:UNUSED_PAD src0_sel:DWORD src1_sel:WORD_1
v_mfma_f32_16x16x32_bf16 v[32:35],a[84:87],v[200:203],v[16:19]
ds_read_u16 v44,v44
ds_read_u16_d16_hi v46,v46
v_fma_mixhi_f16 v8,v59,s99,0
v_mfma_f32_16x16x32_bf16 v[32:35],a[80:83],a[24:27],v[32:35]
s_waitcnt lgkmcnt(14)
ds_read_b64_tr_b16 a[216:217],v1 offset:22576
ds_read_b64_tr_b16 a[218:219],v1 offset:23600
v_lshl_add_u64 v[24:25],v[40:41],0,v[42:43]
v_lshlrev_b32_sdwa v45,1,v45 dst_sel:DWORD dst_unused:UNUSED_PAD src0_sel:DWORD src1_sel:WORD_1
v_pk_fma_f16 v9,v8,v8,s54
v_mfma_f32_16x16x32_bf16 v[32:35],a[76:79],a[8:11],v[32:35]
s_waitcnt lgkmcnt(8)
ds_read_u16 v45,v45
ds_read_b64_tr_b16 a[192:193],v2 offset:22576
v_or3_b32 v252,v14,v15,v252
v_lshlrev_b32_sdwa v47,1,v47 dst_sel:DWORD dst_unused:UNUSED_PAD src0_sel:DWORD src1_sel:WORD_1
v_accvgpr_write_b32 a29,v189
v_mfma_f32_16x16x32_bf16 v[40:43],a[76:79],a[12:15],v[16:19]
ds_read_b64 v[14:15],v7 offset:18992
ds_read_b128 v[16:19],v4 offset:512
v_pk_mul_f16 v9,v8,v9
v_mfma_f32_16x16x32_bf16 v[40:43],a[80:83],v[188:191],v[40:43]
ds_read_u16_d16_hi v47,v47
ds_read_b64_tr_b16 a[194:195],v2 offset:23600
v_lshl_add_u64 v[26:27],v[36:37],0,v[38:39]
v_lshlrev_b32_sdwa v48,1,v48 dst_sel:DWORD dst_unused:UNUSED_PAD src0_sel:DWORD src1_sel:WORD_1
v_mfma_f32_16x16x32_bf16 v[40:43],a[84:87],v[204:207],v[40:43]
ds_read_u16 v48,v48
v_exp_f16 v8,v9
v_mfma_f32_16x16x32_bf16 v[36:39],a[68:71],v[204:207],v[20:23]
ds_read_b128 v[20:23],v4 offset:544
ds_read_b64_tr_b16 a[196:197],v2 offset:20528
v_lshlrev_b32_sdwa v50,1,v50 dst_sel:DWORD dst_unused:UNUSED_PAD src0_sel:DWORD src1_sel:WORD_1
v_mfma_f32_16x16x32_bf16 v[36:39],a[60:63],v[188:191],v[36:39]
ds_read_u16_d16_hi v50,v50
ds_read_b64_tr_b16 a[198:199],v2 offset:21552
v_lshlrev_b32_sdwa v49,1,v49 dst_sel:DWORD dst_unused:UNUSED_PAD src0_sel:DWORD src1_sel:WORD_1
v_mfma_f32_16x16x32_bf16 v[36:39],a[52:55],a[12:15],v[36:39]
s_waitcnt lgkmcnt(8)
ds_read_u16 v49,v49
ds_read_b64_tr_b16 a[200:201],v2 offset:18480
v_or3_b32 v252,v14,v15,v252
v_accvgpr_write_b32 a30,v190
v_mfma_f32_16x16x32_bf16 v[112:115],a[224:227],v[28:31],v[112:115]
s_add_u32 m0,s57,35368
ds_read_b64 v[14:15],v7 offset:19504
ds_read_b64_tr_b16 a[202:203],v2 offset:19504
v_exp_f16_sdwa v8,v9 dst_sel:WORD_1 dst_unused:UNUSED_PRESERVE src0_sel:WORD_1
v_lshlrev_b32_sdwa v51,1,v51 dst_sel:DWORD dst_unused:UNUSED_PAD src0_sel:DWORD src1_sel:WORD_1
v_mfma_f32_16x16x32_bf16 v[116:119],a[224:227],v[24:27],v[116:119]
s_add_u32 s89,s56,3440640
ds_read_u16_d16_hi v51,v51
v_lshlrev_b32_sdwa v52,1,v52 dst_sel:DWORD dst_unused:UNUSED_PAD src0_sel:DWORD src1_sel:WORD_1
v_mfma_f32_16x16x32_bf16 v[76:79],a[228:231],v[24:27],v[76:79]
buffer_load_dwordx4 v6,s[36:39],s89 offen lds
ds_read_u16 v52,v52
v_pk_fma_f16 v8,v8,s55,s55
v_mfma_f32_16x16x32_bf16 v[72:75],a[228:231],v[28:31],v[72:75]
v_rcp_f16 v9,v8
v_lshlrev_b32_sdwa v54,1,v54 dst_sel:DWORD dst_unused:UNUSED_PAD src0_sel:DWORD src1_sel:WORD_1
v_mfma_f32_16x16x32_bf16 v[120:123],a[232:235],v[28:31],v[120:123]
ds_read_u16_d16_hi v54,v54
v_lshlrev_b32_sdwa v53,1,v53 dst_sel:DWORD dst_unused:UNUSED_PAD src0_sel:DWORD src1_sel:WORD_1
v_mfma_f32_16x16x32_bf16 v[124:127],a[232:235],v[24:27],v[124:127]
s_waitcnt lgkmcnt(4)
ds_read_u16 v53,v53
v_or3_b32 v252,v14,v15,v252
v_accvgpr_write_b32 a31,v191
v_mfma_f32_16x16x32_bf16 v[92:95],a[236:239],v[24:27],v[92:95]
ds_read_b64_tr_b16 a[250:251],v3 offset:20144
v_lshlrev_b32_sdwa v55,1,v55 dst_sel:DWORD dst_unused:UNUSED_PAD src0_sel:DWORD src1_sel:WORD_1
v_mfma_f32_16x16x32_bf16 v[88:91],a[236:239],v[28:31],v[88:91]
v_rcp_f16_sdwa v9,v8 dst_sel:WORD_1 dst_unused:UNUSED_PRESERVE src0_sel:WORD_1
v_mfma_f32_16x16x32_bf16 v[128:131],a[240:243],v[28:31],v[128:131]
ds_read_u16_d16_hi v55,v55
v_lshlrev_b32_sdwa v56,1,v56 dst_sel:DWORD dst_unused:UNUSED_PAD src0_sel:DWORD src1_sel:WORD_1
v_mfma_f32_16x16x32_bf16 v[132:135],a[240:243],v[24:27],v[132:135]
ds_read_u16 v56,v56
v_lshlrev_b32_sdwa v58,1,v58 dst_sel:DWORD dst_unused:UNUSED_PAD src0_sel:DWORD src1_sel:WORD_1
v_mfma_f32_16x16x32_bf16 v[108:111],a[244:247],v[24:27],v[108:111]
ds_read_u16_d16_hi v58,v58
v_fma_mix_f32 v57,v57,v9,0 op_sel_hi:[0,1,0]
v_mfma_f32_16x16x32_bf16 v[104:107],a[244:247],v[28:31],v[104:107]
ds_read_b64_tr_b16 a[252:253],v3 offset:18616
v_mfma_f32_32x32x16_bf16 v[176:191],a[88:91],a[88:91],0
v_fma_mix_f32 v59,v59,v9,0 op_sel:[0,1,0] op_sel_hi:[0,1,0]
v_mfma_f32_16x16x32_bf16 v[24:27],a[72:75],a[0:3],v[16:19]
s_add_u32 m0,s57,38896
ds_read_b64_tr_b16 a[254:255],v3 offset:20152
v_lshlrev_b32_sdwa v60,1,v60 dst_sel:DWORD dst_unused:UNUSED_PAD src0_sel:DWORD src1_sel:WORD_1
v_mfma_f32_16x16x32_bf16 v[24:27],a[220:223],a[16:19],v[24:27]
s_add_u32 s89,s56,3538944
ds_read_u16 v60,v60
v_lshl_add_u64 v[44:45],v[44:45],0,v[46:47]
v_accvgpr_write_b32 a32,v192
v_mfma_f32_16x16x32_bf16 v[24:27],a[216:219],v[192:195],v[24:27]
s_waitcnt lgkmcnt(7)
buffer_load_dwordx4 v6,s[36:39],s89 offen lds
v_lshl_add_u64 v[46:47],v[48:49],0,v[50:51]
v_lshlrev_b32_sdwa v62,1,v62 dst_sel:DWORD dst_unused:UNUSED_PAD src0_sel:DWORD src1_sel:WORD_1
v_mfma_f32_16x16x32_bf16 v[28:31],a[192:195],v[192:195],v[20:23]
ds_read_u16_d16_hi v62,v62
v_accvgpr_write_b32 a33,v193
v_lshlrev_b32_sdwa v61,1,v61 dst_sel:DWORD dst_unused:UNUSED_PAD src0_sel:DWORD src1_sel:WORD_1
v_mfma_f32_16x16x32_bf16 v[28:31],a[196:199],a[16:19],v[28:31]
s_waitcnt lgkmcnt(6)
ds_read_u16 v61,v61
v_lshl_add_u64 v[48:49],v[52:53],0,v[54:55]
v_mfma_f32_16x16x32_bf16 v[28:31],a[200:203],a[0:3],v[28:31]
v_lshlrev_b32_sdwa v63,1,v63 dst_sel:DWORD dst_unused:UNUSED_PAD src0_sel:DWORD src1_sel:WORD_1
v_accvgpr_write_b32 a34,v194
v_mfma_f32_16x16x32_bf16 v[52:55],a[200:203],a[4:7],v[20:23]
s_waitcnt lgkmcnt(5)
ds_read_u16_d16_hi v63,v63
v_or_b32 v50,v56,v58
v_lshlrev_b32_sdwa v32,1,v32 dst_sel:DWORD dst_unused:UNUSED_PAD src0_sel:DWORD src1_sel:WORD_1
v_accvgpr_write_b32 a35,v195
v_mfma_f32_16x16x32_bf16 v[52:55],a[196:199],a[20:23],v[52:55]
s_waitcnt vmcnt(9)
ds_read_u16 v32,v32
v_cvt_pk_bf16_f32 v51,v57,v59
v_lshlrev_b32_sdwa v34,1,v34 dst_sel:DWORD dst_unused:UNUSED_PAD src0_sel:DWORD src1_sel:WORD_1
v_mfma_f32_16x16x32_bf16 v[52:55],a[192:195],v[196:199],v[52:55]
s_barrier
ds_read_u16_d16_hi v34,v34
v_accvgpr_write_b32 a36,v196
v_mfma_f32_16x16x32_bf16 v[56:59],a[216:219],v[196:199],v[16:19]
ds_read_b64_tr_b16 a[224:225],v3 offset:0
v_lshlrev_b32_sdwa v33,1,v33 dst_sel:DWORD dst_unused:UNUSED_PAD src0_sel:DWORD src1_sel:WORD_1
v_mfma_f32_16x16x32_bf16 v[56:59],a[220:223],a[20:23],v[56:59]
s_add_u32 m0,s58,12320
ds_read_u16 v33,v33
v_accvgpr_write_b32 a37,v197
v_lshlrev_b32_sdwa v35,1,v35 dst_sel:DWORD dst_unused:UNUSED_PAD src0_sel:DWORD src1_sel:WORD_1
v_mfma_f32_16x16x32_bf16 v[56:59],a[72:75],a[4:7],v[56:59]
s_add_u32 s69,s69,s59
ds_read_u16_d16_hi v35,v35
v_accvgpr_write_b32 a38,v198
v_mfma_f32_16x16x32_bf16 v[136:139],a[92:95],v[44:47],v[136:139]
buffer_load_dwordx4 v5,s[44:47],s69 offen offset:0 lds
v_lshlrev_b32_sdwa v36,1,v36 dst_sel:DWORD dst_unused:UNUSED_PAD src0_sel:DWORD src1_sel:WORD_1
v_mfma_f32_16x16x32_bf16 v[140:143],a[92:95],v[48:51],v[140:143]
ds_read_u16 v36,v36
v_lshlrev_b32_sdwa v38,1,v38 dst_sel:DWORD dst_unused:UNUSED_PAD src0_sel:DWORD src1_sel:WORD_1
v_accvgpr_write_b32 a39,v199
v_mfma_f32_16x16x32_bf16 v[68:71],a[48:51],v[48:51],v[68:71]
ds_read_u16_d16_hi v38,v38
v_lshlrev_b32_sdwa v37,1,v37 dst_sel:DWORD dst_unused:UNUSED_PAD src0_sel:DWORD src1_sel:WORD_1
v_mfma_f32_16x16x32_bf16 v[64:67],a[48:51],v[44:47],v[64:67]
s_add_u32 m0,s57,42432
ds_read_u16 v37,v37
v_accvgpr_write_b32 a40,v200
v_mfma_f32_16x16x32_bf16 v[144:147],a[56:59],v[44:47],v[144:147]
s_add_u32 s89,s56,3637248
ds_read_b64_tr_b16 a[226:227],v3 offset:1536
ds_read_b64_tr_b16 a[228:229],v3 offset:8
v_lshlrev_b32_sdwa v39,1,v39 dst_sel:DWORD dst_unused:UNUSED_PAD src0_sel:DWORD src1_sel:WORD_1
v_mfma_f32_16x16x32_bf16 v[148:151],a[56:59],v[48:51],v[148:151]
buffer_load_dwordx4 v6,s[36:39],s89 offen lds
ds_read_u16_d16_hi v39,v39
v_accvgpr_write_b32 a41,v201
v_lshlrev_b32_sdwa v40,1,v40 dst_sel:DWORD dst_unused:UNUSED_PAD src0_sel:DWORD src1_sel:WORD_1
v_mfma_f32_16x16x32_bf16 v[84:87],a[64:67],v[48:51],v[84:87]
s_add_u32 m0,s58,12320
ds_read_u16 v40,v40
ds_read_b64_tr_b16 a[230:231],v3 offset:1544
v_accvgpr_write_b32 a42,v202
v_mfma_f32_16x16x32_bf16 v[80:83],a[64:67],v[44:47],v[80:83]
s_add_u32 s69,s69,s53
ds_read_b64_tr_b16 a[232:233],v3 offset:64
v_lshlrev_b32_sdwa v42,1,v42 dst_sel:DWORD dst_unused:UNUSED_PAD src0_sel:DWORD src1_sel:WORD_1
v_mfma_f32_16x16x32_bf16 v[152:155],a[248:251],v[44:47],v[152:155]
buffer_load_dwordx4 v5,s[44:47],s69 offen offset:1024 lds
ds_read_u16_d16_hi v42,v42
v_lshlrev_b32_sdwa v41,1,v41 dst_sel:DWORD dst_unused:UNUSED_PAD src0_sel:DWORD src1_sel:WORD_1
v_accvgpr_write_b32 a43,v203
v_mfma_f32_16x16x32_bf16 v[156:159],a[248:251],v[48:51],v[156:159]
s_waitcnt lgkmcnt(10)
ds_read_u16 v41,v41
ds_read_b64_tr_b16 a[234:235],v3 offset:1600
v_lshlrev_b32_sdwa v43,1,v43 dst_sel:DWORD dst_unused:UNUSED_PAD src0_sel:DWORD src1_sel:WORD_1
v_mfma_f32_16x16x32_bf16 v[100:103],a[252:255],v[48:51],v[100:103]
s_add_u32 s69,s69,s53
ds_read_u16_d16_hi v43,v43
ds_read_b64_tr_b16 a[236:237],v3 offset:72
v_accvgpr_write_b32 a44,v204
v_mfma_f32_16x16x32_bf16 v[96:99],a[252:255],v[44:47],v[96:99]
buffer_load_dwordx4 v5,s[44:47],s69 offen offset:2048 lds
ds_read_b64_tr_b16 a[238:239],v3 offset:1608
v_accvgpr_write_b32 a45,v205
v_mfma_f32_16x16x32_bf16 v[48:51],a[72:75],a[8:11],v[16:19]
ds_read_b64 v[14:15],v7 offset:0
ds_read_b64_tr_b16 a[204:205],v1 offset:0
v_lshlrev_b32_sdwa v24,1,v24 dst_sel:DWORD dst_unused:UNUSED_PAD src0_sel:DWORD src1_sel:WORD_1
v_mfma_f32_16x16x32_bf16 v[48:51],a[220:223],a[24:27],v[48:51]
ds_read_b64_tr_b16 a[206:207],v1 offset:1024
ds_read_b64_tr_b16 a[208:209],v1 offset:2048
v_lshl_add_u64 v[60:61],v[60:61],0,v[62:63]
v_fma_mixlo_f16 v8,v53,s99,0
v_mfma_f32_16x16x32_bf16 v[48:51],a[216:219],a[40:43],v[48:51]
ds_read_b64_tr_b16 a[210:211],v1 offset:3072
ds_read_u16 v24,v24
v_lshl_add_u64 v[62:63],v[32:33],0,v[34:35]
v_lshlrev_b32_sdwa v26,1,v26 dst_sel:DWORD dst_unused:UNUSED_PAD src0_sel:DWORD src1_sel:WORD_1
v_mfma_f32_16x16x32_bf16 v[44:47],a[192:195],a[40:43],v[20:23]
ds_read_u16_d16_hi v26,v26
ds_read_b64_tr_b16 a[212:213],v1 offset:4096
v_fma_mixhi_f16 v8,v55,s99,0
v_mfma_f32_16x16x32_bf16 v[44:47],a[196:199],a[24:27],v[44:47]
s_waitcnt lgkmcnt(14)
ds_read_b64_tr_b16 a[214:215],v1 offset:5120
ds_read_b64_tr_b16 a[240:241],v3 offset:128
v_lshl_add_u64 v[32:33],v[36:37],0,v[38:39]
v_lshlrev_b32_sdwa v25,1,v25 dst_sel:DWORD dst_unused:UNUSED_PAD src0_sel:DWORD src1_sel:WORD_1
v_pk_fma_f16 v9,v8,v8,s54
v_mfma_f32_16x16x32_bf16 v[44:47],a[200:203],a[8:11],v[44:47]
s_waitcnt lgkmcnt(9)
ds_read_u16 v25,v25
v_or3_b32 v252,v14,v15,v252
v_lshlrev_b32_sdwa v27,1,v27 dst_sel:DWORD dst_unused:UNUSED_PAD src0_sel:DWORD src1_sel:WORD_1
v_mfma_f32_16x16x32_bf16 v[36:39],a[200:203],a[12:15],v[20:23]
s_add_u32 m0,s57,45976
ds_read_b64 v[14:15],v7 offset:512
ds_read_b128 v[20:23],v4 offset:640
v_pk_mul_f16 v9,v8,v9
v_mfma_f32_16x16x32_bf16 v[36:39],a[196:199],a[28:31],v[36:39]
s_add_u32 s89,s56,3735552
ds_read_u16_d16_hi v27,v27
ds_read_b64_tr_b16 a[200:201],v2 offset:4096
v_lshl_add_u64 v[34:35],v[40:41],0,v[42:43]
v_lshlrev_b32_sdwa v28,1,v28 dst_sel:DWORD dst_unused:UNUSED_PAD src0_sel:DWORD src1_sel:WORD_1
v_accvgpr_write_b32 a46,v206
v_mfma_f32_16x16x32_bf16 v[36:39],a[192:195],v[204:207],v[36:39]
ds_read_u16 v28,v28
v_exp_f16 v8,v9
v_mfma_f32_16x16x32_bf16 v[40:43],a[216:219],v[204:207],v[16:19]
buffer_load_dwordx4 v6,s[36:39],s89 offen lds
ds_read_b128 v[16:19],v4 offset:672
v_lshlrev_b32_sdwa v30,1,v30 dst_sel:DWORD dst_unused:UNUSED_PAD src0_sel:DWORD src1_sel:WORD_1
v_mfma_f32_16x16x32_bf16 v[40:43],a[220:223],a[28:31],v[40:43]
ds_read_u16_d16_hi v30,v30
ds_read_b64_tr_b16 a[202:203],v2 offset:5120
v_lshlrev_b32_sdwa v29,1,v29 dst_sel:DWORD dst_unused:UNUSED_PAD src0_sel:DWORD src1_sel:WORD_1
v_mfma_f32_16x16x32_bf16 v[40:43],a[72:75],a[12:15],v[40:43]
s_waitcnt lgkmcnt(7)
ds_read_u16 v29,v29
ds_read_b64_tr_b16 a[196:197],v2 offset:2048
v_or3_b32 v252,v14,v15,v252
v_mfma_f32_16x16x32_bf16 v[112:115],a[92:95],v[60:63],v[112:115]
ds_read_b64 v[14:15],v7 offset:1024
ds_read_b64_tr_b16 a[198:199],v2 offset:3072
v_exp_f16_sdwa v8,v9 dst_sel:WORD_1 dst_unused:UNUSED_PRESERVE src0_sel:WORD_1
v_lshlrev_b32_sdwa v31,1,v31 dst_sel:DWORD dst_unused:UNUSED_PAD src0_sel:DWORD src1_sel:WORD_1
v_mfma_f32_16x16x32_bf16 v[116:119],a[92:95],v[32:35],v[116:119]
ds_read_u16_d16_hi v31,v31
ds_read_b64_tr_b16 a[192:193],v2 offset:0
v_lshlrev_b32_sdwa v56,1,v56 dst_sel:DWORD dst_unused:UNUSED_PAD src0_sel:DWORD src1_sel:WORD_1
v_mfma_f32_16x16x32_bf16 v[76:79],a[48:51],v[32:35],v[76:79]
ds_read_u16 v56,v56
ds_read_b64_tr_b16 a[194:195],v2 offset:1024
v_pk_fma_f16 v8,v8,s55,s55
v_mfma_f32_16x16x32_bf16 v[72:75],a[48:51],v[60:63],v[72:75]
v_rcp_f16_e64 v9,v8
v_lshlrev_b32_sdwa v58,1,v58 dst_sel:DWORD dst_unused:UNUSED_PAD src0_sel:DWORD src1_sel:WORD_1
v_accvgpr_write_b32 a47,v207
v_mfma_f32_16x16x32_bf16 v[120:123],a[56:59],v[60:63],v[120:123]
s_add_u32 m0,s57,49512
ds_read_u16_d16_hi v58,v58
v_lshlrev_b32_sdwa v57,1,v57 dst_sel:DWORD dst_unused:UNUSED_PAD src0_sel:DWORD src1_sel:WORD_1
v_mfma_f32_16x16x32_bf16 v[124:127],a[56:59],v[32:35],v[124:127]
s_waitcnt lgkmcnt(6)
ds_read_u16 v57,v57
v_or3_b32 v252,v14,v15,v252
v_mfma_f32_16x16x32_bf16 v[92:95],a[64:67],v[32:35],v[92:95]
s_add_u32 s89,s56,3833856
ds_read_b64_tr_b16 a[242:243],v3 offset:1664
v_and_b32 v252,0x40004000,v252
v_lshlrev_b32_sdwa v59,1,v59 dst_sel:DWORD dst_unused:UNUSED_PAD src0_sel:DWORD src1_sel:WORD_1
v_mfma_f32_16x16x32_bf16 v[88:91],a[64:67],v[60:63],v[88:91]
buffer_load_dwordx4 v6,s[36:39],s89 offen lds
v_cmp_ne_u32_e32 vcc,0,v252
s_nop 0
s_or_b64 s[90:91],s[90:91],vcc
v_mfma_f32_16x16x32_bf16 v[128:131],a[248:251],v[60:63],v[128:131]
ds_read_u16_d16_hi v59,v59
v_rcp_f16_sdwa v9,v8 dst_sel:WORD_1 dst_unused:UNUSED_PRESERVE src0_sel:WORD_1
v_lshlrev_b32_sdwa v52,1,v52 dst_sel:DWORD dst_unused:UNUSED_PAD src0_sel:DWORD src1_sel:WORD_1
v_mfma_f32_16x16x32_bf16 v[132:135],a[248:251],v[32:35],v[132:135]
ds_read_u16 v52,v52
v_lshlrev_b32_sdwa v54,1,v54 dst_sel:DWORD dst_unused:UNUSED_PAD src0_sel:DWORD src1_sel:WORD_1
v_mfma_f32_16x16x32_bf16 v[108:111],a[252:255],v[32:35],v[108:111]
ds_read_u16_d16_hi v54,v54
v_fma_mix_f32 v53,v53,v9,0 op_sel_hi:[0,1,0]
v_mfma_f32_16x16x32_bf16 v[104:107],a[252:255],v[60:63],v[104:107]
ds_read_b64_tr_b16 a[244:245],v3 offset:136
v_mfma_f32_32x32x16_bf16 v[192:207],a[88:91],a[88:91],0
v_fma_mix_f32 v55,v55,v9,0 op_sel:[0,1,0] op_sel_hi:[0,1,0]
v_mfma_f32_16x16x32_bf16 v[32:35],a[204:207],a[0:3],v[20:23]
ds_read_b64_tr_b16 a[246:247],v3 offset:1672
v_lshlrev_b32_sdwa v48,1,v48 dst_sel:DWORD dst_unused:UNUSED_PAD src0_sel:DWORD src1_sel:WORD_1
v_mfma_f32_16x16x32_bf16 v[32:35],a[208:211],a[16:19],v[32:35]
ds_read_u16 v48,v48
v_lshl_add_u64 v[24:25],v[24:25],0,v[26:27]
v_mfma_f32_16x16x32_bf16 v[32:35],a[212:215],a[32:35],v[32:35]
s_waitcnt lgkmcnt(7)
v_lshl_add_u64 v[26:27],v[28:29],0,v[30:31]
v_lshlrev_b32_sdwa v50,1,v50 dst_sel:DWORD dst_unused:UNUSED_PAD src0_sel:DWORD src1_sel:WORD_1
v_mfma_f32_16x16x32_bf16 v[60:63],a[200:203],a[32:35],v[16:19]
ds_read_u16_d16_hi v50,v50
v_lshlrev_b32_sdwa v49,1,v49 dst_sel:DWORD dst_unused:UNUSED_PAD src0_sel:DWORD src1_sel:WORD_1
v_mfma_f32_16x16x32_bf16 v[60:63],a[196:199],a[16:19],v[60:63]
s_waitcnt lgkmcnt(6)
ds_read_u16 v49,v49
v_lshl_add_u64 v[28:29],v[56:57],0,v[58:59]
v_mfma_f32_16x16x32_bf16 v[60:63],a[192:195],a[0:3],v[60:63]
v_lshlrev_b32_sdwa v51,1,v51 dst_sel:DWORD dst_unused:UNUSED_PAD src0_sel:DWORD src1_sel:WORD_1
v_lshlrev_b32_sdwa v44,1,v44 dst_sel:DWORD dst_unused:UNUSED_PAD src0_sel:DWORD src1_sel:WORD_1
v_mfma_f32_16x16x32_bf16 v[56:59],a[192:195],a[4:7],v[16:19]
s_waitcnt lgkmcnt(5)
ds_read_u16_d16_hi v51,v51
v_or_b32 v30,v52,v54
v_mfma_f32_16x16x32_bf16 v[56:59],a[196:199],a[20:23],v[56:59]
s_waitcnt vmcnt(11)
ds_read_u16 v44,v44
v_cvt_pk_bf16_f32 v31,v53,v55
v_lshlrev_b32_sdwa v46,1,v46 dst_sel:DWORD dst_unused:UNUSED_PAD src0_sel:DWORD src1_sel:WORD_1
v_mfma_f32_16x16x32_bf16 v[56:59],a[200:203],a[36:39],v[56:59]
s_barrier
ds_read_u16_d16_hi v46,v46
v_lshlrev_b32_sdwa v45,1,v45 dst_sel:DWORD dst_unused:UNUSED_PAD src0_sel:DWORD src1_sel:WORD_1
v_mfma_f32_16x16x32_bf16 v[52:55],a[212:215],a[36:39],v[20:23]
s_add_u32 m0,s57,53040
ds_read_u16 v45,v45
v_lshlrev_b32_sdwa v47,1,v47 dst_sel:DWORD dst_unused:UNUSED_PAD src0_sel:DWORD src1_sel:WORD_1
v_mfma_f32_16x16x32_bf16 v[52:55],a[208:211],a[20:23],v[52:55]
s_add_u32 s89,s56,3932160
ds_read_u16_d16_hi v47,v47
v_mfma_f32_32x32x16_bf16 v[240:255],a[88:91],a[88:91],0
v_lshlrev_b32_sdwa v40,1,v40 dst_sel:DWORD dst_unused:UNUSED_PAD src0_sel:DWORD src1_sel:WORD_1
v_lshlrev_b32_sdwa v42,1,v42 dst_sel:DWORD dst_unused:UNUSED_PAD src0_sel:DWORD src1_sel:WORD_1
v_lshlrev_b32_sdwa v41,1,v41 dst_sel:DWORD dst_unused:UNUSED_PAD src0_sel:DWORD src1_sel:WORD_1
v_lshlrev_b32_sdwa v43,1,v43 dst_sel:DWORD dst_unused:UNUSED_PAD src0_sel:DWORD src1_sel:WORD_1
v_lshlrev_b32_sdwa v36,1,v36 dst_sel:DWORD dst_unused:UNUSED_PAD src0_sel:DWORD src1_sel:WORD_1
v_mfma_f32_16x16x32_bf16 v[52:55],a[204:207],a[4:7],v[52:55]
buffer_load_dwordx4 v6,s[36:39],s89 offen lds
ds_read_u16 v40,v40
v_mfma_f32_16x16x32_bf16 v[136:139],a[224:227],v[24:27],v[136:139]
s_add_u32 m0,s58,18480
ds_read_u16_d16_hi v42,v42
ds_read_u16 v41,v41
v_mfma_f32_16x16x32_bf16 v[140:143],a[224:227],v[28:31],v[140:143]
s_add_u32 s69,s69,s59
ds_read_u16_d16_hi v43,v43
v_lshlrev_b32_sdwa v38,1,v38 dst_sel:DWORD dst_unused:UNUSED_PAD src0_sel:DWORD src1_sel:WORD_1
v_mfma_f32_16x16x32_bf16 v[68:71],a[228:231],v[28:31],v[68:71]
buffer_load_dwordx4 v5,s[44:47],s69 offen offset:0 lds
ds_read_u16 v36,v36
v_mfma_f32_16x16x32_bf16 v[64:67],a[228:231],v[24:27],v[64:67]
s_add_u32 s69,s69,s53
ds_read_u16_d16_hi v38,v38
ds_read_b64_tr_b16 a[248:249],v3 offset:6160
v_mfma_f32_16x16x32_bf16 v[144:147],a[232:235],v[24:27],v[144:147]
buffer_load_dwordx4 v5,s[44:47],s69 offen offset:1024 lds
v_lshlrev_b32_sdwa v37,1,v37 dst_sel:DWORD dst_unused:UNUSED_PAD src0_sel:DWORD src1_sel:WORD_1
v_mfma_f32_16x16x32_bf16 v[148:151],a[232:235],v[28:31],v[148:151]
ds_read_u16 v37,v37
v_lshlrev_b32_sdwa v39,1,v39 dst_sel:DWORD dst_unused:UNUSED_PAD src0_sel:DWORD src1_sel:WORD_1
v_mfma_f32_16x16x32_bf16 v[84:87],a[236:239],v[28:31],v[84:87]
ds_read_u16_d16_hi v39,v39
v_mfma_f32_16x16x32_bf16 v[80:83],a[236:239],v[24:27],v[80:83]
ds_read_b64_tr_b16 a[250:251],v3 offset:7696
v_mfma_f32_16x16x32_bf16 v[152:155],a[240:243],v[24:27],v[152:155]
s_add_u32 s69,s69,s53
ds_read_b64_tr_b16 a[252:253],v3 offset:6168
v_mfma_f32_16x16x32_bf16 v[156:159],a[240:243],v[28:31],v[156:159]
s_waitcnt lgkmcnt(8)
buffer_load_dwordx4 v5,s[44:47],s69 offen offset:2048 lds
v_mfma_f32_16x16x32_bf16 v[100:103],a[244:247],v[28:31],v[100:103]
ds_read_b64_tr_b16 a[254:255],v3 offset:7704
v_mfma_f32_16x16x32_bf16 v[96:99],a[244:247],v[24:27],v[96:99]
s_waitcnt vmcnt(13)
s_barrier
v_add_u32_sdwa v24,s60,v10 dst_sel:DWORD dst_unused:UNUSED_PAD src0_sel:DWORD src1_sel:WORD_1
v_subrev_u32 v8,88400,v24
v_min_u32_e64 v24,v24,v8
v_add_u32 v25,7072,v24
v_subrev_u32 v8,88400,v25
v_min_u32_e64 v25,v25,v8
v_lshl_add_u64 v[48:49],v[48:49],0,v[50:51]
v_lshl_add_u64 v[50:51],v[44:45],0,v[46:47]
s_waitcnt lgkmcnt(8)
v_lshl_add_u64 v[44:45],v[40:41],0,v[42:43]
s_waitcnt lgkmcnt(3)
v_lshl_add_u64 v[46:47],v[36:37],0,v[38:39]
v_mfma_f32_16x16x32_bf16 v[112:115],a[224:227],v[48:51],v[112:115]
s_add_u32 m0,s58,0
s_add_u32 s69,s69,s59
buffer_load_dwordx4 v5,s[44:47],s69 offen offset:0 lds
v_xad_u32 v28,v11,0,v24
v_xad_u32 v29,v11,8,v25
v_mfma_f32_16x16x32_bf16 v[116:119],a[224:227],v[44:47],v[116:119]
TE03_%=:
s_cbranch_execz TP4344_%=
v_xad_u32 v30,v11,16,v24
v_xad_u32 v31,v11,24,v25
v_mfma_f32_16x16x32_bf16 v[76:79],a[228:231],v[44:47],v[76:79]
ds_read_b64 a[48:49],v28 offset:0
ds_read_b64 a[50:51],v28 offset:192
v_mfma_f32_16x16x32_bf16 v[72:75],a[228:231],v[48:51],v[72:75]
ds_read_b64 a[52:53],v29 offset:0
ds_read_b64 a[54:55],v29 offset:192
v_mfma_f32_16x16x32_bf16 v[120:123],a[232:235],v[48:51],v[120:123]
ds_read_b64 a[56:57],v28 offset:8
ds_read_b64 a[58:59],v28 offset:200
v_mfma_f32_16x16x32_bf16 v[124:127],a[232:235],v[44:47],v[124:127]
s_add_u32 s69,s69,s53
buffer_load_dwordx4 v5,s[44:47],s69 offen offset:1024 lds
ds_read_b64 a[60:61],v29 offset:8
ds_read_b64 a[62:63],v29 offset:200
v_mfma_f32_16x16x32_bf16 v[92:95],a[236:239],v[44:47],v[92:95]
v_mfma_f32_16x16x32_bf16 v[88:91],a[236:239],v[48:51],v[88:91]
v_mfma_f32_16x16x32_bf16 v[128:131],a[240:243],v[48:51],v[128:131]
v_mfma_f32_16x16x32_bf16 v[132:135],a[240:243],v[44:47],v[132:135]
v_mfma_f32_16x16x32_bf16 v[108:111],a[244:247],v[44:47],v[108:111]
v_mfma_f32_16x16x32_bf16 v[104:107],a[244:247],v[48:51],v[104:107]
s_nop 0
ds_read_b64 a[64:65],v30 offset:0
ds_read_b64 a[66:67],v30 offset:192
ds_read_b64 a[68:69],v31 offset:0
ds_read_b64 a[70:71],v31 offset:192
s_waitcnt lgkmcnt(8)
v_smfmac_f32_16x16x64_bf16 v[160:163],a[96:99],a[48:55],v10
s_waitcnt lgkmcnt(4)
s_add_u32 s69,s69,s53
buffer_load_dwordx4 v5,s[44:47],s69 offen offset:2048 lds
ds_read_b64 a[48:49],v30 offset:8
ds_read_b64 a[50:51],v30 offset:200
ds_read_b64 a[52:53],v31 offset:8
ds_read_b64 a[54:55],v31 offset:200
v_smfmac_f32_16x16x64_bf16 v[164:167],a[100:103],a[56:63],v10
s_waitcnt lgkmcnt(4)
ds_read_b64 a[56:57],v28 offset:64
ds_read_b64 a[58:59],v28 offset:256
ds_read_b64 a[60:61],v29 offset:64
ds_read_b64 a[62:63],v29 offset:256
v_smfmac_f32_16x16x64_bf16 v[168:171],a[104:107],a[64:71],v10
s_waitcnt lgkmcnt(4)
ds_read_b64 a[64:65],v28 offset:72
ds_read_b64 a[66:67],v28 offset:264
ds_read_b64 a[68:69],v29 offset:72
ds_read_b64 a[70:71],v29 offset:264
v_and_b32 v48,8,v11
v_pk_mul_f32 v[44:45],v[160:161],v[160:161]
v_pk_mul_f32 v[46:47],v[162:163],v[162:163]
v_smfmac_f32_16x16x64_bf16 v[172:175],a[108:111],a[48:55],v10
s_waitcnt lgkmcnt(4)
ds_read_b64 a[48:49],v30 offset:64
ds_read_b64 a[50:51],v30 offset:256
ds_read_b64 a[52:53],v31 offset:64
ds_read_b64 a[54:55],v31 offset:256
v_mul_u32_u24 v48,884,v48
s_add_u32 s64,s60,3536
v_pk_add_f32 v[36:37],v[160:161],v[164:165]
v_pk_fma_f32 v[44:45],v[164:165],v[164:165],v[44:45]
v_pk_add_f32 v[38:39],v[162:163],v[166:167]
v_pk_fma_f32 v[46:47],v[166:167],v[166:167],v[46:47]
v_smfmac_f32_16x16x64_bf16 v[176:179],a[128:131],a[56:63],v10
s_waitcnt lgkmcnt(4)
ds_read_b64 a[56:57],v30 offset:72
ds_read_b64 a[58:59],v30 offset:264
ds_read_b64 a[60:61],v31 offset:72
ds_read_b64 a[62:63],v31 offset:264
v_add_u32 v51,s64,v48
v_pk_add_f32 v[36:37],v[36:37],v[168:169]
v_pk_fma_f32 v[44:45],v[168:169],v[168:169],v[44:45]
v_pk_add_f32 v[38:39],v[38:39],v[170:171]
v_pk_fma_f32 v[46:47],v[170:171],v[170:171],v[46:47]
v_smfmac_f32_16x16x64_bf16 v[180:183],a[132:135],a[64:71],v10
s_waitcnt lgkmcnt(4)
ds_read_b64 a[64:65],v28 offset:128
ds_read_b64 a[66:67],v28 offset:320
ds_read_b64 a[68:69],v29 offset:128
ds_read_b64 a[70:71],v29 offset:320
v_subrev_u32 v8,88400,v51
v_pk_add_f32 v[36:37],v[36:37],v[172:173]
v_pk_fma_f32 v[44:45],v[172:173],v[172:173],v[44:45]
v_pk_add_f32 v[38:39],v[38:39],v[174:175]
v_pk_fma_f32 v[46:47],v[174:175],v[174:175],v[46:47]
v_smfmac_f32_16x16x64_bf16 v[184:187],a[136:139],a[48:55],v10
s_waitcnt lgkmcnt(4)
ds_read_b64 a[48:49],v28 offset:136
ds_read_b64 a[50:51],v28 offset:328
ds_read_b64 a[52:53],v29 offset:136
ds_read_b64 a[54:55],v29 offset:328
v_min_u32 v51,v51,v8
v_pk_add_f32 v[36:37],v[36:37],v[176:177]
v_pk_fma_f32 v[44:45],v[176:177],v[176:177],v[44:45]
v_pk_add_f32 v[38:39],v[38:39],v[178:179]
v_pk_fma_f32 v[46:47],v[178:179],v[178:179],v[46:47]
v_smfmac_f32_16x16x64_bf16 v[188:191],a[140:143],a[56:63],v10
s_waitcnt lgkmcnt(4)
ds_read_b64 a[56:57],v30 offset:128
ds_read_b64 a[58:59],v30 offset:320
ds_read_b64 a[60:61],v31 offset:128
ds_read_b64 a[62:63],v31 offset:320
v_xad_u32 v28,v11,32,v24
v_xad_u32 v29,v11,40,v25
v_pk_add_f32 v[36:37],v[36:37],v[180:181]
v_pk_fma_f32 v[44:45],v[180:181],v[180:181],v[44:45]
v_pk_add_f32 v[38:39],v[38:39],v[182:183]
v_pk_fma_f32 v[46:47],v[182:183],v[182:183],v[46:47]
v_smfmac_f32_16x16x64_bf16 v[192:195],a[160:163],a[64:71],v10
s_waitcnt lgkmcnt(4)
ds_read_b64 a[64:65],v30 offset:136
ds_read_b64 a[66:67],v30 offset:328
ds_read_b64 a[68:69],v31 offset:136
ds_read_b64 a[70:71],v31 offset:328
v_add_u32 v50,3536,v51
v_pk_add_f32 v[36:37],v[36:37],v[184:185]
v_pk_fma_f32 v[44:45],v[184:185],v[184:185],v[44:45]
v_pk_add_f32 v[38:39],v[38:39],v[186:187]
v_pk_fma_f32 v[46:47],v[186:187],v[186:187],v[46:47]
v_smfmac_f32_16x16x64_bf16 v[196:199],a[164:167],a[48:55],v10
s_waitcnt lgkmcnt(4)
ds_read_b64 a[48:49],v28 offset:0
ds_read_b64 a[50:51],v28 offset:192
ds_read_b64 a[52:53],v29 offset:0
ds_read_b64 a[54:55],v29 offset:192
v_xad_u32 v30,v11,48,v24
v_xad_u32 v31,v11,56,v25
v_pk_add_f32 v[36:37],v[36:37],v[188:189]
v_pk_fma_f32 v[44:45],v[188:189],v[188:189],v[44:45]
v_pk_add_f32 v[38:39],v[38:39],v[190:191]
v_pk_fma_f32 v[46:47],v[190:191],v[190:191],v[46:47]
v_smfmac_f32_16x16x64_bf16 v[200:203],a[168:171],a[56:63],v10
s_waitcnt lgkmcnt(4)
ds_read_b64 a[56:57],v28 offset:8
ds_read_b64 a[58:59],v28 offset:200
ds_read_b64 a[60:61],v29 offset:8
ds_read_b64 a[62:63],v29 offset:200
v_subrev_u32 v8,88400,v50
v_pk_add_f32 v[36:37],v[36:37],v[192:193]
v_pk_fma_f32 v[44:45],v[192:193],v[192:193],v[44:45]
v_pk_add_f32 v[38:39],v[38:39],v[194:195]
v_pk_fma_f32 v[46:47],v[194:195],v[194:195],v[46:47]
v_smfmac_f32_16x16x64_bf16 v[204:207],a[172:175],a[64:71],v10
s_waitcnt lgkmcnt(4)
ds_read_b64 a[64:65],v28 offset:64
ds_read_b64 a[66:67],v28 offset:256
ds_read_b64 a[68:69],v29 offset:64
ds_read_b64 a[70:71],v29 offset:256
v_min_u32 v50,v50,v8
v_pk_add_f32 v[36:37],v[36:37],v[196:197]
v_pk_fma_f32 v[44:45],v[196:197],v[196:197],v[44:45]
v_pk_add_f32 v[38:39],v[38:39],v[198:199]
v_pk_fma_f32 v[46:47],v[198:199],v[198:199],v[46:47]
v_smfmac_f32_16x16x64_bf16 v[208:211],a[112:115],a[48:55],v10
s_waitcnt lgkmcnt(4)
ds_read_b64 a[48:49],v28 offset:72
ds_read_b64 a[50:51],v28 offset:264
ds_read_b64 a[52:53],v29 offset:72
ds_read_b64 a[54:55],v29 offset:264
v_xor_b32 v9,0x80008,v12
v_pk_add_f32 v[36:37],v[36:37],v[200:201]
v_pk_fma_f32 v[44:45],v[200:201],v[200:201],v[44:45]
v_pk_add_f32 v[38:39],v[38:39],v[202:203]
v_pk_fma_f32 v[46:47],v[202:203],v[202:203],v[46:47]
v_smfmac_f32_16x16x64_bf16 v[212:215],a[116:119],a[56:63],v10
s_waitcnt lgkmcnt(4)
ds_read_b64 a[56:57],v28 offset:128
ds_read_b64 a[58:59],v28 offset:320
ds_read_b64 a[60:61],v29 offset:128
ds_read_b64 a[62:63],v29 offset:320
v_add_u32_sdwa v48,v12,v51 dst_sel:DWORD dst_unused:UNUSED_PAD src0_sel:WORD_0 src1_sel:DWORD
v_pk_add_f32 v[36:37],v[36:37],v[204:205]
v_pk_fma_f32 v[44:45],v[204:205],v[204:205],v[44:45]
v_pk_add_f32 v[38:39],v[38:39],v[206:207]
v_pk_fma_f32 v[46:47],v[206:207],v[206:207],v[46:47]
v_smfmac_f32_16x16x64_bf16 v[216:219],a[144:147],a[64:71],v10
s_waitcnt lgkmcnt(4)
ds_read_b64 a[64:65],v28 offset:136
ds_read_b64 a[66:67],v28 offset:328
ds_read_b64 a[68:69],v29 offset:136
ds_read_b64 a[70:71],v29 offset:328
v_add_u32_sdwa v49,v12,v51 dst_sel:DWORD dst_unused:UNUSED_PAD src0_sel:WORD_1 src1_sel:DWORD
v_pk_add_f32 v[36:37],v[36:37],v[208:209]
v_pk_fma_f32 v[44:45],v[208:209],v[208:209],v[44:45]
v_pk_add_f32 v[38:39],v[38:39],v[210:211]
v_pk_fma_f32 v[46:47],v[210:211],v[210:211],v[46:47]
v_smfmac_f32_16x16x64_bf16 v[220:223],a[148:151],a[48:55],v10
s_waitcnt lgkmcnt(4)
ds_read_b64 a[48:49],v30 offset:0
ds_read_b64 a[50:51],v30 offset:192
ds_read_b64 a[52:53],v31 offset:0
ds_read_b64 a[54:55],v31 offset:192
v_add_u32_sdwa v51,v9,v50 dst_sel:DWORD dst_unused:UNUSED_PAD src0_sel:WORD_1 src1_sel:DWORD
v_pk_add_f32 v[36:37],v[36:37],v[212:213]
v_pk_fma_f32 v[44:45],v[212:213],v[212:213],v[44:45]
v_pk_add_f32 v[38:39],v[38:39],v[214:215]
v_pk_fma_f32 v[46:47],v[214:215],v[214:215],v[46:47]
v_smfmac_f32_16x16x64_bf16 v[224:227],a[176:179],a[56:63],v10
s_waitcnt lgkmcnt(4)
ds_read_b64 a[56:57],v30 offset:8
ds_read_b64 a[58:59],v30 offset:200
ds_read_b64 a[60:61],v31 offset:8
ds_read_b64 a[62:63],v31 offset:200
v_add_u32_sdwa v50,v9,v50 dst_sel:DWORD dst_unused:UNUSED_PAD src0_sel:WORD_0 src1_sel:DWORD
v_pk_add_f32 v[36:37],v[36:37],v[216:217]
v_pk_fma_f32 v[44:45],v[216:217],v[216:217],v[44:45]
v_pk_add_f32 v[38:39],v[38:39],v[218:219]
v_pk_fma_f32 v[46:47],v[218:219],v[218:219],v[46:47]
v_smfmac_f32_16x16x64_bf16 v[228:231],a[180:183],a[64:71],v10
s_waitcnt lgkmcnt(4)
ds_read_b64 a[64:65],v30 offset:64
ds_read_b64 a[66:67],v30 offset:256
ds_read_b64 a[68:69],v31 offset:64
ds_read_b64 a[70:71],v31 offset:256
v_pk_add_f32 v[36:37],v[36:37],v[220:221]
v_pk_fma_f32 v[44:45],v[220:221],v[220:221],v[44:45]
v_pk_add_f32 v[38:39],v[38:39],v[222:223]
v_pk_fma_f32 v[46:47],v[222:223],v[222:223],v[46:47]
v_smfmac_f32_16x16x64_bf16 v[232:235],a[120:123],a[48:55],v10
s_waitcnt lgkmcnt(4)
ds_read_b64 a[48:49],v30 offset:72
ds_read_b64 a[50:51],v30 offset:264
ds_read_b64 a[52:53],v31 offset:72
ds_read_b64 a[54:55],v31 offset:264
v_pk_add_f32 v[36:37],v[36:37],v[224:225]
v_pk_fma_f32 v[44:45],v[224:225],v[224:225],v[44:45]
v_pk_add_f32 v[38:39],v[38:39],v[226:227]
v_pk_fma_f32 v[46:47],v[226:227],v[226:227],v[46:47]
v_smfmac_f32_16x16x64_bf16 v[236:239],a[124:127],a[56:63],v10
s_waitcnt lgkmcnt(4)
ds_read_b64 a[56:57],v30 offset:128
ds_read_b64 a[58:59],v30 offset:320
ds_read_b64 a[60:61],v31 offset:128
ds_read_b64 a[62:63],v31 offset:320
v_pk_add_f32 v[36:37],v[36:37],v[228:229]
v_pk_fma_f32 v[44:45],v[228:229],v[228:229],v[44:45]
v_pk_add_f32 v[38:39],v[38:39],v[230:231]
v_pk_fma_f32 v[46:47],v[230:231],v[230:231],v[46:47]
v_smfmac_f32_16x16x64_bf16 v[240:243],a[152:155],a[64:71],v10
s_waitcnt lgkmcnt(4)
ds_read_b64 a[64:65],v30 offset:136
ds_read_b64 a[66:67],v30 offset:328
ds_read_b64 a[68:69],v31 offset:136
ds_read_b64 a[70:71],v31 offset:328
v_pk_add_f32 v[36:37],v[36:37],v[232:233]
v_pk_fma_f32 v[44:45],v[232:233],v[232:233],v[44:45]
v_pk_add_f32 v[38:39],v[38:39],v[234:235]
v_pk_fma_f32 v[46:47],v[234:235],v[234:235],v[46:47]
v_smfmac_f32_16x16x64_bf16 v[244:247],a[156:159],a[48:55],v10
s_waitcnt lgkmcnt(4)
ds_read_b64 a[224:225],v48 offset:57344
ds_read_b64 a[226:227],v48 offset:57352
ds_read_b64 a[228:229],v49 offset:57344
ds_read_b64 a[230:231],v49 offset:57352
ds_read_b128 a[240:243],v13 offset:0
v_pk_add_f32 v[36:37],v[36:37],v[236:237]
v_pk_fma_f32 v[44:45],v[236:237],v[236:237],v[44:45]
v_pk_add_f32 v[38:39],v[38:39],v[238:239]
v_pk_fma_f32 v[46:47],v[238:239],v[238:239],v[46:47]
v_smfmac_f32_16x16x64_bf16 v[248:251],a[184:187],a[56:63],v10
s_waitcnt lgkmcnt(5)
ds_read_b64 a[232:233],v50 offset:57344
ds_read_b64 a[234:235],v50 offset:57352
ds_read_b64 a[236:237],v51 offset:57344
ds_read_b64 a[238:239],v51 offset:57352
ds_read_b128 a[244:247],v13 offset:256
v_pk_add_f32 v[36:37],v[36:37],v[240:241]
v_pk_fma_f32 v[44:45],v[240:241],v[240:241],v[44:45]
v_pk_add_f32 v[38:39],v[38:39],v[242:243]
v_pk_fma_f32 v[46:47],v[242:243],v[242:243],v[46:47]
v_smfmac_f32_16x16x64_bf16 v[252:255],a[188:191],a[64:71],v10
ds_read_b128 v[28:31],v4 offset:3584
ds_read_b128 v[24:27],v4 offset:3616
v_pk_add_f32 v[36:37],v[36:37],v[244:245]
v_pk_fma_f32 v[44:45],v[244:245],v[244:245],v[44:45]
v_pk_add_f32 v[38:39],v[38:39],v[246:247]
v_pk_fma_f32 v[46:47],v[246:247],v[246:247],v[46:47]
v_pk_add_f32 v[36:37],v[36:37],v[248:249]
v_pk_fma_f32 v[44:45],v[248:249],v[248:249],v[44:45]
v_pk_add_f32 v[38:39],v[38:39],v[250:251]
v_pk_fma_f32 v[46:47],v[250:251],v[250:251],v[46:47]
v_pk_add_f32 v[36:37],v[36:37],v[252:253]
v_pk_fma_f32 v[44:45],v[252:253],v[252:253],v[44:45]
v_pk_add_f32 v[38:39],v[38:39],v[254:255]
v_pk_fma_f32 v[46:47],v[254:255],v[254:255],v[46:47]
s_nop 0
v_permlane32_swap_b32 v36,v38
v_permlane32_swap_b32 v37,v39
v_permlane32_swap_b32 v44,v46
v_permlane32_swap_b32_e64 v45,v47
v_pk_add_f32 v[36:37],v[36:37],v[38:39]
v_pk_add_f32 v[44:45],v[44:45],v[46:47]
s_add_u32 s60,s60,56576
s_sub_u32 s64,s60,88400
s_min_u32 s60,s60,s64
v_permlane16_swap_b32 v36,v37
v_permlane16_swap_b32 v44,v45
v_add_f32 v8,v36,v37
v_add_f32_e64 v9,v44,v45
v_mul_f32 v8,0x3c2aaaab,v8
v_mul_f32 v9,0x3c2aaaab,v9
v_cmp_ngt_f32 vcc,0x5d800000,v9
v_fma_f32 v9,-v8,v8,v9
v_max_f32 v9,0,v9
s_or_b64 s[90:91],s[90:91],vcc
v_add_f32 v9,0x3727c5ac,v9
v_rsq_f32 v9,v9
s_nop 0
v_mul_f32 v36,s96,v9
v_mul_f32 v37,s96,v9
v_mul_f32 v44,-v8,v36
v_mul_f32 v45,-v8,v36
v_permlane16_swap_b32 v36,v37
s_nop 0
v_permlane16_swap_b32 v44,v45
v_mov_b64 v[38:39],v[36:37]
v_mov_b64 v[46:47],v[44:45]
s_nop 0
v_permlane32_swap_b32 v36,v38
v_permlane32_swap_b32 v37,v39
v_permlane32_swap_b32 v44,v46
v_permlane32_swap_b32 v45,v47
v_pk_fma_f32 v[160:161],v[160:161],v[36:37],v[44:45]
v_pk_fma_f32 v[162:163],v[162:163],v[38:39],v[46:47]
v_pk_fma_f32 v[164:165],v[164:165],v[36:37],v[44:45]
v_pk_fma_f32 v[166:167],v[166:167],v[38:39],v[46:47]
v_pk_fma_f32 v[168:169],v[168:169],v[36:37],v[44:45]
v_pk_fma_f32 v[170:171],v[170:171],v[38:39],v[46:47]
v_pk_fma_f32 v[172:173],v[172:173],v[36:37],v[44:45]
v_pk_fma_f32 v[174:175],v[174:175],v[38:39],v[46:47]
v_pk_fma_f32 v[208:209],v[208:209],v[36:37],v[44:45]
v_pk_fma_f32 v[210:211],v[210:211],v[38:39],v[46:47]
v_pk_fma_f32 v[212:213],v[212:213],v[36:37],v[44:45]
v_pk_fma_f32 v[214:215],v[214:215],v[38:39],v[46:47]
v_pk_fma_f32 v[232:233],v[232:233],v[36:37],v[44:45]
v_pk_fma_f32 v[234:235],v[234:235],v[38:39],v[46:47]
v_pk_fma_f32 v[236:237],v[236:237],v[36:37],v[44:45]
v_pk_fma_f32 v[238:239],v[238:239],v[38:39],v[46:47]
v_pk_fma_f32 v[176:177],v[176:177],v[36:37],v[44:45]
v_pk_fma_f32 v[178:179],v[178:179],v[38:39],v[46:47]
v_pk_fma_f32 v[180:181],v[180:181],v[36:37],v[44:45]
v_pk_fma_f32 v[182:183],v[182:183],v[38:39],v[46:47]
v_pk_fma_f32 v[184:185],v[184:185],v[36:37],v[44:45]
v_pk_fma_f32 v[186:187],v[186:187],v[38:39],v[46:47]
v_pk_fma_f32 v[188:189],v[188:189],v[36:37],v[44:45]
v_pk_fma_f32 v[190:191],v[190:191],v[38:39],v[46:47]
v_pk_fma_f32 v[216:217],v[216:217],v[36:37],v[44:45]
v_pk_fma_f32 v[218:219],v[218:219],v[38:39],v[46:47]
v_pk_fma_f32 v[220:221],v[220:221],v[36:37],v[44:45]
v_pk_fma_f32 v[222:223],v[222:223],v[38:39],v[46:47]
v_pk_fma_f32 v[240:241],v[240:241],v[36:37],v[44:45]
v_pk_fma_f32 v[242:243],v[242:243],v[38:39],v[46:47]
v_pk_fma_f32 v[244:245],v[244:245],v[36:37],v[44:45]
v_pk_fma_f32 v[246:247],v[246:247],v[38:39],v[46:47]
v_pk_fma_f32 v[192:193],v[192:193],v[36:37],v[44:45]
v_pk_fma_f32 v[194:195],v[194:195],v[38:39],v[46:47]
v_pk_fma_f32 v[196:197],v[196:197],v[36:37],v[44:45]
v_pk_fma_f32 v[198:199],v[198:199],v[38:39],v[46:47]
v_pk_fma_f32 v[200:201],v[200:201],v[36:37],v[44:45]
v_pk_fma_f32 v[202:203],v[202:203],v[38:39],v[46:47]
v_pk_fma_f32 v[204:205],v[204:205],v[36:37],v[44:45]
v_pk_fma_f32 v[206:207],v[206:207],v[38:39],v[46:47]
v_pk_fma_f32 v[224:225],v[224:225],v[36:37],v[44:45]
v_pk_fma_f32 v[226:227],v[226:227],v[38:39],v[46:47]
v_pk_fma_f32 v[228:229],v[228:229],v[36:37],v[44:45]
v_pk_fma_f32 v[230:231],v[230:231],v[38:39],v[46:47]
v_pk_fma_f32 v[248:249],v[248:249],v[36:37],v[44:45]
v_pk_fma_f32 v[250:251],v[250:251],v[38:39],v[46:47]
v_pk_fma_f32 v[252:253],v[252:253],v[36:37],v[44:45]
v_pk_fma_f32 v[254:255],v[254:255],v[38:39],v[46:47]
v_mfma_f32_16x16x32_bf16 v[40:43],a[204:207],a[8:11],v[20:23]
ds_read_b64_tr_b16 a[216:217],v1 offset:6160
v_cvt_pk_bf16_f32 v160,v160,v164
v_cvt_pk_bf16_f32 v168,v168,v172
v_cvt_pk_bf16_f32 v208,v208,v212
v_lshlrev_b32_sdwa v32,1,v32 dst_sel:DWORD dst_unused:UNUSED_PAD src0_sel:DWORD src1_sel:WORD_1
v_mfma_f32_16x16x32_bf16 v[40:43],a[208:211],a[24:27],v[40:43]
ds_read_u16 v32,v32
v_accvgpr_write_b32 a48,v160
v_accvgpr_write_b32 a49,v168
v_cvt_pk_bf16_f32 v232,v232,v236
v_accvgpr_write_b32 a50,v208
v_mfma_f32_16x16x32_bf16 v[40:43],a[212:215],a[40:43],v[40:43]
ds_read_b64_tr_b16 a[218:219],v1 offset:7184
v_cvt_pk_bf16_f32 v161,v161,v165
v_accvgpr_write_b32 a51,v232
v_cvt_pk_bf16_f32 v169,v169,v173
v_accvgpr_write_b32 a52,v161
v_mfma_f32_16x16x32_bf16 v[36:39],a[200:203],a[40:43],v[16:19]
ds_read_b64_tr_b16 a[220:221],v1 offset:8208
v_cvt_pk_bf16_f32 v209,v209,v213
v_accvgpr_write_b32 a53,v169
v_cvt_pk_bf16_f32 v233,v233,v237
v_lshlrev_b32_sdwa v34,1,v34 dst_sel:DWORD dst_unused:UNUSED_PAD src0_sel:DWORD src1_sel:WORD_1
v_accvgpr_write_b32 a54,v209
v_mfma_f32_16x16x32_bf16 v[36:39],a[196:199],a[24:27],v[36:39]
ds_read_u16_d16_hi v34,v34
v_cvt_pk_bf16_f32 v162,v162,v166
v_accvgpr_write_b32 a55,v233
v_cvt_pk_bf16_f32 v170,v170,v174
v_accvgpr_write_b32 a56,v162
v_mfma_f32_16x16x32_bf16 v[36:39],a[192:195],a[8:11],v[36:39]
ds_read_b64_tr_b16 a[222:223],v1 offset:9232
v_accvgpr_write_b32 a57,v170
v_cvt_pk_bf16_f32 v210,v210,v214
v_accvgpr_write_b32 a58,v210
v_lshlrev_b32_sdwa v33,1,v33 dst_sel:DWORD dst_unused:UNUSED_PAD src0_sel:DWORD src1_sel:WORD_1
v_cvt_pk_bf16_f32 v234,v234,v238
v_mfma_f32_16x16x32_bf16 v[44:47],a[192:195],a[12:15],v[16:19]
ds_read_u16 v33,v33
v_accvgpr_write_b32 a59,v234
v_cvt_pk_bf16_f32 v163,v163,v167
v_accvgpr_write_b32 a60,v163
v_mfma_f32_16x16x32_bf16 v[44:47],a[196:199],a[28:31],v[44:47]
v_cvt_pk_bf16_f32 v171,v171,v175
v_accvgpr_write_b32 a61,v171
v_cvt_pk_bf16_f32 v211,v211,v215
v_accvgpr_write_b32 a62,v211
v_lshlrev_b32_sdwa v35,1,v35 dst_sel:DWORD dst_unused:UNUSED_PAD src0_sel:DWORD src1_sel:WORD_1
v_mfma_f32_16x16x32_bf16 v[44:47],a[200:203],a[44:47],v[44:47]
s_waitcnt lgkmcnt(7)
ds_read_u16_d16_hi v35,v35
v_cvt_pk_bf16_f32 v235,v235,v239
v_accvgpr_write_b32 a63,v235
v_cvt_pk_bf16_f32 v176,v176,v180
v_cvt_pk_bf16_f32 v184,v184,v188
v_mfma_f32_16x16x32_bf16 v[232:235],a[240:243],a[224:227],v[28:31]
s_nop 0
v_cvt_pk_bf16_f32 v216,v216,v220
v_accvgpr_write_b32 a64,v176
v_accvgpr_write_b32 a65,v184
v_cvt_pk_bf16_f32 v240,v240,v244
v_lshlrev_b32_sdwa v60,1,v60 dst_sel:DWORD dst_unused:UNUSED_PAD src0_sel:DWORD src1_sel:WORD_1
v_mfma_f32_16x16x32_bf16 v[236:239],a[240:243],a[228:231],v[28:31]
ds_read_u16 v60,v60
v_accvgpr_write_b32 a66,v216
v_cvt_pk_bf16_f32 v177,v177,v181
v_accvgpr_write_b32 a67,v240
v_cvt_pk_bf16_f32 v185,v185,v189
v_mfma_f32_16x16x32_bf16 v[208:211],a[240:243],a[232:235],v[28:31]
v_accvgpr_write_b32 a68,v177
v_cvt_pk_bf16_f32 v217,v217,v221
v_accvgpr_write_b32 a69,v185
v_lshlrev_b32_sdwa v62,1,v62 dst_sel:DWORD dst_unused:UNUSED_PAD src0_sel:DWORD src1_sel:WORD_1
v_cvt_pk_bf16_f32 v241,v241,v245
v_mfma_f32_16x16x32_bf16 v[212:215],a[240:243],a[236:239],v[28:31]
ds_read_b128 a[240:243],v13 offset:512
ds_read_b128 v[28:31],v4 offset:3712
v_accvgpr_write_b32 a70,v217
v_cvt_pk_bf16_f32 v178,v178,v182
v_accvgpr_write_b32 a71,v241
v_mfma_f32_16x16x32_bf16 v[160:163],a[244:247],a[224:227],v[24:27]
ds_read_b64 a[224:225],v48 offset:57408
ds_read_b64 a[226:227],v48 offset:57416
v_cvt_pk_bf16_f32 v186,v186,v190
v_accvgpr_write_b32 a72,v178
v_accvgpr_write_b32 a73,v186
v_cvt_pk_bf16_f32 v218,v218,v222
v_lshlrev_b32_sdwa v61,1,v61 dst_sel:DWORD dst_unused:UNUSED_PAD src0_sel:DWORD src1_sel:WORD_1
v_mfma_f32_16x16x32_bf16 v[164:167],a[244:247],a[228:231],v[24:27]
ds_read_b64 a[228:229],v49 offset:57408
ds_read_b64 a[230:231],v49 offset:57416
v_accvgpr_write_b32 a74,v218
v_cvt_pk_bf16_f32 v242,v242,v246
v_accvgpr_write_b32 a75,v242
v_cvt_pk_bf16_f32 v179,v179,v183
v_mfma_f32_16x16x32_bf16 v[168:171],a[244:247],a[232:235],v[24:27]
ds_read_b64 a[232:233],v50 offset:57408
ds_read_b64 a[234:235],v50 offset:57416
v_accvgpr_write_b32 a76,v179
v_cvt_pk_bf16_f32 v187,v187,v191
v_accvgpr_write_b32 a77,v187
v_cvt_pk_bf16_f32 v219,v219,v223
v_lshlrev_b32_sdwa v63,1,v63 dst_sel:DWORD dst_unused:UNUSED_PAD src0_sel:DWORD src1_sel:WORD_1
v_mfma_f32_16x16x32_bf16 v[172:175],a[244:247],a[236:239],v[24:27]
s_waitcnt lgkmcnt(4)
ds_read_b64 a[236:237],v51 offset:57408
ds_read_b64 a[238:239],v51 offset:57416
v_accvgpr_write_b32 a78,v219
v_cvt_pk_bf16_f32 v243,v243,v247
v_accvgpr_write_b32 a79,v243
v_cvt_pk_bf16_f32 v192,v192,v196
v_mfma_f32_16x16x32_bf16 v[240:243],a[240:243],a[224:227],v[28:31]
s_waitcnt lgkmcnt(4)
ds_read_b128 a[244:247],v13 offset:768
ds_read_b128 v[24:27],v4 offset:3744
v_cvt_pk_bf16_f32 v200,v200,v204
v_cvt_pk_bf16_f32 v224,v224,v228
v_accvgpr_write_b32 a80,v192
v_accvgpr_write_b32 a81,v200
v_lshlrev_b32_sdwa v52,1,v52 dst_sel:DWORD dst_unused:UNUSED_PAD src0_sel:DWORD src1_sel:WORD_1
v_mfma_f32_16x16x32_bf16 v[244:247],a[240:243],a[228:231],v[28:31]
s_waitcnt lgkmcnt(4)
ds_read_u16_d16_hi v62,v62
v_cvt_pk_bf16_f32 v248,v248,v252
v_accvgpr_write_b32 a82,v224
v_cvt_pk_bf16_f32 v193,v193,v197
v_mfma_f32_16x16x32_bf16 v[216:219],a[240:243],a[232:235],v[28:31]
s_waitcnt lgkmcnt(3)
ds_read_u16 v61,v61
v_accvgpr_write_b32 a83,v248
v_cvt_pk_bf16_f32 v201,v201,v205
v_accvgpr_write_b32 a84,v193
v_cvt_pk_bf16_f32 v225,v225,v229
v_lshlrev_b32_sdwa v54,1,v54 dst_sel:DWORD dst_unused:UNUSED_PAD src0_sel:DWORD src1_sel:WORD_1
v_mfma_f32_16x16x32_bf16 v[220:223],a[240:243],a[236:239],v[28:31]
s_waitcnt lgkmcnt(2)
ds_read_b128 a[240:243],v13 offset:1024
ds_read_b128 v[28:31],v4 offset:3840
v_accvgpr_write_b32 a85,v201
v_cvt_pk_bf16_f32 v249,v249,v253
v_accvgpr_write_b32 a86,v225
v_cvt_pk_bf16_f32 v194,v194,v198
v_mfma_f32_16x16x32_bf16 v[176:179],a[244:247],a[224:227],v[24:27]
s_nop 0
ds_read_b64 a[224:225],v48 offset:57472
ds_read_b64 a[226:227],v48 offset:57480
v_accvgpr_write_b32 a87,v249
v_cvt_pk_bf16_f32 v202,v202,v206
v_accvgpr_write_b32 a88,v194
v_accvgpr_write_b32 a89,v202
v_lshlrev_b32_sdwa v53,1,v53 dst_sel:DWORD dst_unused:UNUSED_PAD src0_sel:DWORD src1_sel:WORD_1
v_mfma_f32_16x16x32_bf16 v[180:183],a[244:247],a[228:231],v[24:27]
ds_read_b64 a[228:229],v49 offset:57472
ds_read_b64 a[230:231],v49 offset:57480
v_cvt_pk_bf16_f32 v226,v226,v230
v_accvgpr_write_b32 a90,v226
v_cvt_pk_bf16_f32 v250,v250,v254
v_accvgpr_write_b32 a91,v250
v_mfma_f32_16x16x32_bf16 v[184:187],a[244:247],a[232:235],v[24:27]
s_nop 0
ds_read_b64 a[232:233],v50 offset:57472
ds_read_b64 a[234:235],v50 offset:57480
v_cvt_pk_bf16_f32 v195,v195,v199
v_accvgpr_write_b32 a92,v195
v_cvt_pk_bf16_f32 v203,v203,v207
v_accvgpr_write_b32 a93,v203
v_mfma_f32_16x16x32_bf16 v[188:191],a[244:247],a[236:239],v[24:27]
s_waitcnt lgkmcnt(4)
ds_read_b64 a[236:237],v51 offset:57472
ds_read_b64 a[238:239],v51 offset:57480
v_cvt_pk_bf16_f32 v227,v227,v231
v_accvgpr_write_b32 a94,v227
TP2751_%=:
s_cbranch_execz TP4879_%=
v_cvt_pk_bf16_f32 v251,v251,v255
v_lshlrev_b32_sdwa v55,1,v55 dst_sel:DWORD dst_unused:UNUSED_PAD src0_sel:DWORD src1_sel:WORD_1
v_accvgpr_write_b32 a95,v251
v_mfma_f32_16x16x32_bf16 v[248:251],a[240:243],a[224:227],v[28:31]
s_waitcnt lgkmcnt(4)
ds_read_b128 a[244:247],v13 offset:1280
ds_read_b128 v[24:27],v4 offset:3872
v_mfma_f32_16x16x32_bf16 v[252:255],a[240:243],a[228:231],v[28:31]
s_waitcnt lgkmcnt(4)
ds_read_u16_d16_hi v63,v63
ds_read_u16 v52,v52
v_lshlrev_b32_sdwa v56,1,v56 dst_sel:DWORD dst_unused:UNUSED_PAD src0_sel:DWORD src1_sel:WORD_1
v_mfma_f32_16x16x32_bf16 v[224:227],a[240:243],a[232:235],v[28:31]
s_waitcnt lgkmcnt(4)
ds_read_u16_d16_hi v54,v54
v_lshlrev_b32_sdwa v58,1,v58 dst_sel:DWORD dst_unused:UNUSED_PAD src0_sel:DWORD src1_sel:WORD_1
v_mfma_f32_16x16x32_bf16 v[228:231],a[240:243],a[236:239],v[28:31]
s_waitcnt lgkmcnt(3)
ds_read_b64_tr_b16 a[240:241],v3 offset:6224
ds_read_b64_tr_b16 a[242:243],v3 offset:7760
v_mfma_f32_16x16x32_bf16 v[192:195],a[244:247],a[224:227],v[24:27]
ds_read_b64_tr_b16 a[224:225],v3 offset:6232
ds_read_b64_tr_b16 a[226:227],v3 offset:7768
v_mfma_f32_16x16x32_bf16 v[196:199],a[244:247],a[228:231],v[24:27]
ds_read_b64_tr_b16 a[228:229],v3 offset:6288
ds_read_b64_tr_b16 a[230:231],v3 offset:7824
v_lshlrev_b32_sdwa v57,1,v57 dst_sel:DWORD dst_unused:UNUSED_PAD src0_sel:DWORD src1_sel:WORD_1
v_mfma_f32_16x16x32_bf16 v[200:203],a[244:247],a[232:235],v[24:27]
ds_read_b64_tr_b16 a[232:233],v3 offset:6296
ds_read_b64_tr_b16 a[234:235],v3 offset:7832
v_mfma_f32_16x16x32_bf16 v[204:207],a[244:247],a[236:239],v[24:27]
ds_read_u16 v53,v53
ds_read_u16_d16_hi v55,v55
v_lshlrev_b32_sdwa v59,1,v59 dst_sel:DWORD dst_unused:UNUSED_PAD src0_sel:DWORD src1_sel:WORD_1
v_mfma_f32_16x16x32_bf16 v[48:51],a[212:215],a[44:47],v[20:23]
ds_read_u16 v56,v56
v_mfma_f32_16x16x32_bf16 v[48:51],a[208:211],a[28:31],v[48:51]
s_waitcnt vmcnt(9)
ds_read_u16_d16_hi v58,v58
v_mfma_f32_16x16x32_bf16 v[48:51],a[204:207],a[12:15],v[48:51]
s_barrier
ds_read_u16 v57,v57
ds_read_u16_d16_hi v59,v59
v_mfma_f32_16x16x32_bf16 v[28:31],a[204:207],a[48:51],v[20:23]
s_add_u32 m0,s57,56576
ds_read_b64 v[14:15],v7 offset:6160
v_lshlrev_b32_sdwa v40,1,v40 dst_sel:DWORD dst_unused:UNUSED_PAD src0_sel:DWORD src1_sel:WORD_1
v_mfma_f32_16x16x32_bf16 v[28:31],a[208:211],a[64:67],v[28:31]
s_add_u32 s89,s56,4030464
ds_read_u16 v40,v40
v_lshl_add_u64 v[32:33],v[32:33],0,v[34:35]
v_mfma_f32_16x16x32_bf16 v[28:31],a[212:215],a[80:83],v[28:31]
s_waitcnt lgkmcnt(12)
buffer_load_dwordx4 v6,s[36:39],s89 offen lds
v_lshl_add_u64 v[34:35],v[60:61],0,v[62:63]
v_lshlrev_b32_sdwa v42,1,v42 dst_sel:DWORD dst_unused:UNUSED_PAD src0_sel:DWORD src1_sel:WORD_1
v_mfma_f32_16x16x32_bf16 v[24:27],a[200:203],a[80:83],v[16:19]
ds_read_u16_d16_hi v42,v42
v_lshlrev_b32_sdwa v41,1,v41 dst_sel:DWORD dst_unused:UNUSED_PAD src0_sel:DWORD src1_sel:WORD_1
v_mfma_f32_16x16x32_bf16 v[24:27],a[196:199],a[64:67],v[24:27]
s_waitcnt lgkmcnt(7)
ds_read_u16 v41,v41
v_lshl_add_u64 v[60:61],v[52:53],0,v[54:55]
v_mfma_f32_16x16x32_bf16 v[24:27],a[192:195],a[48:51],v[24:27]
s_waitcnt lgkmcnt(3)
v_or3_b32 v13,v14,v15,0
v_lshlrev_b32_sdwa v43,1,v43 dst_sel:DWORD dst_unused:UNUSED_PAD src0_sel:DWORD src1_sel:WORD_1
v_mfma_f32_16x16x32_bf16 v[52:55],a[192:195],a[52:55],v[16:19]
ds_read_b64 v[14:15],v7 offset:6672
v_lshlrev_b32_sdwa v36,1,v36 dst_sel:DWORD dst_unused:UNUSED_PAD src0_sel:DWORD src1_sel:WORD_1
v_mfma_f32_16x16x32_bf16 v[52:55],a[196:199],a[68:71],v[52:55]
ds_read_u16_d16_hi v43,v43
v_lshl_add_u64 v[62:63],v[56:57],0,v[58:59]
v_lshlrev_b32_sdwa v38,1,v38 dst_sel:DWORD dst_unused:UNUSED_PAD src0_sel:DWORD src1_sel:WORD_1
v_mfma_f32_16x16x32_bf16 v[52:55],a[200:203],a[84:87],v[52:55]
ds_read_u16 v36,v36
v_lshlrev_b32_sdwa v37,1,v37 dst_sel:DWORD dst_unused:UNUSED_PAD src0_sel:DWORD src1_sel:WORD_1
v_mfma_f32_16x16x32_bf16 v[56:59],a[212:215],a[84:87],v[20:23]
ds_read_u16_d16_hi v38,v38
v_lshlrev_b32_sdwa v39,1,v39 dst_sel:DWORD dst_unused:UNUSED_PAD src0_sel:DWORD src1_sel:WORD_1
v_mfma_f32_16x16x32_bf16 v[56:59],a[208:211],a[68:71],v[56:59]
ds_read_u16 v37,v37
v_lshlrev_b32_sdwa v48,1,v48 dst_sel:DWORD dst_unused:UNUSED_PAD src0_sel:DWORD src1_sel:WORD_1
v_mfma_f32_16x16x32_bf16 v[56:59],a[204:207],a[52:55],v[56:59]
s_waitcnt lgkmcnt(4)
ds_read_u16_d16_hi v39,v39
v_or3_b32 v13,v14,v15,v13
v_mfma_f32_16x16x32_bf16 v[136:139],a[248:251],v[32:35],v[136:139]
ds_read_b64 v[14:15],v7 offset:7184
v_lshlrev_b32_sdwa v50,1,v50 dst_sel:DWORD dst_unused:UNUSED_PAD src0_sel:DWORD src1_sel:WORD_1
v_mfma_f32_16x16x32_bf16 v[140:143],a[248:251],v[60:63],v[140:143]
ds_read_u16 v48,v48
v_lshlrev_b32_sdwa v49,1,v49 dst_sel:DWORD dst_unused:UNUSED_PAD src0_sel:DWORD src1_sel:WORD_1
v_mfma_f32_16x16x32_bf16 v[68:71],a[252:255],v[60:63],v[68:71]
ds_read_u16_d16_hi v50,v50
v_lshlrev_b32_sdwa v51,1,v51 dst_sel:DWORD dst_unused:UNUSED_PAD src0_sel:DWORD src1_sel:WORD_1
v_mfma_f32_16x16x32_bf16 v[64:67],a[252:255],v[32:35],v[64:67]
ds_read_u16 v49,v49
v_lshlrev_b32_sdwa v44,1,v44 dst_sel:DWORD dst_unused:UNUSED_PAD src0_sel:DWORD src1_sel:WORD_1
v_mfma_f32_16x16x32_bf16 v[144:147],a[240:243],v[32:35],v[144:147]
ds_read_u16_d16_hi v51,v51
v_lshlrev_b32_sdwa v46,1,v46 dst_sel:DWORD dst_unused:UNUSED_PAD src0_sel:DWORD src1_sel:WORD_1
v_mfma_f32_16x16x32_bf16 v[148:151],a[240:243],v[60:63],v[148:151]
s_waitcnt lgkmcnt(4)
ds_read_u16 v44,v44
v_or3_b32 v13,v14,v15,v13
v_mfma_f32_16x16x32_bf16 v[84:87],a[224:227],v[60:63],v[84:87]
ds_read_u16_d16_hi v46,v46
v_lshlrev_b32_sdwa v45,1,v45 dst_sel:DWORD dst_unused:UNUSED_PAD src0_sel:DWORD src1_sel:WORD_1
v_mfma_f32_16x16x32_bf16 v[80:83],a[224:227],v[32:35],v[80:83]
ds_read_u16 v45,v45
v_lshlrev_b32_sdwa v47,1,v47 dst_sel:DWORD dst_unused:UNUSED_PAD src0_sel:DWORD src1_sel:WORD_1
v_mfma_f32_16x16x32_bf16 v[152:155],a[228:231],v[32:35],v[152:155]
ds_read_u16_d16_hi v47,v47
v_mfma_f32_16x16x32_bf16 v[156:159],a[228:231],v[60:63],v[156:159]
v_mfma_f32_16x16x32_bf16 v[100:103],a[232:235],v[60:63],v[100:103]
v_mfma_f32_16x16x32_bf16 v[96:99],a[232:235],v[32:35],v[96:99]
s_add_u32 m0,s57,60120
v_mfma_f32_16x16x32_bf16 v[60:63],a[204:207],a[56:59],v[20:23]
s_add_u32 s89,s56,4128768
v_lshlrev_b32_sdwa v28,1,v28 dst_sel:DWORD dst_unused:UNUSED_PAD src0_sel:DWORD src1_sel:WORD_1
v_fma_mixlo_f16 v8,v53,s99,0
v_mfma_f32_16x16x32_bf16 v[60:63],a[208:211],a[72:75],v[60:63]
buffer_load_dwordx4 v6,s[36:39],s89 offen lds
v_lshl_add_u64 v[40:41],v[40:41],0,v[42:43]
v_mfma_f32_16x16x32_bf16 v[60:63],a[212:215],a[88:91],v[60:63]
ds_read_u16 v28,v28
v_lshl_add_u64 v[42:43],v[36:37],0,v[38:39]
v_lshlrev_b32_sdwa v30,1,v30 dst_sel:DWORD dst_unused:UNUSED_PAD src0_sel:DWORD src1_sel:WORD_1
v_mfma_f32_16x16x32_bf16 v[32:35],a[200:203],a[88:91],v[16:19]
ds_read_u16_d16_hi v30,v30
v_fma_mixhi_f16 v8,v55,s99,0
v_mfma_f32_16x16x32_bf16 v[32:35],a[196:199],a[72:75],v[32:35]
s_waitcnt lgkmcnt(3)
v_lshl_add_u64 v[36:37],v[48:49],0,v[50:51]
v_lshlrev_b32_sdwa v29,1,v29 dst_sel:DWORD dst_unused:UNUSED_PAD src0_sel:DWORD src1_sel:WORD_1
v_pk_fma_f16 v9,v8,v8,s54
v_mfma_f32_16x16x32_bf16 v[32:35],a[192:195],a[56:59],v[32:35]
ds_read_u16 v29,v29
v_lshlrev_b32_sdwa v31,1,v31 dst_sel:DWORD dst_unused:UNUSED_PAD src0_sel:DWORD src1_sel:WORD_1
v_mfma_f32_16x16x32_bf16 v[48:51],a[192:195],a[60:63],v[16:19]
ds_read_b128 v[16:19],v4 offset:768
ds_read_b64_tr_b16 a[192:193],v1 offset:10256
v_pk_mul_f16 v9,v8,v9
v_mfma_f32_16x16x32_bf16 v[48:51],a[196:199],a[76:79],v[48:51]
s_waitcnt lgkmcnt(5)
ds_read_b64_tr_b16 a[194:195],v1 offset:11280
ds_read_b64_tr_b16 a[196:197],v2 offset:10256
v_lshl_add_u64 v[38:39],v[44:45],0,v[46:47]
v_lshlrev_b32_sdwa v24,1,v24 dst_sel:DWORD dst_unused:UNUSED_PAD src0_sel:DWORD src1_sel:WORD_1
v_mfma_f32_16x16x32_bf16 v[48:51],a[200:203],a[92:95],v[48:51]
ds_read_b64_tr_b16 a[198:199],v2 offset:11280
ds_read_b64_tr_b16 a[200:201],v2 offset:8208
v_exp_f16 v8,v9
v_mfma_f32_16x16x32_bf16 v[44:47],a[212:215],a[92:95],v[20:23]
ds_read_b128 v[20:23],v4 offset:800
ds_read_b64_tr_b16 a[202:203],v2 offset:9232
v_lshlrev_b32_sdwa v26,1,v26 dst_sel:DWORD dst_unused:UNUSED_PAD src0_sel:DWORD src1_sel:WORD_1
v_mfma_f32_16x16x32_bf16 v[44:47],a[208:211],a[76:79],v[44:47]
ds_read_b64_tr_b16 a[212:213],v2 offset:6160
ds_read_b64_tr_b16 a[214:215],v2 offset:7184
v_lshlrev_b32_sdwa v25,1,v25 dst_sel:DWORD dst_unused:UNUSED_PAD src0_sel:DWORD src1_sel:WORD_1
v_mfma_f32_16x16x32_bf16 v[44:47],a[204:207],a[60:63],v[44:47]
ds_read_u16_d16_hi v31,v31
v_exp_f16_sdwa v8,v9 dst_sel:WORD_1 dst_unused:UNUSED_PRESERVE src0_sel:WORD_1
v_mfma_f32_16x16x32_bf16 v[112:115],a[248:251],v[40:43],v[112:115]
ds_read_u16 v24,v24
v_lshlrev_b32_sdwa v27,1,v27 dst_sel:DWORD dst_unused:UNUSED_PAD src0_sel:DWORD src1_sel:WORD_1
v_mfma_f32_16x16x32_bf16 v[116:119],a[248:251],v[36:39],v[116:119]
ds_read_u16_d16_hi v26,v26
v_lshlrev_b32_sdwa v56,1,v56 dst_sel:DWORD dst_unused:UNUSED_PAD src0_sel:DWORD src1_sel:WORD_1
v_mfma_f32_16x16x32_bf16 v[76:79],a[252:255],v[36:39],v[76:79]
ds_read_u16 v25,v25
v_pk_fma_f16 v8,v8,s55,s55
v_mfma_f32_16x16x32_bf16 v[72:75],a[252:255],v[40:43],v[72:75]
v_rcp_f16 v9,v8
v_lshlrev_b32_sdwa v58,1,v58 dst_sel:DWORD dst_unused:UNUSED_PAD src0_sel:DWORD src1_sel:WORD_1
v_mfma_f32_16x16x32_bf16 v[120:123],a[240:243],v[40:43],v[120:123]
ds_read_u16_d16_hi v27,v27
v_lshlrev_b32_sdwa v57,1,v57 dst_sel:DWORD dst_unused:UNUSED_PAD src0_sel:DWORD src1_sel:WORD_1
v_mfma_f32_16x16x32_bf16 v[124:127],a[240:243],v[36:39],v[124:127]
ds_read_u16 v56,v56
v_lshlrev_b32_sdwa v59,1,v59 dst_sel:DWORD dst_unused:UNUSED_PAD src0_sel:DWORD src1_sel:WORD_1
v_mfma_f32_16x16x32_bf16 v[92:95],a[224:227],v[36:39],v[92:95]
ds_read_u16_d16_hi v58,v58
v_lshlrev_b32_sdwa v52,1,v52 dst_sel:DWORD dst_unused:UNUSED_PAD src0_sel:DWORD src1_sel:WORD_1
v_mfma_f32_16x16x32_bf16 v[88:91],a[224:227],v[40:43],v[88:91]
v_rcp_f16_sdwa v9,v8 dst_sel:WORD_1 dst_unused:UNUSED_PRESERVE src0_sel:WORD_1
v_mfma_f32_16x16x32_bf16 v[128:131],a[228:231],v[40:43],v[128:131]
ds_read_u16 v57,v57
ds_read_u16_d16_hi v59,v59
v_mfma_f32_16x16x32_bf16 v[132:135],a[228:231],v[36:39],v[132:135]
ds_read_u16 v52,v52
v_lshlrev_b32_sdwa v54,1,v54 dst_sel:DWORD dst_unused:UNUSED_PAD src0_sel:DWORD src1_sel:WORD_1
v_mfma_f32_16x16x32_bf16 v[108:111],a[232:235],v[36:39],v[108:111]
ds_read_u16_d16_hi v54,v54
v_fma_mix_f32 v53,v53,v9,0 op_sel_hi:[0,1,0]
v_mfma_f32_16x16x32_bf16 v[104:107],a[232:235],v[40:43],v[104:107]
s_waitcnt lgkmcnt(7)
v_fma_mix_f32 v55,v55,v9,0 op_sel:[0,1,0] op_sel_hi:[0,1,0]
v_mfma_f32_16x16x32_bf16 v[36:39],a[216:219],a[0:3],v[16:19]
s_add_u32 m0,s57,63656
v_lshlrev_b32_sdwa v60,1,v60 dst_sel:DWORD dst_unused:UNUSED_PAD src0_sel:DWORD src1_sel:WORD_1
v_lshlrev_b32_sdwa v62,1,v62 dst_sel:DWORD dst_unused:UNUSED_PAD src0_sel:DWORD src1_sel:WORD_1
v_mfma_f32_16x16x32_bf16 v[36:39],a[220:223],a[16:19],v[36:39]
s_add_u32 s89,s56,4227072
ds_read_u16 v60,v60
v_lshl_add_u64 v[28:29],v[28:29],0,v[30:31]
v_mfma_f32_16x16x32_bf16 v[36:39],a[192:195],a[32:35],v[36:39]
s_waitcnt lgkmcnt(6)
buffer_load_dwordx4 v6,s[36:39],s89 offen lds
v_lshl_add_u64 v[30:31],v[24:25],0,v[26:27]
v_mfma_f32_16x16x32_bf16 v[40:43],a[196:199],a[32:35],v[20:23]
ds_read_u16_d16_hi v62,v62
v_lshlrev_b32_sdwa v61,1,v61 dst_sel:DWORD dst_unused:UNUSED_PAD src0_sel:DWORD src1_sel:WORD_1
v_mfma_f32_16x16x32_bf16 v[40:43],a[200:203],a[16:19],v[40:43]
s_waitcnt lgkmcnt(4)
ds_read_u16 v61,v61
v_lshl_add_u64 v[24:25],v[56:57],0,v[58:59]
v_mfma_f32_16x16x32_bf16 v[40:43],a[212:215],a[0:3],v[40:43]
v_lshlrev_b32_sdwa v63,1,v63 dst_sel:DWORD dst_unused:UNUSED_PAD src0_sel:DWORD src1_sel:WORD_1
v_lshlrev_b32_sdwa v32,1,v32 dst_sel:DWORD dst_unused:UNUSED_PAD src0_sel:DWORD src1_sel:WORD_1
v_mfma_f32_16x16x32_bf16 v[56:59],a[212:215],a[4:7],v[20:23]
s_waitcnt lgkmcnt(3)
ds_read_u16_d16_hi v63,v63
v_or_b32 v26,v52,v54
v_mfma_f32_16x16x32_bf16 v[56:59],a[200:203],a[20:23],v[56:59]
ds_read_u16 v32,v32
v_cvt_pk_bf16_f32 v27,v53,v55
v_lshlrev_b32_sdwa v34,1,v34 dst_sel:DWORD dst_unused:UNUSED_PAD src0_sel:DWORD src1_sel:WORD_1
v_mfma_f32_16x16x32_bf16 v[56:59],a[196:199],a[36:39],v[56:59]
ds_read_u16_d16_hi v34,v34
v_lshlrev_b32_sdwa v33,1,v33 dst_sel:DWORD dst_unused:UNUSED_PAD src0_sel:DWORD src1_sel:WORD_1
v_mfma_f32_16x16x32_bf16 v[52:55],a[192:195],a[36:39],v[16:19]
ds_read_u16 v33,v33
v_lshlrev_b32_sdwa v35,1,v35 dst_sel:DWORD dst_unused:UNUSED_PAD src0_sel:DWORD src1_sel:WORD_1
v_mfma_f32_16x16x32_bf16 v[52:55],a[220:223],a[20:23],v[52:55]
ds_read_u16_d16_hi v35,v35
v_lshlrev_b32_sdwa v44,1,v44 dst_sel:DWORD dst_unused:UNUSED_PAD src0_sel:DWORD src1_sel:WORD_1
v_mfma_f32_16x16x32_bf16 v[52:55],a[216:219],a[4:7],v[52:55]
ds_read_u16 v44,v44
v_lshlrev_b32_sdwa v46,1,v46 dst_sel:DWORD dst_unused:UNUSED_PAD src0_sel:DWORD src1_sel:WORD_1
v_mfma_f32_16x16x32_bf16 v[232:235],a[248:251],v[28:31],v[232:235]
ds_read_u16_d16_hi v46,v46
v_lshlrev_b32_sdwa v45,1,v45 dst_sel:DWORD dst_unused:UNUSED_PAD src0_sel:DWORD src1_sel:WORD_1
v_mfma_f32_16x16x32_bf16 v[236:239],a[248:251],v[24:27],v[236:239]
ds_read_u16 v45,v45
v_lshlrev_b32_sdwa v47,1,v47 dst_sel:DWORD dst_unused:UNUSED_PAD src0_sel:DWORD src1_sel:WORD_1
v_mfma_f32_16x16x32_bf16 v[164:167],a[252:255],v[24:27],v[164:167]
ds_read_u16_d16_hi v47,v47
ds_read_b64_tr_b16 a[236:237],v3 offset:12320
v_mfma_f32_16x16x32_bf16 v[160:163],a[252:255],v[28:31],v[160:163]
ds_read_b64_tr_b16 a[238:239],v3 offset:13856
v_lshlrev_b32_sdwa v48,1,v48 dst_sel:DWORD dst_unused:UNUSED_PAD src0_sel:DWORD src1_sel:WORD_1
v_mfma_f32_16x16x32_bf16 v[240:243],a[240:243],v[28:31],v[240:243]
ds_read_u16 v48,v48
v_lshlrev_b32_sdwa v50,1,v50 dst_sel:DWORD dst_unused:UNUSED_PAD src0_sel:DWORD src1_sel:WORD_1
v_mfma_f32_16x16x32_bf16 v[244:247],a[240:243],v[24:27],v[244:247]
ds_read_u16_d16_hi v50,v50
v_lshlrev_b32_sdwa v49,1,v49 dst_sel:DWORD dst_unused:UNUSED_PAD src0_sel:DWORD src1_sel:WORD_1
v_mfma_f32_16x16x32_bf16 v[180:183],a[224:227],v[24:27],v[180:183]
s_add_u32 m0,s57,67184
ds_read_u16 v49,v49
v_lshlrev_b32_sdwa v51,1,v51 dst_sel:DWORD dst_unused:UNUSED_PAD src0_sel:DWORD src1_sel:WORD_1
v_mfma_f32_16x16x32_bf16 v[176:179],a[224:227],v[28:31],v[176:179]
s_add_u32 s89,s56,4325376
ds_read_u16_d16_hi v51,v51
v_mfma_f32_16x16x32_bf16 v[248:251],a[228:231],v[28:31],v[248:251]
buffer_load_dwordx4 v6,s[36:39],s89 offen lds
v_mfma_f32_16x16x32_bf16 v[252:255],a[228:231],v[24:27],v[252:255]
ds_read_b64_tr_b16 a[244:245],v3 offset:12328
v_mfma_f32_16x16x32_bf16 v[196:199],a[232:235],v[24:27],v[196:199]
ds_read_b64_tr_b16 a[246:247],v3 offset:13864
v_mfma_f32_16x16x32_bf16 v[192:195],a[232:235],v[28:31],v[192:195]
v_mfma_f32_16x16x32_bf16 v[24:27],a[216:219],a[8:11],v[16:19]
ds_read_b64_tr_b16 a[208:209],v1 offset:12320
v_lshlrev_b32_sdwa v36,1,v36 dst_sel:DWORD dst_unused:UNUSED_PAD src0_sel:DWORD src1_sel:WORD_1
v_mfma_f32_16x16x32_bf16 v[24:27],a[220:223],a[24:27],v[24:27]
s_waitcnt lgkmcnt(6)
ds_read_u16 v36,v36
v_lshl_add_u64 v[60:61],v[60:61],0,v[62:63]
v_fma_mixlo_f16 v8,v57,s99,0
v_mfma_f32_16x16x32_bf16 v[24:27],a[192:195],a[40:43],v[24:27]
ds_read_b64_tr_b16 a[210:211],v1 offset:13344
v_lshl_add_u64 v[62:63],v[32:33],0,v[34:35]
v_lshlrev_b32_sdwa v38,1,v38 dst_sel:DWORD dst_unused:UNUSED_PAD src0_sel:DWORD src1_sel:WORD_1
v_mfma_f32_16x16x32_bf16 v[28:31],a[196:199],a[40:43],v[20:23]
ds_read_u16_d16_hi v38,v38
v_fma_mixhi_f16 v8,v59,s99,0
v_mfma_f32_16x16x32_bf16 v[28:31],a[200:203],a[24:27],v[28:31]
ds_read_b64_tr_b16 a[204:205],v1 offset:14368
v_lshl_add_u64 v[32:33],v[44:45],0,v[46:47]
v_lshlrev_b32_sdwa v37,1,v37 dst_sel:DWORD dst_unused:UNUSED_PAD src0_sel:DWORD src1_sel:WORD_1
v_pk_fma_f16 v9,v8,v8,s54
v_mfma_f32_16x16x32_bf16 v[28:31],a[212:215],a[8:11],v[28:31]
ds_read_u16 v37,v37
v_lshlrev_b32_sdwa v39,1,v39 dst_sel:DWORD dst_unused:UNUSED_PAD src0_sel:DWORD src1_sel:WORD_1
v_mfma_f32_16x16x32_bf16 v[44:47],a[212:215],a[12:15],v[20:23]
ds_read_u16_d16_hi v39,v39
v_pk_mul_f16 v9,v8,v9
v_mfma_f32_16x16x32_bf16 v[44:47],a[200:203],a[28:31],v[44:47]
s_waitcnt lgkmcnt(7)
ds_read_b64_tr_b16 a[206:207],v1 offset:15392
v_lshl_add_u64 v[34:35],v[48:49],0,v[50:51]
v_lshlrev_b32_sdwa v40,1,v40 dst_sel:DWORD dst_unused:UNUSED_PAD src0_sel:DWORD src1_sel:WORD_1
v_mfma_f32_16x16x32_bf16 v[44:47],a[196:199],a[44:47],v[44:47]
v_exp_f16 v8,v9
v_mfma_f32_16x16x32_bf16 v[48:51],a[192:195],a[44:47],v[16:19]
ds_read_u16 v40,v40
v_lshlrev_b32_sdwa v42,1,v42 dst_sel:DWORD dst_unused:UNUSED_PAD src0_sel:DWORD src1_sel:WORD_1
v_mfma_f32_16x16x32_bf16 v[48:51],a[220:223],a[28:31],v[48:51]
ds_read_u16_d16_hi v42,v42
v_lshlrev_b32_sdwa v41,1,v41 dst_sel:DWORD dst_unused:UNUSED_PAD src0_sel:DWORD src1_sel:WORD_1
v_mfma_f32_16x16x32_bf16 v[48:51],a[216:219],a[12:15],v[48:51]
v_exp_f16_sdwa v8,v9 dst_sel:WORD_1 dst_unused:UNUSED_PRESERVE src0_sel:WORD_1
v_mfma_f32_16x16x32_bf16 v[208:211],a[248:251],v[60:63],v[208:211]
ds_read_u16 v41,v41
v_lshlrev_b32_sdwa v43,1,v43 dst_sel:DWORD dst_unused:UNUSED_PAD src0_sel:DWORD src1_sel:WORD_1
v_mfma_f32_16x16x32_bf16 v[212:215],a[248:251],v[32:35],v[212:215]
ds_read_u16_d16_hi v43,v43
v_lshlrev_b32_sdwa v52,1,v52 dst_sel:DWORD dst_unused:UNUSED_PAD src0_sel:DWORD src1_sel:WORD_1
v_mfma_f32_16x16x32_bf16 v[172:175],a[252:255],v[32:35],v[172:175]
ds_read_u16 v52,v52
v_pk_fma_f16 v8,v8,s55,s55
v_mfma_f32_16x16x32_bf16 v[168:171],a[252:255],v[60:63],v[168:171]
ds_read_b64_tr_b16 a[248:249],v3 offset:12384
v_rcp_f16 v9,v8
v_lshlrev_b32_sdwa v54,1,v54 dst_sel:DWORD dst_unused:UNUSED_PAD src0_sel:DWORD src1_sel:WORD_1
v_mfma_f32_16x16x32_bf16 v[216:219],a[240:243],v[60:63],v[216:219]
ds_read_u16_d16_hi v54,v54
v_lshlrev_b32_sdwa v53,1,v53 dst_sel:DWORD dst_unused:UNUSED_PAD src0_sel:DWORD src1_sel:WORD_1
v_mfma_f32_16x16x32_bf16 v[220:223],a[240:243],v[32:35],v[220:223]
ds_read_u16 v53,v53
ds_read_b64_tr_b16 a[250:251],v3 offset:13920
v_mfma_f32_16x16x32_bf16 v[188:191],a[224:227],v[32:35],v[188:191]
ds_read_b64_tr_b16 a[252:253],v3 offset:12392
v_lshlrev_b32_sdwa v55,1,v55 dst_sel:DWORD dst_unused:UNUSED_PAD src0_sel:DWORD src1_sel:WORD_1
v_mfma_f32_16x16x32_bf16 v[184:187],a[224:227],v[60:63],v[184:187]
ds_read_u16_d16_hi v55,v55
v_rcp_f16_sdwa v9,v8 dst_sel:WORD_1 dst_unused:UNUSED_PRESERVE src0_sel:WORD_1
v_mfma_f32_16x16x32_bf16 v[224:227],a[228:231],v[60:63],v[224:227]
ds_read_b64_tr_b16 a[254:255],v3 offset:13928
ds_read_b64_tr_b16 a[240:241],v3 offset:12448
v_lshlrev_b32_sdwa v56,1,v56 dst_sel:DWORD dst_unused:UNUSED_PAD src0_sel:DWORD src1_sel:WORD_1
v_mfma_f32_16x16x32_bf16 v[228:231],a[228:231],v[32:35],v[228:231]
ds_read_u16 v56,v56
v_lshlrev_b32_sdwa v58,1,v58 dst_sel:DWORD dst_unused:UNUSED_PAD src0_sel:DWORD src1_sel:WORD_1
v_mfma_f32_16x16x32_bf16 v[204:207],a[232:235],v[32:35],v[204:207]
ds_read_u16_d16_hi v58,v58
v_fma_mix_f32 v57,v57,v9,0 op_sel_hi:[0,1,0]
v_mfma_f32_16x16x32_bf16 v[200:203],a[232:235],v[60:63],v[200:203]
ds_read_b64_tr_b16 a[242:243],v3 offset:13984
v_fma_mix_f32 v59,v59,v9,0 op_sel:[0,1,0] op_sel_hi:[0,1,0]
v_mfma_f32_16x16x32_bf16 v[32:35],a[216:219],a[48:51],v[16:19]
s_add_u32 m0,s57,70720
ds_read_b64 v[14:15],v7 offset:12320
v_lshlrev_b32_sdwa v24,1,v24 dst_sel:DWORD dst_unused:UNUSED_PAD src0_sel:DWORD src1_sel:WORD_1
v_mfma_f32_16x16x32_bf16 v[32:35],a[220:223],a[64:67],v[32:35]
s_waitcnt lgkmcnt(10)
ds_read_b64_tr_b16 a[224:225],v3 offset:12456
v_lshl_add_u64 v[36:37],v[36:37],0,v[38:39]
v_mfma_f32_16x16x32_bf16 v[32:35],a[192:195],a[80:83],v[32:35]
s_add_u32 s89,s56,4423680
ds_read_b64_tr_b16 a[226:227],v3 offset:13992
v_lshl_add_u64 v[38:39],v[40:41],0,v[42:43]
v_lshlrev_b32_sdwa v26,1,v26 dst_sel:DWORD dst_unused:UNUSED_PAD src0_sel:DWORD src1_sel:WORD_1
v_mfma_f32_16x16x32_bf16 v[60:63],a[196:199],a[80:83],v[20:23]
buffer_load_dwordx4 v6,s[36:39],s89 offen lds
v_lshlrev_b32_sdwa v25,1,v25 dst_sel:DWORD dst_unused:UNUSED_PAD src0_sel:DWORD src1_sel:WORD_1
v_mfma_f32_16x16x32_bf16 v[60:63],a[200:203],a[64:67],v[60:63]
s_waitcnt lgkmcnt(8)
ds_read_u16 v24,v24
v_lshl_add_u64 v[40:41],v[52:53],0,v[54:55]
v_mfma_f32_16x16x32_bf16 v[60:63],a[212:215],a[48:51],v[60:63]
s_waitcnt lgkmcnt(3)
ds_read_u16_d16_hi v26,v26
v_or3_b32 v13,v14,v15,v13
v_lshlrev_b32_sdwa v27,1,v27 dst_sel:DWORD dst_unused:UNUSED_PAD src0_sel:DWORD src1_sel:WORD_1
v_mfma_f32_16x16x32_bf16 v[52:55],a[212:215],a[52:55],v[20:23]
ds_read_b64 v[14:15],v7 offset:12832
v_or_b32 v42,v56,v58
v_lshlrev_b32_sdwa v28,1,v28 dst_sel:DWORD dst_unused:UNUSED_PAD src0_sel:DWORD src1_sel:WORD_1
v_mfma_f32_16x16x32_bf16 v[52:55],a[200:203],a[68:71],v[52:55]
ds_read_u16 v25,v25
v_cvt_pk_bf16_f32 v43,v57,v59
v_lshlrev_b32_sdwa v30,1,v30 dst_sel:DWORD dst_unused:UNUSED_PAD src0_sel:DWORD src1_sel:WORD_1
v_mfma_f32_16x16x32_bf16 v[52:55],a[196:199],a[84:87],v[52:55]
ds_read_u16_d16_hi v27,v27
v_lshlrev_b32_sdwa v29,1,v29 dst_sel:DWORD dst_unused:UNUSED_PAD src0_sel:DWORD src1_sel:WORD_1
v_mfma_f32_16x16x32_bf16 v[56:59],a[192:195],a[84:87],v[16:19]
ds_read_u16 v28,v28
v_lshlrev_b32_sdwa v31,1,v31 dst_sel:DWORD dst_unused:UNUSED_PAD src0_sel:DWORD src1_sel:WORD_1
v_mfma_f32_16x16x32_bf16 v[56:59],a[220:223],a[68:71],v[56:59]
ds_read_u16_d16_hi v30,v30
v_lshlrev_b32_sdwa v48,1,v48 dst_sel:DWORD dst_unused:UNUSED_PAD src0_sel:DWORD src1_sel:WORD_1
v_mfma_f32_16x16x32_bf16 v[56:59],a[216:219],a[52:55],v[56:59]
s_waitcnt lgkmcnt(4)
ds_read_u16 v29,v29
v_or3_b32 v13,v14,v15,v13
v_mfma_f32_16x16x32_bf16 v[136:139],a[236:239],v[36:39],v[136:139]
ds_read_b64 v[14:15],v7 offset:13344
v_lshlrev_b32_sdwa v50,1,v50 dst_sel:DWORD dst_unused:UNUSED_PAD src0_sel:DWORD src1_sel:WORD_1
v_mfma_f32_16x16x32_bf16 v[140:143],a[236:239],v[40:43],v[140:143]
s_add_u32 m0,s57,74264
ds_read_u16_d16_hi v31,v31
v_lshlrev_b32_sdwa v49,1,v49 dst_sel:DWORD dst_unused:UNUSED_PAD src0_sel:DWORD src1_sel:WORD_1
v_mfma_f32_16x16x32_bf16 v[68:71],a[244:247],v[40:43],v[68:71]
s_add_u32 s89,s56,4521984
ds_read_u16 v48,v48
v_lshlrev_b32_sdwa v51,1,v51 dst_sel:DWORD dst_unused:UNUSED_PAD src0_sel:DWORD src1_sel:WORD_1
v_mfma_f32_16x16x32_bf16 v[64:67],a[244:247],v[36:39],v[64:67]
buffer_load_dwordx4 v6,s[36:39],s89 offen lds
v_lshlrev_b32_sdwa v44,1,v44 dst_sel:DWORD dst_unused:UNUSED_PAD src0_sel:DWORD src1_sel:WORD_1
v_mfma_f32_16x16x32_bf16 v[144:147],a[248:251],v[36:39],v[144:147]
ds_read_u16_d16_hi v50,v50
v_lshlrev_b32_sdwa v46,1,v46 dst_sel:DWORD dst_unused:UNUSED_PAD src0_sel:DWORD src1_sel:WORD_1
v_mfma_f32_16x16x32_bf16 v[148:151],a[248:251],v[40:43],v[148:151]
s_waitcnt lgkmcnt(3)
ds_read_u16 v49,v49
v_or3_b32 v13,v14,v15,v13
v_mfma_f32_16x16x32_bf16 v[84:87],a[252:255],v[40:43],v[84:87]
ds_read_u16_d16_hi v51,v51
v_lshlrev_b32_sdwa v45,1,v45 dst_sel:DWORD dst_unused:UNUSED_PAD src0_sel:DWORD src1_sel:WORD_1
v_mfma_f32_16x16x32_bf16 v[80:83],a[252:255],v[36:39],v[80:83]
ds_read_u16 v44,v44
v_lshlrev_b32_sdwa v47,1,v47 dst_sel:DWORD dst_unused:UNUSED_PAD src0_sel:DWORD src1_sel:WORD_1
v_mfma_f32_16x16x32_bf16 v[152:155],a[240:243],v[36:39],v[152:155]
ds_read_u16_d16_hi v46,v46
v_mfma_f32_16x16x32_bf16 v[156:159],a[240:243],v[40:43],v[156:159]
ds_read_u16 v45,v45
v_mfma_f32_16x16x32_bf16 v[100:103],a[224:227],v[40:43],v[100:103]
ds_read_u16_d16_hi v47,v47
v_mfma_f32_16x16x32_bf16 v[96:99],a[224:227],v[36:39],v[96:99]
v_mfma_f32_16x16x32_bf16 v[40:43],a[216:219],a[56:59],v[16:19]
v_lshlrev_b32_sdwa v32,1,v32 dst_sel:DWORD dst_unused:UNUSED_PAD src0_sel:DWORD src1_sel:WORD_1
v_fma_mixlo_f16 v8,v53,s99,0
v_mfma_f32_16x16x32_bf16 v[40:43],a[220:223],a[72:75],v[40:43]
ds_read_u16 v32,v32
v_lshl_add_u64 v[24:25],v[24:25],0,v[26:27]
v_mfma_f32_16x16x32_bf16 v[40:43],a[192:195],a[88:91],v[40:43]
s_waitcnt lgkmcnt(6)
v_lshl_add_u64 v[26:27],v[28:29],0,v[30:31]
v_lshlrev_b32_sdwa v34,1,v34 dst_sel:DWORD dst_unused:UNUSED_PAD src0_sel:DWORD src1_sel:WORD_1
v_mfma_f32_16x16x32_bf16 v[36:39],a[196:199],a[88:91],v[20:23]
ds_read_u16_d16_hi v34,v34
v_fma_mixhi_f16 v8,v55,s99,0
v_mfma_f32_16x16x32_bf16 v[36:39],a[200:203],a[72:75],v[36:39]
s_waitcnt lgkmcnt(5)
v_lshl_add_u64 v[28:29],v[48:49],0,v[50:51]
v_lshlrev_b32_sdwa v33,1,v33 dst_sel:DWORD dst_unused:UNUSED_PAD src0_sel:DWORD src1_sel:WORD_1
v_pk_fma_f16 v9,v8,v8,s54
v_mfma_f32_16x16x32_bf16 v[36:39],a[212:215],a[56:59],v[36:39]
ds_read_u16 v33,v33
v_lshlrev_b32_sdwa v35,1,v35 dst_sel:DWORD dst_unused:UNUSED_PAD src0_sel:DWORD src1_sel:WORD_1
v_mfma_f32_16x16x32_bf16 v[48:51],a[212:215],a[60:63],v[20:23]
ds_read_b128 v[20:23],v4 offset:896
v_pk_mul_f16 v9,v8,v9
v_mfma_f32_16x16x32_bf16 v[48:51],a[200:203],a[76:79],v[48:51]
s_waitcnt lgkmcnt(4)
ds_read_u16_d16_hi v35,v35
ds_read_b64_tr_b16 a[212:213],v1 offset:16416
v_lshl_add_u64 v[30:31],v[44:45],0,v[46:47]
v_lshlrev_b32_sdwa v60,1,v60 dst_sel:DWORD dst_unused:UNUSED_PAD src0_sel:DWORD src1_sel:WORD_1
v_mfma_f32_16x16x32_bf16 v[48:51],a[196:199],a[92:95],v[48:51]
ds_read_b64_tr_b16 a[214:215],v1 offset:17440
v_exp_f16 v8,v9
v_mfma_f32_16x16x32_bf16 v[44:47],a[192:195],a[92:95],v[16:19]
ds_read_b128 v[16:19],v4 offset:928
ds_read_u16 v60,v60
v_lshlrev_b32_sdwa v62,1,v62 dst_sel:DWORD dst_unused:UNUSED_PAD src0_sel:DWORD src1_sel:WORD_1
v_mfma_f32_16x16x32_bf16 v[44:47],a[220:223],a[76:79],v[44:47]
ds_read_u16_d16_hi v62,v62
ds_read_b64_tr_b16 a[200:201],v2 offset:16416
v_lshlrev_b32_sdwa v61,1,v61 dst_sel:DWORD dst_unused:UNUSED_PAD src0_sel:DWORD src1_sel:WORD_1
v_mfma_f32_16x16x32_bf16 v[44:47],a[216:219],a[60:63],v[44:47]
ds_read_u16 v61,v61
v_exp_f16_sdwa v8,v9 dst_sel:WORD_1 dst_unused:UNUSED_PRESERVE src0_sel:WORD_1
v_mfma_f32_16x16x32_bf16 v[112:115],a[236:239],v[24:27],v[112:115]
ds_read_b64_tr_b16 a[202:203],v2 offset:17440
ds_read_b64_tr_b16 a[196:197],v2 offset:14368
v_lshlrev_b32_sdwa v63,1,v63 dst_sel:DWORD dst_unused:UNUSED_PAD src0_sel:DWORD src1_sel:WORD_1
v_mfma_f32_16x16x32_bf16 v[116:119],a[236:239],v[28:31],v[116:119]
ds_read_u16_d16_hi v63,v63
v_lshlrev_b32_sdwa v56,1,v56 dst_sel:DWORD dst_unused:UNUSED_PAD src0_sel:DWORD src1_sel:WORD_1
v_mfma_f32_16x16x32_bf16 v[76:79],a[244:247],v[28:31],v[76:79]
ds_read_b64_tr_b16 a[198:199],v2 offset:15392
v_pk_fma_f16 v8,v8,s55,s55
v_mfma_f32_16x16x32_bf16 v[72:75],a[244:247],v[24:27],v[72:75]
v_rcp_f16 v9,v8
v_lshlrev_b32_sdwa v58,1,v58 dst_sel:DWORD dst_unused:UNUSED_PAD src0_sel:DWORD src1_sel:WORD_1
v_mfma_f32_16x16x32_bf16 v[120:123],a[248:251],v[24:27],v[120:123]
ds_read_u16 v56,v56
v_lshlrev_b32_sdwa v57,1,v57 dst_sel:DWORD dst_unused:UNUSED_PAD src0_sel:DWORD src1_sel:WORD_1
v_mfma_f32_16x16x32_bf16 v[124:127],a[248:251],v[28:31],v[124:127]
ds_read_u16_d16_hi v58,v58
v_lshlrev_b32_sdwa v59,1,v59 dst_sel:DWORD dst_unused:UNUSED_PAD src0_sel:DWORD src1_sel:WORD_1
v_mfma_f32_16x16x32_bf16 v[92:95],a[252:255],v[28:31],v[92:95]
ds_read_u16 v57,v57
v_lshlrev_b32_sdwa v52,1,v52 dst_sel:DWORD dst_unused:UNUSED_PAD src0_sel:DWORD src1_sel:WORD_1
v_mfma_f32_16x16x32_bf16 v[88:91],a[252:255],v[24:27],v[88:91]
ds_read_u16_d16_hi v59,v59
v_rcp_f16_sdwa v9,v8 dst_sel:WORD_1 dst_unused:UNUSED_PRESERVE src0_sel:WORD_1
v_mfma_f32_16x16x32_bf16 v[128:131],a[240:243],v[24:27],v[128:131]
ds_read_b64_tr_b16 a[192:193],v2 offset:12320
v_lshlrev_b32_sdwa v54,1,v54 dst_sel:DWORD dst_unused:UNUSED_PAD src0_sel:DWORD src1_sel:WORD_1
v_mfma_f32_16x16x32_bf16 v[132:135],a[240:243],v[28:31],v[132:135]
ds_read_b64_tr_b16 a[194:195],v2 offset:13344
v_fma_mix_f32 v53,v53,v9,0 op_sel_hi:[0,1,0]
v_mfma_f32_16x16x32_bf16 v[108:111],a[224:227],v[28:31],v[108:111]
ds_read_u16 v52,v52
v_fma_mix_f32 v55,v55,v9,0 op_sel:[0,1,0] op_sel_hi:[0,1,0]
v_mfma_f32_16x16x32_bf16 v[104:107],a[224:227],v[24:27],v[104:107]
s_waitcnt lgkmcnt(7)
ds_read_u16_d16_hi v54,v54
v_mfma_f32_16x16x32_bf16 v[28:31],a[208:211],a[0:3],v[20:23]
s_add_u32 m0,s57,77800
v_lshlrev_b32_sdwa v40,1,v40 dst_sel:DWORD dst_unused:UNUSED_PAD src0_sel:DWORD src1_sel:WORD_1
v_lshlrev_b32_sdwa v42,1,v42 dst_sel:DWORD dst_unused:UNUSED_PAD src0_sel:DWORD src1_sel:WORD_1
v_mfma_f32_16x16x32_bf16 v[28:31],a[204:207],a[16:19],v[28:31]
s_add_u32 s89,s56,4620288
ds_read_u16 v40,v40
v_lshl_add_u64 v[32:33],v[32:33],0,v[34:35]
v_mfma_f32_16x16x32_bf16 v[28:31],a[212:215],a[32:35],v[28:31]
buffer_load_dwordx4 v6,s[36:39],s89 offen lds
v_lshl_add_u64 v[34:35],v[60:61],0,v[62:63]
v_mfma_f32_16x16x32_bf16 v[24:27],a[200:203],a[32:35],v[16:19]
ds_read_u16_d16_hi v42,v42
v_lshlrev_b32_sdwa v41,1,v41 dst_sel:DWORD dst_unused:UNUSED_PAD src0_sel:DWORD src1_sel:WORD_1
v_mfma_f32_16x16x32_bf16 v[24:27],a[196:199],a[16:19],v[24:27]
s_waitcnt lgkmcnt(4)
ds_read_u16 v41,v41
v_lshl_add_u64 v[60:61],v[56:57],0,v[58:59]
v_mfma_f32_16x16x32_bf16 v[24:27],a[192:195],a[0:3],v[24:27]
TP3284_%=:
s_cbranch_execz TP5417_%=
v_lshlrev_b32_sdwa v43,1,v43 dst_sel:DWORD dst_unused:UNUSED_PAD src0_sel:DWORD src1_sel:WORD_1
v_lshlrev_b32_sdwa v36,1,v36 dst_sel:DWORD dst_unused:UNUSED_PAD src0_sel:DWORD src1_sel:WORD_1
v_mfma_f32_16x16x32_bf16 v[56:59],a[192:195],a[4:7],v[16:19]
s_waitcnt lgkmcnt(3)
ds_read_u16_d16_hi v43,v43
v_or_b32_e64 v62,v52,v54
v_mfma_f32_16x16x32_bf16 v[56:59],a[196:199],a[20:23],v[56:59]
s_waitcnt vmcnt(7)
ds_read_u16 v36,v36
v_cvt_pk_bf16_f32 v63,v53,v55
v_lshlrev_b32_sdwa v38,1,v38 dst_sel:DWORD dst_unused:UNUSED_PAD src0_sel:DWORD src1_sel:WORD_1
v_mfma_f32_16x16x32_bf16 v[56:59],a[200:203],a[36:39],v[56:59]
s_barrier
ds_read_u16_d16_hi v38,v38
v_lshlrev_b32_sdwa v37,1,v37 dst_sel:DWORD dst_unused:UNUSED_PAD src0_sel:DWORD src1_sel:WORD_1
v_mfma_f32_16x16x32_bf16 v[52:55],a[212:215],a[36:39],v[20:23]
s_add_u32 m0,s57,81328
ds_read_u16 v37,v37
v_lshlrev_b32_sdwa v39,1,v39 dst_sel:DWORD dst_unused:UNUSED_PAD src0_sel:DWORD src1_sel:WORD_1
v_mfma_f32_16x16x32_bf16 v[52:55],a[204:207],a[20:23],v[52:55]
s_add_u32 s89,s56,4718592
ds_read_u16_d16_hi v39,v39
v_lshlrev_b32_sdwa v44,1,v44 dst_sel:DWORD dst_unused:UNUSED_PAD src0_sel:DWORD src1_sel:WORD_1
v_mfma_f32_16x16x32_bf16 v[52:55],a[208:211],a[4:7],v[52:55]
buffer_load_dwordx4 v6,s[36:39],s89 offen lds
v_lshlrev_b32_sdwa v46,1,v46 dst_sel:DWORD dst_unused:UNUSED_PAD src0_sel:DWORD src1_sel:WORD_1
v_mfma_f32_16x16x32_bf16 v[232:235],a[236:239],v[32:35],v[232:235]
s_add_u32 m0,s58,6160
ds_read_u16 v44,v44
v_lshlrev_b32_sdwa v45,1,v45 dst_sel:DWORD dst_unused:UNUSED_PAD src0_sel:DWORD src1_sel:WORD_1
v_mfma_f32_16x16x32_bf16 v[236:239],a[236:239],v[60:63],v[236:239]
s_add_u32 s69,s69,s59
ds_read_u16_d16_hi v46,v46
v_lshlrev_b32_sdwa v47,1,v47 dst_sel:DWORD dst_unused:UNUSED_PAD src0_sel:DWORD src1_sel:WORD_1
v_mfma_f32_16x16x32_bf16 v[164:167],a[244:247],v[60:63],v[164:167]
buffer_load_dwordx4 v5,s[44:47],s69 offen offset:0 lds
ds_read_u16 v45,v45
v_mfma_f32_16x16x32_bf16 v[160:163],a[244:247],v[32:35],v[160:163]
ds_read_u16_d16_hi v47,v47
ds_read_b64_tr_b16 a[228:229],v3 offset:18480
v_mfma_f32_16x16x32_bf16 v[240:243],a[248:251],v[32:35],v[240:243]
s_add_u32 s69,s69,s53
ds_read_b64_tr_b16 a[230:231],v3 offset:20016
v_lshlrev_b32_sdwa v48,1,v48 dst_sel:DWORD dst_unused:UNUSED_PAD src0_sel:DWORD src1_sel:WORD_1
v_mfma_f32_16x16x32_bf16 v[244:247],a[248:251],v[60:63],v[244:247]
buffer_load_dwordx4 v5,s[44:47],s69 offen offset:1024 lds
v_lshlrev_b32_sdwa v50,1,v50 dst_sel:DWORD dst_unused:UNUSED_PAD src0_sel:DWORD src1_sel:WORD_1
v_mfma_f32_16x16x32_bf16 v[180:183],a[252:255],v[60:63],v[180:183]
ds_read_u16 v48,v48
v_lshlrev_b32_sdwa v49,1,v49 dst_sel:DWORD dst_unused:UNUSED_PAD src0_sel:DWORD src1_sel:WORD_1
v_mfma_f32_16x16x32_bf16 v[176:179],a[252:255],v[32:35],v[176:179]
ds_read_u16_d16_hi v50,v50
v_lshlrev_b32_sdwa v51,1,v51 dst_sel:DWORD dst_unused:UNUSED_PAD src0_sel:DWORD src1_sel:WORD_1
v_mfma_f32_16x16x32_bf16 v[248:251],a[240:243],v[32:35],v[248:251]
ds_read_u16 v49,v49
v_mfma_f32_16x16x32_bf16 v[252:255],a[240:243],v[60:63],v[252:255]
s_add_u32 s69,s69,s53
ds_read_u16_d16_hi v51,v51
v_mfma_f32_16x16x32_bf16 v[196:199],a[224:227],v[60:63],v[196:199]
buffer_load_dwordx4 v5,s[44:47],s69 offen offset:2048 lds
v_mfma_f32_16x16x32_bf16 v[192:195],a[224:227],v[32:35],v[192:195]
ds_read_b64_tr_b16 a[232:233],v3 offset:18488
v_mfma_f32_16x16x32_bf16 v[60:63],a[208:211],a[8:11],v[20:23]
ds_read_b64_tr_b16 a[234:235],v3 offset:20024
v_lshlrev_b32_sdwa v28,1,v28 dst_sel:DWORD dst_unused:UNUSED_PAD src0_sel:DWORD src1_sel:WORD_1
v_mfma_f32_16x16x32_bf16 v[60:63],a[204:207],a[24:27],v[60:63]
s_waitcnt lgkmcnt(6)
ds_read_u16 v28,v28
v_lshl_add_u64 v[40:41],v[40:41],0,v[42:43]
v_fma_mixlo_f16 v8,v57,s99,0
v_mfma_f32_16x16x32_bf16 v[60:63],a[212:215],a[40:43],v[60:63]
ds_read_b64_tr_b16 a[220:221],v1 offset:18480
v_lshl_add_u64 v[42:43],v[36:37],0,v[38:39]
v_lshlrev_b32_sdwa v30,1,v30 dst_sel:DWORD dst_unused:UNUSED_PAD src0_sel:DWORD src1_sel:WORD_1
v_mfma_f32_16x16x32_bf16 v[32:35],a[200:203],a[40:43],v[16:19]
ds_read_u16_d16_hi v30,v30
v_fma_mixhi_f16 v8,v59,s99,0
v_mfma_f32_16x16x32_bf16 v[32:35],a[196:199],a[24:27],v[32:35]
ds_read_b64_tr_b16 a[222:223],v1 offset:19504
v_lshl_add_u64 v[36:37],v[44:45],0,v[46:47]
v_lshlrev_b32_sdwa v29,1,v29 dst_sel:DWORD dst_unused:UNUSED_PAD src0_sel:DWORD src1_sel:WORD_1
v_pk_fma_f16 v9,v8,v8,s54
v_mfma_f32_16x16x32_bf16 v[32:35],a[192:195],a[8:11],v[32:35]
ds_read_u16 v29,v29
v_lshlrev_b32_sdwa v31,1,v31 dst_sel:DWORD dst_unused:UNUSED_PAD src0_sel:DWORD src1_sel:WORD_1
v_mfma_f32_16x16x32_bf16 v[44:47],a[192:195],a[12:15],v[16:19]
ds_read_u16_d16_hi v31,v31
v_pk_mul_f16 v9,v8,v9
v_mfma_f32_16x16x32_bf16 v[44:47],a[196:199],a[28:31],v[44:47]
s_waitcnt lgkmcnt(8)
ds_read_b64_tr_b16 a[216:217],v1 offset:20528
v_lshl_add_u64 v[38:39],v[48:49],0,v[50:51]
v_lshlrev_b32_sdwa v24,1,v24 dst_sel:DWORD dst_unused:UNUSED_PAD src0_sel:DWORD src1_sel:WORD_1
v_mfma_f32_16x16x32_bf16 v[44:47],a[200:203],a[44:47],v[44:47]
v_exp_f16 v8,v9
v_mfma_f32_16x16x32_bf16 v[48:51],a[212:215],a[44:47],v[20:23]
ds_read_u16 v24,v24
v_lshlrev_b32_sdwa v26,1,v26 dst_sel:DWORD dst_unused:UNUSED_PAD src0_sel:DWORD src1_sel:WORD_1
v_mfma_f32_16x16x32_bf16 v[48:51],a[204:207],a[28:31],v[48:51]
ds_read_u16_d16_hi v26,v26
v_lshlrev_b32_sdwa v25,1,v25 dst_sel:DWORD dst_unused:UNUSED_PAD src0_sel:DWORD src1_sel:WORD_1
v_mfma_f32_16x16x32_bf16 v[48:51],a[208:211],a[12:15],v[48:51]
v_exp_f16_sdwa v8,v9 dst_sel:WORD_1 dst_unused:UNUSED_PRESERVE src0_sel:WORD_1
v_mfma_f32_16x16x32_bf16 v[208:211],a[236:239],v[40:43],v[208:211]
ds_read_u16 v25,v25
v_lshlrev_b32_sdwa v27,1,v27 dst_sel:DWORD dst_unused:UNUSED_PAD src0_sel:DWORD src1_sel:WORD_1
v_mfma_f32_16x16x32_bf16 v[212:215],a[236:239],v[36:39],v[212:215]
ds_read_u16_d16_hi v27,v27
v_lshlrev_b32_sdwa v52,1,v52 dst_sel:DWORD dst_unused:UNUSED_PAD src0_sel:DWORD src1_sel:WORD_1
v_mfma_f32_16x16x32_bf16 v[172:175],a[244:247],v[36:39],v[172:175]
ds_read_u16 v52,v52
v_pk_fma_f16 v8,v8,s55,s55
v_mfma_f32_16x16x32_bf16 v[168:171],a[244:247],v[40:43],v[168:171]
ds_read_b64_tr_b16 a[236:237],v3 offset:18544
v_rcp_f16 v9,v8
v_lshlrev_b32_sdwa v54,1,v54 dst_sel:DWORD dst_unused:UNUSED_PAD src0_sel:DWORD src1_sel:WORD_1
v_mfma_f32_16x16x32_bf16 v[216:219],a[248:251],v[40:43],v[216:219]
ds_read_u16_d16_hi v54,v54
v_lshlrev_b32_sdwa v53,1,v53 dst_sel:DWORD dst_unused:UNUSED_PAD src0_sel:DWORD src1_sel:WORD_1
v_mfma_f32_16x16x32_bf16 v[220:223],a[248:251],v[36:39],v[220:223]
ds_read_u16 v53,v53
ds_read_b64_tr_b16 a[238:239],v3 offset:20080
v_mfma_f32_16x16x32_bf16 v[188:191],a[252:255],v[36:39],v[188:191]
ds_read_b64_tr_b16 a[244:245],v3 offset:18552
v_lshlrev_b32_sdwa v55,1,v55 dst_sel:DWORD dst_unused:UNUSED_PAD src0_sel:DWORD src1_sel:WORD_1
v_mfma_f32_16x16x32_bf16 v[184:187],a[252:255],v[40:43],v[184:187]
ds_read_u16_d16_hi v55,v55
v_rcp_f16_sdwa v9,v8 dst_sel:WORD_1 dst_unused:UNUSED_PRESERVE src0_sel:WORD_1
v_mfma_f32_16x16x32_bf16 v[224:227],a[240:243],v[40:43],v[224:227]
ds_read_b64_tr_b16 a[246:247],v3 offset:20088
ds_read_b64_tr_b16 a[248:249],v3 offset:18608
v_lshlrev_b32_sdwa v56,1,v56 dst_sel:DWORD dst_unused:UNUSED_PAD src0_sel:DWORD src1_sel:WORD_1
v_mfma_f32_16x16x32_bf16 v[228:231],a[240:243],v[36:39],v[228:231]
ds_read_u16 v56,v56
v_lshlrev_b32_sdwa v58,1,v58 dst_sel:DWORD dst_unused:UNUSED_PAD src0_sel:DWORD src1_sel:WORD_1
v_mfma_f32_16x16x32_bf16 v[204:207],a[224:227],v[36:39],v[204:207]
ds_read_u16_d16_hi v58,v58
v_fma_mix_f32 v57,v57,v9,0 op_sel_hi:[0,1,0]
v_mfma_f32_16x16x32_bf16 v[200:203],a[224:227],v[40:43],v[200:203]
ds_read_b64_tr_b16 a[250:251],v3 offset:20144
v_fma_mix_f32 v59,v59,v9,0 op_sel:[0,1,0] op_sel_hi:[0,1,0]
v_mfma_f32_16x16x32_bf16 v[36:39],a[208:211],a[48:51],v[20:23]
s_add_u32 m0,s58,12320
ds_read_b64 v[14:15],v7 offset:18480
v_lshlrev_b32_sdwa v60,1,v60 dst_sel:DWORD dst_unused:UNUSED_PAD src0_sel:DWORD src1_sel:WORD_1
v_mfma_f32_16x16x32_bf16 v[36:39],a[204:207],a[64:67],v[36:39]
s_waitcnt lgkmcnt(10)
ds_read_b64_tr_b16 a[252:253],v3 offset:18616
v_lshl_add_u64 v[28:29],v[28:29],0,v[30:31]
v_mfma_f32_16x16x32_bf16 v[36:39],a[212:215],a[80:83],v[36:39]
s_add_u32 s69,s69,s59
ds_read_b64_tr_b16 a[254:255],v3 offset:20152
v_lshl_add_u64 v[30:31],v[24:25],0,v[26:27]
v_lshlrev_b32_sdwa v62,1,v62 dst_sel:DWORD dst_unused:UNUSED_PAD src0_sel:DWORD src1_sel:WORD_1
v_mfma_f32_16x16x32_bf16 v[40:43],a[200:203],a[80:83],v[16:19]
buffer_load_dwordx4 v5,s[44:47],s69 offen offset:0 lds
v_lshlrev_b32_sdwa v61,1,v61 dst_sel:DWORD dst_unused:UNUSED_PAD src0_sel:DWORD src1_sel:WORD_1
v_mfma_f32_16x16x32_bf16 v[40:43],a[196:199],a[64:67],v[40:43]
s_waitcnt lgkmcnt(8)
ds_read_u16 v60,v60
v_lshl_add_u64 v[24:25],v[52:53],0,v[54:55]
v_mfma_f32_16x16x32_bf16 v[40:43],a[192:195],a[48:51],v[40:43]
s_waitcnt lgkmcnt(3)
ds_read_u16_d16_hi v62,v62
v_or3_b32 v13,v14,v15,v13
v_lshlrev_b32_sdwa v63,1,v63 dst_sel:DWORD dst_unused:UNUSED_PAD src0_sel:DWORD src1_sel:WORD_1
v_mfma_f32_16x16x32_bf16 v[52:55],a[192:195],a[52:55],v[16:19]
s_add_u32 s69,s69,s53
ds_read_b64 v[14:15],v7 offset:18992
v_or_b32 v26,v56,v58
v_lshlrev_b32_sdwa v32,1,v32 dst_sel:DWORD dst_unused:UNUSED_PAD src0_sel:DWORD src1_sel:WORD_1
v_mfma_f32_16x16x32_bf16 v[52:55],a[196:199],a[68:71],v[52:55]
buffer_load_dwordx4 v5,s[44:47],s69 offen offset:1024 lds
ds_read_u16 v61,v61
v_cvt_pk_bf16_f32 v27,v57,v59
v_lshlrev_b32_sdwa v34,1,v34 dst_sel:DWORD dst_unused:UNUSED_PAD src0_sel:DWORD src1_sel:WORD_1
v_mfma_f32_16x16x32_bf16 v[52:55],a[200:203],a[84:87],v[52:55]
s_add_u32 m0,s57,84864
ds_read_u16_d16_hi v63,v63
v_lshlrev_b32_sdwa v33,1,v33 dst_sel:DWORD dst_unused:UNUSED_PAD src0_sel:DWORD src1_sel:WORD_1
v_mfma_f32_16x16x32_bf16 v[56:59],a[212:215],a[84:87],v[20:23]
s_add_u32 s89,s56,4816896
ds_read_u16 v32,v32
v_lshlrev_b32_sdwa v35,1,v35 dst_sel:DWORD dst_unused:UNUSED_PAD src0_sel:DWORD src1_sel:WORD_1
v_mfma_f32_16x16x32_bf16 v[56:59],a[204:207],a[68:71],v[56:59]
buffer_load_dwordx4 v6,s[36:39],s89 offen lds
ds_read_u16_d16_hi v34,v34
v_mfma_f32_16x16x32_bf16 v[56:59],a[208:211],a[52:55],v[56:59]
s_waitcnt lgkmcnt(4)
ds_read_u16 v33,v33
v_or3_b32 v13,v14,v15,v13
v_mfma_f32_16x16x32_bf16 v[136:139],a[228:231],v[28:31],v[136:139]
s_add_u32 m0,s58,12320
ds_read_b64 v[14:15],v7 offset:19504
v_lshlrev_b32_sdwa v48,1,v48 dst_sel:DWORD dst_unused:UNUSED_PAD src0_sel:DWORD src1_sel:WORD_1
v_mfma_f32_16x16x32_bf16 v[140:143],a[228:231],v[24:27],v[140:143]
s_add_u32 s69,s69,s53
ds_read_u16_d16_hi v35,v35
v_lshlrev_b32_sdwa v50,1,v50 dst_sel:DWORD dst_unused:UNUSED_PAD src0_sel:DWORD src1_sel:WORD_1
v_mfma_f32_16x16x32_bf16 v[68:71],a[232:235],v[24:27],v[68:71]
buffer_load_dwordx4 v5,s[44:47],s69 offen offset:2048 lds
v_lshlrev_b32_sdwa v49,1,v49 dst_sel:DWORD dst_unused:UNUSED_PAD src0_sel:DWORD src1_sel:WORD_1
v_mfma_f32_16x16x32_bf16 v[64:67],a[232:235],v[28:31],v[64:67]
s_add_u32 m0,s57,8
ds_read_u16 v48,v48
v_lshlrev_b32_sdwa v51,1,v51 dst_sel:DWORD dst_unused:UNUSED_PAD src0_sel:DWORD src1_sel:WORD_1
v_mfma_f32_16x16x32_bf16 v[144:147],a[236:239],v[28:31],v[144:147]
s_add_u32 s89,s56,4915200
ds_read_u16_d16_hi v50,v50
v_lshlrev_b32_sdwa v44,1,v44 dst_sel:DWORD dst_unused:UNUSED_PAD src0_sel:DWORD src1_sel:WORD_1
v_mfma_f32_16x16x32_bf16 v[148:151],a[236:239],v[24:27],v[148:151]
s_waitcnt lgkmcnt(3)
buffer_load_dwordx4 v6,s[36:39],s89 offen lds
ds_read_u16 v49,v49
v_or3_b32 v13,v14,v15,v13
v_mfma_f32_16x16x32_bf16 v[84:87],a[244:247],v[24:27],v[84:87]
ds_read_u16_d16_hi v51,v51
v_lshlrev_b32_sdwa v46,1,v46 dst_sel:DWORD dst_unused:UNUSED_PAD src0_sel:DWORD src1_sel:WORD_1
v_mfma_f32_16x16x32_bf16 v[80:83],a[244:247],v[28:31],v[80:83]
ds_read_u16 v44,v44
v_lshlrev_b32_sdwa v45,1,v45 dst_sel:DWORD dst_unused:UNUSED_PAD src0_sel:DWORD src1_sel:WORD_1
v_mfma_f32_16x16x32_bf16 v[152:155],a[248:251],v[28:31],v[152:155]
ds_read_u16_d16_hi v46,v46
v_lshlrev_b32_sdwa v47,1,v47 dst_sel:DWORD dst_unused:UNUSED_PAD src0_sel:DWORD src1_sel:WORD_1
v_mfma_f32_16x16x32_bf16 v[156:159],a[248:251],v[24:27],v[156:159]
ds_read_u16 v45,v45
v_mfma_f32_16x16x32_bf16 v[100:103],a[252:255],v[24:27],v[100:103]
ds_read_u16_d16_hi v47,v47
v_mfma_f32_16x16x32_bf16 v[96:99],a[252:255],v[28:31],v[96:99]
ds_read_b64_tr_b16 a[218:219],v1 offset:21552
v_mfma_f32_16x16x32_bf16 v[24:27],a[208:211],a[56:59],v[20:23]
v_lshlrev_b32_sdwa v36,1,v36 dst_sel:DWORD dst_unused:UNUSED_PAD src0_sel:DWORD src1_sel:WORD_1
v_fma_mixlo_f16 v8,v53,s99,0
v_mfma_f32_16x16x32_bf16 v[24:27],a[204:207],a[72:75],v[24:27]
ds_read_u16 v36,v36
v_lshl_add_u64 v[60:61],v[60:61],0,v[62:63]
v_mfma_f32_16x16x32_bf16 v[24:27],a[212:215],a[88:91],v[24:27]
s_waitcnt lgkmcnt(7)
v_lshl_add_u64 v[62:63],v[32:33],0,v[34:35]
v_lshlrev_b32_sdwa v38,1,v38 dst_sel:DWORD dst_unused:UNUSED_PAD src0_sel:DWORD src1_sel:WORD_1
v_mfma_f32_16x16x32_bf16 v[28:31],a[200:203],a[88:91],v[16:19]
ds_read_u16_d16_hi v38,v38
v_fma_mixhi_f16 v8,v55,s99,0
v_mfma_f32_16x16x32_bf16 v[28:31],a[196:199],a[72:75],v[28:31]
s_waitcnt lgkmcnt(6)
v_lshl_add_u64 v[32:33],v[48:49],0,v[50:51]
v_lshlrev_b32_sdwa v37,1,v37 dst_sel:DWORD dst_unused:UNUSED_PAD src0_sel:DWORD src1_sel:WORD_1
v_pk_fma_f16 v9,v8,v8,s54
v_mfma_f32_16x16x32_bf16 v[28:31],a[192:195],a[56:59],v[28:31]
ds_read_u16 v37,v37
v_lshlrev_b32_sdwa v39,1,v39 dst_sel:DWORD dst_unused:UNUSED_PAD src0_sel:DWORD src1_sel:WORD_1
v_mfma_f32_16x16x32_bf16 v[48:51],a[192:195],a[60:63],v[16:19]
ds_read_b128 v[16:19],v4 offset:1024
v_pk_mul_f16 v9,v8,v9
v_mfma_f32_16x16x32_bf16 v[48:51],a[196:199],a[76:79],v[48:51]
s_waitcnt lgkmcnt(5)
ds_read_u16_d16_hi v39,v39
ds_read_b64_tr_b16 a[192:193],v1 offset:22576
v_lshl_add_u64 v[34:35],v[44:45],0,v[46:47]
v_lshlrev_b32_sdwa v40,1,v40 dst_sel:DWORD dst_unused:UNUSED_PAD src0_sel:DWORD src1_sel:WORD_1
v_mfma_f32_16x16x32_bf16 v[48:51],a[200:203],a[92:95],v[48:51]
ds_read_b64_tr_b16 a[194:195],v1 offset:23600
v_exp_f16_e64 v8,v9
v_mfma_f32_16x16x32_bf16 v[44:47],a[212:215],a[92:95],v[20:23]
ds_read_b128 v[20:23],v4 offset:1056
ds_read_u16 v40,v40
v_lshlrev_b32_sdwa v42,1,v42 dst_sel:DWORD dst_unused:UNUSED_PAD src0_sel:DWORD src1_sel:WORD_1
v_mfma_f32_16x16x32_bf16 v[44:47],a[204:207],a[76:79],v[44:47]
ds_read_u16_d16_hi v42,v42
ds_read_b64_tr_b16 a[196:197],v2 offset:22576
v_lshlrev_b32_sdwa v41,1,v41 dst_sel:DWORD dst_unused:UNUSED_PAD src0_sel:DWORD src1_sel:WORD_1
v_mfma_f32_16x16x32_bf16 v[44:47],a[208:211],a[60:63],v[44:47]
ds_read_u16 v41,v41
v_exp_f16_sdwa v8,v9 dst_sel:WORD_1 dst_unused:UNUSED_PRESERVE src0_sel:WORD_1
v_mfma_f32_16x16x32_bf16 v[112:115],a[228:231],v[60:63],v[112:115]
ds_read_b64_tr_b16 a[198:199],v2 offset:23600
ds_read_b64_tr_b16 a[200:201],v2 offset:20528
v_lshlrev_b32_sdwa v43,1,v43 dst_sel:DWORD dst_unused:UNUSED_PAD src0_sel:DWORD src1_sel:WORD_1
v_mfma_f32_16x16x32_bf16 v[116:119],a[228:231],v[32:35],v[116:119]
ds_read_u16_d16_hi v43,v43
v_lshlrev_b32_sdwa v56,1,v56 dst_sel:DWORD dst_unused:UNUSED_PAD src0_sel:DWORD src1_sel:WORD_1
v_mfma_f32_16x16x32_bf16 v[76:79],a[232:235],v[32:35],v[76:79]
ds_read_b64_tr_b16 a[202:203],v2 offset:21552
v_pk_fma_f16 v8,v8,s55,s55
v_mfma_f32_16x16x32_bf16 v[72:75],a[232:235],v[60:63],v[72:75]
v_rcp_f16 v9,v8
v_lshlrev_b32_sdwa v58,1,v58 dst_sel:DWORD dst_unused:UNUSED_PAD src0_sel:DWORD src1_sel:WORD_1
v_mfma_f32_16x16x32_bf16 v[120:123],a[236:239],v[60:63],v[120:123]
ds_read_u16 v56,v56
v_lshlrev_b32_sdwa v57,1,v57 dst_sel:DWORD dst_unused:UNUSED_PAD src0_sel:DWORD src1_sel:WORD_1
v_mfma_f32_16x16x32_bf16 v[124:127],a[236:239],v[32:35],v[124:127]
ds_read_u16_d16_hi v58,v58
v_lshlrev_b32_sdwa v59,1,v59 dst_sel:DWORD dst_unused:UNUSED_PAD src0_sel:DWORD src1_sel:WORD_1
v_mfma_f32_16x16x32_bf16 v[92:95],a[244:247],v[32:35],v[92:95]
ds_read_u16 v57,v57
v_lshlrev_b32_sdwa v52,1,v52 dst_sel:DWORD dst_unused:UNUSED_PAD src0_sel:DWORD src1_sel:WORD_1
v_mfma_f32_16x16x32_bf16 v[88:91],a[244:247],v[60:63],v[88:91]
ds_read_u16_d16_hi v59,v59
v_rcp_f16_sdwa v9,v8 dst_sel:WORD_1 dst_unused:UNUSED_PRESERVE src0_sel:WORD_1
v_mfma_f32_16x16x32_bf16 v[128:131],a[248:251],v[60:63],v[128:131]
ds_read_b64_tr_b16 a[212:213],v2 offset:18480
v_lshlrev_b32_sdwa v54,1,v54 dst_sel:DWORD dst_unused:UNUSED_PAD src0_sel:DWORD src1_sel:WORD_1
v_mfma_f32_16x16x32_bf16 v[132:135],a[248:251],v[32:35],v[132:135]
ds_read_b64_tr_b16 a[214:215],v2 offset:19504
v_fma_mix_f32 v53,v53,v9,0 op_sel_hi:[0,1,0]
v_mfma_f32_16x16x32_bf16 v[108:111],a[252:255],v[32:35],v[108:111]
ds_read_u16 v52,v52
v_fma_mix_f32 v55,v55,v9,0 op_sel:[0,1,0] op_sel_hi:[0,1,0]
v_mfma_f32_16x16x32_bf16 v[104:107],a[252:255],v[60:63],v[104:107]
s_waitcnt lgkmcnt(7)
ds_read_u16_d16_hi v54,v54
v_mfma_f32_16x16x32_bf16 v[32:35],a[220:223],a[0:3],v[16:19]
s_add_u32 m0,s57,3544
v_lshlrev_b32_sdwa v24,1,v24 dst_sel:DWORD dst_unused:UNUSED_PAD src0_sel:DWORD src1_sel:WORD_1
v_lshlrev_b32_sdwa v26,1,v26 dst_sel:DWORD dst_unused:UNUSED_PAD src0_sel:DWORD src1_sel:WORD_1
v_mfma_f32_16x16x32_bf16 v[32:35],a[216:219],a[16:19],v[32:35]
s_add_u32 s89,s56,5013504
ds_read_u16 v24,v24
v_lshl_add_u64 v[36:37],v[36:37],0,v[38:39]
v_mfma_f32_16x16x32_bf16 v[32:35],a[192:195],a[32:35],v[32:35]
buffer_load_dwordx4 v6,s[36:39],s89 offen lds
v_lshl_add_u64 v[38:39],v[40:41],0,v[42:43]
v_mfma_f32_16x16x32_bf16 v[60:63],a[196:199],a[32:35],v[20:23]
s_add_u32 m0,s57,7072
ds_read_u16_d16_hi v26,v26
v_lshlrev_b32_sdwa v25,1,v25 dst_sel:DWORD dst_unused:UNUSED_PAD src0_sel:DWORD src1_sel:WORD_1
v_mfma_f32_16x16x32_bf16 v[60:63],a[200:203],a[16:19],v[60:63]
s_waitcnt lgkmcnt(4)
ds_read_u16 v25,v25
v_lshl_add_u64 v[40:41],v[56:57],0,v[58:59]
v_mfma_f32_16x16x32_bf16 v[60:63],a[212:215],a[0:3],v[60:63]
s_add_u32 s89,s56,5111808
v_lshlrev_b32_sdwa v27,1,v27 dst_sel:DWORD dst_unused:UNUSED_PAD src0_sel:DWORD src1_sel:WORD_1
v_lshlrev_b32_sdwa v28,1,v28 dst_sel:DWORD dst_unused:UNUSED_PAD src0_sel:DWORD src1_sel:WORD_1
v_mfma_f32_16x16x32_bf16 v[56:59],a[212:215],a[4:7],v[20:23]
s_waitcnt lgkmcnt(3)
buffer_load_dwordx4 v6,s[36:39],s89 offen lds
v_or_b32 v42,v52,v54
v_mfma_f32_16x16x32_bf16 v[56:59],a[200:203],a[20:23],v[56:59]
ds_read_u16_d16_hi v27,v27
v_cvt_pk_bf16_f32 v43,v53,v55
v_lshlrev_b32_sdwa v30,1,v30 dst_sel:DWORD dst_unused:UNUSED_PAD src0_sel:DWORD src1_sel:WORD_1
v_mfma_f32_16x16x32_bf16 v[56:59],a[196:199],a[36:39],v[56:59]
ds_read_u16 v28,v28
v_lshlrev_b32_sdwa v29,1,v29 dst_sel:DWORD dst_unused:UNUSED_PAD src0_sel:DWORD src1_sel:WORD_1
v_mfma_f32_16x16x32_bf16 v[52:55],a[192:195],a[36:39],v[16:19]
ds_read_u16_d16_hi v30,v30
v_lshlrev_b32_sdwa v31,1,v31 dst_sel:DWORD dst_unused:UNUSED_PAD src0_sel:DWORD src1_sel:WORD_1
v_mfma_f32_16x16x32_bf16 v[52:55],a[216:219],a[20:23],v[52:55]
ds_read_u16 v29,v29
v_lshlrev_b32_sdwa v44,1,v44 dst_sel:DWORD dst_unused:UNUSED_PAD src0_sel:DWORD src1_sel:WORD_1
v_mfma_f32_16x16x32_bf16 v[52:55],a[220:223],a[4:7],v[52:55]
ds_read_u16_d16_hi v31,v31
v_lshlrev_b32_sdwa v46,1,v46 dst_sel:DWORD dst_unused:UNUSED_PAD src0_sel:DWORD src1_sel:WORD_1
v_mfma_f32_16x16x32_bf16 v[232:235],a[228:231],v[36:39],v[232:235]
ds_read_u16 v44,v44
v_lshlrev_b32_sdwa v45,1,v45 dst_sel:DWORD dst_unused:UNUSED_PAD src0_sel:DWORD src1_sel:WORD_1
v_mfma_f32_16x16x32_bf16 v[236:239],a[228:231],v[40:43],v[236:239]
ds_read_u16_d16_hi v46,v46
v_lshlrev_b32_sdwa v47,1,v47 dst_sel:DWORD dst_unused:UNUSED_PAD src0_sel:DWORD src1_sel:WORD_1
v_mfma_f32_16x16x32_bf16 v[164:167],a[232:235],v[40:43],v[164:167]
ds_read_u16 v45,v45
ds_read_u16_d16_hi v47,v47
v_mfma_f32_16x16x32_bf16 v[160:163],a[232:235],v[36:39],v[160:163]
ds_read_b64_tr_b16 a[240:241],v3 offset:0
v_lshlrev_b32_sdwa v48,1,v48 dst_sel:DWORD dst_unused:UNUSED_PAD src0_sel:DWORD src1_sel:WORD_1
v_mfma_f32_16x16x32_bf16 v[240:243],a[236:239],v[36:39],v[240:243]
ds_read_u16 v48,v48
v_lshlrev_b32_sdwa v50,1,v50 dst_sel:DWORD dst_unused:UNUSED_PAD src0_sel:DWORD src1_sel:WORD_1
v_mfma_f32_16x16x32_bf16 v[244:247],a[236:239],v[40:43],v[244:247]
ds_read_u16_d16_hi v50,v50
v_lshlrev_b32_sdwa v49,1,v49 dst_sel:DWORD dst_unused:UNUSED_PAD src0_sel:DWORD src1_sel:WORD_1
v_mfma_f32_16x16x32_bf16 v[180:183],a[244:247],v[40:43],v[180:183]
ds_read_u16 v49,v49
v_lshlrev_b32_sdwa v51,1,v51 dst_sel:DWORD dst_unused:UNUSED_PAD src0_sel:DWORD src1_sel:WORD_1
v_mfma_f32_16x16x32_bf16 v[176:179],a[244:247],v[36:39],v[176:179]
ds_read_u16_d16_hi v51,v51
v_mfma_f32_16x16x32_bf16 v[248:251],a[248:251],v[36:39],v[248:251]
ds_read_b64_tr_b16 a[242:243],v3 offset:1536
v_mfma_f32_16x16x32_bf16 v[252:255],a[248:251],v[40:43],v[252:255]
ds_read_b64_tr_b16 a[224:225],v3 offset:8
v_mfma_f32_16x16x32_bf16 v[196:199],a[252:255],v[40:43],v[196:199]
ds_read_b64_tr_b16 a[226:227],v3 offset:1544
v_mfma_f32_16x16x32_bf16 v[192:195],a[252:255],v[36:39],v[192:195]
v_mfma_f32_16x16x32_bf16 v[40:43],a[220:223],a[8:11],v[16:19]
ds_read_b64_tr_b16 a[204:205],v1 offset:0
v_lshlrev_b32_sdwa v32,1,v32 dst_sel:DWORD dst_unused:UNUSED_PAD src0_sel:DWORD src1_sel:WORD_1
v_mfma_f32_16x16x32_bf16 v[40:43],a[216:219],a[24:27],v[40:43]
s_waitcnt lgkmcnt(7)
ds_read_u16 v32,v32
v_lshl_add_u64 v[24:25],v[24:25],0,v[26:27]
v_fma_mixlo_f16 v8,v57,s99,0
v_mfma_f32_16x16x32_bf16 v[40:43],a[192:195],a[40:43],v[40:43]
ds_read_b64_tr_b16 a[206:207],v1 offset:1024
v_lshl_add_u64 v[26:27],v[28:29],0,v[30:31]
v_lshlrev_b32_sdwa v34,1,v34 dst_sel:DWORD dst_unused:UNUSED_PAD src0_sel:DWORD src1_sel:WORD_1
v_mfma_f32_16x16x32_bf16 v[36:39],a[196:199],a[40:43],v[20:23]
ds_read_u16_d16_hi v34,v34
v_fma_mixhi_f16 v8,v59,s99,0
v_mfma_f32_16x16x32_bf16 v[36:39],a[200:203],a[24:27],v[36:39]
ds_read_b64_tr_b16 a[208:209],v1 offset:2048
v_lshl_add_u64 v[28:29],v[44:45],0,v[46:47]
v_lshlrev_b32_sdwa v33,1,v33 dst_sel:DWORD dst_unused:UNUSED_PAD src0_sel:DWORD src1_sel:WORD_1
v_pk_fma_f16 v9,v8,v8,s54
v_mfma_f32_16x16x32_bf16 v[36:39],a[212:215],a[8:11],v[36:39]
ds_read_u16 v33,v33
v_lshlrev_b32_sdwa v35,1,v35 dst_sel:DWORD dst_unused:UNUSED_PAD src0_sel:DWORD src1_sel:WORD_1
v_mfma_f32_16x16x32_bf16 v[44:47],a[212:215],a[12:15],v[20:23]
ds_read_u16_d16_hi v35,v35
v_pk_mul_f16 v9,v8,v9
v_mfma_f32_16x16x32_bf16 v[44:47],a[200:203],a[28:31],v[44:47]
s_waitcnt lgkmcnt(7)
ds_read_b64_tr_b16 a[210:211],v1 offset:3072
v_lshl_add_u64 v[30:31],v[48:49],0,v[50:51]
v_lshlrev_b32_sdwa v60,1,v60 dst_sel:DWORD dst_unused:UNUSED_PAD src0_sel:DWORD src1_sel:WORD_1
v_mfma_f32_16x16x32_bf16 v[44:47],a[196:199],a[44:47],v[44:47]
v_exp_f16 v8,v9
v_mfma_f32_16x16x32_bf16 v[48:51],a[192:195],a[44:47],v[16:19]
ds_read_u16 v60,v60
v_lshlrev_b32_sdwa v62,1,v62 dst_sel:DWORD dst_unused:UNUSED_PAD src0_sel:DWORD src1_sel:WORD_1
v_mfma_f32_16x16x32_bf16 v[48:51],a[216:219],a[28:31],v[48:51]
ds_read_u16_d16_hi v62,v62
v_lshlrev_b32_sdwa v61,1,v61 dst_sel:DWORD dst_unused:UNUSED_PAD src0_sel:DWORD src1_sel:WORD_1
v_mfma_f32_16x16x32_bf16 v[48:51],a[220:223],a[12:15],v[48:51]
v_exp_f16_sdwa v8,v9 dst_sel:WORD_1 dst_unused:UNUSED_PRESERVE src0_sel:WORD_1
v_mfma_f32_16x16x32_bf16 v[208:211],a[228:231],v[24:27],v[208:211]
ds_read_u16 v61,v61
v_lshlrev_b32_sdwa v63,1,v63 dst_sel:DWORD dst_unused:UNUSED_PAD src0_sel:DWORD src1_sel:WORD_1
v_mfma_f32_16x16x32_bf16 v[212:215],a[228:231],v[28:31],v[212:215]
ds_read_u16_d16_hi v63,v63
v_lshlrev_b32_sdwa v52,1,v52 dst_sel:DWORD dst_unused:UNUSED_PAD src0_sel:DWORD src1_sel:WORD_1
v_mfma_f32_16x16x32_bf16 v[172:175],a[232:235],v[28:31],v[172:175]
ds_read_u16 v52,v52
v_pk_fma_f16 v8,v8,s55,s55
v_mfma_f32_16x16x32_bf16 v[168:171],a[232:235],v[24:27],v[168:171]
ds_read_b64_tr_b16 a[228:229],v3 offset:64
v_rcp_f16 v9,v8
v_lshlrev_b32_sdwa v54,1,v54 dst_sel:DWORD dst_unused:UNUSED_PAD src0_sel:DWORD src1_sel:WORD_1
v_mfma_f32_16x16x32_bf16 v[216:219],a[236:239],v[24:27],v[216:219]
ds_read_u16_d16_hi v54,v54
v_lshlrev_b32_sdwa v53,1,v53 dst_sel:DWORD dst_unused:UNUSED_PAD src0_sel:DWORD src1_sel:WORD_1
v_mfma_f32_16x16x32_bf16 v[220:223],a[236:239],v[28:31],v[220:223]
ds_read_u16 v53,v53
ds_read_b64_tr_b16 a[230:231],v3 offset:1600
v_mfma_f32_16x16x32_bf16 v[188:191],a[244:247],v[28:31],v[188:191]
ds_read_b64_tr_b16 a[232:233],v3 offset:72
v_lshlrev_b32_sdwa v55,1,v55 dst_sel:DWORD dst_unused:UNUSED_PAD src0_sel:DWORD src1_sel:WORD_1
v_mfma_f32_16x16x32_bf16 v[184:187],a[244:247],v[24:27],v[184:187]
ds_read_u16_d16_hi v55,v55
v_rcp_f16_sdwa v9,v8 dst_sel:WORD_1 dst_unused:UNUSED_PRESERVE src0_sel:WORD_1
v_mfma_f32_16x16x32_bf16 v[224:227],a[248:251],v[24:27],v[224:227]
ds_read_b64_tr_b16 a[234:235],v3 offset:1608
ds_read_b64_tr_b16 a[236:237],v3 offset:128
v_lshlrev_b32_sdwa v56,1,v56 dst_sel:DWORD dst_unused:UNUSED_PAD src0_sel:DWORD src1_sel:WORD_1
v_mfma_f32_16x16x32_bf16 v[228:231],a[248:251],v[28:31],v[228:231]
ds_read_u16 v56,v56
v_lshlrev_b32_sdwa v58,1,v58 dst_sel:DWORD dst_unused:UNUSED_PAD src0_sel:DWORD src1_sel:WORD_1
v_mfma_f32_16x16x32_bf16 v[204:207],a[252:255],v[28:31],v[204:207]
ds_read_u16_d16_hi v58,v58
v_fma_mix_f32 v57,v57,v9,0 op_sel_hi:[0,1,0]
v_mfma_f32_16x16x32_bf16 v[200:203],a[252:255],v[24:27],v[200:203]
ds_read_b64_tr_b16 a[238:239],v3 offset:1664
v_fma_mix_f32 v59,v59,v9,0 op_sel:[0,1,0] op_sel_hi:[0,1,0]
v_mfma_f32_16x16x32_bf16 v[28:31],a[220:223],a[48:51],v[16:19]
s_add_u32 m0,s57,10608
ds_read_b64 v[14:15],v7 offset:0
v_lshlrev_b32_sdwa v40,1,v40 dst_sel:DWORD dst_unused:UNUSED_PAD src0_sel:DWORD src1_sel:WORD_1
v_mfma_f32_16x16x32_bf16 v[28:31],a[216:219],a[64:67],v[28:31]
s_waitcnt lgkmcnt(10)
ds_read_b64_tr_b16 a[244:245],v3 offset:136
v_lshl_add_u64 v[32:33],v[32:33],0,v[34:35]
v_mfma_f32_16x16x32_bf16 v[28:31],a[192:195],a[80:83],v[28:31]
s_add_u32 s89,s56,5210112
ds_read_b64_tr_b16 a[246:247],v3 offset:1672
v_lshl_add_u64 v[34:35],v[60:61],0,v[62:63]
v_lshlrev_b32_sdwa v42,1,v42 dst_sel:DWORD dst_unused:UNUSED_PAD src0_sel:DWORD src1_sel:WORD_1
v_mfma_f32_16x16x32_bf16 v[24:27],a[196:199],a[80:83],v[20:23]
buffer_load_dwordx4 v6,s[36:39],s89 offen lds
v_lshlrev_b32_sdwa v41,1,v41 dst_sel:DWORD dst_unused:UNUSED_PAD src0_sel:DWORD src1_sel:WORD_1
v_mfma_f32_16x16x32_bf16 v[24:27],a[200:203],a[64:67],v[24:27]
s_waitcnt lgkmcnt(8)
ds_read_u16 v40,v40
v_lshl_add_u64 v[60:61],v[52:53],0,v[54:55]
v_mfma_f32_16x16x32_bf16 v[24:27],a[212:215],a[48:51],v[24:27]
s_waitcnt lgkmcnt(3)
ds_read_u16_d16_hi v42,v42
v_or3_b32 v13,v14,v15,v13
v_lshlrev_b32_sdwa v43,1,v43 dst_sel:DWORD dst_unused:UNUSED_PAD src0_sel:DWORD src1_sel:WORD_1
v_mfma_f32_16x16x32_bf16 v[52:55],a[212:215],a[52:55],v[20:23]
s_add_u32 m0,s57,14152
ds_read_b64 v[14:15],v7 offset:512
v_or_b32 v62,v56,v58
v_lshlrev_b32_sdwa v36,1,v36 dst_sel:DWORD dst_unused:UNUSED_PAD src0_sel:DWORD src1_sel:WORD_1
v_mfma_f32_16x16x32_bf16 v[52:55],a[200:203],a[68:71],v[52:55]
s_add_u32 s89,s56,5308416
ds_read_u16 v41,v41
v_cvt_pk_bf16_f32 v63,v57,v59
v_lshlrev_b32_sdwa v38,1,v38 dst_sel:DWORD dst_unused:UNUSED_PAD src0_sel:DWORD src1_sel:WORD_1
v_mfma_f32_16x16x32_bf16 v[52:55],a[196:199],a[84:87],v[52:55]
buffer_load_dwordx4 v6,s[36:39],s89 offen lds
v_lshlrev_b32_sdwa v37,1,v37 dst_sel:DWORD dst_unused:UNUSED_PAD src0_sel:DWORD src1_sel:WORD_1
v_mfma_f32_16x16x32_bf16 v[56:59],a[192:195],a[84:87],v[16:19]
ds_read_u16_d16_hi v43,v43
v_lshlrev_b32_sdwa v39,1,v39 dst_sel:DWORD dst_unused:UNUSED_PAD src0_sel:DWORD src1_sel:WORD_1
v_mfma_f32_16x16x32_bf16 v[56:59],a[216:219],a[68:71],v[56:59]
ds_read_u16 v36,v36
v_lshlrev_b32_sdwa v48,1,v48 dst_sel:DWORD dst_unused:UNUSED_PAD src0_sel:DWORD src1_sel:WORD_1
v_mfma_f32_16x16x32_bf16 v[56:59],a[220:223],a[52:55],v[56:59]
s_waitcnt lgkmcnt(3)
ds_read_u16_d16_hi v38,v38
v_or3_b32 v13,v14,v15,v13
v_mfma_f32_16x16x32_bf16 v[136:139],a[240:243],v[32:35],v[136:139]
ds_read_b64 v[14:15],v7 offset:1024
v_lshlrev_b32_sdwa v50,1,v50 dst_sel:DWORD dst_unused:UNUSED_PAD src0_sel:DWORD src1_sel:WORD_1
v_mfma_f32_16x16x32_bf16 v[140:143],a[240:243],v[60:63],v[140:143]
ds_read_u16 v37,v37
v_lshlrev_b32_sdwa v49,1,v49 dst_sel:DWORD dst_unused:UNUSED_PAD src0_sel:DWORD src1_sel:WORD_1
v_mfma_f32_16x16x32_bf16 v[68:71],a[224:227],v[60:63],v[68:71]
ds_read_u16_d16_hi v39,v39
v_lshlrev_b32_sdwa v51,1,v51 dst_sel:DWORD dst_unused:UNUSED_PAD src0_sel:DWORD src1_sel:WORD_1
v_mfma_f32_16x16x32_bf16 v[64:67],a[224:227],v[32:35],v[64:67]
ds_read_u16 v48,v48
v_lshlrev_b32_sdwa v44,1,v44 dst_sel:DWORD dst_unused:UNUSED_PAD src0_sel:DWORD src1_sel:WORD_1
v_mfma_f32_16x16x32_bf16 v[144:147],a[228:231],v[32:35],v[144:147]
ds_read_u16_d16_hi v50,v50
v_lshlrev_b32_sdwa v46,1,v46 dst_sel:DWORD dst_unused:UNUSED_PAD src0_sel:DWORD src1_sel:WORD_1
v_mfma_f32_16x16x32_bf16 v[148:151],a[228:231],v[60:63],v[148:151]
TP3811_%=:
s_cbranch_execz TP5957_%=
s_waitcnt lgkmcnt(4)
ds_read_u16 v49,v49
v_or3_b32 v13,v14,v15,v13
v_mfma_f32_16x16x32_bf16 v[84:87],a[232:235],v[60:63],v[84:87]
s_add_u32 m0,s57,17688
ds_read_u16_d16_hi v51,v51
v_lshlrev_b32_sdwa v45,1,v45 dst_sel:DWORD dst_unused:UNUSED_PAD src0_sel:DWORD src1_sel:WORD_1
v_mfma_f32_16x16x32_bf16 v[80:83],a[232:235],v[32:35],v[80:83]
s_add_u32 s89,s56,5406720
ds_read_u16 v44,v44
v_lshlrev_b32_sdwa v47,1,v47 dst_sel:DWORD dst_unused:UNUSED_PAD src0_sel:DWORD src1_sel:WORD_1
v_mfma_f32_16x16x32_bf16 v[152:155],a[236:239],v[32:35],v[152:155]
buffer_load_dwordx4 v6,s[36:39],s89 offen lds
v_mfma_f32_16x16x32_bf16 v[156:159],a[236:239],v[60:63],v[156:159]
ds_read_u16_d16_hi v46,v46
v_mfma_f32_16x16x32_bf16 v[100:103],a[244:247],v[60:63],v[100:103]
ds_read_u16 v45,v45
v_mfma_f32_16x16x32_bf16 v[96:99],a[244:247],v[32:35],v[96:99]
ds_read_u16_d16_hi v47,v47
v_mfma_f32_16x16x32_bf16 v[60:63],a[220:223],a[56:59],v[16:19]
v_lshlrev_b32_sdwa v28,1,v28 dst_sel:DWORD dst_unused:UNUSED_PAD src0_sel:DWORD src1_sel:WORD_1
v_fma_mixlo_f16 v8,v53,s99,0
v_mfma_f32_16x16x32_bf16 v[60:63],a[216:219],a[72:75],v[60:63]
ds_read_u16 v28,v28
v_lshl_add_u64 v[40:41],v[40:41],0,v[42:43]
v_mfma_f32_16x16x32_bf16 v[60:63],a[192:195],a[88:91],v[60:63]
s_waitcnt lgkmcnt(6)
v_lshl_add_u64 v[42:43],v[36:37],0,v[38:39]
v_lshlrev_b32_sdwa v30,1,v30 dst_sel:DWORD dst_unused:UNUSED_PAD src0_sel:DWORD src1_sel:WORD_1
v_mfma_f32_16x16x32_bf16 v[32:35],a[196:199],a[88:91],v[20:23]
ds_read_u16_d16_hi v30,v30
v_fma_mixhi_f16 v8,v55,s99,0
v_mfma_f32_16x16x32_bf16 v[32:35],a[200:203],a[72:75],v[32:35]
s_waitcnt lgkmcnt(5)
v_lshl_add_u64 v[36:37],v[48:49],0,v[50:51]
v_lshlrev_b32_sdwa v29,1,v29 dst_sel:DWORD dst_unused:UNUSED_PAD src0_sel:DWORD src1_sel:WORD_1
v_pk_fma_f16 v9,v8,v8,s54
v_mfma_f32_16x16x32_bf16 v[32:35],a[212:215],a[56:59],v[32:35]
ds_read_u16 v29,v29
v_lshlrev_b32_sdwa v31,1,v31 dst_sel:DWORD dst_unused:UNUSED_PAD src0_sel:DWORD src1_sel:WORD_1
v_mfma_f32_16x16x32_bf16 v[48:51],a[212:215],a[60:63],v[20:23]
s_nop 0
ds_read_b128 v[20:23],v4 offset:1152
v_pk_mul_f16 v9,v8,v9
v_mfma_f32_16x16x32_bf16 v[48:51],a[200:203],a[76:79],v[48:51]
s_waitcnt lgkmcnt(4)
ds_read_u16_d16_hi v31,v31
ds_read_b64_tr_b16 a[212:213],v1 offset:4096
v_lshl_add_u64 v[38:39],v[44:45],0,v[46:47]
v_lshlrev_b32_sdwa v24,1,v24 dst_sel:DWORD dst_unused:UNUSED_PAD src0_sel:DWORD src1_sel:WORD_1
v_mfma_f32_16x16x32_bf16 v[48:51],a[196:199],a[92:95],v[48:51]
ds_read_b64_tr_b16 a[214:215],v1 offset:5120
v_exp_f16 v8,v9
v_mfma_f32_16x16x32_bf16 v[44:47],a[192:195],a[92:95],v[16:19]
ds_read_b128 v[16:19],v4 offset:1184
ds_read_u16 v24,v24
v_lshlrev_b32_sdwa v26,1,v26 dst_sel:DWORD dst_unused:UNUSED_PAD src0_sel:DWORD src1_sel:WORD_1
v_mfma_f32_16x16x32_bf16 v[44:47],a[216:219],a[76:79],v[44:47]
ds_read_u16_d16_hi v26,v26
ds_read_b64_tr_b16 a[200:201],v2 offset:4096
v_lshlrev_b32_sdwa v25,1,v25 dst_sel:DWORD dst_unused:UNUSED_PAD src0_sel:DWORD src1_sel:WORD_1
v_mfma_f32_16x16x32_bf16 v[44:47],a[220:223],a[60:63],v[44:47]
ds_read_u16 v25,v25
v_exp_f16_sdwa v8,v9 dst_sel:WORD_1 dst_unused:UNUSED_PRESERVE src0_sel:WORD_1
v_mfma_f32_16x16x32_bf16 v[112:115],a[240:243],v[40:43],v[112:115]
ds_read_b64_tr_b16 a[202:203],v2 offset:5120
ds_read_b64_tr_b16 a[196:197],v2 offset:2048
v_lshlrev_b32_sdwa v27,1,v27 dst_sel:DWORD dst_unused:UNUSED_PAD src0_sel:DWORD src1_sel:WORD_1
v_mfma_f32_16x16x32_bf16 v[116:119],a[240:243],v[36:39],v[116:119]
ds_read_u16_d16_hi v27,v27
v_lshlrev_b32_sdwa v56,1,v56 dst_sel:DWORD dst_unused:UNUSED_PAD src0_sel:DWORD src1_sel:WORD_1
v_mfma_f32_16x16x32_bf16 v[76:79],a[224:227],v[36:39],v[76:79]
ds_read_b64_tr_b16 a[198:199],v2 offset:3072
v_pk_fma_f16 v8,v8,s55,s55
v_mfma_f32_16x16x32_bf16 v[72:75],a[224:227],v[40:43],v[72:75]
v_rcp_f16 v9,v8
v_lshlrev_b32_sdwa v58,1,v58 dst_sel:DWORD dst_unused:UNUSED_PAD src0_sel:DWORD src1_sel:WORD_1
v_mfma_f32_16x16x32_bf16 v[120:123],a[228:231],v[40:43],v[120:123]
ds_read_u16 v56,v56
v_lshlrev_b32_sdwa v57,1,v57 dst_sel:DWORD dst_unused:UNUSED_PAD src0_sel:DWORD src1_sel:WORD_1
v_mfma_f32_16x16x32_bf16 v[124:127],a[228:231],v[36:39],v[124:127]
ds_read_u16_d16_hi v58,v58
v_lshlrev_b32_sdwa v59,1,v59 dst_sel:DWORD dst_unused:UNUSED_PAD src0_sel:DWORD src1_sel:WORD_1
v_mfma_f32_16x16x32_bf16 v[92:95],a[232:235],v[36:39],v[92:95]
ds_read_u16 v57,v57
v_lshlrev_b32_sdwa v52,1,v52 dst_sel:DWORD dst_unused:UNUSED_PAD src0_sel:DWORD src1_sel:WORD_1
v_mfma_f32_16x16x32_bf16 v[88:91],a[232:235],v[40:43],v[88:91]
ds_read_u16_d16_hi v59,v59
v_rcp_f16_sdwa v9,v8 dst_sel:WORD_1 dst_unused:UNUSED_PRESERVE src0_sel:WORD_1
v_mfma_f32_16x16x32_bf16 v[128:131],a[236:239],v[40:43],v[128:131]
ds_read_b64_tr_b16 a[192:193],v2 offset:0
v_lshlrev_b32_sdwa v54,1,v54 dst_sel:DWORD dst_unused:UNUSED_PAD src0_sel:DWORD src1_sel:WORD_1
v_mfma_f32_16x16x32_bf16 v[132:135],a[236:239],v[36:39],v[132:135]
ds_read_b64_tr_b16 a[194:195],v2 offset:1024
v_fma_mix_f32 v53,v53,v9,0 op_sel_hi:[0,1,0]
v_mfma_f32_16x16x32_bf16 v[108:111],a[244:247],v[36:39],v[108:111]
ds_read_u16 v52,v52
v_fma_mix_f32 v55,v55,v9,0 op_sel:[0,1,0] op_sel_hi:[0,1,0]
v_mfma_f32_16x16x32_bf16 v[104:107],a[244:247],v[40:43],v[104:107]
s_waitcnt lgkmcnt(7)
ds_read_u16_d16_hi v54,v54
v_mfma_f32_16x16x32_bf16 v[36:39],a[204:207],a[0:3],v[20:23]
s_add_u32 m0,s57,21216
v_lshlrev_b32_sdwa v60,1,v60 dst_sel:DWORD dst_unused:UNUSED_PAD src0_sel:DWORD src1_sel:WORD_1
v_lshlrev_b32_sdwa v62,1,v62 dst_sel:DWORD dst_unused:UNUSED_PAD src0_sel:DWORD src1_sel:WORD_1
v_mfma_f32_16x16x32_bf16 v[36:39],a[208:211],a[16:19],v[36:39]
s_add_u32 s89,s56,5505024
ds_read_u16 v60,v60
v_lshl_add_u64 v[28:29],v[28:29],0,v[30:31]
v_mfma_f32_16x16x32_bf16 v[36:39],a[212:215],a[32:35],v[36:39]
buffer_load_dwordx4 v6,s[36:39],s89 offen lds
v_lshl_add_u64 v[30:31],v[24:25],0,v[26:27]
v_mfma_f32_16x16x32_bf16 v[40:43],a[200:203],a[32:35],v[16:19]
ds_read_u16_d16_hi v62,v62
v_lshlrev_b32_sdwa v61,1,v61 dst_sel:DWORD dst_unused:UNUSED_PAD src0_sel:DWORD src1_sel:WORD_1
v_mfma_f32_16x16x32_bf16 v[40:43],a[196:199],a[16:19],v[40:43]
s_waitcnt lgkmcnt(4)
ds_read_u16 v61,v61
v_lshl_add_u64 v[24:25],v[56:57],0,v[58:59]
v_mfma_f32_16x16x32_bf16 v[40:43],a[192:195],a[0:3],v[40:43]
v_lshlrev_b32_sdwa v63,1,v63 dst_sel:DWORD dst_unused:UNUSED_PAD src0_sel:DWORD src1_sel:WORD_1
v_lshlrev_b32_sdwa v32,1,v32 dst_sel:DWORD dst_unused:UNUSED_PAD src0_sel:DWORD src1_sel:WORD_1
v_mfma_f32_16x16x32_bf16 v[56:59],a[192:195],a[4:7],v[16:19]
s_waitcnt lgkmcnt(3)
ds_read_u16_d16_hi v63,v63
v_or_b32 v26,v52,v54
v_mfma_f32_16x16x32_bf16 v[56:59],a[196:199],a[20:23],v[56:59]
s_waitcnt vmcnt(7)
ds_read_u16 v32,v32
v_cvt_pk_bf16_f32 v27,v53,v55
v_lshlrev_b32_sdwa v34,1,v34 dst_sel:DWORD dst_unused:UNUSED_PAD src0_sel:DWORD src1_sel:WORD_1
v_mfma_f32_16x16x32_bf16 v[56:59],a[200:203],a[36:39],v[56:59]
s_barrier
ds_read_u16_d16_hi v34,v34
v_lshlrev_b32_sdwa v33,1,v33 dst_sel:DWORD dst_unused:UNUSED_PAD src0_sel:DWORD src1_sel:WORD_1
v_mfma_f32_16x16x32_bf16 v[52:55],a[212:215],a[36:39],v[20:23]
ds_read_u16 v33,v33
v_lshlrev_b32_sdwa v35,1,v35 dst_sel:DWORD dst_unused:UNUSED_PAD src0_sel:DWORD src1_sel:WORD_1
v_mfma_f32_16x16x32_bf16 v[52:55],a[208:211],a[20:23],v[52:55]
s_add_u32 m0,s58,18480
ds_read_u16_d16_hi v35,v35
v_lshlrev_b32_sdwa v44,1,v44 dst_sel:DWORD dst_unused:UNUSED_PAD src0_sel:DWORD src1_sel:WORD_1
v_mfma_f32_16x16x32_bf16 v[52:55],a[204:207],a[4:7],v[52:55]
s_mov_b32 s69,s88
ds_read_u16 v44,v44
v_lshlrev_b32_sdwa v46,1,v46 dst_sel:DWORD dst_unused:UNUSED_PAD src0_sel:DWORD src1_sel:WORD_1
v_mfma_f32_16x16x32_bf16 v[232:235],a[240:243],v[28:31],v[232:235]
buffer_load_dwordx4 v5,s[44:47],s69 offen offset:0 lds
v_lshlrev_b32_sdwa v45,1,v45 dst_sel:DWORD dst_unused:UNUSED_PAD src0_sel:DWORD src1_sel:WORD_1
v_mfma_f32_16x16x32_bf16 v[236:239],a[240:243],v[24:27],v[236:239]
ds_read_u16_d16_hi v46,v46
v_lshlrev_b32_sdwa v47,1,v47 dst_sel:DWORD dst_unused:UNUSED_PAD src0_sel:DWORD src1_sel:WORD_1
v_mfma_f32_16x16x32_bf16 v[164:167],a[224:227],v[24:27],v[164:167]
ds_read_u16 v45,v45
ds_read_u16_d16_hi v47,v47
v_mfma_f32_16x16x32_bf16 v[160:163],a[224:227],v[28:31],v[160:163]
ds_read_b64_tr_b16 a[248:249],v3 offset:6160
v_lshlrev_b32_sdwa v48,1,v48 dst_sel:DWORD dst_unused:UNUSED_PAD src0_sel:DWORD src1_sel:WORD_1
v_mfma_f32_16x16x32_bf16 v[240:243],a[228:231],v[28:31],v[240:243]
s_add_u32 s69,s69,s53
ds_read_u16 v48,v48
v_lshlrev_b32_sdwa v50,1,v50 dst_sel:DWORD dst_unused:UNUSED_PAD src0_sel:DWORD src1_sel:WORD_1
v_mfma_f32_16x16x32_bf16 v[244:247],a[228:231],v[24:27],v[244:247]
buffer_load_dwordx4 v5,s[44:47],s69 offen offset:1024 lds
ds_read_u16_d16_hi v50,v50
v_mfma_f32_16x16x32_bf16 v[180:183],a[232:235],v[24:27],v[180:183]
ds_read_b64_tr_b16 a[250:251],v3 offset:7696
v_lshlrev_b32_sdwa v49,1,v49 dst_sel:DWORD dst_unused:UNUSED_PAD src0_sel:DWORD src1_sel:WORD_1
v_mfma_f32_16x16x32_bf16 v[176:179],a[232:235],v[28:31],v[176:179]
ds_read_u16 v49,v49
v_lshlrev_b32_sdwa v51,1,v51 dst_sel:DWORD dst_unused:UNUSED_PAD src0_sel:DWORD src1_sel:WORD_1
v_mfma_f32_16x16x32_bf16 v[248:251],a[236:239],v[28:31],v[248:251]
ds_read_u16_d16_hi v51,v51
v_mfma_f32_16x16x32_bf16 v[252:255],a[236:239],v[24:27],v[252:255]
s_add_u32 s69,s69,s53
ds_read_b64_tr_b16 a[252:253],v3 offset:6168
v_mfma_f32_16x16x32_bf16 v[196:199],a[244:247],v[24:27],v[196:199]
buffer_load_dwordx4 v5,s[44:47],s69 offen offset:2048 lds
v_mfma_f32_16x16x32_bf16 v[192:195],a[244:247],v[28:31],v[192:195]
ds_read_b64_tr_b16 a[254:255],v3 offset:7704
v_mfma_f32_16x16x32_bf16 v[24:27],a[204:207],a[8:11],v[20:23]
ds_read_b64_tr_b16 a[216:217],v1 offset:6160
v_lshlrev_b32_sdwa v36,1,v36 dst_sel:DWORD dst_unused:UNUSED_PAD src0_sel:DWORD src1_sel:WORD_1
v_mfma_f32_16x16x32_bf16 v[24:27],a[208:211],a[24:27],v[24:27]
s_waitcnt lgkmcnt(7)
ds_read_u16 v36,v36
v_lshl_add_u64 v[60:61],v[60:61],0,v[62:63]
v_fma_mixlo_f16 v8,v57,s99,0
v_mfma_f32_16x16x32_bf16 v[24:27],a[212:215],a[40:43],v[24:27]
ds_read_b64_tr_b16 a[218:219],v1 offset:7184
v_lshl_add_u64 v[62:63],v[32:33],0,v[34:35]
v_lshlrev_b32_sdwa v38,1,v38 dst_sel:DWORD dst_unused:UNUSED_PAD src0_sel:DWORD src1_sel:WORD_1
v_mfma_f32_16x16x32_bf16 v[28:31],a[200:203],a[40:43],v[16:19]
ds_read_u16_d16_hi v38,v38
v_fma_mixhi_f16 v8,v59,s99,0
v_mfma_f32_16x16x32_bf16 v[28:31],a[196:199],a[24:27],v[28:31]
ds_read_b64_tr_b16 a[220:221],v1 offset:8208
v_lshl_add_u64 v[32:33],v[44:45],0,v[46:47]
v_lshlrev_b32_sdwa v37,1,v37 dst_sel:DWORD dst_unused:UNUSED_PAD src0_sel:DWORD src1_sel:WORD_1
v_pk_fma_f16 v9,v8,v8,s54
v_mfma_f32_16x16x32_bf16 v[28:31],a[192:195],a[8:11],v[28:31]
ds_read_u16 v37,v37
v_lshlrev_b32_sdwa v39,1,v39 dst_sel:DWORD dst_unused:UNUSED_PAD src0_sel:DWORD src1_sel:WORD_1
v_mfma_f32_16x16x32_bf16 v[44:47],a[192:195],a[12:15],v[16:19]
ds_read_u16_d16_hi v39,v39
v_pk_mul_f16 v9,v8,v9
v_mfma_f32_16x16x32_bf16 v[44:47],a[196:199],a[28:31],v[44:47]
s_waitcnt lgkmcnt(8)
ds_read_b64_tr_b16 a[222:223],v1 offset:9232
v_lshl_add_u64 v[34:35],v[48:49],0,v[50:51]
v_lshlrev_b32_sdwa v40,1,v40 dst_sel:DWORD dst_unused:UNUSED_PAD src0_sel:DWORD src1_sel:WORD_1
v_mfma_f32_16x16x32_bf16 v[44:47],a[200:203],a[44:47],v[44:47]
v_exp_f16 v8,v9
v_mfma_f32_16x16x32_bf16 v[48:51],a[212:215],a[44:47],v[20:23]
ds_read_u16 v40,v40
v_lshlrev_b32_sdwa v42,1,v42 dst_sel:DWORD dst_unused:UNUSED_PAD src0_sel:DWORD src1_sel:WORD_1
v_mfma_f32_16x16x32_bf16 v[48:51],a[208:211],a[28:31],v[48:51]
ds_read_u16_d16_hi v42,v42
v_lshlrev_b32_sdwa v41,1,v41 dst_sel:DWORD dst_unused:UNUSED_PAD src0_sel:DWORD src1_sel:WORD_1
v_mfma_f32_16x16x32_bf16 v[48:51],a[204:207],a[12:15],v[48:51]
v_exp_f16_sdwa v8,v9 dst_sel:WORD_1 dst_unused:UNUSED_PRESERVE src0_sel:WORD_1
v_mfma_f32_16x16x32_bf16 v[208:211],a[240:243],v[60:63],v[208:211]
ds_read_u16 v41,v41
v_lshlrev_b32_sdwa v43,1,v43 dst_sel:DWORD dst_unused:UNUSED_PAD src0_sel:DWORD src1_sel:WORD_1
v_mfma_f32_16x16x32_bf16 v[212:215],a[240:243],v[32:35],v[212:215]
ds_read_u16_d16_hi v43,v43
v_lshlrev_b32_sdwa v52,1,v52 dst_sel:DWORD dst_unused:UNUSED_PAD src0_sel:DWORD src1_sel:WORD_1
v_mfma_f32_16x16x32_bf16 v[172:175],a[224:227],v[32:35],v[172:175]
ds_read_u16 v52,v52
v_pk_fma_f16 v8,v8,s55,s55
v_mfma_f32_16x16x32_bf16 v[168:171],a[224:227],v[60:63],v[168:171]
ds_read_b64_tr_b16 a[240:241],v3 offset:6224
v_rcp_f16 v9,v8
v_lshlrev_b32_sdwa v54,1,v54 dst_sel:DWORD dst_unused:UNUSED_PAD src0_sel:DWORD src1_sel:WORD_1
v_mfma_f32_16x16x32_bf16 v[216:219],a[228:231],v[60:63],v[216:219]
ds_read_u16_d16_hi v54,v54
v_lshlrev_b32_sdwa v53,1,v53 dst_sel:DWORD dst_unused:UNUSED_PAD src0_sel:DWORD src1_sel:WORD_1
v_mfma_f32_16x16x32_bf16 v[220:223],a[228:231],v[32:35],v[220:223]
ds_read_u16 v53,v53
ds_read_b64_tr_b16 a[242:243],v3 offset:7760
v_mfma_f32_16x16x32_bf16 v[188:191],a[232:235],v[32:35],v[188:191]
ds_read_b64_tr_b16 a[224:225],v3 offset:6232
v_lshlrev_b32_sdwa v55,1,v55 dst_sel:DWORD dst_unused:UNUSED_PAD src0_sel:DWORD src1_sel:WORD_1
v_mfma_f32_16x16x32_bf16 v[184:187],a[232:235],v[60:63],v[184:187]
ds_read_u16_d16_hi v55,v55
v_rcp_f16_sdwa v9,v8 dst_sel:WORD_1 dst_unused:UNUSED_PRESERVE src0_sel:WORD_1
v_mfma_f32_16x16x32_bf16 v[224:227],a[236:239],v[60:63],v[224:227]
ds_read_b64_tr_b16 a[226:227],v3 offset:7768
ds_read_b64_tr_b16 a[228:229],v3 offset:6288
v_lshlrev_b32_sdwa v56,1,v56 dst_sel:DWORD dst_unused:UNUSED_PAD src0_sel:DWORD src1_sel:WORD_1
v_mfma_f32_16x16x32_bf16 v[228:231],a[236:239],v[32:35],v[228:231]
ds_read_u16 v56,v56
v_lshlrev_b32_sdwa v58,1,v58 dst_sel:DWORD dst_unused:UNUSED_PAD src0_sel:DWORD src1_sel:WORD_1
v_mfma_f32_16x16x32_bf16 v[204:207],a[244:247],v[32:35],v[204:207]
ds_read_u16_d16_hi v58,v58
v_fma_mix_f32 v57,v57,v9,0 op_sel_hi:[0,1,0]
v_mfma_f32_16x16x32_bf16 v[200:203],a[244:247],v[60:63],v[200:203]
ds_read_b64_tr_b16 a[230:231],v3 offset:7824
v_fma_mix_f32 v59,v59,v9,0 op_sel:[0,1,0] op_sel_hi:[0,1,0]
v_mfma_f32_16x16x32_bf16 v[32:35],a[204:207],a[48:51],v[20:23]
s_add_u32 m0,s58,0
ds_read_b64 v[14:15],v7 offset:6160
v_lshlrev_b32_sdwa v24,1,v24 dst_sel:DWORD dst_unused:UNUSED_PAD src0_sel:DWORD src1_sel:WORD_1
v_mfma_f32_16x16x32_bf16 v[32:35],a[208:211],a[64:67],v[32:35]
s_waitcnt lgkmcnt(10)
ds_read_b64_tr_b16 a[232:233],v3 offset:6296
v_lshl_add_u64 v[36:37],v[36:37],0,v[38:39]
v_mfma_f32_16x16x32_bf16 v[32:35],a[212:215],a[80:83],v[32:35]
s_mov_b32 s69,s87
ds_read_b64_tr_b16 a[234:235],v3 offset:7832
v_lshl_add_u64 v[38:39],v[40:41],0,v[42:43]
v_lshlrev_b32_sdwa v26,1,v26 dst_sel:DWORD dst_unused:UNUSED_PAD src0_sel:DWORD src1_sel:WORD_1
v_mfma_f32_16x16x32_bf16 v[60:63],a[200:203],a[80:83],v[16:19]
buffer_load_dwordx4 v5,s[44:47],s69 offen offset:0 lds
v_lshlrev_b32_sdwa v25,1,v25 dst_sel:DWORD dst_unused:UNUSED_PAD src0_sel:DWORD src1_sel:WORD_1
v_mfma_f32_16x16x32_bf16 v[60:63],a[196:199],a[64:67],v[60:63]
s_waitcnt lgkmcnt(8)
ds_read_u16 v24,v24
v_lshl_add_u64 v[40:41],v[52:53],0,v[54:55]
v_mfma_f32_16x16x32_bf16 v[60:63],a[192:195],a[48:51],v[60:63]
s_waitcnt lgkmcnt(3)
ds_read_u16_d16_hi v26,v26
v_or3_b32 v13,v14,v15,v13
v_lshlrev_b32_sdwa v27,1,v27 dst_sel:DWORD dst_unused:UNUSED_PAD src0_sel:DWORD src1_sel:WORD_1
v_mfma_f32_16x16x32_bf16 v[52:55],a[192:195],a[52:55],v[16:19]
s_add_u32 s69,s69,s53
ds_read_b64 v[14:15],v7 offset:6672
v_or_b32 v42,v56,v58
v_lshlrev_b32_sdwa v28,1,v28 dst_sel:DWORD dst_unused:UNUSED_PAD src0_sel:DWORD src1_sel:WORD_1
v_mfma_f32_16x16x32_bf16 v[52:55],a[196:199],a[68:71],v[52:55]
buffer_load_dwordx4 v5,s[44:47],s69 offen offset:1024 lds
v_cvt_pk_bf16_f32 v43,v57,v59
v_lshlrev_b32_sdwa v30,1,v30 dst_sel:DWORD dst_unused:UNUSED_PAD src0_sel:DWORD src1_sel:WORD_1
v_mfma_f32_16x16x32_bf16 v[52:55],a[200:203],a[84:87],v[52:55]
s_add_u32 s69,s69,s53
ds_read_u16 v25,v25
v_lshlrev_b32_sdwa v29,1,v29 dst_sel:DWORD dst_unused:UNUSED_PAD src0_sel:DWORD src1_sel:WORD_1
v_mfma_f32_16x16x32_bf16 v[56:59],a[212:215],a[84:87],v[20:23]
buffer_load_dwordx4 v5,s[44:47],s69 offen offset:2048 lds
ds_read_u16_d16_hi v27,v27
v_mfma_f32_16x16x32_bf16 v[56:59],a[208:211],a[68:71],v[56:59]
ds_read_u16 v28,v28
v_lshlrev_b32_sdwa v31,1,v31 dst_sel:DWORD dst_unused:UNUSED_PAD src0_sel:DWORD src1_sel:WORD_1
v_mfma_f32_16x16x32_bf16 v[56:59],a[204:207],a[52:55],v[56:59]
s_waitcnt lgkmcnt(3)
ds_read_u16_d16_hi v30,v30
v_or3_b32 v13,v14,v15,v13
v_mfma_f32_16x16x32_bf16 v[136:139],a[248:251],v[36:39],v[136:139]
ds_read_b64 v[14:15],v7 offset:7184
v_lshlrev_b32_sdwa v48,1,v48 dst_sel:DWORD dst_unused:UNUSED_PAD src0_sel:DWORD src1_sel:WORD_1
v_mfma_f32_16x16x32_bf16 v[140:143],a[248:251],v[40:43],v[140:143]
ds_read_u16 v29,v29
v_lshlrev_b32_sdwa v50,1,v50 dst_sel:DWORD dst_unused:UNUSED_PAD src0_sel:DWORD src1_sel:WORD_1
v_mfma_f32_16x16x32_bf16 v[68:71],a[252:255],v[40:43],v[68:71]
ds_read_u16_d16_hi v31,v31
v_lshlrev_b32_sdwa v49,1,v49 dst_sel:DWORD dst_unused:UNUSED_PAD src0_sel:DWORD src1_sel:WORD_1
v_mfma_f32_16x16x32_bf16 v[64:67],a[252:255],v[36:39],v[64:67]
ds_read_u16 v48,v48
v_lshlrev_b32_sdwa v51,1,v51 dst_sel:DWORD dst_unused:UNUSED_PAD src0_sel:DWORD src1_sel:WORD_1
v_mfma_f32_16x16x32_bf16 v[144:147],a[240:243],v[36:39],v[144:147]
ds_read_u16_d16_hi v50,v50
v_lshlrev_b32_sdwa v44,1,v44 dst_sel:DWORD dst_unused:UNUSED_PAD src0_sel:DWORD src1_sel:WORD_1
v_mfma_f32_16x16x32_bf16 v[148:151],a[240:243],v[40:43],v[148:151]
s_waitcnt lgkmcnt(4)
ds_read_u16 v49,v49
v_or3_b32 v13,v14,v15,v13
v_mfma_f32_16x16x32_bf16 v[84:87],a[224:227],v[40:43],v[84:87]
ds_read_u16_d16_hi v51,v51
v_lshlrev_b32_sdwa v46,1,v46 dst_sel:DWORD dst_unused:UNUSED_PAD src0_sel:DWORD src1_sel:WORD_1
v_mfma_f32_16x16x32_bf16 v[80:83],a[224:227],v[36:39],v[80:83]
ds_read_u16 v44,v44
v_lshlrev_b32_sdwa v45,1,v45 dst_sel:DWORD dst_unused:UNUSED_PAD src0_sel:DWORD src1_sel:WORD_1
v_mfma_f32_16x16x32_bf16 v[152:155],a[228:231],v[36:39],v[152:155]
ds_read_u16_d16_hi v46,v46
v_lshlrev_b32_sdwa v47,1,v47 dst_sel:DWORD dst_unused:UNUSED_PAD src0_sel:DWORD src1_sel:WORD_1
v_mfma_f32_16x16x32_bf16 v[156:159],a[228:231],v[40:43],v[156:159]
ds_read_u16 v45,v45
v_mfma_f32_16x16x32_bf16 v[100:103],a[232:235],v[40:43],v[100:103]
ds_read_u16_d16_hi v47,v47
v_mfma_f32_16x16x32_bf16 v[96:99],a[232:235],v[36:39],v[96:99]
v_mfma_f32_16x16x32_bf16 v[40:43],a[204:207],a[56:59],v[20:23]
v_lshlrev_b32_sdwa v32,1,v32 dst_sel:DWORD dst_unused:UNUSED_PAD src0_sel:DWORD src1_sel:WORD_1
v_fma_mixlo_f16 v8,v53,s99,0
v_mfma_f32_16x16x32_bf16 v[40:43],a[208:211],a[72:75],v[40:43]
ds_read_u16 v32,v32
v_lshl_add_u64 v[24:25],v[24:25],0,v[26:27]
v_mfma_f32_16x16x32_bf16 v[40:43],a[212:215],a[88:91],v[40:43]
s_waitcnt lgkmcnt(6)
v_lshl_add_u64 v[26:27],v[28:29],0,v[30:31]
v_lshlrev_b32_sdwa v34,1,v34 dst_sel:DWORD dst_unused:UNUSED_PAD src0_sel:DWORD src1_sel:WORD_1
v_mfma_f32_16x16x32_bf16 v[36:39],a[200:203],a[88:91],v[16:19]
ds_read_u16_d16_hi v34,v34
v_fma_mixhi_f16 v8,v55,s99,0
v_mfma_f32_16x16x32_bf16 v[36:39],a[196:199],a[72:75],v[36:39]
s_waitcnt lgkmcnt(5)
v_lshl_add_u64 v[28:29],v[48:49],0,v[50:51]
v_lshlrev_b32_sdwa v33,1,v33 dst_sel:DWORD dst_unused:UNUSED_PAD src0_sel:DWORD src1_sel:WORD_1
v_pk_fma_f16 v9,v8,v8,s54
v_mfma_f32_16x16x32_bf16 v[36:39],a[192:195],a[56:59],v[36:39]
ds_read_u16 v33,v33
v_lshlrev_b32_sdwa v35,1,v35 dst_sel:DWORD dst_unused:UNUSED_PAD src0_sel:DWORD src1_sel:WORD_1
v_mfma_f32_16x16x32_bf16 v[48:51],a[192:195],a[60:63],v[16:19]
ds_read_b128 v[16:19],v4 offset:1280
v_pk_mul_f16 v9,v8,v9
v_mfma_f32_16x16x32_bf16 v[48:51],a[196:199],a[76:79],v[48:51]
s_waitcnt lgkmcnt(4)
ds_read_u16_d16_hi v35,v35
ds_read_b64_tr_b16 a[192:193],v1 offset:10256
v_lshl_add_u64 v[30:31],v[44:45],0,v[46:47]
v_lshlrev_b32_sdwa v60,1,v60 dst_sel:DWORD dst_unused:UNUSED_PAD src0_sel:DWORD src1_sel:WORD_1
v_mfma_f32_16x16x32_bf16 v[48:51],a[200:203],a[92:95],v[48:51]
ds_read_b64_tr_b16 a[194:195],v1 offset:11280
v_exp_f16_e64 v8,v9
v_mfma_f32_16x16x32_bf16 v[44:47],a[212:215],a[92:95],v[20:23]
ds_read_b128 v[20:23],v4 offset:1312
ds_read_u16 v60,v60
v_lshlrev_b32_sdwa v62,1,v62 dst_sel:DWORD dst_unused:UNUSED_PAD src0_sel:DWORD src1_sel:WORD_1
v_mfma_f32_16x16x32_bf16 v[44:47],a[208:211],a[76:79],v[44:47]
ds_read_u16_d16_hi v62,v62
ds_read_b64_tr_b16 a[196:197],v2 offset:10256
v_lshlrev_b32_sdwa v61,1,v61 dst_sel:DWORD dst_unused:UNUSED_PAD src0_sel:DWORD src1_sel:WORD_1
v_mfma_f32_16x16x32_bf16 v[44:47],a[204:207],a[60:63],v[44:47]
ds_read_u16 v61,v61
v_exp_f16_sdwa v8,v9 dst_sel:WORD_1 dst_unused:UNUSED_PRESERVE src0_sel:WORD_1
v_mfma_f32_16x16x32_bf16 v[112:115],a[248:251],v[24:27],v[112:115]
ds_read_b64_tr_b16 a[198:199],v2 offset:11280
ds_read_b64_tr_b16 a[200:201],v2 offset:8208
v_lshlrev_b32_sdwa v63,1,v63 dst_sel:DWORD dst_unused:UNUSED_PAD src0_sel:DWORD src1_sel:WORD_1
v_mfma_f32_16x16x32_bf16 v[116:119],a[248:251],v[28:31],v[116:119]
ds_read_u16_d16_hi v63,v63
v_lshlrev_b32_sdwa v56,1,v56 dst_sel:DWORD dst_unused:UNUSED_PAD src0_sel:DWORD src1_sel:WORD_1
v_mfma_f32_16x16x32_bf16 v[76:79],a[252:255],v[28:31],v[76:79]
ds_read_b64_tr_b16 a[202:203],v2 offset:9232
v_pk_fma_f16 v8,v8,s55,s55
v_mfma_f32_16x16x32_bf16 v[72:75],a[252:255],v[24:27],v[72:75]
v_rcp_f16 v9,v8
v_lshlrev_b32_sdwa v58,1,v58 dst_sel:DWORD dst_unused:UNUSED_PAD src0_sel:DWORD src1_sel:WORD_1
v_mfma_f32_16x16x32_bf16 v[120:123],a[240:243],v[24:27],v[120:123]
ds_read_u16 v56,v56
v_lshlrev_b32_sdwa v57,1,v57 dst_sel:DWORD dst_unused:UNUSED_PAD src0_sel:DWORD src1_sel:WORD_1
v_mfma_f32_16x16x32_bf16 v[124:127],a[240:243],v[28:31],v[124:127]
ds_read_u16_d16_hi v58,v58
v_lshlrev_b32_sdwa v59,1,v59 dst_sel:DWORD dst_unused:UNUSED_PAD src0_sel:DWORD src1_sel:WORD_1
v_mfma_f32_16x16x32_bf16 v[92:95],a[224:227],v[28:31],v[92:95]
ds_read_u16 v57,v57
v_lshlrev_b32_sdwa v52,1,v52 dst_sel:DWORD dst_unused:UNUSED_PAD src0_sel:DWORD src1_sel:WORD_1
v_mfma_f32_16x16x32_bf16 v[88:91],a[224:227],v[24:27],v[88:91]
ds_read_u16_d16_hi v59,v59
v_rcp_f16_sdwa v9,v8 dst_sel:WORD_1 dst_unused:UNUSED_PRESERVE src0_sel:WORD_1
v_mfma_f32_16x16x32_bf16 v[128:131],a[228:231],v[24:27],v[128:131]
ds_read_b64_tr_b16 a[212:213],v2 offset:6160
v_lshlrev_b32_sdwa v54,1,v54 dst_sel:DWORD dst_unused:UNUSED_PAD src0_sel:DWORD src1_sel:WORD_1
v_mfma_f32_16x16x32_bf16 v[132:135],a[228:231],v[28:31],v[132:135]
ds_read_b64_tr_b16 a[214:215],v2 offset:7184
v_fma_mix_f32 v53,v53,v9,0 op_sel_hi:[0,1,0]
v_mfma_f32_16x16x32_bf16 v[108:111],a[232:235],v[28:31],v[108:111]
ds_read_u16 v52,v52
v_fma_mix_f32 v55,v55,v9,0 op_sel:[0,1,0] op_sel_hi:[0,1,0]
v_mfma_f32_16x16x32_bf16 v[104:107],a[232:235],v[24:27],v[104:107]
s_waitcnt lgkmcnt(7)
ds_read_u16_d16_hi v54,v54
v_mfma_f32_16x16x32_bf16 v[28:31],a[216:219],a[0:3],v[16:19]
v_lshlrev_b32_sdwa v40,1,v40 dst_sel:DWORD dst_unused:UNUSED_PAD src0_sel:DWORD src1_sel:WORD_1
v_lshlrev_b32_sdwa v42,1,v42 dst_sel:DWORD dst_unused:UNUSED_PAD src0_sel:DWORD src1_sel:WORD_1
v_mfma_f32_16x16x32_bf16 v[28:31],a[220:223],a[16:19],v[28:31]
ds_read_u16 v40,v40
v_lshl_add_u64 v[32:33],v[32:33],0,v[34:35]
v_mfma_f32_16x16x32_bf16 v[28:31],a[192:195],a[32:35],v[28:31]
ds_read_u16_d16_hi v42,v42
v_lshl_add_u64 v[34:35],v[60:61],0,v[62:63]
v_mfma_f32_16x16x32_bf16 v[24:27],a[196:199],a[32:35],v[20:23]
v_lshlrev_b32_sdwa v41,1,v41 dst_sel:DWORD dst_unused:UNUSED_PAD src0_sel:DWORD src1_sel:WORD_1
v_lshlrev_b32_sdwa v43,1,v43 dst_sel:DWORD dst_unused:UNUSED_PAD src0_sel:DWORD src1_sel:WORD_1
v_mfma_f32_16x16x32_bf16 v[24:27],a[200:203],a[16:19],v[24:27]
s_waitcnt lgkmcnt(4)
ds_read_u16 v41,v41
v_lshl_add_u64 v[60:61],v[56:57],0,v[58:59]
v_mfma_f32_16x16x32_bf16 v[24:27],a[212:215],a[0:3],v[24:27]
ds_read_u16_d16_hi v43,v43
v_lshlrev_b32_sdwa v36,1,v36 dst_sel:DWORD dst_unused:UNUSED_PAD src0_sel:DWORD src1_sel:WORD_1
v_mfma_f32_16x16x32_bf16 v[56:59],a[212:215],a[4:7],v[20:23]
s_waitcnt lgkmcnt(4)
ds_read_u16 v36,v36
v_or_b32 v62,v52,v54
v_mfma_f32_16x16x32_bf16 v[56:59],a[200:203],a[20:23],v[56:59]
v_cvt_pk_bf16_f32 v63,v53,v55
v_lshlrev_b32_sdwa v38,1,v38 dst_sel:DWORD dst_unused:UNUSED_PAD src0_sel:DWORD src1_sel:WORD_1
v_mfma_f32_16x16x32_bf16 v[56:59],a[196:199],a[36:39],v[56:59]
ds_read_u16_d16_hi v38,v38
v_lshlrev_b32_sdwa v37,1,v37 dst_sel:DWORD dst_unused:UNUSED_PAD src0_sel:DWORD src1_sel:WORD_1
v_mfma_f32_16x16x32_bf16 v[52:55],a[192:195],a[36:39],v[16:19]
ds_read_u16 v37,v37
v_lshlrev_b32_sdwa v39,1,v39 dst_sel:DWORD dst_unused:UNUSED_PAD src0_sel:DWORD src1_sel:WORD_1
v_mfma_f32_16x16x32_bf16 v[52:55],a[220:223],a[20:23],v[52:55]
ds_read_u16_d16_hi v39,v39
v_lshlrev_b32_sdwa v44,1,v44 dst_sel:DWORD dst_unused:UNUSED_PAD src0_sel:DWORD src1_sel:WORD_1
v_mfma_f32_16x16x32_bf16 v[52:55],a[216:219],a[4:7],v[52:55]
ds_read_u16 v44,v44
v_lshlrev_b32_sdwa v46,1,v46 dst_sel:DWORD dst_unused:UNUSED_PAD src0_sel:DWORD src1_sel:WORD_1
v_mfma_f32_16x16x32_bf16 v[232:235],a[248:251],v[32:35],v[232:235]
ds_read_u16_d16_hi v46,v46
v_lshlrev_b32_sdwa v45,1,v45 dst_sel:DWORD dst_unused:UNUSED_PAD src0_sel:DWORD src1_sel:WORD_1
v_mfma_f32_16x16x32_bf16 v[236:239],a[248:251],v[60:63],v[236:239]
ds_read_u16 v45,v45
v_lshlrev_b32_sdwa v47,1,v47 dst_sel:DWORD dst_unused:UNUSED_PAD src0_sel:DWORD src1_sel:WORD_1
v_mfma_f32_16x16x32_bf16 v[164:167],a[252:255],v[60:63],v[164:167]
ds_read_u16_d16_hi v47,v47
ds_read_b64_tr_b16 a[236:237],v3 offset:12320
v_mfma_f32_16x16x32_bf16 v[160:163],a[252:255],v[32:35],v[160:163]
ds_read_b64_tr_b16 a[238:239],v3 offset:13856
v_lshlrev_b32_sdwa v48,1,v48 dst_sel:DWORD dst_unused:UNUSED_PAD src0_sel:DWORD src1_sel:WORD_1
v_mfma_f32_16x16x32_bf16 v[240:243],a[240:243],v[32:35],v[240:243]
ds_read_u16 v48,v48
v_lshlrev_b32_sdwa v50,1,v50 dst_sel:DWORD dst_unused:UNUSED_PAD src0_sel:DWORD src1_sel:WORD_1
v_mfma_f32_16x16x32_bf16 v[244:247],a[240:243],v[60:63],v[244:247]
ds_read_u16_d16_hi v50,v50
v_lshlrev_b32_sdwa v49,1,v49 dst_sel:DWORD dst_unused:UNUSED_PAD src0_sel:DWORD src1_sel:WORD_1
v_mfma_f32_16x16x32_bf16 v[180:183],a[224:227],v[60:63],v[180:183]
ds_read_u16 v49,v49
v_lshlrev_b32_sdwa v51,1,v51 dst_sel:DWORD dst_unused:UNUSED_PAD src0_sel:DWORD src1_sel:WORD_1
v_mfma_f32_16x16x32_bf16 v[176:179],a[224:227],v[32:35],v[176:179]
ds_read_u16_d16_hi v51,v51
v_mfma_f32_16x16x32_bf16 v[248:251],a[228:231],v[32:35],v[248:251]
ds_read_b64_tr_b16 a[244:245],v3 offset:12328
v_mfma_f32_16x16x32_bf16 v[252:255],a[228:231],v[60:63],v[252:255]
ds_read_b64_tr_b16 a[246:247],v3 offset:13864
v_mfma_f32_16x16x32_bf16 v[196:199],a[232:235],v[60:63],v[196:199]
v_mfma_f32_16x16x32_bf16 v[192:195],a[232:235],v[32:35],v[192:195]
v_mfma_f32_16x16x32_bf16 v[60:63],a[216:219],a[8:11],v[16:19]
ds_read_b64_tr_b16 a[208:209],v1 offset:12320
v_lshlrev_b32_sdwa v28,1,v28 dst_sel:DWORD dst_unused:UNUSED_PAD src0_sel:DWORD src1_sel:WORD_1
v_mfma_f32_16x16x32_bf16 v[60:63],a[220:223],a[24:27],v[60:63]
s_waitcnt lgkmcnt(6)
ds_read_u16 v28,v28
v_lshl_add_u64 v[40:41],v[40:41],0,v[42:43]
v_fma_mixlo_f16 v8,v57,s99,0
v_mfma_f32_16x16x32_bf16 v[60:63],a[192:195],a[40:43],v[60:63]
ds_read_b64_tr_b16 a[210:211],v1 offset:13344
v_lshl_add_u64 v[42:43],v[36:37],0,v[38:39]
v_lshlrev_b32_sdwa v30,1,v30 dst_sel:DWORD dst_unused:UNUSED_PAD src0_sel:DWORD src1_sel:WORD_1
v_mfma_f32_16x16x32_bf16 v[32:35],a[196:199],a[40:43],v[20:23]
ds_read_u16_d16_hi v30,v30
v_fma_mixhi_f16 v8,v59,s99,0
v_mfma_f32_16x16x32_bf16 v[32:35],a[200:203],a[24:27],v[32:35]
ds_read_b64_tr_b16 a[204:205],v1 offset:14368
v_lshl_add_u64 v[36:37],v[44:45],0,v[46:47]
v_lshlrev_b32_sdwa v29,1,v29 dst_sel:DWORD dst_unused:UNUSED_PAD src0_sel:DWORD src1_sel:WORD_1
v_pk_fma_f16 v9,v8,v8,s54
v_mfma_f32_16x16x32_bf16 v[32:35],a[212:215],a[8:11],v[32:35]
ds_read_u16 v29,v29
v_lshlrev_b32_sdwa v31,1,v31 dst_sel:DWORD dst_unused:UNUSED_PAD src0_sel:DWORD src1_sel:WORD_1
v_mfma_f32_16x16x32_bf16 v[44:47],a[212:215],a[12:15],v[20:23]
ds_read_u16_d16_hi v31,v31
v_pk_mul_f16 v9,v8,v9
v_mfma_f32_16x16x32_bf16 v[44:47],a[200:203],a[28:31],v[44:47]
s_waitcnt lgkmcnt(7)
ds_read_b64_tr_b16 a[206:207],v1 offset:15392
v_lshl_add_u64 v[38:39],v[48:49],0,v[50:51]
v_lshlrev_b32_sdwa v24,1,v24 dst_sel:DWORD dst_unused:UNUSED_PAD src0_sel:DWORD src1_sel:WORD_1
v_mfma_f32_16x16x32_bf16 v[44:47],a[196:199],a[44:47],v[44:47]
v_exp_f16 v8,v9
v_mfma_f32_16x16x32_bf16 v[48:51],a[192:195],a[44:47],v[16:19]
ds_read_u16 v24,v24
v_lshlrev_b32_sdwa v26,1,v26 dst_sel:DWORD dst_unused:UNUSED_PAD src0_sel:DWORD src1_sel:WORD_1
v_mfma_f32_16x16x32_bf16 v[48:51],a[220:223],a[28:31],v[48:51]
ds_read_u16_d16_hi v26,v26
v_lshlrev_b32_sdwa v25,1,v25 dst_sel:DWORD dst_unused:UNUSED_PAD src0_sel:DWORD src1_sel:WORD_1
v_mfma_f32_16x16x32_bf16 v[48:51],a[216:219],a[12:15],v[48:51]
v_exp_f16_sdwa v8,v9 dst_sel:WORD_1 dst_unused:UNUSED_PRESERVE src0_sel:WORD_1
v_mfma_f32_16x16x32_bf16 v[208:211],a[248:251],v[40:43],v[208:211]
ds_read_u16 v25,v25
v_lshlrev_b32_sdwa v27,1,v27 dst_sel:DWORD dst_unused:UNUSED_PAD src0_sel:DWORD src1_sel:WORD_1
v_mfma_f32_16x16x32_bf16 v[212:215],a[248:251],v[36:39],v[212:215]
TP4344_%=:
s_cbranch_execz TP6486_%=
ds_read_u16_d16_hi v27,v27
v_lshlrev_b32_sdwa v52,1,v52 dst_sel:DWORD dst_unused:UNUSED_PAD src0_sel:DWORD src1_sel:WORD_1
v_mfma_f32_16x16x32_bf16 v[172:175],a[252:255],v[36:39],v[172:175]
ds_read_u16 v52,v52
v_pk_fma_f16 v8,v8,s55,s55
v_mfma_f32_16x16x32_bf16 v[168:171],a[252:255],v[40:43],v[168:171]
ds_read_b64_tr_b16 a[248:249],v3 offset:12384
v_rcp_f16 v9,v8
v_lshlrev_b32_sdwa v54,1,v54 dst_sel:DWORD dst_unused:UNUSED_PAD src0_sel:DWORD src1_sel:WORD_1
v_mfma_f32_16x16x32_bf16 v[216:219],a[240:243],v[40:43],v[216:219]
ds_read_u16_d16_hi v54,v54
v_lshlrev_b32_sdwa v53,1,v53 dst_sel:DWORD dst_unused:UNUSED_PAD src0_sel:DWORD src1_sel:WORD_1
v_mfma_f32_16x16x32_bf16 v[220:223],a[240:243],v[36:39],v[220:223]
ds_read_u16 v53,v53
ds_read_b64_tr_b16 a[250:251],v3 offset:13920
v_mfma_f32_16x16x32_bf16 v[188:191],a[224:227],v[36:39],v[188:191]
ds_read_b64_tr_b16 a[252:253],v3 offset:12392
v_lshlrev_b32_sdwa v55,1,v55 dst_sel:DWORD dst_unused:UNUSED_PAD src0_sel:DWORD src1_sel:WORD_1
v_mfma_f32_16x16x32_bf16 v[184:187],a[224:227],v[40:43],v[184:187]
ds_read_u16_d16_hi v55,v55
v_rcp_f16_sdwa v9,v8 dst_sel:WORD_1 dst_unused:UNUSED_PRESERVE src0_sel:WORD_1
v_mfma_f32_16x16x32_bf16 v[224:227],a[228:231],v[40:43],v[224:227]
ds_read_b64_tr_b16 a[254:255],v3 offset:13928
ds_read_b64_tr_b16 a[240:241],v3 offset:12448
v_lshlrev_b32_sdwa v56,1,v56 dst_sel:DWORD dst_unused:UNUSED_PAD src0_sel:DWORD src1_sel:WORD_1
v_mfma_f32_16x16x32_bf16 v[228:231],a[228:231],v[36:39],v[228:231]
ds_read_u16 v56,v56
v_lshlrev_b32_sdwa v58,1,v58 dst_sel:DWORD dst_unused:UNUSED_PAD src0_sel:DWORD src1_sel:WORD_1
v_mfma_f32_16x16x32_bf16 v[204:207],a[232:235],v[36:39],v[204:207]
ds_read_u16_d16_hi v58,v58
v_fma_mix_f32 v57,v57,v9,0 op_sel_hi:[0,1,0]
v_mfma_f32_16x16x32_bf16 v[200:203],a[232:235],v[40:43],v[200:203]
ds_read_b64_tr_b16 a[242:243],v3 offset:13984
v_fma_mix_f32 v59,v59,v9,0 op_sel:[0,1,0] op_sel_hi:[0,1,0]
v_mbcnt_lo_u32_b32 v8,-1,0
N_qs10_%=:
s_add_u32 s64,s16,N_qs10_%=-N_pf_%=+0
v_and_b32_e64 v8,3,v8
s_mov_b32 m0,12288
v_lshl_add_u32 v8,v8,7,s64
v_min_u32 v8,N_mainend_%=-N_pf_%=+2048,v8
global_load_lds_dword v8,s[14:15]
v_mfma_f32_16x16x32_bf16 v[36:39],a[216:219],a[48:51],v[16:19]
ds_read_b64 v[14:15],v7 offset:12320
v_lshlrev_b32_sdwa v60,1,v60 dst_sel:DWORD dst_unused:UNUSED_PAD src0_sel:DWORD src1_sel:WORD_1
v_mfma_f32_16x16x32_bf16 v[36:39],a[220:223],a[64:67],v[36:39]
s_waitcnt lgkmcnt(10)
ds_read_b64_tr_b16 a[224:225],v3 offset:12456
v_lshl_add_u64 v[28:29],v[28:29],0,v[30:31]
v_mfma_f32_16x16x32_bf16 v[36:39],a[192:195],a[80:83],v[36:39]
ds_read_b64_tr_b16 a[226:227],v3 offset:13992
v_lshl_add_u64 v[30:31],v[24:25],0,v[26:27]
v_lshlrev_b32_sdwa v62,1,v62 dst_sel:DWORD dst_unused:UNUSED_PAD src0_sel:DWORD src1_sel:WORD_1
v_mfma_f32_16x16x32_bf16 v[40:43],a[196:199],a[80:83],v[20:23]
ds_read_u16 v60,v60
v_lshlrev_b32_sdwa v61,1,v61 dst_sel:DWORD dst_unused:UNUSED_PAD src0_sel:DWORD src1_sel:WORD_1
v_mfma_f32_16x16x32_bf16 v[40:43],a[200:203],a[64:67],v[40:43]
s_waitcnt lgkmcnt(9)
ds_read_u16_d16_hi v62,v62
v_lshl_add_u64 v[24:25],v[52:53],0,v[54:55]
v_mfma_f32_16x16x32_bf16 v[40:43],a[212:215],a[48:51],v[40:43]
s_waitcnt lgkmcnt(4)
ds_read_u16 v61,v61
v_or3_b32 v13,v14,v15,v13
v_lshlrev_b32_sdwa v63,1,v63 dst_sel:DWORD dst_unused:UNUSED_PAD src0_sel:DWORD src1_sel:WORD_1
v_mfma_f32_16x16x32_bf16 v[52:55],a[212:215],a[52:55],v[20:23]
ds_read_b64 v[14:15],v7 offset:12832
v_or_b32 v26,v56,v58
v_lshlrev_b32_sdwa v32,1,v32 dst_sel:DWORD dst_unused:UNUSED_PAD src0_sel:DWORD src1_sel:WORD_1
v_mfma_f32_16x16x32_bf16 v[52:55],a[200:203],a[68:71],v[52:55]
ds_read_u16_d16_hi v63,v63
v_cvt_pk_bf16_f32 v27,v57,v59
v_lshlrev_b32_sdwa v34,1,v34 dst_sel:DWORD dst_unused:UNUSED_PAD src0_sel:DWORD src1_sel:WORD_1
v_mfma_f32_16x16x32_bf16 v[52:55],a[196:199],a[84:87],v[52:55]
ds_read_u16 v32,v32
v_lshlrev_b32_sdwa v33,1,v33 dst_sel:DWORD dst_unused:UNUSED_PAD src0_sel:DWORD src1_sel:WORD_1
v_mfma_f32_16x16x32_bf16 v[56:59],a[192:195],a[84:87],v[16:19]
ds_read_u16_d16_hi v34,v34
v_lshlrev_b32_sdwa v35,1,v35 dst_sel:DWORD dst_unused:UNUSED_PAD src0_sel:DWORD src1_sel:WORD_1
v_mfma_f32_16x16x32_bf16 v[56:59],a[220:223],a[68:71],v[56:59]
ds_read_u16 v33,v33
v_lshlrev_b32_sdwa v48,1,v48 dst_sel:DWORD dst_unused:UNUSED_PAD src0_sel:DWORD src1_sel:WORD_1
v_mfma_f32_16x16x32_bf16 v[56:59],a[216:219],a[52:55],v[56:59]
s_waitcnt lgkmcnt(4)
ds_read_u16_d16_hi v35,v35
v_or3_b32 v13,v14,v15,v13
v_mfma_f32_16x16x32_bf16 v[136:139],a[236:239],v[28:31],v[136:139]
ds_read_b64 v[14:15],v7 offset:13344
v_lshlrev_b32_sdwa v50,1,v50 dst_sel:DWORD dst_unused:UNUSED_PAD src0_sel:DWORD src1_sel:WORD_1
v_mfma_f32_16x16x32_bf16 v[140:143],a[236:239],v[24:27],v[140:143]
ds_read_u16 v48,v48
v_lshlrev_b32_sdwa v49,1,v49 dst_sel:DWORD dst_unused:UNUSED_PAD src0_sel:DWORD src1_sel:WORD_1
v_mfma_f32_16x16x32_bf16 v[68:71],a[244:247],v[24:27],v[68:71]
ds_read_u16_d16_hi v50,v50
v_lshlrev_b32_sdwa v51,1,v51 dst_sel:DWORD dst_unused:UNUSED_PAD src0_sel:DWORD src1_sel:WORD_1
v_mfma_f32_16x16x32_bf16 v[64:67],a[244:247],v[28:31],v[64:67]
ds_read_u16 v49,v49
v_lshlrev_b32_sdwa v44,1,v44 dst_sel:DWORD dst_unused:UNUSED_PAD src0_sel:DWORD src1_sel:WORD_1
v_mfma_f32_16x16x32_bf16 v[144:147],a[248:251],v[28:31],v[144:147]
ds_read_u16_d16_hi v51,v51
v_lshlrev_b32_sdwa v46,1,v46 dst_sel:DWORD dst_unused:UNUSED_PAD src0_sel:DWORD src1_sel:WORD_1
v_mfma_f32_16x16x32_bf16 v[148:151],a[248:251],v[24:27],v[148:151]
s_waitcnt lgkmcnt(4)
ds_read_u16 v44,v44
v_or3_b32 v13,v14,v15,v13
v_mfma_f32_16x16x32_bf16 v[84:87],a[252:255],v[24:27],v[84:87]
ds_read_u16_d16_hi v46,v46
v_and_b32 v13,0x40004000,v13
v_mfma_f32_16x16x32_bf16 v[80:83],a[252:255],v[28:31],v[80:83]
v_cmp_ne_u32_e32 vcc,0,v13
s_nop 0
s_or_b64 s[90:91],s[90:91],vcc
v_lshlrev_b32_sdwa v45,1,v45 dst_sel:DWORD dst_unused:UNUSED_PAD src0_sel:DWORD src1_sel:WORD_1
v_mfma_f32_16x16x32_bf16 v[152:155],a[240:243],v[28:31],v[152:155]
ds_read_u16 v45,v45
v_mbcnt_lo_u32_b32 v15,-1,0
v_mfma_f32_16x16x32_bf16 v[156:159],a[240:243],v[24:27],v[156:159]
v_mbcnt_hi_u32_b32 v15,-1,v15
v_lshlrev_b32_sdwa v47,1,v47 dst_sel:DWORD dst_unused:UNUSED_PAD src0_sel:DWORD src1_sel:WORD_1
v_mfma_f32_16x16x32_bf16 v[100:103],a[224:227],v[24:27],v[100:103]
ds_read_u16_d16_hi v47,v47
v_lshlrev_b32 v15,2,v15
v_mfma_f32_16x16x32_bf16 v[96:99],a[224:227],v[28:31],v[96:99]
ds_read_b32 v7,v15 offset:14848
ds_read_b32 v13,v15 offset:15104
v_mfma_f32_16x16x32_bf16 v[24:27],a[216:219],a[56:59],v[16:19]
ds_read_b32 v14,v15 offset:14336
ds_read_b32 v15,v15 offset:14592
v_lshlrev_b32_sdwa v36,1,v36 dst_sel:DWORD dst_unused:UNUSED_PAD src0_sel:DWORD src1_sel:WORD_1
v_mfma_f32_16x16x32_bf16 v[24:27],a[220:223],a[72:75],v[24:27]
ds_read_u16 v36,v36
v_lshl_add_u64 v[60:61],v[60:61],0,v[62:63]
v_fma_mixlo_f16 v8,v53,s99,0
v_mfma_f32_16x16x32_bf16 v[24:27],a[192:195],a[88:91],v[24:27]
v_lshl_add_u64 v[62:63],v[32:33],0,v[34:35]
v_lshlrev_b32_sdwa v38,1,v38 dst_sel:DWORD dst_unused:UNUSED_PAD src0_sel:DWORD src1_sel:WORD_1
v_mfma_f32_16x16x32_bf16 v[28:31],a[196:199],a[88:91],v[20:23]
ds_read_u16_d16_hi v38,v38
v_fma_mixhi_f16 v8,v55,s99,0
v_mfma_f32_16x16x32_bf16 v[28:31],a[200:203],a[72:75],v[28:31]
s_waitcnt lgkmcnt(8)
v_lshl_add_u64 v[32:33],v[48:49],0,v[50:51]
v_lshlrev_b32_sdwa v37,1,v37 dst_sel:DWORD dst_unused:UNUSED_PAD src0_sel:DWORD src1_sel:WORD_1
v_pk_fma_f16 v9,v8,v8,s54
v_mfma_f32_16x16x32_bf16 v[28:31],a[212:215],a[56:59],v[28:31]
ds_read_u16 v37,v37
v_lshlrev_b32_sdwa v39,1,v39 dst_sel:DWORD dst_unused:UNUSED_PAD src0_sel:DWORD src1_sel:WORD_1
v_mfma_f32_16x16x32_bf16 v[48:51],a[212:215],a[60:63],v[20:23]
ds_read_b128 v[20:23],v4 offset:1408
v_pk_mul_f16 v9,v8,v9
v_mfma_f32_16x16x32_bf16 v[48:51],a[200:203],a[76:79],v[48:51]
s_waitcnt lgkmcnt(8)
ds_read_u16_d16_hi v39,v39
ds_read_b64_tr_b16 a[212:213],v1 offset:16416
v_lshl_add_u64 v[34:35],v[44:45],0,v[46:47]
v_lshlrev_b32_sdwa v40,1,v40 dst_sel:DWORD dst_unused:UNUSED_PAD src0_sel:DWORD src1_sel:WORD_1
v_mfma_f32_16x16x32_bf16 v[48:51],a[196:199],a[92:95],v[48:51]
ds_read_b64_tr_b16 a[214:215],v1 offset:17440
v_exp_f16 v8,v9
v_mfma_f32_16x16x32_bf16 v[44:47],a[192:195],a[92:95],v[16:19]
ds_read_b128 v[16:19],v4 offset:1440
ds_read_u16 v40,v40
v_lshlrev_b32_sdwa v42,1,v42 dst_sel:DWORD dst_unused:UNUSED_PAD src0_sel:DWORD src1_sel:WORD_1
v_mfma_f32_16x16x32_bf16 v[44:47],a[220:223],a[76:79],v[44:47]
ds_read_u16_d16_hi v42,v42
ds_read_b64_tr_b16 a[200:201],v2 offset:16416
v_lshlrev_b32_sdwa v41,1,v41 dst_sel:DWORD dst_unused:UNUSED_PAD src0_sel:DWORD src1_sel:WORD_1
v_mfma_f32_16x16x32_bf16 v[44:47],a[216:219],a[60:63],v[44:47]
ds_read_u16 v41,v41
v_exp_f16_sdwa v8,v9 dst_sel:WORD_1 dst_unused:UNUSED_PRESERVE src0_sel:WORD_1
v_mfma_f32_16x16x32_bf16 v[112:115],a[236:239],v[60:63],v[112:115]
ds_read_b64_tr_b16 a[202:203],v2 offset:17440
ds_read_b64_tr_b16 a[196:197],v2 offset:14368
v_lshlrev_b32_sdwa v43,1,v43 dst_sel:DWORD dst_unused:UNUSED_PAD src0_sel:DWORD src1_sel:WORD_1
v_mfma_f32_16x16x32_bf16 v[116:119],a[236:239],v[32:35],v[116:119]
ds_read_u16_d16_hi v43,v43
v_lshlrev_b32_sdwa v56,1,v56 dst_sel:DWORD dst_unused:UNUSED_PAD src0_sel:DWORD src1_sel:WORD_1
v_mfma_f32_16x16x32_bf16 v[76:79],a[244:247],v[32:35],v[76:79]
ds_read_b64_tr_b16 a[198:199],v2 offset:15392
v_pk_fma_f16 v8,v8,s55,s55
v_mfma_f32_16x16x32_bf16 v[72:75],a[244:247],v[60:63],v[72:75]
v_rcp_f16 v9,v8
v_lshlrev_b32_sdwa v58,1,v58 dst_sel:DWORD dst_unused:UNUSED_PAD src0_sel:DWORD src1_sel:WORD_1
v_mfma_f32_16x16x32_bf16 v[120:123],a[248:251],v[60:63],v[120:123]
ds_read_u16 v56,v56
v_lshlrev_b32_sdwa v57,1,v57 dst_sel:DWORD dst_unused:UNUSED_PAD src0_sel:DWORD src1_sel:WORD_1
v_mfma_f32_16x16x32_bf16 v[124:127],a[248:251],v[32:35],v[124:127]
ds_read_u16_d16_hi v58,v58
v_lshlrev_b32_sdwa v59,1,v59 dst_sel:DWORD dst_unused:UNUSED_PAD src0_sel:DWORD src1_sel:WORD_1
v_mfma_f32_16x16x32_bf16 v[92:95],a[252:255],v[32:35],v[92:95]
ds_read_u16 v57,v57
v_lshlrev_b32_sdwa v52,1,v52 dst_sel:DWORD dst_unused:UNUSED_PAD src0_sel:DWORD src1_sel:WORD_1
v_mfma_f32_16x16x32_bf16 v[88:91],a[252:255],v[60:63],v[88:91]
ds_read_u16_d16_hi v59,v59
v_rcp_f16_sdwa v9,v8 dst_sel:WORD_1 dst_unused:UNUSED_PRESERVE src0_sel:WORD_1
v_mfma_f32_16x16x32_bf16 v[128:131],a[240:243],v[60:63],v[128:131]
ds_read_b64_tr_b16 a[192:193],v2 offset:12320
v_lshlrev_b32_sdwa v54,1,v54 dst_sel:DWORD dst_unused:UNUSED_PAD src0_sel:DWORD src1_sel:WORD_1
v_mfma_f32_16x16x32_bf16 v[132:135],a[240:243],v[32:35],v[132:135]
ds_read_b64_tr_b16 a[194:195],v2 offset:13344
v_fma_mix_f32 v53,v53,v9,0 op_sel_hi:[0,1,0]
v_mfma_f32_16x16x32_bf16 v[108:111],a[224:227],v[32:35],v[108:111]
ds_read_u16 v52,v52
v_fma_mix_f32 v55,v55,v9,0 op_sel:[0,1,0] op_sel_hi:[0,1,0]
v_mfma_f32_16x16x32_bf16 v[104:107],a[224:227],v[60:63],v[104:107]
s_waitcnt lgkmcnt(7)
ds_read_u16_d16_hi v54,v54
v_mfma_f32_16x16x32_bf16 v[32:35],a[208:211],a[0:3],v[20:23]
v_lshlrev_b32_sdwa v24,1,v24 dst_sel:DWORD dst_unused:UNUSED_PAD src0_sel:DWORD src1_sel:WORD_1
v_lshlrev_b32_sdwa v26,1,v26 dst_sel:DWORD dst_unused:UNUSED_PAD src0_sel:DWORD src1_sel:WORD_1
v_mfma_f32_16x16x32_bf16 v[32:35],a[204:207],a[16:19],v[32:35]
ds_read_u16 v24,v24
v_lshl_add_u64 v[36:37],v[36:37],0,v[38:39]
v_mfma_f32_16x16x32_bf16 v[32:35],a[212:215],a[32:35],v[32:35]
ds_read_u16_d16_hi v26,v26
v_lshl_add_u64 v[38:39],v[40:41],0,v[42:43]
v_mfma_f32_16x16x32_bf16 v[60:63],a[200:203],a[32:35],v[16:19]
v_lshlrev_b32_sdwa v25,1,v25 dst_sel:DWORD dst_unused:UNUSED_PAD src0_sel:DWORD src1_sel:WORD_1
v_lshlrev_b32_sdwa v27,1,v27 dst_sel:DWORD dst_unused:UNUSED_PAD src0_sel:DWORD src1_sel:WORD_1
v_mfma_f32_16x16x32_bf16 v[60:63],a[196:199],a[16:19],v[60:63]
s_waitcnt lgkmcnt(4)
ds_read_u16 v25,v25
v_lshl_add_u64 v[40:41],v[56:57],0,v[58:59]
v_mfma_f32_16x16x32_bf16 v[60:63],a[192:195],a[0:3],v[60:63]
ds_read_u16_d16_hi v27,v27
v_lshlrev_b32_sdwa v28,1,v28 dst_sel:DWORD dst_unused:UNUSED_PAD src0_sel:DWORD src1_sel:WORD_1
v_mfma_f32_16x16x32_bf16 v[56:59],a[192:195],a[4:7],v[16:19]
s_waitcnt lgkmcnt(4)
ds_read_u16 v28,v28
v_or_b32 v42,v52,v54
v_mfma_f32_16x16x32_bf16 v[56:59],a[196:199],a[20:23],v[56:59]
s_waitcnt vmcnt(4)
v_cvt_pk_bf16_f32 v43,v53,v55
v_lshlrev_b32_sdwa v30,1,v30 dst_sel:DWORD dst_unused:UNUSED_PAD src0_sel:DWORD src1_sel:WORD_1
v_mfma_f32_16x16x32_bf16 v[56:59],a[200:203],a[36:39],v[56:59]
s_barrier
ds_read_u16_d16_hi v30,v30
v_lshlrev_b32_sdwa v29,1,v29 dst_sel:DWORD dst_unused:UNUSED_PAD src0_sel:DWORD src1_sel:WORD_1
v_mfma_f32_16x16x32_bf16 v[52:55],a[212:215],a[36:39],v[20:23]
ds_read_u16 v29,v29
v_lshlrev_b32_sdwa v31,1,v31 dst_sel:DWORD dst_unused:UNUSED_PAD src0_sel:DWORD src1_sel:WORD_1
v_mfma_f32_16x16x32_bf16 v[52:55],a[204:207],a[20:23],v[52:55]
s_add_u32 m0,s58,6160
ds_read_u16_d16_hi v31,v31
v_lshlrev_b32_sdwa v44,1,v44 dst_sel:DWORD dst_unused:UNUSED_PAD src0_sel:DWORD src1_sel:WORD_1
v_mfma_f32_16x16x32_bf16 v[52:55],a[208:211],a[4:7],v[52:55]
s_add_u32 s69,s69,s59
ds_read_u16 v44,v44
v_lshlrev_b32_sdwa v46,1,v46 dst_sel:DWORD dst_unused:UNUSED_PAD src0_sel:DWORD src1_sel:WORD_1
v_mfma_f32_16x16x32_bf16 v[232:235],a[236:239],v[36:39],v[232:235]
buffer_load_dwordx4 v5,s[44:47],s69 offen offset:0 lds
v_lshlrev_b32_sdwa v45,1,v45 dst_sel:DWORD dst_unused:UNUSED_PAD src0_sel:DWORD src1_sel:WORD_1
v_mfma_f32_16x16x32_bf16 v[236:239],a[236:239],v[40:43],v[236:239]
ds_read_u16_d16_hi v46,v46
v_lshlrev_b32_sdwa v47,1,v47 dst_sel:DWORD dst_unused:UNUSED_PAD src0_sel:DWORD src1_sel:WORD_1
v_mfma_f32_16x16x32_bf16 v[164:167],a[244:247],v[40:43],v[164:167]
ds_read_u16 v45,v45
ds_read_u16_d16_hi v47,v47
v_mfma_f32_16x16x32_bf16 v[160:163],a[244:247],v[36:39],v[160:163]
ds_read_b64_tr_b16 a[228:229],v3 offset:18480
v_lshlrev_b32_sdwa v48,1,v48 dst_sel:DWORD dst_unused:UNUSED_PAD src0_sel:DWORD src1_sel:WORD_1
v_mfma_f32_16x16x32_bf16 v[240:243],a[248:251],v[36:39],v[240:243]
s_add_u32 s69,s69,s53
ds_read_u16 v48,v48
v_lshlrev_b32_sdwa v50,1,v50 dst_sel:DWORD dst_unused:UNUSED_PAD src0_sel:DWORD src1_sel:WORD_1
v_mfma_f32_16x16x32_bf16 v[244:247],a[248:251],v[40:43],v[244:247]
buffer_load_dwordx4 v5,s[44:47],s69 offen offset:1024 lds
ds_read_u16_d16_hi v50,v50
v_mfma_f32_16x16x32_bf16 v[180:183],a[252:255],v[40:43],v[180:183]
ds_read_b64_tr_b16 a[230:231],v3 offset:20016
v_lshlrev_b32_sdwa v49,1,v49 dst_sel:DWORD dst_unused:UNUSED_PAD src0_sel:DWORD src1_sel:WORD_1
v_mfma_f32_16x16x32_bf16 v[176:179],a[252:255],v[36:39],v[176:179]
ds_read_u16 v49,v49
v_lshlrev_b32_sdwa v51,1,v51 dst_sel:DWORD dst_unused:UNUSED_PAD src0_sel:DWORD src1_sel:WORD_1
v_mfma_f32_16x16x32_bf16 v[248:251],a[240:243],v[36:39],v[248:251]
ds_read_u16_d16_hi v51,v51
v_mfma_f32_16x16x32_bf16 v[252:255],a[240:243],v[40:43],v[252:255]
s_add_u32 s69,s69,s53
ds_read_b64_tr_b16 a[232:233],v3 offset:18488
v_mfma_f32_16x16x32_bf16 v[196:199],a[224:227],v[40:43],v[196:199]
buffer_load_dwordx4 v5,s[44:47],s69 offen offset:2048 lds
v_mfma_f32_16x16x32_bf16 v[192:195],a[224:227],v[36:39],v[192:195]
ds_read_b64_tr_b16 a[234:235],v3 offset:20024
v_mfma_f32_16x16x32_bf16 v[40:43],a[208:211],a[8:11],v[20:23]
ds_read_b64_tr_b16 a[220:221],v1 offset:18480
v_lshlrev_b32_sdwa v32,1,v32 dst_sel:DWORD dst_unused:UNUSED_PAD src0_sel:DWORD src1_sel:WORD_1
v_mfma_f32_16x16x32_bf16 v[40:43],a[204:207],a[24:27],v[40:43]
s_waitcnt lgkmcnt(7)
ds_read_u16 v32,v32
v_lshl_add_u64 v[24:25],v[24:25],0,v[26:27]
v_fma_mixlo_f16 v8,v57,s99,0
v_mfma_f32_16x16x32_bf16 v[40:43],a[212:215],a[40:43],v[40:43]
ds_read_b64_tr_b16 a[222:223],v1 offset:19504
v_lshl_add_u64 v[26:27],v[28:29],0,v[30:31]
v_lshlrev_b32_sdwa v34,1,v34 dst_sel:DWORD dst_unused:UNUSED_PAD src0_sel:DWORD src1_sel:WORD_1
v_mfma_f32_16x16x32_bf16 v[36:39],a[200:203],a[40:43],v[16:19]
ds_read_u16_d16_hi v34,v34
v_fma_mixhi_f16 v8,v59,s99,0
v_mfma_f32_16x16x32_bf16 v[36:39],a[196:199],a[24:27],v[36:39]
ds_read_b64_tr_b16 a[216:217],v1 offset:20528
v_lshl_add_u64 v[28:29],v[44:45],0,v[46:47]
v_lshlrev_b32_sdwa v33,1,v33 dst_sel:DWORD dst_unused:UNUSED_PAD src0_sel:DWORD src1_sel:WORD_1
v_pk_fma_f16 v9,v8,v8,s54
v_mfma_f32_16x16x32_bf16 v[36:39],a[192:195],a[8:11],v[36:39]
ds_read_u16 v33,v33
v_lshlrev_b32_sdwa v35,1,v35 dst_sel:DWORD dst_unused:UNUSED_PAD src0_sel:DWORD src1_sel:WORD_1
v_mfma_f32_16x16x32_bf16 v[44:47],a[192:195],a[12:15],v[16:19]
ds_read_u16_d16_hi v35,v35
v_pk_mul_f16 v9,v8,v9
v_mfma_f32_16x16x32_bf16 v[44:47],a[196:199],a[28:31],v[44:47]
s_waitcnt lgkmcnt(8)
ds_read_b64_tr_b16 a[218:219],v1 offset:21552
v_lshl_add_u64 v[30:31],v[48:49],0,v[50:51]
v_lshlrev_b32_sdwa v60,1,v60 dst_sel:DWORD dst_unused:UNUSED_PAD src0_sel:DWORD src1_sel:WORD_1
v_mfma_f32_16x16x32_bf16 v[44:47],a[200:203],a[44:47],v[44:47]
v_exp_f16 v8,v9
v_mfma_f32_16x16x32_bf16 v[48:51],a[212:215],a[44:47],v[20:23]
ds_read_u16 v60,v60
v_lshlrev_b32_sdwa v62,1,v62 dst_sel:DWORD dst_unused:UNUSED_PAD src0_sel:DWORD src1_sel:WORD_1
v_mfma_f32_16x16x32_bf16 v[48:51],a[204:207],a[28:31],v[48:51]
ds_read_u16_d16_hi v62,v62
v_lshlrev_b32_sdwa v61,1,v61 dst_sel:DWORD dst_unused:UNUSED_PAD src0_sel:DWORD src1_sel:WORD_1
v_mfma_f32_16x16x32_bf16 v[48:51],a[208:211],a[12:15],v[48:51]
v_exp_f16_sdwa v8,v9 dst_sel:WORD_1 dst_unused:UNUSED_PRESERVE src0_sel:WORD_1
v_mfma_f32_16x16x32_bf16 v[208:211],a[236:239],v[24:27],v[208:211]
ds_read_u16 v61,v61
v_lshlrev_b32_sdwa v63,1,v63 dst_sel:DWORD dst_unused:UNUSED_PAD src0_sel:DWORD src1_sel:WORD_1
v_mfma_f32_16x16x32_bf16 v[212:215],a[236:239],v[28:31],v[212:215]
ds_read_u16_d16_hi v63,v63
v_lshlrev_b32_sdwa v52,1,v52 dst_sel:DWORD dst_unused:UNUSED_PAD src0_sel:DWORD src1_sel:WORD_1
v_mfma_f32_16x16x32_bf16 v[172:175],a[244:247],v[28:31],v[172:175]
ds_read_u16 v52,v52
v_pk_fma_f16 v8,v8,s55,s55
v_mfma_f32_16x16x32_bf16 v[168:171],a[244:247],v[24:27],v[168:171]
ds_read_b64_tr_b16 a[236:237],v3 offset:18544
v_rcp_f16 v9,v8
v_lshlrev_b32_sdwa v54,1,v54 dst_sel:DWORD dst_unused:UNUSED_PAD src0_sel:DWORD src1_sel:WORD_1
v_mfma_f32_16x16x32_bf16 v[216:219],a[248:251],v[24:27],v[216:219]
ds_read_u16_d16_hi v54,v54
v_lshlrev_b32_sdwa v53,1,v53 dst_sel:DWORD dst_unused:UNUSED_PAD src0_sel:DWORD src1_sel:WORD_1
v_mfma_f32_16x16x32_bf16 v[220:223],a[248:251],v[28:31],v[220:223]
ds_read_u16 v53,v53
ds_read_b64_tr_b16 a[238:239],v3 offset:20080
v_mfma_f32_16x16x32_bf16 v[188:191],a[252:255],v[28:31],v[188:191]
ds_read_b64_tr_b16 a[244:245],v3 offset:18552
v_lshlrev_b32_sdwa v55,1,v55 dst_sel:DWORD dst_unused:UNUSED_PAD src0_sel:DWORD src1_sel:WORD_1
v_mfma_f32_16x16x32_bf16 v[184:187],a[252:255],v[24:27],v[184:187]
ds_read_u16_d16_hi v55,v55
v_rcp_f16_sdwa v9,v8 dst_sel:WORD_1 dst_unused:UNUSED_PRESERVE src0_sel:WORD_1
v_mfma_f32_16x16x32_bf16 v[224:227],a[240:243],v[24:27],v[224:227]
ds_read_b64_tr_b16 a[246:247],v3 offset:20088
ds_read_b64_tr_b16 a[248:249],v3 offset:18608
v_lshlrev_b32_sdwa v56,1,v56 dst_sel:DWORD dst_unused:UNUSED_PAD src0_sel:DWORD src1_sel:WORD_1
v_mfma_f32_16x16x32_bf16 v[228:231],a[240:243],v[28:31],v[228:231]
ds_read_u16 v56,v56
v_lshlrev_b32_sdwa v58,1,v58 dst_sel:DWORD dst_unused:UNUSED_PAD src0_sel:DWORD src1_sel:WORD_1
v_mfma_f32_16x16x32_bf16 v[204:207],a[224:227],v[28:31],v[204:207]
ds_read_u16_d16_hi v58,v58
v_fma_mix_f32 v57,v57,v9,0 op_sel_hi:[0,1,0]
v_mfma_f32_16x16x32_bf16 v[200:203],a[224:227],v[24:27],v[200:203]
ds_read_b64_tr_b16 a[250:251],v3 offset:20144
v_fma_mix_f32 v59,v59,v9,0 op_sel:[0,1,0] op_sel_hi:[0,1,0]
v_mfma_f32_16x16x32_bf16 v[28:31],a[208:211],a[48:51],v[20:23]
s_add_u32 m0,s58,12320
ds_read_b64_tr_b16 a[252:253],v3 offset:18616
v_lshlrev_b32_sdwa v40,1,v40 dst_sel:DWORD dst_unused:UNUSED_PAD src0_sel:DWORD src1_sel:WORD_1
v_mfma_f32_16x16x32_bf16 v[28:31],a[204:207],a[64:67],v[28:31]
s_waitcnt lgkmcnt(10)
ds_read_b64_tr_b16 a[254:255],v3 offset:20152
v_lshl_add_u64 v[32:33],v[32:33],0,v[34:35]
v_mfma_f32_16x16x32_bf16 v[28:31],a[212:215],a[80:83],v[28:31]
s_add_u32 s69,s69,s59
ds_read_u16 v40,v40
v_lshl_add_u64 v[34:35],v[60:61],0,v[62:63]
v_lshlrev_b32_sdwa v42,1,v42 dst_sel:DWORD dst_unused:UNUSED_PAD src0_sel:DWORD src1_sel:WORD_1
v_mfma_f32_16x16x32_bf16 v[24:27],a[200:203],a[80:83],v[16:19]
buffer_load_dwordx4 v5,s[44:47],s69 offen offset:0 lds
v_lshlrev_b32_sdwa v41,1,v41 dst_sel:DWORD dst_unused:UNUSED_PAD src0_sel:DWORD src1_sel:WORD_1
v_mfma_f32_16x16x32_bf16 v[24:27],a[196:199],a[64:67],v[24:27]
s_waitcnt lgkmcnt(8)
ds_read_u16_d16_hi v42,v42
v_lshl_add_u64 v[60:61],v[52:53],0,v[54:55]
v_mfma_f32_16x16x32_bf16 v[24:27],a[192:195],a[48:51],v[24:27]
s_add_u32 s69,s69,s53
ds_read_u16 v41,v41
v_lshlrev_b32_sdwa v43,1,v43 dst_sel:DWORD dst_unused:UNUSED_PAD src0_sel:DWORD src1_sel:WORD_1
v_mfma_f32_16x16x32_bf16 v[52:55],a[192:195],a[52:55],v[16:19]
s_waitcnt lgkmcnt(6)
buffer_load_dwordx4 v5,s[44:47],s69 offen offset:1024 lds
v_or_b32 v62,v56,v58
v_lshlrev_b32_sdwa v36,1,v36 dst_sel:DWORD dst_unused:UNUSED_PAD src0_sel:DWORD src1_sel:WORD_1
v_mfma_f32_16x16x32_bf16 v[52:55],a[196:199],a[68:71],v[52:55]
s_add_u32 s69,s69,s53
ds_read_u16_d16_hi v43,v43
v_cvt_pk_bf16_f32 v63,v57,v59
v_lshlrev_b32_sdwa v38,1,v38 dst_sel:DWORD dst_unused:UNUSED_PAD src0_sel:DWORD src1_sel:WORD_1
v_mfma_f32_16x16x32_bf16 v[52:55],a[200:203],a[84:87],v[52:55]
buffer_load_dwordx4 v5,s[44:47],s69 offen offset:2048 lds
v_lshlrev_b32_sdwa v37,1,v37 dst_sel:DWORD dst_unused:UNUSED_PAD src0_sel:DWORD src1_sel:WORD_1
v_mfma_f32_16x16x32_bf16 v[56:59],a[212:215],a[84:87],v[20:23]
ds_read_u16 v36,v36
v_lshlrev_b32_sdwa v39,1,v39 dst_sel:DWORD dst_unused:UNUSED_PAD src0_sel:DWORD src1_sel:WORD_1
v_mfma_f32_16x16x32_bf16 v[56:59],a[204:207],a[68:71],v[56:59]
ds_read_u16_d16_hi v38,v38
v_lshlrev_b32_sdwa v48,1,v48 dst_sel:DWORD dst_unused:UNUSED_PAD src0_sel:DWORD src1_sel:WORD_1
v_mfma_f32_16x16x32_bf16 v[56:59],a[208:211],a[52:55],v[56:59]
ds_read_u16 v37,v37
v_lshlrev_b32_sdwa v50,1,v50 dst_sel:DWORD dst_unused:UNUSED_PAD src0_sel:DWORD src1_sel:WORD_1
v_mfma_f32_16x16x32_bf16 v[136:139],a[228:231],v[32:35],v[136:139]
ds_read_u16_d16_hi v39,v39
v_lshlrev_b32_sdwa v49,1,v49 dst_sel:DWORD dst_unused:UNUSED_PAD src0_sel:DWORD src1_sel:WORD_1
v_mfma_f32_16x16x32_bf16 v[140:143],a[228:231],v[60:63],v[140:143]
ds_read_u16 v48,v48
v_lshlrev_b32_sdwa v51,1,v51 dst_sel:DWORD dst_unused:UNUSED_PAD src0_sel:DWORD src1_sel:WORD_1
v_mfma_f32_16x16x32_bf16 v[68:71],a[232:235],v[60:63],v[68:71]
ds_read_u16_d16_hi v50,v50
v_lshlrev_b32_sdwa v44,1,v44 dst_sel:DWORD dst_unused:UNUSED_PAD src0_sel:DWORD src1_sel:WORD_1
v_mfma_f32_16x16x32_bf16 v[64:67],a[232:235],v[32:35],v[64:67]
ds_read_u16 v49,v49
v_lshlrev_b32_sdwa v46,1,v46 dst_sel:DWORD dst_unused:UNUSED_PAD src0_sel:DWORD src1_sel:WORD_1
v_mfma_f32_16x16x32_bf16 v[144:147],a[236:239],v[32:35],v[144:147]
ds_read_u16_d16_hi v51,v51
v_lshlrev_b32_sdwa v45,1,v45 dst_sel:DWORD dst_unused:UNUSED_PAD src0_sel:DWORD src1_sel:WORD_1
v_mfma_f32_16x16x32_bf16 v[148:151],a[236:239],v[60:63],v[148:151]
ds_read_u16 v44,v44
v_lshlrev_b32_sdwa v47,1,v47 dst_sel:DWORD dst_unused:UNUSED_PAD src0_sel:DWORD src1_sel:WORD_1
v_mfma_f32_16x16x32_bf16 v[84:87],a[244:247],v[60:63],v[84:87]
ds_read_u16_d16_hi v46,v46
v_mfma_f32_16x16x32_bf16 v[80:83],a[244:247],v[32:35],v[80:83]
s_waitcnt lgkmcnt(8)
ds_read_u16 v45,v45
v_mfma_f32_16x16x32_bf16 v[152:155],a[248:251],v[32:35],v[152:155]
ds_read_u16_d16_hi v47,v47
v_mfma_f32_16x16x32_bf16 v[156:159],a[248:251],v[60:63],v[156:159]
v_mfma_f32_16x16x32_bf16 v[100:103],a[252:255],v[60:63],v[100:103]
v_mfma_f32_16x16x32_bf16 v[96:99],a[252:255],v[32:35],v[96:99]
v_mfma_f32_16x16x32_bf16 v[60:63],a[208:211],a[56:59],v[20:23]
v_mul_f32_dpp v136,v14,v136 row_newbcast:0 row_mask:0xf bank_mask:0xf
s_add_u32 s8,s61,0
v_mul_f32_dpp v137,v14,v137 row_newbcast:1 row_mask:0xf bank_mask:0xf
v_mul_f32_dpp v138,v14,v138 row_newbcast:2 row_mask:0xf bank_mask:0xf
v_lshlrev_b32_sdwa v28,1,v28 dst_sel:DWORD dst_unused:UNUSED_PAD src0_sel:DWORD src1_sel:WORD_1
v_mfma_f32_16x16x32_bf16 v[60:63],a[204:207],a[72:75],v[60:63]
s_nop 0
ds_read_u16 v28,v28
v_lshl_add_u64 v[40:41],v[40:41],0,v[42:43]
v_mul_f32_dpp v139,v14,v139 row_newbcast:3 row_mask:0xf bank_mask:0xf
v_mul_f32_dpp v64,v14,v64 row_newbcast:4 row_mask:0xf bank_mask:0xf
v_mul_f32_dpp v65,v14,v65 row_newbcast:5 row_mask:0xf bank_mask:0xf
v_mfma_f32_16x16x32_bf16 v[60:63],a[212:215],a[88:91],v[60:63]
s_waitcnt lgkmcnt(4)
v_lshl_add_u64 v[42:43],v[36:37],0,v[38:39]
v_mul_f32_dpp v66,v14,v66 row_newbcast:6 row_mask:0xf bank_mask:0xf
v_mul_f32_dpp v67,v14,v67 row_newbcast:7 row_mask:0xf bank_mask:0xf
v_lshlrev_b32_sdwa v30,1,v30 dst_sel:DWORD dst_unused:UNUSED_PAD src0_sel:DWORD src1_sel:WORD_1
v_cvt_pk_bf16_f32 v136,v136,v137
v_mfma_f32_16x16x32_bf16 v[32:35],a[200:203],a[88:91],v[16:19]
ds_read_u16_d16_hi v30,v30
v_cvt_pk_bf16_f32 v137,v138,v139
v_cvt_pk_bf16_f32 v138,v64,v65
v_lshlrev_b32_sdwa v29,1,v29 dst_sel:DWORD dst_unused:UNUSED_PAD src0_sel:DWORD src1_sel:WORD_1
v_cvt_pk_bf16_f32 v139,v66,v67
v_mfma_f32_16x16x32_bf16 v[32:35],a[196:199],a[72:75],v[32:35]
buffer_store_dwordx4 v[136:139],v7,s[40:43],s8 offen offset:0 sc1
v_lshl_add_u64 v[36:37],v[48:49],0,v[50:51]
v_mul_f32_dpp v144,v14,v144 row_newbcast:8 row_mask:0xf bank_mask:0xf
v_mul_f32_dpp v145,v14,v145 row_newbcast:9 row_mask:0xf bank_mask:0xf
v_mul_f32_dpp v146,v14,v146 row_newbcast:10 row_mask:0xf bank_mask:0xf
v_mfma_f32_16x16x32_bf16 v[32:35],a[192:195],a[56:59],v[32:35]
ds_read_u16 v29,v29
v_mul_f32_dpp v147,v14,v147 row_newbcast:11 row_mask:0xf bank_mask:0xf
v_mul_f32_dpp v80,v14,v80 row_newbcast:12 row_mask:0xf bank_mask:0xf
v_lshlrev_b32_sdwa v31,1,v31 dst_sel:DWORD dst_unused:UNUSED_PAD src0_sel:DWORD src1_sel:WORD_1
v_mul_f32_dpp v81,v14,v81 row_newbcast:13 row_mask:0xf bank_mask:0xf
v_mfma_f32_16x16x32_bf16 v[48:51],a[192:195],a[60:63],v[16:19]
s_nop 0
ds_read_b128 v[16:19],v4 offset:0
v_mul_f32_dpp v82,v14,v82 row_newbcast:14 row_mask:0xf bank_mask:0xf
v_mul_f32_dpp v83,v14,v83 row_newbcast:15 row_mask:0xf bank_mask:0xf
v_lshlrev_b32_sdwa v24,1,v24 dst_sel:DWORD dst_unused:UNUSED_PAD src0_sel:DWORD src1_sel:WORD_1
v_cvt_pk_bf16_f32 v144,v144,v145
v_mfma_f32_16x16x32_bf16 v[48:51],a[196:199],a[76:79],v[48:51]
s_waitcnt lgkmcnt(4)
ds_read_u16_d16_hi v31,v31
ds_read_b64_tr_b16 a[192:193],v1 offset:22576
v_lshl_add_u64 v[38:39],v[44:45],0,v[46:47]
v_cvt_pk_bf16_f32 v145,v146,v147
v_cvt_pk_bf16_f32 v146,v80,v81
v_cvt_pk_bf16_f32 v147,v82,v83
v_lshlrev_b32_sdwa v26,1,v26 dst_sel:DWORD dst_unused:UNUSED_PAD src0_sel:DWORD src1_sel:WORD_1
v_mfma_f32_16x16x32_bf16 v[48:51],a[200:203],a[92:95],v[48:51]
ds_read_b64_tr_b16 a[194:195],v1 offset:23600
ds_read_u16 v24,v24
v_mul_f32_dpp v152,v15,v152 row_newbcast:0 row_mask:0xf bank_mask:0xf
v_mul_f32_dpp v153,v15,v153 row_newbcast:1 row_mask:0xf bank_mask:0xf
v_mul_f32_dpp v154,v15,v154 row_newbcast:2 row_mask:0xf bank_mask:0xf
v_mfma_f32_16x16x32_bf16 v[44:47],a[212:215],a[92:95],v[20:23]
ds_read_b128 v[20:23],v4 offset:32
ds_read_u16_d16_hi v26,v26
v_mul_f32_dpp v155,v15,v155 row_newbcast:3 row_mask:0xf bank_mask:0xf
v_mul_f32_dpp v96,v15,v96 row_newbcast:4 row_mask:0xf bank_mask:0xf
v_lshlrev_b32_sdwa v25,1,v25 dst_sel:DWORD dst_unused:UNUSED_PAD src0_sel:DWORD src1_sel:WORD_1
v_mul_f32_dpp v97,v15,v97 row_newbcast:5 row_mask:0xf bank_mask:0xf
v_mfma_f32_16x16x32_bf16 v[44:47],a[204:207],a[76:79],v[44:47]
ds_read_u16 v25,v25
ds_read_b64_tr_b16 a[196:197],v2 offset:22576
v_mul_f32_dpp v98,v15,v98 row_newbcast:6 row_mask:0xf bank_mask:0xf
v_mul_f32_dpp v99,v15,v99 row_newbcast:7 row_mask:0xf bank_mask:0xf
v_lshlrev_b32_sdwa v27,1,v27 dst_sel:DWORD dst_unused:UNUSED_PAD src0_sel:DWORD src1_sel:WORD_1
v_cvt_pk_bf16_f32 v152,v152,v153
v_mfma_f32_16x16x32_bf16 v[44:47],a[208:211],a[60:63],v[44:47]
ds_read_u16_d16_hi v27,v27
ds_read_b64_tr_b16 a[198:199],v2 offset:23600
v_cvt_pk_bf16_f32 v153,v154,v155
v_cvt_pk_bf16_f32 v154,v96,v97
v_cvt_pk_bf16_f32 v155,v98,v99
v_mfma_f32_16x16x32_bf16 v[112:115],a[228:231],v[40:43],v[112:115]
ds_read_b64_tr_b16 a[200:201],v2 offset:20528
v_mul_f32_dpp v140,v14,v140 row_newbcast:0 row_mask:0xf bank_mask:0xf
v_mul_f32_dpp v141,v14,v141 row_newbcast:1 row_mask:0xf bank_mask:0xf
v_lshlrev_b32_sdwa v56,1,v56 dst_sel:DWORD dst_unused:UNUSED_PAD src0_sel:DWORD src1_sel:WORD_1
v_mul_f32_dpp v142,v14,v142 row_newbcast:2 row_mask:0xf bank_mask:0xf
v_mfma_f32_16x16x32_bf16 v[116:119],a[228:231],v[36:39],v[116:119]
ds_read_b64_tr_b16 a[202:203],v2 offset:21552
v_mul_f32_dpp v143,v14,v143 row_newbcast:3 row_mask:0xf bank_mask:0xf
v_mul_f32_dpp v68,v14,v68 row_newbcast:4 row_mask:0xf bank_mask:0xf
v_lshlrev_b32_sdwa v58,1,v58 dst_sel:DWORD dst_unused:UNUSED_PAD src0_sel:DWORD src1_sel:WORD_1
v_mul_f32_dpp v69,v14,v69 row_newbcast:5 row_mask:0xf bank_mask:0xf
v_mfma_f32_16x16x32_bf16 v[76:79],a[232:235],v[36:39],v[76:79]
ds_read_u16 v56,v56
v_mul_f32_dpp v70,v14,v70 row_newbcast:6 row_mask:0xf bank_mask:0xf
v_mul_f32_dpp v71,v14,v71 row_newbcast:7 row_mask:0xf bank_mask:0xf
v_cvt_pk_bf16_f32 v140,v140,v141
v_lshlrev_b32_sdwa v57,1,v57 dst_sel:DWORD dst_unused:UNUSED_PAD src0_sel:DWORD src1_sel:WORD_1
v_mfma_f32_16x16x32_bf16 v[72:75],a[232:235],v[40:43],v[72:75]
TP4879_%=:
s_cbranch_execz TP7015_%=
buffer_store_dwordx4 v[144:147],v7,s[40:43],s8 offen offset:64 sc1
ds_read_u16_d16_hi v58,v58
v_cvt_pk_bf16_f32 v141,v142,v143
v_cvt_pk_bf16_f32 v142,v68,v69
v_cvt_pk_bf16_f32 v143,v70,v71
v_mfma_f32_16x16x32_bf16 v[120:123],a[236:239],v[40:43],v[120:123]
s_nop 0
ds_read_u16 v57,v57
v_mul_f32_dpp v148,v14,v148 row_newbcast:8 row_mask:0xf bank_mask:0xf
v_mul_f32_dpp v149,v14,v149 row_newbcast:9 row_mask:0xf bank_mask:0xf
v_lshlrev_b32_sdwa v59,1,v59 dst_sel:DWORD dst_unused:UNUSED_PAD src0_sel:DWORD src1_sel:WORD_1
v_mul_f32_dpp v150,v14,v150 row_newbcast:10 row_mask:0xf bank_mask:0xf
v_mfma_f32_16x16x32_bf16 v[124:127],a[236:239],v[36:39],v[124:127]
ds_read_u16_d16_hi v59,v59
v_mul_f32_dpp v151,v14,v151 row_newbcast:11 row_mask:0xf bank_mask:0xf
v_mul_f32_dpp v84,v14,v84 row_newbcast:12 row_mask:0xf bank_mask:0xf
v_lshlrev_b32_sdwa v52,1,v52 dst_sel:DWORD dst_unused:UNUSED_PAD src0_sel:DWORD src1_sel:WORD_1
v_mul_f32_dpp v85,v14,v85 row_newbcast:13 row_mask:0xf bank_mask:0xf
v_mfma_f32_16x16x32_bf16 v[92:95],a[244:247],v[36:39],v[92:95]
ds_read_b64_tr_b16 a[212:213],v2 offset:18480
v_mul_f32_dpp v86,v14,v86 row_newbcast:14 row_mask:0xf bank_mask:0xf
v_mul_f32_dpp v87,v14,v87 row_newbcast:15 row_mask:0xf bank_mask:0xf
v_cvt_pk_bf16_f32 v148,v148,v149
v_mfma_f32_16x16x32_bf16 v[88:91],a[244:247],v[40:43],v[88:91]
ds_read_b64_tr_b16 a[214:215],v2 offset:19504
v_cvt_pk_bf16_f32 v149,v150,v151
v_cvt_pk_bf16_f32 v150,v84,v85
v_lshlrev_b32_sdwa v54,1,v54 dst_sel:DWORD dst_unused:UNUSED_PAD src0_sel:DWORD src1_sel:WORD_1
v_cvt_pk_bf16_f32 v151,v86,v87
v_mfma_f32_16x16x32_bf16 v[128:131],a[248:251],v[40:43],v[128:131]
ds_read_u16 v52,v52
v_mul_f32_dpp v156,v15,v156 row_newbcast:0 row_mask:0xf bank_mask:0xf
v_mul_f32_dpp v157,v15,v157 row_newbcast:1 row_mask:0xf bank_mask:0xf
v_lshlrev_b32_sdwa v53,1,v53 dst_sel:DWORD dst_unused:UNUSED_PAD src0_sel:DWORD src1_sel:WORD_1
v_mul_f32_dpp v158,v15,v158 row_newbcast:2 row_mask:0xf bank_mask:0xf
v_mfma_f32_16x16x32_bf16 v[132:135],a[248:251],v[36:39],v[132:135]
ds_read_u16_d16_hi v54,v54
v_mul_f32_dpp v159,v15,v159 row_newbcast:3 row_mask:0xf bank_mask:0xf
v_mul_f32_dpp v100,v15,v100 row_newbcast:4 row_mask:0xf bank_mask:0xf
v_mul_f32_dpp v101,v15,v101 row_newbcast:5 row_mask:0xf bank_mask:0xf
v_lshlrev_b32_sdwa v55,1,v55 dst_sel:DWORD dst_unused:UNUSED_PAD src0_sel:DWORD src1_sel:WORD_1
v_mfma_f32_16x16x32_bf16 v[108:111],a[252:255],v[36:39],v[108:111]
ds_read_u16 v53,v53
v_mul_f32_dpp v102,v15,v102 row_newbcast:6 row_mask:0xf bank_mask:0xf
v_mul_f32_dpp v103,v15,v103 row_newbcast:7 row_mask:0xf bank_mask:0xf
v_cvt_pk_bf16_f32 v156,v156,v157
v_mfma_f32_16x16x32_bf16 v[104:107],a[252:255],v[40:43],v[104:107]
s_waitcnt lgkmcnt(8)
ds_read_u16_d16_hi v55,v55
v_cvt_pk_bf16_f32 v157,v158,v159
v_cvt_pk_bf16_f32 v158,v100,v101
v_cvt_pk_bf16_f32 v159,v102,v103
v_mfma_f32_16x16x32_bf16 v[36:39],a[220:223],a[48:51],v[16:19]
s_nop 0
v_mul_f32_dpp v112,v14,v112 row_newbcast:0 row_mask:0xf bank_mask:0xf
s_add_u32 s9,s61,98304
v_accvgpr_write_b32 a16,0
v_accvgpr_write_b32 a17,0
v_lshlrev_b32_sdwa v60,1,v60 dst_sel:DWORD dst_unused:UNUSED_PAD src0_sel:DWORD src1_sel:WORD_1
v_mfma_f32_16x16x32_bf16 v[36:39],a[216:219],a[64:67],v[36:39]
ds_read_u16 v60,v60
v_lshl_add_u64 v[28:29],v[28:29],0,v[30:31]
v_accvgpr_write_b32 a18,0
v_accvgpr_write_b32 a19,0
v_mul_f32_dpp v113,v14,v113 row_newbcast:1 row_mask:0xf bank_mask:0xf
v_mfma_f32_16x16x32_bf16 v[36:39],a[192:195],a[80:83],v[36:39]
v_lshl_add_u64 v[30:31],v[24:25],0,v[26:27]
v_mul_f32_dpp v114,v14,v114 row_newbcast:2 row_mask:0xf bank_mask:0xf
v_mul_f32_dpp v115,v14,v115 row_newbcast:3 row_mask:0xf bank_mask:0xf
v_lshlrev_b32_sdwa v62,1,v62 dst_sel:DWORD dst_unused:UNUSED_PAD src0_sel:DWORD src1_sel:WORD_1
v_mul_f32_dpp v72,v14,v72 row_newbcast:4 row_mask:0xf bank_mask:0xf
v_mfma_f32_16x16x32_bf16 v[40:43],a[196:199],a[80:83],v[20:23]
s_nop 0
buffer_store_dwordx4 v[152:155],v7,s[40:43],s8 offen offset:128 sc1
v_mul_f32_dpp v73,v14,v73 row_newbcast:5 row_mask:0xf bank_mask:0xf
v_mul_f32_dpp v74,v14,v74 row_newbcast:6 row_mask:0xf bank_mask:0xf
v_lshlrev_b32_sdwa v61,1,v61 dst_sel:DWORD dst_unused:UNUSED_PAD src0_sel:DWORD src1_sel:WORD_1
v_mul_f32_dpp v75,v14,v75 row_newbcast:7 row_mask:0xf bank_mask:0xf
v_mfma_f32_16x16x32_bf16 v[40:43],a[200:203],a[64:67],v[40:43]
s_waitcnt lgkmcnt(5)
ds_read_u16_d16_hi v62,v62
v_lshl_add_u64 v[24:25],v[56:57],0,v[58:59]
v_cvt_pk_bf16_f32 v112,v112,v113
v_cvt_pk_bf16_f32 v113,v114,v115
v_cvt_pk_bf16_f32 v114,v72,v73
v_mfma_f32_16x16x32_bf16 v[40:43],a[212:215],a[48:51],v[40:43]
ds_read_u16 v61,v61
v_cvt_pk_bf16_f32 v115,v74,v75
v_mul_f32_dpp v120,v14,v120 row_newbcast:8 row_mask:0xf bank_mask:0xf
v_lshlrev_b32_sdwa v63,1,v63 dst_sel:DWORD dst_unused:UNUSED_PAD src0_sel:DWORD src1_sel:WORD_1
v_accvgpr_write_b32 a24,v112
v_mfma_f32_16x16x32_bf16 v[56:59],a[212:215],a[52:55],v[20:23]
s_nop 0
ds_read_u16_d16_hi v63,v63
v_mul_f32_dpp v121,v14,v121 row_newbcast:9 row_mask:0xf bank_mask:0xf
v_mul_f32_dpp v122,v14,v122 row_newbcast:10 row_mask:0xf bank_mask:0xf
v_lshlrev_b32_sdwa v32,1,v32 dst_sel:DWORD dst_unused:UNUSED_PAD src0_sel:DWORD src1_sel:WORD_1
v_accvgpr_write_b32 a25,v113
v_mfma_f32_16x16x32_bf16 v[56:59],a[200:203],a[68:71],v[56:59]
s_waitcnt lgkmcnt(4)
ds_read_u16 v32,v32
v_lshl_add_u64 v[26:27],v[52:53],0,v[54:55]
v_mul_f32_dpp v123,v14,v123 row_newbcast:11 row_mask:0xf bank_mask:0xf
v_mul_f32_dpp v88,v14,v88 row_newbcast:12 row_mask:0xf bank_mask:0xf
v_accvgpr_write_b32 a26,v114
v_lshlrev_b32_sdwa v34,1,v34 dst_sel:DWORD dst_unused:UNUSED_PAD src0_sel:DWORD src1_sel:WORD_1
v_mfma_f32_16x16x32_bf16 v[56:59],a[196:199],a[84:87],v[56:59]
s_waitcnt vmcnt(10)
s_barrier
ds_read_u16_d16_hi v34,v34
v_mul_f32_dpp v89,v14,v89 row_newbcast:13 row_mask:0xf bank_mask:0xf
v_mul_f32_dpp v90,v14,v90 row_newbcast:14 row_mask:0xf bank_mask:0xf
v_accvgpr_write_b32 a27,v115
v_mfma_f32_16x16x32_bf16 v[52:55],a[192:195],a[84:87],v[16:19]
ds_read_b64_tr_b16 a[240:241],v3 offset:0
v_mul_f32_dpp v91,v14,v91 row_newbcast:15 row_mask:0xf bank_mask:0xf
v_cvt_pk_bf16_f32 v120,v120,v121
v_lshlrev_b32_sdwa v33,1,v33 dst_sel:DWORD dst_unused:UNUSED_PAD src0_sel:DWORD src1_sel:WORD_1
v_cvt_pk_bf16_f32 v121,v122,v123
v_mfma_f32_16x16x32_bf16 v[52:55],a[216:219],a[68:71],v[52:55]
s_add_u32 m0,s58,18480
ds_read_u16 v33,v33
v_cvt_pk_bf16_f32 v122,v88,v89
v_cvt_pk_bf16_f32 v123,v90,v91
v_lshlrev_b32_sdwa v35,1,v35 dst_sel:DWORD dst_unused:UNUSED_PAD src0_sel:DWORD src1_sel:WORD_1
v_mul_f32_dpp v128,v15,v128 row_newbcast:0 row_mask:0xf bank_mask:0xf
v_mfma_f32_16x16x32_bf16 v[52:55],a[220:223],a[52:55],v[52:55]
s_add_u32 s69,s69,s59
ds_read_u16_d16_hi v35,v35
v_accvgpr_write_b32 a28,v120
v_mul_f32_dpp v129,v15,v129 row_newbcast:1 row_mask:0xf bank_mask:0xf
v_mul_f32_dpp v130,v15,v130 row_newbcast:2 row_mask:0xf bank_mask:0xf
v_mfma_f32_16x16x32_bf16 v[232:235],a[228:231],v[28:31],v[232:235]
buffer_load_dwordx4 v5,s[44:47],s69 offen offset:0 lds
v_accvgpr_write_b32 a29,v121
v_mul_f32_dpp v131,v15,v131 row_newbcast:3 row_mask:0xf bank_mask:0xf
v_lshlrev_b32_sdwa v44,1,v44 dst_sel:DWORD dst_unused:UNUSED_PAD src0_sel:DWORD src1_sel:WORD_1
v_mul_f32_dpp v104,v15,v104 row_newbcast:4 row_mask:0xf bank_mask:0xf
v_mfma_f32_16x16x32_bf16 v[236:239],a[228:231],v[24:27],v[236:239]
s_add_u32 s69,s69,s53
ds_read_u16 v44,v44
v_accvgpr_write_b32 a30,v122
v_mul_f32_dpp v105,v15,v105 row_newbcast:5 row_mask:0xf bank_mask:0xf
v_lshlrev_b32_sdwa v46,1,v46 dst_sel:DWORD dst_unused:UNUSED_PAD src0_sel:DWORD src1_sel:WORD_1
v_mul_f32_dpp v106,v15,v106 row_newbcast:6 row_mask:0xf bank_mask:0xf
v_mfma_f32_16x16x32_bf16 v[164:167],a[232:235],v[24:27],v[164:167]
buffer_load_dwordx4 v5,s[44:47],s69 offen offset:1024 lds
ds_read_u16_d16_hi v46,v46
v_accvgpr_write_b32 a31,v123
v_mul_f32_dpp v107,v15,v107 row_newbcast:7 row_mask:0xf bank_mask:0xf
v_cvt_pk_bf16_f32 v128,v128,v129
v_lshlrev_b32_sdwa v45,1,v45 dst_sel:DWORD dst_unused:UNUSED_PAD src0_sel:DWORD src1_sel:WORD_1
v_mfma_f32_16x16x32_bf16 v[160:163],a[232:235],v[28:31],v[160:163]
s_nop 0
buffer_store_dwordx4 v[140:143],v7,s[40:43],s8 offen offset:192 sc1
ds_read_u16 v45,v45
v_cvt_pk_bf16_f32 v129,v130,v131
v_cvt_pk_bf16_f32 v130,v104,v105
v_cvt_pk_bf16_f32 v131,v106,v107
v_mfma_f32_16x16x32_bf16 v[240:243],a[236:239],v[28:31],v[240:243]
s_add_u32 s69,s69,s53
ds_read_b64_tr_b16 a[242:243],v3 offset:1536
v_mul_f32_dpp v116,v14,v116 row_newbcast:0 row_mask:0xf bank_mask:0xf
v_accvgpr_write_b32 a32,v128
v_lshlrev_b32_sdwa v47,1,v47 dst_sel:DWORD dst_unused:UNUSED_PAD src0_sel:DWORD src1_sel:WORD_1
v_mul_f32_dpp v117,v14,v117 row_newbcast:1 row_mask:0xf bank_mask:0xf
v_mfma_f32_16x16x32_bf16 v[244:247],a[236:239],v[24:27],v[244:247]
buffer_load_dwordx4 v5,s[44:47],s69 offen offset:2048 lds
v_mul_f32_dpp v118,v14,v118 row_newbcast:2 row_mask:0xf bank_mask:0xf
v_accvgpr_write_b32 a33,v129
v_lshlrev_b32_sdwa v48,1,v48 dst_sel:DWORD dst_unused:UNUSED_PAD src0_sel:DWORD src1_sel:WORD_1
v_mul_f32_dpp v119,v14,v119 row_newbcast:3 row_mask:0xf bank_mask:0xf
v_mfma_f32_16x16x32_bf16 v[180:183],a[244:247],v[24:27],v[180:183]
ds_read_u16_d16_hi v47,v47
v_mul_f32_dpp v76,v14,v76 row_newbcast:4 row_mask:0xf bank_mask:0xf
v_accvgpr_write_b32 a34,v130
v_mul_f32_dpp v77,v14,v77 row_newbcast:5 row_mask:0xf bank_mask:0xf
v_mfma_f32_16x16x32_bf16 v[176:179],a[244:247],v[28:31],v[176:179]
ds_read_u16 v48,v48
v_mul_f32_dpp v78,v14,v78 row_newbcast:6 row_mask:0xf bank_mask:0xf
v_accvgpr_write_b32 a35,v131
v_lshlrev_b32_sdwa v50,1,v50 dst_sel:DWORD dst_unused:UNUSED_PAD src0_sel:DWORD src1_sel:WORD_1
v_mul_f32_dpp v79,v14,v79 row_newbcast:7 row_mask:0xf bank_mask:0xf
v_mfma_f32_16x16x32_bf16 v[248:251],a[248:251],v[28:31],v[248:251]
ds_read_u16_d16_hi v50,v50
v_cvt_pk_bf16_f32 v116,v116,v117
v_cvt_pk_bf16_f32 v117,v118,v119
v_lshlrev_b32_sdwa v49,1,v49 dst_sel:DWORD dst_unused:UNUSED_PAD src0_sel:DWORD src1_sel:WORD_1
v_cvt_pk_bf16_f32 v118,v76,v77
v_mfma_f32_16x16x32_bf16 v[252:255],a[248:251],v[24:27],v[252:255]
ds_read_u16 v49,v49
v_cvt_pk_bf16_f32 v119,v78,v79
v_mfma_f32_32x32x16_bf16 v[64:79],a[16:19],a[16:19],0
v_mul_f32_dpp v124,v14,v124 row_newbcast:8 row_mask:0xf bank_mask:0xf
v_lshlrev_b32_sdwa v51,1,v51 dst_sel:DWORD dst_unused:UNUSED_PAD src0_sel:DWORD src1_sel:WORD_1
v_accvgpr_write_b32 a36,v116
v_mul_f32_dpp v125,v14,v125 row_newbcast:9 row_mask:0xf bank_mask:0xf
v_mul_f32_dpp v126,v14,v126 row_newbcast:10 row_mask:0xf bank_mask:0xf
v_mfma_f32_16x16x32_bf16 v[196:199],a[252:255],v[24:27],v[196:199]
ds_read_u16_d16_hi v51,v51
v_accvgpr_write_b32 a37,v117
v_mfma_f32_16x16x32_bf16 v[192:195],a[252:255],v[28:31],v[192:195]
s_nop 0
ds_read_b64_tr_b16 a[224:225],v3 offset:8
v_mul_f32_dpp v127,v14,v127 row_newbcast:11 row_mask:0xf bank_mask:0xf
v_mul_f32_dpp v92,v14,v92 row_newbcast:12 row_mask:0xf bank_mask:0xf
ds_read_b64_tr_b16 a[226:227],v3 offset:1544
s_mov_b64 exec,0
s_cmp_eq_u32 s52,0
s_cbranch_scc1 TE10_%=
s_cmp_eq_u32 s52,1
s_cbranch_scc1 TE11_%=
s_cmp_eq_u32 s52,2
s_cbranch_scc1 TE12_%=
s_branch TE13_%=
TR1_%=:
s_mov_b64 exec,-1
v_add_u32_sdwa v28,s60,v10 dst_sel:DWORD dst_unused:UNUSED_PAD src0_sel:DWORD src1_sel:WORD_1
v_subrev_u32 v8,88400,v28
v_min_u32_e64 v28,v28,v8
v_add_u32 v29,7072,v28
v_subrev_u32 v8,88400,v29
v_min_u32_e64 v29,v29,v8
s_waitcnt lgkmcnt(15)
v_lshl_add_u64 v[60:61],v[60:61],0,v[62:63]
s_waitcnt lgkmcnt(11)
v_lshl_add_u64 v[62:63],v[32:33],0,v[34:35]
s_waitcnt lgkmcnt(6)
v_lshl_add_u64 v[32:33],v[44:45],0,v[46:47]
s_waitcnt lgkmcnt(2)
v_lshl_add_u64 v[34:35],v[48:49],0,v[50:51]
v_mfma_f32_16x16x32_bf16 v[208:211],a[228:231],v[60:63],v[208:211]
v_xad_u32 v24,v11,0,v28
v_xad_u32 v25,v11,8,v29
v_xad_u32 v26,v11,16,v28
v_xad_u32 v27,v11,24,v29
v_mfma_f32_16x16x32_bf16 v[212:215],a[228:231],v[32:35],v[212:215]
ds_read_b64 a[0:1],v24 offset:0
v_accvgpr_write_b32 a38,v118
v_mul_f32_dpp v93,v14,v93 row_newbcast:13 row_mask:0xf bank_mask:0xf
v_mul_f32_dpp v94,v14,v94 row_newbcast:14 row_mask:0xf bank_mask:0xf
v_accvgpr_write_b32 a39,v119
v_mfma_f32_16x16x32_bf16 v[172:175],a[232:235],v[32:35],v[172:175]
buffer_store_dwordx4 v[148:151],v7,s[40:43],s8 offen offset:256 sc1
ds_read_b64 a[2:3],v24 offset:192
v_mul_f32_dpp v95,v14,v95 row_newbcast:15 row_mask:0xf bank_mask:0xf
v_cvt_pk_bf16_f32 v124,v124,v125
v_cvt_pk_bf16_f32 v125,v126,v127
v_mfma_f32_16x16x32_bf16 v[168:171],a[232:235],v[60:63],v[168:171]
ds_read_b64 a[4:5],v25 offset:0
v_cvt_pk_bf16_f32 v126,v92,v93
v_cvt_pk_bf16_f32 v127,v94,v95
v_mfma_f32_32x32x16_bf16 v[80:95],a[16:19],a[16:19],0
v_mfma_f32_16x16x32_bf16 v[216:219],a[236:239],v[60:63],v[216:219]
ds_read_b64 a[6:7],v25 offset:192
v_mul_f32_dpp v132,v15,v132 row_newbcast:0 row_mask:0xf bank_mask:0xf
v_accvgpr_write_b32 a40,v124
v_mul_f32_dpp v133,v15,v133 row_newbcast:1 row_mask:0xf bank_mask:0xf
v_mfma_f32_16x16x32_bf16 v[220:223],a[236:239],v[32:35],v[220:223]
ds_read_b64 a[8:9],v24 offset:8
v_mul_f32_dpp v134,v15,v134 row_newbcast:2 row_mask:0xf bank_mask:0xf
v_accvgpr_write_b32 a41,v125
v_mul_f32_dpp v135,v15,v135 row_newbcast:3 row_mask:0xf bank_mask:0xf
v_mfma_f32_16x16x32_bf16 v[188:191],a[244:247],v[32:35],v[188:191]
ds_read_b64 a[10:11],v24 offset:200
v_mul_f32_dpp v108,v15,v108 row_newbcast:4 row_mask:0xf bank_mask:0xf
v_accvgpr_write_b32 a42,v126
v_mul_f32_dpp v109,v15,v109 row_newbcast:5 row_mask:0xf bank_mask:0xf
v_mfma_f32_16x16x32_bf16 v[184:187],a[244:247],v[60:63],v[184:187]
ds_read_b64 a[12:13],v25 offset:8
v_mul_f32_dpp v110,v15,v110 row_newbcast:6 row_mask:0xf bank_mask:0xf
v_accvgpr_write_b32 a43,v127
v_mul_f32_dpp v111,v15,v111 row_newbcast:7 row_mask:0xf bank_mask:0xf
v_mfma_f32_16x16x32_bf16 v[224:227],a[248:251],v[60:63],v[224:227]
ds_read_b64 a[14:15],v25 offset:200
v_cvt_pk_bf16_f32 v132,v132,v133
v_cvt_pk_bf16_f32 v133,v134,v135
v_cvt_pk_bf16_f32 v134,v108,v109
v_mfma_f32_16x16x32_bf16 v[228:231],a[248:251],v[32:35],v[228:231]
v_cvt_pk_bf16_f32 v135,v110,v111
v_mfma_f32_32x32x16_bf16 v[96:111],a[16:19],a[16:19],0
v_accvgpr_write_b32 a44,v132
v_mfma_f32_16x16x32_bf16 v[204:207],a[252:255],v[32:35],v[204:207]
v_accvgpr_write_b32 a45,v133
v_accvgpr_write_b32 a46,v134
v_accvgpr_write_b32 a47,v135
v_mfma_f32_16x16x32_bf16 v[200:203],a[252:255],v[60:63],v[200:203]
v_mfma_f32_32x32x16_bf16 v[112:127],a[16:19],a[16:19],0
v_mov_b64 v[44:45],0
v_mov_b64 v[46:47],0
ds_read_b64 a[16:17],v26 offset:0
ds_read_b64 a[18:19],v26 offset:192
ds_read_b64 a[20:21],v27 offset:0
ds_read_b64 a[22:23],v27 offset:192
s_waitcnt lgkmcnt(8)
v_smfmac_f32_16x16x64_bf16 v[64:67],a[96:99],a[0:7],v10
s_waitcnt lgkmcnt(4)
ds_read_b64 a[0:1],v26 offset:8
ds_read_b64 a[2:3],v26 offset:200
ds_read_b64 a[4:5],v27 offset:8
ds_read_b64 a[6:7],v27 offset:200
v_smfmac_f32_16x16x64_bf16 v[68:71],a[100:103],a[8:15],v10
s_waitcnt lgkmcnt(4)
ds_read_b64 a[8:9],v24 offset:64
ds_read_b64 a[10:11],v24 offset:256
ds_read_b64 a[12:13],v25 offset:64
ds_read_b64 a[14:15],v25 offset:256
v_smfmac_f32_16x16x64_bf16 v[72:75],a[104:107],a[16:23],v10
s_waitcnt lgkmcnt(4)
buffer_store_dwordx4 v[156:159],v7,s[40:43],s8 offen offset:320 sc1
ds_read_b64 a[16:17],v24 offset:72
ds_read_b64 a[18:19],v24 offset:264
ds_read_b64 a[20:21],v25 offset:72
ds_read_b64 a[22:23],v25 offset:264
v_and_b32 v60,8,v11
v_pk_mul_f32 v[32:33],v[64:65],v[64:65]
v_pk_mul_f32 v[34:35],v[66:67],v[66:67]
v_smfmac_f32_16x16x64_bf16 v[76:79],a[108:111],a[0:7],v10
s_waitcnt lgkmcnt(4)
ds_read_b64 a[0:1],v26 offset:64
ds_read_b64 a[2:3],v26 offset:256
ds_read_b64 a[4:5],v27 offset:64
ds_read_b64 a[6:7],v27 offset:256
v_mul_u32_u24 v60,884,v60
s_add_u32 s64,s60,3536
v_pk_add_f32 v[48:49],v[64:65],v[68:69]
v_pk_fma_f32 v[32:33],v[68:69],v[68:69],v[32:33]
v_pk_add_f32 v[50:51],v[66:67],v[70:71]
v_pk_fma_f32 v[34:35],v[70:71],v[70:71],v[34:35]
v_smfmac_f32_16x16x64_bf16 v[80:83],a[128:131],a[8:15],v10
s_waitcnt lgkmcnt(4)
ds_read_b64 a[8:9],v26 offset:72
ds_read_b64 a[10:11],v26 offset:264
ds_read_b64 a[12:13],v27 offset:72
ds_read_b64 a[14:15],v27 offset:264
v_add_u32 v63,s64,v60
v_pk_add_f32 v[48:49],v[48:49],v[72:73]
v_pk_fma_f32 v[32:33],v[72:73],v[72:73],v[32:33]
v_pk_add_f32 v[50:51],v[50:51],v[74:75]
v_pk_fma_f32 v[34:35],v[74:75],v[74:75],v[34:35]
v_smfmac_f32_16x16x64_bf16 v[84:87],a[132:135],a[16:23],v10
s_waitcnt lgkmcnt(4)
ds_read_b64 a[16:17],v24 offset:128
ds_read_b64 a[18:19],v24 offset:320
ds_read_b64 a[20:21],v25 offset:128
ds_read_b64 a[22:23],v25 offset:320
v_subrev_u32 v8,88400,v63
v_pk_add_f32 v[48:49],v[48:49],v[76:77]
v_pk_fma_f32 v[32:33],v[76:77],v[76:77],v[32:33]
v_pk_add_f32 v[50:51],v[50:51],v[78:79]
v_pk_fma_f32 v[34:35],v[78:79],v[78:79],v[34:35]
v_smfmac_f32_16x16x64_bf16 v[88:91],a[136:139],a[0:7],v10
s_waitcnt lgkmcnt(4)
ds_read_b64 a[0:1],v24 offset:136
ds_read_b64 a[2:3],v24 offset:328
ds_read_b64 a[4:5],v25 offset:136
ds_read_b64 a[6:7],v25 offset:328
v_min_u32 v63,v63,v8
v_pk_add_f32 v[48:49],v[48:49],v[80:81]
v_pk_fma_f32 v[32:33],v[80:81],v[80:81],v[32:33]
v_pk_add_f32 v[50:51],v[50:51],v[82:83]
v_pk_fma_f32 v[34:35],v[82:83],v[82:83],v[34:35]
v_smfmac_f32_16x16x64_bf16 v[92:95],a[140:143],a[8:15],v10
s_waitcnt lgkmcnt(4)
ds_read_b64 a[8:9],v26 offset:128
ds_read_b64 a[10:11],v26 offset:320
ds_read_b64 a[12:13],v27 offset:128
ds_read_b64 a[14:15],v27 offset:320
v_xad_u32 v24,v11,32,v28
v_xad_u32 v25,v11,40,v29
v_pk_add_f32 v[48:49],v[48:49],v[84:85]
v_pk_fma_f32 v[32:33],v[84:85],v[84:85],v[32:33]
v_pk_add_f32 v[50:51],v[50:51],v[86:87]
v_pk_fma_f32 v[34:35],v[86:87],v[86:87],v[34:35]
v_smfmac_f32_16x16x64_bf16 v[96:99],a[160:163],a[16:23],v10
s_waitcnt lgkmcnt(4)
ds_read_b64 a[16:17],v26 offset:136
ds_read_b64 a[18:19],v26 offset:328
ds_read_b64 a[20:21],v27 offset:136
ds_read_b64 a[22:23],v27 offset:328
v_add_u32 v62,3536,v63
v_pk_add_f32 v[48:49],v[48:49],v[88:89]
v_pk_fma_f32 v[32:33],v[88:89],v[88:89],v[32:33]
v_pk_add_f32 v[50:51],v[50:51],v[90:91]
v_pk_fma_f32 v[34:35],v[90:91],v[90:91],v[34:35]
v_smfmac_f32_16x16x64_bf16 v[100:103],a[164:167],a[0:7],v10
s_waitcnt lgkmcnt(4)
buffer_store_dwordx4 a[24:27],v7,s[40:43],s9 offen offset:0 sc1
ds_read_b64 a[0:1],v24 offset:0
ds_read_b64 a[2:3],v24 offset:192
ds_read_b64 a[4:5],v25 offset:0
ds_read_b64 a[6:7],v25 offset:192
v_xad_u32 v26,v11,48,v28
v_xad_u32 v27,v11,56,v29
v_pk_add_f32 v[48:49],v[48:49],v[92:93]
v_pk_fma_f32 v[32:33],v[92:93],v[92:93],v[32:33]
v_pk_add_f32 v[50:51],v[50:51],v[94:95]
v_pk_fma_f32 v[34:35],v[94:95],v[94:95],v[34:35]
v_smfmac_f32_16x16x64_bf16 v[104:107],a[168:171],a[8:15],v10
s_waitcnt lgkmcnt(4)
ds_read_b64 a[8:9],v24 offset:8
ds_read_b64 a[10:11],v24 offset:200
ds_read_b64 a[12:13],v25 offset:8
ds_read_b64 a[14:15],v25 offset:200
v_subrev_u32 v8,88400,v62
v_pk_add_f32 v[48:49],v[48:49],v[96:97]
v_pk_fma_f32 v[32:33],v[96:97],v[96:97],v[32:33]
v_pk_add_f32 v[50:51],v[50:51],v[98:99]
v_pk_fma_f32 v[34:35],v[98:99],v[98:99],v[34:35]
v_smfmac_f32_16x16x64_bf16 v[108:111],a[172:175],a[16:23],v10
s_waitcnt lgkmcnt(4)
ds_read_b64 a[16:17],v24 offset:64
ds_read_b64 a[18:19],v24 offset:256
ds_read_b64 a[20:21],v25 offset:64
ds_read_b64 a[22:23],v25 offset:256
v_min_u32 v62,v62,v8
v_pk_add_f32 v[48:49],v[48:49],v[100:101]
v_pk_fma_f32 v[32:33],v[100:101],v[100:101],v[32:33]
v_pk_add_f32 v[50:51],v[50:51],v[102:103]
v_pk_fma_f32 v[34:35],v[102:103],v[102:103],v[34:35]
v_smfmac_f32_16x16x64_bf16 v[112:115],a[112:115],a[0:7],v10
s_waitcnt lgkmcnt(4)
ds_read_b64 a[0:1],v24 offset:72
ds_read_b64 a[2:3],v24 offset:264
ds_read_b64 a[4:5],v25 offset:72
ds_read_b64 a[6:7],v25 offset:264
v_xor_b32 v9,0x80008,v12
v_pk_add_f32 v[48:49],v[48:49],v[104:105]
v_pk_fma_f32 v[32:33],v[104:105],v[104:105],v[32:33]
v_pk_add_f32 v[50:51],v[50:51],v[106:107]
v_pk_fma_f32 v[34:35],v[106:107],v[106:107],v[34:35]
v_mfma_f32_32x32x16_bf16 v[128:143],v[44:47],v[44:47],0
v_smfmac_f32_16x16x64_bf16 v[116:119],a[116:119],a[8:15],v10
s_waitcnt lgkmcnt(4)
ds_read_b64 a[8:9],v24 offset:128
ds_read_b64 a[10:11],v24 offset:320
ds_read_b64 a[12:13],v25 offset:128
ds_read_b64 a[14:15],v25 offset:320
v_add_u32_sdwa v60,v12,v63 dst_sel:DWORD dst_unused:UNUSED_PAD src0_sel:WORD_0 src1_sel:DWORD
v_pk_add_f32 v[48:49],v[48:49],v[108:109]
v_pk_fma_f32 v[32:33],v[108:109],v[108:109],v[32:33]
v_pk_add_f32 v[50:51],v[50:51],v[110:111]
v_pk_fma_f32 v[34:35],v[110:111],v[110:111],v[34:35]
v_smfmac_f32_16x16x64_bf16 v[120:123],a[144:147],a[16:23],v10
s_waitcnt lgkmcnt(4)
ds_read_b64 a[16:17],v24 offset:136
ds_read_b64 a[18:19],v24 offset:328
ds_read_b64 a[20:21],v25 offset:136
ds_read_b64 a[22:23],v25 offset:328
v_add_u32_sdwa v61,v12,v63 dst_sel:DWORD dst_unused:UNUSED_PAD src0_sel:WORD_1 src1_sel:DWORD
v_pk_add_f32 v[48:49],v[48:49],v[112:113]
v_pk_fma_f32 v[32:33],v[112:113],v[112:113],v[32:33]
v_pk_add_f32 v[50:51],v[50:51],v[114:115]
v_pk_fma_f32 v[34:35],v[114:115],v[114:115],v[34:35]
v_smfmac_f32_16x16x64_bf16 v[124:127],a[148:151],a[0:7],v10
s_waitcnt lgkmcnt(4)
buffer_store_dwordx4 a[28:31],v7,s[40:43],s9 offen offset:64 sc1
ds_read_b64 a[0:1],v26 offset:0
ds_read_b64 a[2:3],v26 offset:192
ds_read_b64 a[4:5],v27 offset:0
ds_read_b64 a[6:7],v27 offset:192
v_add_u32_sdwa v63,v9,v62 dst_sel:DWORD dst_unused:UNUSED_PAD src0_sel:WORD_1 src1_sel:DWORD
v_pk_add_f32 v[48:49],v[48:49],v[116:117]
v_pk_fma_f32 v[32:33],v[116:117],v[116:117],v[32:33]
v_pk_add_f32 v[50:51],v[50:51],v[118:119]
v_pk_fma_f32 v[34:35],v[118:119],v[118:119],v[34:35]
v_smfmac_f32_16x16x64_bf16 v[128:131],a[176:179],a[8:15],v10
s_waitcnt lgkmcnt(4)
ds_read_b64 a[8:9],v26 offset:8
ds_read_b64 a[10:11],v26 offset:200
ds_read_b64 a[12:13],v27 offset:8
ds_read_b64 a[14:15],v27 offset:200
v_add_u32_sdwa v62,v9,v62 dst_sel:DWORD dst_unused:UNUSED_PAD src0_sel:WORD_0 src1_sel:DWORD
v_pk_add_f32 v[48:49],v[48:49],v[120:121]
v_pk_fma_f32 v[32:33],v[120:121],v[120:121],v[32:33]
v_pk_add_f32 v[50:51],v[50:51],v[122:123]
v_pk_fma_f32 v[34:35],v[122:123],v[122:123],v[34:35]
v_mfma_f32_32x32x16_bf16 v[144:159],v[44:47],v[44:47],0
v_smfmac_f32_16x16x64_bf16 v[132:135],a[180:183],a[16:23],v10
s_waitcnt lgkmcnt(4)
ds_read_b64 a[16:17],v26 offset:64
ds_read_b64 a[18:19],v26 offset:256
ds_read_b64 a[20:21],v27 offset:64
ds_read_b64 a[22:23],v27 offset:256
v_pk_add_f32 v[48:49],v[48:49],v[124:125]
v_pk_fma_f32 v[32:33],v[124:125],v[124:125],v[32:33]
v_pk_add_f32 v[50:51],v[50:51],v[126:127]
v_pk_fma_f32 v[34:35],v[126:127],v[126:127],v[34:35]
v_smfmac_f32_16x16x64_bf16 v[136:139],a[120:123],a[0:7],v10
s_waitcnt lgkmcnt(4)
ds_read_b64 a[0:1],v26 offset:72
ds_read_b64 a[2:3],v26 offset:264
ds_read_b64 a[4:5],v27 offset:72
ds_read_b64 a[6:7],v27 offset:264
v_pk_add_f32 v[48:49],v[48:49],v[128:129]
v_pk_fma_f32 v[32:33],v[128:129],v[128:129],v[32:33]
v_pk_add_f32 v[50:51],v[50:51],v[130:131]
v_pk_fma_f32 v[34:35],v[130:131],v[130:131],v[34:35]
v_smfmac_f32_16x16x64_bf16 v[140:143],a[124:127],a[8:15],v10
s_waitcnt lgkmcnt(4)
ds_read_b64 a[8:9],v26 offset:128
ds_read_b64 a[10:11],v26 offset:320
ds_read_b64 a[12:13],v27 offset:128
ds_read_b64 a[14:15],v27 offset:320
v_pk_add_f32 v[48:49],v[48:49],v[132:133]
v_pk_fma_f32 v[32:33],v[132:133],v[132:133],v[32:33]
v_pk_add_f32 v[50:51],v[50:51],v[134:135]
v_pk_fma_f32 v[34:35],v[134:135],v[134:135],v[34:35]
v_smfmac_f32_16x16x64_bf16 v[144:147],a[152:155],a[16:23],v10
s_waitcnt lgkmcnt(4)
ds_read_b64 a[16:17],v26 offset:136
ds_read_b64 a[18:19],v26 offset:328
ds_read_b64 a[20:21],v27 offset:136
ds_read_b64 a[22:23],v27 offset:328
v_pk_add_f32 v[48:49],v[48:49],v[136:137]
v_pk_fma_f32 v[32:33],v[136:137],v[136:137],v[32:33]
v_pk_add_f32 v[50:51],v[50:51],v[138:139]
v_pk_fma_f32 v[34:35],v[138:139],v[138:139],v[34:35]
v_smfmac_f32_16x16x64_bf16 v[148:151],a[156:159],a[0:7],v10
s_waitcnt lgkmcnt(4)
ds_read_b64 a[228:229],v60 offset:57344
ds_read_b64 a[230:231],v60 offset:57352
ds_read_b64 a[232:233],v61 offset:57344
ds_read_b64 a[234:235],v61 offset:57352
ds_read_b128 a[248:251],v13 offset:0
v_pk_add_f32 v[48:49],v[48:49],v[140:141]
v_pk_fma_f32 v[32:33],v[140:141],v[140:141],v[32:33]
v_pk_add_f32 v[50:51],v[50:51],v[142:143]
v_pk_fma_f32 v[34:35],v[142:143],v[142:143],v[34:35]
v_smfmac_f32_16x16x64_bf16 v[152:155],a[184:187],a[8:15],v10
s_waitcnt lgkmcnt(5)
buffer_store_dwordx4 a[32:35],v7,s[40:43],s9 offen offset:128 sc1
ds_read_b64 a[236:237],v62 offset:57344
ds_read_b64 a[238:239],v62 offset:57352
ds_read_b64 a[244:245],v63 offset:57344
ds_read_b64 a[246:247],v63 offset:57352
ds_read_b128 a[252:255],v13 offset:256
v_pk_add_f32 v[48:49],v[48:49],v[144:145]
TP5417_%=:
s_cbranch_execz TR0_%=
s_nop 0
v_pk_fma_f32 v[32:33],v[144:145],v[144:145],v[32:33]
v_pk_add_f32 v[50:51],v[50:51],v[146:147]
v_pk_fma_f32 v[34:35],v[146:147],v[146:147],v[34:35]
v_smfmac_f32_16x16x64_bf16 v[156:159],a[188:191],a[16:23],v10
ds_read_b128 v[24:27],v4 offset:3584
ds_read_b128 v[28:31],v4 offset:3616
v_pk_add_f32 v[48:49],v[48:49],v[148:149]
v_pk_fma_f32 v[32:33],v[148:149],v[148:149],v[32:33]
v_pk_add_f32 v[50:51],v[50:51],v[150:151]
v_pk_fma_f32 v[34:35],v[150:151],v[150:151],v[34:35]
v_pk_add_f32 v[48:49],v[48:49],v[152:153]
v_pk_fma_f32 v[32:33],v[152:153],v[152:153],v[32:33]
v_pk_add_f32 v[50:51],v[50:51],v[154:155]
v_pk_fma_f32 v[34:35],v[154:155],v[154:155],v[34:35]
v_pk_add_f32 v[48:49],v[48:49],v[156:157]
v_pk_fma_f32 v[32:33],v[156:157],v[156:157],v[32:33]
v_pk_add_f32 v[50:51],v[50:51],v[158:159]
v_pk_fma_f32 v[34:35],v[158:159],v[158:159],v[34:35]
s_nop 0
v_permlane32_swap_b32 v48,v50
v_permlane32_swap_b32 v49,v51
v_permlane32_swap_b32 v32,v34
v_permlane32_swap_b32_e64 v33,v35
v_pk_add_f32 v[48:49],v[48:49],v[50:51]
v_pk_add_f32 v[32:33],v[32:33],v[34:35]
s_add_u32 s60,s60,56576
s_sub_u32 s64,s60,88400
s_min_u32 s60,s60,s64
v_permlane16_swap_b32 v48,v49
v_permlane16_swap_b32 v32,v33
v_add_f32 v8,v48,v49
v_add_f32_e64 v9,v32,v33
v_mul_f32 v8,0x3c2aaaab,v8
v_mul_f32 v9,0x3c2aaaab,v9
v_cmp_ngt_f32 vcc,0x5d800000,v9
v_fma_f32 v9,-v8,v8,v9
v_max_f32 v9,0,v9
s_or_b64 s[90:91],s[90:91],vcc
v_add_f32 v9,0x3727c5ac,v9
v_rsq_f32 v9,v9
s_nop 0
v_mul_f32 v48,s96,v9
v_mul_f32 v49,s96,v9
v_mul_f32 v32,-v8,v48
v_mul_f32 v33,-v8,v48
v_permlane16_swap_b32 v48,v49
s_nop 0
v_permlane16_swap_b32 v32,v33
v_mov_b64 v[50:51],v[48:49]
v_mov_b64 v[34:35],v[32:33]
s_nop 0
v_permlane32_swap_b32 v48,v50
v_permlane32_swap_b32 v49,v51
v_permlane32_swap_b32 v32,v34
v_permlane32_swap_b32 v33,v35
v_pk_fma_f32 v[64:65],v[64:65],v[48:49],v[32:33]
v_pk_fma_f32 v[66:67],v[66:67],v[50:51],v[34:35]
v_pk_fma_f32 v[68:69],v[68:69],v[48:49],v[32:33]
v_pk_fma_f32 v[70:71],v[70:71],v[50:51],v[34:35]
v_pk_fma_f32 v[72:73],v[72:73],v[48:49],v[32:33]
v_pk_fma_f32 v[74:75],v[74:75],v[50:51],v[34:35]
v_pk_fma_f32 v[76:77],v[76:77],v[48:49],v[32:33]
v_pk_fma_f32 v[78:79],v[78:79],v[50:51],v[34:35]
v_pk_fma_f32 v[112:113],v[112:113],v[48:49],v[32:33]
v_pk_fma_f32 v[114:115],v[114:115],v[50:51],v[34:35]
v_pk_fma_f32 v[116:117],v[116:117],v[48:49],v[32:33]
v_pk_fma_f32 v[118:119],v[118:119],v[50:51],v[34:35]
v_pk_fma_f32 v[136:137],v[136:137],v[48:49],v[32:33]
v_pk_fma_f32 v[138:139],v[138:139],v[50:51],v[34:35]
v_pk_fma_f32 v[140:141],v[140:141],v[48:49],v[32:33]
v_pk_fma_f32 v[142:143],v[142:143],v[50:51],v[34:35]
v_pk_fma_f32 v[80:81],v[80:81],v[48:49],v[32:33]
v_pk_fma_f32 v[82:83],v[82:83],v[50:51],v[34:35]
v_pk_fma_f32 v[84:85],v[84:85],v[48:49],v[32:33]
v_pk_fma_f32 v[86:87],v[86:87],v[50:51],v[34:35]
v_pk_fma_f32 v[88:89],v[88:89],v[48:49],v[32:33]
v_pk_fma_f32 v[90:91],v[90:91],v[50:51],v[34:35]
v_pk_fma_f32 v[92:93],v[92:93],v[48:49],v[32:33]
v_pk_fma_f32 v[94:95],v[94:95],v[50:51],v[34:35]
v_pk_fma_f32 v[120:121],v[120:121],v[48:49],v[32:33]
v_pk_fma_f32 v[122:123],v[122:123],v[50:51],v[34:35]
v_pk_fma_f32 v[124:125],v[124:125],v[48:49],v[32:33]
v_pk_fma_f32 v[126:127],v[126:127],v[50:51],v[34:35]
v_pk_fma_f32 v[144:145],v[144:145],v[48:49],v[32:33]
v_pk_fma_f32 v[146:147],v[146:147],v[50:51],v[34:35]
v_pk_fma_f32 v[148:149],v[148:149],v[48:49],v[32:33]
v_pk_fma_f32 v[150:151],v[150:151],v[50:51],v[34:35]
v_pk_fma_f32 v[96:97],v[96:97],v[48:49],v[32:33]
v_pk_fma_f32 v[98:99],v[98:99],v[50:51],v[34:35]
v_pk_fma_f32 v[100:101],v[100:101],v[48:49],v[32:33]
v_pk_fma_f32 v[102:103],v[102:103],v[50:51],v[34:35]
v_pk_fma_f32 v[104:105],v[104:105],v[48:49],v[32:33]
v_pk_fma_f32 v[106:107],v[106:107],v[50:51],v[34:35]
v_pk_fma_f32 v[108:109],v[108:109],v[48:49],v[32:33]
v_pk_fma_f32 v[110:111],v[110:111],v[50:51],v[34:35]
v_pk_fma_f32 v[128:129],v[128:129],v[48:49],v[32:33]
v_pk_fma_f32 v[130:131],v[130:131],v[50:51],v[34:35]
v_pk_fma_f32 v[132:133],v[132:133],v[48:49],v[32:33]
v_pk_fma_f32 v[134:135],v[134:135],v[50:51],v[34:35]
v_pk_fma_f32 v[152:153],v[152:153],v[48:49],v[32:33]
v_pk_fma_f32 v[154:155],v[154:155],v[50:51],v[34:35]
v_pk_fma_f32 v[156:157],v[156:157],v[48:49],v[32:33]
v_pk_fma_f32 v[158:159],v[158:159],v[50:51],v[34:35]
v_mfma_f32_16x16x32_bf16 v[44:47],a[220:223],a[56:59],v[16:19]
buffer_store_dwordx4 a[36:39],v7,s[40:43],s9 offen offset:192 sc1
v_cvt_pk_bf16_f32 v64,v64,v68
v_cvt_pk_bf16_f32 v72,v72,v76
v_cvt_pk_bf16_f32 v112,v112,v116
v_lshlrev_b32_sdwa v36,1,v36 dst_sel:DWORD dst_unused:UNUSED_PAD src0_sel:DWORD src1_sel:WORD_1
v_mfma_f32_16x16x32_bf16 v[44:47],a[216:219],a[72:75],v[44:47]
ds_read_u16 v36,v36
v_accvgpr_write_b32 a0,v64
v_accvgpr_write_b32 a1,v72
v_cvt_pk_bf16_f32 v136,v136,v140
v_accvgpr_write_b32 a2,v112
v_mfma_f32_16x16x32_bf16 v[44:47],a[192:195],a[88:91],v[44:47]
ds_read_b64_tr_b16 a[204:205],v1 offset:0
v_cvt_pk_bf16_f32 v65,v65,v69
v_accvgpr_write_b32 a3,v136
v_cvt_pk_bf16_f32 v73,v73,v77
v_accvgpr_write_b32 a4,v65
v_mfma_f32_16x16x32_bf16 v[48:51],a[196:199],a[88:91],v[20:23]
ds_read_b64_tr_b16 a[206:207],v1 offset:1024
v_cvt_pk_bf16_f32 v113,v113,v117
v_accvgpr_write_b32 a5,v73
v_cvt_pk_bf16_f32 v137,v137,v141
v_lshlrev_b32_sdwa v38,1,v38 dst_sel:DWORD dst_unused:UNUSED_PAD src0_sel:DWORD src1_sel:WORD_1
v_accvgpr_write_b32 a6,v113
v_mfma_f32_16x16x32_bf16 v[48:51],a[200:203],a[72:75],v[48:51]
ds_read_u16_d16_hi v38,v38
v_cvt_pk_bf16_f32 v66,v66,v70
v_accvgpr_write_b32 a7,v137
v_cvt_pk_bf16_f32 v74,v74,v78
v_accvgpr_write_b32 a8,v66
v_mfma_f32_16x16x32_bf16 v[48:51],a[212:215],a[56:59],v[48:51]
ds_read_b64_tr_b16 a[208:209],v1 offset:2048
v_accvgpr_write_b32 a9,v74
v_cvt_pk_bf16_f32 v114,v114,v118
v_accvgpr_write_b32 a10,v114
v_lshlrev_b32_sdwa v37,1,v37 dst_sel:DWORD dst_unused:UNUSED_PAD src0_sel:DWORD src1_sel:WORD_1
v_cvt_pk_bf16_f32 v138,v138,v142
v_mfma_f32_16x16x32_bf16 v[32:35],a[212:215],a[60:63],v[20:23]
buffer_store_dwordx4 a[40:43],v7,s[40:43],s9 offen offset:256 sc1
v_accvgpr_write_b32 a11,v138
v_cvt_pk_bf16_f32 v67,v67,v71
v_accvgpr_write_b32 a12,v67
v_mfma_f32_16x16x32_bf16 v[32:35],a[200:203],a[76:79],v[32:35]
ds_read_u16 v37,v37
v_cvt_pk_bf16_f32 v75,v75,v79
v_accvgpr_write_b32 a13,v75
v_cvt_pk_bf16_f32 v115,v115,v119
v_accvgpr_write_b32 a14,v115
v_lshlrev_b32_sdwa v39,1,v39 dst_sel:DWORD dst_unused:UNUSED_PAD src0_sel:DWORD src1_sel:WORD_1
v_mfma_f32_16x16x32_bf16 v[32:35],a[196:199],a[92:95],v[32:35]
s_waitcnt lgkmcnt(6)
ds_read_u16_d16_hi v39,v39
v_cvt_pk_bf16_f32 v139,v139,v143
v_accvgpr_write_b32 a15,v139
v_cvt_pk_bf16_f32 v80,v80,v84
v_cvt_pk_bf16_f32 v88,v88,v92
v_mfma_f32_16x16x32_bf16 v[136:139],a[248:251],a[228:231],v[24:27]
s_nop 0
ds_read_b64_tr_b16 a[210:211],v1 offset:3072
v_cvt_pk_bf16_f32 v120,v120,v124
v_accvgpr_write_b32 a16,v80
v_accvgpr_write_b32 a17,v88
v_cvt_pk_bf16_f32 v144,v144,v148
v_lshlrev_b32_sdwa v40,1,v40 dst_sel:DWORD dst_unused:UNUSED_PAD src0_sel:DWORD src1_sel:WORD_1
v_mfma_f32_16x16x32_bf16 v[140:143],a[248:251],a[232:235],v[24:27]
ds_read_u16 v40,v40
v_accvgpr_write_b32 a18,v120
v_cvt_pk_bf16_f32 v81,v81,v85
v_accvgpr_write_b32 a19,v144
v_cvt_pk_bf16_f32 v89,v89,v93
v_mfma_f32_16x16x32_bf16 v[112:115],a[248:251],a[236:239],v[24:27]
v_accvgpr_write_b32 a20,v81
v_cvt_pk_bf16_f32 v121,v121,v125
v_accvgpr_write_b32 a21,v89
v_lshlrev_b32_sdwa v42,1,v42 dst_sel:DWORD dst_unused:UNUSED_PAD src0_sel:DWORD src1_sel:WORD_1
v_cvt_pk_bf16_f32 v145,v145,v149
v_mfma_f32_16x16x32_bf16 v[116:119],a[248:251],a[244:247],v[24:27]
ds_read_b128 a[248:251],v13 offset:512
ds_read_b128 v[24:27],v4 offset:3712
v_accvgpr_write_b32 a22,v121
v_cvt_pk_bf16_f32 v82,v82,v86
v_accvgpr_write_b32 a23,v145
v_mfma_f32_16x16x32_bf16 v[64:67],a[252:255],a[228:231],v[28:31]
ds_read_b64 a[228:229],v60 offset:57408
ds_read_b64 a[230:231],v60 offset:57416
v_cvt_pk_bf16_f32 v90,v90,v94
v_accvgpr_write_b32 a24,v82
v_accvgpr_write_b32 a25,v90
v_cvt_pk_bf16_f32 v122,v122,v126
v_lshlrev_b32_sdwa v41,1,v41 dst_sel:DWORD dst_unused:UNUSED_PAD src0_sel:DWORD src1_sel:WORD_1
v_mfma_f32_16x16x32_bf16 v[68:71],a[252:255],a[232:235],v[28:31]
ds_read_b64 a[232:233],v61 offset:57408
ds_read_b64 a[234:235],v61 offset:57416
v_accvgpr_write_b32 a26,v122
v_cvt_pk_bf16_f32 v146,v146,v150
v_accvgpr_write_b32 a27,v146
v_cvt_pk_bf16_f32 v83,v83,v87
v_mfma_f32_16x16x32_bf16 v[72:75],a[252:255],a[236:239],v[28:31]
ds_read_b64 a[236:237],v62 offset:57408
ds_read_b64 a[238:239],v62 offset:57416
v_accvgpr_write_b32 a28,v83
v_cvt_pk_bf16_f32 v91,v91,v95
v_accvgpr_write_b32 a29,v91
v_cvt_pk_bf16_f32 v123,v123,v127
v_lshlrev_b32_sdwa v43,1,v43 dst_sel:DWORD dst_unused:UNUSED_PAD src0_sel:DWORD src1_sel:WORD_1
v_mfma_f32_16x16x32_bf16 v[76:79],a[252:255],a[244:247],v[28:31]
s_waitcnt lgkmcnt(4)
buffer_store_dwordx4 a[44:47],v7,s[40:43],s9 offen offset:320 sc1
ds_read_b64 a[244:245],v63 offset:57408
v_accvgpr_write_b32 a30,v123
v_cvt_pk_bf16_f32 v147,v147,v151
v_accvgpr_write_b32 a31,v147
v_cvt_pk_bf16_f32 v96,v96,v100
v_mfma_f32_16x16x32_bf16 v[144:147],a[248:251],a[228:231],v[24:27]
s_waitcnt lgkmcnt(3)
ds_read_b64 a[246:247],v63 offset:57416
ds_read_b128 a[252:255],v13 offset:768
v_cvt_pk_bf16_f32 v104,v104,v108
v_cvt_pk_bf16_f32 v128,v128,v132
v_accvgpr_write_b32 a32,v96
v_accvgpr_write_b32 a33,v104
v_lshlrev_b32_sdwa v52,1,v52 dst_sel:DWORD dst_unused:UNUSED_PAD src0_sel:DWORD src1_sel:WORD_1
v_mfma_f32_16x16x32_bf16 v[148:151],a[248:251],a[232:235],v[24:27]
s_waitcnt lgkmcnt(3)
ds_read_b128 v[28:31],v4 offset:3744
ds_read_u16_d16_hi v42,v42
v_cvt_pk_bf16_f32 v152,v152,v156
v_accvgpr_write_b32 a34,v128
v_cvt_pk_bf16_f32 v97,v97,v101
v_mfma_f32_16x16x32_bf16 v[120:123],a[248:251],a[236:239],v[24:27]
s_waitcnt lgkmcnt(3)
ds_read_u16 v41,v41
v_accvgpr_write_b32 a35,v152
v_cvt_pk_bf16_f32 v105,v105,v109
v_accvgpr_write_b32 a36,v97
v_cvt_pk_bf16_f32 v129,v129,v133
v_lshlrev_b32_sdwa v54,1,v54 dst_sel:DWORD dst_unused:UNUSED_PAD src0_sel:DWORD src1_sel:WORD_1
v_mfma_f32_16x16x32_bf16 v[124:127],a[248:251],a[244:247],v[24:27]
s_waitcnt lgkmcnt(2)
ds_read_b128 a[248:251],v13 offset:1024
ds_read_b128 v[24:27],v4 offset:3840
v_accvgpr_write_b32 a37,v105
v_cvt_pk_bf16_f32 v153,v153,v157
v_accvgpr_write_b32 a38,v129
v_cvt_pk_bf16_f32 v98,v98,v102
v_mfma_f32_16x16x32_bf16 v[80:83],a[252:255],a[228:231],v[28:31]
s_nop 0
ds_read_b64 a[228:229],v60 offset:57472
ds_read_b64 a[230:231],v60 offset:57480
v_accvgpr_write_b32 a39,v153
v_cvt_pk_bf16_f32 v106,v106,v110
v_accvgpr_write_b32 a40,v98
v_accvgpr_write_b32 a41,v106
v_lshlrev_b32_sdwa v53,1,v53 dst_sel:DWORD dst_unused:UNUSED_PAD src0_sel:DWORD src1_sel:WORD_1
v_mfma_f32_16x16x32_bf16 v[84:87],a[252:255],a[232:235],v[28:31]
ds_read_b64 a[232:233],v61 offset:57472
ds_read_b64 a[234:235],v61 offset:57480
v_cvt_pk_bf16_f32 v130,v130,v134
v_accvgpr_write_b32 a42,v130
v_cvt_pk_bf16_f32 v154,v154,v158
v_accvgpr_write_b32 a43,v154
v_mfma_f32_16x16x32_bf16 v[88:91],a[252:255],a[236:239],v[28:31]
s_nop 0
ds_read_b64 a[236:237],v62 offset:57472
ds_read_b64 a[238:239],v62 offset:57480
v_cvt_pk_bf16_f32 v99,v99,v103
v_accvgpr_write_b32 a44,v99
v_cvt_pk_bf16_f32 v107,v107,v111
v_accvgpr_write_b32 a45,v107
v_mfma_f32_16x16x32_bf16 v[92:95],a[252:255],a[244:247],v[28:31]
s_waitcnt lgkmcnt(4)
ds_read_b64 a[244:245],v63 offset:57472
ds_read_b64 a[246:247],v63 offset:57480
v_cvt_pk_bf16_f32 v131,v131,v135
v_accvgpr_write_b32 a46,v131
v_cvt_pk_bf16_f32 v155,v155,v159
v_lshlrev_b32_sdwa v55,1,v55 dst_sel:DWORD dst_unused:UNUSED_PAD src0_sel:DWORD src1_sel:WORD_1
v_accvgpr_write_b32 a47,v155
v_mfma_f32_16x16x32_bf16 v[152:155],a[248:251],a[228:231],v[24:27]
s_waitcnt lgkmcnt(4)
ds_read_b128 a[252:255],v13 offset:1280
ds_read_b128 v[28:31],v4 offset:3872
v_mfma_f32_16x16x32_bf16 v[156:159],a[248:251],a[232:235],v[24:27]
s_waitcnt lgkmcnt(4)
ds_read_u16_d16_hi v43,v43
ds_read_u16 v52,v52
v_lshlrev_b32_sdwa v56,1,v56 dst_sel:DWORD dst_unused:UNUSED_PAD src0_sel:DWORD src1_sel:WORD_1
v_mfma_f32_16x16x32_bf16 v[128:131],a[248:251],a[236:239],v[24:27]
s_waitcnt lgkmcnt(4)
ds_read_u16_d16_hi v54,v54
v_lshlrev_b32_sdwa v58,1,v58 dst_sel:DWORD dst_unused:UNUSED_PAD src0_sel:DWORD src1_sel:WORD_1
v_mfma_f32_16x16x32_bf16 v[132:135],a[248:251],a[244:247],v[24:27]
s_waitcnt lgkmcnt(3)
ds_read_b64_tr_b16 a[248:249],v3 offset:64
ds_read_b64_tr_b16 a[250:251],v3 offset:1600
v_mfma_f32_16x16x32_bf16 v[96:99],a[252:255],a[228:231],v[28:31]
ds_read_b64_tr_b16 a[228:229],v3 offset:72
ds_read_b64_tr_b16 a[230:231],v3 offset:1608
v_mfma_f32_16x16x32_bf16 v[100:103],a[252:255],a[232:235],v[28:31]
ds_read_b64_tr_b16 a[232:233],v3 offset:128
ds_read_b64_tr_b16 a[234:235],v3 offset:1664
v_lshlrev_b32_sdwa v57,1,v57 dst_sel:DWORD dst_unused:UNUSED_PAD src0_sel:DWORD src1_sel:WORD_1
v_mfma_f32_16x16x32_bf16 v[104:107],a[252:255],a[236:239],v[28:31]
ds_read_b64_tr_b16 a[236:237],v3 offset:136
ds_read_b64_tr_b16 a[238:239],v3 offset:1672
v_mfma_f32_16x16x32_bf16 v[108:111],a[252:255],a[244:247],v[28:31]
ds_read_u16 v53,v53
ds_read_u16_d16_hi v55,v55
v_lshlrev_b32_sdwa v59,1,v59 dst_sel:DWORD dst_unused:UNUSED_PAD src0_sel:DWORD src1_sel:WORD_1
v_mfma_f32_16x16x32_bf16 v[60:63],a[192:195],a[92:95],v[16:19]
ds_read_u16 v56,v56
v_mfma_f32_16x16x32_bf16 v[60:63],a[216:219],a[76:79],v[60:63]
s_waitcnt vmcnt(18)
ds_read_u16_d16_hi v58,v58
v_mfma_f32_16x16x32_bf16 v[60:63],a[220:223],a[60:63],v[60:63]
s_barrier
ds_read_u16 v57,v57
ds_read_u16_d16_hi v59,v59
v_mfma_f32_16x16x32_bf16 v[24:27],a[220:223],a[0:3],v[16:19]
s_add_u32 m0,s57,24752
v_lshlrev_b32_sdwa v44,1,v44 dst_sel:DWORD dst_unused:UNUSED_PAD src0_sel:DWORD src1_sel:WORD_1
v_lshlrev_b32_sdwa v46,1,v46 dst_sel:DWORD dst_unused:UNUSED_PAD src0_sel:DWORD src1_sel:WORD_1
v_mfma_f32_16x16x32_bf16 v[24:27],a[216:219],a[16:19],v[24:27]
s_add_u32 s89,s56,5603328
ds_read_u16 v44,v44
v_lshl_add_u64 v[36:37],v[36:37],0,v[38:39]
v_mfma_f32_16x16x32_bf16 v[24:27],a[192:195],a[32:35],v[24:27]
s_waitcnt lgkmcnt(11)
buffer_load_dwordx4 v6,s[36:39],s89 offen lds
v_lshl_add_u64 v[38:39],v[40:41],0,v[42:43]
v_mfma_f32_16x16x32_bf16 v[28:31],a[196:199],a[32:35],v[20:23]
ds_read_u16_d16_hi v46,v46
v_lshlrev_b32_sdwa v45,1,v45 dst_sel:DWORD dst_unused:UNUSED_PAD src0_sel:DWORD src1_sel:WORD_1
v_mfma_f32_16x16x32_bf16 v[28:31],a[200:203],a[16:19],v[28:31]
s_waitcnt lgkmcnt(6)
ds_read_u16 v45,v45
v_lshl_add_u64 v[40:41],v[52:53],0,v[54:55]
v_mfma_f32_16x16x32_bf16 v[28:31],a[212:215],a[0:3],v[28:31]
v_lshlrev_b32_sdwa v47,1,v47 dst_sel:DWORD dst_unused:UNUSED_PAD src0_sel:DWORD src1_sel:WORD_1
v_lshlrev_b32_sdwa v48,1,v48 dst_sel:DWORD dst_unused:UNUSED_PAD src0_sel:DWORD src1_sel:WORD_1
v_mfma_f32_16x16x32_bf16 v[52:55],a[212:215],a[4:7],v[20:23]
s_nop 0
ds_read_u16_d16_hi v47,v47
v_lshlrev_b32_sdwa v50,1,v50 dst_sel:DWORD dst_unused:UNUSED_PAD src0_sel:DWORD src1_sel:WORD_1
v_mfma_f32_16x16x32_bf16 v[52:55],a[200:203],a[20:23],v[52:55]
s_waitcnt lgkmcnt(4)
ds_read_u16 v48,v48
v_lshl_add_u64 v[42:43],v[56:57],0,v[58:59]
v_mfma_f32_16x16x32_bf16 v[52:55],a[196:199],a[36:39],v[52:55]
ds_read_u16_d16_hi v50,v50
v_lshlrev_b32_sdwa v49,1,v49 dst_sel:DWORD dst_unused:UNUSED_PAD src0_sel:DWORD src1_sel:WORD_1
v_mfma_f32_16x16x32_bf16 v[56:59],a[192:195],a[36:39],v[16:19]
ds_read_u16 v49,v49
v_lshlrev_b32_sdwa v51,1,v51 dst_sel:DWORD dst_unused:UNUSED_PAD src0_sel:DWORD src1_sel:WORD_1
v_mfma_f32_16x16x32_bf16 v[56:59],a[216:219],a[20:23],v[56:59]
ds_read_u16_d16_hi v51,v51
v_lshlrev_b32_sdwa v60,1,v60 dst_sel:DWORD dst_unused:UNUSED_PAD src0_sel:DWORD src1_sel:WORD_1
v_mfma_f32_16x16x32_bf16 v[56:59],a[220:223],a[4:7],v[56:59]
ds_read_u16 v60,v60
v_lshlrev_b32_sdwa v62,1,v62 dst_sel:DWORD dst_unused:UNUSED_PAD src0_sel:DWORD src1_sel:WORD_1
v_mfma_f32_16x16x32_bf16 v[232:235],a[240:243],v[36:39],v[232:235]
ds_read_u16_d16_hi v62,v62
v_lshlrev_b32_sdwa v61,1,v61 dst_sel:DWORD dst_unused:UNUSED_PAD src0_sel:DWORD src1_sel:WORD_1
v_mfma_f32_16x16x32_bf16 v[236:239],a[240:243],v[40:43],v[236:239]
ds_read_u16 v61,v61
v_lshlrev_b32_sdwa v63,1,v63 dst_sel:DWORD dst_unused:UNUSED_PAD src0_sel:DWORD src1_sel:WORD_1
v_mfma_f32_16x16x32_bf16 v[164:167],a[224:227],v[40:43],v[164:167]
ds_read_u16_d16_hi v63,v63
v_lshlrev_b32_sdwa v32,1,v32 dst_sel:DWORD dst_unused:UNUSED_PAD src0_sel:DWORD src1_sel:WORD_1
v_mfma_f32_16x16x32_bf16 v[160:163],a[224:227],v[36:39],v[160:163]
ds_read_u16 v32,v32
v_lshlrev_b32_sdwa v34,1,v34 dst_sel:DWORD dst_unused:UNUSED_PAD src0_sel:DWORD src1_sel:WORD_1
v_mfma_f32_16x16x32_bf16 v[240:243],a[248:251],v[36:39],v[240:243]
ds_read_u16_d16_hi v34,v34
v_lshlrev_b32_sdwa v33,1,v33 dst_sel:DWORD dst_unused:UNUSED_PAD src0_sel:DWORD src1_sel:WORD_1
v_mfma_f32_16x16x32_bf16 v[244:247],a[248:251],v[40:43],v[244:247]
ds_read_u16 v33,v33
v_lshlrev_b32_sdwa v35,1,v35 dst_sel:DWORD dst_unused:UNUSED_PAD src0_sel:DWORD src1_sel:WORD_1
v_mfma_f32_16x16x32_bf16 v[180:183],a[228:231],v[40:43],v[180:183]
ds_read_u16_d16_hi v35,v35
v_mfma_f32_16x16x32_bf16 v[176:179],a[228:231],v[36:39],v[176:179]
v_mfma_f32_16x16x32_bf16 v[248:251],a[232:235],v[36:39],v[248:251]
v_mfma_f32_16x16x32_bf16 v[252:255],a[232:235],v[40:43],v[252:255]
v_mfma_f32_16x16x32_bf16 v[196:199],a[236:239],v[40:43],v[196:199]
v_mfma_f32_16x16x32_bf16 v[192:195],a[236:239],v[36:39],v[192:195]
v_mfma_f32_16x16x32_bf16 v[40:43],a[220:223],a[8:11],v[16:19]
v_lshlrev_b32_sdwa v24,1,v24 dst_sel:DWORD dst_unused:UNUSED_PAD src0_sel:DWORD src1_sel:WORD_1
v_fma_mixlo_f16 v8,v53,s99,0
v_mfma_f32_16x16x32_bf16 v[40:43],a[216:219],a[24:27],v[40:43]
s_waitcnt lgkmcnt(2)
ds_read_u16 v24,v24
v_lshl_add_u64 v[44:45],v[44:45],0,v[46:47]
v_mfma_f32_16x16x32_bf16 v[40:43],a[192:195],a[40:43],v[40:43]
v_lshl_add_u64 v[46:47],v[48:49],0,v[50:51]
v_lshlrev_b32_sdwa v26,1,v26 dst_sel:DWORD dst_unused:UNUSED_PAD src0_sel:DWORD src1_sel:WORD_1
v_mfma_f32_16x16x32_bf16 v[36:39],a[196:199],a[40:43],v[20:23]
ds_read_u16_d16_hi v26,v26
v_fma_mixhi_f16 v8,v55,s99,0
v_mfma_f32_16x16x32_bf16 v[36:39],a[200:203],a[24:27],v[36:39]
v_lshl_add_u64 v[48:49],v[60:61],0,v[62:63]
v_lshlrev_b32_sdwa v25,1,v25 dst_sel:DWORD dst_unused:UNUSED_PAD src0_sel:DWORD src1_sel:WORD_1
v_pk_fma_f16 v9,v8,v8,s54
v_mfma_f32_16x16x32_bf16 v[36:39],a[212:215],a[8:11],v[36:39]
ds_read_u16 v25,v25
v_lshlrev_b32_sdwa v27,1,v27 dst_sel:DWORD dst_unused:UNUSED_PAD src0_sel:DWORD src1_sel:WORD_1
v_mfma_f32_16x16x32_bf16 v[60:63],a[212:215],a[12:15],v[20:23]
ds_read_b128 v[20:23],v4 offset:128
ds_read_b64_tr_b16 a[212:213],v1 offset:4096
v_pk_mul_f16 v9,v8,v9
v_mfma_f32_16x16x32_bf16 v[60:63],a[200:203],a[28:31],v[60:63]
s_waitcnt lgkmcnt(5)
ds_read_b64_tr_b16 a[214:215],v1 offset:5120
ds_read_b64_tr_b16 a[200:201],v2 offset:4096
v_lshl_add_u64 v[50:51],v[32:33],0,v[34:35]
v_lshlrev_b32_sdwa v28,1,v28 dst_sel:DWORD dst_unused:UNUSED_PAD src0_sel:DWORD src1_sel:WORD_1
v_mfma_f32_16x16x32_bf16 v[60:63],a[196:199],a[44:47],v[60:63]
ds_read_b64_tr_b16 a[202:203],v2 offset:5120
ds_read_b64_tr_b16 a[196:197],v2 offset:2048
v_exp_f16 v8,v9
v_mfma_f32_16x16x32_bf16 v[32:35],a[192:195],a[44:47],v[16:19]
ds_read_b128 v[16:19],v4 offset:160
ds_read_b64_tr_b16 a[198:199],v2 offset:3072
v_lshlrev_b32_sdwa v30,1,v30 dst_sel:DWORD dst_unused:UNUSED_PAD src0_sel:DWORD src1_sel:WORD_1
v_mfma_f32_16x16x32_bf16 v[32:35],a[216:219],a[28:31],v[32:35]
ds_read_b64_tr_b16 a[192:193],v2 offset:0
ds_read_b64_tr_b16 a[194:195],v2 offset:1024
v_lshlrev_b32_sdwa v29,1,v29 dst_sel:DWORD dst_unused:UNUSED_PAD src0_sel:DWORD src1_sel:WORD_1
v_mfma_f32_16x16x32_bf16 v[32:35],a[220:223],a[12:15],v[32:35]
ds_read_u16_d16_hi v27,v27
v_exp_f16_sdwa v8,v9 dst_sel:WORD_1 dst_unused:UNUSED_PRESERVE src0_sel:WORD_1
v_mfma_f32_16x16x32_bf16 v[208:211],a[240:243],v[44:47],v[208:211]
ds_read_u16 v28,v28
v_lshlrev_b32_sdwa v31,1,v31 dst_sel:DWORD dst_unused:UNUSED_PAD src0_sel:DWORD src1_sel:WORD_1
v_mfma_f32_16x16x32_bf16 v[212:215],a[240:243],v[48:51],v[212:215]
ds_read_u16_d16_hi v30,v30
v_lshlrev_b32_sdwa v56,1,v56 dst_sel:DWORD dst_unused:UNUSED_PAD src0_sel:DWORD src1_sel:WORD_1
v_mfma_f32_16x16x32_bf16 v[172:175],a[224:227],v[48:51],v[172:175]
ds_read_u16 v29,v29
v_pk_fma_f16 v8,v8,s55,s55
v_mfma_f32_16x16x32_bf16 v[168:171],a[224:227],v[44:47],v[168:171]
v_rcp_f16 v9,v8
v_lshlrev_b32_sdwa v58,1,v58 dst_sel:DWORD dst_unused:UNUSED_PAD src0_sel:DWORD src1_sel:WORD_1
v_mfma_f32_16x16x32_bf16 v[216:219],a[248:251],v[44:47],v[216:219]
ds_read_u16_d16_hi v31,v31
v_lshlrev_b32_sdwa v57,1,v57 dst_sel:DWORD dst_unused:UNUSED_PAD src0_sel:DWORD src1_sel:WORD_1
v_mfma_f32_16x16x32_bf16 v[220:223],a[248:251],v[48:51],v[220:223]
ds_read_u16 v56,v56
v_lshlrev_b32_sdwa v59,1,v59 dst_sel:DWORD dst_unused:UNUSED_PAD src0_sel:DWORD src1_sel:WORD_1
v_mfma_f32_16x16x32_bf16 v[188:191],a[228:231],v[48:51],v[188:191]
ds_read_u16_d16_hi v58,v58
v_lshlrev_b32_sdwa v52,1,v52 dst_sel:DWORD dst_unused:UNUSED_PAD src0_sel:DWORD src1_sel:WORD_1
v_mfma_f32_16x16x32_bf16 v[184:187],a[228:231],v[44:47],v[184:187]
v_rcp_f16_sdwa v9,v8 dst_sel:WORD_1 dst_unused:UNUSED_PRESERVE src0_sel:WORD_1
v_mfma_f32_16x16x32_bf16 v[224:227],a[232:235],v[44:47],v[224:227]
ds_read_u16 v57,v57
ds_read_u16_d16_hi v59,v59
v_mfma_f32_16x16x32_bf16 v[228:231],a[232:235],v[48:51],v[228:231]
ds_read_u16 v52,v52
v_lshlrev_b32_sdwa v54,1,v54 dst_sel:DWORD dst_unused:UNUSED_PAD src0_sel:DWORD src1_sel:WORD_1
v_mfma_f32_16x16x32_bf16 v[204:207],a[236:239],v[48:51],v[204:207]
ds_read_u16_d16_hi v54,v54
v_fma_mix_f32 v53,v53,v9,0 op_sel_hi:[0,1,0]
v_mfma_f32_16x16x32_bf16 v[200:203],a[236:239],v[44:47],v[200:203]
s_waitcnt lgkmcnt(7)
v_fma_mix_f32 v55,v55,v9,0 op_sel:[0,1,0] op_sel_hi:[0,1,0]
v_mfma_f32_16x16x32_bf16 v[48:51],a[204:207],a[48:51],v[20:23]
s_add_u32 m0,s57,28296
v_lshlrev_b32_sdwa v40,1,v40 dst_sel:DWORD dst_unused:UNUSED_PAD src0_sel:DWORD src1_sel:WORD_1
v_lshlrev_b32_sdwa v42,1,v42 dst_sel:DWORD dst_unused:UNUSED_PAD src0_sel:DWORD src1_sel:WORD_1
v_mfma_f32_16x16x32_bf16 v[48:51],a[208:211],a[64:67],v[48:51]
s_add_u32 s89,s56,5701632
ds_read_u16 v40,v40
v_lshl_add_u64 v[24:25],v[24:25],0,v[26:27]
v_mfma_f32_16x16x32_bf16 v[48:51],a[212:215],a[80:83],v[48:51]
s_waitcnt lgkmcnt(6)
buffer_load_dwordx4 v6,s[36:39],s89 offen lds
v_lshl_add_u64 v[26:27],v[28:29],0,v[30:31]
v_mfma_f32_16x16x32_bf16 v[44:47],a[200:203],a[80:83],v[16:19]
ds_read_u16_d16_hi v42,v42
v_lshlrev_b32_sdwa v41,1,v41 dst_sel:DWORD dst_unused:UNUSED_PAD src0_sel:DWORD src1_sel:WORD_1
v_mfma_f32_16x16x32_bf16 v[44:47],a[196:199],a[64:67],v[44:47]
s_waitcnt lgkmcnt(4)
ds_read_u16 v41,v41
v_lshl_add_u64 v[28:29],v[56:57],0,v[58:59]
v_mfma_f32_16x16x32_bf16 v[44:47],a[192:195],a[48:51],v[44:47]
v_lshlrev_b32_sdwa v43,1,v43 dst_sel:DWORD dst_unused:UNUSED_PAD src0_sel:DWORD src1_sel:WORD_1
v_lshlrev_b32_sdwa v36,1,v36 dst_sel:DWORD dst_unused:UNUSED_PAD src0_sel:DWORD src1_sel:WORD_1
v_mfma_f32_16x16x32_bf16 v[56:59],a[192:195],a[52:55],v[16:19]
s_waitcnt lgkmcnt(3)
ds_read_u16_d16_hi v43,v43
v_or_b32 v30,v52,v54
v_mfma_f32_16x16x32_bf16 v[56:59],a[196:199],a[68:71],v[56:59]
ds_read_u16 v36,v36
v_cvt_pk_bf16_f32 v31,v53,v55
v_lshlrev_b32_sdwa v38,1,v38 dst_sel:DWORD dst_unused:UNUSED_PAD src0_sel:DWORD src1_sel:WORD_1
v_mfma_f32_16x16x32_bf16 v[56:59],a[200:203],a[84:87],v[56:59]
ds_read_u16_d16_hi v38,v38
v_lshlrev_b32_sdwa v37,1,v37 dst_sel:DWORD dst_unused:UNUSED_PAD src0_sel:DWORD src1_sel:WORD_1
v_mfma_f32_16x16x32_bf16 v[52:55],a[212:215],a[84:87],v[20:23]
ds_read_u16 v37,v37
v_lshlrev_b32_sdwa v39,1,v39 dst_sel:DWORD dst_unused:UNUSED_PAD src0_sel:DWORD src1_sel:WORD_1
v_mfma_f32_16x16x32_bf16 v[52:55],a[208:211],a[68:71],v[52:55]
ds_read_u16_d16_hi v39,v39
v_lshlrev_b32_sdwa v32,1,v32 dst_sel:DWORD dst_unused:UNUSED_PAD src0_sel:DWORD src1_sel:WORD_1
v_mfma_f32_16x16x32_bf16 v[52:55],a[204:207],a[52:55],v[52:55]
s_add_u32 m0,s57,31832
ds_read_u16 v32,v32
v_lshlrev_b32_sdwa v34,1,v34 dst_sel:DWORD dst_unused:UNUSED_PAD src0_sel:DWORD src1_sel:WORD_1
v_mfma_f32_16x16x32_bf16 v[136:139],a[240:243],v[24:27],v[136:139]
s_add_u32 s89,s56,5799936
ds_read_u16_d16_hi v34,v34
v_lshlrev_b32_sdwa v33,1,v33 dst_sel:DWORD dst_unused:UNUSED_PAD src0_sel:DWORD src1_sel:WORD_1
v_mfma_f32_16x16x32_bf16 v[140:143],a[240:243],v[28:31],v[140:143]
buffer_load_dwordx4 v6,s[36:39],s89 offen lds
v_lshlrev_b32_sdwa v35,1,v35 dst_sel:DWORD dst_unused:UNUSED_PAD src0_sel:DWORD src1_sel:WORD_1
v_mfma_f32_16x16x32_bf16 v[68:71],a[224:227],v[28:31],v[68:71]
ds_read_u16 v33,v33
ds_read_u16_d16_hi v35,v35
v_mfma_f32_16x16x32_bf16 v[64:67],a[224:227],v[24:27],v[64:67]
ds_read_b64_tr_b16 a[244:245],v3 offset:6160
v_lshlrev_b32_sdwa v60,1,v60 dst_sel:DWORD dst_unused:UNUSED_PAD src0_sel:DWORD src1_sel:WORD_1
v_mfma_f32_16x16x32_bf16 v[144:147],a[248:251],v[24:27],v[144:147]
ds_read_u16 v60,v60
v_lshlrev_b32_sdwa v62,1,v62 dst_sel:DWORD dst_unused:UNUSED_PAD src0_sel:DWORD src1_sel:WORD_1
v_mfma_f32_16x16x32_bf16 v[148:151],a[248:251],v[28:31],v[148:151]
ds_read_u16_d16_hi v62,v62
v_lshlrev_b32_sdwa v61,1,v61 dst_sel:DWORD dst_unused:UNUSED_PAD src0_sel:DWORD src1_sel:WORD_1
v_mfma_f32_16x16x32_bf16 v[84:87],a[228:231],v[28:31],v[84:87]
ds_read_u16 v61,v61
v_lshlrev_b32_sdwa v63,1,v63 dst_sel:DWORD dst_unused:UNUSED_PAD src0_sel:DWORD src1_sel:WORD_1
v_mfma_f32_16x16x32_bf16 v[80:83],a[228:231],v[24:27],v[80:83]
ds_read_u16_d16_hi v63,v63
v_mfma_f32_16x16x32_bf16 v[152:155],a[232:235],v[24:27],v[152:155]
ds_read_b64_tr_b16 a[246:247],v3 offset:7696
v_mfma_f32_16x16x32_bf16 v[156:159],a[232:235],v[28:31],v[156:159]
TP5957_%=:
s_cbranch_execz TR0_%=
ds_read_b64_tr_b16 a[252:253],v3 offset:6168
v_mfma_f32_16x16x32_bf16 v[100:103],a[236:239],v[28:31],v[100:103]
ds_read_b64_tr_b16 a[254:255],v3 offset:7704
v_mfma_f32_16x16x32_bf16 v[96:99],a[236:239],v[24:27],v[96:99]
v_mfma_f32_16x16x32_bf16 v[28:31],a[204:207],a[56:59],v[20:23]
s_nop 0
ds_read_b64_tr_b16 a[216:217],v1 offset:6160
v_lshlrev_b32_sdwa v48,1,v48 dst_sel:DWORD dst_unused:UNUSED_PAD src0_sel:DWORD src1_sel:WORD_1
v_mfma_f32_16x16x32_bf16 v[28:31],a[208:211],a[72:75],v[28:31]
s_waitcnt lgkmcnt(7)
ds_read_u16 v48,v48
v_lshl_add_u64 v[40:41],v[40:41],0,v[42:43]
v_fma_mixlo_f16 v8,v57,s99,0
v_mfma_f32_16x16x32_bf16 v[28:31],a[212:215],a[88:91],v[28:31]
ds_read_b64_tr_b16 a[218:219],v1 offset:7184
v_lshl_add_u64 v[42:43],v[36:37],0,v[38:39]
v_lshlrev_b32_sdwa v50,1,v50 dst_sel:DWORD dst_unused:UNUSED_PAD src0_sel:DWORD src1_sel:WORD_1
v_mfma_f32_16x16x32_bf16 v[24:27],a[200:203],a[88:91],v[16:19]
ds_read_u16_d16_hi v50,v50
v_fma_mixhi_f16 v8,v59,s99,0
v_mfma_f32_16x16x32_bf16 v[24:27],a[196:199],a[72:75],v[24:27]
ds_read_b64_tr_b16 a[220:221],v1 offset:8208
v_lshl_add_u64 v[36:37],v[32:33],0,v[34:35]
v_lshlrev_b32_sdwa v49,1,v49 dst_sel:DWORD dst_unused:UNUSED_PAD src0_sel:DWORD src1_sel:WORD_1
v_pk_fma_f16 v9,v8,v8,s54
v_mfma_f32_16x16x32_bf16 v[24:27],a[192:195],a[56:59],v[24:27]
ds_read_u16 v49,v49
v_lshlrev_b32_sdwa v51,1,v51 dst_sel:DWORD dst_unused:UNUSED_PAD src0_sel:DWORD src1_sel:WORD_1
v_mfma_f32_16x16x32_bf16 v[32:35],a[192:195],a[60:63],v[16:19]
ds_read_u16_d16_hi v51,v51
v_pk_mul_f16 v9,v8,v9
v_mfma_f32_16x16x32_bf16 v[32:35],a[196:199],a[76:79],v[32:35]
s_waitcnt lgkmcnt(7)
ds_read_b64_tr_b16 a[222:223],v1 offset:9232
v_lshl_add_u64 v[38:39],v[60:61],0,v[62:63]
v_lshlrev_b32_sdwa v44,1,v44 dst_sel:DWORD dst_unused:UNUSED_PAD src0_sel:DWORD src1_sel:WORD_1
v_mfma_f32_16x16x32_bf16 v[32:35],a[200:203],a[92:95],v[32:35]
v_exp_f16 v8,v9
v_mfma_f32_16x16x32_bf16 v[60:63],a[212:215],a[92:95],v[20:23]
ds_read_u16 v44,v44
v_lshlrev_b32_sdwa v46,1,v46 dst_sel:DWORD dst_unused:UNUSED_PAD src0_sel:DWORD src1_sel:WORD_1
v_mfma_f32_16x16x32_bf16 v[60:63],a[208:211],a[76:79],v[60:63]
ds_read_u16_d16_hi v46,v46
v_lshlrev_b32_sdwa v45,1,v45 dst_sel:DWORD dst_unused:UNUSED_PAD src0_sel:DWORD src1_sel:WORD_1
v_mfma_f32_16x16x32_bf16 v[60:63],a[204:207],a[60:63],v[60:63]
v_exp_f16_sdwa v8,v9 dst_sel:WORD_1 dst_unused:UNUSED_PRESERVE src0_sel:WORD_1
v_mfma_f32_16x16x32_bf16 v[112:115],a[240:243],v[40:43],v[112:115]
ds_read_u16 v45,v45
v_lshlrev_b32_sdwa v47,1,v47 dst_sel:DWORD dst_unused:UNUSED_PAD src0_sel:DWORD src1_sel:WORD_1
v_mfma_f32_16x16x32_bf16 v[116:119],a[240:243],v[36:39],v[116:119]
ds_read_u16_d16_hi v47,v47
v_lshlrev_b32_sdwa v52,1,v52 dst_sel:DWORD dst_unused:UNUSED_PAD src0_sel:DWORD src1_sel:WORD_1
v_mfma_f32_16x16x32_bf16 v[76:79],a[224:227],v[36:39],v[76:79]
ds_read_u16 v52,v52
v_pk_fma_f16 v8,v8,s55,s55
v_mfma_f32_16x16x32_bf16 v[72:75],a[224:227],v[40:43],v[72:75]
ds_read_b64_tr_b16 a[240:241],v3 offset:6224
v_rcp_f16_e64 v9,v8
v_lshlrev_b32_sdwa v54,1,v54 dst_sel:DWORD dst_unused:UNUSED_PAD src0_sel:DWORD src1_sel:WORD_1
v_mfma_f32_16x16x32_bf16 v[120:123],a[248:251],v[40:43],v[120:123]
ds_read_u16_d16_hi v54,v54
v_lshlrev_b32_sdwa v53,1,v53 dst_sel:DWORD dst_unused:UNUSED_PAD src0_sel:DWORD src1_sel:WORD_1
v_mfma_f32_16x16x32_bf16 v[124:127],a[248:251],v[36:39],v[124:127]
ds_read_u16 v53,v53
ds_read_b64_tr_b16 a[242:243],v3 offset:7760
v_mfma_f32_16x16x32_bf16 v[92:95],a[228:231],v[36:39],v[92:95]
ds_read_b64_tr_b16 a[224:225],v3 offset:6232
v_lshlrev_b32_sdwa v55,1,v55 dst_sel:DWORD dst_unused:UNUSED_PAD src0_sel:DWORD src1_sel:WORD_1
v_mfma_f32_16x16x32_bf16 v[88:91],a[228:231],v[40:43],v[88:91]
ds_read_u16_d16_hi v55,v55
v_rcp_f16_sdwa v9,v8 dst_sel:WORD_1 dst_unused:UNUSED_PRESERVE src0_sel:WORD_1
v_mfma_f32_16x16x32_bf16 v[128:131],a[232:235],v[40:43],v[128:131]
ds_read_b64_tr_b16 a[226:227],v3 offset:7768
ds_read_b64_tr_b16 a[248:249],v3 offset:6288
v_lshlrev_b32_sdwa v56,1,v56 dst_sel:DWORD dst_unused:UNUSED_PAD src0_sel:DWORD src1_sel:WORD_1
v_mfma_f32_16x16x32_bf16 v[132:135],a[232:235],v[36:39],v[132:135]
ds_read_u16 v56,v56
v_lshlrev_b32_sdwa v58,1,v58 dst_sel:DWORD dst_unused:UNUSED_PAD src0_sel:DWORD src1_sel:WORD_1
v_mfma_f32_16x16x32_bf16 v[108:111],a[236:239],v[36:39],v[108:111]
ds_read_u16_d16_hi v58,v58
v_fma_mix_f32 v57,v57,v9,0 op_sel_hi:[0,1,0]
v_mfma_f32_16x16x32_bf16 v[104:107],a[236:239],v[40:43],v[104:107]
ds_read_b64_tr_b16 a[250:251],v3 offset:7824
v_fma_mix_f32 v59,v59,v9,0 op_sel:[0,1,0] op_sel_hi:[0,1,0]
v_mfma_f32_16x16x32_bf16 v[36:39],a[204:207],a[0:3],v[20:23]
s_add_u32 m0,s57,35360
ds_read_b64_tr_b16 a[228:229],v3 offset:6296
v_lshlrev_b32_sdwa v28,1,v28 dst_sel:DWORD dst_unused:UNUSED_PAD src0_sel:DWORD src1_sel:WORD_1
v_mfma_f32_16x16x32_bf16 v[36:39],a[208:211],a[16:19],v[36:39]
s_waitcnt lgkmcnt(10)
ds_read_b64_tr_b16 a[230:231],v3 offset:7832
v_lshl_add_u64 v[48:49],v[48:49],0,v[50:51]
v_mfma_f32_16x16x32_bf16 v[36:39],a[212:215],a[32:35],v[36:39]
s_add_u32 s89,s56,5898240
ds_read_u16 v28,v28
v_lshl_add_u64 v[50:51],v[44:45],0,v[46:47]
v_lshlrev_b32_sdwa v30,1,v30 dst_sel:DWORD dst_unused:UNUSED_PAD src0_sel:DWORD src1_sel:WORD_1
v_mfma_f32_16x16x32_bf16 v[40:43],a[200:203],a[32:35],v[16:19]
buffer_load_dwordx4 v6,s[36:39],s89 offen lds
v_lshlrev_b32_sdwa v29,1,v29 dst_sel:DWORD dst_unused:UNUSED_PAD src0_sel:DWORD src1_sel:WORD_1
v_mfma_f32_16x16x32_bf16 v[40:43],a[196:199],a[16:19],v[40:43]
s_waitcnt lgkmcnt(8)
ds_read_u16_d16_hi v30,v30
v_lshl_add_u64 v[44:45],v[52:53],0,v[54:55]
v_mfma_f32_16x16x32_bf16 v[40:43],a[192:195],a[0:3],v[40:43]
ds_read_u16 v29,v29
v_lshlrev_b32_sdwa v31,1,v31 dst_sel:DWORD dst_unused:UNUSED_PAD src0_sel:DWORD src1_sel:WORD_1
v_mfma_f32_16x16x32_bf16 v[52:55],a[192:195],a[4:7],v[16:19]
s_waitcnt lgkmcnt(6)
ds_read_u16_d16_hi v31,v31
v_or_b32 v46,v56,v58
v_lshlrev_b32_sdwa v24,1,v24 dst_sel:DWORD dst_unused:UNUSED_PAD src0_sel:DWORD src1_sel:WORD_1
v_mfma_f32_16x16x32_bf16 v[52:55],a[196:199],a[20:23],v[52:55]
ds_read_u16 v24,v24
v_cvt_pk_bf16_f32 v47,v57,v59
v_lshlrev_b32_sdwa v26,1,v26 dst_sel:DWORD dst_unused:UNUSED_PAD src0_sel:DWORD src1_sel:WORD_1
v_mfma_f32_16x16x32_bf16 v[52:55],a[200:203],a[36:39],v[52:55]
ds_read_u16_d16_hi v26,v26
v_lshlrev_b32_sdwa v25,1,v25 dst_sel:DWORD dst_unused:UNUSED_PAD src0_sel:DWORD src1_sel:WORD_1
v_mfma_f32_16x16x32_bf16 v[56:59],a[212:215],a[36:39],v[20:23]
ds_read_u16 v25,v25
v_lshlrev_b32_sdwa v27,1,v27 dst_sel:DWORD dst_unused:UNUSED_PAD src0_sel:DWORD src1_sel:WORD_1
v_mfma_f32_16x16x32_bf16 v[56:59],a[208:211],a[20:23],v[56:59]
ds_read_u16_d16_hi v27,v27
v_lshlrev_b32_sdwa v60,1,v60 dst_sel:DWORD dst_unused:UNUSED_PAD src0_sel:DWORD src1_sel:WORD_1
v_mfma_f32_16x16x32_bf16 v[56:59],a[204:207],a[4:7],v[56:59]
ds_read_u16 v60,v60
v_lshlrev_b32_sdwa v62,1,v62 dst_sel:DWORD dst_unused:UNUSED_PAD src0_sel:DWORD src1_sel:WORD_1
v_mfma_f32_16x16x32_bf16 v[232:235],a[244:247],v[48:51],v[232:235]
ds_read_u16_d16_hi v62,v62
v_lshlrev_b32_sdwa v61,1,v61 dst_sel:DWORD dst_unused:UNUSED_PAD src0_sel:DWORD src1_sel:WORD_1
v_mfma_f32_16x16x32_bf16 v[236:239],a[244:247],v[44:47],v[236:239]
ds_read_u16 v61,v61
v_lshlrev_b32_sdwa v63,1,v63 dst_sel:DWORD dst_unused:UNUSED_PAD src0_sel:DWORD src1_sel:WORD_1
v_mfma_f32_16x16x32_bf16 v[164:167],a[252:255],v[44:47],v[164:167]
ds_read_u16_d16_hi v63,v63
v_lshlrev_b32_sdwa v32,1,v32 dst_sel:DWORD dst_unused:UNUSED_PAD src0_sel:DWORD src1_sel:WORD_1
v_mfma_f32_16x16x32_bf16 v[160:163],a[252:255],v[48:51],v[160:163]
ds_read_u16 v32,v32
v_lshlrev_b32_sdwa v34,1,v34 dst_sel:DWORD dst_unused:UNUSED_PAD src0_sel:DWORD src1_sel:WORD_1
v_mfma_f32_16x16x32_bf16 v[240:243],a[240:243],v[48:51],v[240:243]
ds_read_u16_d16_hi v34,v34
v_lshlrev_b32_sdwa v33,1,v33 dst_sel:DWORD dst_unused:UNUSED_PAD src0_sel:DWORD src1_sel:WORD_1
v_mfma_f32_16x16x32_bf16 v[244:247],a[240:243],v[44:47],v[244:247]
ds_read_u16 v33,v33
v_lshlrev_b32_sdwa v35,1,v35 dst_sel:DWORD dst_unused:UNUSED_PAD src0_sel:DWORD src1_sel:WORD_1
v_mfma_f32_16x16x32_bf16 v[180:183],a[224:227],v[44:47],v[180:183]
ds_read_u16_d16_hi v35,v35
v_mfma_f32_16x16x32_bf16 v[176:179],a[224:227],v[48:51],v[176:179]
s_waitcnt lgkmcnt(8)
v_mfma_f32_16x16x32_bf16 v[248:251],a[248:251],v[48:51],v[248:251]
s_add_u32 m0,s57,38896
v_mfma_f32_16x16x32_bf16 v[252:255],a[248:251],v[44:47],v[252:255]
s_add_u32 s89,s56,5996544
v_mfma_f32_16x16x32_bf16 v[196:199],a[228:231],v[44:47],v[196:199]
buffer_load_dwordx4 v6,s[36:39],s89 offen lds
v_mfma_f32_16x16x32_bf16 v[192:195],a[228:231],v[48:51],v[192:195]
v_mfma_f32_16x16x32_bf16 v[44:47],a[204:207],a[8:11],v[20:23]
v_lshlrev_b32_sdwa v36,1,v36 dst_sel:DWORD dst_unused:UNUSED_PAD src0_sel:DWORD src1_sel:WORD_1
v_fma_mixlo_f16 v8,v53,s99,0
v_mfma_f32_16x16x32_bf16 v[44:47],a[208:211],a[24:27],v[44:47]
ds_read_u16 v36,v36
v_lshl_add_u64 v[28:29],v[28:29],0,v[30:31]
v_mfma_f32_16x16x32_bf16 v[44:47],a[212:215],a[40:43],v[44:47]
v_lshl_add_u64 v[30:31],v[24:25],0,v[26:27]
v_lshlrev_b32_sdwa v38,1,v38 dst_sel:DWORD dst_unused:UNUSED_PAD src0_sel:DWORD src1_sel:WORD_1
v_mfma_f32_16x16x32_bf16 v[48:51],a[200:203],a[40:43],v[16:19]
ds_read_u16_d16_hi v38,v38
v_fma_mixhi_f16 v8,v55,s99,0
v_mfma_f32_16x16x32_bf16 v[48:51],a[196:199],a[24:27],v[48:51]
s_waitcnt lgkmcnt(2)
v_lshl_add_u64 v[24:25],v[60:61],0,v[62:63]
v_lshlrev_b32_sdwa v37,1,v37 dst_sel:DWORD dst_unused:UNUSED_PAD src0_sel:DWORD src1_sel:WORD_1
v_pk_fma_f16 v9,v8,v8,s54
v_mfma_f32_16x16x32_bf16 v[48:51],a[192:195],a[8:11],v[48:51]
ds_read_u16 v37,v37
v_lshlrev_b32_sdwa v39,1,v39 dst_sel:DWORD dst_unused:UNUSED_PAD src0_sel:DWORD src1_sel:WORD_1
v_mfma_f32_16x16x32_bf16 v[60:63],a[192:195],a[12:15],v[16:19]
ds_read_b128 v[16:19],v4 offset:256
v_pk_mul_f16 v9,v8,v9
v_mfma_f32_16x16x32_bf16 v[60:63],a[196:199],a[28:31],v[60:63]
ds_read_u16_d16_hi v39,v39
ds_read_b64_tr_b16 a[192:193],v1 offset:10256
v_lshl_add_u64 v[26:27],v[32:33],0,v[34:35]
v_lshlrev_b32_sdwa v40,1,v40 dst_sel:DWORD dst_unused:UNUSED_PAD src0_sel:DWORD src1_sel:WORD_1
v_mfma_f32_16x16x32_bf16 v[60:63],a[200:203],a[44:47],v[60:63]
ds_read_b64_tr_b16 a[194:195],v1 offset:11280
v_exp_f16_e64 v8,v9
v_mfma_f32_16x16x32_bf16 v[32:35],a[212:215],a[44:47],v[20:23]
ds_read_b128 v[20:23],v4 offset:288
ds_read_u16 v40,v40
v_lshlrev_b32_sdwa v42,1,v42 dst_sel:DWORD dst_unused:UNUSED_PAD src0_sel:DWORD src1_sel:WORD_1
v_mfma_f32_16x16x32_bf16 v[32:35],a[208:211],a[28:31],v[32:35]
ds_read_u16_d16_hi v42,v42
ds_read_b64_tr_b16 a[196:197],v2 offset:10256
v_lshlrev_b32_sdwa v41,1,v41 dst_sel:DWORD dst_unused:UNUSED_PAD src0_sel:DWORD src1_sel:WORD_1
v_mfma_f32_16x16x32_bf16 v[32:35],a[204:207],a[12:15],v[32:35]
ds_read_u16 v41,v41
v_exp_f16_sdwa v8,v9 dst_sel:WORD_1 dst_unused:UNUSED_PRESERVE src0_sel:WORD_1
v_mfma_f32_16x16x32_bf16 v[208:211],a[244:247],v[28:31],v[208:211]
ds_read_b64_tr_b16 a[198:199],v2 offset:11280
ds_read_b64_tr_b16 a[200:201],v2 offset:8208
v_lshlrev_b32_sdwa v43,1,v43 dst_sel:DWORD dst_unused:UNUSED_PAD src0_sel:DWORD src1_sel:WORD_1
v_mfma_f32_16x16x32_bf16 v[212:215],a[244:247],v[24:27],v[212:215]
ds_read_u16_d16_hi v43,v43
v_lshlrev_b32_sdwa v56,1,v56 dst_sel:DWORD dst_unused:UNUSED_PAD src0_sel:DWORD src1_sel:WORD_1
v_mfma_f32_16x16x32_bf16 v[172:175],a[252:255],v[24:27],v[172:175]
ds_read_b64_tr_b16 a[202:203],v2 offset:9232
v_pk_fma_f16 v8,v8,s55,s55
v_mfma_f32_16x16x32_bf16 v[168:171],a[252:255],v[28:31],v[168:171]
v_rcp_f16 v9,v8
v_lshlrev_b32_sdwa v58,1,v58 dst_sel:DWORD dst_unused:UNUSED_PAD src0_sel:DWORD src1_sel:WORD_1
v_mfma_f32_16x16x32_bf16 v[216:219],a[240:243],v[28:31],v[216:219]
ds_read_u16 v56,v56
v_lshlrev_b32_sdwa v57,1,v57 dst_sel:DWORD dst_unused:UNUSED_PAD src0_sel:DWORD src1_sel:WORD_1
v_mfma_f32_16x16x32_bf16 v[220:223],a[240:243],v[24:27],v[220:223]
ds_read_u16_d16_hi v58,v58
v_lshlrev_b32_sdwa v59,1,v59 dst_sel:DWORD dst_unused:UNUSED_PAD src0_sel:DWORD src1_sel:WORD_1
v_mfma_f32_16x16x32_bf16 v[188:191],a[224:227],v[24:27],v[188:191]
ds_read_u16 v57,v57
v_lshlrev_b32_sdwa v52,1,v52 dst_sel:DWORD dst_unused:UNUSED_PAD src0_sel:DWORD src1_sel:WORD_1
v_mfma_f32_16x16x32_bf16 v[184:187],a[224:227],v[28:31],v[184:187]
ds_read_u16_d16_hi v59,v59
v_rcp_f16_sdwa v9,v8 dst_sel:WORD_1 dst_unused:UNUSED_PRESERVE src0_sel:WORD_1
v_mfma_f32_16x16x32_bf16 v[224:227],a[248:251],v[28:31],v[224:227]
ds_read_b64_tr_b16 a[212:213],v2 offset:6160
v_lshlrev_b32_sdwa v54,1,v54 dst_sel:DWORD dst_unused:UNUSED_PAD src0_sel:DWORD src1_sel:WORD_1
v_mfma_f32_16x16x32_bf16 v[228:231],a[248:251],v[24:27],v[228:231]
ds_read_b64_tr_b16 a[214:215],v2 offset:7184
v_fma_mix_f32 v53,v53,v9,0 op_sel_hi:[0,1,0]
v_mfma_f32_16x16x32_bf16 v[204:207],a[228:231],v[24:27],v[204:207]
ds_read_u16 v52,v52
v_fma_mix_f32 v55,v55,v9,0 op_sel:[0,1,0] op_sel_hi:[0,1,0]
v_mfma_f32_16x16x32_bf16 v[200:203],a[228:231],v[28:31],v[200:203]
s_waitcnt lgkmcnt(7)
ds_read_u16_d16_hi v54,v54
v_mfma_f32_16x16x32_bf16 v[24:27],a[216:219],a[48:51],v[16:19]
v_lshlrev_b32_sdwa v44,1,v44 dst_sel:DWORD dst_unused:UNUSED_PAD src0_sel:DWORD src1_sel:WORD_1
v_lshlrev_b32_sdwa v46,1,v46 dst_sel:DWORD dst_unused:UNUSED_PAD src0_sel:DWORD src1_sel:WORD_1
v_mfma_f32_16x16x32_bf16 v[24:27],a[220:223],a[64:67],v[24:27]
s_add_u32 m0,s57,42440
ds_read_u16 v44,v44
v_lshl_add_u64 v[36:37],v[36:37],0,v[38:39]
v_mfma_f32_16x16x32_bf16 v[24:27],a[192:195],a[80:83],v[24:27]
s_add_u32 s89,s56,6094848
ds_read_u16_d16_hi v46,v46
v_lshl_add_u64 v[38:39],v[40:41],0,v[42:43]
v_mfma_f32_16x16x32_bf16 v[28:31],a[196:199],a[80:83],v[20:23]
buffer_load_dwordx4 v6,s[36:39],s89 offen lds
v_lshlrev_b32_sdwa v45,1,v45 dst_sel:DWORD dst_unused:UNUSED_PAD src0_sel:DWORD src1_sel:WORD_1
v_mfma_f32_16x16x32_bf16 v[28:31],a[200:203],a[64:67],v[28:31]
s_waitcnt lgkmcnt(4)
ds_read_u16 v45,v45
v_lshl_add_u64 v[40:41],v[56:57],0,v[58:59]
v_mfma_f32_16x16x32_bf16 v[28:31],a[212:215],a[48:51],v[28:31]
v_lshlrev_b32_sdwa v47,1,v47 dst_sel:DWORD dst_unused:UNUSED_PAD src0_sel:DWORD src1_sel:WORD_1
v_lshlrev_b32_sdwa v48,1,v48 dst_sel:DWORD dst_unused:UNUSED_PAD src0_sel:DWORD src1_sel:WORD_1
v_mfma_f32_16x16x32_bf16 v[56:59],a[212:215],a[52:55],v[20:23]
s_waitcnt lgkmcnt(3)
ds_read_u16_d16_hi v47,v47
v_or_b32 v42,v52,v54
v_mfma_f32_16x16x32_bf16 v[56:59],a[200:203],a[68:71],v[56:59]
s_waitcnt vmcnt(14)
ds_read_u16 v48,v48
v_cvt_pk_bf16_f32 v43,v53,v55
v_lshlrev_b32_sdwa v50,1,v50 dst_sel:DWORD dst_unused:UNUSED_PAD src0_sel:DWORD src1_sel:WORD_1
v_mfma_f32_16x16x32_bf16 v[56:59],a[196:199],a[84:87],v[56:59]
s_barrier
ds_read_u16_d16_hi v50,v50
v_lshlrev_b32_sdwa v49,1,v49 dst_sel:DWORD dst_unused:UNUSED_PAD src0_sel:DWORD src1_sel:WORD_1
v_mfma_f32_16x16x32_bf16 v[52:55],a[192:195],a[84:87],v[16:19]
ds_read_u16 v49,v49
v_lshlrev_b32_sdwa v51,1,v51 dst_sel:DWORD dst_unused:UNUSED_PAD src0_sel:DWORD src1_sel:WORD_1
v_mfma_f32_16x16x32_bf16 v[52:55],a[220:223],a[68:71],v[52:55]
s_add_u32 m0,s58,0
ds_read_u16_d16_hi v51,v51
v_lshlrev_b32_sdwa v32,1,v32 dst_sel:DWORD dst_unused:UNUSED_PAD src0_sel:DWORD src1_sel:WORD_1
v_mfma_f32_16x16x32_bf16 v[52:55],a[216:219],a[52:55],v[52:55]
s_add_u32 s69,s69,s59
ds_read_u16 v32,v32
v_lshlrev_b32_sdwa v34,1,v34 dst_sel:DWORD dst_unused:UNUSED_PAD src0_sel:DWORD src1_sel:WORD_1
v_mfma_f32_16x16x32_bf16 v[136:139],a[244:247],v[36:39],v[136:139]
buffer_load_dwordx4 v5,s[44:47],s69 offen offset:0 lds
v_lshlrev_b32_sdwa v33,1,v33 dst_sel:DWORD dst_unused:UNUSED_PAD src0_sel:DWORD src1_sel:WORD_1
v_mfma_f32_16x16x32_bf16 v[140:143],a[244:247],v[40:43],v[140:143]
ds_read_u16_d16_hi v34,v34
v_lshlrev_b32_sdwa v35,1,v35 dst_sel:DWORD dst_unused:UNUSED_PAD src0_sel:DWORD src1_sel:WORD_1
v_mfma_f32_16x16x32_bf16 v[68:71],a[252:255],v[40:43],v[68:71]
ds_read_u16 v33,v33
ds_read_u16_d16_hi v35,v35
v_mfma_f32_16x16x32_bf16 v[64:67],a[252:255],v[36:39],v[64:67]
ds_read_b64_tr_b16 a[232:233],v3 offset:12320
v_lshlrev_b32_sdwa v60,1,v60 dst_sel:DWORD dst_unused:UNUSED_PAD src0_sel:DWORD src1_sel:WORD_1
v_mfma_f32_16x16x32_bf16 v[144:147],a[240:243],v[36:39],v[144:147]
s_add_u32 s69,s69,s53
ds_read_u16 v60,v60
v_lshlrev_b32_sdwa v62,1,v62 dst_sel:DWORD dst_unused:UNUSED_PAD src0_sel:DWORD src1_sel:WORD_1
v_mfma_f32_16x16x32_bf16 v[148:151],a[240:243],v[40:43],v[148:151]
buffer_load_dwordx4 v5,s[44:47],s69 offen offset:1024 lds
ds_read_u16_d16_hi v62,v62
v_mfma_f32_16x16x32_bf16 v[84:87],a[224:227],v[40:43],v[84:87]
ds_read_b64_tr_b16 a[234:235],v3 offset:13856
v_lshlrev_b32_sdwa v61,1,v61 dst_sel:DWORD dst_unused:UNUSED_PAD src0_sel:DWORD src1_sel:WORD_1
v_mfma_f32_16x16x32_bf16 v[80:83],a[224:227],v[36:39],v[80:83]
ds_read_u16 v61,v61
v_lshlrev_b32_sdwa v63,1,v63 dst_sel:DWORD dst_unused:UNUSED_PAD src0_sel:DWORD src1_sel:WORD_1
v_mfma_f32_16x16x32_bf16 v[152:155],a[248:251],v[36:39],v[152:155]
ds_read_u16_d16_hi v63,v63
v_mfma_f32_16x16x32_bf16 v[156:159],a[248:251],v[40:43],v[156:159]
s_add_u32 s69,s69,s53
ds_read_b64_tr_b16 a[236:237],v3 offset:12328
v_mfma_f32_16x16x32_bf16 v[100:103],a[228:231],v[40:43],v[100:103]
buffer_load_dwordx4 v5,s[44:47],s69 offen offset:2048 lds
v_mfma_f32_16x16x32_bf16 v[96:99],a[228:231],v[36:39],v[96:99]
ds_read_b64_tr_b16 a[238:239],v3 offset:13864
v_mfma_f32_16x16x32_bf16 v[40:43],a[216:219],a[56:59],v[16:19]
ds_read_b64_tr_b16 a[208:209],v1 offset:12320
v_lshlrev_b32_sdwa v24,1,v24 dst_sel:DWORD dst_unused:UNUSED_PAD src0_sel:DWORD src1_sel:WORD_1
v_mfma_f32_16x16x32_bf16 v[40:43],a[220:223],a[72:75],v[40:43]
s_waitcnt lgkmcnt(7)
ds_read_u16 v24,v24
v_lshl_add_u64 v[44:45],v[44:45],0,v[46:47]
v_fma_mixlo_f16 v8,v57,s99,0
v_mfma_f32_16x16x32_bf16 v[40:43],a[192:195],a[88:91],v[40:43]
ds_read_b64_tr_b16 a[210:211],v1 offset:13344
v_lshl_add_u64 v[46:47],v[48:49],0,v[50:51]
v_lshlrev_b32_sdwa v26,1,v26 dst_sel:DWORD dst_unused:UNUSED_PAD src0_sel:DWORD src1_sel:WORD_1
v_mfma_f32_16x16x32_bf16 v[36:39],a[196:199],a[88:91],v[20:23]
ds_read_u16_d16_hi v26,v26
v_fma_mixhi_f16 v8,v59,s99,0
v_mfma_f32_16x16x32_bf16 v[36:39],a[200:203],a[72:75],v[36:39]
ds_read_b64_tr_b16 a[204:205],v1 offset:14368
v_lshl_add_u64 v[48:49],v[32:33],0,v[34:35]
v_lshlrev_b32_sdwa v25,1,v25 dst_sel:DWORD dst_unused:UNUSED_PAD src0_sel:DWORD src1_sel:WORD_1
v_pk_fma_f16 v9,v8,v8,s54
v_mfma_f32_16x16x32_bf16 v[36:39],a[212:215],a[56:59],v[36:39]
ds_read_u16 v25,v25
v_lshlrev_b32_sdwa v27,1,v27 dst_sel:DWORD dst_unused:UNUSED_PAD src0_sel:DWORD src1_sel:WORD_1
v_mfma_f32_16x16x32_bf16 v[32:35],a[212:215],a[60:63],v[20:23]
ds_read_u16_d16_hi v27,v27
v_pk_mul_f16 v9,v8,v9
v_mfma_f32_16x16x32_bf16 v[32:35],a[200:203],a[76:79],v[32:35]
s_waitcnt lgkmcnt(8)
ds_read_b64_tr_b16 a[206:207],v1 offset:15392
v_lshl_add_u64 v[50:51],v[60:61],0,v[62:63]
v_lshlrev_b32_sdwa v28,1,v28 dst_sel:DWORD dst_unused:UNUSED_PAD src0_sel:DWORD src1_sel:WORD_1
v_mfma_f32_16x16x32_bf16 v[32:35],a[196:199],a[92:95],v[32:35]
v_exp_f16 v8,v9
v_mfma_f32_16x16x32_bf16 v[60:63],a[192:195],a[92:95],v[16:19]
ds_read_u16 v28,v28
v_lshlrev_b32_sdwa v30,1,v30 dst_sel:DWORD dst_unused:UNUSED_PAD src0_sel:DWORD src1_sel:WORD_1
v_mfma_f32_16x16x32_bf16 v[60:63],a[220:223],a[76:79],v[60:63]
ds_read_u16_d16_hi v30,v30
v_lshlrev_b32_sdwa v29,1,v29 dst_sel:DWORD dst_unused:UNUSED_PAD src0_sel:DWORD src1_sel:WORD_1
v_mfma_f32_16x16x32_bf16 v[60:63],a[216:219],a[60:63],v[60:63]
v_exp_f16_sdwa v8,v9 dst_sel:WORD_1 dst_unused:UNUSED_PRESERVE src0_sel:WORD_1
v_mfma_f32_16x16x32_bf16 v[112:115],a[244:247],v[44:47],v[112:115]
ds_read_u16 v29,v29
v_lshlrev_b32_sdwa v31,1,v31 dst_sel:DWORD dst_unused:UNUSED_PAD src0_sel:DWORD src1_sel:WORD_1
v_mfma_f32_16x16x32_bf16 v[116:119],a[244:247],v[48:51],v[116:119]
ds_read_u16_d16_hi v31,v31
v_lshlrev_b32_sdwa v52,1,v52 dst_sel:DWORD dst_unused:UNUSED_PAD src0_sel:DWORD src1_sel:WORD_1
v_mfma_f32_16x16x32_bf16 v[76:79],a[252:255],v[48:51],v[76:79]
ds_read_u16 v52,v52
v_pk_fma_f16 v8,v8,s55,s55
v_mfma_f32_16x16x32_bf16 v[72:75],a[252:255],v[44:47],v[72:75]
ds_read_b64_tr_b16 a[244:245],v3 offset:12384
v_rcp_f16_e64 v9,v8
v_lshlrev_b32_sdwa v54,1,v54 dst_sel:DWORD dst_unused:UNUSED_PAD src0_sel:DWORD src1_sel:WORD_1
v_mfma_f32_16x16x32_bf16 v[120:123],a[240:243],v[44:47],v[120:123]
ds_read_u16_d16_hi v54,v54
v_lshlrev_b32_sdwa v53,1,v53 dst_sel:DWORD dst_unused:UNUSED_PAD src0_sel:DWORD src1_sel:WORD_1
v_mfma_f32_16x16x32_bf16 v[124:127],a[240:243],v[48:51],v[124:127]
ds_read_u16 v53,v53
ds_read_b64_tr_b16 a[246:247],v3 offset:13920
v_mfma_f32_16x16x32_bf16 v[92:95],a[224:227],v[48:51],v[92:95]
ds_read_b64_tr_b16 a[252:253],v3 offset:12392
v_lshlrev_b32_sdwa v55,1,v55 dst_sel:DWORD dst_unused:UNUSED_PAD src0_sel:DWORD src1_sel:WORD_1
v_mfma_f32_16x16x32_bf16 v[88:91],a[224:227],v[44:47],v[88:91]
ds_read_u16_d16_hi v55,v55
v_rcp_f16_sdwa v9,v8 dst_sel:WORD_1 dst_unused:UNUSED_PRESERVE src0_sel:WORD_1
v_mfma_f32_16x16x32_bf16 v[128:131],a[248:251],v[44:47],v[128:131]
ds_read_b64_tr_b16 a[254:255],v3 offset:13928
ds_read_b64_tr_b16 a[240:241],v3 offset:12448
v_lshlrev_b32_sdwa v56,1,v56 dst_sel:DWORD dst_unused:UNUSED_PAD src0_sel:DWORD src1_sel:WORD_1
v_mfma_f32_16x16x32_bf16 v[132:135],a[248:251],v[48:51],v[132:135]
ds_read_u16 v56,v56
v_lshlrev_b32_sdwa v58,1,v58 dst_sel:DWORD dst_unused:UNUSED_PAD src0_sel:DWORD src1_sel:WORD_1
v_mfma_f32_16x16x32_bf16 v[108:111],a[228:231],v[48:51],v[108:111]
ds_read_u16_d16_hi v58,v58
v_fma_mix_f32 v57,v57,v9,0 op_sel_hi:[0,1,0]
v_mfma_f32_16x16x32_bf16 v[104:107],a[228:231],v[44:47],v[104:107]
ds_read_b64_tr_b16 a[242:243],v3 offset:13984
v_fma_mix_f32 v59,v59,v9,0 op_sel:[0,1,0] op_sel_hi:[0,1,0]
v_mfma_f32_16x16x32_bf16 v[48:51],a[216:219],a[0:3],v[16:19]
s_add_u32 m0,s58,6160
ds_read_b64_tr_b16 a[224:225],v3 offset:12456
v_lshlrev_b32_sdwa v40,1,v40 dst_sel:DWORD dst_unused:UNUSED_PAD src0_sel:DWORD src1_sel:WORD_1
v_mfma_f32_16x16x32_bf16 v[48:51],a[220:223],a[16:19],v[48:51]
s_waitcnt lgkmcnt(10)
ds_read_b64_tr_b16 a[226:227],v3 offset:13992
v_lshl_add_u64 v[24:25],v[24:25],0,v[26:27]
v_mfma_f32_16x16x32_bf16 v[48:51],a[192:195],a[32:35],v[48:51]
s_add_u32 s69,s69,s59
ds_read_u16 v40,v40
v_lshl_add_u64 v[26:27],v[28:29],0,v[30:31]
v_lshlrev_b32_sdwa v42,1,v42 dst_sel:DWORD dst_unused:UNUSED_PAD src0_sel:DWORD src1_sel:WORD_1
v_mfma_f32_16x16x32_bf16 v[44:47],a[196:199],a[32:35],v[20:23]
buffer_load_dwordx4 v5,s[44:47],s69 offen offset:0 lds
v_lshlrev_b32_sdwa v41,1,v41 dst_sel:DWORD dst_unused:UNUSED_PAD src0_sel:DWORD src1_sel:WORD_1
v_mfma_f32_16x16x32_bf16 v[44:47],a[200:203],a[16:19],v[44:47]
s_waitcnt lgkmcnt(8)
ds_read_u16_d16_hi v42,v42
v_lshl_add_u64 v[28:29],v[52:53],0,v[54:55]
v_mfma_f32_16x16x32_bf16 v[44:47],a[212:215],a[0:3],v[44:47]
s_add_u32 m0,s57,45976
ds_read_u16 v41,v41
v_lshlrev_b32_sdwa v43,1,v43 dst_sel:DWORD dst_unused:UNUSED_PAD src0_sel:DWORD src1_sel:WORD_1
v_mfma_f32_16x16x32_bf16 v[52:55],a[212:215],a[4:7],v[20:23]
s_waitcnt lgkmcnt(6)
ds_read_u16_d16_hi v43,v43
v_or_b32 v30,v56,v58
v_lshlrev_b32_sdwa v36,1,v36 dst_sel:DWORD dst_unused:UNUSED_PAD src0_sel:DWORD src1_sel:WORD_1
v_mfma_f32_16x16x32_bf16 v[52:55],a[200:203],a[20:23],v[52:55]
s_add_u32 s89,s56,6193152
ds_read_u16 v36,v36
v_cvt_pk_bf16_f32 v31,v57,v59
v_lshlrev_b32_sdwa v38,1,v38 dst_sel:DWORD dst_unused:UNUSED_PAD src0_sel:DWORD src1_sel:WORD_1
v_mfma_f32_16x16x32_bf16 v[52:55],a[196:199],a[36:39],v[52:55]
buffer_load_dwordx4 v6,s[36:39],s89 offen lds
v_lshlrev_b32_sdwa v37,1,v37 dst_sel:DWORD dst_unused:UNUSED_PAD src0_sel:DWORD src1_sel:WORD_1
v_mfma_f32_16x16x32_bf16 v[56:59],a[192:195],a[36:39],v[16:19]
s_add_u32 m0,s58,6160
ds_read_u16_d16_hi v38,v38
v_lshlrev_b32_sdwa v39,1,v39 dst_sel:DWORD dst_unused:UNUSED_PAD src0_sel:DWORD src1_sel:WORD_1
v_mfma_f32_16x16x32_bf16 v[56:59],a[220:223],a[20:23],v[56:59]
s_add_u32 s69,s69,s53
ds_read_u16 v37,v37
v_lshlrev_b32_sdwa v60,1,v60 dst_sel:DWORD dst_unused:UNUSED_PAD src0_sel:DWORD src1_sel:WORD_1
v_mfma_f32_16x16x32_bf16 v[56:59],a[216:219],a[4:7],v[56:59]
buffer_load_dwordx4 v5,s[44:47],s69 offen offset:1024 lds
v_lshlrev_b32_sdwa v62,1,v62 dst_sel:DWORD dst_unused:UNUSED_PAD src0_sel:DWORD src1_sel:WORD_1
v_mfma_f32_16x16x32_bf16 v[232:235],a[232:235],v[24:27],v[232:235]
s_add_u32 s69,s69,s53
ds_read_u16_d16_hi v39,v39
v_lshlrev_b32_sdwa v61,1,v61 dst_sel:DWORD dst_unused:UNUSED_PAD src0_sel:DWORD src1_sel:WORD_1
v_mfma_f32_16x16x32_bf16 v[236:239],a[232:235],v[28:31],v[236:239]
buffer_load_dwordx4 v5,s[44:47],s69 offen offset:2048 lds
v_lshlrev_b32_sdwa v63,1,v63 dst_sel:DWORD dst_unused:UNUSED_PAD src0_sel:DWORD src1_sel:WORD_1
v_mfma_f32_16x16x32_bf16 v[164:167],a[236:239],v[28:31],v[164:167]
s_add_u32 m0,s57,49504
ds_read_u16 v60,v60
v_lshlrev_b32_sdwa v32,1,v32 dst_sel:DWORD dst_unused:UNUSED_PAD src0_sel:DWORD src1_sel:WORD_1
v_mfma_f32_16x16x32_bf16 v[160:163],a[236:239],v[24:27],v[160:163]
s_add_u32 s89,s56,6291456
ds_read_u16_d16_hi v62,v62
v_lshlrev_b32_sdwa v34,1,v34 dst_sel:DWORD dst_unused:UNUSED_PAD src0_sel:DWORD src1_sel:WORD_1
v_mfma_f32_16x16x32_bf16 v[240:243],a[244:247],v[24:27],v[240:243]
buffer_load_dwordx4 v6,s[36:39],s89 offen lds
ds_read_u16 v61,v61
v_mfma_f32_16x16x32_bf16 v[244:247],a[244:247],v[28:31],v[244:247]
ds_read_u16_d16_hi v63,v63
v_lshlrev_b32_sdwa v33,1,v33 dst_sel:DWORD dst_unused:UNUSED_PAD src0_sel:DWORD src1_sel:WORD_1
v_mfma_f32_16x16x32_bf16 v[180:183],a[252:255],v[28:31],v[180:183]
ds_read_u16 v32,v32
v_lshlrev_b32_sdwa v35,1,v35 dst_sel:DWORD dst_unused:UNUSED_PAD src0_sel:DWORD src1_sel:WORD_1
v_mfma_f32_16x16x32_bf16 v[176:179],a[252:255],v[24:27],v[176:179]
s_waitcnt lgkmcnt(6)
ds_read_u16_d16_hi v34,v34
v_mfma_f32_16x16x32_bf16 v[248:251],a[240:243],v[24:27],v[248:251]
ds_read_u16 v33,v33
v_mfma_f32_16x16x32_bf16 v[252:255],a[240:243],v[28:31],v[252:255]
ds_read_u16_d16_hi v35,v35
v_mfma_f32_16x16x32_bf16 v[196:199],a[224:227],v[28:31],v[196:199]
v_mfma_f32_16x16x32_bf16 v[192:195],a[224:227],v[24:27],v[192:195]
v_mfma_f32_16x16x32_bf16 v[28:31],a[216:219],a[8:11],v[16:19]
v_lshlrev_b32_sdwa v48,1,v48 dst_sel:DWORD dst_unused:UNUSED_PAD src0_sel:DWORD src1_sel:WORD_1
v_fma_mixlo_f16 v8,v53,s99,0
v_mfma_f32_16x16x32_bf16 v[28:31],a[220:223],a[24:27],v[28:31]
ds_read_u16 v48,v48
v_lshl_add_u64 v[40:41],v[40:41],0,v[42:43]
v_mfma_f32_16x16x32_bf16 v[28:31],a[192:195],a[40:43],v[28:31]
s_waitcnt lgkmcnt(5)
v_lshl_add_u64 v[42:43],v[36:37],0,v[38:39]
v_lshlrev_b32_sdwa v50,1,v50 dst_sel:DWORD dst_unused:UNUSED_PAD src0_sel:DWORD src1_sel:WORD_1
v_mfma_f32_16x16x32_bf16 v[24:27],a[196:199],a[40:43],v[20:23]
ds_read_u16_d16_hi v50,v50
v_fma_mixhi_f16 v8,v55,s99,0
v_mfma_f32_16x16x32_bf16 v[24:27],a[200:203],a[24:27],v[24:27]
v_lshl_add_u64 v[36:37],v[60:61],0,v[62:63]
v_lshlrev_b32_sdwa v49,1,v49 dst_sel:DWORD dst_unused:UNUSED_PAD src0_sel:DWORD src1_sel:WORD_1
v_pk_fma_f16 v9,v8,v8,s54
v_mfma_f32_16x16x32_bf16 v[24:27],a[212:215],a[8:11],v[24:27]
ds_read_u16 v49,v49
v_lshlrev_b32_sdwa v51,1,v51 dst_sel:DWORD dst_unused:UNUSED_PAD src0_sel:DWORD src1_sel:WORD_1
v_mfma_f32_16x16x32_bf16 v[60:63],a[212:215],a[12:15],v[20:23]
ds_read_b128 v[20:23],v4 offset:384
v_pk_mul_f16 v9,v8,v9
v_mfma_f32_16x16x32_bf16 v[60:63],a[200:203],a[28:31],v[60:63]
s_waitcnt lgkmcnt(4)
ds_read_u16_d16_hi v51,v51
ds_read_b64_tr_b16 a[212:213],v1 offset:16416
v_lshl_add_u64 v[38:39],v[32:33],0,v[34:35]
v_lshlrev_b32_sdwa v44,1,v44 dst_sel:DWORD dst_unused:UNUSED_PAD src0_sel:DWORD src1_sel:WORD_1
v_mfma_f32_16x16x32_bf16 v[60:63],a[196:199],a[44:47],v[60:63]
ds_read_b64_tr_b16 a[214:215],v1 offset:17440
v_exp_f16_e64 v8,v9
v_mfma_f32_16x16x32_bf16 v[32:35],a[192:195],a[44:47],v[16:19]
ds_read_b128 v[16:19],v4 offset:416
ds_read_u16 v44,v44
v_lshlrev_b32_sdwa v46,1,v46 dst_sel:DWORD dst_unused:UNUSED_PAD src0_sel:DWORD src1_sel:WORD_1
v_mfma_f32_16x16x32_bf16 v[32:35],a[220:223],a[28:31],v[32:35]
ds_read_u16_d16_hi v46,v46
ds_read_b64_tr_b16 a[200:201],v2 offset:16416
v_lshlrev_b32_sdwa v45,1,v45 dst_sel:DWORD dst_unused:UNUSED_PAD src0_sel:DWORD src1_sel:WORD_1
v_mfma_f32_16x16x32_bf16 v[32:35],a[216:219],a[12:15],v[32:35]
ds_read_u16 v45,v45
v_exp_f16_sdwa v8,v9 dst_sel:WORD_1 dst_unused:UNUSED_PRESERVE src0_sel:WORD_1
v_mfma_f32_16x16x32_bf16 v[208:211],a[232:235],v[40:43],v[208:211]
ds_read_b64_tr_b16 a[202:203],v2 offset:17440
ds_read_b64_tr_b16 a[196:197],v2 offset:14368
v_lshlrev_b32_sdwa v47,1,v47 dst_sel:DWORD dst_unused:UNUSED_PAD src0_sel:DWORD src1_sel:WORD_1
v_mfma_f32_16x16x32_bf16 v[212:215],a[232:235],v[36:39],v[212:215]
ds_read_u16_d16_hi v47,v47
v_lshlrev_b32_sdwa v56,1,v56 dst_sel:DWORD dst_unused:UNUSED_PAD src0_sel:DWORD src1_sel:WORD_1
v_mfma_f32_16x16x32_bf16 v[172:175],a[236:239],v[36:39],v[172:175]
ds_read_b64_tr_b16 a[198:199],v2 offset:15392
v_pk_fma_f16 v8,v8,s55,s55
v_mfma_f32_16x16x32_bf16 v[168:171],a[236:239],v[40:43],v[168:171]
TP6486_%=:
s_cbranch_execz TR0_%=
v_rcp_f16 v9,v8
v_lshlrev_b32_sdwa v58,1,v58 dst_sel:DWORD dst_unused:UNUSED_PAD src0_sel:DWORD src1_sel:WORD_1
v_mfma_f32_16x16x32_bf16 v[216:219],a[244:247],v[40:43],v[216:219]
ds_read_u16 v56,v56
v_lshlrev_b32_sdwa v57,1,v57 dst_sel:DWORD dst_unused:UNUSED_PAD src0_sel:DWORD src1_sel:WORD_1
v_mfma_f32_16x16x32_bf16 v[220:223],a[244:247],v[36:39],v[220:223]
ds_read_u16_d16_hi v58,v58
v_lshlrev_b32_sdwa v59,1,v59 dst_sel:DWORD dst_unused:UNUSED_PAD src0_sel:DWORD src1_sel:WORD_1
v_mfma_f32_16x16x32_bf16 v[188:191],a[252:255],v[36:39],v[188:191]
ds_read_u16 v57,v57
v_lshlrev_b32_sdwa v52,1,v52 dst_sel:DWORD dst_unused:UNUSED_PAD src0_sel:DWORD src1_sel:WORD_1
v_mfma_f32_16x16x32_bf16 v[184:187],a[252:255],v[40:43],v[184:187]
ds_read_u16_d16_hi v59,v59
v_rcp_f16_sdwa v9,v8 dst_sel:WORD_1 dst_unused:UNUSED_PRESERVE src0_sel:WORD_1
v_mfma_f32_16x16x32_bf16 v[224:227],a[240:243],v[40:43],v[224:227]
ds_read_b64_tr_b16 a[192:193],v2 offset:12320
v_lshlrev_b32_sdwa v54,1,v54 dst_sel:DWORD dst_unused:UNUSED_PAD src0_sel:DWORD src1_sel:WORD_1
v_mfma_f32_16x16x32_bf16 v[228:231],a[240:243],v[36:39],v[228:231]
ds_read_b64_tr_b16 a[194:195],v2 offset:13344
v_fma_mix_f32 v53,v53,v9,0 op_sel_hi:[0,1,0]
v_mfma_f32_16x16x32_bf16 v[204:207],a[224:227],v[36:39],v[204:207]
ds_read_u16 v52,v52
v_fma_mix_f32 v55,v55,v9,0 op_sel:[0,1,0] op_sel_hi:[0,1,0]
v_mfma_f32_16x16x32_bf16 v[200:203],a[224:227],v[40:43],v[200:203]
s_waitcnt lgkmcnt(7)
ds_read_u16_d16_hi v54,v54
v_mfma_f32_16x16x32_bf16 v[36:39],a[208:211],a[48:51],v[20:23]
s_add_u32 m0,s57,53040
v_lshlrev_b32_sdwa v28,1,v28 dst_sel:DWORD dst_unused:UNUSED_PAD src0_sel:DWORD src1_sel:WORD_1
v_lshlrev_b32_sdwa v30,1,v30 dst_sel:DWORD dst_unused:UNUSED_PAD src0_sel:DWORD src1_sel:WORD_1
v_mfma_f32_16x16x32_bf16 v[36:39],a[204:207],a[64:67],v[36:39]
s_add_u32 s89,s56,6389760
ds_read_u16 v28,v28
v_lshl_add_u64 v[48:49],v[48:49],0,v[50:51]
v_mfma_f32_16x16x32_bf16 v[36:39],a[212:215],a[80:83],v[36:39]
buffer_load_dwordx4 v6,s[36:39],s89 offen lds
v_lshl_add_u64 v[50:51],v[44:45],0,v[46:47]
v_mfma_f32_16x16x32_bf16 v[40:43],a[200:203],a[80:83],v[16:19]
ds_read_u16_d16_hi v30,v30
v_lshlrev_b32_sdwa v29,1,v29 dst_sel:DWORD dst_unused:UNUSED_PAD src0_sel:DWORD src1_sel:WORD_1
v_mfma_f32_16x16x32_bf16 v[40:43],a[196:199],a[64:67],v[40:43]
s_waitcnt lgkmcnt(4)
ds_read_u16 v29,v29
v_lshl_add_u64 v[44:45],v[56:57],0,v[58:59]
v_mfma_f32_16x16x32_bf16 v[40:43],a[192:195],a[48:51],v[40:43]
v_lshlrev_b32_sdwa v31,1,v31 dst_sel:DWORD dst_unused:UNUSED_PAD src0_sel:DWORD src1_sel:WORD_1
v_lshlrev_b32_sdwa v24,1,v24 dst_sel:DWORD dst_unused:UNUSED_PAD src0_sel:DWORD src1_sel:WORD_1
v_mfma_f32_16x16x32_bf16 v[56:59],a[192:195],a[52:55],v[16:19]
s_waitcnt lgkmcnt(3)
ds_read_u16_d16_hi v31,v31
v_or_b32_e64 v46,v52,v54
v_mfma_f32_16x16x32_bf16 v[56:59],a[196:199],a[68:71],v[56:59]
ds_read_u16 v24,v24
v_cvt_pk_bf16_f32 v47,v53,v55
v_lshlrev_b32_sdwa v26,1,v26 dst_sel:DWORD dst_unused:UNUSED_PAD src0_sel:DWORD src1_sel:WORD_1
v_mfma_f32_16x16x32_bf16 v[56:59],a[200:203],a[84:87],v[56:59]
ds_read_u16_d16_hi v26,v26
v_lshlrev_b32_sdwa v25,1,v25 dst_sel:DWORD dst_unused:UNUSED_PAD src0_sel:DWORD src1_sel:WORD_1
v_mfma_f32_16x16x32_bf16 v[52:55],a[212:215],a[84:87],v[20:23]
ds_read_u16 v25,v25
v_lshlrev_b32_sdwa v27,1,v27 dst_sel:DWORD dst_unused:UNUSED_PAD src0_sel:DWORD src1_sel:WORD_1
v_mfma_f32_16x16x32_bf16 v[52:55],a[204:207],a[68:71],v[52:55]
ds_read_u16_d16_hi v27,v27
v_lshlrev_b32_sdwa v32,1,v32 dst_sel:DWORD dst_unused:UNUSED_PAD src0_sel:DWORD src1_sel:WORD_1
v_mfma_f32_16x16x32_bf16 v[52:55],a[208:211],a[52:55],v[52:55]
ds_read_u16 v32,v32
v_lshlrev_b32_sdwa v34,1,v34 dst_sel:DWORD dst_unused:UNUSED_PAD src0_sel:DWORD src1_sel:WORD_1
v_mfma_f32_16x16x32_bf16 v[136:139],a[232:235],v[48:51],v[136:139]
ds_read_u16_d16_hi v34,v34
v_lshlrev_b32_sdwa v33,1,v33 dst_sel:DWORD dst_unused:UNUSED_PAD src0_sel:DWORD src1_sel:WORD_1
v_mfma_f32_16x16x32_bf16 v[140:143],a[232:235],v[44:47],v[140:143]
ds_read_u16 v33,v33
v_lshlrev_b32_sdwa v35,1,v35 dst_sel:DWORD dst_unused:UNUSED_PAD src0_sel:DWORD src1_sel:WORD_1
v_mfma_f32_16x16x32_bf16 v[68:71],a[236:239],v[44:47],v[68:71]
ds_read_u16_d16_hi v35,v35
ds_read_b64_tr_b16 a[248:249],v3 offset:18480
v_mfma_f32_16x16x32_bf16 v[64:67],a[236:239],v[48:51],v[64:67]
ds_read_b64_tr_b16 a[250:251],v3 offset:20016
v_lshlrev_b32_sdwa v60,1,v60 dst_sel:DWORD dst_unused:UNUSED_PAD src0_sel:DWORD src1_sel:WORD_1
v_mfma_f32_16x16x32_bf16 v[144:147],a[244:247],v[48:51],v[144:147]
ds_read_u16 v60,v60
v_lshlrev_b32_sdwa v62,1,v62 dst_sel:DWORD dst_unused:UNUSED_PAD src0_sel:DWORD src1_sel:WORD_1
v_mfma_f32_16x16x32_bf16 v[148:151],a[244:247],v[44:47],v[148:151]
ds_read_u16_d16_hi v62,v62
v_lshlrev_b32_sdwa v61,1,v61 dst_sel:DWORD dst_unused:UNUSED_PAD src0_sel:DWORD src1_sel:WORD_1
v_mfma_f32_16x16x32_bf16 v[84:87],a[252:255],v[44:47],v[84:87]
ds_read_u16 v61,v61
v_lshlrev_b32_sdwa v63,1,v63 dst_sel:DWORD dst_unused:UNUSED_PAD src0_sel:DWORD src1_sel:WORD_1
v_mfma_f32_16x16x32_bf16 v[80:83],a[252:255],v[48:51],v[80:83]
ds_read_u16_d16_hi v63,v63
v_mfma_f32_16x16x32_bf16 v[152:155],a[240:243],v[48:51],v[152:155]
ds_read_b64_tr_b16 a[228:229],v3 offset:18488
v_mfma_f32_16x16x32_bf16 v[156:159],a[240:243],v[44:47],v[156:159]
ds_read_b64_tr_b16 a[230:231],v3 offset:20024
v_mfma_f32_16x16x32_bf16 v[100:103],a[224:227],v[44:47],v[100:103]
v_mfma_f32_16x16x32_bf16 v[96:99],a[224:227],v[48:51],v[96:99]
v_mfma_f32_16x16x32_bf16 v[44:47],a[208:211],a[56:59],v[20:23]
ds_read_b64_tr_b16 a[220:221],v1 offset:18480
v_lshlrev_b32_sdwa v36,1,v36 dst_sel:DWORD dst_unused:UNUSED_PAD src0_sel:DWORD src1_sel:WORD_1
v_mfma_f32_16x16x32_bf16 v[44:47],a[204:207],a[72:75],v[44:47]
s_waitcnt lgkmcnt(6)
ds_read_u16 v36,v36
v_lshl_add_u64 v[28:29],v[28:29],0,v[30:31]
v_fma_mixlo_f16 v8,v57,s99,0
v_mfma_f32_16x16x32_bf16 v[44:47],a[212:215],a[88:91],v[44:47]
ds_read_b64_tr_b16 a[222:223],v1 offset:19504
v_lshl_add_u64 v[30:31],v[24:25],0,v[26:27]
v_lshlrev_b32_sdwa v38,1,v38 dst_sel:DWORD dst_unused:UNUSED_PAD src0_sel:DWORD src1_sel:WORD_1
v_mfma_f32_16x16x32_bf16 v[48:51],a[200:203],a[88:91],v[16:19]
ds_read_u16_d16_hi v38,v38
v_fma_mixhi_f16 v8,v59,s99,0
v_mfma_f32_16x16x32_bf16 v[48:51],a[196:199],a[72:75],v[48:51]
ds_read_b64_tr_b16 a[216:217],v1 offset:20528
v_lshl_add_u64 v[24:25],v[32:33],0,v[34:35]
v_lshlrev_b32_sdwa v37,1,v37 dst_sel:DWORD dst_unused:UNUSED_PAD src0_sel:DWORD src1_sel:WORD_1
v_pk_fma_f16 v9,v8,v8,s54
v_mfma_f32_16x16x32_bf16 v[48:51],a[192:195],a[56:59],v[48:51]
ds_read_u16 v37,v37
v_lshlrev_b32_sdwa v39,1,v39 dst_sel:DWORD dst_unused:UNUSED_PAD src0_sel:DWORD src1_sel:WORD_1
v_mfma_f32_16x16x32_bf16 v[32:35],a[192:195],a[60:63],v[16:19]
ds_read_u16_d16_hi v39,v39
v_pk_mul_f16 v9,v8,v9
v_mfma_f32_16x16x32_bf16 v[32:35],a[196:199],a[76:79],v[32:35]
s_waitcnt lgkmcnt(7)
ds_read_b64_tr_b16 a[218:219],v1 offset:21552
v_lshl_add_u64 v[26:27],v[60:61],0,v[62:63]
v_lshlrev_b32_sdwa v40,1,v40 dst_sel:DWORD dst_unused:UNUSED_PAD src0_sel:DWORD src1_sel:WORD_1
v_mfma_f32_16x16x32_bf16 v[32:35],a[200:203],a[92:95],v[32:35]
v_exp_f16 v8,v9
v_mfma_f32_16x16x32_bf16 v[60:63],a[212:215],a[92:95],v[20:23]
ds_read_u16 v40,v40
v_lshlrev_b32_sdwa v42,1,v42 dst_sel:DWORD dst_unused:UNUSED_PAD src0_sel:DWORD src1_sel:WORD_1
v_mfma_f32_16x16x32_bf16 v[60:63],a[204:207],a[76:79],v[60:63]
ds_read_u16_d16_hi v42,v42
v_lshlrev_b32_sdwa v41,1,v41 dst_sel:DWORD dst_unused:UNUSED_PAD src0_sel:DWORD src1_sel:WORD_1
v_mfma_f32_16x16x32_bf16 v[60:63],a[208:211],a[60:63],v[60:63]
v_exp_f16_sdwa v8,v9 dst_sel:WORD_1 dst_unused:UNUSED_PRESERVE src0_sel:WORD_1
v_mfma_f32_16x16x32_bf16 v[112:115],a[232:235],v[28:31],v[112:115]
ds_read_u16 v41,v41
v_lshlrev_b32_sdwa v43,1,v43 dst_sel:DWORD dst_unused:UNUSED_PAD src0_sel:DWORD src1_sel:WORD_1
v_mfma_f32_16x16x32_bf16 v[116:119],a[232:235],v[24:27],v[116:119]
ds_read_u16_d16_hi v43,v43
v_lshlrev_b32_sdwa v52,1,v52 dst_sel:DWORD dst_unused:UNUSED_PAD src0_sel:DWORD src1_sel:WORD_1
v_mfma_f32_16x16x32_bf16 v[76:79],a[236:239],v[24:27],v[76:79]
ds_read_u16 v52,v52
v_pk_fma_f16 v8,v8,s55,s55
v_mfma_f32_16x16x32_bf16 v[72:75],a[236:239],v[28:31],v[72:75]
ds_read_b64_tr_b16 a[232:233],v3 offset:18544
v_rcp_f16_e64 v9,v8
v_lshlrev_b32_sdwa v54,1,v54 dst_sel:DWORD dst_unused:UNUSED_PAD src0_sel:DWORD src1_sel:WORD_1
v_mfma_f32_16x16x32_bf16 v[120:123],a[244:247],v[28:31],v[120:123]
ds_read_u16_d16_hi v54,v54
v_lshlrev_b32_sdwa v53,1,v53 dst_sel:DWORD dst_unused:UNUSED_PAD src0_sel:DWORD src1_sel:WORD_1
v_mfma_f32_16x16x32_bf16 v[124:127],a[244:247],v[24:27],v[124:127]
ds_read_u16 v53,v53
ds_read_b64_tr_b16 a[234:235],v3 offset:20080
v_mfma_f32_16x16x32_bf16 v[92:95],a[252:255],v[24:27],v[92:95]
ds_read_b64_tr_b16 a[236:237],v3 offset:18552
v_lshlrev_b32_sdwa v55,1,v55 dst_sel:DWORD dst_unused:UNUSED_PAD src0_sel:DWORD src1_sel:WORD_1
v_mfma_f32_16x16x32_bf16 v[88:91],a[252:255],v[28:31],v[88:91]
ds_read_u16_d16_hi v55,v55
v_rcp_f16_sdwa v9,v8 dst_sel:WORD_1 dst_unused:UNUSED_PRESERVE src0_sel:WORD_1
v_mfma_f32_16x16x32_bf16 v[128:131],a[240:243],v[28:31],v[128:131]
ds_read_b64_tr_b16 a[238:239],v3 offset:20088
ds_read_b64_tr_b16 a[244:245],v3 offset:18608
v_lshlrev_b32_sdwa v56,1,v56 dst_sel:DWORD dst_unused:UNUSED_PAD src0_sel:DWORD src1_sel:WORD_1
v_mfma_f32_16x16x32_bf16 v[132:135],a[240:243],v[24:27],v[132:135]
ds_read_u16 v56,v56
v_lshlrev_b32_sdwa v58,1,v58 dst_sel:DWORD dst_unused:UNUSED_PAD src0_sel:DWORD src1_sel:WORD_1
v_mfma_f32_16x16x32_bf16 v[108:111],a[224:227],v[24:27],v[108:111]
ds_read_u16_d16_hi v58,v58
v_fma_mix_f32 v57,v57,v9,0 op_sel_hi:[0,1,0]
v_mfma_f32_16x16x32_bf16 v[104:107],a[224:227],v[28:31],v[104:107]
ds_read_b64_tr_b16 a[246:247],v3 offset:20144
v_fma_mix_f32 v59,v59,v9,0 op_sel:[0,1,0] op_sel_hi:[0,1,0]
v_mfma_f32_16x16x32_bf16 v[24:27],a[208:211],a[0:3],v[20:23]
ds_read_b64_tr_b16 a[252:253],v3 offset:18616
v_lshlrev_b32_sdwa v44,1,v44 dst_sel:DWORD dst_unused:UNUSED_PAD src0_sel:DWORD src1_sel:WORD_1
v_mfma_f32_16x16x32_bf16 v[24:27],a[204:207],a[16:19],v[24:27]
s_waitcnt lgkmcnt(10)
ds_read_b64_tr_b16 a[254:255],v3 offset:20152
v_lshl_add_u64 v[36:37],v[36:37],0,v[38:39]
v_mfma_f32_16x16x32_bf16 v[24:27],a[212:215],a[32:35],v[24:27]
ds_read_u16 v44,v44
v_lshl_add_u64 v[38:39],v[40:41],0,v[42:43]
v_lshlrev_b32_sdwa v46,1,v46 dst_sel:DWORD dst_unused:UNUSED_PAD src0_sel:DWORD src1_sel:WORD_1
v_mfma_f32_16x16x32_bf16 v[28:31],a[200:203],a[32:35],v[16:19]
ds_read_u16_d16_hi v46,v46
v_lshlrev_b32_sdwa v45,1,v45 dst_sel:DWORD dst_unused:UNUSED_PAD src0_sel:DWORD src1_sel:WORD_1
v_mfma_f32_16x16x32_bf16 v[28:31],a[196:199],a[16:19],v[28:31]
s_waitcnt lgkmcnt(9)
ds_read_u16 v45,v45
v_lshl_add_u64 v[40:41],v[52:53],0,v[54:55]
v_mfma_f32_16x16x32_bf16 v[28:31],a[192:195],a[0:3],v[28:31]
v_lshlrev_b32_sdwa v47,1,v47 dst_sel:DWORD dst_unused:UNUSED_PAD src0_sel:DWORD src1_sel:WORD_1
v_lshlrev_b32_sdwa v48,1,v48 dst_sel:DWORD dst_unused:UNUSED_PAD src0_sel:DWORD src1_sel:WORD_1
v_mfma_f32_16x16x32_bf16 v[52:55],a[192:195],a[4:7],v[16:19]
s_waitcnt lgkmcnt(6)
ds_read_u16_d16_hi v47,v47
v_or_b32 v42,v56,v58
v_mfma_f32_16x16x32_bf16 v[52:55],a[196:199],a[20:23],v[52:55]
ds_read_u16 v48,v48
v_cvt_pk_bf16_f32 v43,v57,v59
v_lshlrev_b32_sdwa v50,1,v50 dst_sel:DWORD dst_unused:UNUSED_PAD src0_sel:DWORD src1_sel:WORD_1
v_mfma_f32_16x16x32_bf16 v[52:55],a[200:203],a[36:39],v[52:55]
ds_read_u16_d16_hi v50,v50
v_lshlrev_b32_sdwa v49,1,v49 dst_sel:DWORD dst_unused:UNUSED_PAD src0_sel:DWORD src1_sel:WORD_1
v_mfma_f32_16x16x32_bf16 v[56:59],a[212:215],a[36:39],v[20:23]
ds_read_u16 v49,v49
v_lshlrev_b32_sdwa v51,1,v51 dst_sel:DWORD dst_unused:UNUSED_PAD src0_sel:DWORD src1_sel:WORD_1
v_mfma_f32_16x16x32_bf16 v[56:59],a[204:207],a[20:23],v[56:59]
ds_read_u16_d16_hi v51,v51
v_lshlrev_b32_sdwa v60,1,v60 dst_sel:DWORD dst_unused:UNUSED_PAD src0_sel:DWORD src1_sel:WORD_1
v_mfma_f32_16x16x32_bf16 v[56:59],a[208:211],a[4:7],v[56:59]
ds_read_u16 v60,v60
v_lshlrev_b32_sdwa v62,1,v62 dst_sel:DWORD dst_unused:UNUSED_PAD src0_sel:DWORD src1_sel:WORD_1
v_mfma_f32_16x16x32_bf16 v[232:235],a[248:251],v[36:39],v[232:235]
ds_read_u16_d16_hi v62,v62
v_lshlrev_b32_sdwa v61,1,v61 dst_sel:DWORD dst_unused:UNUSED_PAD src0_sel:DWORD src1_sel:WORD_1
v_mfma_f32_16x16x32_bf16 v[236:239],a[248:251],v[40:43],v[236:239]
ds_read_u16 v61,v61
v_lshlrev_b32_sdwa v63,1,v63 dst_sel:DWORD dst_unused:UNUSED_PAD src0_sel:DWORD src1_sel:WORD_1
v_mfma_f32_16x16x32_bf16 v[164:167],a[228:231],v[40:43],v[164:167]
ds_read_u16_d16_hi v63,v63
v_lshlrev_b32_sdwa v32,1,v32 dst_sel:DWORD dst_unused:UNUSED_PAD src0_sel:DWORD src1_sel:WORD_1
v_mfma_f32_16x16x32_bf16 v[160:163],a[228:231],v[36:39],v[160:163]
ds_read_u16 v32,v32
v_lshlrev_b32_sdwa v34,1,v34 dst_sel:DWORD dst_unused:UNUSED_PAD src0_sel:DWORD src1_sel:WORD_1
v_mfma_f32_16x16x32_bf16 v[240:243],a[232:235],v[36:39],v[240:243]
ds_read_u16_d16_hi v34,v34
v_lshlrev_b32_sdwa v33,1,v33 dst_sel:DWORD dst_unused:UNUSED_PAD src0_sel:DWORD src1_sel:WORD_1
v_mfma_f32_16x16x32_bf16 v[244:247],a[232:235],v[40:43],v[244:247]
ds_read_u16 v33,v33
v_lshlrev_b32_sdwa v35,1,v35 dst_sel:DWORD dst_unused:UNUSED_PAD src0_sel:DWORD src1_sel:WORD_1
v_mfma_f32_16x16x32_bf16 v[180:183],a[236:239],v[40:43],v[180:183]
ds_read_u16_d16_hi v35,v35
v_mfma_f32_16x16x32_bf16 v[176:179],a[236:239],v[36:39],v[176:179]
s_waitcnt lgkmcnt(8)
v_mfma_f32_16x16x32_bf16 v[248:251],a[244:247],v[36:39],v[248:251]
v_mfma_f32_16x16x32_bf16 v[252:255],a[244:247],v[40:43],v[252:255]
v_mfma_f32_16x16x32_bf16 v[196:199],a[252:255],v[40:43],v[196:199]
v_mfma_f32_16x16x32_bf16 v[192:195],a[252:255],v[36:39],v[192:195]
v_mfma_f32_16x16x32_bf16 v[40:43],a[208:211],a[8:11],v[20:23]
v_lshlrev_b32_sdwa v24,1,v24 dst_sel:DWORD dst_unused:UNUSED_PAD src0_sel:DWORD src1_sel:WORD_1
v_fma_mixlo_f16 v8,v53,s99,0
v_mfma_f32_16x16x32_bf16 v[40:43],a[204:207],a[24:27],v[40:43]
ds_read_u16 v24,v24
v_lshl_add_u64 v[44:45],v[44:45],0,v[46:47]
v_mfma_f32_16x16x32_bf16 v[40:43],a[212:215],a[40:43],v[40:43]
v_lshl_add_u64 v[46:47],v[48:49],0,v[50:51]
v_lshlrev_b32_sdwa v26,1,v26 dst_sel:DWORD dst_unused:UNUSED_PAD src0_sel:DWORD src1_sel:WORD_1
v_mfma_f32_16x16x32_bf16 v[36:39],a[200:203],a[40:43],v[16:19]
ds_read_u16_d16_hi v26,v26
v_fma_mixhi_f16 v8,v55,s99,0
v_mfma_f32_16x16x32_bf16 v[36:39],a[196:199],a[24:27],v[36:39]
s_waitcnt lgkmcnt(2)
v_lshl_add_u64 v[48:49],v[60:61],0,v[62:63]
v_lshlrev_b32_sdwa v25,1,v25 dst_sel:DWORD dst_unused:UNUSED_PAD src0_sel:DWORD src1_sel:WORD_1
v_pk_fma_f16 v9,v8,v8,s54
v_mfma_f32_16x16x32_bf16 v[36:39],a[192:195],a[8:11],v[36:39]
ds_read_u16 v25,v25
v_lshlrev_b32_sdwa v27,1,v27 dst_sel:DWORD dst_unused:UNUSED_PAD src0_sel:DWORD src1_sel:WORD_1
v_mfma_f32_16x16x32_bf16 v[60:63],a[192:195],a[12:15],v[16:19]
ds_read_b128 v[16:19],v4 offset:512
v_pk_mul_f16 v9,v8,v9
v_mfma_f32_16x16x32_bf16 v[60:63],a[196:199],a[28:31],v[60:63]
ds_read_u16_d16_hi v27,v27
ds_read_b64_tr_b16 a[192:193],v1 offset:22576
v_lshl_add_u64 v[50:51],v[32:33],0,v[34:35]
v_lshlrev_b32_sdwa v28,1,v28 dst_sel:DWORD dst_unused:UNUSED_PAD src0_sel:DWORD src1_sel:WORD_1
v_mfma_f32_16x16x32_bf16 v[60:63],a[200:203],a[44:47],v[60:63]
ds_read_b64_tr_b16 a[194:195],v1 offset:23600
v_exp_f16 v8,v9
v_mfma_f32_16x16x32_bf16 v[32:35],a[212:215],a[44:47],v[20:23]
ds_read_b128 v[20:23],v4 offset:544
ds_read_u16 v28,v28
v_lshlrev_b32_sdwa v30,1,v30 dst_sel:DWORD dst_unused:UNUSED_PAD src0_sel:DWORD src1_sel:WORD_1
v_mfma_f32_16x16x32_bf16 v[32:35],a[204:207],a[28:31],v[32:35]
ds_read_u16_d16_hi v30,v30
ds_read_b64_tr_b16 a[196:197],v2 offset:22576
v_lshlrev_b32_sdwa v29,1,v29 dst_sel:DWORD dst_unused:UNUSED_PAD src0_sel:DWORD src1_sel:WORD_1
v_mfma_f32_16x16x32_bf16 v[32:35],a[208:211],a[12:15],v[32:35]
ds_read_u16 v29,v29
v_exp_f16_sdwa v8,v9 dst_sel:WORD_1 dst_unused:UNUSED_PRESERVE src0_sel:WORD_1
v_mfma_f32_16x16x32_bf16 v[208:211],a[248:251],v[44:47],v[208:211]
ds_read_b64_tr_b16 a[198:199],v2 offset:23600
ds_read_b64_tr_b16 a[200:201],v2 offset:20528
v_lshlrev_b32_sdwa v31,1,v31 dst_sel:DWORD dst_unused:UNUSED_PAD src0_sel:DWORD src1_sel:WORD_1
v_mfma_f32_16x16x32_bf16 v[212:215],a[248:251],v[48:51],v[212:215]
ds_read_u16_d16_hi v31,v31
v_lshlrev_b32_sdwa v56,1,v56 dst_sel:DWORD dst_unused:UNUSED_PAD src0_sel:DWORD src1_sel:WORD_1
v_mfma_f32_16x16x32_bf16 v[172:175],a[228:231],v[48:51],v[172:175]
ds_read_b64_tr_b16 a[202:203],v2 offset:21552
v_pk_fma_f16 v8,v8,s55,s55
v_mfma_f32_16x16x32_bf16 v[168:171],a[228:231],v[44:47],v[168:171]
v_rcp_f16 v9,v8
v_lshlrev_b32_sdwa v58,1,v58 dst_sel:DWORD dst_unused:UNUSED_PAD src0_sel:DWORD src1_sel:WORD_1
v_mfma_f32_16x16x32_bf16 v[216:219],a[232:235],v[44:47],v[216:219]
ds_read_u16 v56,v56
v_lshlrev_b32_sdwa v57,1,v57 dst_sel:DWORD dst_unused:UNUSED_PAD src0_sel:DWORD src1_sel:WORD_1
v_mfma_f32_16x16x32_bf16 v[220:223],a[232:235],v[48:51],v[220:223]
ds_read_u16_d16_hi v58,v58
v_lshlrev_b32_sdwa v59,1,v59 dst_sel:DWORD dst_unused:UNUSED_PAD src0_sel:DWORD src1_sel:WORD_1
v_mfma_f32_16x16x32_bf16 v[188:191],a[236:239],v[48:51],v[188:191]
ds_read_u16 v57,v57
v_lshlrev_b32_sdwa v52,1,v52 dst_sel:DWORD dst_unused:UNUSED_PAD src0_sel:DWORD src1_sel:WORD_1
v_mfma_f32_16x16x32_bf16 v[184:187],a[236:239],v[44:47],v[184:187]
ds_read_u16_d16_hi v59,v59
v_rcp_f16_sdwa v9,v8 dst_sel:WORD_1 dst_unused:UNUSED_PRESERVE src0_sel:WORD_1
v_mfma_f32_16x16x32_bf16 v[224:227],a[244:247],v[44:47],v[224:227]
ds_read_b64_tr_b16 a[212:213],v2 offset:18480
v_lshlrev_b32_sdwa v54,1,v54 dst_sel:DWORD dst_unused:UNUSED_PAD src0_sel:DWORD src1_sel:WORD_1
v_mfma_f32_16x16x32_bf16 v[228:231],a[244:247],v[48:51],v[228:231]
ds_read_b64_tr_b16 a[214:215],v2 offset:19504
v_fma_mix_f32 v53,v53,v9,0 op_sel_hi:[0,1,0]
v_mfma_f32_16x16x32_bf16 v[204:207],a[252:255],v[48:51],v[204:207]
ds_read_u16 v52,v52
v_fma_mix_f32 v55,v55,v9,0 op_sel:[0,1,0] op_sel_hi:[0,1,0]
v_mfma_f32_16x16x32_bf16 v[200:203],a[252:255],v[44:47],v[200:203]
s_waitcnt lgkmcnt(7)
ds_read_u16_d16_hi v54,v54
v_mfma_f32_16x16x32_bf16 v[48:51],a[220:223],a[48:51],v[16:19]
v_lshlrev_b32_sdwa v40,1,v40 dst_sel:DWORD dst_unused:UNUSED_PAD src0_sel:DWORD src1_sel:WORD_1
v_lshlrev_b32_sdwa v42,1,v42 dst_sel:DWORD dst_unused:UNUSED_PAD src0_sel:DWORD src1_sel:WORD_1
v_mfma_f32_16x16x32_bf16 v[48:51],a[216:219],a[64:67],v[48:51]
ds_read_u16 v40,v40
v_lshl_add_u64 v[24:25],v[24:25],0,v[26:27]
v_mfma_f32_16x16x32_bf16 v[48:51],a[192:195],a[80:83],v[48:51]
ds_read_u16_d16_hi v42,v42
v_lshl_add_u64 v[26:27],v[28:29],0,v[30:31]
v_mfma_f32_16x16x32_bf16 v[44:47],a[196:199],a[80:83],v[20:23]
v_lshlrev_b32_sdwa v41,1,v41 dst_sel:DWORD dst_unused:UNUSED_PAD src0_sel:DWORD src1_sel:WORD_1
v_lshlrev_b32_sdwa v43,1,v43 dst_sel:DWORD dst_unused:UNUSED_PAD src0_sel:DWORD src1_sel:WORD_1
v_mfma_f32_16x16x32_bf16 v[44:47],a[200:203],a[64:67],v[44:47]
s_waitcnt lgkmcnt(4)
ds_read_u16 v41,v41
v_lshl_add_u64 v[28:29],v[56:57],0,v[58:59]
v_mfma_f32_16x16x32_bf16 v[44:47],a[212:215],a[48:51],v[44:47]
ds_read_u16_d16_hi v43,v43
v_lshlrev_b32_sdwa v36,1,v36 dst_sel:DWORD dst_unused:UNUSED_PAD src0_sel:DWORD src1_sel:WORD_1
v_mfma_f32_16x16x32_bf16 v[56:59],a[212:215],a[52:55],v[20:23]
s_waitcnt lgkmcnt(4)
ds_read_u16 v36,v36
v_or_b32 v30,v52,v54
v_mfma_f32_16x16x32_bf16 v[56:59],a[200:203],a[68:71],v[56:59]
s_waitcnt vmcnt(6)
v_cvt_pk_bf16_f32 v31,v53,v55
v_lshlrev_b32_sdwa v38,1,v38 dst_sel:DWORD dst_unused:UNUSED_PAD src0_sel:DWORD src1_sel:WORD_1
v_mfma_f32_16x16x32_bf16 v[56:59],a[196:199],a[84:87],v[56:59]
s_barrier
ds_read_u16_d16_hi v38,v38
v_lshlrev_b32_sdwa v37,1,v37 dst_sel:DWORD dst_unused:UNUSED_PAD src0_sel:DWORD src1_sel:WORD_1
v_mfma_f32_16x16x32_bf16 v[52:55],a[192:195],a[84:87],v[16:19]
ds_read_u16 v37,v37
v_lshlrev_b32_sdwa v39,1,v39 dst_sel:DWORD dst_unused:UNUSED_PAD src0_sel:DWORD src1_sel:WORD_1
v_mfma_f32_16x16x32_bf16 v[52:55],a[216:219],a[68:71],v[52:55]
s_add_u32 m0,s58,12320
ds_read_u16_d16_hi v39,v39
v_lshlrev_b32_sdwa v32,1,v32 dst_sel:DWORD dst_unused:UNUSED_PAD src0_sel:DWORD src1_sel:WORD_1
v_mfma_f32_16x16x32_bf16 v[52:55],a[220:223],a[52:55],v[52:55]
s_add_u32 s69,s69,s59
ds_read_u16 v32,v32
v_lshlrev_b32_sdwa v34,1,v34 dst_sel:DWORD dst_unused:UNUSED_PAD src0_sel:DWORD src1_sel:WORD_1
v_mfma_f32_16x16x32_bf16 v[136:139],a[248:251],v[24:27],v[136:139]
buffer_load_dwordx4 v5,s[44:47],s69 offen offset:0 lds
v_lshlrev_b32_sdwa v33,1,v33 dst_sel:DWORD dst_unused:UNUSED_PAD src0_sel:DWORD src1_sel:WORD_1
v_mfma_f32_16x16x32_bf16 v[140:143],a[248:251],v[28:31],v[140:143]
ds_read_u16_d16_hi v34,v34
v_lshlrev_b32_sdwa v35,1,v35 dst_sel:DWORD dst_unused:UNUSED_PAD src0_sel:DWORD src1_sel:WORD_1
v_mfma_f32_16x16x32_bf16 v[68:71],a[228:231],v[28:31],v[68:71]
ds_read_u16 v33,v33
ds_read_u16_d16_hi v35,v35
v_mfma_f32_16x16x32_bf16 v[64:67],a[228:231],v[24:27],v[64:67]
ds_read_b64_tr_b16 a[240:241],v3 offset:0
v_lshlrev_b32_sdwa v60,1,v60 dst_sel:DWORD dst_unused:UNUSED_PAD src0_sel:DWORD src1_sel:WORD_1
v_mfma_f32_16x16x32_bf16 v[144:147],a[232:235],v[24:27],v[144:147]
s_add_u32 s69,s69,s53
ds_read_u16 v60,v60
v_lshlrev_b32_sdwa v62,1,v62 dst_sel:DWORD dst_unused:UNUSED_PAD src0_sel:DWORD src1_sel:WORD_1
v_mfma_f32_16x16x32_bf16 v[148:151],a[232:235],v[28:31],v[148:151]
buffer_load_dwordx4 v5,s[44:47],s69 offen offset:1024 lds
ds_read_u16_d16_hi v62,v62
v_mfma_f32_16x16x32_bf16 v[84:87],a[236:239],v[28:31],v[84:87]
ds_read_b64_tr_b16 a[242:243],v3 offset:1536
v_lshlrev_b32_sdwa v61,1,v61 dst_sel:DWORD dst_unused:UNUSED_PAD src0_sel:DWORD src1_sel:WORD_1
v_mfma_f32_16x16x32_bf16 v[80:83],a[236:239],v[24:27],v[80:83]
ds_read_u16 v61,v61
v_lshlrev_b32_sdwa v63,1,v63 dst_sel:DWORD dst_unused:UNUSED_PAD src0_sel:DWORD src1_sel:WORD_1
v_mfma_f32_16x16x32_bf16 v[152:155],a[244:247],v[24:27],v[152:155]
ds_read_u16_d16_hi v63,v63
v_mfma_f32_16x16x32_bf16 v[156:159],a[244:247],v[28:31],v[156:159]
s_add_u32 s69,s69,s53
ds_read_b64_tr_b16 a[224:225],v3 offset:8
v_mfma_f32_16x16x32_bf16 v[100:103],a[252:255],v[28:31],v[100:103]
buffer_load_dwordx4 v5,s[44:47],s69 offen offset:2048 lds
v_mfma_f32_16x16x32_bf16 v[96:99],a[252:255],v[24:27],v[96:99]
ds_read_b64_tr_b16 a[226:227],v3 offset:1544
v_mfma_f32_16x16x32_bf16 v[28:31],a[220:223],a[56:59],v[16:19]
ds_read_b64_tr_b16 a[204:205],v1 offset:0
v_lshlrev_b32_sdwa v48,1,v48 dst_sel:DWORD dst_unused:UNUSED_PAD src0_sel:DWORD src1_sel:WORD_1
v_mfma_f32_16x16x32_bf16 v[28:31],a[216:219],a[72:75],v[28:31]
s_waitcnt lgkmcnt(7)
ds_read_u16 v48,v48
v_lshl_add_u64 v[40:41],v[40:41],0,v[42:43]
v_fma_mixlo_f16 v8,v57,s99,0
v_mfma_f32_16x16x32_bf16 v[28:31],a[192:195],a[88:91],v[28:31]
ds_read_b64_tr_b16 a[206:207],v1 offset:1024
v_lshl_add_u64 v[42:43],v[36:37],0,v[38:39]
v_lshlrev_b32_sdwa v50,1,v50 dst_sel:DWORD dst_unused:UNUSED_PAD src0_sel:DWORD src1_sel:WORD_1
v_mfma_f32_16x16x32_bf16 v[24:27],a[196:199],a[88:91],v[20:23]
ds_read_u16_d16_hi v50,v50
v_fma_mixhi_f16 v8,v59,s99,0
v_mfma_f32_16x16x32_bf16 v[24:27],a[200:203],a[72:75],v[24:27]
ds_read_b64_tr_b16 a[208:209],v1 offset:2048
v_lshl_add_u64 v[36:37],v[32:33],0,v[34:35]
v_lshlrev_b32_sdwa v49,1,v49 dst_sel:DWORD dst_unused:UNUSED_PAD src0_sel:DWORD src1_sel:WORD_1
v_pk_fma_f16 v9,v8,v8,s54
v_mfma_f32_16x16x32_bf16 v[24:27],a[212:215],a[56:59],v[24:27]
ds_read_u16 v49,v49
v_lshlrev_b32_sdwa v51,1,v51 dst_sel:DWORD dst_unused:UNUSED_PAD src0_sel:DWORD src1_sel:WORD_1
v_mfma_f32_16x16x32_bf16 v[32:35],a[212:215],a[60:63],v[20:23]
ds_read_u16_d16_hi v51,v51
v_pk_mul_f16 v9,v8,v9
v_mfma_f32_16x16x32_bf16 v[32:35],a[200:203],a[76:79],v[32:35]
s_waitcnt lgkmcnt(8)
ds_read_b64_tr_b16 a[210:211],v1 offset:3072
v_lshl_add_u64 v[38:39],v[60:61],0,v[62:63]
v_lshlrev_b32_sdwa v44,1,v44 dst_sel:DWORD dst_unused:UNUSED_PAD src0_sel:DWORD src1_sel:WORD_1
v_mfma_f32_16x16x32_bf16 v[32:35],a[196:199],a[92:95],v[32:35]
v_exp_f16 v8,v9
v_mfma_f32_16x16x32_bf16 v[60:63],a[192:195],a[92:95],v[16:19]
ds_read_u16 v44,v44
v_lshlrev_b32_sdwa v46,1,v46 dst_sel:DWORD dst_unused:UNUSED_PAD src0_sel:DWORD src1_sel:WORD_1
v_mfma_f32_16x16x32_bf16 v[60:63],a[216:219],a[76:79],v[60:63]
ds_read_u16_d16_hi v46,v46
v_lshlrev_b32_sdwa v45,1,v45 dst_sel:DWORD dst_unused:UNUSED_PAD src0_sel:DWORD src1_sel:WORD_1
v_mfma_f32_16x16x32_bf16 v[60:63],a[220:223],a[60:63],v[60:63]
v_exp_f16_sdwa v8,v9 dst_sel:WORD_1 dst_unused:UNUSED_PRESERVE src0_sel:WORD_1
v_mfma_f32_16x16x32_bf16 v[112:115],a[248:251],v[40:43],v[112:115]
ds_read_u16 v45,v45
v_lshlrev_b32_sdwa v47,1,v47 dst_sel:DWORD dst_unused:UNUSED_PAD src0_sel:DWORD src1_sel:WORD_1
v_mfma_f32_16x16x32_bf16 v[116:119],a[248:251],v[36:39],v[116:119]
ds_read_u16_d16_hi v47,v47
v_lshlrev_b32_sdwa v52,1,v52 dst_sel:DWORD dst_unused:UNUSED_PAD src0_sel:DWORD src1_sel:WORD_1
v_mfma_f32_16x16x32_bf16 v[76:79],a[228:231],v[36:39],v[76:79]
ds_read_u16 v52,v52
v_pk_fma_f16 v8,v8,s55,s55
v_mfma_f32_16x16x32_bf16 v[72:75],a[228:231],v[40:43],v[72:75]
ds_read_b64_tr_b16 a[248:249],v3 offset:64
v_rcp_f16_e64 v9,v8
v_lshlrev_b32_sdwa v54,1,v54 dst_sel:DWORD dst_unused:UNUSED_PAD src0_sel:DWORD src1_sel:WORD_1
v_mfma_f32_16x16x32_bf16 v[120:123],a[232:235],v[40:43],v[120:123]
ds_read_u16_d16_hi v54,v54
v_lshlrev_b32_sdwa v53,1,v53 dst_sel:DWORD dst_unused:UNUSED_PAD src0_sel:DWORD src1_sel:WORD_1
v_mfma_f32_16x16x32_bf16 v[124:127],a[232:235],v[36:39],v[124:127]
ds_read_u16 v53,v53
ds_read_b64_tr_b16 a[250:251],v3 offset:1600
v_mfma_f32_16x16x32_bf16 v[92:95],a[236:239],v[36:39],v[92:95]
ds_read_b64_tr_b16 a[228:229],v3 offset:72
v_lshlrev_b32_sdwa v55,1,v55 dst_sel:DWORD dst_unused:UNUSED_PAD src0_sel:DWORD src1_sel:WORD_1
v_mfma_f32_16x16x32_bf16 v[88:91],a[236:239],v[40:43],v[88:91]
ds_read_u16_d16_hi v55,v55
v_rcp_f16_sdwa v9,v8 dst_sel:WORD_1 dst_unused:UNUSED_PRESERVE src0_sel:WORD_1
v_mfma_f32_16x16x32_bf16 v[128:131],a[244:247],v[40:43],v[128:131]
ds_read_b64_tr_b16 a[230:231],v3 offset:1608
ds_read_b64_tr_b16 a[232:233],v3 offset:128
v_lshlrev_b32_sdwa v56,1,v56 dst_sel:DWORD dst_unused:UNUSED_PAD src0_sel:DWORD src1_sel:WORD_1
v_mfma_f32_16x16x32_bf16 v[132:135],a[244:247],v[36:39],v[132:135]
ds_read_u16 v56,v56
v_lshlrev_b32_sdwa v58,1,v58 dst_sel:DWORD dst_unused:UNUSED_PAD src0_sel:DWORD src1_sel:WORD_1
v_mfma_f32_16x16x32_bf16 v[108:111],a[252:255],v[36:39],v[108:111]
ds_read_u16_d16_hi v58,v58
v_fma_mix_f32 v57,v57,v9,0 op_sel_hi:[0,1,0]
v_mfma_f32_16x16x32_bf16 v[104:107],a[252:255],v[40:43],v[104:107]
ds_read_b64_tr_b16 a[234:235],v3 offset:1664
v_fma_mix_f32 v59,v59,v9,0 op_sel:[0,1,0] op_sel_hi:[0,1,0]
v_mfma_f32_16x16x32_bf16 v[36:39],a[220:223],a[0:3],v[16:19]
s_add_u32 m0,s58,18480
ds_read_b64_tr_b16 a[236:237],v3 offset:136
v_lshlrev_b32_sdwa v28,1,v28 dst_sel:DWORD dst_unused:UNUSED_PAD src0_sel:DWORD src1_sel:WORD_1
v_mfma_f32_16x16x32_bf16 v[36:39],a[216:219],a[16:19],v[36:39]
s_waitcnt lgkmcnt(10)
ds_read_b64_tr_b16 a[238:239],v3 offset:1672
v_lshl_add_u64 v[48:49],v[48:49],0,v[50:51]
v_mfma_f32_16x16x32_bf16 v[36:39],a[192:195],a[32:35],v[36:39]
s_add_u32 s69,s69,s59
ds_read_u16 v28,v28
v_lshl_add_u64 v[50:51],v[44:45],0,v[46:47]
v_lshlrev_b32_sdwa v30,1,v30 dst_sel:DWORD dst_unused:UNUSED_PAD src0_sel:DWORD src1_sel:WORD_1
v_mfma_f32_16x16x32_bf16 v[40:43],a[196:199],a[32:35],v[20:23]
buffer_load_dwordx4 v5,s[44:47],s69 offen offset:0 lds
v_lshlrev_b32_sdwa v29,1,v29 dst_sel:DWORD dst_unused:UNUSED_PAD src0_sel:DWORD src1_sel:WORD_1
v_mfma_f32_16x16x32_bf16 v[40:43],a[200:203],a[16:19],v[40:43]
s_waitcnt lgkmcnt(8)
ds_read_u16_d16_hi v30,v30
v_lshl_add_u64 v[44:45],v[52:53],0,v[54:55]
v_mfma_f32_16x16x32_bf16 v[40:43],a[212:215],a[0:3],v[40:43]
s_add_u32 s69,s69,s53
ds_read_u16 v29,v29
v_lshlrev_b32_sdwa v31,1,v31 dst_sel:DWORD dst_unused:UNUSED_PAD src0_sel:DWORD src1_sel:WORD_1
v_mfma_f32_16x16x32_bf16 v[52:55],a[212:215],a[4:7],v[20:23]
s_waitcnt lgkmcnt(6)
buffer_load_dwordx4 v5,s[44:47],s69 offen offset:1024 lds
v_or_b32 v46,v56,v58
v_lshlrev_b32_sdwa v24,1,v24 dst_sel:DWORD dst_unused:UNUSED_PAD src0_sel:DWORD src1_sel:WORD_1
v_mfma_f32_16x16x32_bf16 v[52:55],a[200:203],a[20:23],v[52:55]
s_add_u32 s69,s69,s53
ds_read_u16_d16_hi v31,v31
v_cvt_pk_bf16_f32 v47,v57,v59
v_lshlrev_b32_sdwa v26,1,v26 dst_sel:DWORD dst_unused:UNUSED_PAD src0_sel:DWORD src1_sel:WORD_1
v_mfma_f32_16x16x32_bf16 v[52:55],a[196:199],a[36:39],v[52:55]
buffer_load_dwordx4 v5,s[44:47],s69 offen offset:2048 lds
v_lshlrev_b32_sdwa v25,1,v25 dst_sel:DWORD dst_unused:UNUSED_PAD src0_sel:DWORD src1_sel:WORD_1
v_mfma_f32_16x16x32_bf16 v[56:59],a[192:195],a[36:39],v[16:19]
ds_read_u16 v24,v24
v_lshlrev_b32_sdwa v27,1,v27 dst_sel:DWORD dst_unused:UNUSED_PAD src0_sel:DWORD src1_sel:WORD_1
v_mfma_f32_16x16x32_bf16 v[56:59],a[216:219],a[20:23],v[56:59]
ds_read_u16_d16_hi v26,v26
v_lshlrev_b32_sdwa v60,1,v60 dst_sel:DWORD dst_unused:UNUSED_PAD src0_sel:DWORD src1_sel:WORD_1
v_mfma_f32_16x16x32_bf16 v[56:59],a[220:223],a[4:7],v[56:59]
ds_read_u16 v25,v25
v_lshlrev_b32_sdwa v62,1,v62 dst_sel:DWORD dst_unused:UNUSED_PAD src0_sel:DWORD src1_sel:WORD_1
v_mfma_f32_16x16x32_bf16 v[232:235],a[240:243],v[48:51],v[232:235]
ds_read_u16_d16_hi v27,v27
v_lshlrev_b32_sdwa v61,1,v61 dst_sel:DWORD dst_unused:UNUSED_PAD src0_sel:DWORD src1_sel:WORD_1
v_mfma_f32_16x16x32_bf16 v[236:239],a[240:243],v[44:47],v[236:239]
ds_read_u16 v60,v60
v_lshlrev_b32_sdwa v63,1,v63 dst_sel:DWORD dst_unused:UNUSED_PAD src0_sel:DWORD src1_sel:WORD_1
v_mfma_f32_16x16x32_bf16 v[164:167],a[224:227],v[44:47],v[164:167]
TP7015_%=:
s_cbranch_execz TR0_%=
ds_read_u16_d16_hi v62,v62
v_lshlrev_b32_sdwa v32,1,v32 dst_sel:DWORD dst_unused:UNUSED_PAD src0_sel:DWORD src1_sel:WORD_1
v_mfma_f32_16x16x32_bf16 v[160:163],a[224:227],v[48:51],v[160:163]
ds_read_u16 v61,v61
v_lshlrev_b32_sdwa v34,1,v34 dst_sel:DWORD dst_unused:UNUSED_PAD src0_sel:DWORD src1_sel:WORD_1
v_mfma_f32_16x16x32_bf16 v[240:243],a[248:251],v[48:51],v[240:243]
ds_read_u16_d16_hi v63,v63
v_lshlrev_b32_sdwa v33,1,v33 dst_sel:DWORD dst_unused:UNUSED_PAD src0_sel:DWORD src1_sel:WORD_1
v_mfma_f32_16x16x32_bf16 v[244:247],a[248:251],v[44:47],v[244:247]
ds_read_u16 v32,v32
v_lshlrev_b32_sdwa v35,1,v35 dst_sel:DWORD dst_unused:UNUSED_PAD src0_sel:DWORD src1_sel:WORD_1
v_mfma_f32_16x16x32_bf16 v[180:183],a[228:231],v[44:47],v[180:183]
s_nop 0
ds_read_u16_d16_hi v34,v34
v_mfma_f32_16x16x32_bf16 v[176:179],a[228:231],v[48:51],v[176:179]
s_waitcnt lgkmcnt(8)
ds_read_u16 v33,v33
v_mfma_f32_16x16x32_bf16 v[248:251],a[232:235],v[48:51],v[248:251]
ds_read_u16_d16_hi v35,v35
v_mfma_f32_16x16x32_bf16 v[252:255],a[232:235],v[44:47],v[252:255]
v_mfma_f32_16x16x32_bf16 v[196:199],a[236:239],v[44:47],v[196:199]
v_mfma_f32_16x16x32_bf16 v[192:195],a[236:239],v[48:51],v[192:195]
v_mfma_f32_16x16x32_bf16 v[44:47],a[220:223],a[8:11],v[16:19]
v_mul_f32_dpp v232,v14,v232 row_newbcast:0 row_mask:0xf bank_mask:0xf
s_add_u32 s10,s61,1572864
v_mul_f32_dpp v233,v14,v233 row_newbcast:1 row_mask:0xf bank_mask:0xf
v_mul_f32_dpp v234,v14,v234 row_newbcast:2 row_mask:0xf bank_mask:0xf
v_lshlrev_b32_sdwa v36,1,v36 dst_sel:DWORD dst_unused:UNUSED_PAD src0_sel:DWORD src1_sel:WORD_1
v_mfma_f32_16x16x32_bf16 v[44:47],a[216:219],a[24:27],v[44:47]
s_nop 0
ds_read_u16 v36,v36
v_lshl_add_u64 v[28:29],v[28:29],0,v[30:31]
v_mul_f32_dpp v235,v14,v235 row_newbcast:3 row_mask:0xf bank_mask:0xf
v_mul_f32_dpp v160,v14,v160 row_newbcast:4 row_mask:0xf bank_mask:0xf
v_mul_f32_dpp v161,v14,v161 row_newbcast:5 row_mask:0xf bank_mask:0xf
v_mfma_f32_16x16x32_bf16 v[44:47],a[192:195],a[40:43],v[44:47]
s_waitcnt lgkmcnt(4)
v_lshl_add_u64 v[30:31],v[24:25],0,v[26:27]
v_mul_f32_dpp v162,v14,v162 row_newbcast:6 row_mask:0xf bank_mask:0xf
v_mul_f32_dpp v163,v14,v163 row_newbcast:7 row_mask:0xf bank_mask:0xf
v_lshlrev_b32_sdwa v38,1,v38 dst_sel:DWORD dst_unused:UNUSED_PAD src0_sel:DWORD src1_sel:WORD_1
v_cvt_pk_bf16_f32 v232,v232,v233
v_mfma_f32_16x16x32_bf16 v[48:51],a[196:199],a[40:43],v[20:23]
ds_read_u16_d16_hi v38,v38
v_cvt_pk_bf16_f32 v233,v234,v235
v_cvt_pk_bf16_f32 v234,v160,v161
v_lshlrev_b32_sdwa v37,1,v37 dst_sel:DWORD dst_unused:UNUSED_PAD src0_sel:DWORD src1_sel:WORD_1
v_cvt_pk_bf16_f32 v235,v162,v163
v_mfma_f32_16x16x32_bf16 v[48:51],a[200:203],a[24:27],v[48:51]
buffer_store_dwordx4 v[232:235],v7,s[40:43],s10 offen offset:0 sc1
v_lshl_add_u64 v[24:25],v[60:61],0,v[62:63]
v_mul_f32_dpp v240,v14,v240 row_newbcast:8 row_mask:0xf bank_mask:0xf
v_mul_f32_dpp v241,v14,v241 row_newbcast:9 row_mask:0xf bank_mask:0xf
v_mul_f32_dpp v242,v14,v242 row_newbcast:10 row_mask:0xf bank_mask:0xf
v_mfma_f32_16x16x32_bf16 v[48:51],a[212:215],a[8:11],v[48:51]
ds_read_u16 v37,v37
v_mul_f32_dpp v243,v14,v243 row_newbcast:11 row_mask:0xf bank_mask:0xf
v_mul_f32_dpp v176,v14,v176 row_newbcast:12 row_mask:0xf bank_mask:0xf
v_lshlrev_b32_sdwa v39,1,v39 dst_sel:DWORD dst_unused:UNUSED_PAD src0_sel:DWORD src1_sel:WORD_1
v_mul_f32_dpp v177,v14,v177 row_newbcast:13 row_mask:0xf bank_mask:0xf
v_mfma_f32_16x16x32_bf16 v[60:63],a[212:215],a[12:15],v[20:23]
s_nop 0
ds_read_b128 v[20:23],v4 offset:640
v_mul_f32_dpp v178,v14,v178 row_newbcast:14 row_mask:0xf bank_mask:0xf
v_mul_f32_dpp v179,v14,v179 row_newbcast:15 row_mask:0xf bank_mask:0xf
v_lshlrev_b32_sdwa v40,1,v40 dst_sel:DWORD dst_unused:UNUSED_PAD src0_sel:DWORD src1_sel:WORD_1
v_cvt_pk_bf16_f32 v240,v240,v241
v_mfma_f32_16x16x32_bf16 v[60:63],a[200:203],a[28:31],v[60:63]
s_waitcnt lgkmcnt(4)
ds_read_u16_d16_hi v39,v39
ds_read_b64_tr_b16 a[212:213],v1 offset:4096
v_lshl_add_u64 v[26:27],v[32:33],0,v[34:35]
v_cvt_pk_bf16_f32 v241,v242,v243
v_cvt_pk_bf16_f32 v242,v176,v177
v_cvt_pk_bf16_f32 v243,v178,v179
v_lshlrev_b32_sdwa v42,1,v42 dst_sel:DWORD dst_unused:UNUSED_PAD src0_sel:DWORD src1_sel:WORD_1
v_mfma_f32_16x16x32_bf16 v[60:63],a[196:199],a[44:47],v[60:63]
ds_read_b64_tr_b16 a[214:215],v1 offset:5120
ds_read_u16 v40,v40
v_mul_f32_dpp v248,v15,v248 row_newbcast:0 row_mask:0xf bank_mask:0xf
v_mul_f32_dpp v249,v15,v249 row_newbcast:1 row_mask:0xf bank_mask:0xf
v_mul_f32_dpp v250,v15,v250 row_newbcast:2 row_mask:0xf bank_mask:0xf
v_mfma_f32_16x16x32_bf16 v[32:35],a[192:195],a[44:47],v[16:19]
ds_read_b128 v[16:19],v4 offset:672
ds_read_u16_d16_hi v42,v42
v_mul_f32_dpp v251,v15,v251 row_newbcast:3 row_mask:0xf bank_mask:0xf
v_mul_f32_dpp v192,v15,v192 row_newbcast:4 row_mask:0xf bank_mask:0xf
v_lshlrev_b32_sdwa v41,1,v41 dst_sel:DWORD dst_unused:UNUSED_PAD src0_sel:DWORD src1_sel:WORD_1
v_mul_f32_dpp v193,v15,v193 row_newbcast:5 row_mask:0xf bank_mask:0xf
v_mfma_f32_16x16x32_bf16 v[32:35],a[216:219],a[28:31],v[32:35]
ds_read_u16 v41,v41
ds_read_b64_tr_b16 a[200:201],v2 offset:4096
v_mul_f32_dpp v194,v15,v194 row_newbcast:6 row_mask:0xf bank_mask:0xf
v_mul_f32_dpp v195,v15,v195 row_newbcast:7 row_mask:0xf bank_mask:0xf
v_lshlrev_b32_sdwa v43,1,v43 dst_sel:DWORD dst_unused:UNUSED_PAD src0_sel:DWORD src1_sel:WORD_1
v_cvt_pk_bf16_f32 v248,v248,v249
v_mfma_f32_16x16x32_bf16 v[32:35],a[220:223],a[12:15],v[32:35]
ds_read_u16_d16_hi v43,v43
ds_read_b64_tr_b16 a[202:203],v2 offset:5120
v_cvt_pk_bf16_f32 v249,v250,v251
v_cvt_pk_bf16_f32 v250,v192,v193
v_cvt_pk_bf16_f32 v251,v194,v195
v_mfma_f32_16x16x32_bf16 v[208:211],a[240:243],v[28:31],v[208:211]
ds_read_b64_tr_b16 a[196:197],v2 offset:2048
v_mul_f32_dpp v236,v14,v236 row_newbcast:0 row_mask:0xf bank_mask:0xf
v_mul_f32_dpp v237,v14,v237 row_newbcast:1 row_mask:0xf bank_mask:0xf
v_lshlrev_b32_sdwa v56,1,v56 dst_sel:DWORD dst_unused:UNUSED_PAD src0_sel:DWORD src1_sel:WORD_1
v_mul_f32_dpp v238,v14,v238 row_newbcast:2 row_mask:0xf bank_mask:0xf
v_mfma_f32_16x16x32_bf16 v[212:215],a[240:243],v[24:27],v[212:215]
ds_read_b64_tr_b16 a[198:199],v2 offset:3072
v_mul_f32_dpp v239,v14,v239 row_newbcast:3 row_mask:0xf bank_mask:0xf
v_mul_f32_dpp v164,v14,v164 row_newbcast:4 row_mask:0xf bank_mask:0xf
v_lshlrev_b32_sdwa v58,1,v58 dst_sel:DWORD dst_unused:UNUSED_PAD src0_sel:DWORD src1_sel:WORD_1
v_mul_f32_dpp v165,v14,v165 row_newbcast:5 row_mask:0xf bank_mask:0xf
v_mfma_f32_16x16x32_bf16 v[172:175],a[224:227],v[24:27],v[172:175]
ds_read_u16 v56,v56
v_mul_f32_dpp v166,v14,v166 row_newbcast:6 row_mask:0xf bank_mask:0xf
v_mul_f32_dpp v167,v14,v167 row_newbcast:7 row_mask:0xf bank_mask:0xf
v_cvt_pk_bf16_f32 v236,v236,v237
v_lshlrev_b32_sdwa v57,1,v57 dst_sel:DWORD dst_unused:UNUSED_PAD src0_sel:DWORD src1_sel:WORD_1
v_mfma_f32_16x16x32_bf16 v[168:171],a[224:227],v[28:31],v[168:171]
buffer_store_dwordx4 v[240:243],v7,s[40:43],s10 offen offset:64 sc1
ds_read_u16_d16_hi v58,v58
v_cvt_pk_bf16_f32 v237,v238,v239
v_cvt_pk_bf16_f32 v238,v164,v165
v_cvt_pk_bf16_f32 v239,v166,v167
v_mfma_f32_16x16x32_bf16 v[216:219],a[248:251],v[28:31],v[216:219]
ds_read_u16 v57,v57
v_mul_f32_dpp v244,v14,v244 row_newbcast:8 row_mask:0xf bank_mask:0xf
v_mul_f32_dpp v245,v14,v245 row_newbcast:9 row_mask:0xf bank_mask:0xf
v_lshlrev_b32_sdwa v59,1,v59 dst_sel:DWORD dst_unused:UNUSED_PAD src0_sel:DWORD src1_sel:WORD_1
v_mul_f32_dpp v246,v14,v246 row_newbcast:10 row_mask:0xf bank_mask:0xf
v_mfma_f32_16x16x32_bf16 v[220:223],a[248:251],v[24:27],v[220:223]
ds_read_u16_d16_hi v59,v59
v_mul_f32_dpp v247,v14,v247 row_newbcast:11 row_mask:0xf bank_mask:0xf
v_mul_f32_dpp v180,v14,v180 row_newbcast:12 row_mask:0xf bank_mask:0xf
v_lshlrev_b32_sdwa v52,1,v52 dst_sel:DWORD dst_unused:UNUSED_PAD src0_sel:DWORD src1_sel:WORD_1
v_mul_f32_dpp v181,v14,v181 row_newbcast:13 row_mask:0xf bank_mask:0xf
v_mfma_f32_16x16x32_bf16 v[188:191],a[228:231],v[24:27],v[188:191]
ds_read_b64_tr_b16 a[192:193],v2 offset:0
v_mul_f32_dpp v182,v14,v182 row_newbcast:14 row_mask:0xf bank_mask:0xf
v_mul_f32_dpp v183,v14,v183 row_newbcast:15 row_mask:0xf bank_mask:0xf
v_cvt_pk_bf16_f32 v244,v244,v245
v_mfma_f32_16x16x32_bf16 v[184:187],a[228:231],v[28:31],v[184:187]
ds_read_b64_tr_b16 a[194:195],v2 offset:1024
v_cvt_pk_bf16_f32 v245,v246,v247
v_cvt_pk_bf16_f32 v246,v180,v181
v_lshlrev_b32_sdwa v54,1,v54 dst_sel:DWORD dst_unused:UNUSED_PAD src0_sel:DWORD src1_sel:WORD_1
v_cvt_pk_bf16_f32 v247,v182,v183
v_mfma_f32_16x16x32_bf16 v[224:227],a[232:235],v[28:31],v[224:227]
ds_read_u16 v52,v52
v_mul_f32_dpp v252,v15,v252 row_newbcast:0 row_mask:0xf bank_mask:0xf
v_mul_f32_dpp v253,v15,v253 row_newbcast:1 row_mask:0xf bank_mask:0xf
v_lshlrev_b32_sdwa v53,1,v53 dst_sel:DWORD dst_unused:UNUSED_PAD src0_sel:DWORD src1_sel:WORD_1
v_mul_f32_dpp v254,v15,v254 row_newbcast:2 row_mask:0xf bank_mask:0xf
v_mfma_f32_16x16x32_bf16 v[228:231],a[232:235],v[24:27],v[228:231]
ds_read_u16_d16_hi v54,v54
v_mul_f32_dpp v255,v15,v255 row_newbcast:3 row_mask:0xf bank_mask:0xf
v_mul_f32_dpp v196,v15,v196 row_newbcast:4 row_mask:0xf bank_mask:0xf
v_mul_f32_dpp v197,v15,v197 row_newbcast:5 row_mask:0xf bank_mask:0xf
v_lshlrev_b32_sdwa v55,1,v55 dst_sel:DWORD dst_unused:UNUSED_PAD src0_sel:DWORD src1_sel:WORD_1
v_mfma_f32_16x16x32_bf16 v[204:207],a[236:239],v[24:27],v[204:207]
s_nop 0
ds_read_u16 v53,v53
v_mul_f32_dpp v198,v15,v198 row_newbcast:6 row_mask:0xf bank_mask:0xf
v_mul_f32_dpp v199,v15,v199 row_newbcast:7 row_mask:0xf bank_mask:0xf
v_cvt_pk_bf16_f32 v252,v252,v253
v_mfma_f32_16x16x32_bf16 v[200:203],a[236:239],v[28:31],v[200:203]
s_waitcnt lgkmcnt(8)
ds_read_u16_d16_hi v55,v55
v_cvt_pk_bf16_f32 v253,v254,v255
v_cvt_pk_bf16_f32 v254,v196,v197
v_cvt_pk_bf16_f32 v255,v198,v199
v_mfma_f32_16x16x32_bf16 v[24:27],a[204:207],a[0:3],v[20:23]
v_mul_f32_dpp v208,v14,v208 row_newbcast:0 row_mask:0xf bank_mask:0xf
s_add_u32 s11,s61,1671168
v_accvgpr_write_b32 a64,0
v_accvgpr_write_b32 a65,0
v_lshlrev_b32_sdwa v44,1,v44 dst_sel:DWORD dst_unused:UNUSED_PAD src0_sel:DWORD src1_sel:WORD_1
v_mfma_f32_16x16x32_bf16 v[24:27],a[208:211],a[16:19],v[24:27]
ds_read_u16 v44,v44
v_lshl_add_u64 v[36:37],v[36:37],0,v[38:39]
v_accvgpr_write_b32 a66,0
v_accvgpr_write_b32 a67,0
v_mul_f32_dpp v209,v14,v209 row_newbcast:1 row_mask:0xf bank_mask:0xf
v_mfma_f32_16x16x32_bf16 v[24:27],a[212:215],a[32:35],v[24:27]
v_lshl_add_u64 v[38:39],v[40:41],0,v[42:43]
v_mul_f32_dpp v210,v14,v210 row_newbcast:2 row_mask:0xf bank_mask:0xf
v_mul_f32_dpp v211,v14,v211 row_newbcast:3 row_mask:0xf bank_mask:0xf
v_lshlrev_b32_sdwa v46,1,v46 dst_sel:DWORD dst_unused:UNUSED_PAD src0_sel:DWORD src1_sel:WORD_1
v_mul_f32_dpp v168,v14,v168 row_newbcast:4 row_mask:0xf bank_mask:0xf
v_mfma_f32_16x16x32_bf16 v[28:31],a[200:203],a[32:35],v[16:19]
s_nop 0
buffer_store_dwordx4 v[248:251],v7,s[40:43],s10 offen offset:128 sc1
v_mul_f32_dpp v169,v14,v169 row_newbcast:5 row_mask:0xf bank_mask:0xf
v_mul_f32_dpp v170,v14,v170 row_newbcast:6 row_mask:0xf bank_mask:0xf
v_lshlrev_b32_sdwa v45,1,v45 dst_sel:DWORD dst_unused:UNUSED_PAD src0_sel:DWORD src1_sel:WORD_1
v_mul_f32_dpp v171,v14,v171 row_newbcast:7 row_mask:0xf bank_mask:0xf
v_mfma_f32_16x16x32_bf16 v[28:31],a[196:199],a[16:19],v[28:31]
s_waitcnt lgkmcnt(5)
ds_read_u16_d16_hi v46,v46
v_lshl_add_u64 v[40:41],v[56:57],0,v[58:59]
v_cvt_pk_bf16_f32 v208,v208,v209
v_cvt_pk_bf16_f32 v209,v210,v211
v_cvt_pk_bf16_f32 v210,v168,v169
v_mfma_f32_16x16x32_bf16 v[28:31],a[192:195],a[0:3],v[28:31]
ds_read_u16 v45,v45
v_cvt_pk_bf16_f32 v211,v170,v171
v_mul_f32_dpp v216,v14,v216 row_newbcast:8 row_mask:0xf bank_mask:0xf
v_lshlrev_b32_sdwa v47,1,v47 dst_sel:DWORD dst_unused:UNUSED_PAD src0_sel:DWORD src1_sel:WORD_1
v_accvgpr_write_b32 a72,v208
v_mfma_f32_16x16x32_bf16 v[56:59],a[192:195],a[4:7],v[16:19]
s_nop 0
ds_read_u16_d16_hi v47,v47
v_mul_f32_dpp v217,v14,v217 row_newbcast:9 row_mask:0xf bank_mask:0xf
v_mul_f32_dpp v218,v14,v218 row_newbcast:10 row_mask:0xf bank_mask:0xf
v_lshlrev_b32_sdwa v48,1,v48 dst_sel:DWORD dst_unused:UNUSED_PAD src0_sel:DWORD src1_sel:WORD_1
v_accvgpr_write_b32 a73,v209
v_mfma_f32_16x16x32_bf16 v[56:59],a[196:199],a[20:23],v[56:59]
s_waitcnt lgkmcnt(4)
ds_read_u16 v48,v48
v_lshl_add_u64 v[42:43],v[52:53],0,v[54:55]
v_mul_f32_dpp v219,v14,v219 row_newbcast:11 row_mask:0xf bank_mask:0xf
v_mul_f32_dpp v184,v14,v184 row_newbcast:12 row_mask:0xf bank_mask:0xf
v_accvgpr_write_b32 a74,v210
v_lshlrev_b32_sdwa v50,1,v50 dst_sel:DWORD dst_unused:UNUSED_PAD src0_sel:DWORD src1_sel:WORD_1
v_mfma_f32_16x16x32_bf16 v[56:59],a[200:203],a[36:39],v[56:59]
s_waitcnt vmcnt(9)
s_barrier
ds_read_u16_d16_hi v50,v50
v_mul_f32_dpp v185,v14,v185 row_newbcast:13 row_mask:0xf bank_mask:0xf
v_mul_f32_dpp v186,v14,v186 row_newbcast:14 row_mask:0xf bank_mask:0xf
v_accvgpr_write_b32 a75,v211
v_mfma_f32_16x16x32_bf16 v[52:55],a[212:215],a[36:39],v[20:23]
ds_read_b64_tr_b16 a[244:245],v3 offset:6160
v_mul_f32_dpp v187,v14,v187 row_newbcast:15 row_mask:0xf bank_mask:0xf
v_cvt_pk_bf16_f32 v216,v216,v217
v_lshlrev_b32_sdwa v49,1,v49 dst_sel:DWORD dst_unused:UNUSED_PAD src0_sel:DWORD src1_sel:WORD_1
v_cvt_pk_bf16_f32 v217,v218,v219
v_mfma_f32_16x16x32_bf16 v[52:55],a[208:211],a[20:23],v[52:55]
s_add_u32 m0,s58,0
ds_read_u16 v49,v49
v_cvt_pk_bf16_f32 v218,v184,v185
v_cvt_pk_bf16_f32 v219,v186,v187
v_lshlrev_b32_sdwa v51,1,v51 dst_sel:DWORD dst_unused:UNUSED_PAD src0_sel:DWORD src1_sel:WORD_1
v_mul_f32_dpp v224,v15,v224 row_newbcast:0 row_mask:0xf bank_mask:0xf
v_mfma_f32_16x16x32_bf16 v[52:55],a[204:207],a[4:7],v[52:55]
s_add_u32 s69,s69,s59
ds_read_u16_d16_hi v51,v51
v_accvgpr_write_b32 a76,v216
v_mul_f32_dpp v225,v15,v225 row_newbcast:1 row_mask:0xf bank_mask:0xf
v_mul_f32_dpp v226,v15,v226 row_newbcast:2 row_mask:0xf bank_mask:0xf
v_mfma_f32_16x16x32_bf16 v[136:139],a[240:243],v[36:39],v[136:139]
s_nop 0
buffer_load_dwordx4 v5,s[44:47],s69 offen offset:0 lds
v_accvgpr_write_b32 a77,v217
v_mul_f32_dpp v227,v15,v227 row_newbcast:3 row_mask:0xf bank_mask:0xf
v_lshlrev_b32_sdwa v32,1,v32 dst_sel:DWORD dst_unused:UNUSED_PAD src0_sel:DWORD src1_sel:WORD_1
v_mul_f32_dpp v200,v15,v200 row_newbcast:4 row_mask:0xf bank_mask:0xf
v_mfma_f32_16x16x32_bf16 v[140:143],a[240:243],v[40:43],v[140:143]
s_add_u32 s69,s69,s53
ds_read_u16 v32,v32
v_accvgpr_write_b32 a78,v218
v_mul_f32_dpp v201,v15,v201 row_newbcast:5 row_mask:0xf bank_mask:0xf
v_lshlrev_b32_sdwa v34,1,v34 dst_sel:DWORD dst_unused:UNUSED_PAD src0_sel:DWORD src1_sel:WORD_1
v_mul_f32_dpp v202,v15,v202 row_newbcast:6 row_mask:0xf bank_mask:0xf
v_mfma_f32_16x16x32_bf16 v[68:71],a[224:227],v[40:43],v[68:71]
buffer_load_dwordx4 v5,s[44:47],s69 offen offset:1024 lds
ds_read_u16_d16_hi v34,v34
v_accvgpr_write_b32 a79,v219
v_mul_f32_dpp v203,v15,v203 row_newbcast:7 row_mask:0xf bank_mask:0xf
v_cvt_pk_bf16_f32 v224,v224,v225
v_lshlrev_b32_sdwa v33,1,v33 dst_sel:DWORD dst_unused:UNUSED_PAD src0_sel:DWORD src1_sel:WORD_1
v_mfma_f32_16x16x32_bf16 v[64:67],a[224:227],v[36:39],v[64:67]
s_nop 0
buffer_store_dwordx4 v[236:239],v7,s[40:43],s10 offen offset:192 sc1
ds_read_u16 v33,v33
v_cvt_pk_bf16_f32 v225,v226,v227
v_cvt_pk_bf16_f32 v226,v200,v201
v_cvt_pk_bf16_f32 v227,v202,v203
v_mfma_f32_16x16x32_bf16 v[144:147],a[248:251],v[36:39],v[144:147]
s_add_u32 s69,s69,s53
ds_read_b64_tr_b16 a[246:247],v3 offset:7696
v_mul_f32_dpp v212,v14,v212 row_newbcast:0 row_mask:0xf bank_mask:0xf
v_accvgpr_write_b32 a80,v224
v_lshlrev_b32_sdwa v35,1,v35 dst_sel:DWORD dst_unused:UNUSED_PAD src0_sel:DWORD src1_sel:WORD_1
v_mul_f32_dpp v213,v14,v213 row_newbcast:1 row_mask:0xf bank_mask:0xf
v_mfma_f32_16x16x32_bf16 v[148:151],a[248:251],v[40:43],v[148:151]
buffer_load_dwordx4 v5,s[44:47],s69 offen offset:2048 lds
v_mul_f32_dpp v214,v14,v214 row_newbcast:2 row_mask:0xf bank_mask:0xf
v_accvgpr_write_b32 a81,v225
v_lshlrev_b32_sdwa v60,1,v60 dst_sel:DWORD dst_unused:UNUSED_PAD src0_sel:DWORD src1_sel:WORD_1
v_mul_f32_dpp v215,v14,v215 row_newbcast:3 row_mask:0xf bank_mask:0xf
v_mfma_f32_16x16x32_bf16 v[84:87],a[228:231],v[40:43],v[84:87]
ds_read_u16_d16_hi v35,v35
v_mul_f32_dpp v172,v14,v172 row_newbcast:4 row_mask:0xf bank_mask:0xf
v_accvgpr_write_b32 a82,v226
v_mul_f32_dpp v173,v14,v173 row_newbcast:5 row_mask:0xf bank_mask:0xf
v_mfma_f32_16x16x32_bf16 v[80:83],a[228:231],v[36:39],v[80:83]
ds_read_u16 v60,v60
v_mul_f32_dpp v174,v14,v174 row_newbcast:6 row_mask:0xf bank_mask:0xf
v_accvgpr_write_b32 a83,v227
v_lshlrev_b32_sdwa v62,1,v62 dst_sel:DWORD dst_unused:UNUSED_PAD src0_sel:DWORD src1_sel:WORD_1
v_mul_f32_dpp v175,v14,v175 row_newbcast:7 row_mask:0xf bank_mask:0xf
v_mfma_f32_16x16x32_bf16 v[152:155],a[232:235],v[36:39],v[152:155]
ds_read_u16_d16_hi v62,v62
v_cvt_pk_bf16_f32 v212,v212,v213
v_cvt_pk_bf16_f32 v213,v214,v215
v_lshlrev_b32_sdwa v61,1,v61 dst_sel:DWORD dst_unused:UNUSED_PAD src0_sel:DWORD src1_sel:WORD_1
v_cvt_pk_bf16_f32 v214,v172,v173
v_mfma_f32_16x16x32_bf16 v[156:159],a[232:235],v[40:43],v[156:159]
ds_read_u16 v61,v61
v_cvt_pk_bf16_f32 v215,v174,v175
v_mfma_f32_32x32x16_bf16 v[160:175],a[64:67],a[64:67],0
v_mul_f32_dpp v220,v14,v220 row_newbcast:8 row_mask:0xf bank_mask:0xf
v_lshlrev_b32_sdwa v63,1,v63 dst_sel:DWORD dst_unused:UNUSED_PAD src0_sel:DWORD src1_sel:WORD_1
v_accvgpr_write_b32 a84,v212
v_mul_f32_dpp v221,v14,v221 row_newbcast:9 row_mask:0xf bank_mask:0xf
v_mul_f32_dpp v222,v14,v222 row_newbcast:10 row_mask:0xf bank_mask:0xf
v_mfma_f32_16x16x32_bf16 v[100:103],a[236:239],v[40:43],v[100:103]
ds_read_u16_d16_hi v63,v63
v_accvgpr_write_b32 a85,v213
v_mfma_f32_16x16x32_bf16 v[96:99],a[236:239],v[36:39],v[96:99]
ds_read_b64_tr_b16 a[252:253],v3 offset:6168
v_mul_f32_dpp v223,v14,v223 row_newbcast:11 row_mask:0xf bank_mask:0xf
v_mul_f32_dpp v188,v14,v188 row_newbcast:12 row_mask:0xf bank_mask:0xf
ds_read_b64_tr_b16 a[254:255],v3 offset:7704
v_add_u32_sdwa v36,s60,v10 dst_sel:DWORD dst_unused:UNUSED_PAD src0_sel:DWORD src1_sel:WORD_1
v_subrev_u32 v8,88400,v36
v_min_u32_e64 v36,v36,v8
v_add_u32 v37,7072,v36
v_subrev_u32 v8,88400,v37
v_min_u32_e64 v37,v37,v8
s_waitcnt lgkmcnt(15)
v_lshl_add_u64 v[44:45],v[44:45],0,v[46:47]
s_waitcnt lgkmcnt(11)
v_lshl_add_u64 v[46:47],v[48:49],0,v[50:51]
s_waitcnt lgkmcnt(6)
v_lshl_add_u64 v[48:49],v[32:33],0,v[34:35]
s_waitcnt lgkmcnt(2)
v_lshl_add_u64 v[50:51],v[60:61],0,v[62:63]
v_mfma_f32_16x16x32_bf16 v[112:115],a[240:243],v[44:47],v[112:115]
v_xad_u32 v40,v11,0,v36
v_xad_u32 v41,v11,8,v37
v_xad_u32 v42,v11,16,v36
v_xad_u32 v43,v11,24,v37
v_mfma_f32_16x16x32_bf16 v[116:119],a[240:243],v[48:51],v[116:119]
ds_read_b64 a[48:49],v40 offset:0
v_accvgpr_write_b32 a86,v214
v_mul_f32_dpp v189,v14,v189 row_newbcast:13 row_mask:0xf bank_mask:0xf
v_mul_f32_dpp v190,v14,v190 row_newbcast:14 row_mask:0xf bank_mask:0xf
v_accvgpr_write_b32 a87,v215
v_mfma_f32_16x16x32_bf16 v[76:79],a[224:227],v[48:51],v[76:79]
buffer_store_dwordx4 v[244:247],v7,s[40:43],s10 offen offset:256 sc1
ds_read_b64 a[50:51],v40 offset:192
v_mul_f32_dpp v191,v14,v191 row_newbcast:15 row_mask:0xf bank_mask:0xf
v_cvt_pk_bf16_f32 v220,v220,v221
v_cvt_pk_bf16_f32 v221,v222,v223
v_mfma_f32_16x16x32_bf16 v[72:75],a[224:227],v[44:47],v[72:75]
ds_read_b64 a[52:53],v41 offset:0
v_cvt_pk_bf16_f32 v222,v188,v189
v_cvt_pk_bf16_f32 v223,v190,v191
v_mfma_f32_32x32x16_bf16 v[176:191],a[64:67],a[64:67],0
v_mfma_f32_16x16x32_bf16 v[120:123],a[248:251],v[44:47],v[120:123]
ds_read_b64 a[54:55],v41 offset:192
v_mul_f32_dpp v228,v15,v228 row_newbcast:0 row_mask:0xf bank_mask:0xf
v_accvgpr_write_b32 a88,v220
v_mul_f32_dpp v229,v15,v229 row_newbcast:1 row_mask:0xf bank_mask:0xf
v_mfma_f32_16x16x32_bf16 v[124:127],a[248:251],v[48:51],v[124:127]
ds_read_b64 a[56:57],v40 offset:8
v_mul_f32_dpp v230,v15,v230 row_newbcast:2 row_mask:0xf bank_mask:0xf
v_accvgpr_write_b32 a89,v221
v_mul_f32_dpp v231,v15,v231 row_newbcast:3 row_mask:0xf bank_mask:0xf
v_mfma_f32_16x16x32_bf16 v[92:95],a[228:231],v[48:51],v[92:95]
ds_read_b64 a[58:59],v40 offset:200
v_mul_f32_dpp v204,v15,v204 row_newbcast:4 row_mask:0xf bank_mask:0xf
v_accvgpr_write_b32 a90,v222
v_mul_f32_dpp v205,v15,v205 row_newbcast:5 row_mask:0xf bank_mask:0xf
v_mfma_f32_16x16x32_bf16 v[88:91],a[228:231],v[44:47],v[88:91]
ds_read_b64 a[60:61],v41 offset:8
v_mul_f32_dpp v206,v15,v206 row_newbcast:6 row_mask:0xf bank_mask:0xf
v_accvgpr_write_b32 a91,v223
v_mul_f32_dpp v207,v15,v207 row_newbcast:7 row_mask:0xf bank_mask:0xf
v_mfma_f32_16x16x32_bf16 v[128:131],a[232:235],v[44:47],v[128:131]
ds_read_b64 a[62:63],v41 offset:200
v_cvt_pk_bf16_f32 v228,v228,v229
v_cvt_pk_bf16_f32 v229,v230,v231
v_cvt_pk_bf16_f32 v230,v204,v205
v_mfma_f32_16x16x32_bf16 v[132:135],a[232:235],v[48:51],v[132:135]
v_cvt_pk_bf16_f32 v231,v206,v207
v_mfma_f32_32x32x16_bf16 v[192:207],a[64:67],a[64:67],0
v_accvgpr_write_b32 a92,v228
v_mfma_f32_16x16x32_bf16 v[108:111],a[236:239],v[48:51],v[108:111]
v_accvgpr_write_b32 a93,v229
v_accvgpr_write_b32 a94,v230
v_accvgpr_write_b32 a95,v231
v_mfma_f32_16x16x32_bf16 v[104:107],a[236:239],v[44:47],v[104:107]
v_mfma_f32_32x32x16_bf16 v[208:223],a[64:67],a[64:67],0
v_mov_b64 v[32:33],0
v_mov_b64 v[34:35],0
ds_read_b64 a[64:65],v42 offset:0
ds_read_b64 a[66:67],v42 offset:192
ds_read_b64 a[68:69],v43 offset:0
ds_read_b64 a[70:71],v43 offset:192
s_waitcnt lgkmcnt(8)
v_smfmac_f32_16x16x64_bf16 v[160:163],a[96:99],a[48:55],v10
s_waitcnt lgkmcnt(4)
ds_read_b64 a[48:49],v42 offset:8
ds_read_b64 a[50:51],v42 offset:200
ds_read_b64 a[52:53],v43 offset:8
ds_read_b64 a[54:55],v43 offset:200
v_smfmac_f32_16x16x64_bf16 v[164:167],a[100:103],a[56:63],v10
s_waitcnt lgkmcnt(4)
ds_read_b64 a[56:57],v40 offset:64
ds_read_b64 a[58:59],v40 offset:256
ds_read_b64 a[60:61],v41 offset:64
ds_read_b64 a[62:63],v41 offset:256
v_smfmac_f32_16x16x64_bf16 v[168:171],a[104:107],a[64:71],v10
s_waitcnt lgkmcnt(4)
buffer_store_dwordx4 v[252:255],v7,s[40:43],s10 offen offset:320 sc1
ds_read_b64 a[64:65],v40 offset:72
ds_read_b64 a[66:67],v40 offset:264
ds_read_b64 a[68:69],v41 offset:72
ds_read_b64 a[70:71],v41 offset:264
v_and_b32 v44,8,v11
v_pk_mul_f32 v[48:49],v[160:161],v[160:161]
v_pk_mul_f32 v[50:51],v[162:163],v[162:163]
v_smfmac_f32_16x16x64_bf16 v[172:175],a[108:111],a[48:55],v10
s_waitcnt lgkmcnt(4)
ds_read_b64 a[48:49],v42 offset:64
ds_read_b64 a[50:51],v42 offset:256
ds_read_b64 a[52:53],v43 offset:64
ds_read_b64 a[54:55],v43 offset:256
v_mul_u32_u24 v44,884,v44
s_add_u32 s64,s60,3536
v_pk_add_f32 v[60:61],v[160:161],v[164:165]
v_pk_fma_f32 v[48:49],v[164:165],v[164:165],v[48:49]
v_pk_add_f32 v[62:63],v[162:163],v[166:167]
v_pk_fma_f32 v[50:51],v[166:167],v[166:167],v[50:51]
v_smfmac_f32_16x16x64_bf16 v[176:179],a[128:131],a[56:63],v10
s_waitcnt lgkmcnt(4)
ds_read_b64 a[56:57],v42 offset:72
ds_read_b64 a[58:59],v42 offset:264
ds_read_b64 a[60:61],v43 offset:72
ds_read_b64 a[62:63],v43 offset:264
v_add_u32 v47,s64,v44
v_pk_add_f32 v[60:61],v[60:61],v[168:169]
v_pk_fma_f32 v[48:49],v[168:169],v[168:169],v[48:49]
v_pk_add_f32 v[62:63],v[62:63],v[170:171]
v_pk_fma_f32 v[50:51],v[170:171],v[170:171],v[50:51]
v_smfmac_f32_16x16x64_bf16 v[180:183],a[132:135],a[64:71],v10
s_waitcnt lgkmcnt(4)
ds_read_b64 a[64:65],v40 offset:128
ds_read_b64 a[66:67],v40 offset:320
ds_read_b64 a[68:69],v41 offset:128
ds_read_b64 a[70:71],v41 offset:320
v_subrev_u32 v8,88400,v47
v_pk_add_f32 v[60:61],v[60:61],v[172:173]
v_pk_fma_f32 v[48:49],v[172:173],v[172:173],v[48:49]
v_pk_add_f32 v[62:63],v[62:63],v[174:175]
v_pk_fma_f32 v[50:51],v[174:175],v[174:175],v[50:51]
v_smfmac_f32_16x16x64_bf16 v[184:187],a[136:139],a[48:55],v10
s_waitcnt lgkmcnt(4)
ds_read_b64 a[48:49],v40 offset:136
ds_read_b64 a[50:51],v40 offset:328
ds_read_b64 a[52:53],v41 offset:136
ds_read_b64 a[54:55],v41 offset:328
v_min_u32 v47,v47,v8
v_pk_add_f32 v[60:61],v[60:61],v[176:177]
v_pk_fma_f32 v[48:49],v[176:177],v[176:177],v[48:49]
v_pk_add_f32 v[62:63],v[62:63],v[178:179]
v_pk_fma_f32 v[50:51],v[178:179],v[178:179],v[50:51]
v_smfmac_f32_16x16x64_bf16 v[188:191],a[140:143],a[56:63],v10
s_waitcnt lgkmcnt(4)
ds_read_b64 a[56:57],v42 offset:128
ds_read_b64 a[58:59],v42 offset:320
ds_read_b64 a[60:61],v43 offset:128
ds_read_b64 a[62:63],v43 offset:320
v_xad_u32 v40,v11,32,v36
v_xad_u32 v41,v11,40,v37
v_pk_add_f32 v[60:61],v[60:61],v[180:181]
v_pk_fma_f32 v[48:49],v[180:181],v[180:181],v[48:49]
v_pk_add_f32 v[62:63],v[62:63],v[182:183]
v_pk_fma_f32 v[50:51],v[182:183],v[182:183],v[50:51]
v_smfmac_f32_16x16x64_bf16 v[192:195],a[160:163],a[64:71],v10
s_waitcnt lgkmcnt(4)
ds_read_b64 a[64:65],v42 offset:136
ds_read_b64 a[66:67],v42 offset:328
ds_read_b64 a[68:69],v43 offset:136
ds_read_b64 a[70:71],v43 offset:328
v_add_u32 v46,3536,v47
v_pk_add_f32 v[60:61],v[60:61],v[184:185]
v_pk_fma_f32 v[48:49],v[184:185],v[184:185],v[48:49]
v_pk_add_f32 v[62:63],v[62:63],v[186:187]
v_pk_fma_f32 v[50:51],v[186:187],v[186:187],v[50:51]
v_smfmac_f32_16x16x64_bf16 v[196:199],a[164:167],a[48:55],v10
s_waitcnt lgkmcnt(4)
buffer_store_dwordx4 a[72:75],v7,s[40:43],s11 offen offset:0 sc1
ds_read_b64 a[48:49],v40 offset:0
ds_read_b64 a[50:51],v40 offset:192
ds_read_b64 a[52:53],v41 offset:0
ds_read_b64 a[54:55],v41 offset:192
v_xad_u32 v42,v11,48,v36
v_xad_u32 v43,v11,56,v37
v_pk_add_f32 v[60:61],v[60:61],v[188:189]
v_pk_fma_f32 v[48:49],v[188:189],v[188:189],v[48:49]
v_pk_add_f32 v[62:63],v[62:63],v[190:191]
v_pk_fma_f32 v[50:51],v[190:191],v[190:191],v[50:51]
v_smfmac_f32_16x16x64_bf16 v[200:203],a[168:171],a[56:63],v10
s_waitcnt lgkmcnt(4)
ds_read_b64 a[56:57],v40 offset:8
ds_read_b64 a[58:59],v40 offset:200
ds_read_b64 a[60:61],v41 offset:8
ds_read_b64 a[62:63],v41 offset:200
v_subrev_u32 v8,88400,v46
v_pk_add_f32 v[60:61],v[60:61],v[192:193]
v_pk_fma_f32 v[48:49],v[192:193],v[192:193],v[48:49]
v_pk_add_f32 v[62:63],v[62:63],v[194:195]
v_pk_fma_f32 v[50:51],v[194:195],v[194:195],v[50:51]
v_smfmac_f32_16x16x64_bf16 v[204:207],a[172:175],a[64:71],v10
s_waitcnt lgkmcnt(4)
ds_read_b64 a[64:65],v40 offset:64
ds_read_b64 a[66:67],v40 offset:256
ds_read_b64 a[68:69],v41 offset:64
ds_read_b64 a[70:71],v41 offset:256
v_min_u32 v46,v46,v8
v_pk_add_f32 v[60:61],v[60:61],v[196:197]
v_pk_fma_f32 v[48:49],v[196:197],v[196:197],v[48:49]
v_pk_add_f32 v[62:63],v[62:63],v[198:199]
v_pk_fma_f32 v[50:51],v[198:199],v[198:199],v[50:51]
v_smfmac_f32_16x16x64_bf16 v[208:211],a[112:115],a[48:55],v10
TE10_%=:
s_cbranch_execz TP9680_%=
s_waitcnt lgkmcnt(4)
s_nop 0
ds_read_b64 a[48:49],v40 offset:72
ds_read_b64 a[50:51],v40 offset:264
ds_read_b64 a[52:53],v41 offset:72
ds_read_b64 a[54:55],v41 offset:264
v_xor_b32 v9,0x80008,v12
v_pk_add_f32 v[60:61],v[60:61],v[200:201]
v_pk_fma_f32 v[48:49],v[200:201],v[200:201],v[48:49]
v_pk_add_f32 v[62:63],v[62:63],v[202:203]
v_pk_fma_f32 v[50:51],v[202:203],v[202:203],v[50:51]
v_mfma_f32_32x32x16_bf16 v[224:239],v[32:35],v[32:35],0
v_smfmac_f32_16x16x64_bf16 v[212:215],a[116:119],a[56:63],v10
s_waitcnt lgkmcnt(4)
ds_read_b64 a[56:57],v40 offset:128
ds_read_b64 a[58:59],v40 offset:320
ds_read_b64 a[60:61],v41 offset:128
ds_read_b64 a[62:63],v41 offset:320
v_add_u32_sdwa v44,v12,v47 dst_sel:DWORD dst_unused:UNUSED_PAD src0_sel:WORD_0 src1_sel:DWORD
v_pk_add_f32 v[60:61],v[60:61],v[204:205]
v_pk_fma_f32 v[48:49],v[204:205],v[204:205],v[48:49]
v_pk_add_f32 v[62:63],v[62:63],v[206:207]
v_pk_fma_f32 v[50:51],v[206:207],v[206:207],v[50:51]
v_smfmac_f32_16x16x64_bf16 v[216:219],a[144:147],a[64:71],v10
s_waitcnt lgkmcnt(4)
ds_read_b64 a[64:65],v40 offset:136
ds_read_b64 a[66:67],v40 offset:328
ds_read_b64 a[68:69],v41 offset:136
ds_read_b64 a[70:71],v41 offset:328
v_add_u32_sdwa v45,v12,v47 dst_sel:DWORD dst_unused:UNUSED_PAD src0_sel:WORD_1 src1_sel:DWORD
v_pk_add_f32 v[60:61],v[60:61],v[208:209]
v_pk_fma_f32 v[48:49],v[208:209],v[208:209],v[48:49]
v_pk_add_f32 v[62:63],v[62:63],v[210:211]
v_pk_fma_f32 v[50:51],v[210:211],v[210:211],v[50:51]
v_smfmac_f32_16x16x64_bf16 v[220:223],a[148:151],a[48:55],v10
s_waitcnt lgkmcnt(4)
buffer_store_dwordx4 a[76:79],v7,s[40:43],s11 offen offset:64 sc1
ds_read_b64 a[48:49],v42 offset:0
ds_read_b64 a[50:51],v42 offset:192
ds_read_b64 a[52:53],v43 offset:0
ds_read_b64 a[54:55],v43 offset:192
v_add_u32_sdwa v47,v9,v46 dst_sel:DWORD dst_unused:UNUSED_PAD src0_sel:WORD_1 src1_sel:DWORD
v_pk_add_f32 v[60:61],v[60:61],v[212:213]
v_pk_fma_f32 v[48:49],v[212:213],v[212:213],v[48:49]
v_pk_add_f32 v[62:63],v[62:63],v[214:215]
v_pk_fma_f32 v[50:51],v[214:215],v[214:215],v[50:51]
v_smfmac_f32_16x16x64_bf16 v[224:227],a[176:179],a[56:63],v10
s_waitcnt lgkmcnt(4)
ds_read_b64 a[56:57],v42 offset:8
ds_read_b64 a[58:59],v42 offset:200
ds_read_b64 a[60:61],v43 offset:8
ds_read_b64 a[62:63],v43 offset:200
v_add_u32_sdwa v46,v9,v46 dst_sel:DWORD dst_unused:UNUSED_PAD src0_sel:WORD_0 src1_sel:DWORD
v_pk_add_f32 v[60:61],v[60:61],v[216:217]
v_pk_fma_f32 v[48:49],v[216:217],v[216:217],v[48:49]
v_pk_add_f32 v[62:63],v[62:63],v[218:219]
v_pk_fma_f32 v[50:51],v[218:219],v[218:219],v[50:51]
v_mfma_f32_32x32x16_bf16 v[240:255],v[32:35],v[32:35],0
v_smfmac_f32_16x16x64_bf16 v[228:231],a[180:183],a[64:71],v10
s_waitcnt lgkmcnt(4)
ds_read_b64 a[64:65],v42 offset:64
ds_read_b64 a[66:67],v42 offset:256
ds_read_b64 a[68:69],v43 offset:64
ds_read_b64 a[70:71],v43 offset:256
v_pk_add_f32 v[60:61],v[60:61],v[220:221]
v_pk_fma_f32 v[48:49],v[220:221],v[220:221],v[48:49]
v_pk_add_f32 v[62:63],v[62:63],v[222:223]
v_pk_fma_f32 v[50:51],v[222:223],v[222:223],v[50:51]
v_smfmac_f32_16x16x64_bf16 v[232:235],a[120:123],a[48:55],v10
s_waitcnt lgkmcnt(4)
ds_read_b64 a[48:49],v42 offset:72
ds_read_b64 a[50:51],v42 offset:264
ds_read_b64 a[52:53],v43 offset:72
ds_read_b64 a[54:55],v43 offset:264
v_pk_add_f32 v[60:61],v[60:61],v[224:225]
v_pk_fma_f32 v[48:49],v[224:225],v[224:225],v[48:49]
v_pk_add_f32 v[62:63],v[62:63],v[226:227]
v_pk_fma_f32 v[50:51],v[226:227],v[226:227],v[50:51]
v_smfmac_f32_16x16x64_bf16 v[236:239],a[124:127],a[56:63],v10
s_waitcnt lgkmcnt(4)
ds_read_b64 a[56:57],v42 offset:128
ds_read_b64 a[58:59],v42 offset:320
ds_read_b64 a[60:61],v43 offset:128
ds_read_b64 a[62:63],v43 offset:320
v_pk_add_f32 v[60:61],v[60:61],v[228:229]
v_pk_fma_f32 v[48:49],v[228:229],v[228:229],v[48:49]
v_pk_add_f32 v[62:63],v[62:63],v[230:231]
v_pk_fma_f32 v[50:51],v[230:231],v[230:231],v[50:51]
v_smfmac_f32_16x16x64_bf16 v[240:243],a[152:155],a[64:71],v10
s_waitcnt lgkmcnt(4)
ds_read_b64 a[64:65],v42 offset:136
ds_read_b64 a[66:67],v42 offset:328
ds_read_b64 a[68:69],v43 offset:136
ds_read_b64 a[70:71],v43 offset:328
v_pk_add_f32 v[60:61],v[60:61],v[232:233]
v_pk_fma_f32 v[48:49],v[232:233],v[232:233],v[48:49]
v_pk_add_f32 v[62:63],v[62:63],v[234:235]
v_pk_fma_f32 v[50:51],v[234:235],v[234:235],v[50:51]
v_smfmac_f32_16x16x64_bf16 v[244:247],a[156:159],a[48:55],v10
s_waitcnt lgkmcnt(4)
ds_read_b64 a[240:241],v44 offset:57344
ds_read_b64 a[242:243],v44 offset:57352
ds_read_b64 a[224:225],v45 offset:57344
ds_read_b64 a[226:227],v45 offset:57352
ds_read_b128 a[232:235],v13 offset:0
v_pk_add_f32 v[60:61],v[60:61],v[236:237]
v_pk_fma_f32 v[48:49],v[236:237],v[236:237],v[48:49]
v_pk_add_f32 v[62:63],v[62:63],v[238:239]
v_pk_fma_f32 v[50:51],v[238:239],v[238:239],v[50:51]
v_smfmac_f32_16x16x64_bf16 v[248:251],a[184:187],a[56:63],v10
s_waitcnt lgkmcnt(5)
buffer_store_dwordx4 a[80:83],v7,s[40:43],s11 offen offset:128 sc1
ds_read_b64 a[248:249],v46 offset:57344
ds_read_b64 a[250:251],v46 offset:57352
ds_read_b64 a[228:229],v47 offset:57344
ds_read_b64 a[230:231],v47 offset:57352
ds_read_b128 a[236:239],v13 offset:256
v_pk_add_f32 v[60:61],v[60:61],v[240:241]
v_pk_fma_f32 v[48:49],v[240:241],v[240:241],v[48:49]
v_pk_add_f32 v[62:63],v[62:63],v[242:243]
v_pk_fma_f32 v[50:51],v[242:243],v[242:243],v[50:51]
v_smfmac_f32_16x16x64_bf16 v[252:255],a[188:191],a[64:71],v10
ds_read_b128 v[40:43],v4 offset:3584
ds_read_b128 v[36:39],v4 offset:3616
v_pk_add_f32 v[60:61],v[60:61],v[244:245]
v_pk_fma_f32 v[48:49],v[244:245],v[244:245],v[48:49]
v_pk_add_f32 v[62:63],v[62:63],v[246:247]
v_pk_fma_f32 v[50:51],v[246:247],v[246:247],v[50:51]
v_pk_add_f32 v[60:61],v[60:61],v[248:249]
v_pk_fma_f32 v[48:49],v[248:249],v[248:249],v[48:49]
v_pk_add_f32 v[62:63],v[62:63],v[250:251]
v_pk_fma_f32 v[50:51],v[250:251],v[250:251],v[50:51]
v_pk_add_f32 v[60:61],v[60:61],v[252:253]
v_pk_fma_f32 v[48:49],v[252:253],v[252:253],v[48:49]
v_pk_add_f32 v[62:63],v[62:63],v[254:255]
v_pk_fma_f32 v[50:51],v[254:255],v[254:255],v[50:51]
s_nop 0
v_permlane32_swap_b32 v60,v62
v_permlane32_swap_b32 v61,v63
v_permlane32_swap_b32 v48,v50
v_permlane32_swap_b32_e64 v49,v51
v_pk_add_f32 v[60:61],v[60:61],v[62:63]
v_pk_add_f32 v[48:49],v[48:49],v[50:51]
s_nop 0
v_permlane16_swap_b32 v60,v61
v_permlane16_swap_b32 v48,v49
v_add_f32 v8,v60,v61
v_add_f32_e64 v9,v48,v49
v_mul_f32 v8,0x3c2aaaab,v8
v_mul_f32 v9,0x3c2aaaab,v9
v_cmp_ngt_f32 vcc,0x5d800000,v9
v_fma_f32 v9,-v8,v8,v9
v_max_f32 v9,0,v9
s_or_b64 s[90:91],s[90:91],vcc
v_add_f32 v9,0x3727c5ac,v9
v_rsq_f32 v9,v9
s_nop 0
v_mul_f32 v60,s96,v9
v_mul_f32 v61,s96,v9
v_mul_f32 v48,-v8,v60
v_mul_f32 v49,-v8,v60
v_permlane16_swap_b32 v60,v61
s_nop 0
v_permlane16_swap_b32 v48,v49
v_mov_b64 v[62:63],v[60:61]
v_mov_b64 v[50:51],v[48:49]
s_nop 0
v_permlane32_swap_b32 v60,v62
v_permlane32_swap_b32 v61,v63
v_permlane32_swap_b32 v48,v50
v_permlane32_swap_b32 v49,v51
s_or_b32 s64,s90,s91
s_lshl_b32 s65,s52,2
v_mov_b32 v8,s64
v_mov_b32 v9,s65
ds_write_b32 v9,v8 offset:11648
v_pk_fma_f32 v[160:161],v[160:161],v[60:61],v[48:49]
v_pk_fma_f32 v[162:163],v[162:163],v[62:63],v[50:51]
v_pk_fma_f32 v[164:165],v[164:165],v[60:61],v[48:49]
v_pk_fma_f32 v[166:167],v[166:167],v[62:63],v[50:51]
v_pk_fma_f32 v[168:169],v[168:169],v[60:61],v[48:49]
v_pk_fma_f32 v[170:171],v[170:171],v[62:63],v[50:51]
v_pk_fma_f32 v[172:173],v[172:173],v[60:61],v[48:49]
v_pk_fma_f32 v[174:175],v[174:175],v[62:63],v[50:51]
v_pk_fma_f32 v[208:209],v[208:209],v[60:61],v[48:49]
v_pk_fma_f32 v[210:211],v[210:211],v[62:63],v[50:51]
v_pk_fma_f32 v[212:213],v[212:213],v[60:61],v[48:49]
v_pk_fma_f32 v[214:215],v[214:215],v[62:63],v[50:51]
v_pk_fma_f32 v[232:233],v[232:233],v[60:61],v[48:49]
v_pk_fma_f32 v[234:235],v[234:235],v[62:63],v[50:51]
v_pk_fma_f32 v[236:237],v[236:237],v[60:61],v[48:49]
v_pk_fma_f32 v[238:239],v[238:239],v[62:63],v[50:51]
v_pk_fma_f32 v[176:177],v[176:177],v[60:61],v[48:49]
v_pk_fma_f32 v[178:179],v[178:179],v[62:63],v[50:51]
v_pk_fma_f32 v[180:181],v[180:181],v[60:61],v[48:49]
v_pk_fma_f32 v[182:183],v[182:183],v[62:63],v[50:51]
v_pk_fma_f32 v[184:185],v[184:185],v[60:61],v[48:49]
v_pk_fma_f32 v[186:187],v[186:187],v[62:63],v[50:51]
v_pk_fma_f32 v[188:189],v[188:189],v[60:61],v[48:49]
v_pk_fma_f32 v[190:191],v[190:191],v[62:63],v[50:51]
v_pk_fma_f32 v[216:217],v[216:217],v[60:61],v[48:49]
v_pk_fma_f32 v[218:219],v[218:219],v[62:63],v[50:51]
v_pk_fma_f32 v[220:221],v[220:221],v[60:61],v[48:49]
v_pk_fma_f32 v[222:223],v[222:223],v[62:63],v[50:51]
v_pk_fma_f32 v[240:241],v[240:241],v[60:61],v[48:49]
v_pk_fma_f32 v[242:243],v[242:243],v[62:63],v[50:51]
v_pk_fma_f32 v[244:245],v[244:245],v[60:61],v[48:49]
v_pk_fma_f32 v[246:247],v[246:247],v[62:63],v[50:51]
v_pk_fma_f32 v[192:193],v[192:193],v[60:61],v[48:49]
v_pk_fma_f32 v[194:195],v[194:195],v[62:63],v[50:51]
v_pk_fma_f32 v[196:197],v[196:197],v[60:61],v[48:49]
v_pk_fma_f32 v[198:199],v[198:199],v[62:63],v[50:51]
v_pk_fma_f32 v[200:201],v[200:201],v[60:61],v[48:49]
v_pk_fma_f32 v[202:203],v[202:203],v[62:63],v[50:51]
v_pk_fma_f32 v[204:205],v[204:205],v[60:61],v[48:49]
v_pk_fma_f32 v[206:207],v[206:207],v[62:63],v[50:51]
v_pk_fma_f32 v[224:225],v[224:225],v[60:61],v[48:49]
v_pk_fma_f32 v[226:227],v[226:227],v[62:63],v[50:51]
v_pk_fma_f32 v[228:229],v[228:229],v[60:61],v[48:49]
v_pk_fma_f32 v[230:231],v[230:231],v[62:63],v[50:51]
v_pk_fma_f32 v[248:249],v[248:249],v[60:61],v[48:49]
v_pk_fma_f32 v[250:251],v[250:251],v[62:63],v[50:51]
v_pk_fma_f32 v[252:253],v[252:253],v[60:61],v[48:49]
v_pk_fma_f32 v[254:255],v[254:255],v[62:63],v[50:51]
v_mfma_f32_16x16x32_bf16 v[32:35],a[204:207],a[8:11],v[20:23]
buffer_store_dwordx4 a[84:87],v7,s[40:43],s11 offen offset:192 sc1
v_cvt_pk_bf16_f32 v160,v160,v164
v_cvt_pk_bf16_f32 v168,v168,v172
v_cvt_pk_bf16_f32 v208,v208,v212
v_lshlrev_b32_sdwa v24,1,v24 dst_sel:DWORD dst_unused:UNUSED_PAD src0_sel:DWORD src1_sel:WORD_1
v_mfma_f32_16x16x32_bf16 v[32:35],a[208:211],a[24:27],v[32:35]
ds_read_u16 v24,v24
v_accvgpr_write_b32 a48,v160
v_accvgpr_write_b32 a49,v168
v_cvt_pk_bf16_f32 v232,v232,v236
v_accvgpr_write_b32 a50,v208
v_mfma_f32_16x16x32_bf16 v[32:35],a[212:215],a[40:43],v[32:35]
ds_read_b64_tr_b16 a[216:217],v1 offset:6160
v_cvt_pk_bf16_f32 v161,v161,v165
v_accvgpr_write_b32 a51,v232
v_cvt_pk_bf16_f32 v169,v169,v173
v_accvgpr_write_b32 a52,v161
v_mfma_f32_16x16x32_bf16 v[60:63],a[200:203],a[40:43],v[16:19]
ds_read_b64_tr_b16 a[218:219],v1 offset:7184
v_cvt_pk_bf16_f32 v209,v209,v213
v_accvgpr_write_b32 a53,v169
v_cvt_pk_bf16_f32 v233,v233,v237
v_lshlrev_b32_sdwa v26,1,v26 dst_sel:DWORD dst_unused:UNUSED_PAD src0_sel:DWORD src1_sel:WORD_1
v_accvgpr_write_b32 a54,v209
v_mfma_f32_16x16x32_bf16 v[60:63],a[196:199],a[24:27],v[60:63]
ds_read_u16_d16_hi v26,v26
v_cvt_pk_bf16_f32 v162,v162,v166
v_accvgpr_write_b32 a55,v233
v_cvt_pk_bf16_f32 v170,v170,v174
v_accvgpr_write_b32 a56,v162
v_mfma_f32_16x16x32_bf16 v[60:63],a[192:195],a[8:11],v[60:63]
buffer_store_dwordx4 a[88:91],v7,s[40:43],s11 offen offset:256 sc1
v_accvgpr_write_b32 a57,v170
v_cvt_pk_bf16_f32 v210,v210,v214
v_accvgpr_write_b32 a58,v210
v_lshlrev_b32_sdwa v25,1,v25 dst_sel:DWORD dst_unused:UNUSED_PAD src0_sel:DWORD src1_sel:WORD_1
v_cvt_pk_bf16_f32 v234,v234,v238
v_mfma_f32_16x16x32_bf16 v[48:51],a[192:195],a[12:15],v[16:19]
ds_read_u16 v25,v25
v_accvgpr_write_b32 a59,v234
v_cvt_pk_bf16_f32 v163,v163,v167
v_accvgpr_write_b32 a60,v163
v_mfma_f32_16x16x32_bf16 v[48:51],a[196:199],a[28:31],v[48:51]
ds_read_b64_tr_b16 a[220:221],v1 offset:8208
v_cvt_pk_bf16_f32 v171,v171,v175
v_accvgpr_write_b32 a61,v171
v_cvt_pk_bf16_f32 v211,v211,v215
v_accvgpr_write_b32 a62,v211
v_lshlrev_b32_sdwa v27,1,v27 dst_sel:DWORD dst_unused:UNUSED_PAD src0_sel:DWORD src1_sel:WORD_1
v_mfma_f32_16x16x32_bf16 v[48:51],a[200:203],a[44:47],v[48:51]
s_waitcnt lgkmcnt(6)
ds_read_u16_d16_hi v27,v27
v_cvt_pk_bf16_f32 v235,v235,v239
v_accvgpr_write_b32 a63,v235
v_cvt_pk_bf16_f32 v176,v176,v180
v_cvt_pk_bf16_f32 v184,v184,v188
v_mfma_f32_16x16x32_bf16 v[232:235],a[232:235],a[240:243],v[40:43]
s_nop 0
ds_read_b64_tr_b16 a[222:223],v1 offset:9232
v_cvt_pk_bf16_f32 v216,v216,v220
v_accvgpr_write_b32 a64,v176
v_accvgpr_write_b32 a65,v184
v_cvt_pk_bf16_f32 v240,v240,v244
v_lshlrev_b32_sdwa v28,1,v28 dst_sel:DWORD dst_unused:UNUSED_PAD src0_sel:DWORD src1_sel:WORD_1
v_mfma_f32_16x16x32_bf16 v[236:239],a[232:235],a[224:227],v[40:43]
ds_read_u16 v28,v28
v_accvgpr_write_b32 a66,v216
v_cvt_pk_bf16_f32 v177,v177,v181
v_accvgpr_write_b32 a67,v240
v_cvt_pk_bf16_f32 v185,v185,v189
v_mfma_f32_16x16x32_bf16 v[208:211],a[232:235],a[248:251],v[40:43]
v_accvgpr_write_b32 a68,v177
v_cvt_pk_bf16_f32 v217,v217,v221
v_accvgpr_write_b32 a69,v185
v_lshlrev_b32_sdwa v30,1,v30 dst_sel:DWORD dst_unused:UNUSED_PAD src0_sel:DWORD src1_sel:WORD_1
v_cvt_pk_bf16_f32 v241,v241,v245
v_mfma_f32_16x16x32_bf16 v[212:215],a[232:235],a[228:231],v[40:43]
ds_read_b128 a[232:235],v13 offset:512
ds_read_b128 v[40:43],v4 offset:3712
v_accvgpr_write_b32 a70,v217
v_cvt_pk_bf16_f32 v178,v178,v182
v_accvgpr_write_b32 a71,v241
v_mfma_f32_16x16x32_bf16 v[160:163],a[236:239],a[240:243],v[36:39]
ds_read_b64 a[240:241],v44 offset:57408
ds_read_b64 a[242:243],v44 offset:57416
v_cvt_pk_bf16_f32 v186,v186,v190
v_accvgpr_write_b32 a72,v178
v_accvgpr_write_b32 a73,v186
v_cvt_pk_bf16_f32 v218,v218,v222
v_lshlrev_b32_sdwa v29,1,v29 dst_sel:DWORD dst_unused:UNUSED_PAD src0_sel:DWORD src1_sel:WORD_1
v_mfma_f32_16x16x32_bf16 v[164:167],a[236:239],a[224:227],v[36:39]
ds_read_b64 a[224:225],v45 offset:57408
ds_read_b64 a[226:227],v45 offset:57416
v_accvgpr_write_b32 a74,v218
v_cvt_pk_bf16_f32 v242,v242,v246
v_accvgpr_write_b32 a75,v242
v_cvt_pk_bf16_f32 v179,v179,v183
v_mfma_f32_16x16x32_bf16 v[168:171],a[236:239],a[248:251],v[36:39]
ds_read_b64 a[248:249],v46 offset:57408
ds_read_b64 a[250:251],v46 offset:57416
v_accvgpr_write_b32 a76,v179
v_cvt_pk_bf16_f32 v187,v187,v191
v_accvgpr_write_b32 a77,v187
v_cvt_pk_bf16_f32 v219,v219,v223
v_lshlrev_b32_sdwa v31,1,v31 dst_sel:DWORD dst_unused:UNUSED_PAD src0_sel:DWORD src1_sel:WORD_1
v_mfma_f32_16x16x32_bf16 v[172:175],a[236:239],a[228:231],v[36:39]
s_waitcnt lgkmcnt(4)
buffer_store_dwordx4 a[92:95],v7,s[40:43],s11 offen offset:320 sc1
ds_read_b64 a[228:229],v47 offset:57408
v_accvgpr_write_b32 a78,v219
v_cvt_pk_bf16_f32 v243,v243,v247
v_accvgpr_write_b32 a79,v243
v_cvt_pk_bf16_f32 v192,v192,v196
v_mfma_f32_16x16x32_bf16 v[240:243],a[232:235],a[240:243],v[40:43]
s_waitcnt lgkmcnt(3)
ds_read_b64 a[230:231],v47 offset:57416
ds_read_b128 a[236:239],v13 offset:768
v_cvt_pk_bf16_f32 v200,v200,v204
v_cvt_pk_bf16_f32 v224,v224,v228
v_accvgpr_write_b32 a80,v192
v_accvgpr_write_b32 a81,v200
v_lshlrev_b32_sdwa v52,1,v52 dst_sel:DWORD dst_unused:UNUSED_PAD src0_sel:DWORD src1_sel:WORD_1
v_mfma_f32_16x16x32_bf16 v[244:247],a[232:235],a[224:227],v[40:43]
s_waitcnt lgkmcnt(3)
ds_read_b128 v[36:39],v4 offset:3744
ds_read_u16_d16_hi v30,v30
v_cvt_pk_bf16_f32 v248,v248,v252
v_accvgpr_write_b32 a82,v224
v_cvt_pk_bf16_f32 v193,v193,v197
v_mfma_f32_16x16x32_bf16 v[216:219],a[232:235],a[248:251],v[40:43]
s_waitcnt lgkmcnt(3)
ds_read_u16 v29,v29
v_accvgpr_write_b32 a83,v248
v_cvt_pk_bf16_f32 v201,v201,v205
v_accvgpr_write_b32 a84,v193
v_cvt_pk_bf16_f32 v225,v225,v229
v_lshlrev_b32_sdwa v54,1,v54 dst_sel:DWORD dst_unused:UNUSED_PAD src0_sel:DWORD src1_sel:WORD_1
v_mfma_f32_16x16x32_bf16 v[220:223],a[232:235],a[228:231],v[40:43]
s_waitcnt lgkmcnt(2)
ds_read_b128 a[232:235],v13 offset:1024
ds_read_b128 v[40:43],v4 offset:3840
v_accvgpr_write_b32 a85,v201
v_cvt_pk_bf16_f32 v249,v249,v253
v_accvgpr_write_b32 a86,v225
v_cvt_pk_bf16_f32 v194,v194,v198
v_mfma_f32_16x16x32_bf16 v[176:179],a[236:239],a[240:243],v[36:39]
s_nop 0
ds_read_b64 a[240:241],v44 offset:57472
ds_read_b64 a[242:243],v44 offset:57480
v_accvgpr_write_b32 a87,v249
v_cvt_pk_bf16_f32 v202,v202,v206
v_accvgpr_write_b32 a88,v194
v_accvgpr_write_b32 a89,v202
v_lshlrev_b32_sdwa v53,1,v53 dst_sel:DWORD dst_unused:UNUSED_PAD src0_sel:DWORD src1_sel:WORD_1
v_mfma_f32_16x16x32_bf16 v[180:183],a[236:239],a[224:227],v[36:39]
ds_read_b64 a[224:225],v45 offset:57472
ds_read_b64 a[226:227],v45 offset:57480
v_cvt_pk_bf16_f32 v226,v226,v230
v_accvgpr_write_b32 a90,v226
v_cvt_pk_bf16_f32 v250,v250,v254
v_accvgpr_write_b32 a91,v250
v_mfma_f32_16x16x32_bf16 v[184:187],a[236:239],a[248:251],v[36:39]
s_nop 0
ds_read_b64 a[248:249],v46 offset:57472
ds_read_b64 a[250:251],v46 offset:57480
v_cvt_pk_bf16_f32 v195,v195,v199
v_accvgpr_write_b32 a92,v195
v_cvt_pk_bf16_f32 v203,v203,v207
v_accvgpr_write_b32 a93,v203
v_mfma_f32_16x16x32_bf16 v[188:191],a[236:239],a[228:231],v[36:39]
s_waitcnt lgkmcnt(4)
ds_read_b64 a[228:229],v47 offset:57472
ds_read_b64 a[230:231],v47 offset:57480
v_cvt_pk_bf16_f32 v227,v227,v231
v_accvgpr_write_b32 a94,v227
v_cvt_pk_bf16_f32 v251,v251,v255
v_lshlrev_b32_sdwa v55,1,v55 dst_sel:DWORD dst_unused:UNUSED_PAD src0_sel:DWORD src1_sel:WORD_1
v_accvgpr_write_b32 a95,v251
v_mfma_f32_16x16x32_bf16 v[248:251],a[232:235],a[240:243],v[40:43]
s_waitcnt lgkmcnt(4)
ds_read_b128 a[236:239],v13 offset:1280
ds_read_b128 v[36:39],v4 offset:3872
v_mfma_f32_16x16x32_bf16 v[252:255],a[232:235],a[224:227],v[40:43]
s_waitcnt lgkmcnt(4)
ds_read_u16_d16_hi v31,v31
ds_read_u16 v52,v52
v_lshlrev_b32_sdwa v56,1,v56 dst_sel:DWORD dst_unused:UNUSED_PAD src0_sel:DWORD src1_sel:WORD_1
v_mfma_f32_16x16x32_bf16 v[224:227],a[232:235],a[248:251],v[40:43]
s_waitcnt lgkmcnt(4)
ds_read_u16_d16_hi v54,v54
v_lshlrev_b32_sdwa v58,1,v58 dst_sel:DWORD dst_unused:UNUSED_PAD src0_sel:DWORD src1_sel:WORD_1
v_mfma_f32_16x16x32_bf16 v[228:231],a[232:235],a[228:231],v[40:43]
s_waitcnt lgkmcnt(3)
ds_read_b64_tr_b16 a[232:233],v3 offset:6224
ds_read_b64_tr_b16 a[234:235],v3 offset:7760
v_mfma_f32_16x16x32_bf16 v[192:195],a[236:239],a[240:243],v[36:39]
ds_read_b64_tr_b16 a[240:241],v3 offset:6232
ds_read_b64_tr_b16 a[242:243],v3 offset:7768
v_mfma_f32_16x16x32_bf16 v[196:199],a[236:239],a[224:227],v[36:39]
ds_read_b64_tr_b16 a[224:225],v3 offset:6288
ds_read_b64_tr_b16 a[226:227],v3 offset:7824
v_lshlrev_b32_sdwa v57,1,v57 dst_sel:DWORD dst_unused:UNUSED_PAD src0_sel:DWORD src1_sel:WORD_1
v_mfma_f32_16x16x32_bf16 v[200:203],a[236:239],a[248:251],v[36:39]
ds_read_b64_tr_b16 a[248:249],v3 offset:6296
ds_read_b64_tr_b16 a[250:251],v3 offset:7832
v_mfma_f32_16x16x32_bf16 v[204:207],a[236:239],a[228:231],v[36:39]
ds_read_u16 v53,v53
ds_read_u16_d16_hi v55,v55
v_lshlrev_b32_sdwa v59,1,v59 dst_sel:DWORD dst_unused:UNUSED_PAD src0_sel:DWORD src1_sel:WORD_1
v_mfma_f32_16x16x32_bf16 v[44:47],a[212:215],a[44:47],v[20:23]
ds_read_u16 v56,v56
v_mfma_f32_16x16x32_bf16 v[44:47],a[208:211],a[28:31],v[44:47]
s_waitcnt vmcnt(18)
ds_read_u16_d16_hi v58,v58
v_mfma_f32_16x16x32_bf16 v[44:47],a[204:207],a[12:15],v[44:47]
s_barrier
ds_read_u16 v57,v57
ds_read_u16_d16_hi v59,v59
v_mfma_f32_16x16x32_bf16 v[40:43],a[204:207],a[48:51],v[20:23]
v_lshlrev_b32_sdwa v32,1,v32 dst_sel:DWORD dst_unused:UNUSED_PAD src0_sel:DWORD src1_sel:WORD_1
v_lshlrev_b32_sdwa v34,1,v34 dst_sel:DWORD dst_unused:UNUSED_PAD src0_sel:DWORD src1_sel:WORD_1
v_mfma_f32_16x16x32_bf16 v[40:43],a[208:211],a[64:67],v[40:43]
ds_read_u16 v32,v32
v_lshl_add_u64 v[24:25],v[24:25],0,v[26:27]
v_mfma_f32_16x16x32_bf16 v[40:43],a[212:215],a[80:83],v[40:43]
s_waitcnt lgkmcnt(11)
ds_read_u16_d16_hi v34,v34
v_lshl_add_u64 v[26:27],v[28:29],0,v[30:31]
v_mfma_f32_16x16x32_bf16 v[36:39],a[200:203],a[80:83],v[16:19]
v_lshlrev_b32_sdwa v33,1,v33 dst_sel:DWORD dst_unused:UNUSED_PAD src0_sel:DWORD src1_sel:WORD_1
v_lshlrev_b32_sdwa v35,1,v35 dst_sel:DWORD dst_unused:UNUSED_PAD src0_sel:DWORD src1_sel:WORD_1
v_mfma_f32_16x16x32_bf16 v[36:39],a[196:199],a[64:67],v[36:39]
s_waitcnt lgkmcnt(6)
ds_read_u16 v33,v33
v_lshl_add_u64 v[28:29],v[52:53],0,v[54:55]
v_mfma_f32_16x16x32_bf16 v[36:39],a[192:195],a[48:51],v[36:39]
ds_read_u16_d16_hi v35,v35
v_lshlrev_b32_sdwa v60,1,v60 dst_sel:DWORD dst_unused:UNUSED_PAD src0_sel:DWORD src1_sel:WORD_1
v_mfma_f32_16x16x32_bf16 v[52:55],a[192:195],a[52:55],v[16:19]
ds_read_u16 v60,v60
v_lshlrev_b32_sdwa v62,1,v62 dst_sel:DWORD dst_unused:UNUSED_PAD src0_sel:DWORD src1_sel:WORD_1
v_mfma_f32_16x16x32_bf16 v[52:55],a[196:199],a[68:71],v[52:55]
s_waitcnt lgkmcnt(5)
ds_read_u16_d16_hi v62,v62
v_lshl_add_u64 v[30:31],v[56:57],0,v[58:59]
v_mfma_f32_16x16x32_bf16 v[52:55],a[200:203],a[84:87],v[52:55]
s_nop 0
v_lshlrev_b32_sdwa v61,1,v61 dst_sel:DWORD dst_unused:UNUSED_PAD src0_sel:DWORD src1_sel:WORD_1
v_lshlrev_b32_sdwa v63,1,v63 dst_sel:DWORD dst_unused:UNUSED_PAD src0_sel:DWORD src1_sel:WORD_1
v_mfma_f32_16x16x32_bf16 v[56:59],a[212:215],a[84:87],v[20:23]
ds_read_u16 v61,v61
v_lshlrev_b32_sdwa v44,1,v44 dst_sel:DWORD dst_unused:UNUSED_PAD src0_sel:DWORD src1_sel:WORD_1
v_mfma_f32_16x16x32_bf16 v[56:59],a[208:211],a[68:71],v[56:59]
ds_read_u16_d16_hi v63,v63
v_lshlrev_b32_sdwa v46,1,v46 dst_sel:DWORD dst_unused:UNUSED_PAD src0_sel:DWORD src1_sel:WORD_1
v_mfma_f32_16x16x32_bf16 v[56:59],a[204:207],a[52:55],v[56:59]
ds_read_u16 v44,v44
v_lshlrev_b32_sdwa v45,1,v45 dst_sel:DWORD dst_unused:UNUSED_PAD src0_sel:DWORD src1_sel:WORD_1
v_mfma_f32_16x16x32_bf16 v[136:139],a[244:247],v[24:27],v[136:139]
ds_read_u16_d16_hi v46,v46
v_lshlrev_b32_sdwa v47,1,v47 dst_sel:DWORD dst_unused:UNUSED_PAD src0_sel:DWORD src1_sel:WORD_1
v_mfma_f32_16x16x32_bf16 v[140:143],a[244:247],v[28:31],v[140:143]
ds_read_u16 v45,v45
v_lshlrev_b32_sdwa v48,1,v48 dst_sel:DWORD dst_unused:UNUSED_PAD src0_sel:DWORD src1_sel:WORD_1
v_mfma_f32_16x16x32_bf16 v[68:71],a[252:255],v[28:31],v[68:71]
ds_read_u16_d16_hi v47,v47
v_lshlrev_b32_sdwa v50,1,v50 dst_sel:DWORD dst_unused:UNUSED_PAD src0_sel:DWORD src1_sel:WORD_1
v_mfma_f32_16x16x32_bf16 v[64:67],a[252:255],v[24:27],v[64:67]
ds_read_u16 v48,v48
v_lshlrev_b32_sdwa v49,1,v49 dst_sel:DWORD dst_unused:UNUSED_PAD src0_sel:DWORD src1_sel:WORD_1
v_mfma_f32_16x16x32_bf16 v[144:147],a[232:235],v[24:27],v[144:147]
ds_read_u16_d16_hi v50,v50
v_lshlrev_b32_sdwa v51,1,v51 dst_sel:DWORD dst_unused:UNUSED_PAD src0_sel:DWORD src1_sel:WORD_1
v_mfma_f32_16x16x32_bf16 v[148:151],a[232:235],v[28:31],v[148:151]
ds_read_u16 v49,v49
v_mfma_f32_16x16x32_bf16 v[84:87],a[240:243],v[28:31],v[84:87]
ds_read_u16_d16_hi v51,v51
v_mfma_f32_16x16x32_bf16 v[80:83],a[240:243],v[24:27],v[80:83]
v_mfma_f32_16x16x32_bf16 v[152:155],a[224:227],v[24:27],v[152:155]
v_mfma_f32_16x16x32_bf16 v[156:159],a[224:227],v[28:31],v[156:159]
v_mfma_f32_16x16x32_bf16 v[100:103],a[248:251],v[28:31],v[100:103]
v_mfma_f32_16x16x32_bf16 v[96:99],a[248:251],v[24:27],v[96:99]
v_mfma_f32_16x16x32_bf16 v[28:31],a[204:207],a[56:59],v[20:23]
v_lshlrev_b32_sdwa v40,1,v40 dst_sel:DWORD dst_unused:UNUSED_PAD src0_sel:DWORD src1_sel:WORD_1
v_fma_mixlo_f16 v8,v53,s99,0
v_mfma_f32_16x16x32_bf16 v[28:31],a[208:211],a[72:75],v[28:31]
s_waitcnt lgkmcnt(2)
ds_read_u16 v40,v40
v_lshl_add_u64 v[32:33],v[32:33],0,v[34:35]
v_mfma_f32_16x16x32_bf16 v[28:31],a[212:215],a[88:91],v[28:31]
v_lshl_add_u64 v[34:35],v[60:61],0,v[62:63]
v_lshlrev_b32_sdwa v42,1,v42 dst_sel:DWORD dst_unused:UNUSED_PAD src0_sel:DWORD src1_sel:WORD_1
v_mfma_f32_16x16x32_bf16 v[24:27],a[200:203],a[88:91],v[16:19]
ds_read_u16_d16_hi v42,v42
v_fma_mixhi_f16 v8,v55,s99,0
v_mfma_f32_16x16x32_bf16 v[24:27],a[196:199],a[72:75],v[24:27]
v_lshl_add_u64 v[60:61],v[44:45],0,v[46:47]
v_lshlrev_b32_sdwa v41,1,v41 dst_sel:DWORD dst_unused:UNUSED_PAD src0_sel:DWORD src1_sel:WORD_1
v_pk_fma_f16 v9,v8,v8,s54
v_mfma_f32_16x16x32_bf16 v[24:27],a[192:195],a[56:59],v[24:27]
ds_read_u16 v41,v41
v_lshlrev_b32_sdwa v43,1,v43 dst_sel:DWORD dst_unused:UNUSED_PAD src0_sel:DWORD src1_sel:WORD_1
v_mfma_f32_16x16x32_bf16 v[44:47],a[192:195],a[60:63],v[16:19]
ds_read_b128 v[16:19],v4 offset:768
ds_read_b64_tr_b16 a[192:193],v1 offset:10256
v_pk_mul_f16 v9,v8,v9
v_mfma_f32_16x16x32_bf16 v[44:47],a[196:199],a[76:79],v[44:47]
s_waitcnt lgkmcnt(5)
ds_read_b64_tr_b16 a[194:195],v1 offset:11280
ds_read_b64_tr_b16 a[196:197],v2 offset:10256
v_lshl_add_u64 v[62:63],v[48:49],0,v[50:51]
v_lshlrev_b32_sdwa v36,1,v36 dst_sel:DWORD dst_unused:UNUSED_PAD src0_sel:DWORD src1_sel:WORD_1
v_mfma_f32_16x16x32_bf16 v[44:47],a[200:203],a[92:95],v[44:47]
ds_read_b64_tr_b16 a[198:199],v2 offset:11280
ds_read_b64_tr_b16 a[200:201],v2 offset:8208
v_exp_f16 v8,v9
v_mfma_f32_16x16x32_bf16 v[48:51],a[212:215],a[92:95],v[20:23]
ds_read_b128 v[20:23],v4 offset:800
ds_read_b64_tr_b16 a[202:203],v2 offset:9232
v_lshlrev_b32_sdwa v38,1,v38 dst_sel:DWORD dst_unused:UNUSED_PAD src0_sel:DWORD src1_sel:WORD_1
v_mfma_f32_16x16x32_bf16 v[48:51],a[208:211],a[76:79],v[48:51]
TE11_%=:
s_cbranch_execz TP10203_%=
ds_read_b64_tr_b16 a[212:213],v2 offset:6160
ds_read_b64_tr_b16 a[214:215],v2 offset:7184
v_lshlrev_b32_sdwa v37,1,v37 dst_sel:DWORD dst_unused:UNUSED_PAD src0_sel:DWORD src1_sel:WORD_1
v_mfma_f32_16x16x32_bf16 v[48:51],a[204:207],a[60:63],v[48:51]
ds_read_u16_d16_hi v43,v43
v_exp_f16_sdwa v8,v9 dst_sel:WORD_1 dst_unused:UNUSED_PRESERVE src0_sel:WORD_1
v_mfma_f32_16x16x32_bf16 v[112:115],a[244:247],v[32:35],v[112:115]
ds_read_u16 v36,v36
v_lshlrev_b32_sdwa v39,1,v39 dst_sel:DWORD dst_unused:UNUSED_PAD src0_sel:DWORD src1_sel:WORD_1
v_mfma_f32_16x16x32_bf16 v[116:119],a[244:247],v[60:63],v[116:119]
ds_read_u16_d16_hi v38,v38
v_lshlrev_b32_sdwa v56,1,v56 dst_sel:DWORD dst_unused:UNUSED_PAD src0_sel:DWORD src1_sel:WORD_1
v_mfma_f32_16x16x32_bf16 v[76:79],a[252:255],v[60:63],v[76:79]
ds_read_u16 v37,v37
v_pk_fma_f16 v8,v8,s55,s55
v_mfma_f32_16x16x32_bf16 v[72:75],a[252:255],v[32:35],v[72:75]
v_rcp_f16_e64 v9,v8
v_lshlrev_b32_sdwa v58,1,v58 dst_sel:DWORD dst_unused:UNUSED_PAD src0_sel:DWORD src1_sel:WORD_1
v_mfma_f32_16x16x32_bf16 v[120:123],a[232:235],v[32:35],v[120:123]
ds_read_u16_d16_hi v39,v39
v_lshlrev_b32_sdwa v57,1,v57 dst_sel:DWORD dst_unused:UNUSED_PAD src0_sel:DWORD src1_sel:WORD_1
v_mfma_f32_16x16x32_bf16 v[124:127],a[232:235],v[60:63],v[124:127]
ds_read_u16 v56,v56
v_lshlrev_b32_sdwa v59,1,v59 dst_sel:DWORD dst_unused:UNUSED_PAD src0_sel:DWORD src1_sel:WORD_1
v_mfma_f32_16x16x32_bf16 v[92:95],a[240:243],v[60:63],v[92:95]
ds_read_u16_d16_hi v58,v58
v_lshlrev_b32_sdwa v52,1,v52 dst_sel:DWORD dst_unused:UNUSED_PAD src0_sel:DWORD src1_sel:WORD_1
v_mfma_f32_16x16x32_bf16 v[88:91],a[240:243],v[32:35],v[88:91]
v_rcp_f16_sdwa v9,v8 dst_sel:WORD_1 dst_unused:UNUSED_PRESERVE src0_sel:WORD_1
v_mfma_f32_16x16x32_bf16 v[128:131],a[224:227],v[32:35],v[128:131]
ds_read_u16 v57,v57
ds_read_u16_d16_hi v59,v59
v_mfma_f32_16x16x32_bf16 v[132:135],a[224:227],v[60:63],v[132:135]
ds_read_u16 v52,v52
v_lshlrev_b32_sdwa v54,1,v54 dst_sel:DWORD dst_unused:UNUSED_PAD src0_sel:DWORD src1_sel:WORD_1
v_mfma_f32_16x16x32_bf16 v[108:111],a[248:251],v[60:63],v[108:111]
ds_read_u16_d16_hi v54,v54
v_fma_mix_f32 v53,v53,v9,0 op_sel_hi:[0,1,0]
v_mfma_f32_16x16x32_bf16 v[104:107],a[248:251],v[32:35],v[104:107]
s_waitcnt lgkmcnt(7)
v_fma_mix_f32 v55,v55,v9,0 op_sel:[0,1,0] op_sel_hi:[0,1,0]
v_mfma_f32_16x16x32_bf16 v[60:63],a[216:219],a[0:3],v[16:19]
s_add_u32 m0,s62,0
v_lshlrev_b32_sdwa v28,1,v28 dst_sel:DWORD dst_unused:UNUSED_PAD src0_sel:DWORD src1_sel:WORD_1
v_lshlrev_b32_sdwa v30,1,v30 dst_sel:DWORD dst_unused:UNUSED_PAD src0_sel:DWORD src1_sel:WORD_1
v_mfma_f32_16x16x32_bf16 v[60:63],a[220:223],a[16:19],v[60:63]
s_mov_b32 s68,s87
ds_read_u16 v28,v28
v_lshl_add_u64 v[40:41],v[40:41],0,v[42:43]
v_mfma_f32_16x16x32_bf16 v[60:63],a[192:195],a[32:35],v[60:63]
s_waitcnt lgkmcnt(6)
buffer_load_dwordx4 v5,s[44:47],s68 offen offset:0 lds
v_lshl_add_u64 v[42:43],v[36:37],0,v[38:39]
v_mfma_f32_16x16x32_bf16 v[32:35],a[196:199],a[32:35],v[20:23]
s_add_u32 s68,s68,s53
ds_read_u16_d16_hi v30,v30
v_lshlrev_b32_sdwa v29,1,v29 dst_sel:DWORD dst_unused:UNUSED_PAD src0_sel:DWORD src1_sel:WORD_1
v_mfma_f32_16x16x32_bf16 v[32:35],a[200:203],a[16:19],v[32:35]
s_waitcnt lgkmcnt(4)
buffer_load_dwordx4 v5,s[44:47],s68 offen offset:1024 lds
v_lshl_add_u64 v[36:37],v[56:57],0,v[58:59]
v_mfma_f32_16x16x32_bf16 v[32:35],a[212:215],a[0:3],v[32:35]
ds_read_u16 v29,v29
v_lshlrev_b32_sdwa v31,1,v31 dst_sel:DWORD dst_unused:UNUSED_PAD src0_sel:DWORD src1_sel:WORD_1
v_mfma_f32_16x16x32_bf16 v[56:59],a[212:215],a[4:7],v[20:23]
s_waitcnt lgkmcnt(3)
ds_read_u16_d16_hi v31,v31
v_or_b32 v38,v52,v54
v_lshlrev_b32_sdwa v24,1,v24 dst_sel:DWORD dst_unused:UNUSED_PAD src0_sel:DWORD src1_sel:WORD_1
v_mfma_f32_16x16x32_bf16 v[56:59],a[200:203],a[20:23],v[56:59]
ds_read_u16 v24,v24
v_cvt_pk_bf16_f32 v39,v53,v55
v_lshlrev_b32_sdwa v26,1,v26 dst_sel:DWORD dst_unused:UNUSED_PAD src0_sel:DWORD src1_sel:WORD_1
v_mfma_f32_16x16x32_bf16 v[56:59],a[196:199],a[36:39],v[56:59]
ds_read_u16_d16_hi v26,v26
v_lshlrev_b32_sdwa v25,1,v25 dst_sel:DWORD dst_unused:UNUSED_PAD src0_sel:DWORD src1_sel:WORD_1
v_mfma_f32_16x16x32_bf16 v[52:55],a[192:195],a[36:39],v[16:19]
ds_read_u16 v25,v25
v_lshlrev_b32_sdwa v27,1,v27 dst_sel:DWORD dst_unused:UNUSED_PAD src0_sel:DWORD src1_sel:WORD_1
v_mfma_f32_16x16x32_bf16 v[52:55],a[220:223],a[20:23],v[52:55]
ds_read_u16_d16_hi v27,v27
v_lshlrev_b32_sdwa v48,1,v48 dst_sel:DWORD dst_unused:UNUSED_PAD src0_sel:DWORD src1_sel:WORD_1
v_mfma_f32_16x16x32_bf16 v[52:55],a[216:219],a[4:7],v[52:55]
ds_read_u16 v48,v48
v_lshlrev_b32_sdwa v50,1,v50 dst_sel:DWORD dst_unused:UNUSED_PAD src0_sel:DWORD src1_sel:WORD_1
v_mfma_f32_16x16x32_bf16 v[232:235],a[244:247],v[40:43],v[232:235]
ds_read_u16_d16_hi v50,v50
v_lshlrev_b32_sdwa v49,1,v49 dst_sel:DWORD dst_unused:UNUSED_PAD src0_sel:DWORD src1_sel:WORD_1
v_mfma_f32_16x16x32_bf16 v[236:239],a[244:247],v[36:39],v[236:239]
ds_read_u16 v49,v49
v_lshlrev_b32_sdwa v51,1,v51 dst_sel:DWORD dst_unused:UNUSED_PAD src0_sel:DWORD src1_sel:WORD_1
v_mfma_f32_16x16x32_bf16 v[164:167],a[252:255],v[36:39],v[164:167]
ds_read_u16_d16_hi v51,v51
ds_read_b64_tr_b16 a[228:229],v3 offset:12320
v_mfma_f32_16x16x32_bf16 v[160:163],a[252:255],v[40:43],v[160:163]
s_add_u32 s68,s68,s53
ds_read_b64_tr_b16 a[230:231],v3 offset:13856
v_lshlrev_b32_sdwa v44,1,v44 dst_sel:DWORD dst_unused:UNUSED_PAD src0_sel:DWORD src1_sel:WORD_1
v_mfma_f32_16x16x32_bf16 v[240:243],a[232:235],v[40:43],v[240:243]
buffer_load_dwordx4 v5,s[44:47],s68 offen offset:2048 lds
v_lshlrev_b32_sdwa v46,1,v46 dst_sel:DWORD dst_unused:UNUSED_PAD src0_sel:DWORD src1_sel:WORD_1
v_mfma_f32_16x16x32_bf16 v[244:247],a[232:235],v[36:39],v[244:247]
ds_read_u16 v44,v44
v_lshlrev_b32_sdwa v45,1,v45 dst_sel:DWORD dst_unused:UNUSED_PAD src0_sel:DWORD src1_sel:WORD_1
v_mfma_f32_16x16x32_bf16 v[180:183],a[240:243],v[36:39],v[180:183]
ds_read_u16_d16_hi v46,v46
v_lshlrev_b32_sdwa v47,1,v47 dst_sel:DWORD dst_unused:UNUSED_PAD src0_sel:DWORD src1_sel:WORD_1
v_mfma_f32_16x16x32_bf16 v[176:179],a[240:243],v[40:43],v[176:179]
ds_read_u16 v45,v45
v_mfma_f32_16x16x32_bf16 v[248:251],a[224:227],v[40:43],v[248:251]
ds_read_u16_d16_hi v47,v47
v_mfma_f32_16x16x32_bf16 v[252:255],a[224:227],v[36:39],v[252:255]
ds_read_b64_tr_b16 a[236:237],v3 offset:12328
v_mfma_f32_16x16x32_bf16 v[196:199],a[248:251],v[36:39],v[196:199]
ds_read_b64_tr_b16 a[238:239],v3 offset:13864
v_mfma_f32_16x16x32_bf16 v[192:195],a[248:251],v[40:43],v[192:195]
v_mfma_f32_16x16x32_bf16 v[36:39],a[216:219],a[8:11],v[16:19]
ds_read_b64_tr_b16 a[208:209],v1 offset:12320
v_lshlrev_b32_sdwa v60,1,v60 dst_sel:DWORD dst_unused:UNUSED_PAD src0_sel:DWORD src1_sel:WORD_1
v_mfma_f32_16x16x32_bf16 v[36:39],a[220:223],a[24:27],v[36:39]
s_waitcnt lgkmcnt(7)
ds_read_u16 v60,v60
v_lshl_add_u64 v[28:29],v[28:29],0,v[30:31]
v_fma_mixlo_f16 v8,v57,s99,0
v_mfma_f32_16x16x32_bf16 v[36:39],a[192:195],a[40:43],v[36:39]
ds_read_b64_tr_b16 a[210:211],v1 offset:13344
v_lshl_add_u64 v[30:31],v[24:25],0,v[26:27]
v_lshlrev_b32_sdwa v62,1,v62 dst_sel:DWORD dst_unused:UNUSED_PAD src0_sel:DWORD src1_sel:WORD_1
v_mfma_f32_16x16x32_bf16 v[40:43],a[196:199],a[40:43],v[20:23]
ds_read_u16_d16_hi v62,v62
v_fma_mixhi_f16 v8,v59,s99,0
v_mfma_f32_16x16x32_bf16 v[40:43],a[200:203],a[24:27],v[40:43]
ds_read_b64_tr_b16 a[204:205],v1 offset:14368
v_lshl_add_u64 v[24:25],v[48:49],0,v[50:51]
v_lshlrev_b32_sdwa v61,1,v61 dst_sel:DWORD dst_unused:UNUSED_PAD src0_sel:DWORD src1_sel:WORD_1
v_pk_fma_f16 v9,v8,v8,s54
v_mfma_f32_16x16x32_bf16 v[40:43],a[212:215],a[8:11],v[40:43]
ds_read_u16 v61,v61
v_lshlrev_b32_sdwa v63,1,v63 dst_sel:DWORD dst_unused:UNUSED_PAD src0_sel:DWORD src1_sel:WORD_1
v_mfma_f32_16x16x32_bf16 v[48:51],a[212:215],a[12:15],v[20:23]
ds_read_u16_d16_hi v63,v63
v_pk_mul_f16 v9,v8,v9
v_mfma_f32_16x16x32_bf16 v[48:51],a[200:203],a[28:31],v[48:51]
s_waitcnt lgkmcnt(7)
ds_read_b64_tr_b16 a[206:207],v1 offset:15392
v_lshl_add_u64 v[26:27],v[44:45],0,v[46:47]
v_lshlrev_b32_sdwa v32,1,v32 dst_sel:DWORD dst_unused:UNUSED_PAD src0_sel:DWORD src1_sel:WORD_1
v_mfma_f32_16x16x32_bf16 v[48:51],a[196:199],a[44:47],v[48:51]
v_exp_f16 v8,v9
v_mfma_f32_16x16x32_bf16 v[44:47],a[192:195],a[44:47],v[16:19]
ds_read_u16 v32,v32
v_lshlrev_b32_sdwa v34,1,v34 dst_sel:DWORD dst_unused:UNUSED_PAD src0_sel:DWORD src1_sel:WORD_1
v_mfma_f32_16x16x32_bf16 v[44:47],a[220:223],a[28:31],v[44:47]
ds_read_u16_d16_hi v34,v34
v_lshlrev_b32_sdwa v33,1,v33 dst_sel:DWORD dst_unused:UNUSED_PAD src0_sel:DWORD src1_sel:WORD_1
v_mfma_f32_16x16x32_bf16 v[44:47],a[216:219],a[12:15],v[44:47]
v_exp_f16_sdwa v8,v9 dst_sel:WORD_1 dst_unused:UNUSED_PRESERVE src0_sel:WORD_1
v_mfma_f32_16x16x32_bf16 v[208:211],a[244:247],v[28:31],v[208:211]
ds_read_u16 v33,v33
v_lshlrev_b32_sdwa v35,1,v35 dst_sel:DWORD dst_unused:UNUSED_PAD src0_sel:DWORD src1_sel:WORD_1
v_mfma_f32_16x16x32_bf16 v[212:215],a[244:247],v[24:27],v[212:215]
ds_read_u16_d16_hi v35,v35
v_lshlrev_b32_sdwa v52,1,v52 dst_sel:DWORD dst_unused:UNUSED_PAD src0_sel:DWORD src1_sel:WORD_1
v_mfma_f32_16x16x32_bf16 v[172:175],a[252:255],v[24:27],v[172:175]
ds_read_u16 v52,v52
v_pk_fma_f16 v8,v8,s55,s55
v_mfma_f32_16x16x32_bf16 v[168:171],a[252:255],v[28:31],v[168:171]
ds_read_b64_tr_b16 a[244:245],v3 offset:12384
v_rcp_f16 v9,v8
v_lshlrev_b32_sdwa v54,1,v54 dst_sel:DWORD dst_unused:UNUSED_PAD src0_sel:DWORD src1_sel:WORD_1
v_mfma_f32_16x16x32_bf16 v[216:219],a[232:235],v[28:31],v[216:219]
ds_read_u16_d16_hi v54,v54
v_lshlrev_b32_sdwa v53,1,v53 dst_sel:DWORD dst_unused:UNUSED_PAD src0_sel:DWORD src1_sel:WORD_1
v_mfma_f32_16x16x32_bf16 v[220:223],a[232:235],v[24:27],v[220:223]
ds_read_u16 v53,v53
ds_read_b64_tr_b16 a[246:247],v3 offset:13920
v_mfma_f32_16x16x32_bf16 v[188:191],a[240:243],v[24:27],v[188:191]
ds_read_b64_tr_b16 a[252:253],v3 offset:12392
v_lshlrev_b32_sdwa v55,1,v55 dst_sel:DWORD dst_unused:UNUSED_PAD src0_sel:DWORD src1_sel:WORD_1
v_mfma_f32_16x16x32_bf16 v[184:187],a[240:243],v[28:31],v[184:187]
ds_read_u16_d16_hi v55,v55
v_rcp_f16_sdwa v9,v8 dst_sel:WORD_1 dst_unused:UNUSED_PRESERVE src0_sel:WORD_1
v_mfma_f32_16x16x32_bf16 v[224:227],a[224:227],v[28:31],v[224:227]
ds_read_b64_tr_b16 a[254:255],v3 offset:13928
ds_read_b64_tr_b16 a[232:233],v3 offset:12448
v_lshlrev_b32_sdwa v56,1,v56 dst_sel:DWORD dst_unused:UNUSED_PAD src0_sel:DWORD src1_sel:WORD_1
v_mfma_f32_16x16x32_bf16 v[228:231],a[224:227],v[24:27],v[228:231]
ds_read_u16 v56,v56
v_lshlrev_b32_sdwa v58,1,v58 dst_sel:DWORD dst_unused:UNUSED_PAD src0_sel:DWORD src1_sel:WORD_1
v_mfma_f32_16x16x32_bf16 v[204:207],a[248:251],v[24:27],v[204:207]
ds_read_u16_d16_hi v58,v58
v_fma_mix_f32 v57,v57,v9,0 op_sel_hi:[0,1,0]
v_mfma_f32_16x16x32_bf16 v[200:203],a[248:251],v[28:31],v[200:203]
ds_read_b64_tr_b16 a[234:235],v3 offset:13984
v_fma_mix_f32 v59,v59,v9,0 op_sel:[0,1,0] op_sel_hi:[0,1,0]
v_mfma_f32_16x16x32_bf16 v[24:27],a[216:219],a[48:51],v[16:19]
s_add_u32 m0,s62,6160
ds_read_b64_tr_b16 a[240:241],v3 offset:12456
v_lshlrev_b32_sdwa v36,1,v36 dst_sel:DWORD dst_unused:UNUSED_PAD src0_sel:DWORD src1_sel:WORD_1
v_mfma_f32_16x16x32_bf16 v[24:27],a[220:223],a[64:67],v[24:27]
s_waitcnt lgkmcnt(10)
ds_read_b64_tr_b16 a[242:243],v3 offset:13992
v_lshl_add_u64 v[60:61],v[60:61],0,v[62:63]
v_mfma_f32_16x16x32_bf16 v[24:27],a[192:195],a[80:83],v[24:27]
s_add_u32 s68,s68,s59
ds_read_u16 v36,v36
v_lshl_add_u64 v[62:63],v[32:33],0,v[34:35]
v_lshlrev_b32_sdwa v38,1,v38 dst_sel:DWORD dst_unused:UNUSED_PAD src0_sel:DWORD src1_sel:WORD_1
v_mfma_f32_16x16x32_bf16 v[28:31],a[196:199],a[80:83],v[20:23]
buffer_load_dwordx4 v5,s[44:47],s68 offen offset:0 lds
v_lshlrev_b32_sdwa v37,1,v37 dst_sel:DWORD dst_unused:UNUSED_PAD src0_sel:DWORD src1_sel:WORD_1
v_mfma_f32_16x16x32_bf16 v[28:31],a[200:203],a[64:67],v[28:31]
s_waitcnt lgkmcnt(8)
ds_read_u16_d16_hi v38,v38
v_lshl_add_u64 v[32:33],v[52:53],0,v[54:55]
v_mfma_f32_16x16x32_bf16 v[28:31],a[212:215],a[48:51],v[28:31]
s_add_u32 s68,s68,s53
ds_read_u16 v37,v37
v_lshlrev_b32_sdwa v39,1,v39 dst_sel:DWORD dst_unused:UNUSED_PAD src0_sel:DWORD src1_sel:WORD_1
v_mfma_f32_16x16x32_bf16 v[52:55],a[212:215],a[52:55],v[20:23]
s_waitcnt lgkmcnt(6)
buffer_load_dwordx4 v5,s[44:47],s68 offen offset:1024 lds
v_or_b32 v34,v56,v58
v_lshlrev_b32_sdwa v40,1,v40 dst_sel:DWORD dst_unused:UNUSED_PAD src0_sel:DWORD src1_sel:WORD_1
v_mfma_f32_16x16x32_bf16 v[52:55],a[200:203],a[68:71],v[52:55]
s_add_u32 s68,s68,s53
ds_read_u16_d16_hi v39,v39
v_cvt_pk_bf16_f32 v35,v57,v59
v_lshlrev_b32_sdwa v42,1,v42 dst_sel:DWORD dst_unused:UNUSED_PAD src0_sel:DWORD src1_sel:WORD_1
v_mfma_f32_16x16x32_bf16 v[52:55],a[196:199],a[84:87],v[52:55]
buffer_load_dwordx4 v5,s[44:47],s68 offen offset:2048 lds
v_lshlrev_b32_sdwa v41,1,v41 dst_sel:DWORD dst_unused:UNUSED_PAD src0_sel:DWORD src1_sel:WORD_1
v_mfma_f32_16x16x32_bf16 v[56:59],a[192:195],a[84:87],v[16:19]
ds_read_u16 v40,v40
v_lshlrev_b32_sdwa v43,1,v43 dst_sel:DWORD dst_unused:UNUSED_PAD src0_sel:DWORD src1_sel:WORD_1
v_mfma_f32_16x16x32_bf16 v[56:59],a[220:223],a[68:71],v[56:59]
ds_read_u16_d16_hi v42,v42
v_lshlrev_b32_sdwa v44,1,v44 dst_sel:DWORD dst_unused:UNUSED_PAD src0_sel:DWORD src1_sel:WORD_1
v_mfma_f32_16x16x32_bf16 v[56:59],a[216:219],a[52:55],v[56:59]
ds_read_u16 v41,v41
v_lshlrev_b32_sdwa v46,1,v46 dst_sel:DWORD dst_unused:UNUSED_PAD src0_sel:DWORD src1_sel:WORD_1
v_mfma_f32_16x16x32_bf16 v[136:139],a[228:231],v[60:63],v[136:139]
ds_read_u16_d16_hi v43,v43
v_lshlrev_b32_sdwa v45,1,v45 dst_sel:DWORD dst_unused:UNUSED_PAD src0_sel:DWORD src1_sel:WORD_1
v_mfma_f32_16x16x32_bf16 v[140:143],a[228:231],v[32:35],v[140:143]
ds_read_u16 v44,v44
v_lshlrev_b32_sdwa v47,1,v47 dst_sel:DWORD dst_unused:UNUSED_PAD src0_sel:DWORD src1_sel:WORD_1
v_mfma_f32_16x16x32_bf16 v[68:71],a[236:239],v[32:35],v[68:71]
ds_read_u16_d16_hi v46,v46
v_lshlrev_b32_sdwa v48,1,v48 dst_sel:DWORD dst_unused:UNUSED_PAD src0_sel:DWORD src1_sel:WORD_1
v_mfma_f32_16x16x32_bf16 v[64:67],a[236:239],v[60:63],v[64:67]
ds_read_u16 v45,v45
v_lshlrev_b32_sdwa v50,1,v50 dst_sel:DWORD dst_unused:UNUSED_PAD src0_sel:DWORD src1_sel:WORD_1
v_mfma_f32_16x16x32_bf16 v[144:147],a[244:247],v[60:63],v[144:147]
s_add_u32 m0,s62,12320
ds_read_u16_d16_hi v47,v47
v_lshlrev_b32_sdwa v49,1,v49 dst_sel:DWORD dst_unused:UNUSED_PAD src0_sel:DWORD src1_sel:WORD_1
v_mfma_f32_16x16x32_bf16 v[148:151],a[244:247],v[32:35],v[148:151]
s_add_u32 s68,s68,s59
ds_read_u16 v48,v48
v_lshlrev_b32_sdwa v51,1,v51 dst_sel:DWORD dst_unused:UNUSED_PAD src0_sel:DWORD src1_sel:WORD_1
v_mfma_f32_16x16x32_bf16 v[84:87],a[252:255],v[32:35],v[84:87]
buffer_load_dwordx4 v5,s[44:47],s68 offen offset:0 lds
v_mfma_f32_16x16x32_bf16 v[80:83],a[252:255],v[60:63],v[80:83]
s_waitcnt lgkmcnt(7)
ds_read_u16_d16_hi v50,v50
v_mfma_f32_16x16x32_bf16 v[152:155],a[232:235],v[60:63],v[152:155]
ds_read_u16 v49,v49
v_mfma_f32_16x16x32_bf16 v[156:159],a[232:235],v[32:35],v[156:159]
ds_read_u16_d16_hi v51,v51
v_mfma_f32_16x16x32_bf16 v[100:103],a[240:243],v[32:35],v[100:103]
v_mfma_f32_16x16x32_bf16 v[96:99],a[240:243],v[60:63],v[96:99]
v_mfma_f32_16x16x32_bf16 v[32:35],a[216:219],a[56:59],v[16:19]
v_lshlrev_b32_sdwa v24,1,v24 dst_sel:DWORD dst_unused:UNUSED_PAD src0_sel:DWORD src1_sel:WORD_1
v_fma_mixlo_f16 v8,v53,s99,0
v_mfma_f32_16x16x32_bf16 v[32:35],a[220:223],a[72:75],v[32:35]
ds_read_u16 v24,v24
v_lshl_add_u64 v[36:37],v[36:37],0,v[38:39]
v_mfma_f32_16x16x32_bf16 v[32:35],a[192:195],a[88:91],v[32:35]
s_waitcnt lgkmcnt(4)
v_lshl_add_u64 v[38:39],v[40:41],0,v[42:43]
v_lshlrev_b32_sdwa v26,1,v26 dst_sel:DWORD dst_unused:UNUSED_PAD src0_sel:DWORD src1_sel:WORD_1
v_mfma_f32_16x16x32_bf16 v[60:63],a[196:199],a[88:91],v[20:23]
ds_read_u16_d16_hi v26,v26
v_fma_mixhi_f16 v8,v55,s99,0
v_mfma_f32_16x16x32_bf16 v[60:63],a[200:203],a[72:75],v[60:63]
v_lshl_add_u64 v[40:41],v[44:45],0,v[46:47]
v_lshlrev_b32_sdwa v25,1,v25 dst_sel:DWORD dst_unused:UNUSED_PAD src0_sel:DWORD src1_sel:WORD_1
v_pk_fma_f16 v9,v8,v8,s54
v_mfma_f32_16x16x32_bf16 v[60:63],a[212:215],a[56:59],v[60:63]
ds_read_u16 v25,v25
v_lshlrev_b32_sdwa v27,1,v27 dst_sel:DWORD dst_unused:UNUSED_PAD src0_sel:DWORD src1_sel:WORD_1
v_mfma_f32_16x16x32_bf16 v[44:47],a[212:215],a[60:63],v[20:23]
ds_read_b128 v[20:23],v4 offset:896
v_pk_mul_f16 v9,v8,v9
v_mfma_f32_16x16x32_bf16 v[44:47],a[200:203],a[76:79],v[44:47]
s_waitcnt lgkmcnt(4)
ds_read_u16_d16_hi v27,v27
ds_read_b64_tr_b16 a[212:213],v1 offset:16416
v_lshl_add_u64 v[42:43],v[48:49],0,v[50:51]
v_lshlrev_b32_sdwa v28,1,v28 dst_sel:DWORD dst_unused:UNUSED_PAD src0_sel:DWORD src1_sel:WORD_1
v_mfma_f32_16x16x32_bf16 v[44:47],a[196:199],a[92:95],v[44:47]
ds_read_b64_tr_b16 a[214:215],v1 offset:17440
v_exp_f16 v8,v9
v_mfma_f32_16x16x32_bf16 v[48:51],a[192:195],a[92:95],v[16:19]
ds_read_b128 v[16:19],v4 offset:928
ds_read_u16 v28,v28
v_lshlrev_b32_sdwa v30,1,v30 dst_sel:DWORD dst_unused:UNUSED_PAD src0_sel:DWORD src1_sel:WORD_1
v_mfma_f32_16x16x32_bf16 v[48:51],a[220:223],a[76:79],v[48:51]
ds_read_u16_d16_hi v30,v30
ds_read_b64_tr_b16 a[200:201],v2 offset:16416
v_lshlrev_b32_sdwa v29,1,v29 dst_sel:DWORD dst_unused:UNUSED_PAD src0_sel:DWORD src1_sel:WORD_1
v_mfma_f32_16x16x32_bf16 v[48:51],a[216:219],a[60:63],v[48:51]
ds_read_u16 v29,v29
v_exp_f16_sdwa v8,v9 dst_sel:WORD_1 dst_unused:UNUSED_PRESERVE src0_sel:WORD_1
v_mfma_f32_16x16x32_bf16 v[112:115],a[228:231],v[36:39],v[112:115]
ds_read_b64_tr_b16 a[202:203],v2 offset:17440
ds_read_b64_tr_b16 a[196:197],v2 offset:14368
v_lshlrev_b32_sdwa v31,1,v31 dst_sel:DWORD dst_unused:UNUSED_PAD src0_sel:DWORD src1_sel:WORD_1
v_mfma_f32_16x16x32_bf16 v[116:119],a[228:231],v[40:43],v[116:119]
ds_read_u16_d16_hi v31,v31
v_lshlrev_b32_sdwa v56,1,v56 dst_sel:DWORD dst_unused:UNUSED_PAD src0_sel:DWORD src1_sel:WORD_1
v_mfma_f32_16x16x32_bf16 v[76:79],a[236:239],v[40:43],v[76:79]
ds_read_b64_tr_b16 a[198:199],v2 offset:15392
v_pk_fma_f16 v8,v8,s55,s55
v_mfma_f32_16x16x32_bf16 v[72:75],a[236:239],v[36:39],v[72:75]
v_rcp_f16 v9,v8
v_lshlrev_b32_sdwa v58,1,v58 dst_sel:DWORD dst_unused:UNUSED_PAD src0_sel:DWORD src1_sel:WORD_1
v_mfma_f32_16x16x32_bf16 v[120:123],a[244:247],v[36:39],v[120:123]
ds_read_u16 v56,v56
v_lshlrev_b32_sdwa v57,1,v57 dst_sel:DWORD dst_unused:UNUSED_PAD src0_sel:DWORD src1_sel:WORD_1
v_mfma_f32_16x16x32_bf16 v[124:127],a[244:247],v[40:43],v[124:127]
ds_read_u16_d16_hi v58,v58
v_lshlrev_b32_sdwa v59,1,v59 dst_sel:DWORD dst_unused:UNUSED_PAD src0_sel:DWORD src1_sel:WORD_1
v_mfma_f32_16x16x32_bf16 v[92:95],a[252:255],v[40:43],v[92:95]
ds_read_u16 v57,v57
v_lshlrev_b32_sdwa v52,1,v52 dst_sel:DWORD dst_unused:UNUSED_PAD src0_sel:DWORD src1_sel:WORD_1
v_mfma_f32_16x16x32_bf16 v[88:91],a[252:255],v[36:39],v[88:91]
ds_read_u16_d16_hi v59,v59
v_rcp_f16_sdwa v9,v8 dst_sel:WORD_1 dst_unused:UNUSED_PRESERVE src0_sel:WORD_1
v_mfma_f32_16x16x32_bf16 v[128:131],a[232:235],v[36:39],v[128:131]
ds_read_b64_tr_b16 a[192:193],v2 offset:12320
v_lshlrev_b32_sdwa v54,1,v54 dst_sel:DWORD dst_unused:UNUSED_PAD src0_sel:DWORD src1_sel:WORD_1
v_mfma_f32_16x16x32_bf16 v[132:135],a[232:235],v[40:43],v[132:135]
ds_read_b64_tr_b16 a[194:195],v2 offset:13344
v_fma_mix_f32 v53,v53,v9,0 op_sel_hi:[0,1,0]
v_mfma_f32_16x16x32_bf16 v[108:111],a[240:243],v[40:43],v[108:111]
ds_read_u16 v52,v52
v_fma_mix_f32 v55,v55,v9,0 op_sel:[0,1,0] op_sel_hi:[0,1,0]
v_mfma_f32_16x16x32_bf16 v[104:107],a[240:243],v[36:39],v[104:107]
s_waitcnt lgkmcnt(7)
ds_read_u16_d16_hi v54,v54
v_mfma_f32_16x16x32_bf16 v[40:43],a[208:211],a[0:3],v[20:23]
s_add_u32 s68,s68,s53
v_lshlrev_b32_sdwa v32,1,v32 dst_sel:DWORD dst_unused:UNUSED_PAD src0_sel:DWORD src1_sel:WORD_1
v_lshlrev_b32_sdwa v34,1,v34 dst_sel:DWORD dst_unused:UNUSED_PAD src0_sel:DWORD src1_sel:WORD_1
v_mfma_f32_16x16x32_bf16 v[40:43],a[204:207],a[16:19],v[40:43]
buffer_load_dwordx4 v5,s[44:47],s68 offen offset:1024 lds
v_lshl_add_u64 v[24:25],v[24:25],0,v[26:27]
v_mfma_f32_16x16x32_bf16 v[40:43],a[212:215],a[32:35],v[40:43]
s_add_u32 s68,s68,s53
ds_read_u16 v32,v32
v_lshl_add_u64 v[26:27],v[28:29],0,v[30:31]
v_mfma_f32_16x16x32_bf16 v[36:39],a[200:203],a[32:35],v[16:19]
buffer_load_dwordx4 v5,s[44:47],s68 offen offset:2048 lds
v_lshlrev_b32_sdwa v33,1,v33 dst_sel:DWORD dst_unused:UNUSED_PAD src0_sel:DWORD src1_sel:WORD_1
v_mfma_f32_16x16x32_bf16 v[36:39],a[196:199],a[16:19],v[36:39]
s_waitcnt lgkmcnt(3)
ds_read_u16_d16_hi v34,v34
v_lshl_add_u64 v[28:29],v[56:57],0,v[58:59]
v_mfma_f32_16x16x32_bf16 v[36:39],a[192:195],a[0:3],v[36:39]
ds_read_u16 v33,v33
v_lshlrev_b32_sdwa v35,1,v35 dst_sel:DWORD dst_unused:UNUSED_PAD src0_sel:DWORD src1_sel:WORD_1
v_mfma_f32_16x16x32_bf16 v[56:59],a[192:195],a[4:7],v[16:19]
s_waitcnt lgkmcnt(3)
ds_read_u16_d16_hi v35,v35
v_or_b32 v30,v52,v54
v_lshlrev_b32_sdwa v60,1,v60 dst_sel:DWORD dst_unused:UNUSED_PAD src0_sel:DWORD src1_sel:WORD_1
v_mfma_f32_16x16x32_bf16 v[56:59],a[196:199],a[20:23],v[56:59]
s_waitcnt vmcnt(17)
ds_read_u16 v60,v60
v_cvt_pk_bf16_f32 v31,v53,v55
v_lshlrev_b32_sdwa v62,1,v62 dst_sel:DWORD dst_unused:UNUSED_PAD src0_sel:DWORD src1_sel:WORD_1
v_mfma_f32_16x16x32_bf16 v[56:59],a[200:203],a[36:39],v[56:59]
s_barrier
ds_read_u16_d16_hi v62,v62
v_lshlrev_b32_sdwa v61,1,v61 dst_sel:DWORD dst_unused:UNUSED_PAD src0_sel:DWORD src1_sel:WORD_1
v_mfma_f32_16x16x32_bf16 v[52:55],a[212:215],a[36:39],v[20:23]
s_add_u32 m0,s62,18480
ds_read_u16 v61,v61
v_lshlrev_b32_sdwa v63,1,v63 dst_sel:DWORD dst_unused:UNUSED_PAD src0_sel:DWORD src1_sel:WORD_1
v_mfma_f32_16x16x32_bf16 v[52:55],a[204:207],a[20:23],v[52:55]
s_add_u32 s68,s68,s59
ds_read_u16_d16_hi v63,v63
v_lshlrev_b32_sdwa v48,1,v48 dst_sel:DWORD dst_unused:UNUSED_PAD src0_sel:DWORD src1_sel:WORD_1
v_mfma_f32_16x16x32_bf16 v[52:55],a[208:211],a[4:7],v[52:55]
buffer_load_dwordx4 v5,s[44:47],s68 offen offset:0 lds
v_lshlrev_b32_sdwa v50,1,v50 dst_sel:DWORD dst_unused:UNUSED_PAD src0_sel:DWORD src1_sel:WORD_1
v_mfma_f32_16x16x32_bf16 v[232:235],a[228:231],v[24:27],v[232:235]
s_add_u32 m0,s58,6160
ds_read_u16 v48,v48
v_lshlrev_b32_sdwa v49,1,v49 dst_sel:DWORD dst_unused:UNUSED_PAD src0_sel:DWORD src1_sel:WORD_1
v_mfma_f32_16x16x32_bf16 v[236:239],a[228:231],v[28:31],v[236:239]
s_add_u32 s69,s69,s59
ds_read_u16_d16_hi v50,v50
v_lshlrev_b32_sdwa v51,1,v51 dst_sel:DWORD dst_unused:UNUSED_PAD src0_sel:DWORD src1_sel:WORD_1
v_mfma_f32_16x16x32_bf16 v[164:167],a[236:239],v[28:31],v[164:167]
buffer_load_dwordx4 v5,s[44:47],s69 offen offset:0 lds
ds_read_u16 v49,v49
v_mfma_f32_16x16x32_bf16 v[160:163],a[236:239],v[24:27],v[160:163]
ds_read_u16_d16_hi v51,v51
ds_read_b64_tr_b16 a[224:225],v3 offset:18480
v_mfma_f32_16x16x32_bf16 v[240:243],a[244:247],v[24:27],v[240:243]
s_add_u32 s69,s69,s53
ds_read_b64_tr_b16 a[226:227],v3 offset:20016
v_lshlrev_b32_sdwa v44,1,v44 dst_sel:DWORD dst_unused:UNUSED_PAD src0_sel:DWORD src1_sel:WORD_1
v_mfma_f32_16x16x32_bf16 v[244:247],a[244:247],v[28:31],v[244:247]
buffer_load_dwordx4 v5,s[44:47],s69 offen offset:1024 lds
v_lshlrev_b32_sdwa v46,1,v46 dst_sel:DWORD dst_unused:UNUSED_PAD src0_sel:DWORD src1_sel:WORD_1
v_mfma_f32_16x16x32_bf16 v[180:183],a[252:255],v[28:31],v[180:183]
ds_read_u16 v44,v44
v_lshlrev_b32_sdwa v45,1,v45 dst_sel:DWORD dst_unused:UNUSED_PAD src0_sel:DWORD src1_sel:WORD_1
v_mfma_f32_16x16x32_bf16 v[176:179],a[252:255],v[24:27],v[176:179]
s_add_u32 m0,s62,18480
ds_read_u16_d16_hi v46,v46
v_lshlrev_b32_sdwa v47,1,v47 dst_sel:DWORD dst_unused:UNUSED_PAD src0_sel:DWORD src1_sel:WORD_1
v_mfma_f32_16x16x32_bf16 v[248:251],a[232:235],v[24:27],v[248:251]
s_add_u32 s68,s68,s53
ds_read_u16 v45,v45
v_mfma_f32_16x16x32_bf16 v[252:255],a[232:235],v[28:31],v[252:255]
buffer_load_dwordx4 v5,s[44:47],s68 offen offset:1024 lds
v_mfma_f32_16x16x32_bf16 v[196:199],a[240:243],v[28:31],v[196:199]
s_add_u32 m0,s58,6160
ds_read_u16_d16_hi v47,v47
v_mfma_f32_16x16x32_bf16 v[192:195],a[240:243],v[24:27],v[192:195]
s_add_u32 s69,s69,s53
ds_read_b64_tr_b16 a[248:249],v3 offset:18488
v_mfma_f32_16x16x32_bf16 v[28:31],a[208:211],a[8:11],v[20:23]
buffer_load_dwordx4 v5,s[44:47],s69 offen offset:2048 lds
v_lshlrev_b32_sdwa v40,1,v40 dst_sel:DWORD dst_unused:UNUSED_PAD src0_sel:DWORD src1_sel:WORD_1
v_mfma_f32_16x16x32_bf16 v[28:31],a[204:207],a[24:27],v[28:31]
s_waitcnt lgkmcnt(5)
ds_read_u16 v40,v40
v_lshl_add_u64 v[32:33],v[32:33],0,v[34:35]
v_fma_mixlo_f16 v8,v57,s99,0
v_mfma_f32_16x16x32_bf16 v[28:31],a[212:215],a[40:43],v[28:31]
ds_read_b64_tr_b16 a[250:251],v3 offset:20024
v_lshl_add_u64 v[34:35],v[60:61],0,v[62:63]
v_lshlrev_b32_sdwa v42,1,v42 dst_sel:DWORD dst_unused:UNUSED_PAD src0_sel:DWORD src1_sel:WORD_1
v_mfma_f32_16x16x32_bf16 v[24:27],a[200:203],a[40:43],v[16:19]
ds_read_u16_d16_hi v42,v42
v_fma_mixhi_f16 v8,v59,s99,0
v_mfma_f32_16x16x32_bf16 v[24:27],a[196:199],a[24:27],v[24:27]
ds_read_b64_tr_b16 a[220:221],v1 offset:18480
v_lshl_add_u64 v[60:61],v[48:49],0,v[50:51]
v_lshlrev_b32_sdwa v41,1,v41 dst_sel:DWORD dst_unused:UNUSED_PAD src0_sel:DWORD src1_sel:WORD_1
v_pk_fma_f16 v9,v8,v8,s54
v_mfma_f32_16x16x32_bf16 v[24:27],a[192:195],a[8:11],v[24:27]
ds_read_u16 v41,v41
v_lshlrev_b32_sdwa v43,1,v43 dst_sel:DWORD dst_unused:UNUSED_PAD src0_sel:DWORD src1_sel:WORD_1
v_mfma_f32_16x16x32_bf16 v[48:51],a[192:195],a[12:15],v[16:19]
ds_read_u16_d16_hi v43,v43
v_pk_mul_f16 v9,v8,v9
v_mfma_f32_16x16x32_bf16 v[48:51],a[196:199],a[28:31],v[48:51]
s_waitcnt lgkmcnt(7)
ds_read_b64_tr_b16 a[222:223],v1 offset:19504
v_lshl_add_u64 v[62:63],v[44:45],0,v[46:47]
v_lshlrev_b32_sdwa v36,1,v36 dst_sel:DWORD dst_unused:UNUSED_PAD src0_sel:DWORD src1_sel:WORD_1
v_mfma_f32_16x16x32_bf16 v[48:51],a[200:203],a[44:47],v[48:51]
v_exp_f16 v8,v9
v_mfma_f32_16x16x32_bf16 v[44:47],a[212:215],a[44:47],v[20:23]
ds_read_u16 v36,v36
v_lshlrev_b32_sdwa v38,1,v38 dst_sel:DWORD dst_unused:UNUSED_PAD src0_sel:DWORD src1_sel:WORD_1
v_mfma_f32_16x16x32_bf16 v[44:47],a[204:207],a[28:31],v[44:47]
ds_read_u16_d16_hi v38,v38
v_lshlrev_b32_sdwa v37,1,v37 dst_sel:DWORD dst_unused:UNUSED_PAD src0_sel:DWORD src1_sel:WORD_1
v_mfma_f32_16x16x32_bf16 v[44:47],a[208:211],a[12:15],v[44:47]
v_exp_f16_sdwa v8,v9 dst_sel:WORD_1 dst_unused:UNUSED_PRESERVE src0_sel:WORD_1
v_mfma_f32_16x16x32_bf16 v[208:211],a[228:231],v[32:35],v[208:211]
ds_read_u16 v37,v37
v_lshlrev_b32_sdwa v39,1,v39 dst_sel:DWORD dst_unused:UNUSED_PAD src0_sel:DWORD src1_sel:WORD_1
v_mfma_f32_16x16x32_bf16 v[212:215],a[228:231],v[60:63],v[212:215]
ds_read_u16_d16_hi v39,v39
v_lshlrev_b32_sdwa v52,1,v52 dst_sel:DWORD dst_unused:UNUSED_PAD src0_sel:DWORD src1_sel:WORD_1
v_mfma_f32_16x16x32_bf16 v[172:175],a[236:239],v[60:63],v[172:175]
ds_read_u16 v52,v52
v_pk_fma_f16 v8,v8,s55,s55
v_mfma_f32_16x16x32_bf16 v[168:171],a[236:239],v[32:35],v[168:171]
ds_read_b64_tr_b16 a[228:229],v3 offset:18544
v_rcp_f16_e64 v9,v8
v_lshlrev_b32_sdwa v54,1,v54 dst_sel:DWORD dst_unused:UNUSED_PAD src0_sel:DWORD src1_sel:WORD_1
v_mfma_f32_16x16x32_bf16 v[216:219],a[244:247],v[32:35],v[216:219]
ds_read_u16_d16_hi v54,v54
v_lshlrev_b32_sdwa v53,1,v53 dst_sel:DWORD dst_unused:UNUSED_PAD src0_sel:DWORD src1_sel:WORD_1
v_mfma_f32_16x16x32_bf16 v[220:223],a[244:247],v[60:63],v[220:223]
ds_read_u16 v53,v53
ds_read_b64_tr_b16 a[230:231],v3 offset:20080
v_mfma_f32_16x16x32_bf16 v[188:191],a[252:255],v[60:63],v[188:191]
ds_read_b64_tr_b16 a[236:237],v3 offset:18552
v_lshlrev_b32_sdwa v55,1,v55 dst_sel:DWORD dst_unused:UNUSED_PAD src0_sel:DWORD src1_sel:WORD_1
v_mfma_f32_16x16x32_bf16 v[184:187],a[252:255],v[32:35],v[184:187]
ds_read_u16_d16_hi v55,v55
v_rcp_f16_sdwa v9,v8 dst_sel:WORD_1 dst_unused:UNUSED_PRESERVE src0_sel:WORD_1
v_mfma_f32_16x16x32_bf16 v[224:227],a[232:235],v[32:35],v[224:227]
ds_read_b64_tr_b16 a[238:239],v3 offset:20088
ds_read_b64_tr_b16 a[244:245],v3 offset:18608
v_lshlrev_b32_sdwa v56,1,v56 dst_sel:DWORD dst_unused:UNUSED_PAD src0_sel:DWORD src1_sel:WORD_1
v_mfma_f32_16x16x32_bf16 v[228:231],a[232:235],v[60:63],v[228:231]
ds_read_u16 v56,v56
v_lshlrev_b32_sdwa v58,1,v58 dst_sel:DWORD dst_unused:UNUSED_PAD src0_sel:DWORD src1_sel:WORD_1
v_mfma_f32_16x16x32_bf16 v[204:207],a[240:243],v[60:63],v[204:207]
ds_read_u16_d16_hi v58,v58
v_fma_mix_f32 v57,v57,v9,0 op_sel_hi:[0,1,0]
v_mfma_f32_16x16x32_bf16 v[200:203],a[240:243],v[32:35],v[200:203]
ds_read_b64_tr_b16 a[246:247],v3 offset:20144
v_fma_mix_f32 v59,v59,v9,0 op_sel:[0,1,0] op_sel_hi:[0,1,0]
v_mfma_f32_16x16x32_bf16 v[60:63],a[208:211],a[48:51],v[20:23]
s_add_u32 m0,s58,12320
ds_read_b64_tr_b16 a[252:253],v3 offset:18616
v_lshlrev_b32_sdwa v28,1,v28 dst_sel:DWORD dst_unused:UNUSED_PAD src0_sel:DWORD src1_sel:WORD_1
v_mfma_f32_16x16x32_bf16 v[60:63],a[204:207],a[64:67],v[60:63]
s_waitcnt lgkmcnt(10)
ds_read_b64_tr_b16 a[254:255],v3 offset:20152
v_lshl_add_u64 v[40:41],v[40:41],0,v[42:43]
v_mfma_f32_16x16x32_bf16 v[60:63],a[212:215],a[80:83],v[60:63]
s_add_u32 s69,s69,s59
ds_read_u16 v28,v28
v_lshl_add_u64 v[42:43],v[36:37],0,v[38:39]
v_lshlrev_b32_sdwa v30,1,v30 dst_sel:DWORD dst_unused:UNUSED_PAD src0_sel:DWORD src1_sel:WORD_1
v_mfma_f32_16x16x32_bf16 v[32:35],a[200:203],a[80:83],v[16:19]
TE12_%=:
s_cbranch_execz TP10729_%=
buffer_load_dwordx4 v5,s[44:47],s69 offen offset:0 lds
v_lshlrev_b32_sdwa v29,1,v29 dst_sel:DWORD dst_unused:UNUSED_PAD src0_sel:DWORD src1_sel:WORD_1
v_mfma_f32_16x16x32_bf16 v[32:35],a[196:199],a[64:67],v[32:35]
s_waitcnt lgkmcnt(8)
ds_read_u16_d16_hi v30,v30
v_lshl_add_u64 v[36:37],v[52:53],0,v[54:55]
v_mfma_f32_16x16x32_bf16 v[32:35],a[192:195],a[48:51],v[32:35]
s_add_u32 s69,s69,s53
ds_read_u16 v29,v29
v_lshlrev_b32_sdwa v31,1,v31 dst_sel:DWORD dst_unused:UNUSED_PAD src0_sel:DWORD src1_sel:WORD_1
v_mfma_f32_16x16x32_bf16 v[52:55],a[192:195],a[52:55],v[16:19]
s_waitcnt lgkmcnt(6)
buffer_load_dwordx4 v5,s[44:47],s69 offen offset:1024 lds
v_or_b32_e64 v38,v56,v58
v_lshlrev_b32_sdwa v24,1,v24 dst_sel:DWORD dst_unused:UNUSED_PAD src0_sel:DWORD src1_sel:WORD_1
v_mfma_f32_16x16x32_bf16 v[52:55],a[196:199],a[68:71],v[52:55]
s_add_u32 m0,s62,18480
ds_read_u16_d16_hi v31,v31
v_cvt_pk_bf16_f32 v39,v57,v59
v_lshlrev_b32_sdwa v26,1,v26 dst_sel:DWORD dst_unused:UNUSED_PAD src0_sel:DWORD src1_sel:WORD_1
v_mfma_f32_16x16x32_bf16 v[52:55],a[200:203],a[84:87],v[52:55]
s_add_u32 s68,s68,s53
ds_read_u16 v24,v24
v_lshlrev_b32_sdwa v25,1,v25 dst_sel:DWORD dst_unused:UNUSED_PAD src0_sel:DWORD src1_sel:WORD_1
v_mfma_f32_16x16x32_bf16 v[56:59],a[212:215],a[84:87],v[20:23]
buffer_load_dwordx4 v5,s[44:47],s68 offen offset:2048 lds
ds_read_u16_d16_hi v26,v26
v_mfma_f32_16x16x32_bf16 v[56:59],a[204:207],a[68:71],v[56:59]
s_add_u32 m0,s58,12320
ds_read_u16 v25,v25
v_lshlrev_b32_sdwa v27,1,v27 dst_sel:DWORD dst_unused:UNUSED_PAD src0_sel:DWORD src1_sel:WORD_1
v_mfma_f32_16x16x32_bf16 v[56:59],a[208:211],a[52:55],v[56:59]
s_add_u32 s69,s69,s53
ds_read_u16_d16_hi v27,v27
v_lshlrev_b32_sdwa v44,1,v44 dst_sel:DWORD dst_unused:UNUSED_PAD src0_sel:DWORD src1_sel:WORD_1
v_mfma_f32_16x16x32_bf16 v[136:139],a[224:227],v[40:43],v[136:139]
buffer_load_dwordx4 v5,s[44:47],s69 offen offset:2048 lds
v_lshlrev_b32_sdwa v46,1,v46 dst_sel:DWORD dst_unused:UNUSED_PAD src0_sel:DWORD src1_sel:WORD_1
v_mfma_f32_16x16x32_bf16 v[140:143],a[224:227],v[36:39],v[140:143]
s_add_u32 m0,s62,24640
ds_read_u16 v44,v44
v_lshlrev_b32_sdwa v45,1,v45 dst_sel:DWORD dst_unused:UNUSED_PAD src0_sel:DWORD src1_sel:WORD_1
v_mfma_f32_16x16x32_bf16 v[68:71],a[248:251],v[36:39],v[68:71]
s_add_u32 s68,s68,s59
ds_read_u16_d16_hi v46,v46
v_lshlrev_b32_sdwa v47,1,v47 dst_sel:DWORD dst_unused:UNUSED_PAD src0_sel:DWORD src1_sel:WORD_1
v_mfma_f32_16x16x32_bf16 v[64:67],a[248:251],v[40:43],v[64:67]
buffer_load_dwordx4 v5,s[44:47],s68 offen offset:0 lds
ds_read_u16 v45,v45
v_mfma_f32_16x16x32_bf16 v[144:147],a[228:231],v[40:43],v[144:147]
s_add_u32 s68,s68,s53
ds_read_u16_d16_hi v47,v47
v_lshlrev_b32_sdwa v48,1,v48 dst_sel:DWORD dst_unused:UNUSED_PAD src0_sel:DWORD src1_sel:WORD_1
v_mfma_f32_16x16x32_bf16 v[148:151],a[228:231],v[36:39],v[148:151]
buffer_load_dwordx4 v5,s[44:47],s68 offen offset:1024 lds
v_lshlrev_b32_sdwa v50,1,v50 dst_sel:DWORD dst_unused:UNUSED_PAD src0_sel:DWORD src1_sel:WORD_1
v_mfma_f32_16x16x32_bf16 v[84:87],a[236:239],v[36:39],v[84:87]
ds_read_u16 v48,v48
v_lshlrev_b32_sdwa v49,1,v49 dst_sel:DWORD dst_unused:UNUSED_PAD src0_sel:DWORD src1_sel:WORD_1
v_mfma_f32_16x16x32_bf16 v[80:83],a[236:239],v[40:43],v[80:83]
s_waitcnt lgkmcnt(6)
ds_read_u16_d16_hi v50,v50
v_lshlrev_b32_sdwa v51,1,v51 dst_sel:DWORD dst_unused:UNUSED_PAD src0_sel:DWORD src1_sel:WORD_1
v_mfma_f32_16x16x32_bf16 v[152:155],a[244:247],v[40:43],v[152:155]
ds_read_u16 v49,v49
v_mfma_f32_16x16x32_bf16 v[156:159],a[244:247],v[36:39],v[156:159]
s_add_u32 s68,s68,s53
ds_read_u16_d16_hi v51,v51
v_mfma_f32_16x16x32_bf16 v[100:103],a[252:255],v[36:39],v[100:103]
buffer_load_dwordx4 v5,s[44:47],s68 offen offset:2048 lds
v_mfma_f32_16x16x32_bf16 v[96:99],a[252:255],v[40:43],v[96:99]
ds_read_b64_tr_b16 a[216:217],v1 offset:20528
v_mfma_f32_16x16x32_bf16 v[36:39],a[208:211],a[56:59],v[20:23]
ds_read_b64_tr_b16 a[218:219],v1 offset:21552
v_lshlrev_b32_sdwa v60,1,v60 dst_sel:DWORD dst_unused:UNUSED_PAD src0_sel:DWORD src1_sel:WORD_1
v_mfma_f32_16x16x32_bf16 v[36:39],a[204:207],a[72:75],v[36:39]
ds_read_u16 v60,v60
v_lshl_add_u64 v[28:29],v[28:29],0,v[30:31]
v_fma_mixlo_f16 v8,v53,s99,0
v_mfma_f32_16x16x32_bf16 v[36:39],a[212:215],a[88:91],v[36:39]
s_waitcnt lgkmcnt(7)
v_lshl_add_u64 v[30:31],v[24:25],0,v[26:27]
v_lshlrev_b32_sdwa v62,1,v62 dst_sel:DWORD dst_unused:UNUSED_PAD src0_sel:DWORD src1_sel:WORD_1
v_mfma_f32_16x16x32_bf16 v[40:43],a[200:203],a[88:91],v[16:19]
ds_read_u16_d16_hi v62,v62
v_fma_mixhi_f16 v8,v55,s99,0
v_mfma_f32_16x16x32_bf16 v[40:43],a[196:199],a[72:75],v[40:43]
v_lshl_add_u64 v[24:25],v[44:45],0,v[46:47]
v_lshlrev_b32_sdwa v61,1,v61 dst_sel:DWORD dst_unused:UNUSED_PAD src0_sel:DWORD src1_sel:WORD_1
v_pk_fma_f16 v9,v8,v8,s54
v_mfma_f32_16x16x32_bf16 v[40:43],a[192:195],a[56:59],v[40:43]
ds_read_u16 v61,v61
v_lshlrev_b32_sdwa v63,1,v63 dst_sel:DWORD dst_unused:UNUSED_PAD src0_sel:DWORD src1_sel:WORD_1
v_mfma_f32_16x16x32_bf16 v[44:47],a[192:195],a[60:63],v[16:19]
ds_read_b128 v[16:19],v4 offset:1024
v_pk_mul_f16 v9,v8,v9
v_mfma_f32_16x16x32_bf16 v[44:47],a[196:199],a[76:79],v[44:47]
s_waitcnt lgkmcnt(6)
ds_read_u16_d16_hi v63,v63
ds_read_b64_tr_b16 a[192:193],v1 offset:22576
v_lshl_add_u64 v[26:27],v[48:49],0,v[50:51]
v_lshlrev_b32_sdwa v32,1,v32 dst_sel:DWORD dst_unused:UNUSED_PAD src0_sel:DWORD src1_sel:WORD_1
v_mfma_f32_16x16x32_bf16 v[44:47],a[200:203],a[92:95],v[44:47]
ds_read_b64_tr_b16 a[194:195],v1 offset:23600
v_exp_f16_e64 v8,v9
v_mfma_f32_16x16x32_bf16 v[48:51],a[212:215],a[92:95],v[20:23]
ds_read_b128 v[20:23],v4 offset:1056
ds_read_u16 v32,v32
v_lshlrev_b32_sdwa v34,1,v34 dst_sel:DWORD dst_unused:UNUSED_PAD src0_sel:DWORD src1_sel:WORD_1
v_mfma_f32_16x16x32_bf16 v[48:51],a[204:207],a[76:79],v[48:51]
ds_read_u16_d16_hi v34,v34
ds_read_b64_tr_b16 a[196:197],v2 offset:22576
v_lshlrev_b32_sdwa v33,1,v33 dst_sel:DWORD dst_unused:UNUSED_PAD src0_sel:DWORD src1_sel:WORD_1
v_mfma_f32_16x16x32_bf16 v[48:51],a[208:211],a[60:63],v[48:51]
ds_read_u16 v33,v33
v_exp_f16_sdwa v8,v9 dst_sel:WORD_1 dst_unused:UNUSED_PRESERVE src0_sel:WORD_1
v_mfma_f32_16x16x32_bf16 v[112:115],a[224:227],v[28:31],v[112:115]
ds_read_b64_tr_b16 a[198:199],v2 offset:23600
ds_read_b64_tr_b16 a[200:201],v2 offset:20528
v_lshlrev_b32_sdwa v35,1,v35 dst_sel:DWORD dst_unused:UNUSED_PAD src0_sel:DWORD src1_sel:WORD_1
v_mfma_f32_16x16x32_bf16 v[116:119],a[224:227],v[24:27],v[116:119]
ds_read_u16_d16_hi v35,v35
v_lshlrev_b32_sdwa v56,1,v56 dst_sel:DWORD dst_unused:UNUSED_PAD src0_sel:DWORD src1_sel:WORD_1
v_mfma_f32_16x16x32_bf16 v[76:79],a[248:251],v[24:27],v[76:79]
ds_read_b64_tr_b16 a[202:203],v2 offset:21552
v_pk_fma_f16 v8,v8,s55,s55
v_mfma_f32_16x16x32_bf16 v[72:75],a[248:251],v[28:31],v[72:75]
v_rcp_f16 v9,v8
v_lshlrev_b32_sdwa v58,1,v58 dst_sel:DWORD dst_unused:UNUSED_PAD src0_sel:DWORD src1_sel:WORD_1
v_mfma_f32_16x16x32_bf16 v[120:123],a[228:231],v[28:31],v[120:123]
ds_read_u16 v56,v56
v_lshlrev_b32_sdwa v57,1,v57 dst_sel:DWORD dst_unused:UNUSED_PAD src0_sel:DWORD src1_sel:WORD_1
v_mfma_f32_16x16x32_bf16 v[124:127],a[228:231],v[24:27],v[124:127]
ds_read_u16_d16_hi v58,v58
v_lshlrev_b32_sdwa v59,1,v59 dst_sel:DWORD dst_unused:UNUSED_PAD src0_sel:DWORD src1_sel:WORD_1
v_mfma_f32_16x16x32_bf16 v[92:95],a[236:239],v[24:27],v[92:95]
ds_read_u16 v57,v57
v_lshlrev_b32_sdwa v52,1,v52 dst_sel:DWORD dst_unused:UNUSED_PAD src0_sel:DWORD src1_sel:WORD_1
v_mfma_f32_16x16x32_bf16 v[88:91],a[236:239],v[28:31],v[88:91]
ds_read_u16_d16_hi v59,v59
v_rcp_f16_sdwa v9,v8 dst_sel:WORD_1 dst_unused:UNUSED_PRESERVE src0_sel:WORD_1
v_mfma_f32_16x16x32_bf16 v[128:131],a[244:247],v[28:31],v[128:131]
ds_read_b64_tr_b16 a[212:213],v2 offset:18480
v_lshlrev_b32_sdwa v54,1,v54 dst_sel:DWORD dst_unused:UNUSED_PAD src0_sel:DWORD src1_sel:WORD_1
v_mfma_f32_16x16x32_bf16 v[132:135],a[244:247],v[24:27],v[132:135]
ds_read_b64_tr_b16 a[214:215],v2 offset:19504
v_fma_mix_f32 v53,v53,v9,0 op_sel_hi:[0,1,0]
v_mfma_f32_16x16x32_bf16 v[108:111],a[252:255],v[24:27],v[108:111]
ds_read_u16 v52,v52
v_fma_mix_f32 v55,v55,v9,0 op_sel:[0,1,0] op_sel_hi:[0,1,0]
v_mfma_f32_16x16x32_bf16 v[104:107],a[252:255],v[28:31],v[104:107]
s_waitcnt lgkmcnt(7)
ds_read_u16_d16_hi v54,v54
v_mfma_f32_16x16x32_bf16 v[24:27],a[220:223],a[0:3],v[16:19]
v_lshlrev_b32_sdwa v36,1,v36 dst_sel:DWORD dst_unused:UNUSED_PAD src0_sel:DWORD src1_sel:WORD_1
v_lshlrev_b32_sdwa v38,1,v38 dst_sel:DWORD dst_unused:UNUSED_PAD src0_sel:DWORD src1_sel:WORD_1
v_mfma_f32_16x16x32_bf16 v[24:27],a[216:219],a[16:19],v[24:27]
ds_read_u16 v36,v36
v_lshl_add_u64 v[60:61],v[60:61],0,v[62:63]
v_mfma_f32_16x16x32_bf16 v[24:27],a[192:195],a[32:35],v[24:27]
ds_read_u16_d16_hi v38,v38
v_lshl_add_u64 v[62:63],v[32:33],0,v[34:35]
v_mfma_f32_16x16x32_bf16 v[28:31],a[196:199],a[32:35],v[20:23]
v_lshlrev_b32_sdwa v37,1,v37 dst_sel:DWORD dst_unused:UNUSED_PAD src0_sel:DWORD src1_sel:WORD_1
v_lshlrev_b32_sdwa v39,1,v39 dst_sel:DWORD dst_unused:UNUSED_PAD src0_sel:DWORD src1_sel:WORD_1
v_mfma_f32_16x16x32_bf16 v[28:31],a[200:203],a[16:19],v[28:31]
s_waitcnt lgkmcnt(4)
ds_read_u16 v37,v37
v_lshl_add_u64 v[32:33],v[56:57],0,v[58:59]
v_mfma_f32_16x16x32_bf16 v[28:31],a[212:215],a[0:3],v[28:31]
ds_read_u16_d16_hi v39,v39
v_lshlrev_b32_sdwa v40,1,v40 dst_sel:DWORD dst_unused:UNUSED_PAD src0_sel:DWORD src1_sel:WORD_1
v_mfma_f32_16x16x32_bf16 v[56:59],a[212:215],a[4:7],v[20:23]
s_waitcnt lgkmcnt(4)
ds_read_u16 v40,v40
v_or_b32 v34,v52,v54
v_mfma_f32_16x16x32_bf16 v[56:59],a[200:203],a[20:23],v[56:59]
v_cvt_pk_bf16_f32 v35,v53,v55
v_lshlrev_b32_sdwa v42,1,v42 dst_sel:DWORD dst_unused:UNUSED_PAD src0_sel:DWORD src1_sel:WORD_1
v_mfma_f32_16x16x32_bf16 v[56:59],a[196:199],a[36:39],v[56:59]
ds_read_u16_d16_hi v42,v42
v_lshlrev_b32_sdwa v41,1,v41 dst_sel:DWORD dst_unused:UNUSED_PAD src0_sel:DWORD src1_sel:WORD_1
v_mfma_f32_16x16x32_bf16 v[52:55],a[192:195],a[36:39],v[16:19]
ds_read_u16 v41,v41
v_lshlrev_b32_sdwa v43,1,v43 dst_sel:DWORD dst_unused:UNUSED_PAD src0_sel:DWORD src1_sel:WORD_1
v_mfma_f32_16x16x32_bf16 v[52:55],a[216:219],a[20:23],v[52:55]
ds_read_u16_d16_hi v43,v43
v_lshlrev_b32_sdwa v48,1,v48 dst_sel:DWORD dst_unused:UNUSED_PAD src0_sel:DWORD src1_sel:WORD_1
v_mfma_f32_16x16x32_bf16 v[52:55],a[220:223],a[4:7],v[52:55]
ds_read_u16 v48,v48
v_lshlrev_b32_sdwa v50,1,v50 dst_sel:DWORD dst_unused:UNUSED_PAD src0_sel:DWORD src1_sel:WORD_1
v_mfma_f32_16x16x32_bf16 v[232:235],a[224:227],v[60:63],v[232:235]
ds_read_u16_d16_hi v50,v50
v_lshlrev_b32_sdwa v49,1,v49 dst_sel:DWORD dst_unused:UNUSED_PAD src0_sel:DWORD src1_sel:WORD_1
v_mfma_f32_16x16x32_bf16 v[236:239],a[224:227],v[32:35],v[236:239]
ds_read_u16 v49,v49
v_lshlrev_b32_sdwa v51,1,v51 dst_sel:DWORD dst_unused:UNUSED_PAD src0_sel:DWORD src1_sel:WORD_1
v_mfma_f32_16x16x32_bf16 v[164:167],a[248:251],v[32:35],v[164:167]
ds_read_u16_d16_hi v51,v51
ds_read_b64_tr_b16 a[232:233],v3 offset:0
v_mfma_f32_16x16x32_bf16 v[160:163],a[248:251],v[60:63],v[160:163]
ds_read_b64_tr_b16 a[234:235],v3 offset:1536
v_lshlrev_b32_sdwa v44,1,v44 dst_sel:DWORD dst_unused:UNUSED_PAD src0_sel:DWORD src1_sel:WORD_1
v_mfma_f32_16x16x32_bf16 v[240:243],a[228:231],v[60:63],v[240:243]
ds_read_u16 v44,v44
v_lshlrev_b32_sdwa v46,1,v46 dst_sel:DWORD dst_unused:UNUSED_PAD src0_sel:DWORD src1_sel:WORD_1
v_mfma_f32_16x16x32_bf16 v[244:247],a[228:231],v[32:35],v[244:247]
ds_read_u16_d16_hi v46,v46
v_lshlrev_b32_sdwa v45,1,v45 dst_sel:DWORD dst_unused:UNUSED_PAD src0_sel:DWORD src1_sel:WORD_1
v_mfma_f32_16x16x32_bf16 v[180:183],a[236:239],v[32:35],v[180:183]
ds_read_u16 v45,v45
v_lshlrev_b32_sdwa v47,1,v47 dst_sel:DWORD dst_unused:UNUSED_PAD src0_sel:DWORD src1_sel:WORD_1
v_mfma_f32_16x16x32_bf16 v[176:179],a[236:239],v[60:63],v[176:179]
ds_read_u16_d16_hi v47,v47
v_mfma_f32_16x16x32_bf16 v[248:251],a[244:247],v[60:63],v[248:251]
ds_read_b64_tr_b16 a[240:241],v3 offset:8
v_mfma_f32_16x16x32_bf16 v[252:255],a[244:247],v[32:35],v[252:255]
ds_read_b64_tr_b16 a[242:243],v3 offset:1544
v_mfma_f32_16x16x32_bf16 v[196:199],a[252:255],v[32:35],v[196:199]
v_mfma_f32_16x16x32_bf16 v[192:195],a[252:255],v[60:63],v[192:195]
v_mfma_f32_16x16x32_bf16 v[32:35],a[220:223],a[8:11],v[16:19]
ds_read_b64_tr_b16 a[204:205],v1 offset:0
v_lshlrev_b32_sdwa v24,1,v24 dst_sel:DWORD dst_unused:UNUSED_PAD src0_sel:DWORD src1_sel:WORD_1
v_mfma_f32_16x16x32_bf16 v[32:35],a[216:219],a[24:27],v[32:35]
s_waitcnt lgkmcnt(6)
ds_read_u16 v24,v24
v_lshl_add_u64 v[36:37],v[36:37],0,v[38:39]
v_fma_mixlo_f16 v8,v57,s99,0
v_mfma_f32_16x16x32_bf16 v[32:35],a[192:195],a[40:43],v[32:35]
ds_read_b64_tr_b16 a[206:207],v1 offset:1024
v_lshl_add_u64 v[38:39],v[40:41],0,v[42:43]
v_lshlrev_b32_sdwa v26,1,v26 dst_sel:DWORD dst_unused:UNUSED_PAD src0_sel:DWORD src1_sel:WORD_1
v_mfma_f32_16x16x32_bf16 v[60:63],a[196:199],a[40:43],v[20:23]
ds_read_u16_d16_hi v26,v26
v_fma_mixhi_f16 v8,v59,s99,0
v_mfma_f32_16x16x32_bf16 v[60:63],a[200:203],a[24:27],v[60:63]
ds_read_b64_tr_b16 a[208:209],v1 offset:2048
v_lshl_add_u64 v[40:41],v[48:49],0,v[50:51]
v_lshlrev_b32_sdwa v25,1,v25 dst_sel:DWORD dst_unused:UNUSED_PAD src0_sel:DWORD src1_sel:WORD_1
v_pk_fma_f16 v9,v8,v8,s54
v_mfma_f32_16x16x32_bf16 v[60:63],a[212:215],a[8:11],v[60:63]
ds_read_u16 v25,v25
v_lshlrev_b32_sdwa v27,1,v27 dst_sel:DWORD dst_unused:UNUSED_PAD src0_sel:DWORD src1_sel:WORD_1
v_mfma_f32_16x16x32_bf16 v[48:51],a[212:215],a[12:15],v[20:23]
ds_read_u16_d16_hi v27,v27
v_pk_mul_f16 v9,v8,v9
v_mfma_f32_16x16x32_bf16 v[48:51],a[200:203],a[28:31],v[48:51]
s_waitcnt lgkmcnt(7)
ds_read_b64_tr_b16 a[210:211],v1 offset:3072
v_lshl_add_u64 v[42:43],v[44:45],0,v[46:47]
v_lshlrev_b32_sdwa v28,1,v28 dst_sel:DWORD dst_unused:UNUSED_PAD src0_sel:DWORD src1_sel:WORD_1
v_mfma_f32_16x16x32_bf16 v[48:51],a[196:199],a[44:47],v[48:51]
v_exp_f16 v8,v9
v_mfma_f32_16x16x32_bf16 v[44:47],a[192:195],a[44:47],v[16:19]
ds_read_u16 v28,v28
v_lshlrev_b32_sdwa v30,1,v30 dst_sel:DWORD dst_unused:UNUSED_PAD src0_sel:DWORD src1_sel:WORD_1
v_mfma_f32_16x16x32_bf16 v[44:47],a[216:219],a[28:31],v[44:47]
ds_read_u16_d16_hi v30,v30
v_lshlrev_b32_sdwa v29,1,v29 dst_sel:DWORD dst_unused:UNUSED_PAD src0_sel:DWORD src1_sel:WORD_1
v_mfma_f32_16x16x32_bf16 v[44:47],a[220:223],a[12:15],v[44:47]
v_exp_f16_sdwa v8,v9 dst_sel:WORD_1 dst_unused:UNUSED_PRESERVE src0_sel:WORD_1
v_mfma_f32_16x16x32_bf16 v[208:211],a[224:227],v[36:39],v[208:211]
ds_read_u16 v29,v29
v_lshlrev_b32_sdwa v31,1,v31 dst_sel:DWORD dst_unused:UNUSED_PAD src0_sel:DWORD src1_sel:WORD_1
v_mfma_f32_16x16x32_bf16 v[212:215],a[224:227],v[40:43],v[212:215]
ds_read_u16_d16_hi v31,v31
v_lshlrev_b32_sdwa v52,1,v52 dst_sel:DWORD dst_unused:UNUSED_PAD src0_sel:DWORD src1_sel:WORD_1
v_mfma_f32_16x16x32_bf16 v[172:175],a[248:251],v[40:43],v[172:175]
ds_read_u16 v52,v52
v_pk_fma_f16 v8,v8,s55,s55
v_mfma_f32_16x16x32_bf16 v[168:171],a[248:251],v[36:39],v[168:171]
ds_read_b64_tr_b16 a[224:225],v3 offset:64
v_rcp_f16 v9,v8
v_lshlrev_b32_sdwa v54,1,v54 dst_sel:DWORD dst_unused:UNUSED_PAD src0_sel:DWORD src1_sel:WORD_1
v_mfma_f32_16x16x32_bf16 v[216:219],a[228:231],v[36:39],v[216:219]
ds_read_u16_d16_hi v54,v54
v_lshlrev_b32_sdwa v53,1,v53 dst_sel:DWORD dst_unused:UNUSED_PAD src0_sel:DWORD src1_sel:WORD_1
v_mfma_f32_16x16x32_bf16 v[220:223],a[228:231],v[40:43],v[220:223]
ds_read_u16 v53,v53
ds_read_b64_tr_b16 a[226:227],v3 offset:1600
v_mfma_f32_16x16x32_bf16 v[188:191],a[236:239],v[40:43],v[188:191]
ds_read_b64_tr_b16 a[248:249],v3 offset:72
v_lshlrev_b32_sdwa v55,1,v55 dst_sel:DWORD dst_unused:UNUSED_PAD src0_sel:DWORD src1_sel:WORD_1
v_mfma_f32_16x16x32_bf16 v[184:187],a[236:239],v[36:39],v[184:187]
ds_read_u16_d16_hi v55,v55
v_rcp_f16_sdwa v9,v8 dst_sel:WORD_1 dst_unused:UNUSED_PRESERVE src0_sel:WORD_1
v_mfma_f32_16x16x32_bf16 v[224:227],a[244:247],v[36:39],v[224:227]
ds_read_b64_tr_b16 a[250:251],v3 offset:1608
ds_read_b64_tr_b16 a[228:229],v3 offset:128
v_lshlrev_b32_sdwa v56,1,v56 dst_sel:DWORD dst_unused:UNUSED_PAD src0_sel:DWORD src1_sel:WORD_1
v_mfma_f32_16x16x32_bf16 v[228:231],a[244:247],v[40:43],v[228:231]
ds_read_u16 v56,v56
v_lshlrev_b32_sdwa v58,1,v58 dst_sel:DWORD dst_unused:UNUSED_PAD src0_sel:DWORD src1_sel:WORD_1
v_mfma_f32_16x16x32_bf16 v[204:207],a[252:255],v[40:43],v[204:207]
ds_read_u16_d16_hi v58,v58
v_fma_mix_f32 v57,v57,v9,0 op_sel_hi:[0,1,0]
v_mfma_f32_16x16x32_bf16 v[200:203],a[252:255],v[36:39],v[200:203]
ds_read_b64_tr_b16 a[230:231],v3 offset:1664
v_fma_mix_f32 v59,v59,v9,0 op_sel:[0,1,0] op_sel_hi:[0,1,0]
v_mbcnt_lo_u32_b32 v8,-1,0
N_qs20_%=:
s_add_u32 s64,s16,N_qs20_%=-N_pf_%=+0
v_and_b32 v8,3,v8
s_mov_b32 m0,12288
v_lshl_add_u32 v8,v8,7,s64
v_min_u32 v8,N_mainend_%=-N_pf_%=+2048,v8
global_load_lds_dword v8,s[14:15]
v_mfma_f32_16x16x32_bf16 v[40:43],a[220:223],a[48:51],v[16:19]
ds_read_b64_tr_b16 a[236:237],v3 offset:136
v_lshlrev_b32_sdwa v32,1,v32 dst_sel:DWORD dst_unused:UNUSED_PAD src0_sel:DWORD src1_sel:WORD_1
v_mfma_f32_16x16x32_bf16 v[40:43],a[216:219],a[64:67],v[40:43]
s_waitcnt lgkmcnt(10)
ds_read_b64_tr_b16 a[238:239],v3 offset:1672
v_lshl_add_u64 v[24:25],v[24:25],0,v[26:27]
v_mfma_f32_16x16x32_bf16 v[40:43],a[192:195],a[80:83],v[40:43]
ds_read_u16 v32,v32
v_lshl_add_u64 v[26:27],v[28:29],0,v[30:31]
v_lshlrev_b32_sdwa v34,1,v34 dst_sel:DWORD dst_unused:UNUSED_PAD src0_sel:DWORD src1_sel:WORD_1
v_mfma_f32_16x16x32_bf16 v[36:39],a[196:199],a[80:83],v[20:23]
ds_read_u16_d16_hi v34,v34
v_lshlrev_b32_sdwa v33,1,v33 dst_sel:DWORD dst_unused:UNUSED_PAD src0_sel:DWORD src1_sel:WORD_1
v_mfma_f32_16x16x32_bf16 v[36:39],a[200:203],a[64:67],v[36:39]
s_waitcnt lgkmcnt(9)
ds_read_u16 v33,v33
v_lshl_add_u64 v[28:29],v[52:53],0,v[54:55]
v_mfma_f32_16x16x32_bf16 v[36:39],a[212:215],a[48:51],v[36:39]
v_lshlrev_b32_sdwa v35,1,v35 dst_sel:DWORD dst_unused:UNUSED_PAD src0_sel:DWORD src1_sel:WORD_1
v_lshlrev_b32_sdwa v60,1,v60 dst_sel:DWORD dst_unused:UNUSED_PAD src0_sel:DWORD src1_sel:WORD_1
v_mfma_f32_16x16x32_bf16 v[52:55],a[212:215],a[52:55],v[20:23]
s_waitcnt lgkmcnt(6)
ds_read_u16_d16_hi v35,v35
v_or_b32 v30,v56,v58
v_mfma_f32_16x16x32_bf16 v[52:55],a[200:203],a[68:71],v[52:55]
ds_read_u16 v60,v60
v_cvt_pk_bf16_f32 v31,v57,v59
v_lshlrev_b32_sdwa v62,1,v62 dst_sel:DWORD dst_unused:UNUSED_PAD src0_sel:DWORD src1_sel:WORD_1
v_mfma_f32_16x16x32_bf16 v[52:55],a[196:199],a[84:87],v[52:55]
ds_read_u16_d16_hi v62,v62
v_lshlrev_b32_sdwa v61,1,v61 dst_sel:DWORD dst_unused:UNUSED_PAD src0_sel:DWORD src1_sel:WORD_1
v_mfma_f32_16x16x32_bf16 v[56:59],a[192:195],a[84:87],v[16:19]
ds_read_u16 v61,v61
v_lshlrev_b32_sdwa v63,1,v63 dst_sel:DWORD dst_unused:UNUSED_PAD src0_sel:DWORD src1_sel:WORD_1
v_mfma_f32_16x16x32_bf16 v[56:59],a[216:219],a[68:71],v[56:59]
ds_read_u16_d16_hi v63,v63
v_lshlrev_b32_sdwa v44,1,v44 dst_sel:DWORD dst_unused:UNUSED_PAD src0_sel:DWORD src1_sel:WORD_1
v_mfma_f32_16x16x32_bf16 v[56:59],a[220:223],a[52:55],v[56:59]
ds_read_u16 v44,v44
v_lshlrev_b32_sdwa v46,1,v46 dst_sel:DWORD dst_unused:UNUSED_PAD src0_sel:DWORD src1_sel:WORD_1
v_mfma_f32_16x16x32_bf16 v[136:139],a[232:235],v[24:27],v[136:139]
ds_read_u16_d16_hi v46,v46
v_lshlrev_b32_sdwa v45,1,v45 dst_sel:DWORD dst_unused:UNUSED_PAD src0_sel:DWORD src1_sel:WORD_1
v_mfma_f32_16x16x32_bf16 v[140:143],a[232:235],v[28:31],v[140:143]
ds_read_u16 v45,v45
v_lshlrev_b32_sdwa v47,1,v47 dst_sel:DWORD dst_unused:UNUSED_PAD src0_sel:DWORD src1_sel:WORD_1
v_mfma_f32_16x16x32_bf16 v[68:71],a[240:243],v[28:31],v[68:71]
ds_read_u16_d16_hi v47,v47
v_lshlrev_b32_sdwa v48,1,v48 dst_sel:DWORD dst_unused:UNUSED_PAD src0_sel:DWORD src1_sel:WORD_1
v_mfma_f32_16x16x32_bf16 v[64:67],a[240:243],v[24:27],v[64:67]
ds_read_u16 v48,v48
v_lshlrev_b32_sdwa v50,1,v50 dst_sel:DWORD dst_unused:UNUSED_PAD src0_sel:DWORD src1_sel:WORD_1
v_mfma_f32_16x16x32_bf16 v[144:147],a[224:227],v[24:27],v[144:147]
ds_read_u16_d16_hi v50,v50
v_lshlrev_b32_sdwa v49,1,v49 dst_sel:DWORD dst_unused:UNUSED_PAD src0_sel:DWORD src1_sel:WORD_1
v_mfma_f32_16x16x32_bf16 v[148:151],a[224:227],v[28:31],v[148:151]
ds_read_u16 v49,v49
v_lshlrev_b32_sdwa v51,1,v51 dst_sel:DWORD dst_unused:UNUSED_PAD src0_sel:DWORD src1_sel:WORD_1
v_mfma_f32_16x16x32_bf16 v[84:87],a[248:251],v[28:31],v[84:87]
ds_read_u16_d16_hi v51,v51
v_mfma_f32_16x16x32_bf16 v[80:83],a[248:251],v[24:27],v[80:83]
s_waitcnt lgkmcnt(8)
v_mfma_f32_16x16x32_bf16 v[152:155],a[228:231],v[24:27],v[152:155]
v_mfma_f32_16x16x32_bf16 v[156:159],a[228:231],v[28:31],v[156:159]
v_mfma_f32_16x16x32_bf16 v[100:103],a[236:239],v[28:31],v[100:103]
v_mfma_f32_16x16x32_bf16 v[96:99],a[236:239],v[24:27],v[96:99]
v_mfma_f32_16x16x32_bf16 v[28:31],a[220:223],a[56:59],v[16:19]
v_lshlrev_b32_sdwa v40,1,v40 dst_sel:DWORD dst_unused:UNUSED_PAD src0_sel:DWORD src1_sel:WORD_1
v_fma_mixlo_f16 v8,v53,s99,0
v_mfma_f32_16x16x32_bf16 v[28:31],a[216:219],a[72:75],v[28:31]
ds_read_u16 v40,v40
v_lshl_add_u64 v[32:33],v[32:33],0,v[34:35]
v_mfma_f32_16x16x32_bf16 v[28:31],a[192:195],a[88:91],v[28:31]
v_lshl_add_u64 v[34:35],v[60:61],0,v[62:63]
v_lshlrev_b32_sdwa v42,1,v42 dst_sel:DWORD dst_unused:UNUSED_PAD src0_sel:DWORD src1_sel:WORD_1
v_mfma_f32_16x16x32_bf16 v[24:27],a[196:199],a[88:91],v[20:23]
ds_read_u16_d16_hi v42,v42
v_fma_mixhi_f16 v8,v55,s99,0
v_mfma_f32_16x16x32_bf16 v[24:27],a[200:203],a[72:75],v[24:27]
s_waitcnt lgkmcnt(2)
v_lshl_add_u64 v[60:61],v[44:45],0,v[46:47]
v_lshlrev_b32_sdwa v41,1,v41 dst_sel:DWORD dst_unused:UNUSED_PAD src0_sel:DWORD src1_sel:WORD_1
v_pk_fma_f16 v9,v8,v8,s54
v_mfma_f32_16x16x32_bf16 v[24:27],a[212:215],a[56:59],v[24:27]
ds_read_u16 v41,v41
v_lshlrev_b32_sdwa v43,1,v43 dst_sel:DWORD dst_unused:UNUSED_PAD src0_sel:DWORD src1_sel:WORD_1
v_mfma_f32_16x16x32_bf16 v[44:47],a[212:215],a[60:63],v[20:23]
ds_read_b128 v[20:23],v4 offset:1152
v_pk_mul_f16 v9,v8,v9
v_mfma_f32_16x16x32_bf16 v[44:47],a[200:203],a[76:79],v[44:47]
ds_read_u16_d16_hi v43,v43
ds_read_b64_tr_b16 a[212:213],v1 offset:4096
v_lshl_add_u64 v[62:63],v[48:49],0,v[50:51]
v_lshlrev_b32_sdwa v36,1,v36 dst_sel:DWORD dst_unused:UNUSED_PAD src0_sel:DWORD src1_sel:WORD_1
v_mfma_f32_16x16x32_bf16 v[44:47],a[196:199],a[92:95],v[44:47]
ds_read_b64_tr_b16 a[214:215],v1 offset:5120
v_exp_f16 v8,v9
v_mfma_f32_16x16x32_bf16 v[48:51],a[192:195],a[92:95],v[16:19]
ds_read_b128 v[16:19],v4 offset:1184
ds_read_u16 v36,v36
v_lshlrev_b32_sdwa v38,1,v38 dst_sel:DWORD dst_unused:UNUSED_PAD src0_sel:DWORD src1_sel:WORD_1
v_mfma_f32_16x16x32_bf16 v[48:51],a[216:219],a[76:79],v[48:51]
ds_read_u16_d16_hi v38,v38
ds_read_b64_tr_b16 a[200:201],v2 offset:4096
v_lshlrev_b32_sdwa v37,1,v37 dst_sel:DWORD dst_unused:UNUSED_PAD src0_sel:DWORD src1_sel:WORD_1
v_mfma_f32_16x16x32_bf16 v[48:51],a[220:223],a[60:63],v[48:51]
ds_read_u16 v37,v37
v_exp_f16_sdwa v8,v9 dst_sel:WORD_1 dst_unused:UNUSED_PRESERVE src0_sel:WORD_1
v_mfma_f32_16x16x32_bf16 v[112:115],a[232:235],v[32:35],v[112:115]
ds_read_b64_tr_b16 a[202:203],v2 offset:5120
ds_read_b64_tr_b16 a[196:197],v2 offset:2048
v_lshlrev_b32_sdwa v39,1,v39 dst_sel:DWORD dst_unused:UNUSED_PAD src0_sel:DWORD src1_sel:WORD_1
v_mfma_f32_16x16x32_bf16 v[116:119],a[232:235],v[60:63],v[116:119]
ds_read_u16_d16_hi v39,v39
v_lshlrev_b32_sdwa v56,1,v56 dst_sel:DWORD dst_unused:UNUSED_PAD src0_sel:DWORD src1_sel:WORD_1
v_mfma_f32_16x16x32_bf16 v[76:79],a[240:243],v[60:63],v[76:79]
ds_read_b64_tr_b16 a[198:199],v2 offset:3072
v_pk_fma_f16 v8,v8,s55,s55
v_mfma_f32_16x16x32_bf16 v[72:75],a[240:243],v[32:35],v[72:75]
v_rcp_f16 v9,v8
v_lshlrev_b32_sdwa v58,1,v58 dst_sel:DWORD dst_unused:UNUSED_PAD src0_sel:DWORD src1_sel:WORD_1
v_mfma_f32_16x16x32_bf16 v[120:123],a[224:227],v[32:35],v[120:123]
ds_read_u16 v56,v56
v_lshlrev_b32_sdwa v57,1,v57 dst_sel:DWORD dst_unused:UNUSED_PAD src0_sel:DWORD src1_sel:WORD_1
v_mfma_f32_16x16x32_bf16 v[124:127],a[224:227],v[60:63],v[124:127]
ds_read_u16_d16_hi v58,v58
v_lshlrev_b32_sdwa v59,1,v59 dst_sel:DWORD dst_unused:UNUSED_PAD src0_sel:DWORD src1_sel:WORD_1
v_mfma_f32_16x16x32_bf16 v[92:95],a[248:251],v[60:63],v[92:95]
ds_read_u16 v57,v57
v_lshlrev_b32_sdwa v52,1,v52 dst_sel:DWORD dst_unused:UNUSED_PAD src0_sel:DWORD src1_sel:WORD_1
v_mfma_f32_16x16x32_bf16 v[88:91],a[248:251],v[32:35],v[88:91]
ds_read_u16_d16_hi v59,v59
v_rcp_f16_sdwa v9,v8 dst_sel:WORD_1 dst_unused:UNUSED_PRESERVE src0_sel:WORD_1
v_mfma_f32_16x16x32_bf16 v[128:131],a[228:231],v[32:35],v[128:131]
ds_read_b64_tr_b16 a[192:193],v2 offset:0
v_lshlrev_b32_sdwa v54,1,v54 dst_sel:DWORD dst_unused:UNUSED_PAD src0_sel:DWORD src1_sel:WORD_1
v_mfma_f32_16x16x32_bf16 v[132:135],a[228:231],v[60:63],v[132:135]
ds_read_b64_tr_b16 a[194:195],v2 offset:1024
v_fma_mix_f32 v53,v53,v9,0 op_sel_hi:[0,1,0]
v_mfma_f32_16x16x32_bf16 v[108:111],a[236:239],v[60:63],v[108:111]
ds_read_u16 v52,v52
v_fma_mix_f32 v55,v55,v9,0 op_sel:[0,1,0] op_sel_hi:[0,1,0]
v_mfma_f32_16x16x32_bf16 v[104:107],a[236:239],v[32:35],v[104:107]
s_waitcnt lgkmcnt(7)
ds_read_u16_d16_hi v54,v54
v_mfma_f32_16x16x32_bf16 v[60:63],a[204:207],a[0:3],v[20:23]
v_lshlrev_b32_sdwa v28,1,v28 dst_sel:DWORD dst_unused:UNUSED_PAD src0_sel:DWORD src1_sel:WORD_1
v_lshlrev_b32_sdwa v30,1,v30 dst_sel:DWORD dst_unused:UNUSED_PAD src0_sel:DWORD src1_sel:WORD_1
v_mfma_f32_16x16x32_bf16 v[60:63],a[208:211],a[16:19],v[60:63]
ds_read_u16 v28,v28
v_lshl_add_u64 v[40:41],v[40:41],0,v[42:43]
v_mfma_f32_16x16x32_bf16 v[60:63],a[212:215],a[32:35],v[60:63]
ds_read_u16_d16_hi v30,v30
v_lshl_add_u64 v[42:43],v[36:37],0,v[38:39]
v_mfma_f32_16x16x32_bf16 v[32:35],a[200:203],a[32:35],v[16:19]
v_lshlrev_b32_sdwa v29,1,v29 dst_sel:DWORD dst_unused:UNUSED_PAD src0_sel:DWORD src1_sel:WORD_1
v_lshlrev_b32_sdwa v31,1,v31 dst_sel:DWORD dst_unused:UNUSED_PAD src0_sel:DWORD src1_sel:WORD_1
v_mfma_f32_16x16x32_bf16 v[32:35],a[196:199],a[16:19],v[32:35]
s_waitcnt lgkmcnt(4)
ds_read_u16 v29,v29
v_lshl_add_u64 v[36:37],v[56:57],0,v[58:59]
v_mfma_f32_16x16x32_bf16 v[32:35],a[192:195],a[0:3],v[32:35]
ds_read_u16_d16_hi v31,v31
v_lshlrev_b32_sdwa v24,1,v24 dst_sel:DWORD dst_unused:UNUSED_PAD src0_sel:DWORD src1_sel:WORD_1
v_mfma_f32_16x16x32_bf16 v[56:59],a[192:195],a[4:7],v[16:19]
s_waitcnt lgkmcnt(4)
ds_read_u16 v24,v24
v_or_b32 v38,v52,v54
v_mfma_f32_16x16x32_bf16 v[56:59],a[196:199],a[20:23],v[56:59]
s_waitcnt vmcnt(4)
v_cvt_pk_bf16_f32 v39,v53,v55
v_lshlrev_b32_sdwa v26,1,v26 dst_sel:DWORD dst_unused:UNUSED_PAD src0_sel:DWORD src1_sel:WORD_1
v_mfma_f32_16x16x32_bf16 v[56:59],a[200:203],a[36:39],v[56:59]
s_barrier
ds_read_u16_d16_hi v26,v26
v_lshlrev_b32_sdwa v25,1,v25 dst_sel:DWORD dst_unused:UNUSED_PAD src0_sel:DWORD src1_sel:WORD_1
v_mfma_f32_16x16x32_bf16 v[52:55],a[212:215],a[36:39],v[20:23]
ds_read_u16 v25,v25
v_lshlrev_b32_sdwa v27,1,v27 dst_sel:DWORD dst_unused:UNUSED_PAD src0_sel:DWORD src1_sel:WORD_1
v_mfma_f32_16x16x32_bf16 v[52:55],a[208:211],a[20:23],v[52:55]
s_add_u32 m0,s58,18480
ds_read_u16_d16_hi v27,v27
v_lshlrev_b32_sdwa v48,1,v48 dst_sel:DWORD dst_unused:UNUSED_PAD src0_sel:DWORD src1_sel:WORD_1
v_mfma_f32_16x16x32_bf16 v[52:55],a[204:207],a[4:7],v[52:55]
s_mov_b32 s69,s88
ds_read_u16 v48,v48
v_lshlrev_b32_sdwa v50,1,v50 dst_sel:DWORD dst_unused:UNUSED_PAD src0_sel:DWORD src1_sel:WORD_1
v_mfma_f32_16x16x32_bf16 v[232:235],a[232:235],v[40:43],v[232:235]
buffer_load_dwordx4 v5,s[44:47],s69 offen offset:0 lds
v_lshlrev_b32_sdwa v49,1,v49 dst_sel:DWORD dst_unused:UNUSED_PAD src0_sel:DWORD src1_sel:WORD_1
v_mfma_f32_16x16x32_bf16 v[236:239],a[232:235],v[36:39],v[236:239]
ds_read_u16_d16_hi v50,v50
v_lshlrev_b32_sdwa v51,1,v51 dst_sel:DWORD dst_unused:UNUSED_PAD src0_sel:DWORD src1_sel:WORD_1
v_mfma_f32_16x16x32_bf16 v[164:167],a[240:243],v[36:39],v[164:167]
ds_read_u16 v49,v49
ds_read_u16_d16_hi v51,v51
v_mfma_f32_16x16x32_bf16 v[160:163],a[240:243],v[40:43],v[160:163]
ds_read_b64_tr_b16 a[244:245],v3 offset:6160
v_lshlrev_b32_sdwa v44,1,v44 dst_sel:DWORD dst_unused:UNUSED_PAD src0_sel:DWORD src1_sel:WORD_1
v_mfma_f32_16x16x32_bf16 v[240:243],a[224:227],v[40:43],v[240:243]
s_add_u32 s69,s69,s53
ds_read_u16 v44,v44
v_lshlrev_b32_sdwa v46,1,v46 dst_sel:DWORD dst_unused:UNUSED_PAD src0_sel:DWORD src1_sel:WORD_1
v_mfma_f32_16x16x32_bf16 v[244:247],a[224:227],v[36:39],v[244:247]
buffer_load_dwordx4 v5,s[44:47],s69 offen offset:1024 lds
ds_read_u16_d16_hi v46,v46
v_mfma_f32_16x16x32_bf16 v[180:183],a[248:251],v[36:39],v[180:183]
ds_read_b64_tr_b16 a[246:247],v3 offset:7696
v_lshlrev_b32_sdwa v45,1,v45 dst_sel:DWORD dst_unused:UNUSED_PAD src0_sel:DWORD src1_sel:WORD_1
v_mfma_f32_16x16x32_bf16 v[176:179],a[248:251],v[40:43],v[176:179]
ds_read_u16 v45,v45
v_lshlrev_b32_sdwa v47,1,v47 dst_sel:DWORD dst_unused:UNUSED_PAD src0_sel:DWORD src1_sel:WORD_1
v_mfma_f32_16x16x32_bf16 v[248:251],a[228:231],v[40:43],v[248:251]
ds_read_u16_d16_hi v47,v47
v_mfma_f32_16x16x32_bf16 v[252:255],a[228:231],v[36:39],v[252:255]
s_add_u32 s69,s69,s53
ds_read_b64_tr_b16 a[252:253],v3 offset:6168
v_mfma_f32_16x16x32_bf16 v[196:199],a[236:239],v[36:39],v[196:199]
buffer_load_dwordx4 v5,s[44:47],s69 offen offset:2048 lds
v_mfma_f32_16x16x32_bf16 v[192:195],a[236:239],v[40:43],v[192:195]
ds_read_b64_tr_b16 a[254:255],v3 offset:7704
v_add_u32 v11,88272,v1
v_add_u32 v12,88272,v2
v_mfma_f32_16x16x32_bf16 v[36:39],a[204:207],a[8:11],v[20:23]
TE13_%=:
s_cbranch_execz TR1_%=
ds_read_b64_tr_b16 a[96:97],v11 offset:0
ds_read_b64_tr_b16 a[98:99],v11 offset:1024
v_lshlrev_b32_sdwa v60,1,v60 dst_sel:DWORD dst_unused:UNUSED_PAD src0_sel:DWORD src1_sel:WORD_1
v_mfma_f32_16x16x32_bf16 v[36:39],a[208:211],a[24:27],v[36:39]
s_waitcnt lgkmcnt(8)
ds_read_b64_tr_b16 a[100:101],v11 offset:2048
ds_read_b64_tr_b16 a[102:103],v11 offset:3072
v_lshl_add_u64 v[28:29],v[28:29],0,v[30:31]
v_fma_mixlo_f16 v8,v57,s99,0
v_mfma_f32_16x16x32_bf16 v[36:39],a[212:215],a[40:43],v[36:39]
s_nop 0
ds_read_u16 v60,v60
ds_read_b64_tr_b16 a[104:105],v11 offset:4096
v_lshl_add_u64 v[30:31],v[24:25],0,v[26:27]
v_lshlrev_b32_sdwa v62,1,v62 dst_sel:DWORD dst_unused:UNUSED_PAD src0_sel:DWORD src1_sel:WORD_1
v_mfma_f32_16x16x32_bf16 v[40:43],a[200:203],a[40:43],v[16:19]
ds_read_u16_d16_hi v62,v62
ds_read_b64_tr_b16 a[106:107],v11 offset:5120
v_fma_mixhi_f16 v8,v59,s99,0
v_mfma_f32_16x16x32_bf16 v[40:43],a[196:199],a[24:27],v[40:43]
ds_read_b64_tr_b16 a[108:109],v12 offset:4096
v_lshl_add_u64 v[24:25],v[48:49],0,v[50:51]
v_lshlrev_b32_sdwa v61,1,v61 dst_sel:DWORD dst_unused:UNUSED_PAD src0_sel:DWORD src1_sel:WORD_1
v_pk_fma_f16 v9,v8,v8,s54
v_mfma_f32_16x16x32_bf16 v[40:43],a[192:195],a[8:11],v[40:43]
ds_read_u16 v61,v61
v_lshlrev_b32_sdwa v63,1,v63 dst_sel:DWORD dst_unused:UNUSED_PAD src0_sel:DWORD src1_sel:WORD_1
v_mfma_f32_16x16x32_bf16 v[48:51],a[192:195],a[12:15],v[16:19]
s_nop 0
ds_read_u16_d16_hi v63,v63
v_pk_mul_f16 v9,v8,v9
v_mfma_f32_16x16x32_bf16 v[48:51],a[196:199],a[28:31],v[48:51]
s_waitcnt lgkmcnt(12)
ds_read_b64_tr_b16 a[110:111],v12 offset:5120
ds_read_b64_tr_b16 a[112:113],v12 offset:2048
v_lshl_add_u64 v[26:27],v[44:45],0,v[46:47]
v_lshlrev_b32_sdwa v32,1,v32 dst_sel:DWORD dst_unused:UNUSED_PAD src0_sel:DWORD src1_sel:WORD_1
v_mfma_f32_16x16x32_bf16 v[48:51],a[200:203],a[44:47],v[48:51]
ds_read_u16 v32,v32
v_exp_f16 v8,v9
v_mfma_f32_16x16x32_bf16 v[44:47],a[212:215],a[44:47],v[20:23]
ds_read_b64_tr_b16 a[114:115],v12 offset:3072
v_lshlrev_b32_sdwa v34,1,v34 dst_sel:DWORD dst_unused:UNUSED_PAD src0_sel:DWORD src1_sel:WORD_1
v_mfma_f32_16x16x32_bf16 v[44:47],a[208:211],a[28:31],v[44:47]
ds_read_u16_d16_hi v34,v34
v_lshlrev_b32_sdwa v33,1,v33 dst_sel:DWORD dst_unused:UNUSED_PAD src0_sel:DWORD src1_sel:WORD_1
v_mfma_f32_16x16x32_bf16 v[44:47],a[204:207],a[12:15],v[44:47]
v_exp_f16_sdwa v8,v9 dst_sel:WORD_1 dst_unused:UNUSED_PRESERVE src0_sel:WORD_1
v_mfma_f32_16x16x32_bf16 v[208:211],a[232:235],v[28:31],v[208:211]
ds_read_u16 v33,v33
v_lshlrev_b32_sdwa v35,1,v35 dst_sel:DWORD dst_unused:UNUSED_PAD src0_sel:DWORD src1_sel:WORD_1
v_mfma_f32_16x16x32_bf16 v[212:215],a[232:235],v[24:27],v[212:215]
ds_read_u16_d16_hi v35,v35
ds_read_b64_tr_b16 a[232:233],v3 offset:6224
v_lshlrev_b32_sdwa v52,1,v52 dst_sel:DWORD dst_unused:UNUSED_PAD src0_sel:DWORD src1_sel:WORD_1
v_mfma_f32_16x16x32_bf16 v[172:175],a[240:243],v[24:27],v[172:175]
ds_read_u16 v52,v52
v_pk_fma_f16 v8,v8,s55,s55
v_mfma_f32_16x16x32_bf16 v[168:171],a[240:243],v[28:31],v[168:171]
ds_read_b64_tr_b16 a[234:235],v3 offset:7760
v_rcp_f16 v9,v8
v_lshlrev_b32_sdwa v54,1,v54 dst_sel:DWORD dst_unused:UNUSED_PAD src0_sel:DWORD src1_sel:WORD_1
v_mfma_f32_16x16x32_bf16 v[216:219],a[224:227],v[28:31],v[216:219]
ds_read_u16_d16_hi v54,v54
ds_read_b64_tr_b16 a[240:241],v3 offset:6232
v_mfma_f32_16x16x32_bf16 v[220:223],a[224:227],v[24:27],v[220:223]
ds_read_b64_tr_b16 a[242:243],v3 offset:7768
ds_read_b64_tr_b16 a[224:225],v3 offset:6288
v_lshlrev_b32_sdwa v53,1,v53 dst_sel:DWORD dst_unused:UNUSED_PAD src0_sel:DWORD src1_sel:WORD_1
v_mfma_f32_16x16x32_bf16 v[188:191],a[248:251],v[24:27],v[188:191]
ds_read_u16 v53,v53
ds_read_b64_tr_b16 a[226:227],v3 offset:7824
v_lshlrev_b32_sdwa v55,1,v55 dst_sel:DWORD dst_unused:UNUSED_PAD src0_sel:DWORD src1_sel:WORD_1
v_mfma_f32_16x16x32_bf16 v[184:187],a[248:251],v[28:31],v[184:187]
ds_read_u16_d16_hi v55,v55
v_rcp_f16_sdwa v9,v8 dst_sel:WORD_1 dst_unused:UNUSED_PRESERVE src0_sel:WORD_1
v_mfma_f32_16x16x32_bf16 v[224:227],a[228:231],v[28:31],v[224:227]
ds_read_b64_tr_b16 a[248:249],v3 offset:6296
ds_read_b64_tr_b16 a[250:251],v3 offset:7832
v_lshlrev_b32_sdwa v56,1,v56 dst_sel:DWORD dst_unused:UNUSED_PAD src0_sel:DWORD src1_sel:WORD_1
v_mfma_f32_16x16x32_bf16 v[228:231],a[228:231],v[24:27],v[228:231]
ds_read_u16 v56,v56
ds_read_b64_tr_b16 a[116:117],v12 offset:0
v_lshlrev_b32_sdwa v58,1,v58 dst_sel:DWORD dst_unused:UNUSED_PAD src0_sel:DWORD src1_sel:WORD_1
v_mfma_f32_16x16x32_bf16 v[204:207],a[236:239],v[24:27],v[204:207]
ds_read_u16_d16_hi v58,v58
v_fma_mix_f32 v57,v57,v9,0 op_sel_hi:[0,1,0]
v_mfma_f32_16x16x32_bf16 v[200:203],a[236:239],v[28:31],v[200:203]
ds_read_b64_tr_b16 a[118:119],v12 offset:1024
ds_read_b64_tr_b16 a[120:121],v12 offset:6160
v_fma_mix_f32 v59,v59,v9,0 op_sel:[0,1,0] op_sel_hi:[0,1,0]
v_mfma_f32_16x16x32_bf16 v[24:27],a[204:207],a[48:51],v[20:23]
ds_read_b64_tr_b16 a[122:123],v12 offset:7184
ds_read_b64_tr_b16 a[216:217],v1 offset:6160
v_lshlrev_b32_sdwa v36,1,v36 dst_sel:DWORD dst_unused:UNUSED_PAD src0_sel:DWORD src1_sel:WORD_1
v_mfma_f32_16x16x32_bf16 v[24:27],a[208:211],a[64:67],v[24:27]
s_waitcnt lgkmcnt(14)
ds_read_u16 v36,v36
ds_read_b64_tr_b16 a[218:219],v1 offset:7184
v_lshl_add_u64 v[60:61],v[60:61],0,v[62:63]
v_mfma_f32_16x16x32_bf16 v[24:27],a[212:215],a[80:83],v[24:27]
ds_read_b64_tr_b16 a[124:125],v12 offset:8208
v_lshl_add_u64 v[62:63],v[32:33],0,v[34:35]
v_lshlrev_b32_sdwa v38,1,v38 dst_sel:DWORD dst_unused:UNUSED_PAD src0_sel:DWORD src1_sel:WORD_1
v_mfma_f32_16x16x32_bf16 v[28:31],a[200:203],a[80:83],v[16:19]
ds_read_u16_d16_hi v38,v38
v_lshlrev_b32_sdwa v37,1,v37 dst_sel:DWORD dst_unused:UNUSED_PAD src0_sel:DWORD src1_sel:WORD_1
v_mfma_f32_16x16x32_bf16 v[28:31],a[196:199],a[64:67],v[28:31]
s_waitcnt lgkmcnt(13)
ds_read_u16 v37,v37
v_lshl_add_u64 v[32:33],v[52:53],0,v[54:55]
v_mfma_f32_16x16x32_bf16 v[28:31],a[192:195],a[48:51],v[28:31]
ds_read_b64_tr_b16 a[126:127],v12 offset:9232
v_lshlrev_b32_sdwa v39,1,v39 dst_sel:DWORD dst_unused:UNUSED_PAD src0_sel:DWORD src1_sel:WORD_1
v_mfma_f32_16x16x32_bf16 v[52:55],a[192:195],a[52:55],v[16:19]
s_waitcnt lgkmcnt(10)
ds_read_u16_d16_hi v39,v39
v_or_b32 v34,v56,v58
v_lshlrev_b32_sdwa v40,1,v40 dst_sel:DWORD dst_unused:UNUSED_PAD src0_sel:DWORD src1_sel:WORD_1
v_mfma_f32_16x16x32_bf16 v[52:55],a[196:199],a[68:71],v[52:55]
ds_read_u16 v40,v40
ds_read_b64_tr_b16 a[220:221],v1 offset:8208
v_cvt_pk_bf16_f32 v35,v57,v59
v_lshlrev_b32_sdwa v42,1,v42 dst_sel:DWORD dst_unused:UNUSED_PAD src0_sel:DWORD src1_sel:WORD_1
v_mfma_f32_16x16x32_bf16 v[52:55],a[200:203],a[84:87],v[52:55]
ds_read_u16_d16_hi v42,v42
v_lshlrev_b32_sdwa v41,1,v41 dst_sel:DWORD dst_unused:UNUSED_PAD src0_sel:DWORD src1_sel:WORD_1
v_mfma_f32_16x16x32_bf16 v[56:59],a[212:215],a[84:87],v[20:23]
ds_read_u16 v41,v41
v_lshlrev_b32_sdwa v43,1,v43 dst_sel:DWORD dst_unused:UNUSED_PAD src0_sel:DWORD src1_sel:WORD_1
v_mfma_f32_16x16x32_bf16 v[56:59],a[208:211],a[68:71],v[56:59]
ds_read_u16_d16_hi v43,v43
v_lshlrev_b32_sdwa v44,1,v44 dst_sel:DWORD dst_unused:UNUSED_PAD src0_sel:DWORD src1_sel:WORD_1
v_mfma_f32_16x16x32_bf16 v[56:59],a[204:207],a[52:55],v[56:59]
ds_read_u16 v44,v44
v_lshlrev_b32_sdwa v46,1,v46 dst_sel:DWORD dst_unused:UNUSED_PAD src0_sel:DWORD src1_sel:WORD_1
v_mfma_f32_16x16x32_bf16 v[136:139],a[244:247],v[60:63],v[136:139]
ds_read_u16_d16_hi v46,v46
v_lshlrev_b32_sdwa v45,1,v45 dst_sel:DWORD dst_unused:UNUSED_PAD src0_sel:DWORD src1_sel:WORD_1
v_mfma_f32_16x16x32_bf16 v[140:143],a[244:247],v[32:35],v[140:143]
ds_read_u16 v45,v45
v_lshlrev_b32_sdwa v47,1,v47 dst_sel:DWORD dst_unused:UNUSED_PAD src0_sel:DWORD src1_sel:WORD_1
v_mfma_f32_16x16x32_bf16 v[68:71],a[252:255],v[32:35],v[68:71]
ds_read_u16_d16_hi v47,v47
v_lshlrev_b32_sdwa v48,1,v48 dst_sel:DWORD dst_unused:UNUSED_PAD src0_sel:DWORD src1_sel:WORD_1
v_mfma_f32_16x16x32_bf16 v[64:67],a[252:255],v[60:63],v[64:67]
ds_read_u16 v48,v48
ds_read_b64_tr_b16 a[222:223],v1 offset:9232
v_mfma_f32_16x16x32_bf16 v[144:147],a[232:235],v[60:63],v[144:147]
ds_read_b64_tr_b16 a[128:129],v12 offset:10256
v_lshlrev_b32_sdwa v50,1,v50 dst_sel:DWORD dst_unused:UNUSED_PAD src0_sel:DWORD src1_sel:WORD_1
v_mfma_f32_16x16x32_bf16 v[148:151],a[232:235],v[32:35],v[148:151]
ds_read_u16_d16_hi v50,v50
v_lshlrev_b32_sdwa v49,1,v49 dst_sel:DWORD dst_unused:UNUSED_PAD src0_sel:DWORD src1_sel:WORD_1
v_mfma_f32_16x16x32_bf16 v[84:87],a[240:243],v[32:35],v[84:87]
ds_read_u16 v49,v49
v_lshlrev_b32_sdwa v51,1,v51 dst_sel:DWORD dst_unused:UNUSED_PAD src0_sel:DWORD src1_sel:WORD_1
v_mfma_f32_16x16x32_bf16 v[80:83],a[240:243],v[60:63],v[80:83]
ds_read_u16_d16_hi v51,v51
v_mfma_f32_16x16x32_bf16 v[152:155],a[224:227],v[60:63],v[152:155]
ds_read_b64_tr_b16 a[130:131],v12 offset:11280
v_mfma_f32_16x16x32_bf16 v[156:159],a[224:227],v[32:35],v[156:159]
ds_read_b64_tr_b16 a[132:133],v11 offset:10256
v_mfma_f32_16x16x32_bf16 v[100:103],a[248:251],v[32:35],v[100:103]
ds_read_b64_tr_b16 a[134:135],v11 offset:11280
v_mfma_f32_16x16x32_bf16 v[96:99],a[248:251],v[60:63],v[96:99]
ds_read_b64_tr_b16 a[136:137],v11 offset:8208
v_mfma_f32_16x16x32_bf16 v[32:35],a[204:207],a[56:59],v[20:23]
s_nop 0
ds_read_b64_tr_b16 a[138:139],v11 offset:9232
v_lshlrev_b32_sdwa v24,1,v24 dst_sel:DWORD dst_unused:UNUSED_PAD src0_sel:DWORD src1_sel:WORD_1
v_mfma_f32_16x16x32_bf16 v[32:35],a[208:211],a[72:75],v[32:35]
s_waitcnt lgkmcnt(8)
ds_read_u16 v24,v24
v_lshl_add_u64 v[36:37],v[36:37],0,v[38:39]
v_fma_mixlo_f16 v8,v53,s99,0
v_mfma_f32_16x16x32_bf16 v[32:35],a[212:215],a[88:91],v[32:35]
ds_read_b64_tr_b16 a[140:141],v11 offset:6160
v_lshl_add_u64 v[38:39],v[40:41],0,v[42:43]
v_lshlrev_b32_sdwa v26,1,v26 dst_sel:DWORD dst_unused:UNUSED_PAD src0_sel:DWORD src1_sel:WORD_1
v_mfma_f32_16x16x32_bf16 v[60:63],a[200:203],a[88:91],v[16:19]
ds_read_u16_d16_hi v26,v26
v_fma_mixhi_f16 v8,v55,s99,0
v_mfma_f32_16x16x32_bf16 v[60:63],a[196:199],a[72:75],v[60:63]
ds_read_b64_tr_b16 a[142:143],v11 offset:7184
v_lshl_add_u64 v[40:41],v[44:45],0,v[46:47]
v_lshlrev_b32_sdwa v25,1,v25 dst_sel:DWORD dst_unused:UNUSED_PAD src0_sel:DWORD src1_sel:WORD_1
v_pk_fma_f16 v9,v8,v8,s54
v_mfma_f32_16x16x32_bf16 v[60:63],a[192:195],a[56:59],v[60:63]
ds_read_u16 v25,v25
v_lshlrev_b32_sdwa v27,1,v27 dst_sel:DWORD dst_unused:UNUSED_PAD src0_sel:DWORD src1_sel:WORD_1
v_mfma_f32_16x16x32_bf16 v[44:47],a[192:195],a[60:63],v[16:19]
s_nop 0
ds_read_b128 v[16:19],v4 offset:1280
v_pk_mul_f16 v9,v8,v9
v_mfma_f32_16x16x32_bf16 v[44:47],a[196:199],a[76:79],v[44:47]
s_waitcnt lgkmcnt(8)
ds_read_u16_d16_hi v27,v27
ds_read_b64_tr_b16 a[192:193],v1 offset:10256
v_lshl_add_u64 v[42:43],v[48:49],0,v[50:51]
v_lshlrev_b32_sdwa v28,1,v28 dst_sel:DWORD dst_unused:UNUSED_PAD src0_sel:DWORD src1_sel:WORD_1
v_mfma_f32_16x16x32_bf16 v[44:47],a[200:203],a[92:95],v[44:47]
ds_read_b64_tr_b16 a[194:195],v1 offset:11280
v_exp_f16 v8,v9
v_mfma_f32_16x16x32_bf16 v[48:51],a[212:215],a[92:95],v[20:23]
ds_read_b128 v[20:23],v4 offset:1312
ds_read_u16 v28,v28
v_lshlrev_b32_sdwa v30,1,v30 dst_sel:DWORD dst_unused:UNUSED_PAD src0_sel:DWORD src1_sel:WORD_1
v_mfma_f32_16x16x32_bf16 v[48:51],a[208:211],a[76:79],v[48:51]
ds_read_u16_d16_hi v30,v30
ds_read_b64_tr_b16 a[196:197],v2 offset:10256
v_lshlrev_b32_sdwa v29,1,v29 dst_sel:DWORD dst_unused:UNUSED_PAD src0_sel:DWORD src1_sel:WORD_1
v_mfma_f32_16x16x32_bf16 v[48:51],a[204:207],a[60:63],v[48:51]
ds_read_u16 v29,v29
v_exp_f16_sdwa v8,v9 dst_sel:WORD_1 dst_unused:UNUSED_PRESERVE src0_sel:WORD_1
v_mfma_f32_16x16x32_bf16 v[112:115],a[244:247],v[36:39],v[112:115]
ds_read_b64_tr_b16 a[198:199],v2 offset:11280
ds_read_b64_tr_b16 a[200:201],v2 offset:8208
v_lshlrev_b32_sdwa v31,1,v31 dst_sel:DWORD dst_unused:UNUSED_PAD src0_sel:DWORD src1_sel:WORD_1
v_mfma_f32_16x16x32_bf16 v[116:119],a[244:247],v[40:43],v[116:119]
ds_read_u16_d16_hi v31,v31
v_lshlrev_b32_sdwa v56,1,v56 dst_sel:DWORD dst_unused:UNUSED_PAD src0_sel:DWORD src1_sel:WORD_1
v_mfma_f32_16x16x32_bf16 v[76:79],a[252:255],v[40:43],v[76:79]
ds_read_b64_tr_b16 a[202:203],v2 offset:9232
v_pk_fma_f16 v8,v8,s55,s55
v_mfma_f32_16x16x32_bf16 v[72:75],a[252:255],v[36:39],v[72:75]
ds_read_u16 v56,v56
v_rcp_f16 v9,v8
v_lshlrev_b32_sdwa v58,1,v58 dst_sel:DWORD dst_unused:UNUSED_PAD src0_sel:DWORD src1_sel:WORD_1
v_mfma_f32_16x16x32_bf16 v[120:123],a[232:235],v[36:39],v[120:123]
ds_read_u16_d16_hi v58,v58
ds_read_b64_tr_b16 a[212:213],v2 offset:6160
v_mfma_f32_16x16x32_bf16 v[124:127],a[232:235],v[40:43],v[124:127]
ds_read_b64_tr_b16 a[214:215],v2 offset:7184
v_lshlrev_b32_sdwa v57,1,v57 dst_sel:DWORD dst_unused:UNUSED_PAD src0_sel:DWORD src1_sel:WORD_1
v_mfma_f32_16x16x32_bf16 v[92:95],a[240:243],v[40:43],v[92:95]
ds_read_u16 v57,v57
v_lshlrev_b32_sdwa v59,1,v59 dst_sel:DWORD dst_unused:UNUSED_PAD src0_sel:DWORD src1_sel:WORD_1
v_mfma_f32_16x16x32_bf16 v[88:91],a[240:243],v[36:39],v[88:91]
v_rcp_f16_sdwa v9,v8 dst_sel:WORD_1 dst_unused:UNUSED_PRESERVE src0_sel:WORD_1
v_mfma_f32_16x16x32_bf16 v[128:131],a[224:227],v[36:39],v[128:131]
ds_read_u16_d16_hi v59,v59
v_lshlrev_b32_sdwa v52,1,v52 dst_sel:DWORD dst_unused:UNUSED_PAD src0_sel:DWORD src1_sel:WORD_1
v_mfma_f32_16x16x32_bf16 v[132:135],a[224:227],v[40:43],v[132:135]
ds_read_u16 v52,v52
v_lshlrev_b32_sdwa v54,1,v54 dst_sel:DWORD dst_unused:UNUSED_PAD src0_sel:DWORD src1_sel:WORD_1
v_mfma_f32_16x16x32_bf16 v[108:111],a[248:251],v[40:43],v[108:111]
ds_read_u16_d16_hi v54,v54
v_fma_mix_f32 v53,v53,v9,0 op_sel_hi:[0,1,0]
v_mfma_f32_16x16x32_bf16 v[104:107],a[248:251],v[36:39],v[104:107]
s_waitcnt lgkmcnt(8)
ds_read_b64_tr_b16 a[144:145],v11 offset:12320
v_fma_mix_f32 v55,v55,v9,0 op_sel:[0,1,0] op_sel_hi:[0,1,0]
v_mfma_f32_16x16x32_bf16 v[40:43],a[216:219],a[0:3],v[16:19]
ds_read_b64_tr_b16 a[146:147],v11 offset:13344
v_lshlrev_b32_sdwa v32,1,v32 dst_sel:DWORD dst_unused:UNUSED_PAD src0_sel:DWORD src1_sel:WORD_1
v_mfma_f32_16x16x32_bf16 v[40:43],a[220:223],a[16:19],v[40:43]
ds_read_u16 v32,v32
v_lshl_add_u64 v[24:25],v[24:25],0,v[26:27]
v_mfma_f32_16x16x32_bf16 v[40:43],a[192:195],a[32:35],v[40:43]
ds_read_b64_tr_b16 a[148:149],v11 offset:14368
v_lshl_add_u64 v[26:27],v[28:29],0,v[30:31]
v_lshlrev_b32_sdwa v34,1,v34 dst_sel:DWORD dst_unused:UNUSED_PAD src0_sel:DWORD src1_sel:WORD_1
v_mfma_f32_16x16x32_bf16 v[36:39],a[196:199],a[32:35],v[20:23]
ds_read_u16_d16_hi v34,v34
v_lshlrev_b32_sdwa v33,1,v33 dst_sel:DWORD dst_unused:UNUSED_PAD src0_sel:DWORD src1_sel:WORD_1
v_mfma_f32_16x16x32_bf16 v[36:39],a[200:203],a[16:19],v[36:39]
s_waitcnt lgkmcnt(7)
ds_read_u16 v33,v33
v_lshl_add_u64 v[28:29],v[56:57],0,v[58:59]
v_mfma_f32_16x16x32_bf16 v[36:39],a[212:215],a[0:3],v[36:39]
ds_read_b64_tr_b16 a[150:151],v11 offset:15392
v_lshlrev_b32_sdwa v35,1,v35 dst_sel:DWORD dst_unused:UNUSED_PAD src0_sel:DWORD src1_sel:WORD_1
v_mfma_f32_16x16x32_bf16 v[56:59],a[212:215],a[4:7],v[20:23]
s_waitcnt lgkmcnt(7)
ds_read_u16_d16_hi v35,v35
v_or_b32 v30,v52,v54
v_lshlrev_b32_sdwa v60,1,v60 dst_sel:DWORD dst_unused:UNUSED_PAD src0_sel:DWORD src1_sel:WORD_1
v_mfma_f32_16x16x32_bf16 v[56:59],a[200:203],a[20:23],v[56:59]
ds_read_u16 v60,v60
v_cvt_pk_bf16_f32 v31,v53,v55
v_lshlrev_b32_sdwa v62,1,v62 dst_sel:DWORD dst_unused:UNUSED_PAD src0_sel:DWORD src1_sel:WORD_1
v_mfma_f32_16x16x32_bf16 v[56:59],a[196:199],a[36:39],v[56:59]
ds_read_u16_d16_hi v62,v62
v_lshlrev_b32_sdwa v61,1,v61 dst_sel:DWORD dst_unused:UNUSED_PAD src0_sel:DWORD src1_sel:WORD_1
v_mfma_f32_16x16x32_bf16 v[52:55],a[192:195],a[36:39],v[16:19]
ds_read_u16 v61,v61
v_lshlrev_b32_sdwa v63,1,v63 dst_sel:DWORD dst_unused:UNUSED_PAD src0_sel:DWORD src1_sel:WORD_1
v_mfma_f32_16x16x32_bf16 v[52:55],a[220:223],a[20:23],v[52:55]
ds_read_u16_d16_hi v63,v63
v_lshlrev_b32_sdwa v48,1,v48 dst_sel:DWORD dst_unused:UNUSED_PAD src0_sel:DWORD src1_sel:WORD_1
v_mfma_f32_16x16x32_bf16 v[52:55],a[216:219],a[4:7],v[52:55]
ds_read_u16 v48,v48
v_lshlrev_b32_sdwa v50,1,v50 dst_sel:DWORD dst_unused:UNUSED_PAD src0_sel:DWORD src1_sel:WORD_1
v_mfma_f32_16x16x32_bf16 v[232:235],a[244:247],v[24:27],v[232:235]
ds_read_u16_d16_hi v50,v50
ds_read_b64_tr_b16 a[152:153],v11 offset:16416
v_mfma_f32_16x16x32_bf16 v[236:239],a[244:247],v[28:31],v[236:239]
ds_read_b64_tr_b16 a[154:155],v11 offset:17440
v_lshlrev_b32_sdwa v49,1,v49 dst_sel:DWORD dst_unused:UNUSED_PAD src0_sel:DWORD src1_sel:WORD_1
v_mfma_f32_16x16x32_bf16 v[164:167],a[252:255],v[28:31],v[164:167]
ds_read_u16 v49,v49
ds_read_b64_tr_b16 a[228:229],v3 offset:12320
v_mfma_f32_16x16x32_bf16 v[160:163],a[252:255],v[24:27],v[160:163]
ds_read_b64_tr_b16 a[230:231],v3 offset:13856
ds_read_b64_tr_b16 a[236:237],v3 offset:12328
v_mfma_f32_16x16x32_bf16 v[240:243],a[232:235],v[24:27],v[240:243]
ds_read_b64_tr_b16 a[238:239],v3 offset:13864
v_lshlrev_b32_sdwa v51,1,v51 dst_sel:DWORD dst_unused:UNUSED_PAD src0_sel:DWORD src1_sel:WORD_1
v_mfma_f32_16x16x32_bf16 v[244:247],a[232:235],v[28:31],v[244:247]
ds_read_u16_d16_hi v51,v51
v_lshlrev_b32_sdwa v44,1,v44 dst_sel:DWORD dst_unused:UNUSED_PAD src0_sel:DWORD src1_sel:WORD_1
v_mfma_f32_16x16x32_bf16 v[180:183],a[240:243],v[28:31],v[180:183]
ds_read_u16 v44,v44
v_lshlrev_b32_sdwa v46,1,v46 dst_sel:DWORD dst_unused:UNUSED_PAD src0_sel:DWORD src1_sel:WORD_1
v_mfma_f32_16x16x32_bf16 v[176:179],a[240:243],v[24:27],v[176:179]
ds_read_u16_d16_hi v46,v46
v_lshlrev_b32_sdwa v45,1,v45 dst_sel:DWORD dst_unused:UNUSED_PAD src0_sel:DWORD src1_sel:WORD_1
v_mfma_f32_16x16x32_bf16 v[248:251],a[224:227],v[24:27],v[248:251]
ds_read_u16 v45,v45
v_lshlrev_b32_sdwa v47,1,v47 dst_sel:DWORD dst_unused:UNUSED_PAD src0_sel:DWORD src1_sel:WORD_1
v_mfma_f32_16x16x32_bf16 v[252:255],a[224:227],v[28:31],v[252:255]
ds_read_u16_d16_hi v47,v47
v_mfma_f32_16x16x32_bf16 v[196:199],a[248:251],v[28:31],v[196:199]
ds_read_b64_tr_b16 a[156:157],v12 offset:16416
v_mfma_f32_16x16x32_bf16 v[192:195],a[248:251],v[24:27],v[192:195]
ds_read_b64_tr_b16 a[158:159],v12 offset:17440
v_mfma_f32_16x16x32_bf16 v[28:31],a[216:219],a[8:11],v[16:19]
ds_read_b64_tr_b16 a[160:161],v12 offset:14368
v_lshlrev_b32_sdwa v40,1,v40 dst_sel:DWORD dst_unused:UNUSED_PAD src0_sel:DWORD src1_sel:WORD_1
v_mfma_f32_16x16x32_bf16 v[28:31],a[220:223],a[24:27],v[28:31]
s_waitcnt lgkmcnt(8)
ds_read_u16 v40,v40
v_lshl_add_u64 v[32:33],v[32:33],0,v[34:35]
v_fma_mixlo_f16 v8,v57,s99,0
v_mfma_f32_16x16x32_bf16 v[28:31],a[192:195],a[40:43],v[28:31]
ds_read_b64_tr_b16 a[162:163],v12 offset:15392
v_lshl_add_u64 v[34:35],v[60:61],0,v[62:63]
v_lshlrev_b32_sdwa v42,1,v42 dst_sel:DWORD dst_unused:UNUSED_PAD src0_sel:DWORD src1_sel:WORD_1
v_mfma_f32_16x16x32_bf16 v[24:27],a[196:199],a[40:43],v[20:23]
ds_read_u16_d16_hi v42,v42
v_fma_mixhi_f16 v8,v59,s99,0
v_mfma_f32_16x16x32_bf16 v[24:27],a[200:203],a[24:27],v[24:27]
s_waitcnt lgkmcnt(8)
ds_read_b64_tr_b16 a[164:165],v12 offset:12320
v_lshl_add_u64 v[60:61],v[48:49],0,v[50:51]
v_lshlrev_b32_sdwa v41,1,v41 dst_sel:DWORD dst_unused:UNUSED_PAD src0_sel:DWORD src1_sel:WORD_1
v_pk_fma_f16 v9,v8,v8,s54
v_mfma_f32_16x16x32_bf16 v[24:27],a[212:215],a[8:11],v[24:27]
ds_read_u16 v41,v41
v_lshlrev_b32_sdwa v43,1,v43 dst_sel:DWORD dst_unused:UNUSED_PAD src0_sel:DWORD src1_sel:WORD_1
v_mfma_f32_16x16x32_bf16 v[48:51],a[212:215],a[12:15],v[20:23]
ds_read_u16_d16_hi v43,v43
v_pk_mul_f16 v9,v8,v9
v_mfma_f32_16x16x32_bf16 v[48:51],a[200:203],a[28:31],v[48:51]
s_waitcnt lgkmcnt(8)
ds_read_b64_tr_b16 a[166:167],v12 offset:13344
v_lshl_add_u64 v[62:63],v[44:45],0,v[46:47]
v_lshlrev_b32_sdwa v36,1,v36 dst_sel:DWORD dst_unused:UNUSED_PAD src0_sel:DWORD src1_sel:WORD_1
v_mfma_f32_16x16x32_bf16 v[48:51],a[196:199],a[44:47],v[48:51]
v_exp_f16 v8,v9
v_mfma_f32_16x16x32_bf16 v[44:47],a[192:195],a[44:47],v[16:19]
ds_read_u16 v36,v36
v_lshlrev_b32_sdwa v38,1,v38 dst_sel:DWORD dst_unused:UNUSED_PAD src0_sel:DWORD src1_sel:WORD_1
v_mfma_f32_16x16x32_bf16 v[44:47],a[220:223],a[28:31],v[44:47]
ds_read_u16_d16_hi v38,v38
v_lshlrev_b32_sdwa v37,1,v37 dst_sel:DWORD dst_unused:UNUSED_PAD src0_sel:DWORD src1_sel:WORD_1
v_mfma_f32_16x16x32_bf16 v[44:47],a[216:219],a[12:15],v[44:47]
v_exp_f16_sdwa v8,v9 dst_sel:WORD_1 dst_unused:UNUSED_PRESERVE src0_sel:WORD_1
v_mfma_f32_16x16x32_bf16 v[208:211],a[244:247],v[32:35],v[208:211]
ds_read_u16 v37,v37
v_lshlrev_b32_sdwa v39,1,v39 dst_sel:DWORD dst_unused:UNUSED_PAD src0_sel:DWORD src1_sel:WORD_1
v_mfma_f32_16x16x32_bf16 v[212:215],a[244:247],v[60:63],v[212:215]
ds_read_u16_d16_hi v39,v39
v_lshlrev_b32_sdwa v52,1,v52 dst_sel:DWORD dst_unused:UNUSED_PAD src0_sel:DWORD src1_sel:WORD_1
v_mfma_f32_16x16x32_bf16 v[172:175],a[252:255],v[60:63],v[172:175]
ds_read_u16 v52,v52
v_pk_fma_f16 v8,v8,s55,s55
v_mfma_f32_16x16x32_bf16 v[168:171],a[252:255],v[32:35],v[168:171]
ds_read_b64_tr_b16 a[244:245],v3 offset:12384
v_rcp_f16_e64 v9,v8
v_lshlrev_b32_sdwa v54,1,v54 dst_sel:DWORD dst_unused:UNUSED_PAD src0_sel:DWORD src1_sel:WORD_1
v_mfma_f32_16x16x32_bf16 v[216:219],a[232:235],v[32:35],v[216:219]
ds_read_u16_d16_hi v54,v54
ds_read_b64_tr_b16 a[246:247],v3 offset:13920
v_mfma_f32_16x16x32_bf16 v[220:223],a[232:235],v[60:63],v[220:223]
ds_read_b64_tr_b16 a[252:253],v3 offset:12392
ds_read_b64_tr_b16 a[254:255],v3 offset:13928
v_lshlrev_b32_sdwa v53,1,v53 dst_sel:DWORD dst_unused:UNUSED_PAD src0_sel:DWORD src1_sel:WORD_1
v_mfma_f32_16x16x32_bf16 v[188:191],a[240:243],v[60:63],v[188:191]
ds_read_u16 v53,v53
ds_read_b64_tr_b16 a[232:233],v3 offset:12448
v_lshlrev_b32_sdwa v55,1,v55 dst_sel:DWORD dst_unused:UNUSED_PAD src0_sel:DWORD src1_sel:WORD_1
v_mfma_f32_16x16x32_bf16 v[184:187],a[240:243],v[32:35],v[184:187]
ds_read_u16_d16_hi v55,v55
v_rcp_f16_sdwa v9,v8 dst_sel:WORD_1 dst_unused:UNUSED_PRESERVE src0_sel:WORD_1
v_mfma_f32_16x16x32_bf16 v[224:227],a[224:227],v[32:35],v[224:227]
ds_read_b64_tr_b16 a[234:235],v3 offset:13984
ds_read_b64_tr_b16 a[240:241],v3 offset:12456
v_lshlrev_b32_sdwa v56,1,v56 dst_sel:DWORD dst_unused:UNUSED_PAD src0_sel:DWORD src1_sel:WORD_1
v_mfma_f32_16x16x32_bf16 v[228:231],a[224:227],v[60:63],v[228:231]
ds_read_u16 v56,v56
v_lshlrev_b32_sdwa v58,1,v58 dst_sel:DWORD dst_unused:UNUSED_PAD src0_sel:DWORD src1_sel:WORD_1
v_mfma_f32_16x16x32_bf16 v[204:207],a[248:251],v[60:63],v[204:207]
ds_read_u16_d16_hi v58,v58
v_fma_mix_f32 v57,v57,v9,0 op_sel_hi:[0,1,0]
v_mfma_f32_16x16x32_bf16 v[200:203],a[248:251],v[32:35],v[200:203]
ds_read_b64_tr_b16 a[242:243],v3 offset:13992
ds_read_b64_tr_b16 a[168:169],v12 offset:18480
v_fma_mix_f32 v59,v59,v9,0 op_sel:[0,1,0] op_sel_hi:[0,1,0]
v_mfma_f32_16x16x32_bf16 v[60:63],a[216:219],a[48:51],v[16:19]
ds_read_b64_tr_b16 a[170:171],v12 offset:19504
ds_read_b64_tr_b16 a[172:173],v12 offset:20528
v_lshlrev_b32_sdwa v28,1,v28 dst_sel:DWORD dst_unused:UNUSED_PAD src0_sel:DWORD src1_sel:WORD_1
v_mfma_f32_16x16x32_bf16 v[60:63],a[220:223],a[64:67],v[60:63]
s_waitcnt lgkmcnt(13)
ds_read_u16 v28,v28
ds_read_b64_tr_b16 a[174:175],v12 offset:21552
v_lshl_add_u64 v[40:41],v[40:41],0,v[42:43]
v_mfma_f32_16x16x32_bf16 v[60:63],a[192:195],a[80:83],v[60:63]
ds_read_b64_tr_b16 a[208:209],v1 offset:12320
v_lshl_add_u64 v[42:43],v[36:37],0,v[38:39]
v_lshlrev_b32_sdwa v30,1,v30 dst_sel:DWORD dst_unused:UNUSED_PAD src0_sel:DWORD src1_sel:WORD_1
v_mfma_f32_16x16x32_bf16 v[32:35],a[196:199],a[80:83],v[20:23]
ds_read_u16_d16_hi v30,v30
v_lshlrev_b32_sdwa v29,1,v29 dst_sel:DWORD dst_unused:UNUSED_PAD src0_sel:DWORD src1_sel:WORD_1
v_mfma_f32_16x16x32_bf16 v[32:35],a[200:203],a[64:67],v[32:35]
s_waitcnt lgkmcnt(12)
ds_read_u16 v29,v29
v_lshl_add_u64 v[36:37],v[52:53],0,v[54:55]
v_mfma_f32_16x16x32_bf16 v[32:35],a[212:215],a[48:51],v[32:35]
ds_read_b64_tr_b16 a[210:211],v1 offset:13344
v_lshlrev_b32_sdwa v31,1,v31 dst_sel:DWORD dst_unused:UNUSED_PAD src0_sel:DWORD src1_sel:WORD_1
v_mfma_f32_16x16x32_bf16 v[52:55],a[212:215],a[52:55],v[20:23]
s_waitcnt lgkmcnt(10)
ds_read_u16_d16_hi v31,v31
v_or_b32 v38,v56,v58
v_lshlrev_b32_sdwa v24,1,v24 dst_sel:DWORD dst_unused:UNUSED_PAD src0_sel:DWORD src1_sel:WORD_1
v_mfma_f32_16x16x32_bf16 v[52:55],a[200:203],a[68:71],v[52:55]
ds_read_u16 v24,v24
ds_read_b64_tr_b16 a[204:205],v1 offset:14368
v_cvt_pk_bf16_f32 v39,v57,v59
v_lshlrev_b32_sdwa v26,1,v26 dst_sel:DWORD dst_unused:UNUSED_PAD src0_sel:DWORD src1_sel:WORD_1
v_mfma_f32_16x16x32_bf16 v[52:55],a[196:199],a[84:87],v[52:55]
ds_read_u16_d16_hi v26,v26
v_lshlrev_b32_sdwa v25,1,v25 dst_sel:DWORD dst_unused:UNUSED_PAD src0_sel:DWORD src1_sel:WORD_1
v_mfma_f32_16x16x32_bf16 v[56:59],a[192:195],a[84:87],v[16:19]
ds_read_u16 v25,v25
v_lshlrev_b32_sdwa v27,1,v27 dst_sel:DWORD dst_unused:UNUSED_PAD src0_sel:DWORD src1_sel:WORD_1
v_mfma_f32_16x16x32_bf16 v[56:59],a[220:223],a[68:71],v[56:59]
ds_read_u16_d16_hi v27,v27
v_lshlrev_b32_sdwa v44,1,v44 dst_sel:DWORD dst_unused:UNUSED_PAD src0_sel:DWORD src1_sel:WORD_1
v_mfma_f32_16x16x32_bf16 v[56:59],a[216:219],a[52:55],v[56:59]
ds_read_u16 v44,v44
v_lshlrev_b32_sdwa v46,1,v46 dst_sel:DWORD dst_unused:UNUSED_PAD src0_sel:DWORD src1_sel:WORD_1
v_mfma_f32_16x16x32_bf16 v[136:139],a[228:231],v[40:43],v[136:139]
ds_read_u16_d16_hi v46,v46
v_lshlrev_b32_sdwa v45,1,v45 dst_sel:DWORD dst_unused:UNUSED_PAD src0_sel:DWORD src1_sel:WORD_1
v_mfma_f32_16x16x32_bf16 v[140:143],a[228:231],v[36:39],v[140:143]
ds_read_u16 v45,v45
v_lshlrev_b32_sdwa v47,1,v47 dst_sel:DWORD dst_unused:UNUSED_PAD src0_sel:DWORD src1_sel:WORD_1
v_mfma_f32_16x16x32_bf16 v[68:71],a[236:239],v[36:39],v[68:71]
ds_read_u16_d16_hi v47,v47
v_lshlrev_b32_sdwa v48,1,v48 dst_sel:DWORD dst_unused:UNUSED_PAD src0_sel:DWORD src1_sel:WORD_1
v_mfma_f32_16x16x32_bf16 v[64:67],a[236:239],v[40:43],v[64:67]
ds_read_u16 v48,v48
ds_read_b64_tr_b16 a[206:207],v1 offset:15392
v_mfma_f32_16x16x32_bf16 v[144:147],a[244:247],v[40:43],v[144:147]
ds_read_b64_tr_b16 a[176:177],v12 offset:22576
v_lshlrev_b32_sdwa v50,1,v50 dst_sel:DWORD dst_unused:UNUSED_PAD src0_sel:DWORD src1_sel:WORD_1
v_mfma_f32_16x16x32_bf16 v[148:151],a[244:247],v[36:39],v[148:151]
ds_read_u16_d16_hi v50,v50
v_lshlrev_b32_sdwa v49,1,v49 dst_sel:DWORD dst_unused:UNUSED_PAD src0_sel:DWORD src1_sel:WORD_1
v_mfma_f32_16x16x32_bf16 v[84:87],a[252:255],v[36:39],v[84:87]
ds_read_u16 v49,v49
v_lshlrev_b32_sdwa v51,1,v51 dst_sel:DWORD dst_unused:UNUSED_PAD src0_sel:DWORD src1_sel:WORD_1
v_mfma_f32_16x16x32_bf16 v[80:83],a[252:255],v[40:43],v[80:83]
ds_read_u16_d16_hi v51,v51
v_mfma_f32_16x16x32_bf16 v[152:155],a[232:235],v[40:43],v[152:155]
ds_read_b64_tr_b16 a[178:179],v12 offset:23600
v_mfma_f32_16x16x32_bf16 v[156:159],a[232:235],v[36:39],v[156:159]
s_waitcnt lgkmcnt(9)
ds_read_b64_tr_b16 a[180:181],v11 offset:22576
v_mfma_f32_16x16x32_bf16 v[100:103],a[240:243],v[36:39],v[100:103]
ds_read_b64_tr_b16 a[182:183],v11 offset:23600
v_mfma_f32_16x16x32_bf16 v[96:99],a[240:243],v[40:43],v[96:99]
ds_read_b64_tr_b16 a[184:185],v11 offset:20528
v_mfma_f32_16x16x32_bf16 v[36:39],a[216:219],a[56:59],v[16:19]
ds_read_b64_tr_b16 a[186:187],v11 offset:21552
v_lshlrev_b32_sdwa v60,1,v60 dst_sel:DWORD dst_unused:UNUSED_PAD src0_sel:DWORD src1_sel:WORD_1
v_mfma_f32_16x16x32_bf16 v[36:39],a[220:223],a[72:75],v[36:39]
ds_read_u16 v60,v60
v_lshl_add_u64 v[28:29],v[28:29],0,v[30:31]
v_fma_mixlo_f16 v8,v53,s99,0
v_mfma_f32_16x16x32_bf16 v[36:39],a[192:195],a[88:91],v[36:39]
ds_read_b64_tr_b16 a[188:189],v11 offset:18480
v_lshl_add_u64 v[30:31],v[24:25],0,v[26:27]
v_lshlrev_b32_sdwa v62,1,v62 dst_sel:DWORD dst_unused:UNUSED_PAD src0_sel:DWORD src1_sel:WORD_1
v_mfma_f32_16x16x32_bf16 v[40:43],a[196:199],a[88:91],v[20:23]
ds_read_u16_d16_hi v62,v62
v_fma_mixhi_f16 v8,v55,s99,0
v_mfma_f32_16x16x32_bf16 v[40:43],a[200:203],a[72:75],v[40:43]
s_waitcnt lgkmcnt(8)
ds_read_b64_tr_b16 a[190:191],v11 offset:19504
v_lshl_add_u64 v[24:25],v[44:45],0,v[46:47]
v_lshlrev_b32_sdwa v61,1,v61 dst_sel:DWORD dst_unused:UNUSED_PAD src0_sel:DWORD src1_sel:WORD_1
v_pk_fma_f16 v9,v8,v8,s54
v_mfma_f32_16x16x32_bf16 v[40:43],a[212:215],a[56:59],v[40:43]
ds_read_u16 v61,v61
v_lshlrev_b32_sdwa v63,1,v63 dst_sel:DWORD dst_unused:UNUSED_PAD src0_sel:DWORD src1_sel:WORD_1
v_mfma_f32_16x16x32_bf16 v[44:47],a[212:215],a[60:63],v[20:23]
ds_read_b128 v[20:23],v4 offset:1408
v_pk_mul_f16 v9,v8,v9
v_mfma_f32_16x16x32_bf16 v[44:47],a[200:203],a[76:79],v[44:47]
ds_read_u16_d16_hi v63,v63
ds_read_b64_tr_b16 a[212:213],v1 offset:16416
v_lshl_add_u64 v[26:27],v[48:49],0,v[50:51]
v_lshlrev_b32_sdwa v32,1,v32 dst_sel:DWORD dst_unused:UNUSED_PAD src0_sel:DWORD src1_sel:WORD_1
v_mfma_f32_16x16x32_bf16 v[44:47],a[196:199],a[92:95],v[44:47]
ds_read_b64_tr_b16 a[214:215],v1 offset:17440
v_exp_f16_e64 v8,v9
v_mfma_f32_16x16x32_bf16 v[48:51],a[192:195],a[92:95],v[16:19]
ds_read_b128 v[16:19],v4 offset:1440
ds_read_u16 v32,v32
v_lshlrev_b32_sdwa v34,1,v34 dst_sel:DWORD dst_unused:UNUSED_PAD src0_sel:DWORD src1_sel:WORD_1
v_mfma_f32_16x16x32_bf16 v[48:51],a[220:223],a[76:79],v[48:51]
TP9680_%=:
s_cbranch_execz TR1_%=
ds_read_u16_d16_hi v34,v34
ds_read_b64_tr_b16 a[200:201],v2 offset:16416
v_lshlrev_b32_sdwa v33,1,v33 dst_sel:DWORD dst_unused:UNUSED_PAD src0_sel:DWORD src1_sel:WORD_1
v_mfma_f32_16x16x32_bf16 v[48:51],a[216:219],a[60:63],v[48:51]
ds_read_u16 v33,v33
v_exp_f16_sdwa v8,v9 dst_sel:WORD_1 dst_unused:UNUSED_PRESERVE src0_sel:WORD_1
v_mfma_f32_16x16x32_bf16 v[112:115],a[228:231],v[28:31],v[112:115]
ds_read_b64_tr_b16 a[202:203],v2 offset:17440
ds_read_b64_tr_b16 a[196:197],v2 offset:14368
v_lshlrev_b32_sdwa v35,1,v35 dst_sel:DWORD dst_unused:UNUSED_PAD src0_sel:DWORD src1_sel:WORD_1
v_mfma_f32_16x16x32_bf16 v[116:119],a[228:231],v[24:27],v[116:119]
ds_read_u16_d16_hi v35,v35
v_lshlrev_b32_sdwa v56,1,v56 dst_sel:DWORD dst_unused:UNUSED_PAD src0_sel:DWORD src1_sel:WORD_1
v_mfma_f32_16x16x32_bf16 v[76:79],a[236:239],v[24:27],v[76:79]
ds_read_b64_tr_b16 a[198:199],v2 offset:15392
v_pk_fma_f16 v8,v8,s55,s55
v_mfma_f32_16x16x32_bf16 v[72:75],a[236:239],v[28:31],v[72:75]
v_rcp_f16 v9,v8
v_lshlrev_b32_sdwa v58,1,v58 dst_sel:DWORD dst_unused:UNUSED_PAD src0_sel:DWORD src1_sel:WORD_1
v_mfma_f32_16x16x32_bf16 v[120:123],a[244:247],v[28:31],v[120:123]
ds_read_u16 v56,v56
v_lshlrev_b32_sdwa v57,1,v57 dst_sel:DWORD dst_unused:UNUSED_PAD src0_sel:DWORD src1_sel:WORD_1
v_mfma_f32_16x16x32_bf16 v[124:127],a[244:247],v[24:27],v[124:127]
ds_read_u16_d16_hi v58,v58
v_lshlrev_b32_sdwa v59,1,v59 dst_sel:DWORD dst_unused:UNUSED_PAD src0_sel:DWORD src1_sel:WORD_1
v_mfma_f32_16x16x32_bf16 v[92:95],a[252:255],v[24:27],v[92:95]
ds_read_u16 v57,v57
v_lshlrev_b32_sdwa v52,1,v52 dst_sel:DWORD dst_unused:UNUSED_PAD src0_sel:DWORD src1_sel:WORD_1
v_mfma_f32_16x16x32_bf16 v[88:91],a[252:255],v[28:31],v[88:91]
ds_read_u16_d16_hi v59,v59
v_rcp_f16_sdwa v9,v8 dst_sel:WORD_1 dst_unused:UNUSED_PRESERVE src0_sel:WORD_1
v_mfma_f32_16x16x32_bf16 v[128:131],a[232:235],v[28:31],v[128:131]
ds_read_b64_tr_b16 a[192:193],v2 offset:12320
v_lshlrev_b32_sdwa v54,1,v54 dst_sel:DWORD dst_unused:UNUSED_PAD src0_sel:DWORD src1_sel:WORD_1
v_mfma_f32_16x16x32_bf16 v[132:135],a[232:235],v[24:27],v[132:135]
ds_read_b64_tr_b16 a[194:195],v2 offset:13344
v_fma_mix_f32 v53,v53,v9,0 op_sel_hi:[0,1,0]
v_mfma_f32_16x16x32_bf16 v[108:111],a[240:243],v[24:27],v[108:111]
ds_read_u16 v52,v52
v_fma_mix_f32 v55,v55,v9,0 op_sel:[0,1,0] op_sel_hi:[0,1,0]
v_mfma_f32_16x16x32_bf16 v[104:107],a[240:243],v[28:31],v[104:107]
s_waitcnt lgkmcnt(7)
ds_read_u16_d16_hi v54,v54
v_mfma_f32_16x16x32_bf16 v[24:27],a[208:211],a[0:3],v[20:23]
v_lshlrev_b32_sdwa v36,1,v36 dst_sel:DWORD dst_unused:UNUSED_PAD src0_sel:DWORD src1_sel:WORD_1
v_lshlrev_b32_sdwa v38,1,v38 dst_sel:DWORD dst_unused:UNUSED_PAD src0_sel:DWORD src1_sel:WORD_1
v_mfma_f32_16x16x32_bf16 v[24:27],a[204:207],a[16:19],v[24:27]
ds_read_u16 v36,v36
v_lshl_add_u64 v[60:61],v[60:61],0,v[62:63]
v_mfma_f32_16x16x32_bf16 v[24:27],a[212:215],a[32:35],v[24:27]
ds_read_u16_d16_hi v38,v38
v_lshl_add_u64 v[62:63],v[32:33],0,v[34:35]
v_mfma_f32_16x16x32_bf16 v[28:31],a[200:203],a[32:35],v[16:19]
v_lshlrev_b32_sdwa v37,1,v37 dst_sel:DWORD dst_unused:UNUSED_PAD src0_sel:DWORD src1_sel:WORD_1
v_lshlrev_b32_sdwa v39,1,v39 dst_sel:DWORD dst_unused:UNUSED_PAD src0_sel:DWORD src1_sel:WORD_1
v_mfma_f32_16x16x32_bf16 v[28:31],a[196:199],a[16:19],v[28:31]
s_waitcnt lgkmcnt(4)
ds_read_u16 v37,v37
v_lshl_add_u64 v[32:33],v[56:57],0,v[58:59]
v_mfma_f32_16x16x32_bf16 v[28:31],a[192:195],a[0:3],v[28:31]
ds_read_u16_d16_hi v39,v39
v_lshlrev_b32_sdwa v40,1,v40 dst_sel:DWORD dst_unused:UNUSED_PAD src0_sel:DWORD src1_sel:WORD_1
v_mfma_f32_16x16x32_bf16 v[56:59],a[192:195],a[4:7],v[16:19]
s_waitcnt lgkmcnt(4)
ds_read_u16 v40,v40
v_or_b32_e64 v34,v52,v54
v_mfma_f32_16x16x32_bf16 v[56:59],a[196:199],a[20:23],v[56:59]
s_waitcnt vmcnt(0)
v_cvt_pk_bf16_f32 v35,v53,v55
v_lshlrev_b32_sdwa v42,1,v42 dst_sel:DWORD dst_unused:UNUSED_PAD src0_sel:DWORD src1_sel:WORD_1
v_mfma_f32_16x16x32_bf16 v[56:59],a[200:203],a[36:39],v[56:59]
s_barrier
ds_read_u16_d16_hi v42,v42
v_lshlrev_b32_sdwa v41,1,v41 dst_sel:DWORD dst_unused:UNUSED_PAD src0_sel:DWORD src1_sel:WORD_1
v_mfma_f32_16x16x32_bf16 v[52:55],a[212:215],a[36:39],v[20:23]
ds_read_u16 v41,v41
v_lshlrev_b32_sdwa v43,1,v43 dst_sel:DWORD dst_unused:UNUSED_PAD src0_sel:DWORD src1_sel:WORD_1
v_mfma_f32_16x16x32_bf16 v[52:55],a[204:207],a[20:23],v[52:55]
ds_read_u16_d16_hi v43,v43
v_lshlrev_b32_sdwa v48,1,v48 dst_sel:DWORD dst_unused:UNUSED_PAD src0_sel:DWORD src1_sel:WORD_1
v_mfma_f32_16x16x32_bf16 v[52:55],a[208:211],a[4:7],v[52:55]
ds_read_u16 v48,v48
v_lshlrev_b32_sdwa v50,1,v50 dst_sel:DWORD dst_unused:UNUSED_PAD src0_sel:DWORD src1_sel:WORD_1
v_mfma_f32_16x16x32_bf16 v[232:235],a[228:231],v[60:63],v[232:235]
ds_read_u16_d16_hi v50,v50
v_lshlrev_b32_sdwa v49,1,v49 dst_sel:DWORD dst_unused:UNUSED_PAD src0_sel:DWORD src1_sel:WORD_1
v_mfma_f32_16x16x32_bf16 v[236:239],a[228:231],v[32:35],v[236:239]
ds_read_u16 v49,v49
v_lshlrev_b32_sdwa v51,1,v51 dst_sel:DWORD dst_unused:UNUSED_PAD src0_sel:DWORD src1_sel:WORD_1
v_mfma_f32_16x16x32_bf16 v[164:167],a[236:239],v[32:35],v[164:167]
ds_read_u16_d16_hi v51,v51
ds_read_b64_tr_b16 a[224:225],v3 offset:18480
v_mfma_f32_16x16x32_bf16 v[160:163],a[236:239],v[60:63],v[160:163]
ds_read_b64_tr_b16 a[226:227],v3 offset:20016
v_lshlrev_b32_sdwa v44,1,v44 dst_sel:DWORD dst_unused:UNUSED_PAD src0_sel:DWORD src1_sel:WORD_1
v_mfma_f32_16x16x32_bf16 v[240:243],a[244:247],v[60:63],v[240:243]
ds_read_u16 v44,v44
v_lshlrev_b32_sdwa v46,1,v46 dst_sel:DWORD dst_unused:UNUSED_PAD src0_sel:DWORD src1_sel:WORD_1
v_mfma_f32_16x16x32_bf16 v[244:247],a[244:247],v[32:35],v[244:247]
ds_read_u16_d16_hi v46,v46
v_lshlrev_b32_sdwa v45,1,v45 dst_sel:DWORD dst_unused:UNUSED_PAD src0_sel:DWORD src1_sel:WORD_1
v_mfma_f32_16x16x32_bf16 v[180:183],a[252:255],v[32:35],v[180:183]
ds_read_u16 v45,v45
v_lshlrev_b32_sdwa v47,1,v47 dst_sel:DWORD dst_unused:UNUSED_PAD src0_sel:DWORD src1_sel:WORD_1
v_mfma_f32_16x16x32_bf16 v[176:179],a[252:255],v[60:63],v[176:179]
ds_read_u16_d16_hi v47,v47
v_mfma_f32_16x16x32_bf16 v[248:251],a[232:235],v[60:63],v[248:251]
ds_read_b64_tr_b16 a[248:249],v3 offset:18488
v_mfma_f32_16x16x32_bf16 v[252:255],a[232:235],v[32:35],v[252:255]
ds_read_b64_tr_b16 a[250:251],v3 offset:20024
v_mfma_f32_16x16x32_bf16 v[196:199],a[240:243],v[32:35],v[196:199]
v_mfma_f32_16x16x32_bf16 v[192:195],a[240:243],v[60:63],v[192:195]
v_mfma_f32_16x16x32_bf16 v[32:35],a[208:211],a[8:11],v[20:23]
ds_read_b64_tr_b16 a[220:221],v1 offset:18480
v_lshlrev_b32_sdwa v24,1,v24 dst_sel:DWORD dst_unused:UNUSED_PAD src0_sel:DWORD src1_sel:WORD_1
v_mfma_f32_16x16x32_bf16 v[32:35],a[204:207],a[24:27],v[32:35]
s_waitcnt lgkmcnt(6)
ds_read_u16 v24,v24
v_lshl_add_u64 v[36:37],v[36:37],0,v[38:39]
v_fma_mixlo_f16 v8,v57,s99,0
v_mfma_f32_16x16x32_bf16 v[32:35],a[212:215],a[40:43],v[32:35]
ds_read_b64_tr_b16 a[222:223],v1 offset:19504
v_lshl_add_u64 v[38:39],v[40:41],0,v[42:43]
v_lshlrev_b32_sdwa v26,1,v26 dst_sel:DWORD dst_unused:UNUSED_PAD src0_sel:DWORD src1_sel:WORD_1
v_mfma_f32_16x16x32_bf16 v[60:63],a[200:203],a[40:43],v[16:19]
ds_read_u16_d16_hi v26,v26
v_fma_mixhi_f16 v8,v59,s99,0
v_mfma_f32_16x16x32_bf16 v[60:63],a[196:199],a[24:27],v[60:63]
ds_read_b64_tr_b16 a[216:217],v1 offset:20528
v_lshl_add_u64 v[40:41],v[48:49],0,v[50:51]
v_lshlrev_b32_sdwa v25,1,v25 dst_sel:DWORD dst_unused:UNUSED_PAD src0_sel:DWORD src1_sel:WORD_1
v_pk_fma_f16 v9,v8,v8,s54
v_mfma_f32_16x16x32_bf16 v[60:63],a[192:195],a[8:11],v[60:63]
ds_read_u16 v25,v25
v_lshlrev_b32_sdwa v27,1,v27 dst_sel:DWORD dst_unused:UNUSED_PAD src0_sel:DWORD src1_sel:WORD_1
v_mfma_f32_16x16x32_bf16 v[48:51],a[192:195],a[12:15],v[16:19]
ds_read_u16_d16_hi v27,v27
v_pk_mul_f16 v9,v8,v9
v_mfma_f32_16x16x32_bf16 v[48:51],a[196:199],a[28:31],v[48:51]
s_waitcnt lgkmcnt(7)
ds_read_b64_tr_b16 a[218:219],v1 offset:21552
v_lshl_add_u64 v[42:43],v[44:45],0,v[46:47]
v_lshlrev_b32_sdwa v28,1,v28 dst_sel:DWORD dst_unused:UNUSED_PAD src0_sel:DWORD src1_sel:WORD_1
v_mfma_f32_16x16x32_bf16 v[48:51],a[200:203],a[44:47],v[48:51]
v_exp_f16 v8,v9
v_mfma_f32_16x16x32_bf16 v[44:47],a[212:215],a[44:47],v[20:23]
ds_read_u16 v28,v28
v_lshlrev_b32_sdwa v30,1,v30 dst_sel:DWORD dst_unused:UNUSED_PAD src0_sel:DWORD src1_sel:WORD_1
v_mfma_f32_16x16x32_bf16 v[44:47],a[204:207],a[28:31],v[44:47]
ds_read_u16_d16_hi v30,v30
v_lshlrev_b32_sdwa v29,1,v29 dst_sel:DWORD dst_unused:UNUSED_PAD src0_sel:DWORD src1_sel:WORD_1
v_mfma_f32_16x16x32_bf16 v[44:47],a[208:211],a[12:15],v[44:47]
ds_read_b64_tr_b16 a[0:1],v3 offset:38992
ds_read_b64_tr_b16 a[2:3],v3 offset:40528
v_exp_f16_sdwa v8,v9 dst_sel:WORD_1 dst_unused:UNUSED_PRESERVE src0_sel:WORD_1
v_mfma_f32_16x16x32_bf16 v[208:211],a[228:231],v[36:39],v[208:211]
ds_read_b64_tr_b16 a[4:5],v3 offset:45152
ds_read_b64_tr_b16 a[6:7],v3 offset:46688
v_lshlrev_b32_sdwa v31,1,v31 dst_sel:DWORD dst_unused:UNUSED_PAD src0_sel:DWORD src1_sel:WORD_1
v_mfma_f32_16x16x32_bf16 v[212:215],a[228:231],v[40:43],v[212:215]
ds_read_u16 v29,v29
ds_read_u16_d16_hi v31,v31
v_lshlrev_b32_sdwa v52,1,v52 dst_sel:DWORD dst_unused:UNUSED_PAD src0_sel:DWORD src1_sel:WORD_1
v_mfma_f32_16x16x32_bf16 v[172:175],a[236:239],v[40:43],v[172:175]
ds_read_u16 v52,v52
ds_read_b64_tr_b16 a[8:9],v3 offset:45160
v_pk_fma_f16 v8,v8,s55,s55
v_mfma_f32_16x16x32_bf16 v[168:171],a[236:239],v[36:39],v[168:171]
ds_read_b64_tr_b16 a[10:11],v3 offset:46696
v_rcp_f16 v9,v8
v_lshlrev_b32_sdwa v54,1,v54 dst_sel:DWORD dst_unused:UNUSED_PAD src0_sel:DWORD src1_sel:WORD_1
v_mfma_f32_16x16x32_bf16 v[216:219],a[244:247],v[36:39],v[216:219]
ds_read_u16_d16_hi v54,v54
ds_read_b64_tr_b16 a[12:13],v3 offset:39000
v_mfma_f32_16x16x32_bf16 v[220:223],a[244:247],v[40:43],v[220:223]
ds_read_b64_tr_b16 a[14:15],v3 offset:40536
ds_read_b64_tr_b16 a[228:229],v3 offset:18544
v_lshlrev_b32_sdwa v53,1,v53 dst_sel:DWORD dst_unused:UNUSED_PAD src0_sel:DWORD src1_sel:WORD_1
v_mfma_f32_16x16x32_bf16 v[188:191],a[252:255],v[40:43],v[188:191]
ds_read_u16 v53,v53
ds_read_b64_tr_b16 a[230:231],v3 offset:20080
v_lshlrev_b32_sdwa v55,1,v55 dst_sel:DWORD dst_unused:UNUSED_PAD src0_sel:DWORD src1_sel:WORD_1
v_mfma_f32_16x16x32_bf16 v[184:187],a[252:255],v[36:39],v[184:187]
ds_read_u16_d16_hi v55,v55
v_rcp_f16_sdwa v9,v8 dst_sel:WORD_1 dst_unused:UNUSED_PRESERVE src0_sel:WORD_1
v_mfma_f32_16x16x32_bf16 v[224:227],a[232:235],v[36:39],v[224:227]
ds_read_b64_tr_b16 a[236:237],v3 offset:18552
ds_read_b64_tr_b16 a[238:239],v3 offset:20088
v_lshlrev_b32_sdwa v56,1,v56 dst_sel:DWORD dst_unused:UNUSED_PAD src0_sel:DWORD src1_sel:WORD_1
v_mfma_f32_16x16x32_bf16 v[228:231],a[232:235],v[40:43],v[228:231]
ds_read_u16 v56,v56
ds_read_b64_tr_b16 a[244:245],v3 offset:18608
v_lshlrev_b32_sdwa v58,1,v58 dst_sel:DWORD dst_unused:UNUSED_PAD src0_sel:DWORD src1_sel:WORD_1
v_mfma_f32_16x16x32_bf16 v[204:207],a[240:243],v[40:43],v[204:207]
ds_read_u16_d16_hi v58,v58
ds_read_b64_tr_b16 a[246:247],v3 offset:20144
v_fma_mix_f32 v57,v57,v9,0 op_sel_hi:[0,1,0]
v_mfma_f32_16x16x32_bf16 v[200:203],a[240:243],v[36:39],v[200:203]
ds_read_b64_tr_b16 a[252:253],v3 offset:18616
ds_read_b64_tr_b16 a[254:255],v3 offset:20152
v_fma_mix_f32 v59,v59,v9,0 op_sel:[0,1,0] op_sel_hi:[0,1,0]
v_mfma_f32_16x16x32_bf16 v[40:43],a[208:211],a[48:51],v[20:23]
ds_read_b64_tr_b16 a[16:17],v3 offset:39056
ds_read_b64_tr_b16 a[18:19],v3 offset:40592
v_lshlrev_b32_sdwa v32,1,v32 dst_sel:DWORD dst_unused:UNUSED_PAD src0_sel:DWORD src1_sel:WORD_1
v_mfma_f32_16x16x32_bf16 v[40:43],a[204:207],a[64:67],v[40:43]
s_waitcnt lgkmcnt(15)
ds_read_u16 v32,v32
ds_read_b64_tr_b16 a[20:21],v3 offset:45216
v_lshl_add_u64 v[24:25],v[24:25],0,v[26:27]
v_mfma_f32_16x16x32_bf16 v[40:43],a[212:215],a[80:83],v[40:43]
ds_read_b64_tr_b16 a[22:23],v3 offset:46752
ds_read_b64_tr_b16 a[24:25],v3 offset:45224
v_lshl_add_u64 v[26:27],v[28:29],0,v[30:31]
v_lshlrev_b32_sdwa v34,1,v34 dst_sel:DWORD dst_unused:UNUSED_PAD src0_sel:DWORD src1_sel:WORD_1
v_mfma_f32_16x16x32_bf16 v[36:39],a[200:203],a[80:83],v[16:19]
ds_read_u16_d16_hi v34,v34
v_lshlrev_b32_sdwa v33,1,v33 dst_sel:DWORD dst_unused:UNUSED_PAD src0_sel:DWORD src1_sel:WORD_1
v_mfma_f32_16x16x32_bf16 v[36:39],a[196:199],a[64:67],v[36:39]
s_waitcnt lgkmcnt(15)
ds_read_u16 v33,v33
v_lshl_add_u64 v[28:29],v[52:53],0,v[54:55]
v_mfma_f32_16x16x32_bf16 v[36:39],a[192:195],a[48:51],v[36:39]
ds_read_b64_tr_b16 a[26:27],v3 offset:46760
v_lshlrev_b32_sdwa v35,1,v35 dst_sel:DWORD dst_unused:UNUSED_PAD src0_sel:DWORD src1_sel:WORD_1
v_mfma_f32_16x16x32_bf16 v[52:55],a[192:195],a[52:55],v[16:19]
s_waitcnt lgkmcnt(12)
ds_read_u16_d16_hi v35,v35
v_or_b32_e64 v30,v56,v58
v_lshlrev_b32_sdwa v60,1,v60 dst_sel:DWORD dst_unused:UNUSED_PAD src0_sel:DWORD src1_sel:WORD_1
v_mfma_f32_16x16x32_bf16 v[52:55],a[196:199],a[68:71],v[52:55]
ds_read_u16 v60,v60
ds_read_b64_tr_b16 a[28:29],v3 offset:39064
v_cvt_pk_bf16_f32 v31,v57,v59
v_lshlrev_b32_sdwa v62,1,v62 dst_sel:DWORD dst_unused:UNUSED_PAD src0_sel:DWORD src1_sel:WORD_1
v_mfma_f32_16x16x32_bf16 v[52:55],a[200:203],a[84:87],v[52:55]
ds_read_u16_d16_hi v62,v62
v_lshlrev_b32_sdwa v61,1,v61 dst_sel:DWORD dst_unused:UNUSED_PAD src0_sel:DWORD src1_sel:WORD_1
v_mfma_f32_16x16x32_bf16 v[56:59],a[212:215],a[84:87],v[20:23]
ds_read_u16 v61,v61
v_lshlrev_b32_sdwa v63,1,v63 dst_sel:DWORD dst_unused:UNUSED_PAD src0_sel:DWORD src1_sel:WORD_1
v_mfma_f32_16x16x32_bf16 v[56:59],a[204:207],a[68:71],v[56:59]
ds_read_u16_d16_hi v63,v63
v_lshlrev_b32_sdwa v44,1,v44 dst_sel:DWORD dst_unused:UNUSED_PAD src0_sel:DWORD src1_sel:WORD_1
v_mfma_f32_16x16x32_bf16 v[56:59],a[208:211],a[52:55],v[56:59]
ds_read_u16 v44,v44
v_lshlrev_b32_sdwa v46,1,v46 dst_sel:DWORD dst_unused:UNUSED_PAD src0_sel:DWORD src1_sel:WORD_1
v_mfma_f32_16x16x32_bf16 v[136:139],a[224:227],v[24:27],v[136:139]
ds_read_u16_d16_hi v46,v46
v_lshlrev_b32_sdwa v45,1,v45 dst_sel:DWORD dst_unused:UNUSED_PAD src0_sel:DWORD src1_sel:WORD_1
v_mfma_f32_16x16x32_bf16 v[140:143],a[224:227],v[28:31],v[140:143]
ds_read_u16 v45,v45
v_lshlrev_b32_sdwa v47,1,v47 dst_sel:DWORD dst_unused:UNUSED_PAD src0_sel:DWORD src1_sel:WORD_1
v_mfma_f32_16x16x32_bf16 v[68:71],a[248:251],v[28:31],v[68:71]
ds_read_u16_d16_hi v47,v47
v_lshlrev_b32_sdwa v48,1,v48 dst_sel:DWORD dst_unused:UNUSED_PAD src0_sel:DWORD src1_sel:WORD_1
v_mfma_f32_16x16x32_bf16 v[64:67],a[248:251],v[24:27],v[64:67]
ds_read_u16 v48,v48
ds_read_b64_tr_b16 a[30:31],v3 offset:40600
v_mfma_f32_16x16x32_bf16 v[144:147],a[228:231],v[24:27],v[144:147]
ds_read_b64_tr_b16 a[32:33],v3 offset:39120
v_lshlrev_b32_sdwa v50,1,v50 dst_sel:DWORD dst_unused:UNUSED_PAD src0_sel:DWORD src1_sel:WORD_1
v_mfma_f32_16x16x32_bf16 v[148:151],a[228:231],v[28:31],v[148:151]
ds_read_u16_d16_hi v50,v50
v_lshlrev_b32_sdwa v49,1,v49 dst_sel:DWORD dst_unused:UNUSED_PAD src0_sel:DWORD src1_sel:WORD_1
v_mfma_f32_16x16x32_bf16 v[84:87],a[236:239],v[28:31],v[84:87]
ds_read_u16 v49,v49
v_lshlrev_b32_sdwa v51,1,v51 dst_sel:DWORD dst_unused:UNUSED_PAD src0_sel:DWORD src1_sel:WORD_1
v_mfma_f32_16x16x32_bf16 v[80:83],a[236:239],v[24:27],v[80:83]
s_waitcnt lgkmcnt(9)
ds_read_u16_d16_hi v51,v51
v_mfma_f32_16x16x32_bf16 v[152:155],a[244:247],v[24:27],v[152:155]
s_nop 0
ds_read_b64_tr_b16 a[34:35],v3 offset:40656
v_mfma_f32_16x16x32_bf16 v[156:159],a[244:247],v[28:31],v[156:159]
ds_read_b64_tr_b16 a[36:37],v3 offset:45280
v_mfma_f32_16x16x32_bf16 v[100:103],a[252:255],v[28:31],v[100:103]
ds_read_b64_tr_b16 a[38:39],v3 offset:46816
v_mfma_f32_16x16x32_bf16 v[96:99],a[252:255],v[24:27],v[96:99]
ds_read_b64_tr_b16 a[40:41],v3 offset:45288
v_mfma_f32_16x16x32_bf16 v[28:31],a[208:211],a[56:59],v[20:23]
ds_read_b64_tr_b16 a[42:43],v3 offset:46824
v_mul_f32_dpp v136,v14,v136 row_newbcast:0 row_mask:0xf bank_mask:0xf
s_add_u32 s8,s61,3145728
v_lshlrev_b32_sdwa v40,1,v40 dst_sel:DWORD dst_unused:UNUSED_PAD src0_sel:DWORD src1_sel:WORD_1
v_mfma_f32_16x16x32_bf16 v[28:31],a[204:207],a[72:75],v[28:31]
ds_read_u16 v40,v40
v_lshl_add_u64 v[32:33],v[32:33],0,v[34:35]
v_mul_f32_dpp v137,v14,v137 row_newbcast:1 row_mask:0xf bank_mask:0xf
v_mul_f32_dpp v138,v14,v138 row_newbcast:2 row_mask:0xf bank_mask:0xf
v_mfma_f32_16x16x32_bf16 v[28:31],a[212:215],a[88:91],v[28:31]
ds_read_b64_tr_b16 a[44:45],v3 offset:39128
v_lshl_add_u64 v[34:35],v[60:61],0,v[62:63]
v_mul_f32_dpp v139,v14,v139 row_newbcast:3 row_mask:0xf bank_mask:0xf
v_lshlrev_b32_sdwa v42,1,v42 dst_sel:DWORD dst_unused:UNUSED_PAD src0_sel:DWORD src1_sel:WORD_1
v_mfma_f32_16x16x32_bf16 v[24:27],a[200:203],a[88:91],v[16:19]
ds_read_u16_d16_hi v42,v42
v_mul_f32_dpp v64,v14,v64 row_newbcast:4 row_mask:0xf bank_mask:0xf
v_lshlrev_b32_sdwa v41,1,v41 dst_sel:DWORD dst_unused:UNUSED_PAD src0_sel:DWORD src1_sel:WORD_1
v_mul_f32_dpp v65,v14,v65 row_newbcast:5 row_mask:0xf bank_mask:0xf
v_mfma_f32_16x16x32_bf16 v[24:27],a[196:199],a[72:75],v[24:27]
s_waitcnt lgkmcnt(8)
ds_read_u16 v41,v41
v_lshl_add_u64 v[60:61],v[44:45],0,v[46:47]
v_mul_f32_dpp v66,v14,v66 row_newbcast:6 row_mask:0xf bank_mask:0xf
v_mfma_f32_16x16x32_bf16 v[24:27],a[192:195],a[56:59],v[24:27]
s_nop 0
ds_read_b64_tr_b16 a[46:47],v3 offset:40664
v_mul_f32_dpp v67,v14,v67 row_newbcast:7 row_mask:0xf bank_mask:0xf
v_lshlrev_b32_sdwa v43,1,v43 dst_sel:DWORD dst_unused:UNUSED_PAD src0_sel:DWORD src1_sel:WORD_1
v_cvt_pk_bf16_f32 v136,v136,v137
v_mfma_f32_16x16x32_bf16 v[44:47],a[192:195],a[60:63],v[16:19]
ds_read_b128 v[16:19],v4 offset:0
v_cvt_pk_bf16_f32 v137,v138,v139
v_lshlrev_b32_sdwa v36,1,v36 dst_sel:DWORD dst_unused:UNUSED_PAD src0_sel:DWORD src1_sel:WORD_1
v_mfma_f32_16x16x32_bf16 v[44:47],a[196:199],a[76:79],v[44:47]
ds_read_u16_d16_hi v43,v43
ds_read_b64_tr_b16 a[192:193],v1 offset:22576
v_lshl_add_u64 v[62:63],v[48:49],0,v[50:51]
v_cvt_pk_bf16_f32 v138,v64,v65
v_cvt_pk_bf16_f32 v139,v66,v67
v_lshlrev_b32_sdwa v38,1,v38 dst_sel:DWORD dst_unused:UNUSED_PAD src0_sel:DWORD src1_sel:WORD_1
v_mfma_f32_16x16x32_bf16 v[44:47],a[200:203],a[92:95],v[44:47]
buffer_store_dwordx4 v[136:139],v7,s[40:43],s8 offen offset:0 sc1
ds_read_b64_tr_b16 a[194:195],v1 offset:23600
v_mul_f32_dpp v144,v14,v144 row_newbcast:8 row_mask:0xf bank_mask:0xf
v_mfma_f32_16x16x32_bf16 v[48:51],a[212:215],a[92:95],v[20:23]
ds_read_b128 v[20:23],v4 offset:32
ds_read_u16 v36,v36
v_mul_f32_dpp v145,v14,v145 row_newbcast:9 row_mask:0xf bank_mask:0xf
v_lshlrev_b32_sdwa v37,1,v37 dst_sel:DWORD dst_unused:UNUSED_PAD src0_sel:DWORD src1_sel:WORD_1
v_mul_f32_dpp v146,v14,v146 row_newbcast:10 row_mask:0xf bank_mask:0xf
v_mfma_f32_16x16x32_bf16 v[48:51],a[204:207],a[76:79],v[48:51]
ds_read_u16_d16_hi v38,v38
ds_read_u16 v37,v37
v_mul_f32_dpp v147,v14,v147 row_newbcast:11 row_mask:0xf bank_mask:0xf
v_lshlrev_b32_sdwa v39,1,v39 dst_sel:DWORD dst_unused:UNUSED_PAD src0_sel:DWORD src1_sel:WORD_1
v_mfma_f32_16x16x32_bf16 v[48:51],a[208:211],a[60:63],v[48:51]
ds_read_u16_d16_hi v39,v39
ds_read_b64_tr_b16 a[196:197],v2 offset:22576
v_mul_f32_dpp v80,v14,v80 row_newbcast:12 row_mask:0xf bank_mask:0xf
v_mul_f32_dpp v81,v14,v81 row_newbcast:13 row_mask:0xf bank_mask:0xf
v_mfma_f32_16x16x32_bf16 v[112:115],a[224:227],v[32:35],v[112:115]
ds_read_b64_tr_b16 a[198:199],v2 offset:23600
ds_read_b64_tr_b16 a[200:201],v2 offset:20528
v_lshlrev_b32_sdwa v56,1,v56 dst_sel:DWORD dst_unused:UNUSED_PAD src0_sel:DWORD src1_sel:WORD_1
v_mul_f32_dpp v82,v14,v82 row_newbcast:14 row_mask:0xf bank_mask:0xf
v_mfma_f32_16x16x32_bf16 v[116:119],a[224:227],v[60:63],v[116:119]
ds_read_b64_tr_b16 a[202:203],v2 offset:21552
v_mul_f32_dpp v83,v14,v83 row_newbcast:15 row_mask:0xf bank_mask:0xf
v_lshlrev_b32_sdwa v58,1,v58 dst_sel:DWORD dst_unused:UNUSED_PAD src0_sel:DWORD src1_sel:WORD_1
v_cvt_pk_bf16_f32 v144,v144,v145
v_mfma_f32_16x16x32_bf16 v[76:79],a[248:251],v[60:63],v[76:79]
ds_read_u16 v56,v56
v_cvt_pk_bf16_f32 v145,v146,v147
v_lshlrev_b32_sdwa v57,1,v57 dst_sel:DWORD dst_unused:UNUSED_PAD src0_sel:DWORD src1_sel:WORD_1
v_mfma_f32_16x16x32_bf16 v[72:75],a[248:251],v[32:35],v[72:75]
ds_read_u16_d16_hi v58,v58
v_cvt_pk_bf16_f32 v146,v80,v81
v_cvt_pk_bf16_f32 v147,v82,v83
v_mfma_f32_16x16x32_bf16 v[120:123],a[228:231],v[32:35],v[120:123]
ds_read_u16 v57,v57
v_mul_f32_dpp v152,v15,v152 row_newbcast:0 row_mask:0xf bank_mask:0xf
v_lshlrev_b32_sdwa v59,1,v59 dst_sel:DWORD dst_unused:UNUSED_PAD src0_sel:DWORD src1_sel:WORD_1
v_mfma_f32_16x16x32_bf16 v[124:127],a[228:231],v[60:63],v[124:127]
ds_read_u16_d16_hi v59,v59
v_mul_f32_dpp v153,v15,v153 row_newbcast:1 row_mask:0xf bank_mask:0xf
v_lshlrev_b32_sdwa v52,1,v52 dst_sel:DWORD dst_unused:UNUSED_PAD src0_sel:DWORD src1_sel:WORD_1
v_mul_f32_dpp v154,v15,v154 row_newbcast:2 row_mask:0xf bank_mask:0xf
v_mfma_f32_16x16x32_bf16 v[92:95],a[236:239],v[60:63],v[92:95]
buffer_store_dwordx4 v[144:147],v7,s[40:43],s8 offen offset:64 sc1
v_mul_f32_dpp v155,v15,v155 row_newbcast:3 row_mask:0xf bank_mask:0xf
v_mfma_f32_16x16x32_bf16 v[88:91],a[236:239],v[32:35],v[88:91]
ds_read_b64_tr_b16 a[212:213],v2 offset:18480
v_mul_f32_dpp v96,v15,v96 row_newbcast:4 row_mask:0xf bank_mask:0xf
v_lshlrev_b32_sdwa v54,1,v54 dst_sel:DWORD dst_unused:UNUSED_PAD src0_sel:DWORD src1_sel:WORD_1
v_mul_f32_dpp v97,v15,v97 row_newbcast:5 row_mask:0xf bank_mask:0xf
v_mfma_f32_16x16x32_bf16 v[128:131],a[244:247],v[32:35],v[128:131]
ds_read_b64_tr_b16 a[214:215],v2 offset:19504
v_mul_f32_dpp v98,v15,v98 row_newbcast:6 row_mask:0xf bank_mask:0xf
v_lshlrev_b32_sdwa v53,1,v53 dst_sel:DWORD dst_unused:UNUSED_PAD src0_sel:DWORD src1_sel:WORD_1
v_mfma_f32_16x16x32_bf16 v[132:135],a[244:247],v[60:63],v[132:135]
ds_read_u16 v52,v52
v_mul_f32_dpp v99,v15,v99 row_newbcast:7 row_mask:0xf bank_mask:0xf
v_cvt_pk_bf16_f32 v152,v152,v153
v_lshlrev_b32_sdwa v55,1,v55 dst_sel:DWORD dst_unused:UNUSED_PAD src0_sel:DWORD src1_sel:WORD_1
v_mfma_f32_16x16x32_bf16 v[108:111],a[252:255],v[60:63],v[108:111]
s_nop 0
ds_read_u16_d16_hi v54,v54
v_cvt_pk_bf16_f32 v153,v154,v155
v_mfma_f32_16x16x32_bf16 v[104:107],a[252:255],v[32:35],v[104:107]
s_waitcnt lgkmcnt(7)
ds_read_u16 v53,v53
ds_read_u16_d16_hi v55,v55
v_cvt_pk_bf16_f32 v154,v96,v97
v_cvt_pk_bf16_f32 v155,v98,v99
v_mfma_f32_16x16x32_bf16 v[60:63],a[220:223],a[48:51],v[16:19]
v_mul_f32_dpp v140,v14,v140 row_newbcast:0 row_mask:0xf bank_mask:0xf
v_lshlrev_b32_sdwa v28,1,v28 dst_sel:DWORD dst_unused:UNUSED_PAD src0_sel:DWORD src1_sel:WORD_1
v_mfma_f32_16x16x32_bf16 v[60:63],a[216:219],a[64:67],v[60:63]
ds_read_u16 v28,v28
v_lshl_add_u64 v[40:41],v[40:41],0,v[42:43]
v_mul_f32_dpp v141,v14,v141 row_newbcast:1 row_mask:0xf bank_mask:0xf
v_mul_f32_dpp v142,v14,v142 row_newbcast:2 row_mask:0xf bank_mask:0xf
v_mfma_f32_16x16x32_bf16 v[60:63],a[192:195],a[80:83],v[60:63]
v_lshl_add_u64 v[42:43],v[36:37],0,v[38:39]
v_mul_f32_dpp v143,v14,v143 row_newbcast:3 row_mask:0xf bank_mask:0xf
v_lshlrev_b32_sdwa v30,1,v30 dst_sel:DWORD dst_unused:UNUSED_PAD src0_sel:DWORD src1_sel:WORD_1
v_mfma_f32_16x16x32_bf16 v[32:35],a[196:199],a[80:83],v[20:23]
ds_read_u16_d16_hi v30,v30
v_mul_f32_dpp v68,v14,v68 row_newbcast:4 row_mask:0xf bank_mask:0xf
v_lshlrev_b32_sdwa v29,1,v29 dst_sel:DWORD dst_unused:UNUSED_PAD src0_sel:DWORD src1_sel:WORD_1
v_mul_f32_dpp v69,v14,v69 row_newbcast:5 row_mask:0xf bank_mask:0xf
v_mfma_f32_16x16x32_bf16 v[32:35],a[200:203],a[64:67],v[32:35]
s_waitcnt lgkmcnt(6)
ds_read_u16 v29,v29
v_lshl_add_u64 v[36:37],v[56:57],0,v[58:59]
v_mul_f32_dpp v70,v14,v70 row_newbcast:6 row_mask:0xf bank_mask:0xf
v_mfma_f32_16x16x32_bf16 v[32:35],a[212:215],a[48:51],v[32:35]
buffer_store_dwordx4 v[152:155],v7,s[40:43],s8 offen offset:128 sc1
v_mul_f32_dpp v71,v14,v71 row_newbcast:7 row_mask:0xf bank_mask:0xf
v_lshlrev_b32_sdwa v31,1,v31 dst_sel:DWORD dst_unused:UNUSED_PAD src0_sel:DWORD src1_sel:WORD_1
v_cvt_pk_bf16_f32 v140,v140,v141
v_mfma_f32_16x16x32_bf16 v[56:59],a[212:215],a[52:55],v[20:23]
ds_read_u16_d16_hi v31,v31
v_cvt_pk_bf16_f32 v141,v142,v143
v_lshlrev_b32_sdwa v24,1,v24 dst_sel:DWORD dst_unused:UNUSED_PAD src0_sel:DWORD src1_sel:WORD_1
v_mfma_f32_16x16x32_bf16 v[56:59],a[200:203],a[68:71],v[56:59]
s_waitcnt lgkmcnt(4)
ds_read_u16 v24,v24
v_lshl_add_u64 v[38:39],v[52:53],0,v[54:55]
v_cvt_pk_bf16_f32 v142,v68,v69
v_cvt_pk_bf16_f32 v143,v70,v71
v_lshlrev_b32_sdwa v26,1,v26 dst_sel:DWORD dst_unused:UNUSED_PAD src0_sel:DWORD src1_sel:WORD_1
v_mfma_f32_16x16x32_bf16 v[56:59],a[196:199],a[84:87],v[56:59]
ds_read_u16_d16_hi v26,v26
v_mul_f32_dpp v148,v14,v148 row_newbcast:8 row_mask:0xf bank_mask:0xf
v_mfma_f32_16x16x32_bf16 v[52:55],a[192:195],a[84:87],v[16:19]
ds_read_b64_tr_b16 a[232:233],v3 offset:32832
v_mul_f32_dpp v149,v14,v149 row_newbcast:9 row_mask:0xf bank_mask:0xf
v_lshlrev_b32_sdwa v25,1,v25 dst_sel:DWORD dst_unused:UNUSED_PAD src0_sel:DWORD src1_sel:WORD_1
v_mul_f32_dpp v150,v14,v150 row_newbcast:10 row_mask:0xf bank_mask:0xf
v_mfma_f32_16x16x32_bf16 v[52:55],a[216:219],a[68:71],v[52:55]
ds_read_u16 v25,v25
v_mul_f32_dpp v151,v14,v151 row_newbcast:11 row_mask:0xf bank_mask:0xf
v_lshlrev_b32_sdwa v27,1,v27 dst_sel:DWORD dst_unused:UNUSED_PAD src0_sel:DWORD src1_sel:WORD_1
v_mfma_f32_16x16x32_bf16 v[52:55],a[220:223],a[52:55],v[52:55]
ds_read_u16_d16_hi v27,v27
v_mul_f32_dpp v84,v14,v84 row_newbcast:12 row_mask:0xf bank_mask:0xf
v_mul_f32_dpp v85,v14,v85 row_newbcast:13 row_mask:0xf bank_mask:0xf
v_mfma_f32_16x16x32_bf16 v[232:235],a[224:227],v[40:43],v[232:235]
ds_read_b64_tr_b16 a[234:235],v3 offset:34368
v_lshlrev_b32_sdwa v48,1,v48 dst_sel:DWORD dst_unused:UNUSED_PAD src0_sel:DWORD src1_sel:WORD_1
v_mul_f32_dpp v86,v14,v86 row_newbcast:14 row_mask:0xf bank_mask:0xf
v_mfma_f32_16x16x32_bf16 v[236:239],a[224:227],v[36:39],v[236:239]
ds_read_u16 v48,v48
v_mul_f32_dpp v87,v14,v87 row_newbcast:15 row_mask:0xf bank_mask:0xf
v_lshlrev_b32_sdwa v50,1,v50 dst_sel:DWORD dst_unused:UNUSED_PAD src0_sel:DWORD src1_sel:WORD_1
v_cvt_pk_bf16_f32 v148,v148,v149
v_mfma_f32_16x16x32_bf16 v[164:167],a[248:251],v[36:39],v[164:167]
ds_read_u16_d16_hi v50,v50
v_cvt_pk_bf16_f32 v149,v150,v151
v_lshlrev_b32_sdwa v49,1,v49 dst_sel:DWORD dst_unused:UNUSED_PAD src0_sel:DWORD src1_sel:WORD_1
v_mfma_f32_16x16x32_bf16 v[160:163],a[248:251],v[40:43],v[160:163]
ds_read_u16 v49,v49
v_cvt_pk_bf16_f32 v150,v84,v85
v_cvt_pk_bf16_f32 v151,v86,v87
v_mfma_f32_16x16x32_bf16 v[240:243],a[228:231],v[40:43],v[240:243]
buffer_store_dwordx4 v[140:143],v7,s[40:43],s8 offen offset:192 sc1
v_mul_f32_dpp v156,v15,v156 row_newbcast:0 row_mask:0xf bank_mask:0xf
v_lshlrev_b32_sdwa v51,1,v51 dst_sel:DWORD dst_unused:UNUSED_PAD src0_sel:DWORD src1_sel:WORD_1
v_mfma_f32_16x16x32_bf16 v[244:247],a[228:231],v[36:39],v[244:247]
ds_read_u16_d16_hi v51,v51
v_mul_f32_dpp v157,v15,v157 row_newbcast:1 row_mask:0xf bank_mask:0xf
v_lshlrev_b32_sdwa v44,1,v44 dst_sel:DWORD dst_unused:UNUSED_PAD src0_sel:DWORD src1_sel:WORD_1
v_mul_f32_dpp v158,v15,v158 row_newbcast:2 row_mask:0xf bank_mask:0xf
v_mfma_f32_16x16x32_bf16 v[180:183],a[236:239],v[36:39],v[180:183]
ds_read_u16 v44,v44
v_mul_f32_dpp v159,v15,v159 row_newbcast:3 row_mask:0xf bank_mask:0xf
v_mfma_f32_16x16x32_bf16 v[176:179],a[236:239],v[40:43],v[176:179]
ds_read_b64_tr_b16 a[240:241],v3 offset:32840
v_mul_f32_dpp v100,v15,v100 row_newbcast:4 row_mask:0xf bank_mask:0xf
v_lshlrev_b32_sdwa v46,1,v46 dst_sel:DWORD dst_unused:UNUSED_PAD src0_sel:DWORD src1_sel:WORD_1
v_mul_f32_dpp v101,v15,v101 row_newbcast:5 row_mask:0xf bank_mask:0xf
v_mfma_f32_16x16x32_bf16 v[248:251],a[244:247],v[40:43],v[248:251]
ds_read_u16_d16_hi v46,v46
v_mul_f32_dpp v102,v15,v102 row_newbcast:6 row_mask:0xf bank_mask:0xf
v_lshlrev_b32_sdwa v45,1,v45 dst_sel:DWORD dst_unused:UNUSED_PAD src0_sel:DWORD src1_sel:WORD_1
v_mfma_f32_16x16x32_bf16 v[252:255],a[244:247],v[36:39],v[252:255]
ds_read_u16 v45,v45
v_mul_f32_dpp v103,v15,v103 row_newbcast:7 row_mask:0xf bank_mask:0xf
v_cvt_pk_bf16_f32 v156,v156,v157
v_lshlrev_b32_sdwa v47,1,v47 dst_sel:DWORD dst_unused:UNUSED_PAD src0_sel:DWORD src1_sel:WORD_1
v_mfma_f32_16x16x32_bf16 v[196:199],a[252:255],v[36:39],v[196:199]
ds_read_u16_d16_hi v47,v47
v_cvt_pk_bf16_f32 v157,v158,v159
v_mfma_f32_16x16x32_bf16 v[192:195],a[252:255],v[40:43],v[192:195]
ds_read_b64_tr_b16 a[242:243],v3 offset:34376
v_cvt_pk_bf16_f32 v158,v100,v101
v_cvt_pk_bf16_f32 v159,v102,v103
v_mfma_f32_16x16x32_bf16 v[36:39],a[220:223],a[56:59],v[16:19]
s_nop 0
ds_read_b128 v[64:67],v4 offset:128
ds_read_b128 v[68:71],v4 offset:160
v_mul_f32_dpp v112,v14,v112 row_newbcast:0 row_mask:0xf bank_mask:0xf
s_add_u32 s9,s61,3244032
v_lshlrev_b32_sdwa v60,1,v60 dst_sel:DWORD dst_unused:UNUSED_PAD src0_sel:DWORD src1_sel:WORD_1
v_mfma_f32_16x16x32_bf16 v[36:39],a[216:219],a[72:75],v[36:39]
s_waitcnt lgkmcnt(9)
ds_read_u16 v60,v60
ds_read_b128 v[80:83],v4 offset:256
v_lshl_add_u64 v[28:29],v[28:29],0,v[30:31]
v_mul_f32_dpp v113,v14,v113 row_newbcast:1 row_mask:0xf bank_mask:0xf
v_mul_f32_dpp v114,v14,v114 row_newbcast:2 row_mask:0xf bank_mask:0xf
v_mfma_f32_16x16x32_bf16 v[36:39],a[192:195],a[88:91],v[36:39]
ds_read_b128 v[84:87],v4 offset:288
v_lshl_add_u64 v[30:31],v[24:25],0,v[26:27]
v_mul_f32_dpp v115,v14,v115 row_newbcast:3 row_mask:0xf bank_mask:0xf
v_lshlrev_b32_sdwa v62,1,v62 dst_sel:DWORD dst_unused:UNUSED_PAD src0_sel:DWORD src1_sel:WORD_1
v_mfma_f32_16x16x32_bf16 v[40:43],a[196:199],a[88:91],v[20:23]
buffer_store_dwordx4 v[148:151],v7,s[40:43],s8 offen offset:256 sc1
v_mul_f32_dpp v72,v14,v72 row_newbcast:4 row_mask:0xf bank_mask:0xf
v_lshlrev_b32_sdwa v61,1,v61 dst_sel:DWORD dst_unused:UNUSED_PAD src0_sel:DWORD src1_sel:WORD_1
TP10203_%=:
s_cbranch_execz TR1_%=
v_mul_f32_dpp v73,v14,v73 row_newbcast:5 row_mask:0xf bank_mask:0xf
v_mfma_f32_16x16x32_bf16 v[40:43],a[200:203],a[72:75],v[40:43]
s_waitcnt lgkmcnt(9)
ds_read_u16_d16_hi v62,v62
v_lshl_add_u64 v[24:25],v[48:49],0,v[50:51]
v_mul_f32_dpp v74,v14,v74 row_newbcast:6 row_mask:0xf bank_mask:0xf
v_mfma_f32_16x16x32_bf16 v[40:43],a[212:215],a[56:59],v[40:43]
ds_read_u16 v61,v61
v_mul_f32_dpp v75,v14,v75 row_newbcast:7 row_mask:0xf bank_mask:0xf
v_lshlrev_b32_sdwa v63,1,v63 dst_sel:DWORD dst_unused:UNUSED_PAD src0_sel:DWORD src1_sel:WORD_1
v_cvt_pk_bf16_f32 v112,v112,v113
v_mfma_f32_16x16x32_bf16 v[48:51],a[212:215],a[60:63],v[20:23]
s_nop 0
ds_read_b64_tr_b16 a[212:213],v3 offset:51312
ds_read_b64_tr_b16 a[214:215],v3 offset:52848
v_cvt_pk_bf16_f32 v113,v114,v115
v_lshlrev_b32_sdwa v32,1,v32 dst_sel:DWORD dst_unused:UNUSED_PAD src0_sel:DWORD src1_sel:WORD_1
v_mfma_f32_16x16x32_bf16 v[48:51],a[200:203],a[76:79],v[48:51]
s_waitcnt lgkmcnt(10)
ds_read_b64_tr_b16 a[200:201],v3 offset:57472
ds_read_b64_tr_b16 a[202:203],v3 offset:59008
v_lshl_add_u64 v[26:27],v[44:45],0,v[46:47]
v_cvt_pk_bf16_f32 v114,v72,v73
v_cvt_pk_bf16_f32 v115,v74,v75
v_lshlrev_b32_sdwa v34,1,v34 dst_sel:DWORD dst_unused:UNUSED_PAD src0_sel:DWORD src1_sel:WORD_1
v_mfma_f32_16x16x32_bf16 v[48:51],a[196:199],a[92:95],v[48:51]
ds_read_u16_d16_hi v63,v63
ds_read_u16 v32,v32
v_mul_f32_dpp v120,v14,v120 row_newbcast:8 row_mask:0xf bank_mask:0xf
v_mfma_f32_16x16x32_bf16 v[44:47],a[192:195],a[92:95],v[16:19]
ds_read_u16_d16_hi v34,v34
ds_read_b64_tr_b16 a[196:197],v3 offset:57480
v_mul_f32_dpp v121,v14,v121 row_newbcast:9 row_mask:0xf bank_mask:0xf
v_lshlrev_b32_sdwa v33,1,v33 dst_sel:DWORD dst_unused:UNUSED_PAD src0_sel:DWORD src1_sel:WORD_1
v_mul_f32_dpp v122,v14,v122 row_newbcast:10 row_mask:0xf bank_mask:0xf
v_mfma_f32_16x16x32_bf16 v[44:47],a[216:219],a[76:79],v[44:47]
ds_read_u16 v33,v33
ds_read_b64_tr_b16 a[198:199],v3 offset:59016
v_mul_f32_dpp v123,v14,v123 row_newbcast:11 row_mask:0xf bank_mask:0xf
v_lshlrev_b32_sdwa v35,1,v35 dst_sel:DWORD dst_unused:UNUSED_PAD src0_sel:DWORD src1_sel:WORD_1
v_mfma_f32_16x16x32_bf16 v[44:47],a[220:223],a[60:63],v[44:47]
ds_read_u16_d16_hi v35,v35
ds_read_b64_tr_b16 a[192:193],v3 offset:51320
v_mul_f32_dpp v88,v14,v88 row_newbcast:12 row_mask:0xf bank_mask:0xf
v_mul_f32_dpp v89,v14,v89 row_newbcast:13 row_mask:0xf bank_mask:0xf
v_mfma_f32_16x16x32_bf16 v[208:211],a[224:227],v[28:31],v[208:211]
ds_read_b64_tr_b16 a[194:195],v3 offset:52856
ds_read_b128 v[96:99],v4 offset:384
v_lshlrev_b32_sdwa v52,1,v52 dst_sel:DWORD dst_unused:UNUSED_PAD src0_sel:DWORD src1_sel:WORD_1
v_mul_f32_dpp v90,v14,v90 row_newbcast:14 row_mask:0xf bank_mask:0xf
v_mfma_f32_16x16x32_bf16 v[212:215],a[224:227],v[24:27],v[212:215]
buffer_store_dwordx4 v[156:159],v7,s[40:43],s8 offen offset:320 sc1
ds_read_u16 v52,v52
v_mul_f32_dpp v91,v14,v91 row_newbcast:15 row_mask:0xf bank_mask:0xf
v_lshlrev_b32_sdwa v54,1,v54 dst_sel:DWORD dst_unused:UNUSED_PAD src0_sel:DWORD src1_sel:WORD_1
v_cvt_pk_bf16_f32 v120,v120,v121
v_mfma_f32_16x16x32_bf16 v[172:175],a[248:251],v[24:27],v[172:175]
ds_read_u16_d16_hi v54,v54
ds_read_b64_tr_b16 a[224:225],v3 offset:32896
v_cvt_pk_bf16_f32 v121,v122,v123
v_lshlrev_b32_sdwa v53,1,v53 dst_sel:DWORD dst_unused:UNUSED_PAD src0_sel:DWORD src1_sel:WORD_1
v_mfma_f32_16x16x32_bf16 v[168:171],a[248:251],v[28:31],v[168:171]
ds_read_u16 v53,v53
ds_read_b64_tr_b16 a[226:227],v3 offset:34432
v_cvt_pk_bf16_f32 v122,v88,v89
v_cvt_pk_bf16_f32 v123,v90,v91
v_mfma_f32_16x16x32_bf16 v[216:219],a[228:231],v[28:31],v[216:219]
ds_read_b64_tr_b16 a[248:249],v3 offset:32904
ds_read_b64_tr_b16 a[250:251],v3 offset:34440
v_mul_f32_dpp v128,v15,v128 row_newbcast:0 row_mask:0xf bank_mask:0xf
v_lshlrev_b32_sdwa v55,1,v55 dst_sel:DWORD dst_unused:UNUSED_PAD src0_sel:DWORD src1_sel:WORD_1
v_mfma_f32_16x16x32_bf16 v[220:223],a[228:231],v[24:27],v[220:223]
ds_read_u16_d16_hi v55,v55
ds_read_b128 v[100:103],v4 offset:416
v_mul_f32_dpp v129,v15,v129 row_newbcast:1 row_mask:0xf bank_mask:0xf
v_lshlrev_b32_sdwa v56,1,v56 dst_sel:DWORD dst_unused:UNUSED_PAD src0_sel:DWORD src1_sel:WORD_1
v_mul_f32_dpp v130,v15,v130 row_newbcast:2 row_mask:0xf bank_mask:0xf
v_mfma_f32_16x16x32_bf16 v[188:191],a[236:239],v[24:27],v[188:191]
ds_read_u16 v56,v56
ds_read_b64_tr_b16 a[228:229],v3 offset:32960
v_mul_f32_dpp v131,v15,v131 row_newbcast:3 row_mask:0xf bank_mask:0xf
v_mfma_f32_16x16x32_bf16 v[184:187],a[236:239],v[28:31],v[184:187]
ds_read_b64_tr_b16 a[230:231],v3 offset:34496
ds_read_b64_tr_b16 a[236:237],v3 offset:32968
v_mul_f32_dpp v104,v15,v104 row_newbcast:4 row_mask:0xf bank_mask:0xf
v_lshlrev_b32_sdwa v58,1,v58 dst_sel:DWORD dst_unused:UNUSED_PAD src0_sel:DWORD src1_sel:WORD_1
v_mul_f32_dpp v105,v15,v105 row_newbcast:5 row_mask:0xf bank_mask:0xf
v_mfma_f32_16x16x32_bf16 v[224:227],a[244:247],v[28:31],v[224:227]
ds_read_u16_d16_hi v58,v58
ds_read_b64_tr_b16 a[238:239],v3 offset:34504
v_mul_f32_dpp v106,v15,v106 row_newbcast:6 row_mask:0xf bank_mask:0xf
v_lshlrev_b32_sdwa v57,1,v57 dst_sel:DWORD dst_unused:UNUSED_PAD src0_sel:DWORD src1_sel:WORD_1
v_mfma_f32_16x16x32_bf16 v[228:231],a[244:247],v[24:27],v[228:231]
ds_read_u16 v57,v57
ds_read_b64_tr_b16 a[216:217],v3 offset:51376
v_mul_f32_dpp v107,v15,v107 row_newbcast:7 row_mask:0xf bank_mask:0xf
v_cvt_pk_bf16_f32 v128,v128,v129
v_lshlrev_b32_sdwa v59,1,v59 dst_sel:DWORD dst_unused:UNUSED_PAD src0_sel:DWORD src1_sel:WORD_1
v_mfma_f32_16x16x32_bf16 v[204:207],a[252:255],v[24:27],v[204:207]
s_nop 0
buffer_store_dwordx4 v[112:115],v7,s[40:43],s9 offen offset:0 sc1
ds_read_u16_d16_hi v59,v59
v_cvt_pk_bf16_f32 v129,v130,v131
v_mfma_f32_16x16x32_bf16 v[200:203],a[252:255],v[28:31],v[200:203]
s_waitcnt lgkmcnt(15)
ds_read_b64_tr_b16 a[218:219],v3 offset:52912
ds_read_b128 v[20:23],v4 offset:512
v_cvt_pk_bf16_f32 v130,v104,v105
v_cvt_pk_bf16_f32 v131,v106,v107
v_mfma_f32_16x16x32_bf16 v[24:27],a[96:99],a[48:51],v[64:67]
ds_read_b128 v[16:19],v4 offset:544
ds_read_b64_tr_b16 a[220:221],v3 offset:57536
v_mul_f32_dpp v116,v14,v116 row_newbcast:0 row_mask:0xf bank_mask:0xf
v_lshlrev_b32_sdwa v36,1,v36 dst_sel:DWORD dst_unused:UNUSED_PAD src0_sel:DWORD src1_sel:WORD_1
v_mfma_f32_16x16x32_bf16 v[24:27],a[100:103],a[64:67],v[24:27]
ds_read_u16 v36,v36
ds_read_b64_tr_b16 a[222:223],v3 offset:59072
v_lshl_add_u64 v[60:61],v[60:61],0,v[62:63]
v_mul_f32_dpp v117,v14,v117 row_newbcast:1 row_mask:0xf bank_mask:0xf
v_mul_f32_dpp v118,v14,v118 row_newbcast:2 row_mask:0xf bank_mask:0xf
v_mfma_f32_16x16x32_bf16 v[24:27],a[104:107],a[80:83],v[24:27]
ds_read_b64_tr_b16 a[204:205],v3 offset:57544
v_lshl_add_u64 v[62:63],v[32:33],0,v[34:35]
v_mul_f32_dpp v119,v14,v119 row_newbcast:3 row_mask:0xf bank_mask:0xf
v_lshlrev_b32_sdwa v38,1,v38 dst_sel:DWORD dst_unused:UNUSED_PAD src0_sel:DWORD src1_sel:WORD_1
v_mfma_f32_16x16x32_bf16 v[28:31],a[108:111],a[80:83],v[68:71]
ds_read_u16_d16_hi v38,v38
v_mul_f32_dpp v76,v14,v76 row_newbcast:4 row_mask:0xf bank_mask:0xf
v_lshlrev_b32_sdwa v37,1,v37 dst_sel:DWORD dst_unused:UNUSED_PAD src0_sel:DWORD src1_sel:WORD_1
v_mul_f32_dpp v77,v14,v77 row_newbcast:5 row_mask:0xf bank_mask:0xf
v_mfma_f32_16x16x32_bf16 v[28:31],a[112:115],a[64:67],v[28:31]
s_waitcnt lgkmcnt(13)
ds_read_u16 v37,v37
v_lshl_add_u64 v[32:33],v[52:53],0,v[54:55]
v_mul_f32_dpp v78,v14,v78 row_newbcast:6 row_mask:0xf bank_mask:0xf
v_mfma_f32_16x16x32_bf16 v[28:31],a[116:119],a[48:51],v[28:31]
s_nop 0
ds_read_b64_tr_b16 a[206:207],v3 offset:59080
v_mul_f32_dpp v79,v14,v79 row_newbcast:7 row_mask:0xf bank_mask:0xf
v_lshlrev_b32_sdwa v39,1,v39 dst_sel:DWORD dst_unused:UNUSED_PAD src0_sel:DWORD src1_sel:WORD_1
v_cvt_pk_bf16_f32 v116,v116,v117
v_mfma_f32_16x16x32_bf16 v[52:55],a[120:123],a[48:51],v[84:87]
s_nop 0
ds_read_u16_d16_hi v39,v39
v_cvt_pk_bf16_f32 v117,v118,v119
v_lshlrev_b32_sdwa v40,1,v40 dst_sel:DWORD dst_unused:UNUSED_PAD src0_sel:DWORD src1_sel:WORD_1
v_mfma_f32_16x16x32_bf16 v[52:55],a[124:127],a[64:67],v[52:55]
s_waitcnt lgkmcnt(11)
ds_read_u16 v40,v40
ds_read_b64_tr_b16 a[208:209],v3 offset:51384
v_lshl_add_u64 v[34:35],v[56:57],0,v[58:59]
v_cvt_pk_bf16_f32 v118,v76,v77
v_cvt_pk_bf16_f32 v119,v78,v79
v_lshlrev_b32_sdwa v42,1,v42 dst_sel:DWORD dst_unused:UNUSED_PAD src0_sel:DWORD src1_sel:WORD_1
v_mfma_f32_16x16x32_bf16 v[52:55],a[128:131],a[80:83],v[52:55]
buffer_store_dwordx4 v[120:123],v7,s[40:43],s9 offen offset:64 sc1
ds_read_u16_d16_hi v42,v42
v_mul_f32_dpp v124,v14,v124 row_newbcast:8 row_mask:0xf bank_mask:0xf
v_mfma_f32_16x16x32_bf16 v[56:59],a[132:135],a[80:83],v[80:83]
ds_read_b64_tr_b16 a[210:211],v3 offset:52920
v_mul_f32_dpp v125,v14,v125 row_newbcast:9 row_mask:0xf bank_mask:0xf
v_lshlrev_b32_sdwa v41,1,v41 dst_sel:DWORD dst_unused:UNUSED_PAD src0_sel:DWORD src1_sel:WORD_1
v_mul_f32_dpp v126,v14,v126 row_newbcast:10 row_mask:0xf bank_mask:0xf
v_mfma_f32_16x16x32_bf16 v[56:59],a[136:139],a[64:67],v[56:59]
ds_read_u16 v41,v41
v_mul_f32_dpp v127,v14,v127 row_newbcast:11 row_mask:0xf bank_mask:0xf
v_lshlrev_b32_sdwa v43,1,v43 dst_sel:DWORD dst_unused:UNUSED_PAD src0_sel:DWORD src1_sel:WORD_1
v_mfma_f32_16x16x32_bf16 v[56:59],a[140:143],a[48:51],v[56:59]
ds_read_u16_d16_hi v43,v43
v_mul_f32_dpp v92,v14,v92 row_newbcast:12 row_mask:0xf bank_mask:0xf
v_mul_f32_dpp v93,v14,v93 row_newbcast:13 row_mask:0xf bank_mask:0xf
v_mfma_f32_16x16x32_bf16 v[232:235],a[232:235],v[60:63],v[232:235]
ds_read_b64_tr_b16 a[244:245],v3 offset:51440
v_lshlrev_b32_sdwa v44,1,v44 dst_sel:DWORD dst_unused:UNUSED_PAD src0_sel:DWORD src1_sel:WORD_1
v_mul_f32_dpp v94,v14,v94 row_newbcast:14 row_mask:0xf bank_mask:0xf
v_mfma_f32_16x16x32_bf16 v[236:239],a[232:235],v[32:35],v[236:239]
ds_read_u16 v44,v44
v_mul_f32_dpp v95,v14,v95 row_newbcast:15 row_mask:0xf bank_mask:0xf
v_lshlrev_b32_sdwa v46,1,v46 dst_sel:DWORD dst_unused:UNUSED_PAD src0_sel:DWORD src1_sel:WORD_1
v_cvt_pk_bf16_f32 v124,v124,v125
v_mfma_f32_16x16x32_bf16 v[164:167],a[240:243],v[32:35],v[164:167]
ds_read_u16_d16_hi v46,v46
v_cvt_pk_bf16_f32 v125,v126,v127
v_lshlrev_b32_sdwa v45,1,v45 dst_sel:DWORD dst_unused:UNUSED_PAD src0_sel:DWORD src1_sel:WORD_1
v_mfma_f32_16x16x32_bf16 v[160:163],a[240:243],v[60:63],v[160:163]
ds_read_u16 v45,v45
v_cvt_pk_bf16_f32 v126,v92,v93
v_cvt_pk_bf16_f32 v127,v94,v95
v_mfma_f32_16x16x32_bf16 v[240:243],a[224:227],v[60:63],v[240:243]
ds_read_b64_tr_b16 a[246:247],v3 offset:52976
v_mul_f32_dpp v132,v15,v132 row_newbcast:0 row_mask:0xf bank_mask:0xf
v_lshlrev_b32_sdwa v47,1,v47 dst_sel:DWORD dst_unused:UNUSED_PAD src0_sel:DWORD src1_sel:WORD_1
v_mfma_f32_16x16x32_bf16 v[244:247],a[224:227],v[32:35],v[244:247]
ds_read_u16_d16_hi v47,v47
v_mul_f32_dpp v133,v15,v133 row_newbcast:1 row_mask:0xf bank_mask:0xf
v_lshlrev_b32_sdwa v48,1,v48 dst_sel:DWORD dst_unused:UNUSED_PAD src0_sel:DWORD src1_sel:WORD_1
v_mul_f32_dpp v134,v15,v134 row_newbcast:2 row_mask:0xf bank_mask:0xf
v_mfma_f32_16x16x32_bf16 v[180:183],a[248:251],v[32:35],v[180:183]
ds_read_u16 v48,v48
v_mul_f32_dpp v135,v15,v135 row_newbcast:3 row_mask:0xf bank_mask:0xf
v_mfma_f32_16x16x32_bf16 v[176:179],a[248:251],v[60:63],v[176:179]
buffer_store_dwordx4 v[128:131],v7,s[40:43],s9 offen offset:128 sc1
v_mul_f32_dpp v108,v15,v108 row_newbcast:4 row_mask:0xf bank_mask:0xf
v_lshlrev_b32_sdwa v50,1,v50 dst_sel:DWORD dst_unused:UNUSED_PAD src0_sel:DWORD src1_sel:WORD_1
v_mul_f32_dpp v109,v15,v109 row_newbcast:5 row_mask:0xf bank_mask:0xf
v_mfma_f32_16x16x32_bf16 v[248:251],a[228:231],v[60:63],v[248:251]
ds_read_u16_d16_hi v50,v50
v_mul_f32_dpp v110,v15,v110 row_newbcast:6 row_mask:0xf bank_mask:0xf
v_lshlrev_b32_sdwa v49,1,v49 dst_sel:DWORD dst_unused:UNUSED_PAD src0_sel:DWORD src1_sel:WORD_1
v_mfma_f32_16x16x32_bf16 v[252:255],a[228:231],v[32:35],v[252:255]
ds_read_u16 v49,v49
v_mul_f32_dpp v111,v15,v111 row_newbcast:7 row_mask:0xf bank_mask:0xf
v_cvt_pk_bf16_f32 v132,v132,v133
v_lshlrev_b32_sdwa v51,1,v51 dst_sel:DWORD dst_unused:UNUSED_PAD src0_sel:DWORD src1_sel:WORD_1
v_mfma_f32_16x16x32_bf16 v[196:199],a[236:239],v[32:35],v[196:199]
ds_read_u16_d16_hi v51,v51
v_cvt_pk_bf16_f32 v133,v134,v135
v_mfma_f32_16x16x32_bf16 v[192:195],a[236:239],v[60:63],v[192:195]
ds_read_b64_tr_b16 a[252:253],v3 offset:57600
v_cvt_pk_bf16_f32 v134,v108,v109
v_cvt_pk_bf16_f32 v135,v110,v111
v_mfma_f32_16x16x32_bf16 v[32:35],a[144:147],a[48:51],v[96:99]
ds_read_b64_tr_b16 a[254:255],v3 offset:59136
v_lshlrev_b32_sdwa v24,1,v24 dst_sel:DWORD dst_unused:UNUSED_PAD src0_sel:DWORD src1_sel:WORD_1
v_mfma_f32_16x16x32_bf16 v[32:35],a[148:151],a[64:67],v[32:35]
s_waitcnt lgkmcnt(7)
ds_read_u16 v24,v24
v_lshl_add_u64 v[36:37],v[36:37],0,v[38:39]
v_mfma_f32_16x16x32_bf16 v[32:35],a[152:155],a[80:83],v[32:35]
s_nop 0
v_lshl_add_u64 v[38:39],v[40:41],0,v[42:43]
v_lshlrev_b32_sdwa v26,1,v26 dst_sel:DWORD dst_unused:UNUSED_PAD src0_sel:DWORD src1_sel:WORD_1
v_mfma_f32_16x16x32_bf16 v[60:63],a[156:159],a[80:83],v[100:103]
ds_read_u16_d16_hi v26,v26
v_lshlrev_b32_sdwa v25,1,v25 dst_sel:DWORD dst_unused:UNUSED_PAD src0_sel:DWORD src1_sel:WORD_1
v_mfma_f32_16x16x32_bf16 v[60:63],a[160:163],a[64:67],v[60:63]
s_waitcnt lgkmcnt(7)
ds_read_u16 v25,v25
v_lshl_add_u64 v[40:41],v[44:45],0,v[46:47]
v_mfma_f32_16x16x32_bf16 v[60:63],a[164:167],a[48:51],v[60:63]
v_lshlrev_b32_sdwa v27,1,v27 dst_sel:DWORD dst_unused:UNUSED_PAD src0_sel:DWORD src1_sel:WORD_1
v_lshlrev_b32_sdwa v28,1,v28 dst_sel:DWORD dst_unused:UNUSED_PAD src0_sel:DWORD src1_sel:WORD_1
v_mfma_f32_16x16x32_bf16 v[44:47],a[168:171],a[48:51],v[16:19]
ds_read_u16_d16_hi v27,v27
v_lshlrev_b32_sdwa v30,1,v30 dst_sel:DWORD dst_unused:UNUSED_PAD src0_sel:DWORD src1_sel:WORD_1
v_mfma_f32_16x16x32_bf16 v[44:47],a[172:175],a[64:67],v[44:47]
s_waitcnt lgkmcnt(6)
ds_read_u16 v28,v28
v_lshl_add_u64 v[42:43],v[48:49],0,v[50:51]
v_mfma_f32_16x16x32_bf16 v[44:47],a[176:179],a[80:83],v[44:47]
buffer_store_dwordx4 v[116:119],v7,s[40:43],s9 offen offset:192 sc1
v_lshlrev_b32_sdwa v29,1,v29 dst_sel:DWORD dst_unused:UNUSED_PAD src0_sel:DWORD src1_sel:WORD_1
v_mfma_f32_16x16x32_bf16 v[48:51],a[180:183],a[80:83],v[20:23]
ds_read_u16_d16_hi v30,v30
v_lshlrev_b32_sdwa v31,1,v31 dst_sel:DWORD dst_unused:UNUSED_PAD src0_sel:DWORD src1_sel:WORD_1
v_mfma_f32_16x16x32_bf16 v[48:51],a[184:187],a[64:67],v[48:51]
ds_read_u16 v29,v29
v_lshlrev_b32_sdwa v56,1,v56 dst_sel:DWORD dst_unused:UNUSED_PAD src0_sel:DWORD src1_sel:WORD_1
v_mfma_f32_16x16x32_bf16 v[48:51],a[188:191],a[48:51],v[48:51]
ds_read_u16_d16_hi v31,v31
v_lshlrev_b32_sdwa v58,1,v58 dst_sel:DWORD dst_unused:UNUSED_PAD src0_sel:DWORD src1_sel:WORD_1
v_mfma_f32_16x16x32_bf16 v[208:211],a[232:235],v[36:39],v[208:211]
ds_read_u16 v56,v56
v_lshlrev_b32_sdwa v57,1,v57 dst_sel:DWORD dst_unused:UNUSED_PAD src0_sel:DWORD src1_sel:WORD_1
v_mfma_f32_16x16x32_bf16 v[212:215],a[232:235],v[40:43],v[212:215]
ds_read_u16_d16_hi v58,v58
v_lshlrev_b32_sdwa v59,1,v59 dst_sel:DWORD dst_unused:UNUSED_PAD src0_sel:DWORD src1_sel:WORD_1
v_mfma_f32_16x16x32_bf16 v[172:175],a[240:243],v[40:43],v[172:175]
ds_read_u16 v57,v57
v_lshlrev_b32_sdwa v52,1,v52 dst_sel:DWORD dst_unused:UNUSED_PAD src0_sel:DWORD src1_sel:WORD_1
v_mfma_f32_16x16x32_bf16 v[168:171],a[240:243],v[36:39],v[168:171]
ds_read_u16_d16_hi v59,v59
ds_read_u16 v52,v52
v_mfma_f32_16x16x32_bf16 v[216:219],a[224:227],v[36:39],v[216:219]
ds_read_b64_tr_b16 a[232:233],v3 offset:57608
v_lshlrev_b32_sdwa v54,1,v54 dst_sel:DWORD dst_unused:UNUSED_PAD src0_sel:DWORD src1_sel:WORD_1
v_mfma_f32_16x16x32_bf16 v[220:223],a[224:227],v[40:43],v[220:223]
ds_read_u16_d16_hi v54,v54
v_lshlrev_b32_sdwa v53,1,v53 dst_sel:DWORD dst_unused:UNUSED_PAD src0_sel:DWORD src1_sel:WORD_1
v_mfma_f32_16x16x32_bf16 v[188:191],a[248:251],v[40:43],v[188:191]
ds_read_u16 v53,v53
v_lshlrev_b32_sdwa v55,1,v55 dst_sel:DWORD dst_unused:UNUSED_PAD src0_sel:DWORD src1_sel:WORD_1
v_mfma_f32_16x16x32_bf16 v[184:187],a[248:251],v[36:39],v[184:187]
ds_read_u16_d16_hi v55,v55
v_mfma_f32_16x16x32_bf16 v[224:227],a[228:231],v[36:39],v[224:227]
ds_read_b64_tr_b16 a[234:235],v3 offset:59144
v_mfma_f32_16x16x32_bf16 v[228:231],a[228:231],v[40:43],v[228:231]
ds_read_b64_tr_b16 a[240:241],v3 offset:51448
v_mfma_f32_16x16x32_bf16 v[204:207],a[236:239],v[40:43],v[204:207]
ds_read_b64_tr_b16 a[242:243],v3 offset:52984
v_mfma_f32_16x16x32_bf16 v[200:203],a[236:239],v[36:39],v[200:203]
buffer_store_dwordx4 v[124:127],v7,s[40:43],s9 offen offset:256 sc1
v_mfma_f32_16x16x32_bf16 v[40:43],a[96:99],a[52:55],v[64:67]
v_lshlrev_b32_sdwa v32,1,v32 dst_sel:DWORD dst_unused:UNUSED_PAD src0_sel:DWORD src1_sel:WORD_1
v_lshlrev_b32_sdwa v34,1,v34 dst_sel:DWORD dst_unused:UNUSED_PAD src0_sel:DWORD src1_sel:WORD_1
v_mfma_f32_16x16x32_bf16 v[40:43],a[100:103],a[68:71],v[40:43]
s_waitcnt lgkmcnt(6)
ds_read_u16 v32,v32
v_lshl_add_u64 v[24:25],v[24:25],0,v[26:27]
v_mfma_f32_16x16x32_bf16 v[40:43],a[104:107],a[84:87],v[40:43]
ds_read_u16_d16_hi v34,v34
v_lshl_add_u64 v[26:27],v[28:29],0,v[30:31]
v_mfma_f32_16x16x32_bf16 v[36:39],a[108:111],a[84:87],v[68:71]
v_lshlrev_b32_sdwa v33,1,v33 dst_sel:DWORD dst_unused:UNUSED_PAD src0_sel:DWORD src1_sel:WORD_1
v_lshlrev_b32_sdwa v35,1,v35 dst_sel:DWORD dst_unused:UNUSED_PAD src0_sel:DWORD src1_sel:WORD_1
v_mfma_f32_16x16x32_bf16 v[36:39],a[112:115],a[68:71],v[36:39]
ds_read_u16 v33,v33
v_lshl_add_u64 v[28:29],v[56:57],0,v[58:59]
v_mfma_f32_16x16x32_bf16 v[36:39],a[116:119],a[52:55],v[36:39]
ds_read_u16_d16_hi v35,v35
v_lshlrev_b32_sdwa v60,1,v60 dst_sel:DWORD dst_unused:UNUSED_PAD src0_sel:DWORD src1_sel:WORD_1
v_mfma_f32_16x16x32_bf16 v[56:59],a[120:123],a[52:55],v[84:87]
ds_read_u16 v60,v60
v_lshlrev_b32_sdwa v62,1,v62 dst_sel:DWORD dst_unused:UNUSED_PAD src0_sel:DWORD src1_sel:WORD_1
v_mfma_f32_16x16x32_bf16 v[56:59],a[124:127],a[68:71],v[56:59]
s_waitcnt lgkmcnt(5)
ds_read_u16_d16_hi v62,v62
v_lshl_add_u64 v[30:31],v[52:53],0,v[54:55]
v_mfma_f32_16x16x32_bf16 v[56:59],a[128:131],a[84:87],v[56:59]
v_lshlrev_b32_sdwa v61,1,v61 dst_sel:DWORD dst_unused:UNUSED_PAD src0_sel:DWORD src1_sel:WORD_1
v_lshlrev_b32_sdwa v63,1,v63 dst_sel:DWORD dst_unused:UNUSED_PAD src0_sel:DWORD src1_sel:WORD_1
v_mfma_f32_16x16x32_bf16 v[52:55],a[132:135],a[84:87],v[80:83]
ds_read_u16 v61,v61
v_lshlrev_b32_sdwa v48,1,v48 dst_sel:DWORD dst_unused:UNUSED_PAD src0_sel:DWORD src1_sel:WORD_1
v_mfma_f32_16x16x32_bf16 v[52:55],a[136:139],a[68:71],v[52:55]
ds_read_u16_d16_hi v63,v63
v_lshlrev_b32_sdwa v50,1,v50 dst_sel:DWORD dst_unused:UNUSED_PAD src0_sel:DWORD src1_sel:WORD_1
v_mfma_f32_16x16x32_bf16 v[52:55],a[140:143],a[52:55],v[52:55]
ds_read_u16 v48,v48
v_lshlrev_b32_sdwa v49,1,v49 dst_sel:DWORD dst_unused:UNUSED_PAD src0_sel:DWORD src1_sel:WORD_1
v_mfma_f32_16x16x32_bf16 v[232:235],a[0:3],v[24:27],v[232:235]
ds_read_u16_d16_hi v50,v50
v_lshlrev_b32_sdwa v51,1,v51 dst_sel:DWORD dst_unused:UNUSED_PAD src0_sel:DWORD src1_sel:WORD_1
v_mfma_f32_16x16x32_bf16 v[232:235],a[4:7],v[28:31],v[232:235]
ds_read_u16 v49,v49
v_lshlrev_b32_sdwa v44,1,v44 dst_sel:DWORD dst_unused:UNUSED_PAD src0_sel:DWORD src1_sel:WORD_1
v_mfma_f32_16x16x32_bf16 v[160:163],a[8:11],v[28:31],v[160:163]
buffer_store_dwordx4 v[132:135],v7,s[40:43],s9 offen offset:320 sc1
v_lshlrev_b32_sdwa v46,1,v46 dst_sel:DWORD dst_unused:UNUSED_PAD src0_sel:DWORD src1_sel:WORD_1
v_mfma_f32_16x16x32_bf16 v[160:163],a[12:15],v[24:27],v[160:163]
ds_read_u16_d16_hi v51,v51
v_lshlrev_b32_sdwa v45,1,v45 dst_sel:DWORD dst_unused:UNUSED_PAD src0_sel:DWORD src1_sel:WORD_1
v_mfma_f32_16x16x32_bf16 v[240:243],a[16:19],v[24:27],v[240:243]
ds_read_u16 v44,v44
v_lshlrev_b32_sdwa v47,1,v47 dst_sel:DWORD dst_unused:UNUSED_PAD src0_sel:DWORD src1_sel:WORD_1
v_mfma_f32_16x16x32_bf16 v[240:243],a[20:23],v[28:31],v[240:243]
ds_read_u16_d16_hi v46,v46
v_mfma_f32_16x16x32_bf16 v[176:179],a[24:27],v[28:31],v[176:179]
ds_read_u16 v45,v45
v_mfma_f32_16x16x32_bf16 v[176:179],a[28:31],v[24:27],v[176:179]
ds_read_u16_d16_hi v47,v47
v_mfma_f32_16x16x32_bf16 v[248:251],a[32:35],v[24:27],v[248:251]
v_mfma_f32_16x16x32_bf16 v[248:251],a[36:39],v[28:31],v[248:251]
v_mfma_f32_16x16x32_bf16 v[192:195],a[40:43],v[28:31],v[192:195]
v_mfma_f32_16x16x32_bf16 v[192:195],a[44:47],v[24:27],v[192:195]
v_mfma_f32_16x16x32_bf16 v[28:31],a[144:147],a[52:55],v[96:99]
v_lshlrev_b32_sdwa v40,1,v40 dst_sel:DWORD dst_unused:UNUSED_PAD src0_sel:DWORD src1_sel:WORD_1
v_lshlrev_b32_sdwa v42,1,v42 dst_sel:DWORD dst_unused:UNUSED_PAD src0_sel:DWORD src1_sel:WORD_1
v_mfma_f32_16x16x32_bf16 v[28:31],a[148:151],a[68:71],v[28:31]
s_waitcnt lgkmcnt(3)
ds_read_u16 v40,v40
v_lshl_add_u64 v[32:33],v[32:33],0,v[34:35]
v_mfma_f32_16x16x32_bf16 v[28:31],a[152:155],a[84:87],v[28:31]
ds_read_u16_d16_hi v42,v42
v_lshl_add_u64 v[34:35],v[60:61],0,v[62:63]
v_mfma_f32_16x16x32_bf16 v[24:27],a[156:159],a[84:87],v[100:103]
v_lshl_add_u64 v[60:61],v[48:49],0,v[50:51]
v_lshlrev_b32_sdwa v41,1,v41 dst_sel:DWORD dst_unused:UNUSED_PAD src0_sel:DWORD src1_sel:WORD_1
v_mfma_f32_16x16x32_bf16 v[24:27],a[160:163],a[68:71],v[24:27]
s_waitcnt lgkmcnt(2)
ds_read_u16 v41,v41
v_lshl_add_u64 v[62:63],v[44:45],0,v[46:47]
v_mfma_f32_16x16x32_bf16 v[24:27],a[164:167],a[52:55],v[24:27]
v_lshlrev_b32_sdwa v43,1,v43 dst_sel:DWORD dst_unused:UNUSED_PAD src0_sel:DWORD src1_sel:WORD_1
v_lshlrev_b32_sdwa v36,1,v36 dst_sel:DWORD dst_unused:UNUSED_PAD src0_sel:DWORD src1_sel:WORD_1
v_mfma_f32_16x16x32_bf16 v[232:235],a[212:215],v[32:35],v[232:235]
ds_read_u16_d16_hi v43,v43
v_lshlrev_b32_sdwa v38,1,v38 dst_sel:DWORD dst_unused:UNUSED_PAD src0_sel:DWORD src1_sel:WORD_1
v_mfma_f32_16x16x32_bf16 v[232:235],a[200:203],v[60:63],v[232:235]
ds_read_u16 v36,v36
v_lshlrev_b32_sdwa v37,1,v37 dst_sel:DWORD dst_unused:UNUSED_PAD src0_sel:DWORD src1_sel:WORD_1
v_mfma_f32_16x16x32_bf16 v[160:163],a[196:199],v[60:63],v[160:163]
ds_read_u16_d16_hi v38,v38
v_lshlrev_b32_sdwa v39,1,v39 dst_sel:DWORD dst_unused:UNUSED_PAD src0_sel:DWORD src1_sel:WORD_1
v_mfma_f32_16x16x32_bf16 v[160:163],a[192:195],v[32:35],v[160:163]
ds_read_u16 v37,v37
v_lshlrev_b32_sdwa v52,1,v52 dst_sel:DWORD dst_unused:UNUSED_PAD src0_sel:DWORD src1_sel:WORD_1
v_mfma_f32_16x16x32_bf16 v[240:243],a[216:219],v[32:35],v[240:243]
ds_read_u16_d16_hi v39,v39
v_lshlrev_b32_sdwa v54,1,v54 dst_sel:DWORD dst_unused:UNUSED_PAD src0_sel:DWORD src1_sel:WORD_1
v_mfma_f32_16x16x32_bf16 v[240:243],a[220:223],v[60:63],v[240:243]
ds_read_u16 v52,v52
v_mul_f32_dpp v232,v14,v232 row_newbcast:0 row_mask:0xf bank_mask:0xf
s_add_u32 s10,s61,4718592
v_mfma_f32_16x16x32_bf16 v[176:179],a[204:207],v[60:63],v[176:179]
ds_read_u16_d16_hi v54,v54
v_mul_f32_dpp v233,v14,v233 row_newbcast:1 row_mask:0xf bank_mask:0xf
v_mfma_f32_16x16x32_bf16 v[176:179],a[208:211],v[32:35],v[176:179]
v_mul_f32_dpp v234,v14,v234 row_newbcast:2 row_mask:0xf bank_mask:0xf
v_lshlrev_b32_sdwa v53,1,v53 dst_sel:DWORD dst_unused:UNUSED_PAD src0_sel:DWORD src1_sel:WORD_1
v_mfma_f32_16x16x32_bf16 v[248:251],a[244:247],v[32:35],v[248:251]
ds_read_u16 v53,v53
v_mul_f32_dpp v235,v14,v235 row_newbcast:3 row_mask:0xf bank_mask:0xf
v_mfma_f32_16x16x32_bf16 v[248:251],a[252:255],v[60:63],v[248:251]
v_mul_f32_dpp v160,v14,v160 row_newbcast:4 row_mask:0xf bank_mask:0xf
v_lshlrev_b32_sdwa v55,1,v55 dst_sel:DWORD dst_unused:UNUSED_PAD src0_sel:DWORD src1_sel:WORD_1
v_mfma_f32_16x16x32_bf16 v[192:195],a[232:235],v[60:63],v[192:195]
ds_read_u16_d16_hi v55,v55
v_mul_f32_dpp v161,v14,v161 row_newbcast:5 row_mask:0xf bank_mask:0xf
v_mfma_f32_16x16x32_bf16 v[192:195],a[240:243],v[32:35],v[192:195]
v_mul_f32_dpp v162,v14,v162 row_newbcast:6 row_mask:0xf bank_mask:0xf
v_lshlrev_b32_sdwa v56,1,v56 dst_sel:DWORD dst_unused:UNUSED_PAD src0_sel:DWORD src1_sel:WORD_1
v_mfma_f32_16x16x32_bf16 v[48:51],a[168:171],a[52:55],v[16:19]
ds_read_u16 v56,v56
v_mul_f32_dpp v163,v14,v163 row_newbcast:7 row_mask:0xf bank_mask:0xf
v_mfma_f32_16x16x32_bf16 v[48:51],a[172:175],a[68:71],v[48:51]
v_cvt_pk_bf16_f32 v232,v232,v233
v_lshlrev_b32_sdwa v58,1,v58 dst_sel:DWORD dst_unused:UNUSED_PAD src0_sel:DWORD src1_sel:WORD_1
v_mfma_f32_16x16x32_bf16 v[48:51],a[176:179],a[84:87],v[48:51]
ds_read_u16_d16_hi v58,v58
v_cvt_pk_bf16_f32 v233,v234,v235
v_lshlrev_b32_sdwa v57,1,v57 dst_sel:DWORD dst_unused:UNUSED_PAD src0_sel:DWORD src1_sel:WORD_1
v_mfma_f32_16x16x32_bf16 v[44:47],a[180:183],a[84:87],v[20:23]
ds_read_u16 v57,v57
v_cvt_pk_bf16_f32 v234,v160,v161
v_lshlrev_b32_sdwa v59,1,v59 dst_sel:DWORD dst_unused:UNUSED_PAD src0_sel:DWORD src1_sel:WORD_1
v_mfma_f32_16x16x32_bf16 v[44:47],a[184:187],a[68:71],v[44:47]
ds_read_u16_d16_hi v59,v59
v_cvt_pk_bf16_f32 v235,v162,v163
v_mfma_f32_16x16x32_bf16 v[44:47],a[188:191],a[52:55],v[44:47]
buffer_store_dwordx4 v[232:235],v7,s[40:43],s10 offen offset:0 sc1
v_mul_f32_dpp v240,v14,v240 row_newbcast:8 row_mask:0xf bank_mask:0xf
v_mfma_f32_16x16x32_bf16 v[60:63],a[96:99],a[56:59],v[64:67]
v_lshlrev_b32_sdwa v28,1,v28 dst_sel:DWORD dst_unused:UNUSED_PAD src0_sel:DWORD src1_sel:WORD_1
v_mul_f32_dpp v241,v14,v241 row_newbcast:9 row_mask:0xf bank_mask:0xf
v_mfma_f32_16x16x32_bf16 v[60:63],a[100:103],a[72:75],v[60:63]
s_waitcnt lgkmcnt(4)
ds_read_u16 v28,v28
v_lshl_add_u64 v[40:41],v[40:41],0,v[42:43]
v_mfma_f32_16x16x32_bf16 v[60:63],a[104:107],a[88:91],v[60:63]
v_lshl_add_u64 v[42:43],v[36:37],0,v[38:39]
v_mul_f32_dpp v242,v14,v242 row_newbcast:10 row_mask:0xf bank_mask:0xf
v_lshlrev_b32_sdwa v30,1,v30 dst_sel:DWORD dst_unused:UNUSED_PAD src0_sel:DWORD src1_sel:WORD_1
v_mfma_f32_16x16x32_bf16 v[32:35],a[108:111],a[88:91],v[68:71]
ds_read_u16_d16_hi v30,v30
v_mul_f32_dpp v243,v14,v243 row_newbcast:11 row_mask:0xf bank_mask:0xf
v_lshlrev_b32_sdwa v29,1,v29 dst_sel:DWORD dst_unused:UNUSED_PAD src0_sel:DWORD src1_sel:WORD_1
v_mfma_f32_16x16x32_bf16 v[32:35],a[112:115],a[72:75],v[32:35]
ds_read_u16 v29,v29
v_lshl_add_u64 v[36:37],v[52:53],0,v[54:55]
v_mul_f32_dpp v176,v14,v176 row_newbcast:12 row_mask:0xf bank_mask:0xf
v_mfma_f32_16x16x32_bf16 v[32:35],a[116:119],a[56:59],v[32:35]
v_mul_f32_dpp v177,v14,v177 row_newbcast:13 row_mask:0xf bank_mask:0xf
v_lshlrev_b32_sdwa v31,1,v31 dst_sel:DWORD dst_unused:UNUSED_PAD src0_sel:DWORD src1_sel:WORD_1
v_mfma_f32_16x16x32_bf16 v[52:55],a[120:123],a[56:59],v[84:87]
ds_read_u16_d16_hi v31,v31
v_mul_f32_dpp v178,v14,v178 row_newbcast:14 row_mask:0xf bank_mask:0xf
v_lshlrev_b32_sdwa v24,1,v24 dst_sel:DWORD dst_unused:UNUSED_PAD src0_sel:DWORD src1_sel:WORD_1
v_mfma_f32_16x16x32_bf16 v[52:55],a[124:127],a[72:75],v[52:55]
s_waitcnt lgkmcnt(4)
ds_read_u16 v24,v24
v_lshl_add_u64 v[38:39],v[56:57],0,v[58:59]
v_mul_f32_dpp v179,v14,v179 row_newbcast:15 row_mask:0xf bank_mask:0xf
v_lshlrev_b32_sdwa v26,1,v26 dst_sel:DWORD dst_unused:UNUSED_PAD src0_sel:DWORD src1_sel:WORD_1
v_mfma_f32_16x16x32_bf16 v[52:55],a[128:131],a[88:91],v[52:55]
ds_read_u16_d16_hi v26,v26
v_cvt_pk_bf16_f32 v240,v240,v241
v_mfma_f32_16x16x32_bf16 v[56:59],a[132:135],a[88:91],v[80:83]
v_cvt_pk_bf16_f32 v241,v242,v243
v_lshlrev_b32_sdwa v25,1,v25 dst_sel:DWORD dst_unused:UNUSED_PAD src0_sel:DWORD src1_sel:WORD_1
v_mfma_f32_16x16x32_bf16 v[56:59],a[136:139],a[72:75],v[56:59]
ds_read_u16 v25,v25
v_cvt_pk_bf16_f32 v242,v176,v177
v_lshlrev_b32_sdwa v27,1,v27 dst_sel:DWORD dst_unused:UNUSED_PAD src0_sel:DWORD src1_sel:WORD_1
v_mfma_f32_16x16x32_bf16 v[56:59],a[140:143],a[56:59],v[56:59]
ds_read_u16_d16_hi v27,v27
v_cvt_pk_bf16_f32 v243,v178,v179
v_mfma_f32_16x16x32_bf16 v[236:239],a[0:3],v[40:43],v[236:239]
buffer_store_dwordx4 v[240:243],v7,s[40:43],s10 offen offset:64 sc1
v_lshlrev_b32_sdwa v44,1,v44 dst_sel:DWORD dst_unused:UNUSED_PAD src0_sel:DWORD src1_sel:WORD_1
v_mul_f32_dpp v248,v15,v248 row_newbcast:0 row_mask:0xf bank_mask:0xf
v_mfma_f32_16x16x32_bf16 v[236:239],a[4:7],v[36:39],v[236:239]
ds_read_u16 v44,v44
v_mul_f32_dpp v249,v15,v249 row_newbcast:1 row_mask:0xf bank_mask:0xf
v_lshlrev_b32_sdwa v46,1,v46 dst_sel:DWORD dst_unused:UNUSED_PAD src0_sel:DWORD src1_sel:WORD_1
v_mfma_f32_16x16x32_bf16 v[164:167],a[8:11],v[36:39],v[164:167]
ds_read_u16_d16_hi v46,v46
v_mul_f32_dpp v250,v15,v250 row_newbcast:2 row_mask:0xf bank_mask:0xf
v_lshlrev_b32_sdwa v45,1,v45 dst_sel:DWORD dst_unused:UNUSED_PAD src0_sel:DWORD src1_sel:WORD_1
v_mfma_f32_16x16x32_bf16 v[164:167],a[12:15],v[40:43],v[164:167]
ds_read_u16 v45,v45
v_mul_f32_dpp v251,v15,v251 row_newbcast:3 row_mask:0xf bank_mask:0xf
v_mfma_f32_16x16x32_bf16 v[244:247],a[16:19],v[40:43],v[244:247]
v_lshlrev_b32_sdwa v47,1,v47 dst_sel:DWORD dst_unused:UNUSED_PAD src0_sel:DWORD src1_sel:WORD_1
v_mul_f32_dpp v192,v15,v192 row_newbcast:4 row_mask:0xf bank_mask:0xf
v_mfma_f32_16x16x32_bf16 v[244:247],a[20:23],v[36:39],v[244:247]
ds_read_u16_d16_hi v47,v47
v_mul_f32_dpp v193,v15,v193 row_newbcast:5 row_mask:0xf bank_mask:0xf
v_lshlrev_b32_sdwa v48,1,v48 dst_sel:DWORD dst_unused:UNUSED_PAD src0_sel:DWORD src1_sel:WORD_1
v_mfma_f32_16x16x32_bf16 v[180:183],a[24:27],v[36:39],v[180:183]
ds_read_u16 v48,v48
v_mul_f32_dpp v194,v15,v194 row_newbcast:6 row_mask:0xf bank_mask:0xf
v_mfma_f32_16x16x32_bf16 v[180:183],a[28:31],v[40:43],v[180:183]
v_lshlrev_b32_sdwa v50,1,v50 dst_sel:DWORD dst_unused:UNUSED_PAD src0_sel:DWORD src1_sel:WORD_1
v_mul_f32_dpp v195,v15,v195 row_newbcast:7 row_mask:0xf bank_mask:0xf
v_mfma_f32_16x16x32_bf16 v[252:255],a[32:35],v[40:43],v[252:255]
ds_read_u16_d16_hi v50,v50
v_lshlrev_b32_sdwa v49,1,v49 dst_sel:DWORD dst_unused:UNUSED_PAD src0_sel:DWORD src1_sel:WORD_1
v_cvt_pk_bf16_f32 v248,v248,v249
v_mfma_f32_16x16x32_bf16 v[252:255],a[36:39],v[36:39],v[252:255]
ds_read_u16 v49,v49
v_cvt_pk_bf16_f32 v249,v250,v251
v_lshlrev_b32_sdwa v51,1,v51 dst_sel:DWORD dst_unused:UNUSED_PAD src0_sel:DWORD src1_sel:WORD_1
v_mfma_f32_16x16x32_bf16 v[196:199],a[40:43],v[36:39],v[196:199]
ds_read_u16_d16_hi v51,v51
v_cvt_pk_bf16_f32 v250,v192,v193
v_mfma_f32_16x16x32_bf16 v[196:199],a[44:47],v[40:43],v[196:199]
v_cvt_pk_bf16_f32 v251,v194,v195
v_mfma_f32_16x16x32_bf16 v[36:39],a[144:147],a[56:59],v[96:99]
v_lshlrev_b32_sdwa v60,1,v60 dst_sel:DWORD dst_unused:UNUSED_PAD src0_sel:DWORD src1_sel:WORD_1
v_lshlrev_b32_sdwa v62,1,v62 dst_sel:DWORD dst_unused:UNUSED_PAD src0_sel:DWORD src1_sel:WORD_1
v_mfma_f32_16x16x32_bf16 v[36:39],a[148:151],a[72:75],v[36:39]
s_waitcnt lgkmcnt(5)
buffer_store_dwordx4 v[248:251],v7,s[40:43],s10 offen offset:128 sc1
v_lshl_add_u64 v[28:29],v[28:29],0,v[30:31]
v_mfma_f32_16x16x32_bf16 v[36:39],a[152:155],a[88:91],v[36:39]
TP10729_%=:
s_cbranch_execz TR1_%=
ds_read_u16 v60,v60
v_lshl_add_u64 v[30:31],v[24:25],0,v[26:27]
v_mfma_f32_16x16x32_bf16 v[40:43],a[156:159],a[88:91],v[100:103]
s_waitcnt lgkmcnt(4)
ds_read_u16_d16_hi v62,v62
v_lshl_add_u64 v[24:25],v[44:45],0,v[46:47]
v_lshlrev_b32_sdwa v61,1,v61 dst_sel:DWORD dst_unused:UNUSED_PAD src0_sel:DWORD src1_sel:WORD_1
v_mfma_f32_16x16x32_bf16 v[40:43],a[160:163],a[72:75],v[40:43]
s_waitcnt lgkmcnt(2)
ds_read_u16 v61,v61
v_lshl_add_u64 v[26:27],v[48:49],0,v[50:51]
v_mfma_f32_16x16x32_bf16 v[40:43],a[164:167],a[56:59],v[40:43]
v_lshlrev_b32_sdwa v63,1,v63 dst_sel:DWORD dst_unused:UNUSED_PAD src0_sel:DWORD src1_sel:WORD_1
v_lshlrev_b32_sdwa v32,1,v32 dst_sel:DWORD dst_unused:UNUSED_PAD src0_sel:DWORD src1_sel:WORD_1
v_mfma_f32_16x16x32_bf16 v[236:239],a[212:215],v[28:31],v[236:239]
ds_read_u16_d16_hi v63,v63
v_lshlrev_b32_sdwa v34,1,v34 dst_sel:DWORD dst_unused:UNUSED_PAD src0_sel:DWORD src1_sel:WORD_1
v_mfma_f32_16x16x32_bf16 v[236:239],a[200:203],v[24:27],v[236:239]
ds_read_u16 v32,v32
v_lshlrev_b32_sdwa v33,1,v33 dst_sel:DWORD dst_unused:UNUSED_PAD src0_sel:DWORD src1_sel:WORD_1
v_mfma_f32_16x16x32_bf16 v[164:167],a[196:199],v[24:27],v[164:167]
ds_read_u16_d16_hi v34,v34
v_lshlrev_b32_sdwa v35,1,v35 dst_sel:DWORD dst_unused:UNUSED_PAD src0_sel:DWORD src1_sel:WORD_1
v_mfma_f32_16x16x32_bf16 v[164:167],a[192:195],v[28:31],v[164:167]
ds_read_u16 v33,v33
v_lshlrev_b32_sdwa v56,1,v56 dst_sel:DWORD dst_unused:UNUSED_PAD src0_sel:DWORD src1_sel:WORD_1
v_mfma_f32_16x16x32_bf16 v[244:247],a[216:219],v[28:31],v[244:247]
ds_read_u16_d16_hi v35,v35
v_lshlrev_b32_sdwa v58,1,v58 dst_sel:DWORD dst_unused:UNUSED_PAD src0_sel:DWORD src1_sel:WORD_1
v_mfma_f32_16x16x32_bf16 v[244:247],a[220:223],v[24:27],v[244:247]
ds_read_u16 v56,v56
v_mul_f32_dpp v236,v14,v236 row_newbcast:0 row_mask:0xf bank_mask:0xf
v_mfma_f32_16x16x32_bf16 v[180:183],a[204:207],v[24:27],v[180:183]
ds_read_u16_d16_hi v58,v58
v_mul_f32_dpp v237,v14,v237 row_newbcast:1 row_mask:0xf bank_mask:0xf
v_mfma_f32_16x16x32_bf16 v[180:183],a[208:211],v[28:31],v[180:183]
v_mul_f32_dpp v238,v14,v238 row_newbcast:2 row_mask:0xf bank_mask:0xf
v_lshlrev_b32_sdwa v57,1,v57 dst_sel:DWORD dst_unused:UNUSED_PAD src0_sel:DWORD src1_sel:WORD_1
v_mfma_f32_16x16x32_bf16 v[252:255],a[244:247],v[28:31],v[252:255]
ds_read_u16 v57,v57
v_mul_f32_dpp v239,v14,v239 row_newbcast:3 row_mask:0xf bank_mask:0xf
v_mfma_f32_16x16x32_bf16 v[252:255],a[252:255],v[24:27],v[252:255]
v_mul_f32_dpp v164,v14,v164 row_newbcast:4 row_mask:0xf bank_mask:0xf
v_lshlrev_b32_sdwa v59,1,v59 dst_sel:DWORD dst_unused:UNUSED_PAD src0_sel:DWORD src1_sel:WORD_1
v_mfma_f32_16x16x32_bf16 v[196:199],a[232:235],v[24:27],v[196:199]
ds_read_u16_d16_hi v59,v59
v_mul_f32_dpp v165,v14,v165 row_newbcast:5 row_mask:0xf bank_mask:0xf
v_mfma_f32_16x16x32_bf16 v[196:199],a[240:243],v[28:31],v[196:199]
v_mul_f32_dpp v166,v14,v166 row_newbcast:6 row_mask:0xf bank_mask:0xf
v_lshlrev_b32_sdwa v52,1,v52 dst_sel:DWORD dst_unused:UNUSED_PAD src0_sel:DWORD src1_sel:WORD_1
v_mfma_f32_16x16x32_bf16 v[44:47],a[168:171],a[56:59],v[16:19]
ds_read_u16 v52,v52
v_mul_f32_dpp v167,v14,v167 row_newbcast:7 row_mask:0xf bank_mask:0xf
v_mfma_f32_16x16x32_bf16 v[44:47],a[172:175],a[72:75],v[44:47]
v_cvt_pk_bf16_f32 v236,v236,v237
v_lshlrev_b32_sdwa v54,1,v54 dst_sel:DWORD dst_unused:UNUSED_PAD src0_sel:DWORD src1_sel:WORD_1
v_mfma_f32_16x16x32_bf16 v[44:47],a[176:179],a[88:91],v[44:47]
ds_read_u16_d16_hi v54,v54
v_cvt_pk_bf16_f32 v237,v238,v239
v_lshlrev_b32_sdwa v53,1,v53 dst_sel:DWORD dst_unused:UNUSED_PAD src0_sel:DWORD src1_sel:WORD_1
v_mfma_f32_16x16x32_bf16 v[48:51],a[180:183],a[88:91],v[20:23]
ds_read_u16 v53,v53
v_cvt_pk_bf16_f32 v238,v164,v165
v_lshlrev_b32_sdwa v55,1,v55 dst_sel:DWORD dst_unused:UNUSED_PAD src0_sel:DWORD src1_sel:WORD_1
v_mfma_f32_16x16x32_bf16 v[48:51],a[184:187],a[72:75],v[48:51]
ds_read_u16_d16_hi v55,v55
v_cvt_pk_bf16_f32 v239,v166,v167
v_mfma_f32_16x16x32_bf16 v[48:51],a[188:191],a[56:59],v[48:51]
buffer_store_dwordx4 v[236:239],v7,s[40:43],s10 offen offset:192 sc1
v_mul_f32_dpp v244,v14,v244 row_newbcast:8 row_mask:0xf bank_mask:0xf
v_mfma_f32_16x16x32_bf16 v[24:27],a[96:99],a[60:63],v[64:67]
v_lshlrev_b32_sdwa v36,1,v36 dst_sel:DWORD dst_unused:UNUSED_PAD src0_sel:DWORD src1_sel:WORD_1
v_mul_f32_dpp v245,v14,v245 row_newbcast:9 row_mask:0xf bank_mask:0xf
v_mfma_f32_16x16x32_bf16 v[24:27],a[100:103],a[76:79],v[24:27]
s_waitcnt lgkmcnt(4)
ds_read_u16 v36,v36
v_lshl_add_u64 v[60:61],v[60:61],0,v[62:63]
v_mfma_f32_16x16x32_bf16 v[24:27],a[104:107],a[92:95],v[24:27]
v_lshl_add_u64 v[62:63],v[32:33],0,v[34:35]
v_mul_f32_dpp v246,v14,v246 row_newbcast:10 row_mask:0xf bank_mask:0xf
v_lshlrev_b32_sdwa v38,1,v38 dst_sel:DWORD dst_unused:UNUSED_PAD src0_sel:DWORD src1_sel:WORD_1
v_mfma_f32_16x16x32_bf16 v[28:31],a[108:111],a[92:95],v[68:71]
ds_read_u16_d16_hi v38,v38
v_mul_f32_dpp v247,v14,v247 row_newbcast:11 row_mask:0xf bank_mask:0xf
v_lshlrev_b32_sdwa v37,1,v37 dst_sel:DWORD dst_unused:UNUSED_PAD src0_sel:DWORD src1_sel:WORD_1
v_mfma_f32_16x16x32_bf16 v[28:31],a[112:115],a[76:79],v[28:31]
ds_read_u16 v37,v37
v_lshl_add_u64 v[32:33],v[56:57],0,v[58:59]
v_mul_f32_dpp v180,v14,v180 row_newbcast:12 row_mask:0xf bank_mask:0xf
v_mfma_f32_16x16x32_bf16 v[28:31],a[116:119],a[60:63],v[28:31]
v_mul_f32_dpp v181,v14,v181 row_newbcast:13 row_mask:0xf bank_mask:0xf
v_lshlrev_b32_sdwa v39,1,v39 dst_sel:DWORD dst_unused:UNUSED_PAD src0_sel:DWORD src1_sel:WORD_1
v_mfma_f32_16x16x32_bf16 v[56:59],a[120:123],a[60:63],v[84:87]
ds_read_u16_d16_hi v39,v39
v_mul_f32_dpp v182,v14,v182 row_newbcast:14 row_mask:0xf bank_mask:0xf
v_lshlrev_b32_sdwa v40,1,v40 dst_sel:DWORD dst_unused:UNUSED_PAD src0_sel:DWORD src1_sel:WORD_1
v_mfma_f32_16x16x32_bf16 v[56:59],a[124:127],a[76:79],v[56:59]
s_waitcnt lgkmcnt(4)
ds_read_u16 v40,v40
v_lshl_add_u64 v[34:35],v[52:53],0,v[54:55]
v_mul_f32_dpp v183,v14,v183 row_newbcast:15 row_mask:0xf bank_mask:0xf
v_lshlrev_b32_sdwa v42,1,v42 dst_sel:DWORD dst_unused:UNUSED_PAD src0_sel:DWORD src1_sel:WORD_1
v_mfma_f32_16x16x32_bf16 v[56:59],a[128:131],a[92:95],v[56:59]
ds_read_u16_d16_hi v42,v42
v_cvt_pk_bf16_f32 v244,v244,v245
v_mfma_f32_16x16x32_bf16 v[52:55],a[132:135],a[92:95],v[80:83]
v_cvt_pk_bf16_f32 v245,v246,v247
v_lshlrev_b32_sdwa v41,1,v41 dst_sel:DWORD dst_unused:UNUSED_PAD src0_sel:DWORD src1_sel:WORD_1
v_mfma_f32_16x16x32_bf16 v[52:55],a[136:139],a[76:79],v[52:55]
ds_read_u16 v41,v41
v_cvt_pk_bf16_f32 v246,v180,v181
v_lshlrev_b32_sdwa v43,1,v43 dst_sel:DWORD dst_unused:UNUSED_PAD src0_sel:DWORD src1_sel:WORD_1
v_mfma_f32_16x16x32_bf16 v[52:55],a[140:143],a[60:63],v[52:55]
ds_read_u16_d16_hi v43,v43
v_cvt_pk_bf16_f32 v247,v182,v183
v_mfma_f32_16x16x32_bf16 v[208:211],a[0:3],v[60:63],v[208:211]
buffer_store_dwordx4 v[244:247],v7,s[40:43],s10 offen offset:256 sc1
v_lshlrev_b32_sdwa v48,1,v48 dst_sel:DWORD dst_unused:UNUSED_PAD src0_sel:DWORD src1_sel:WORD_1
v_mul_f32_dpp v252,v15,v252 row_newbcast:0 row_mask:0xf bank_mask:0xf
v_mfma_f32_16x16x32_bf16 v[208:211],a[4:7],v[32:35],v[208:211]
ds_read_u16 v48,v48
v_mul_f32_dpp v253,v15,v253 row_newbcast:1 row_mask:0xf bank_mask:0xf
v_lshlrev_b32_sdwa v50,1,v50 dst_sel:DWORD dst_unused:UNUSED_PAD src0_sel:DWORD src1_sel:WORD_1
v_mfma_f32_16x16x32_bf16 v[168:171],a[8:11],v[32:35],v[168:171]
ds_read_u16_d16_hi v50,v50
v_mul_f32_dpp v254,v15,v254 row_newbcast:2 row_mask:0xf bank_mask:0xf
v_lshlrev_b32_sdwa v49,1,v49 dst_sel:DWORD dst_unused:UNUSED_PAD src0_sel:DWORD src1_sel:WORD_1
v_mfma_f32_16x16x32_bf16 v[168:171],a[12:15],v[60:63],v[168:171]
ds_read_u16 v49,v49
v_mul_f32_dpp v255,v15,v255 row_newbcast:3 row_mask:0xf bank_mask:0xf
v_mfma_f32_16x16x32_bf16 v[216:219],a[16:19],v[60:63],v[216:219]
v_lshlrev_b32_sdwa v51,1,v51 dst_sel:DWORD dst_unused:UNUSED_PAD src0_sel:DWORD src1_sel:WORD_1
v_mul_f32_dpp v196,v15,v196 row_newbcast:4 row_mask:0xf bank_mask:0xf
v_mfma_f32_16x16x32_bf16 v[216:219],a[20:23],v[32:35],v[216:219]
ds_read_u16_d16_hi v51,v51
v_mul_f32_dpp v197,v15,v197 row_newbcast:5 row_mask:0xf bank_mask:0xf
v_lshlrev_b32_sdwa v44,1,v44 dst_sel:DWORD dst_unused:UNUSED_PAD src0_sel:DWORD src1_sel:WORD_1
v_mfma_f32_16x16x32_bf16 v[184:187],a[24:27],v[32:35],v[184:187]
ds_read_u16 v44,v44
v_mul_f32_dpp v198,v15,v198 row_newbcast:6 row_mask:0xf bank_mask:0xf
v_mfma_f32_16x16x32_bf16 v[184:187],a[28:31],v[60:63],v[184:187]
v_lshlrev_b32_sdwa v46,1,v46 dst_sel:DWORD dst_unused:UNUSED_PAD src0_sel:DWORD src1_sel:WORD_1
v_mul_f32_dpp v199,v15,v199 row_newbcast:7 row_mask:0xf bank_mask:0xf
v_mfma_f32_16x16x32_bf16 v[224:227],a[32:35],v[60:63],v[224:227]
ds_read_u16_d16_hi v46,v46
v_lshlrev_b32_sdwa v45,1,v45 dst_sel:DWORD dst_unused:UNUSED_PAD src0_sel:DWORD src1_sel:WORD_1
v_cvt_pk_bf16_f32 v252,v252,v253
v_mfma_f32_16x16x32_bf16 v[224:227],a[36:39],v[32:35],v[224:227]
ds_read_u16 v45,v45
v_cvt_pk_bf16_f32 v253,v254,v255
v_lshlrev_b32_sdwa v47,1,v47 dst_sel:DWORD dst_unused:UNUSED_PAD src0_sel:DWORD src1_sel:WORD_1
v_mfma_f32_16x16x32_bf16 v[200:203],a[40:43],v[32:35],v[200:203]
ds_read_u16_d16_hi v47,v47
v_cvt_pk_bf16_f32 v254,v196,v197
v_mfma_f32_16x16x32_bf16 v[200:203],a[44:47],v[60:63],v[200:203]
v_cvt_pk_bf16_f32 v255,v198,v199
v_mov_b32 v10,0
v_mfma_f32_16x16x32_bf16 v[32:35],a[144:147],a[60:63],v[96:99]
ds_read_b128 v[72:75],v10 offset:11648
v_lshlrev_b32_sdwa v24,1,v24 dst_sel:DWORD dst_unused:UNUSED_PAD src0_sel:DWORD src1_sel:WORD_1
v_mfma_f32_16x16x32_bf16 v[32:35],a[148:151],a[76:79],v[32:35]
s_waitcnt lgkmcnt(6)
buffer_store_dwordx4 v[252:255],v7,s[40:43],s10 offen offset:320 sc1
v_lshl_add_u64 v[36:37],v[36:37],0,v[38:39]
v_mfma_f32_16x16x32_bf16 v[32:35],a[152:155],a[92:95],v[32:35]
ds_read_u16 v24,v24
v_lshl_add_u64 v[38:39],v[40:41],0,v[42:43]
v_lshlrev_b32_sdwa v26,1,v26 dst_sel:DWORD dst_unused:UNUSED_PAD src0_sel:DWORD src1_sel:WORD_1
v_mfma_f32_16x16x32_bf16 v[60:63],a[156:159],a[92:95],v[100:103]
s_waitcnt lgkmcnt(5)
ds_read_u16_d16_hi v26,v26
v_lshl_add_u64 v[40:41],v[48:49],0,v[50:51]
v_lshlrev_b32_sdwa v25,1,v25 dst_sel:DWORD dst_unused:UNUSED_PAD src0_sel:DWORD src1_sel:WORD_1
v_mfma_f32_16x16x32_bf16 v[60:63],a[160:163],a[76:79],v[60:63]
s_waitcnt lgkmcnt(3)
ds_read_u16 v25,v25
v_lshl_add_u64 v[42:43],v[44:45],0,v[46:47]
v_mfma_f32_16x16x32_bf16 v[60:63],a[164:167],a[60:63],v[60:63]
v_lshlrev_b32_sdwa v27,1,v27 dst_sel:DWORD dst_unused:UNUSED_PAD src0_sel:DWORD src1_sel:WORD_1
v_lshlrev_b32_sdwa v28,1,v28 dst_sel:DWORD dst_unused:UNUSED_PAD src0_sel:DWORD src1_sel:WORD_1
v_mfma_f32_16x16x32_bf16 v[208:211],a[212:215],v[36:39],v[208:211]
ds_read_u16_d16_hi v27,v27
v_lshlrev_b32_sdwa v30,1,v30 dst_sel:DWORD dst_unused:UNUSED_PAD src0_sel:DWORD src1_sel:WORD_1
v_mfma_f32_16x16x32_bf16 v[208:211],a[200:203],v[40:43],v[208:211]
ds_read_u16 v28,v28
v_lshlrev_b32_sdwa v29,1,v29 dst_sel:DWORD dst_unused:UNUSED_PAD src0_sel:DWORD src1_sel:WORD_1
v_mfma_f32_16x16x32_bf16 v[168:171],a[196:199],v[40:43],v[168:171]
ds_read_u16_d16_hi v30,v30
v_lshlrev_b32_sdwa v31,1,v31 dst_sel:DWORD dst_unused:UNUSED_PAD src0_sel:DWORD src1_sel:WORD_1
v_mfma_f32_16x16x32_bf16 v[168:171],a[192:195],v[36:39],v[168:171]
ds_read_u16 v29,v29
v_lshlrev_b32_sdwa v52,1,v52 dst_sel:DWORD dst_unused:UNUSED_PAD src0_sel:DWORD src1_sel:WORD_1
v_mfma_f32_16x16x32_bf16 v[216:219],a[216:219],v[36:39],v[216:219]
ds_read_u16_d16_hi v31,v31
v_lshlrev_b32_sdwa v54,1,v54 dst_sel:DWORD dst_unused:UNUSED_PAD src0_sel:DWORD src1_sel:WORD_1
v_mfma_f32_16x16x32_bf16 v[216:219],a[220:223],v[40:43],v[216:219]
ds_read_u16 v52,v52
v_mul_f32_dpp v208,v14,v208 row_newbcast:0 row_mask:0xf bank_mask:0xf
s_add_u32 s11,s61,4816896
v_mul_f32_dpp v209,v14,v209 row_newbcast:1 row_mask:0xf bank_mask:0xf
v_mul_f32_dpp v210,v14,v210 row_newbcast:2 row_mask:0xf bank_mask:0xf
v_mfma_f32_16x16x32_bf16 v[184:187],a[204:207],v[40:43],v[184:187]
ds_read_u16_d16_hi v54,v54
v_mul_f32_dpp v211,v14,v211 row_newbcast:3 row_mask:0xf bank_mask:0xf
v_mul_f32_dpp v168,v14,v168 row_newbcast:4 row_mask:0xf bank_mask:0xf
v_mul_f32_dpp v169,v14,v169 row_newbcast:5 row_mask:0xf bank_mask:0xf
v_mfma_f32_16x16x32_bf16 v[184:187],a[208:211],v[36:39],v[184:187]
v_mul_f32_dpp v170,v14,v170 row_newbcast:6 row_mask:0xf bank_mask:0xf
v_mul_f32_dpp v171,v14,v171 row_newbcast:7 row_mask:0xf bank_mask:0xf
v_cvt_pk_bf16_f32 v208,v208,v209
v_mfma_f32_16x16x32_bf16 v[224:227],a[244:247],v[36:39],v[224:227]
v_cvt_pk_bf16_f32 v209,v210,v211
v_cvt_pk_bf16_f32 v210,v168,v169
v_cvt_pk_bf16_f32 v211,v170,v171
v_lshlrev_b32_sdwa v53,1,v53 dst_sel:DWORD dst_unused:UNUSED_PAD src0_sel:DWORD src1_sel:WORD_1
v_mfma_f32_16x16x32_bf16 v[224:227],a[252:255],v[40:43],v[224:227]
buffer_store_dwordx4 v[208:211],v7,s[40:43],s11 offen offset:0 sc1
v_mul_f32_dpp v216,v14,v216 row_newbcast:8 row_mask:0xf bank_mask:0xf
v_mul_f32_dpp v217,v14,v217 row_newbcast:9 row_mask:0xf bank_mask:0xf
v_mul_f32_dpp v218,v14,v218 row_newbcast:10 row_mask:0xf bank_mask:0xf
v_mfma_f32_16x16x32_bf16 v[200:203],a[232:235],v[40:43],v[200:203]
ds_read_u16 v53,v53
v_mul_f32_dpp v219,v14,v219 row_newbcast:11 row_mask:0xf bank_mask:0xf
v_mul_f32_dpp v184,v14,v184 row_newbcast:12 row_mask:0xf bank_mask:0xf
v_mul_f32_dpp v185,v14,v185 row_newbcast:13 row_mask:0xf bank_mask:0xf
v_lshlrev_b32_sdwa v55,1,v55 dst_sel:DWORD dst_unused:UNUSED_PAD src0_sel:DWORD src1_sel:WORD_1
v_mfma_f32_16x16x32_bf16 v[200:203],a[240:243],v[36:39],v[200:203]
ds_read_u16_d16_hi v55,v55
v_mul_f32_dpp v186,v14,v186 row_newbcast:14 row_mask:0xf bank_mask:0xf
v_mul_f32_dpp v187,v14,v187 row_newbcast:15 row_mask:0xf bank_mask:0xf
v_cvt_pk_bf16_f32 v216,v216,v217
v_lshlrev_b32_sdwa v56,1,v56 dst_sel:DWORD dst_unused:UNUSED_PAD src0_sel:DWORD src1_sel:WORD_1
v_mfma_f32_16x16x32_bf16 v[48:51],a[168:171],a[60:63],v[16:19]
ds_read_u16 v56,v56
v_cvt_pk_bf16_f32 v217,v218,v219
v_cvt_pk_bf16_f32 v218,v184,v185
v_cvt_pk_bf16_f32 v219,v186,v187
v_mfma_f32_16x16x32_bf16 v[48:51],a[172:175],a[76:79],v[48:51]
v_mul_f32_dpp v224,v15,v224 row_newbcast:0 row_mask:0xf bank_mask:0xf
v_mul_f32_dpp v225,v15,v225 row_newbcast:1 row_mask:0xf bank_mask:0xf
v_mul_f32_dpp v226,v15,v226 row_newbcast:2 row_mask:0xf bank_mask:0xf
v_mul_f32_dpp v227,v15,v227 row_newbcast:3 row_mask:0xf bank_mask:0xf
v_lshlrev_b32_sdwa v58,1,v58 dst_sel:DWORD dst_unused:UNUSED_PAD src0_sel:DWORD src1_sel:WORD_1
v_mfma_f32_16x16x32_bf16 v[48:51],a[176:179],a[92:95],v[48:51]
ds_read_u16_d16_hi v58,v58
v_mul_f32_dpp v200,v15,v200 row_newbcast:4 row_mask:0xf bank_mask:0xf
v_mul_f32_dpp v201,v15,v201 row_newbcast:5 row_mask:0xf bank_mask:0xf
v_mul_f32_dpp v202,v15,v202 row_newbcast:6 row_mask:0xf bank_mask:0xf
v_lshlrev_b32_sdwa v57,1,v57 dst_sel:DWORD dst_unused:UNUSED_PAD src0_sel:DWORD src1_sel:WORD_1
v_mfma_f32_16x16x32_bf16 v[44:47],a[180:183],a[92:95],v[20:23]
ds_read_u16 v57,v57
v_mul_f32_dpp v203,v15,v203 row_newbcast:7 row_mask:0xf bank_mask:0xf
v_cvt_pk_bf16_f32 v224,v224,v225
v_cvt_pk_bf16_f32 v225,v226,v227
v_lshlrev_b32_sdwa v59,1,v59 dst_sel:DWORD dst_unused:UNUSED_PAD src0_sel:DWORD src1_sel:WORD_1
v_mfma_f32_16x16x32_bf16 v[44:47],a[184:187],a[76:79],v[44:47]
ds_read_u16_d16_hi v59,v59
v_cvt_pk_bf16_f32 v226,v200,v201
v_cvt_pk_bf16_f32 v227,v202,v203
v_mfma_f32_16x16x32_bf16 v[44:47],a[188:191],a[60:63],v[44:47]
buffer_store_dwordx4 v[216:219],v7,s[40:43],s11 offen offset:64 sc1
s_waitcnt lgkmcnt(12)
v_lshl_add_u64 v[24:25],v[24:25],0,v[26:27]
s_waitcnt lgkmcnt(8)
v_lshl_add_u64 v[26:27],v[28:29],0,v[30:31]
s_waitcnt lgkmcnt(4)
v_lshl_add_u64 v[28:29],v[52:53],0,v[54:55]
s_waitcnt lgkmcnt(0)
v_lshl_add_u64 v[30:31],v[56:57],0,v[58:59]
v_mfma_f32_16x16x32_bf16 v[212:215],a[0:3],v[24:27],v[212:215]
v_lshlrev_b32_sdwa v32,1,v32 dst_sel:DWORD dst_unused:UNUSED_PAD src0_sel:DWORD src1_sel:WORD_1
v_lshlrev_b32_sdwa v34,1,v34 dst_sel:DWORD dst_unused:UNUSED_PAD src0_sel:DWORD src1_sel:WORD_1
v_mfma_f32_16x16x32_bf16 v[212:215],a[4:7],v[28:31],v[212:215]
ds_read_u16 v32,v32
v_lshlrev_b32_sdwa v33,1,v33 dst_sel:DWORD dst_unused:UNUSED_PAD src0_sel:DWORD src1_sel:WORD_1
v_mfma_f32_16x16x32_bf16 v[172:175],a[8:11],v[28:31],v[172:175]
ds_read_u16_d16_hi v34,v34
v_lshlrev_b32_sdwa v35,1,v35 dst_sel:DWORD dst_unused:UNUSED_PAD src0_sel:DWORD src1_sel:WORD_1
v_mfma_f32_16x16x32_bf16 v[172:175],a[12:15],v[24:27],v[172:175]
ds_read_u16 v33,v33
v_lshlrev_b32_sdwa v60,1,v60 dst_sel:DWORD dst_unused:UNUSED_PAD src0_sel:DWORD src1_sel:WORD_1
v_mfma_f32_16x16x32_bf16 v[220:223],a[16:19],v[24:27],v[220:223]
ds_read_u16_d16_hi v35,v35
v_lshlrev_b32_sdwa v62,1,v62 dst_sel:DWORD dst_unused:UNUSED_PAD src0_sel:DWORD src1_sel:WORD_1
v_mfma_f32_16x16x32_bf16 v[220:223],a[20:23],v[28:31],v[220:223]
ds_read_u16 v60,v60
v_lshlrev_b32_sdwa v61,1,v61 dst_sel:DWORD dst_unused:UNUSED_PAD src0_sel:DWORD src1_sel:WORD_1
v_lshlrev_b32_sdwa v63,1,v63 dst_sel:DWORD dst_unused:UNUSED_PAD src0_sel:DWORD src1_sel:WORD_1
v_mfma_f32_16x16x32_bf16 v[188:191],a[24:27],v[28:31],v[188:191]
ds_read_u16_d16_hi v62,v62
ds_read_u16 v61,v61
v_lshlrev_b32_sdwa v44,1,v44 dst_sel:DWORD dst_unused:UNUSED_PAD src0_sel:DWORD src1_sel:WORD_1
v_mfma_f32_16x16x32_bf16 v[188:191],a[28:31],v[24:27],v[188:191]
ds_read_u16_d16_hi v63,v63
v_lshlrev_b32_sdwa v46,1,v46 dst_sel:DWORD dst_unused:UNUSED_PAD src0_sel:DWORD src1_sel:WORD_1
v_lshlrev_b32_sdwa v45,1,v45 dst_sel:DWORD dst_unused:UNUSED_PAD src0_sel:DWORD src1_sel:WORD_1
v_mfma_f32_16x16x32_bf16 v[228:231],a[32:35],v[24:27],v[228:231]
ds_read_u16 v44,v44
ds_read_u16_d16_hi v46,v46
v_lshlrev_b32_sdwa v47,1,v47 dst_sel:DWORD dst_unused:UNUSED_PAD src0_sel:DWORD src1_sel:WORD_1
v_mfma_f32_16x16x32_bf16 v[228:231],a[36:39],v[28:31],v[228:231]
buffer_store_dwordx4 v[224:227],v7,s[40:43],s11 offen offset:128 sc1
ds_read_u16 v45,v45
v_lshlrev_b32_sdwa v48,1,v48 dst_sel:DWORD dst_unused:UNUSED_PAD src0_sel:DWORD src1_sel:WORD_1
v_mfma_f32_16x16x32_bf16 v[204:207],a[40:43],v[28:31],v[204:207]
ds_read_u16_d16_hi v47,v47
v_lshlrev_b32_sdwa v50,1,v50 dst_sel:DWORD dst_unused:UNUSED_PAD src0_sel:DWORD src1_sel:WORD_1
v_lshlrev_b32_sdwa v49,1,v49 dst_sel:DWORD dst_unused:UNUSED_PAD src0_sel:DWORD src1_sel:WORD_1
v_mfma_f32_16x16x32_bf16 v[204:207],a[44:47],v[24:27],v[204:207]
ds_read_u16 v48,v48
ds_read_u16_d16_hi v50,v50
v_lshlrev_b32_sdwa v51,1,v51 dst_sel:DWORD dst_unused:UNUSED_PAD src0_sel:DWORD src1_sel:WORD_1
ds_read_u16 v49,v49
ds_read_u16_d16_hi v51,v51
s_waitcnt lgkmcnt(12)
v_lshl_add_u64 v[32:33],v[32:33],0,v[34:35]
s_waitcnt lgkmcnt(8)
v_lshl_add_u64 v[34:35],v[60:61],0,v[62:63]
s_waitcnt lgkmcnt(4)
v_lshl_add_u64 v[60:61],v[44:45],0,v[46:47]
s_waitcnt lgkmcnt(0)
v_lshl_add_u64 v[62:63],v[48:49],0,v[50:51]
v_mfma_f32_16x16x32_bf16 v[212:215],a[212:215],v[32:35],v[212:215]
s_nop 0
v_mfma_f32_16x16x32_bf16 v[212:215],a[200:203],v[60:63],v[212:215]
v_mfma_f32_16x16x32_bf16 v[172:175],a[196:199],v[60:63],v[172:175]
v_mfma_f32_16x16x32_bf16 v[172:175],a[192:195],v[32:35],v[172:175]
v_mfma_f32_16x16x32_bf16 v[220:223],a[216:219],v[32:35],v[220:223]
v_mfma_f32_16x16x32_bf16 v[220:223],a[220:223],v[60:63],v[220:223]
v_mfma_f32_16x16x32_bf16 v[188:191],a[204:207],v[60:63],v[188:191]
s_nop 2
v_mul_f32_dpp v212,v14,v212 row_newbcast:0 row_mask:0xf bank_mask:0xf
v_mul_f32_dpp v213,v14,v213 row_newbcast:1 row_mask:0xf bank_mask:0xf
v_mul_f32_dpp v214,v14,v214 row_newbcast:2 row_mask:0xf bank_mask:0xf
v_mfma_f32_16x16x32_bf16 v[188:191],a[208:211],v[32:35],v[188:191]
v_mul_f32_dpp v215,v14,v215 row_newbcast:3 row_mask:0xf bank_mask:0xf
v_mul_f32_dpp v172,v14,v172 row_newbcast:4 row_mask:0xf bank_mask:0xf
v_mul_f32_dpp v173,v14,v173 row_newbcast:5 row_mask:0xf bank_mask:0xf
v_mfma_f32_16x16x32_bf16 v[228:231],a[244:247],v[32:35],v[228:231]
v_mul_f32_dpp v174,v14,v174 row_newbcast:6 row_mask:0xf bank_mask:0xf
v_mul_f32_dpp v175,v14,v175 row_newbcast:7 row_mask:0xf bank_mask:0xf
v_cvt_pk_bf16_f32 v212,v212,v213
v_mfma_f32_16x16x32_bf16 v[228:231],a[252:255],v[60:63],v[228:231]
v_cvt_pk_bf16_f32 v213,v214,v215
v_cvt_pk_bf16_f32 v214,v172,v173
v_cvt_pk_bf16_f32 v215,v174,v175
buffer_store_dwordx4 v[212:215],v7,s[40:43],s11 offen offset:192 sc1
v_mfma_f32_16x16x32_bf16 v[204:207],a[232:235],v[60:63],v[204:207]
v_mul_f32_dpp v220,v14,v220 row_newbcast:8 row_mask:0xf bank_mask:0xf
v_mul_f32_dpp v221,v14,v221 row_newbcast:9 row_mask:0xf bank_mask:0xf
v_mul_f32_dpp v222,v14,v222 row_newbcast:10 row_mask:0xf bank_mask:0xf
v_mfma_f32_16x16x32_bf16 v[204:207],a[240:243],v[32:35],v[204:207]
v_mul_f32_dpp v223,v14,v223 row_newbcast:11 row_mask:0xf bank_mask:0xf
v_mul_f32_dpp v188,v14,v188 row_newbcast:12 row_mask:0xf bank_mask:0xf
v_mul_f32_dpp v189,v14,v189 row_newbcast:13 row_mask:0xf bank_mask:0xf
v_mul_f32_dpp v190,v14,v190 row_newbcast:14 row_mask:0xf bank_mask:0xf
v_mul_f32_dpp v191,v14,v191 row_newbcast:15 row_mask:0xf bank_mask:0xf
v_cvt_pk_bf16_f32 v220,v220,v221
v_cvt_pk_bf16_f32 v221,v222,v223
v_cvt_pk_bf16_f32 v222,v188,v189
v_cvt_pk_bf16_f32 v223,v190,v191
buffer_store_dwordx4 v[220:223],v7,s[40:43],s11 offen offset:256 sc1
v_mul_f32_dpp v228,v15,v228 row_newbcast:0 row_mask:0xf bank_mask:0xf
v_mul_f32_dpp v229,v15,v229 row_newbcast:1 row_mask:0xf bank_mask:0xf
v_mul_f32_dpp v230,v15,v230 row_newbcast:2 row_mask:0xf bank_mask:0xf
v_mul_f32_dpp v231,v15,v231 row_newbcast:3 row_mask:0xf bank_mask:0xf
v_mul_f32_dpp v204,v15,v204 row_newbcast:4 row_mask:0xf bank_mask:0xf
v_mul_f32_dpp v205,v15,v205 row_newbcast:5 row_mask:0xf bank_mask:0xf
v_mul_f32_dpp v206,v15,v206 row_newbcast:6 row_mask:0xf bank_mask:0xf
v_mul_f32_dpp v207,v15,v207 row_newbcast:7 row_mask:0xf bank_mask:0xf
v_cvt_pk_bf16_f32 v228,v228,v229
v_cvt_pk_bf16_f32 v229,v230,v231
v_cvt_pk_bf16_f32 v230,v204,v205
v_cvt_pk_bf16_f32 v231,v206,v207
buffer_store_dwordx4 v[228:231],v7,s[40:43],s11 offen offset:320 sc1
v_or3_b32 v72,v72,v73,v74
v_or_b32 v72,v72,v75
s_nop 0
v_readfirstlane_b32 s64,v72
s_cmp_lg_u32 s64,0
s_cbranch_scc1 N_fallback_%=
s_endpgm
N_mainend_%=:
.fill 512,4,0xbf800000
N_fallback_%=:
s_waitcnt vmcnt(0) lgkmcnt(0)
s_barrier
)ASM"
    :: "s"(blockIdx.x), "v"(tid), "s"(ka)
    : "v1", "v2", "v3", "v4", "v5", "v6", "v7", "v8", "v9", "v10", "v11", "v12", "v13", "v14", "v15", "v16", "v17", "v18", "v19", "v20", "v21", "v22", "v23", "v24", "v25", "v26", "v27", "v28", "v29", "v30", "v31", "v32", "v33", "v34", "v35", "v36", "v37", "v38", "v39", "v40", "v41", "v42", "v43", "v44", "v45", "v46", "v47", "v48", "v49", "v50", "v51", "v52", "v53", "v54", "v55", "v56", "v57", "v58", "v59", "v60", "v61", "v62", "v63", "v64", "v65", "v66", "v67", "v68", "v69", "v70", "v71", "v72", "v73", "v74", "v75", "v76", "v77", "v78", "v79", "v80", "v81", "v82", "v83", "v84", "v85", "v86", "v87", "v88", "v89", "v90", "v91", "v92", "v93", "v94", "v95", "v96", "v97", "v98", "v99", "v100", "v101", "v102", "v103", "v104", "v105", "v106", "v107", "v108", "v109", "v110", "v111", "v112", "v113", "v114", "v115", "v116", "v117", "v118", "v119", "v120", "v121", "v122", "v123", "v124", "v125", "v126", "v127", "v128", "v129", "v130", "v131", "v132", "v133", "v134", "v135", "v136", "v137", "v138", "v139", "v140", "v141", "v142", "v143", "v144", "v145", "v146", "v147", "v148", "v149", "v150", "v151", "v152", "v153", "v154", "v155", "v156", "v157", "v158", "v159", "v160", "v161", "v162", "v163", "v164", "v165", "v166", "v167", "v168", "v169", "v170", "v171", "v172", "v173", "v174", "v175", "v176", "v177", "v178", "v179", "v180", "v181", "v182", "v183", "v184", "v185", "v186", "v187", "v188", "v189", "v190", "v191", "v192", "v193", "v194", "v195", "v196", "v197", "v198", "v199", "v200", "v201", "v202", "v203", "v204", "v205", "v206", "v207", "v208", "v209", "v210", "v211", "v212", "v213", "v214", "v215", "v216", "v217", "v218", "v219", "v220", "v221", "v222", "v223", "v224", "v225", "v226", "v227", "v228", "v229", "v230", "v231", "v232", "v233", "v234", "v235", "v236", "v237", "v238", "v239", "v240", "v241", "v242", "v243", "v244", "v245", "v246", "v247", "v248", "v249", "v250", "v251", "v252", "v253", "v254", "v255", "a0", "a1", "a2", "a3", "a4", "a5", "a6", "a7", "a8", "a9", "a10", "a11", "a12", "a13", "a14", "a15", "a16", "a17", "a18", "a19", "a20", "a21", "a22", "a23", "a24", "a25", "a26", "a27", "a28", "a29", "a30", "a31", "a32", "a33", "a34", "a35", "a36", "a37", "a38", "a39", "a40", "a41", "a42", "a43", "a44", "a45", "a46", "a47", "a48", "a49", "a50", "a51", "a52", "a53", "a54", "a55", "a56", "a57", "a58", "a59", "a60", "a61", "a62", "a63", "a64", "a65", "a66", "a67", "a68", "a69", "a70", "a71", "a72", "a73", "a74", "a75", "a76", "a77", "a78", "a79", "a80", "a81", "a82", "a83", "a84", "a85", "a86", "a87", "a88", "a89", "a90", "a91", "a92", "a93", "a94", "a95", "a96", "a97", "a98", "a99", "a100", "a101", "a102", "a103", "a104", "a105", "a106", "a107", "a108", "a109", "a110", "a111", "a112", "a113", "a114", "a115", "a116", "a117", "a118", "a119", "a120", "a121", "a122", "a123", "a124", "a125", "a126", "a127", "a128", "a129", "a130", "a131", "a132", "a133", "a134", "a135", "a136", "a137", "a138", "a139", "a140", "a141", "a142", "a143", "a144", "a145", "a146", "a147", "a148", "a149", "a150", "a151", "a152", "a153", "a154", "a155", "a156", "a157", "a158", "a159", "a160", "a161", "a162", "a163", "a164", "a165", "a166", "a167", "a168", "a169", "a170", "a171", "a172", "a173", "a174", "a175", "a176", "a177", "a178", "a179", "a180", "a181", "a182", "a183", "a184", "a185", "a186", "a187", "a188", "a189", "a190", "a191", "a192", "a193", "a194", "a195", "a196", "a197", "a198", "a199", "a200", "a201", "a202", "a203", "a204", "a205", "a206", "a207", "a208", "a209", "a210", "a211", "a212", "a213", "a214", "a215", "a216", "a217", "a218", "a219", "a220", "a221", "a222", "a223", "a224", "a225", "a226", "a227", "a228", "a229", "a230", "a231", "a232", "a233", "a234", "a235", "a236", "a237", "a238", "a239", "a240", "a241", "a242", "a243", "a244", "a245", "a246", "a247", "a248", "a249", "a250", "a251", "a252", "a253", "a254", "a255", "s4", "s5", "s6", "s7", "s8", "s9", "s10", "s11", "s12", "s13", "s14", "s15", "s16", "s17", "s18", "s19", "s20", "s21", "s22", "s23", "s24", "s25", "s26", "s27", "s28", "s29", "s30", "s31", "s32", "s33", "s34", "s35", "s36", "s37", "s38", "s39", "s40", "s41", "s42", "s43", "s44", "s45", "s46", "s47", "s48", "s49", "s50", "s51", "s52", "s53", "s54", "s55", "s56", "s57", "s58", "s59", "s60", "s61", "s62", "s63", "s64", "s65", "s66", "s67", "s68", "s69", "s70", "s71", "s72", "s73", "s74", "s75", "s76", "s77", "s78", "s79", "s80", "s81", "s82", "s83", "s84", "s85", "s86", "s87", "s88", "s89", "s90", "s91", "s92", "s93", "s94", "s95", "s96", "s97", "s98", "s99", "vcc", "scc", "memory");
    asm volatile("" : "+s"(ka));
    typedef const unsigned short* __attribute__((address_space(1))) gp;
    unsigned long long const __attribute__((address_space(4)))* kp = (unsigned long long const __attribute__((address_space(4)))*)ka;
    const unsigned short* x = (const unsigned short*)kp[0]; const unsigned short* dw = (const unsigned short*)kp[1];
    const unsigned short* w1 = (const unsigned short*)kp[2]; const unsigned short* b1 = (const unsigned short*)kp[3];
    const unsigned short* w2 = (const unsigned short*)kp[4]; const unsigned short* b2 = (const unsigned short*)kp[5];
    const unsigned short* gamma = (const unsigned short*)kp[6]; unsigned short* out = (unsigned short*)kp[7];
    extern __shared__ float sm[];
    float* cv = sm; float* hid = sm + 16 * 96; float* stt = hid + 16 * 384;
    const int x0 = (blockIdx.x >> 3) * 16, y0 = (blockIdx.x & 7) * 64;
    for (int y = y0; y < y0 + 64; ++y) {
        for (int i = tid; i < 16 * 96; i += 256) {
            int p = i / 96, c = i - p * 96; float s = 0.f;
            for (int ky = 0; ky < 3; ++ky) for (int kx = 0; kx < 3; ++kx) {
                int yy = y + ky - 1, xx = x0 + p + kx - 1;
                float v = (yy >= 0 && yy < 512 && xx >= 0 && xx < 512) ? bf2f(x[((size_t)yy * 512 + xx) * 96 + c]) : 0.f;
                s = fmaf(v, bf2f(dw[(ky * 3 + kx) * 96 + c]), s);
            }
            cv[i] = s;
        }
        __syncthreads();
        if (tid < 16) {
            float m = 0.f; for (int c = 0; c < 96; ++c) m += cv[tid * 96 + c];
            m *= (1.f / 96); float q = 0.f;
            for (int c = 0; c < 96; ++c) { float d = cv[tid * 96 + c] - m; q = fmaf(d, d, q); }
            stt[tid] = m; stt[16 + tid] = 1.f / sqrtf(q * (1.f / 96) + 1e-5f);
        }
        __syncthreads();
        for (int i = tid; i < 16 * 96; i += 256) { int p = i / 96; cv[i] = rbf((cv[i] - stt[p]) * stt[16 + p]); }
        __syncthreads();
        for (int e = tid; e < 384; e += 256) {
            float a[16];
#pragma unroll
            for (int p = 0; p < 16; ++p) a[p] = 0.f;
            for (int c = 0; c < 96; ++c) {
                float w = bf2f(w1[c * 384 + e]);
#pragma unroll
                for (int p = 0; p < 16; ++p) a[p] = fmaf(cv[p * 96 + c], w, a[p]);
            }
            float bb = bf2f(b1[e]);
#pragma unroll
            for (int p = 0; p < 16; ++p) {
                float h = a[p] + bb;
                hid[p * 384 + e] = rbf(0.5f * h * (1.f + tanhf(0.7978845608028654f * (h + 0.044715f * h * h * h))));
            }
        }
        __syncthreads();
        for (int i = tid; i < 16 * 96; i += 256) {
            int p = i / 96, c = i - p * 96; float s = 0.f;
            for (int e = 0; e < 384; ++e) s = fmaf(hid[p * 384 + e], bf2f(w2[e * 96 + c]), s);
            size_t o = ((size_t)y * 512 + x0 + p) * 96 + c;
            out[o] = f2bf(bf2f(x[o]) + bf2f(gamma[c]) * (s + bf2f(b2[c])));
        }
        __syncthreads();
    }

}

extern "C" void kernel_launch(void* const* d_in, const int* in_sizes, int n_in,
                              void* d_out, int out_size, void* d_ws, size_t ws_size,
                              hipStream_t stream) {
    (void)hipFuncSetAttribute((const void*)convnext_kernel, hipFuncAttributeMaxDynamicSharedMemorySize, LDS_BYTES);
    convnext_kernel<<<256, 256, LDS_BYTES, stream>>>(
        (const unsigned short*)d_in[0], (const unsigned short*)d_in[1], (const unsigned short*)d_in[2], (const unsigned short*)d_in[3],
        (const unsigned short*)d_in[4], (const unsigned short*)d_in[5], (const unsigned short*)d_in[6], (unsigned short*)d_out);
}
```

```cpp
#include <hip/hip_runtime.h>
#include <stdint.h>

constexpr int LDS_BYTES = 163584;

__device__ __forceinline__ float bf2f(unsigned short b) { return __uint_as_float((unsigned)b << 16); }
__device__ __forceinline__ unsigned short f2bf(float f) { unsigned r; asm("v_cvt_pk_bf16_f32 %0, %1, %1" : "=v"(r) : "v"(f)); return (unsigned short)r; }
__device__ __forceinline__ float rbf(float f) { return bf2f(f2bf(f)); }

__global__ __launch_bounds__(256, 1) __attribute__((aligned(4096))) void convnext_kernel(
    const unsigned short* __restrict__ x_, const unsigned short* __restrict__ dw_, const unsigned short* __restrict__ w1_,
    const unsigned short* __restrict__ b1_, const unsigned short* __restrict__ w2_, const unsigned short* __restrict__ b2_,
    const unsigned short* __restrict__ gamma_, unsigned short* __restrict__ out_) {
    const unsigned tid = threadIdx.x;
    unsigned long long ka = (unsigned long long)__builtin_amdgcn_kernarg_segment_ptr();
    asm volatile(
R"ASM(
s_load_dwordx16 s[20:35],%2,0
v_lshrrev_b32 v27,6,%1
v_and_b32 v24,63,%1
v_readfirstlane_b32 s52,v27
s_lshr_b32 s70,%0,3
v_and_b32_e64 v61,8,v24
v_lshl_add_u32 v62,s70,2,v27
v_mul_u32_u24 v61,15,v61
s_getpc_b64 s[14:15]
N_pf_%=:
v_or_b32 v61,7,v61
v_and_b32 v62,v61,v62
v_and_b32_e64 v61,7,v24
v_lshl_add_u32 v62,v62,3,v61
v_lshlrev_b32_e64 v62,7,v62
v_min_u32 v62,N_mainend_%=-N_pf_%=+2048,v62
global_load_dword v255,v62,s[14:15]
v_mul_u32_u24 v30,52,v27
v_add_u32 v30,v30,v24
v_subrev_u32_e64 v29,4,v30
v_max_i32 v29,0xc0,v29
v_min_u32_e64 v30,v30,v29
v_mul_u32_u24 v28,0xaaab,v30
s_and_b32 s64,%0,7
s_lshr_b32 s70,%0,3
v_lshrrev_b32 v28,19,v28
s_lshl_b32 s71,s64,6
s_mul_i32 s68,s70,3072
v_bfe_u32 v27,v28,2,2
s_sub_u32 s65,s71,1
s_mul_i32 s56,s65,98304
v_xor_b32 v27,v30,v27
s_add_u32 s56,s56,s68
s_cmp_lt_i32 s65,0
s_cselect_b32 s89,0x70000000,s56
v_lshlrev_b32 v27,4,v27
s_cmp_eq_u32 s70,0
s_cselect_b32 s67,0,99
s_cmp_eq_u32 s70,31
s_cselect_b32 s67,17,s67
v_mov_b32 v29,0x7fff0000
v_cmp_eq_u32_e32 vcc,s67,v28
s_mul_i32 s57,s52,832
s_add_u32 s57,s57,73728
v_cndmask_b32_e32 v6,v27,v29,vcc
s_mov_b32 s38,50331840
s_mov_b32 s39,0x00020000
s_mov_b32 s50,1728
s_mov_b32 s51,0x00020000
v_lshlrev_b32_e64 v8,4,v24
v_mul_u32_u24 v44,79,v24
v_add_u32 v45,5056,v44
v_lshrrev_b32 v44,10,v44
v_lshrrev_b32 v45,10,v45
v_sub_u32 v44,v24,v44
v_sub_u32 v45,v24,v45
v_lshlrev_b32_e64 v44,4,v44
v_add_lshl_u32 v45,v45,64,4
v_min_u32 v28,191,%1
v_lshlrev_b32_e64 v32,2,v28
v_min_u32 v34,95,%1
v_lshlrev_b32 v33,1,v34
v_and_b32 v27,15,v24
v_lshrrev_b32_e64 v28,4,v24
s_mul_i32 s66,s52,192
v_min_u32_e64 v27,11,v27
v_mul_u32_u24 v28,18432,v28
v_lshl_add_u32 v27,v27,4,s66
s_mul_i32 s66,s52,18432
v_add_u32 v172,v27,v28
v_add_u32 v173,s66,v8
s_and_b32 s64,s52,1
s_mul_i32 s65,s64,48
v_bfe_u32 v27,v24,3,2
v_and_b32 v28,4,v24
v_lshrrev_b32 v29,5,v24
v_add3_u32 v27,v27,v28,s65
v_lshl_add_u32 v27,v29,3,v27
v_mul_u32_u24 v27,768,v27
v_and_b32 v28,3,v24
v_xor_b32 v28,v28,v29
v_lshl_add_u32 v27,v28,4,v27
s_mul_i32 s65,s64,3072
v_add_u32 v28,s65,v8
s_mul_i32 s67,s64,3080
s_add_u32 s67,s67,40896
s_add_u32 s65,s65,16256
s_cmp_lt_u32 s52,2
s_cselect_b32 s58,s65,s67
s_cselect_b64 vcc,-1,0
s_movk_i32 s59,6144
s_cselect_b32 s59,-22464,s59
s_cselect_b32 s53,11264,0
s_cselect_b32 s88,0,67584
s_cselect_b32 s87,64,0
s_cselect_b32 s69,0,0x70000000
s_mov_b32 s62,32832
s_cselect_b32 s62,88272,s62
s_add_u32 s62,s62,s58
v_cndmask_b32_e32 v5,v28,v27,vcc
s_mov_b32 s46,73728
s_mov_b32 s47,0x00020000
s_waitcnt lgkmcnt(0)
s_sub_u32 s36,s20,192
s_subb_u32 s37,s21,0
s_and_b32 s37,s37,0xffff
s_add_u32 m0,s57,0
s_nop 0
buffer_load_dwordx4 v6,s[36:39],s89 offen lds
s_add_u32 m0,s57,3536
s_add_u32 s89,s56,98304
buffer_load_dwordx4 v6,s[36:39],s89 offen lds
s_add_u32 m0,s57,7080
s_add_u32 s89,s56,196608
s_nop 0
buffer_load_dwordx4 v6,s[36:39],s89 offen lds
s_mov_b32 s48,s22
s_and_b32 s49,s23,0xffff
s_mov_b32 m0,12288
global_load_ushort v37,v33,s[32:33]
buffer_load_dwordx4 v44,s[48:51],0 offen lds
s_mov_b32 m0,13312
global_load_dword v35,v32,s[26:27]
buffer_load_dwordx4 v45,s[48:51],0 offen lds
global_load_ushort v36,v33,s[30:31]
global_load_dwordx4 v[160:163],v172,s[24:25]
global_load_dwordx4 v[164:167],v172,s[24:25] offset:3840
global_load_dwordx4 v[168:171],v173,s[28:29]
s_cmp_lt_u32 s52,2
s_cselect_b64 s[44:45],s[24:25],s[28:29]
s_and_b32 s45,s45,0xffff
s_mov_b32 s99,0x72800000
s_mov_b32 s98,0xffff0000
s_mov_b64 s[90:91],0
v_mov_b32 v96,0x409d3ef8
v_lshlrev_b32 v102,4,%1
v_lshlrev_b32 v104,19,%1
v_cmp_eq_u32_e64 s[94:95],0,%1
v_add_u32 v103,0x8000,v102
s_mov_b32 s4,0x3f000000
s_mov_b32 s5,0x3fc00000
s_mov_b32 s6,0x40200000
s_mov_b32 s7,0x40600000
s_mov_b32 s8,0x40900000
s_mov_b32 s9,0x40b00000
s_mov_b32 s10,0x40d00000
s_mov_b32 s11,0x40f00000
s_mov_b32 s12,0x3f80222e
s_mov_b32 s13,0x13089abf
s_mov_b32 s96,0xebefe000
v_add_u32 v28,0x10000,v104
s_add_u32 m0,s57,10616
s_add_u32 s89,s56,294912
buffer_load_dwordx4 v6,s[36:39],s89 offen lds
v_mul_f32_e64 v98,s99,v104
v_fma_f32 v100,v28,s99,-v98
v_pk_fma_f32 v[64:65],v[100:101],s[4:5],v[98:99] op_sel_hi:[0,1,0]
v_pk_fma_f32 v[66:67],v[100:101],s[6:7],v[98:99] op_sel_hi:[0,1,0]
v_pk_fma_f32 v[68:69],v[100:101],s[8:9],v[98:99] op_sel_hi:[0,1,0]
v_pk_fma_f32 v[70:71],v[100:101],s[10:11],v[98:99] op_sel_hi:[0,1,0]
v_cndmask_b32_e64 v64,v64,0,s[94:95]
v_pk_mul_f32 v[72:73],v[64:65],v[64:65]
v_pk_mul_f32 v[74:75],v[66:67],v[66:67]
v_pk_mul_f32 v[76:77],v[68:69],v[68:69]
v_pk_mul_f32 v[78:79],v[70:71],v[70:71]
v_pk_fma_f32 v[72:73],v[72:73],s[12:13],v[96:97] op_sel_hi:[1,0,0]
v_pk_fma_f32 v[74:75],v[74:75],s[12:13],v[96:97] op_sel_hi:[1,0,0]
v_pk_fma_f32 v[76:77],v[76:77],s[12:13],v[96:97] op_sel_hi:[1,0,0]
v_pk_fma_f32 v[78:79],v[78:79],s[12:13],v[96:97] op_sel_hi:[1,0,0]
s_add_u32 m0,s57,14144
s_add_u32 s89,s56,393216
buffer_load_dwordx4 v6,s[36:39],s89 offen lds
v_pk_mul_f32 v[72:73],v[72:73],v[64:65]
v_pk_mul_f32 v[74:75],v[74:75],v[66:67]
v_pk_mul_f32 v[76:77],v[76:77],v[68:69]
v_pk_mul_f32 v[78:79],v[78:79],v[70:71]
v_exp_f32 v72,v72
v_exp_f32 v73,v73
v_exp_f32 v74,v74
v_exp_f32 v75,v75
v_exp_f32 v76,v76
v_exp_f32 v77,v77
s_add_u32 m0,s57,17680
s_add_u32 s89,s56,491520
buffer_load_dwordx4 v6,s[36:39],s89 offen lds
v_exp_f32 v78,v78
v_exp_f32 v79,v79
v_pk_fma_f32 v[72:73],v[72:73],s[96:97],s[96:97] op_sel_hi:[1,0,0]
v_pk_fma_f32 v[74:75],v[74:75],s[96:97],s[96:97] op_sel_hi:[1,0,0]
v_pk_fma_f32 v[76:77],v[76:77],s[96:97],s[96:97] op_sel_hi:[1,0,0]
v_pk_fma_f32 v[78:79],v[78:79],s[96:97],s[96:97] op_sel_hi:[1,0,0]
v_rcp_f32 v72,v72
v_rcp_f32 v73,v73
v_rcp_f32 v74,v74
v_rcp_f32 v75,v75
s_add_u32 m0,s57,21224
s_add_u32 s89,s56,589824
buffer_load_dwordx4 v6,s[36:39],s89 offen lds
v_rcp_f32 v76,v76
v_rcp_f32 v77,v77
v_rcp_f32 v78,v78
v_rcp_f32 v79,v79
v_pk_mul_f32 v[72:73],v[72:73],v[64:65]
v_pk_mul_f32 v[74:75],v[74:75],v[66:67]
v_pk_mul_f32 v[76:77],v[76:77],v[68:69]
v_pk_mul_f32 v[78:79],v[78:79],v[70:71]
v_pk_fma_f32 v[64:65],v[64:65],s[12:13],v[72:73] op_sel:[0,1,0] op_sel_hi:[1,1,1]
v_pk_fma_f32 v[66:67],v[66:67],s[12:13],v[74:75] op_sel:[0,1,0] op_sel_hi:[1,1,1]
v_pk_fma_f32 v[68:69],v[68:69],s[12:13],v[76:77] op_sel:[0,1,0] op_sel_hi:[1,1,1]
s_add_u32 m0,s57,24760
s_add_u32 s89,s56,688128
buffer_load_dwordx4 v6,s[36:39],s89 offen lds
v_pk_fma_f32 v[70:71],v[70:71],s[12:13],v[78:79] op_sel:[0,1,0] op_sel_hi:[1,1,1]
v_cvt_pk_bf16_f32 v80,v72,v73
v_cvt_pk_bf16_f32 v81,v74,v75
v_cvt_pk_bf16_f32 v82,v76,v77
v_cvt_pk_bf16_f32 v83,v78,v79
v_cvt_pk_bf16_f32 v84,v64,v65
v_cvt_pk_bf16_f32 v85,v66,v67
v_cvt_pk_bf16_f32 v86,v68,v69
v_cvt_pk_bf16_f32 v87,v70,v71
ds_write_b128 v102,v[80:83] offset:0
ds_write_b128 v103,v[84:87] offset:32768
v_add_u32 v104,0x8000000,v104
v_add_u32 v28,0x10000,v104
v_mul_f32_e64 v98,s99,v104
v_fma_f32 v100,v28,s99,-v98
s_add_u32 m0,s57,28288
s_add_u32 s89,s56,786432
buffer_load_dwordx4 v6,s[36:39],s89 offen lds
v_pk_fma_f32 v[64:65],v[100:101],s[4:5],v[98:99] op_sel_hi:[0,1,0]
v_pk_fma_f32 v[66:67],v[100:101],s[6:7],v[98:99] op_sel_hi:[0,1,0]
v_pk_fma_f32 v[68:69],v[100:101],s[8:9],v[98:99] op_sel_hi:[0,1,0]
v_pk_fma_f32 v[70:71],v[100:101],s[10:11],v[98:99] op_sel_hi:[0,1,0]
v_pk_mul_f32 v[72:73],v[64:65],v[64:65]
v_pk_mul_f32 v[74:75],v[66:67],v[66:67]
v_pk_mul_f32 v[76:77],v[68:69],v[68:69]
v_pk_mul_f32 v[78:79],v[70:71],v[70:71]
v_pk_fma_f32 v[72:73],v[72:73],s[12:13],v[96:97] op_sel_hi:[1,0,0]
v_pk_fma_f32 v[74:75],v[74:75],s[12:13],v[96:97] op_sel_hi:[1,0,0]
v_pk_fma_f32 v[76:77],v[76:77],s[12:13],v[96:97] op_sel_hi:[1,0,0]
v_pk_fma_f32 v[78:79],v[78:79],s[12:13],v[96:97] op_sel_hi:[1,0,0]
v_pk_mul_f32 v[72:73],v[72:73],v[64:65]
v_pk_mul_f32 v[74:75],v[74:75],v[66:67]
v_pk_mul_f32 v[76:77],v[76:77],v[68:69]
s_add_u32 m0,s57,31824
s_add_u32 s89,s56,884736
buffer_load_dwordx4 v6,s[36:39],s89 offen lds
v_pk_mul_f32 v[78:79],v[78:79],v[70:71]
v_exp_f32 v72,v72
v_exp_f32 v73,v73
v_exp_f32 v74,v74
v_exp_f32 v75,v75
v_exp_f32 v76,v76
v_exp_f32 v77,v77
v_exp_f32_e64 v78,v78
s_add_u32 m0,s57,35368
s_add_u32 s89,s56,983040
buffer_load_dwordx4 v6,s[36:39],s89 offen lds
v_exp_f32_e64 v79,v79
v_pk_fma_f32 v[72:73],v[72:73],s[96:97],s[96:97] op_sel_hi:[1,0,0]
v_pk_fma_f32 v[74:75],v[74:75],s[96:97],s[96:97] op_sel_hi:[1,0,0]
v_pk_fma_f32 v[76:77],v[76:77],s[96:97],s[96:97] op_sel_hi:[1,0,0]
v_pk_fma_f32 v[78:79],v[78:79],s[96:97],s[96:97] op_sel_hi:[1,0,0]
v_rcp_f32 v72,v72
v_rcp_f32 v73,v73
v_rcp_f32 v74,v74
v_rcp_f32 v75,v75
v_rcp_f32_e64 v76,v76
s_add_u32 m0,s57,38904
s_add_u32 s89,s56,1081344
buffer_load_dwordx4 v6,s[36:39],s89 offen lds
v_rcp_f32 v77,v77
v_rcp_f32 v78,v78
v_rcp_f32_e64 v79,v79
v_pk_mul_f32 v[72:73],v[72:73],v[64:65]
v_pk_mul_f32 v[74:75],v[74:75],v[66:67]
v_pk_mul_f32 v[76:77],v[76:77],v[68:69]
v_pk_mul_f32 v[78:79],v[78:79],v[70:71]
v_pk_fma_f32 v[64:65],v[64:65],s[12:13],v[72:73] op_sel:[0,1,0] op_sel_hi:[1,1,1]
v_pk_fma_f32 v[66:67],v[66:67],s[12:13],v[74:75] op_sel:[0,1,0] op_sel_hi:[1,1,1]
v_pk_fma_f32 v[68:69],v[68:69],s[12:13],v[76:77] op_sel:[0,1,0] op_sel_hi:[1,1,1]
v_pk_fma_f32 v[70:71],v[70:71],s[12:13],v[78:79] op_sel:[0,1,0] op_sel_hi:[1,1,1]
v_cvt_pk_bf16_f32 v88,v72,v73
v_cvt_pk_bf16_f32 v89,v74,v75
v_cvt_pk_bf16_f32 v90,v76,v77
v_cvt_pk_bf16_f32 v91,v78,v79
v_cvt_pk_bf16_f32 v92,v64,v65
v_cvt_pk_bf16_f32 v93,v66,v67
v_cvt_pk_bf16_f32 v94,v68,v69
v_cvt_pk_bf16_f32 v95,v70,v71
ds_write_b128 v102,v[88:91] offset:4096
ds_write_b128 v103,v[92:95] offset:36864
s_mov_b32 s96,0x8befe000
s_lshl_b32 s66,s52,2
s_mul_i32 s60,s66,3536
s_add_u32 s66,s66,s71
s_lshl2_add_u32 s16,s70,s52
s_mul_i32 s16,s16,512
v_and_b32 v25,15,v24
v_lshrrev_b32_e64 v26,4,v24
s_mul_i32 s61,s66,98304
s_add_u32 s61,s61,s68
s_mov_b32 s54,0x44ea44ea
s_mov_b32 s55,0x877f877f
s_mov_b64 s[40:41],s[34:35]
s_and_b32 s41,s41,0xffff
s_mov_b32 s42,50331648
s_mov_b32 s43,0x00020000
v_mov_b32 v41,0x70000
s_add_u32 m0,s57,42432
s_add_u32 s89,s56,1179648
buffer_load_dwordx4 v6,s[36:39],s89 offen lds
v_and_b32 v39,8,v24
v_and_b32 v40,7,v24
v_bfe_u32 v27,v24,1,1
v_sub_u32_e64 v27,v26,v27
v_cmp_gt_u32_e64 s[92:93],3,v27
v_and_b32 v28,1,v24
v_mul_u32_u24 v27,3,v27
v_sub_u32_e64 v27,v27,v28
v_cmp_eq_u32_e64 s[94:95],0,v28
v_lshrrev_b32_e64 v29,2,v25
v_mul_i32_i24 v27,208,v27
v_lshl_add_u32 v27,v29,1,v27
v_mul_u32_u24 v10,0x5555,v29
v_add_u32 v27,12288,v27
v_cndmask_b32_e64 v45,v41,v27,s[92:93]
v_cndmask_b32_e64 v44,v41,v45,s[94:95]
v_cndmask_b32_e64 v47,v45,v41,s[94:95]
s_waitcnt vmcnt(13)
s_nop 0
ds_read_u16 a96,v44 offset:0
ds_read_u16 a97,v45 offset:208
ds_read_u16 a98,v45 offset:416
ds_read_u16 a99,v47 offset:624
ds_read_u16 a100,v44 offset:8
ds_read_u16 a101,v45 offset:216
ds_read_u16 a102,v45 offset:424
ds_read_u16 a103,v47 offset:632
v_mov_b64 v[20:21],0
v_mov_b64 v[22:23],0
v_cmp_eq_u32_e64 s[92:93],v26,v29
v_lshlrev_b32_e64 v27,4,v25
v_add_u32 v27,10112,v27
v_cndmask_b32_e64 v13,v41,v27,s[92:93]
v_lshrrev_b32 v27,2,v39
v_lshrrev_b32 v28,5,v24
v_add_u32_e64 v27,v27,v28
s_add_u32 m0,s57,45968
s_add_u32 s89,s56,1277952
buffer_load_dwordx4 v6,s[36:39],s89 offen lds
v_mul_u32_u24 v27,3536,v27
v_lshl_or_b32 v10,v27,16,v10
v_and_b32 v27,1,v26
v_add_u32 v27,v27,v40
v_mul_u32_u24 v28,384,v27
v_lshrrev_b32_e64 v27,1,v27
v_lshl_add_u32 v28,v27,4,v28
v_mfma_f32_32x32x16_bf16 v[64:79],v[20:23],v[20:23],0
ds_read_u16 a104,v44 offset:16
ds_read_u16 a105,v45 offset:224
ds_read_u16 a106,v45 offset:432
ds_read_u16 a107,v47 offset:640
ds_read_u16 a108,v44 offset:24
ds_read_u16 a109,v45 offset:232
ds_read_u16 a110,v45 offset:440
ds_read_u16 a111,v47 offset:648
v_add_u32 v30,73728,v39
v_add_u32 v11,v28,v30
s_movk_i32 s66,16384
v_lshl_add_u32 v27,v40,1,1
v_lshrrev_b32 v28,2,v27
v_xor_b32 v28,v28,v26
v_mul_u32_u24 v27,192,v27
v_lshl_add_u32 v27,v28,4,v27
v_add3_u32 v12,v27,v39,s66
v_lshl_add_u32 v27,v40,1,2
v_lshrrev_b32 v28,2,v27
v_xor_b32 v28,v28,v26
v_mul_u32_u24 v27,192,v27
v_lshl_add_u32 v27,v28,4,v27
v_add3_u32 v27,v27,v39,s66
v_lshl_or_b32 v12,v27,16,v12
v_mfma_f32_32x32x16_bf16 v[80:95],v[20:23],v[20:23],0
ds_read_u16 a112,v44 offset:32
ds_read_u16 a113,v45 offset:240
ds_read_u16 a114,v45 offset:448
ds_read_u16 a115,v47 offset:656
ds_read_u16 a116,v44 offset:40
ds_read_u16 a117,v45 offset:248
ds_read_u16 a118,v45 offset:456
ds_read_u16 a119,v47 offset:664
v_mul_u32_u24 v27,24576,v39
s_add_u32 m0,s57,49512
s_add_u32 s89,s56,1376256
buffer_load_dwordx4 v6,s[36:39],s89 offen lds
v_mul_u32_u24 v28,384,v40
v_lshl_add_u32 v27,v26,4,v27
v_add_u32 v7,v27,v28
v_lshrrev_b32 v27,3,v25
v_bfe_u32 v28,v25,2,1
v_lshl_add_u32 v28,v27,3,v28
v_lshl_add_u32 v28,v26,1,v28
v_lshlrev_b32_e64 v28,6,v28
v_lshl_add_u32 v28,v27,4,v28
v_and_b32_e64 v29,1,v25
v_lshl_add_u32 v28,v29,5,v28
v_bfe_u32 v29,v25,1,1
v_lshl_add_u32 v28,v29,3,v28
v_xor_b32_e64 v29,16,v28
v_add_u32 v1,16256,v28
v_mfma_f32_32x32x16_bf16 v[96:111],v[20:23],v[20:23],0
ds_read_u16 a120,v44 offset:48
ds_read_u16 a121,v45 offset:256
ds_read_u16 a122,v45 offset:464
ds_read_u16 a123,v47 offset:672
ds_read_u16 a124,v44 offset:56
ds_read_u16 a125,v45 offset:264
ds_read_u16 a126,v45 offset:472
ds_read_u16 a127,v47 offset:680
v_add_u32 v2,16256,v29
v_lshrrev_b32_e64 v27,3,v25
v_bfe_u32 v28,v25,2,1
v_lshl_add_u32 v27,v28,1,v27
v_lshrrev_b32_e64 v28,1,v26
v_lshl_add_u32 v27,v28,2,v27
v_mul_u32_u24 v27,192,v27
v_and_b32_e64 v29,1,v26
v_mul_u32_u24 v30,3080,v29
v_add_u32 v27,v27,v30
v_and_b32 v30,3,v25
s_add_u32 m0,s57,53048
s_add_u32 s89,s56,1474560
buffer_load_dwordx4 v6,s[36:39],s89 offen lds
v_lshl_add_u32 v27,v30,4,v27
v_add_u32 v3,40896,v27
v_lshlrev_b32_e64 v29,6,v29
v_lshl_add_u32 v29,v28,4,v29
v_add_u32 v4,8192,v29
v_mfma_f32_32x32x16_bf16 v[112:127],v[20:23],v[20:23],0
ds_read_u16 a128,v44 offset:64
ds_read_u16 a129,v45 offset:272
ds_read_u16 a130,v45 offset:480
ds_read_u16 a131,v47 offset:688
ds_read_u16 a132,v44 offset:72
ds_read_u16 a133,v45 offset:280
ds_read_u16 a134,v45 offset:488
ds_read_u16 a135,v47 offset:696
v_lshrrev_b32_e64 v27,5,v34
v_bfe_u32 v28,v34,2,1
v_and_b32 v29,3,v34
v_lshlrev_b32 v49,7,v27
v_lshl_add_u32 v49,v28,5,v49
v_lshl_add_u32 v49,v29,2,v49
v_bfe_u32 v30,v34,3,1
v_lshl_add_u32 v49,v30,6,v49
v_bfe_u32 v30,v34,4,1
v_lshl_add_u32 v49,v30,4,v49
v_add_u32 v49,11776,v49
v_lshrrev_b32_e64 v55,6,v34
v_bfe_u32 v30,v34,3,2
v_lshl_add_u32 v55,v55,2,v30
v_and_b32_e64 v30,1,v27
v_lshl_add_u32 v55,v55,1,v30
v_mfma_f32_32x32x16_bf16 v[128:143],v[20:23],v[20:23],0
ds_read_u16 a136,v44 offset:80
ds_read_u16 a137,v45 offset:288
ds_read_u16 a138,v45 offset:496
ds_read_u16 a139,v47 offset:704
ds_read_u16 a140,v44 offset:88
ds_read_u16 a141,v45 offset:296
ds_read_u16 a142,v45 offset:504
ds_read_u16 a143,v47 offset:712
v_and_b32_e64 v30,7,v34
s_add_u32 m0,s57,56576
s_add_u32 s89,s56,1572864
buffer_load_dwordx4 v6,s[36:39],s89 offen lds
v_lshl_add_u32 v55,v55,3,v30
v_lshlrev_b32 v55,2,v55
v_lshlrev_b32 v15,2,v24
v_lshl_add_u32 v50,v27,1,v28
v_lshlrev_b32_e64 v50,4,v50
v_bfe_u32 v30,v34,3,2
v_lshl_add_u32 v50,v30,2,v50
v_add_u32 v50,v50,v29
v_lshlrev_b32 v50,4,v50
v_add_u32 v50,10112,v50
v_lshlrev_b32_e64 v51,4,v29
v_cmp_eq_u32_e64 s[94:95],1,v28
v_lshlrev_b32_e64 v48,1,v32
v_add_u32 v48,8192,v48
v_mov_b32_e64 v53,0
v_mfma_f32_32x32x16_bf16 v[144:159],v[20:23],v[20:23],0
ds_read_u16 a144,v44 offset:96
ds_read_u16 a145,v45 offset:304
ds_read_u16 a146,v45 offset:512
ds_read_u16 a147,v47 offset:720
ds_read_u16 a148,v44 offset:104
ds_read_u16 a149,v45 offset:312
ds_read_u16 a150,v45 offset:520
ds_read_u16 a151,v47 offset:728
v_lshlrev_b32 v37,16,v37
v_lshlrev_b32 v36,16,v36
v_mov_b32 v27,0x45800000
v_mov_b32 v28,0x53800000
v_cmp_nlt_f32_e64 s[90:91],|v37|,v27
v_cmp_nlt_f32_e64 vcc,|v36|,v28
v_mov_b32 v27,0x42800000
v_mul_f32 v37,0x6c800000,v37
v_mul_f32 v36,0x12800000,v36
v_rcp_f32_e64 v56,v37
s_add_u32 m0,s57,60112
s_add_u32 s89,s56,1671168
buffer_load_dwordx4 v6,s[36:39],s89 offen lds
v_lshlrev_b32_e64 v42,16,v35
v_and_b32 v43,s98,v35
s_or_b64 s[90:91],s[90:91],vcc
v_cmp_nlt_f32_e64 vcc,|v42|,v27
v_cmp_nlt_f32_e64 s[92:93],|v43|,v27
v_med3_f32 v56,v56,1.0,-1.0
ds_read_u16 a152,v44 offset:112
ds_read_u16 a153,v45 offset:320
ds_read_u16 a154,v45 offset:528
ds_read_u16 a155,v47 offset:736
ds_read_u16 a156,v44 offset:120
ds_read_u16 a157,v45 offset:328
ds_read_u16 a158,v45 offset:536
ds_read_u16 a159,v47 offset:744
v_pk_mul_f32 v[42:43],v[42:43],s[96:97] op_sel_hi:[1,0]
v_cvt_pk_bf16_f32 v52,v56,v53
ds_write_b64 v48,v[42:43]
ds_write_b32 v49,v36
v_lshlrev_b32 v54,16,v52
v_rcp_f32 v54,v54
v_lshlrev_b64 v[52:53],v51,v[52:53]
s_or_b64 s[90:91],s[90:91],vcc
s_or_b64 s[90:91],s[90:91],s[92:93]
v_cndmask_b32_e64 v56,v52,0,s[94:95]
v_cndmask_b32_e64 v58,0,v52,s[94:95]
v_cndmask_b32_e64 v57,v53,0,s[94:95]
v_cndmask_b32_e64 v59,0,v53,s[94:95]
ds_write_b32 v15,v7 offset:14848
ds_write_b32 v15,v13 offset:15104
s_mul_i32 s66,s52,1536
ds_read_u16 a160,v44 offset:128
ds_read_u16 a161,v45 offset:336
ds_read_u16 a162,v45 offset:544
ds_read_u16 a163,v47 offset:752
ds_read_u16 a164,v44 offset:136
ds_read_u16 a165,v45 offset:344
ds_read_u16 a166,v45 offset:552
ds_read_u16 a167,v47 offset:760
s_add_u32 m0,s58,18480
s_nop 0
buffer_load_dwordx4 v5,s[44:47],s69 offen offset:0 lds
s_add_u32 s69,s69,s53
buffer_load_dwordx4 v5,s[44:47],s69 offen offset:1024 lds
s_mov_b64 exec,0
s_cmp_eq_u32 s52,0
s_cbranch_scc1 TE00_%=
s_cmp_eq_u32 s52,1
s_cbranch_scc1 TE01_%=
s_cmp_eq_u32 s52,2
s_cbranch_scc1 TE02_%=
s_branch TE03_%=
TR0_%=:
s_mov_b64 exec,-1
s_add_u32 s66,s66,16256
v_lshl_add_u32 v7,v24,3,s66
ds_write_b128 v50,v[56:59]
ds_write_b32 v55,v54 offset:14336
s_waitcnt vmcnt(17)
v_and_b32 v160,0x7fff7fff,v160
v_and_b32 v161,0x7fff7fff,v161
v_and_b32 v162,0x7fff7fff,v162
v_and_b32 v163,0x7fff7fff,v163
v_and_b32 v164,0x7fff7fff,v164
v_and_b32 v165,0x7fff7fff,v165
v_and_b32 v166,0x7fff7fff,v166
v_and_b32 v167,0x7fff7fff,v167
v_and_b32 v168,0x7fff7fff,v168
v_and_b32 v169,0x7fff7fff,v169
v_and_b32 v170,0x7fff7fff,v170
TE00_%=:
s_cbranch_execz TP2752_%=
ds_read_u16 a168,v44 offset:144
ds_read_u16 a169,v45 offset:352
ds_read_u16 a170,v45 offset:560
ds_read_u16 a171,v47 offset:768
ds_read_u16 a172,v44 offset:152
ds_read_u16 a173,v45 offset:360
ds_read_u16 a174,v45 offset:568
ds_read_u16 a175,v47 offset:776
v_and_b32 v171,0x7fff7fff,v171
s_add_u32 s69,s69,s53
buffer_load_dwordx4 v5,s[44:47],s69 offen offset:2048 lds
v_pk_max_u16 v160,v160,v164
v_pk_max_u16 v161,v161,v165
v_pk_max_u16 v162,v162,v166
v_pk_max_u16 v163,v163,v167
v_mov_b64 v[164:165],v[160:161]
v_mov_b64 v[166:167],v[162:163]
s_nop 0
v_permlane32_swap_b32 v160,v164
v_permlane32_swap_b32 v161,v165
v_permlane32_swap_b32 v162,v166
v_permlane32_swap_b32_e64 v163,v167
v_pk_max_u16 v160,v160,v164
v_pk_max_u16 v161,v161,v165
v_pk_max_u16 v162,v162,v166
v_pk_max_u16 v163,v163,v167
v_mov_b64_e64 v[164:165],v[160:161]
ds_read_u16 a176,v44 offset:160
ds_read_u16 a177,v45 offset:368
ds_read_u16 a178,v45 offset:576
ds_read_u16 a179,v47 offset:784
ds_read_u16 a180,v44 offset:168
ds_read_u16 a181,v45 offset:376
ds_read_u16 a182,v45 offset:584
ds_read_u16 a183,v47 offset:792
v_mov_b64 v[166:167],v[162:163]
v_permlane16_swap_b32 v160,v164
v_permlane16_swap_b32 v161,v165
v_permlane16_swap_b32 v162,v166
v_permlane16_swap_b32_e64 v163,v167
v_pk_max_u16 v160,v160,v164
v_pk_max_u16 v161,v161,v165
v_pk_max_u16 v162,v162,v166
v_pk_max_u16 v163,v163,v167
v_pk_min_u16 v160,v160,v161
v_pk_min_u16 v162,v162,v163
v_pk_min_u16 v160,v160,v162
v_pk_min_u16 v160,v160,v160 op_sel:[0,1] op_sel_hi:[1,0]
v_pk_max_u16 v168,v168,v169
v_pk_max_u16 v170,v170,v171
v_pk_max_u16 v168,v168,v170
ds_read_u16 a184,v44 offset:176
ds_read_u16 a185,v45 offset:384
ds_read_u16 a186,v45 offset:592
ds_read_u16 a187,v47 offset:800
ds_read_u16 a188,v44 offset:184
ds_read_u16 a189,v45 offset:392
ds_read_u16 a190,v45 offset:600
ds_read_u16 a191,v47 offset:808
v_pk_max_u16 v168,v168,v168 op_sel:[0,1] op_sel_hi:[1,0]
s_add_u32 m0,s58,0
s_mov_b32 s69,s87
buffer_load_dwordx4 v5,s[44:47],s69 offen offset:0 lds
v_cmp_gt_u16_e32 vcc,0x3880,v160
s_nop 0
s_or_b64 s[90:91],s[90:91],vcc
v_cmp_le_u16_e32 vcc,0x5380,v168
s_nop 0
s_or_b64 s[90:91],s[90:91],vcc
v_accvgpr_write_b32 a88,0
v_accvgpr_write_b32 a89,0
v_accvgpr_write_b32 a90,0
v_accvgpr_write_b32 a91,0
s_add_u32 s69,s69,s53
buffer_load_dwordx4 v5,s[44:47],s69 offen offset:1024 lds
s_add_u32 s69,s69,s53
buffer_load_dwordx4 v5,s[44:47],s69 offen offset:2048 lds
s_add_u32 m0,s58,6160
s_add_u32 s69,s69,s59
buffer_load_dwordx4 v5,s[44:47],s69 offen offset:0 lds
s_add_u32 s69,s69,s53
buffer_load_dwordx4 v5,s[44:47],s69 offen offset:1024 lds
s_add_u32 s69,s69,s53
buffer_load_dwordx4 v5,s[44:47],s69 offen offset:2048 lds
s_add_u32 m0,s58,12320
s_add_u32 s69,s69,s59
buffer_load_dwordx4 v5,s[44:47],s69 offen offset:0 lds
s_add_u32 s69,s69,s53
buffer_load_dwordx4 v5,s[44:47],s69 offen offset:1024 lds
s_add_u32 s69,s69,s53
buffer_load_dwordx4 v5,s[44:47],s69 offen offset:2048 lds
v_add_u32_sdwa v28,s60,v10 dst_sel:DWORD dst_unused:UNUSED_PAD src0_sel:DWORD src1_sel:WORD_1
v_subrev_u32 v8,88400,v28
v_min_u32_e64 v28,v28,v8
v_add_u32 v29,7072,v28
v_subrev_u32 v8,88400,v29
v_min_u32 v29,v29,v8
v_xad_u32 v24,v11,0,v28
v_xad_u32 v25,v11,8,v29
v_xad_u32 v26,v11,16,v28
v_xad_u32 v27,v11,24,v29
s_waitcnt lgkmcnt(0)
s_waitcnt vmcnt(12)
s_barrier
ds_read_b64 a[0:1],v24 offset:0
ds_read_b64 a[2:3],v24 offset:192
ds_read_b64 a[4:5],v25 offset:0
ds_read_b64 a[6:7],v25 offset:192
ds_read_b64 a[8:9],v24 offset:8
ds_read_b64 a[10:11],v24 offset:200
ds_read_b64 a[12:13],v25 offset:8
ds_read_b64 a[14:15],v25 offset:200
ds_read_b64 a[16:17],v26 offset:0
ds_read_b64 a[18:19],v26 offset:192
ds_read_b64 a[20:21],v27 offset:0
ds_read_b64 a[22:23],v27 offset:192
s_waitcnt lgkmcnt(8)
v_smfmac_f32_16x16x64_bf16 v[64:67],a[96:99],a[0:7],v10
s_waitcnt lgkmcnt(4)
ds_read_b64 a[0:1],v26 offset:8
ds_read_b64 a[2:3],v26 offset:200
ds_read_b64 a[4:5],v27 offset:8
ds_read_b64 a[6:7],v27 offset:200
v_smfmac_f32_16x16x64_bf16 v[68:71],a[100:103],a[8:15],v10
s_waitcnt lgkmcnt(4)
ds_read_b64 a[8:9],v24 offset:64
ds_read_b64 a[10:11],v24 offset:256
ds_read_b64 a[12:13],v25 offset:64
ds_read_b64 a[14:15],v25 offset:256
v_smfmac_f32_16x16x64_bf16 v[72:75],a[104:107],a[16:23],v10
s_waitcnt lgkmcnt(4)
ds_read_b64 a[16:17],v24 offset:72
ds_read_b64 a[18:19],v24 offset:264
ds_read_b64 a[20:21],v25 offset:72
ds_read_b64 a[22:23],v25 offset:264
v_and_b32 v248,8,v11
v_pk_mul_f32 v[40:41],v[64:65],v[64:65]
v_pk_mul_f32 v[42:43],v[66:67],v[66:67]
v_smfmac_f32_16x16x64_bf16 v[76:79],a[108:111],a[0:7],v10
s_waitcnt lgkmcnt(4)
ds_read_b64 a[0:1],v26 offset:64
ds_read_b64 a[2:3],v26 offset:256
ds_read_b64 a[4:5],v27 offset:64
ds_read_b64 a[6:7],v27 offset:256
v_mul_u32_u24 v248,884,v248
s_add_u32 s64,s60,3536
v_pk_add_f32 v[36:37],v[64:65],v[68:69]
v_pk_fma_f32 v[40:41],v[68:69],v[68:69],v[40:41]
v_pk_add_f32 v[38:39],v[66:67],v[70:71]
v_pk_fma_f32 v[42:43],v[70:71],v[70:71],v[42:43]
v_smfmac_f32_16x16x64_bf16 v[80:83],a[128:131],a[8:15],v10
s_waitcnt lgkmcnt(4)
ds_read_b64 a[8:9],v26 offset:72
ds_read_b64 a[10:11],v26 offset:264
ds_read_b64 a[12:13],v27 offset:72
ds_read_b64 a[14:15],v27 offset:264
v_add_u32 v251,s64,v248
v_pk_add_f32 v[36:37],v[36:37],v[72:73]
v_pk_fma_f32 v[40:41],v[72:73],v[72:73],v[40:41]
v_pk_add_f32 v[38:39],v[38:39],v[74:75]
v_pk_fma_f32 v[42:43],v[74:75],v[74:75],v[42:43]
v_smfmac_f32_16x16x64_bf16 v[84:87],a[132:135],a[16:23],v10
s_waitcnt lgkmcnt(4)
s_nop 0
ds_read_b64 a[16:17],v24 offset:128
ds_read_b64 a[18:19],v24 offset:320
ds_read_b64 a[20:21],v25 offset:128
ds_read_b64 a[22:23],v25 offset:320
v_subrev_u32 v8,88400,v251
v_pk_add_f32 v[36:37],v[36:37],v[76:77]
v_pk_fma_f32 v[40:41],v[76:77],v[76:77],v[40:41]
v_pk_add_f32 v[38:39],v[38:39],v[78:79]
v_pk_fma_f32 v[42:43],v[78:79],v[78:79],v[42:43]
v_smfmac_f32_16x16x64_bf16 v[88:91],a[136:139],a[0:7],v10
s_waitcnt lgkmcnt(4)
ds_read_b64 a[0:1],v24 offset:136
ds_read_b64 a[2:3],v24 offset:328
ds_read_b64 a[4:5],v25 offset:136
ds_read_b64 a[6:7],v25 offset:328
v_min_u32 v251,v251,v8
v_pk_add_f32 v[36:37],v[36:37],v[80:81]
v_pk_fma_f32 v[40:41],v[80:81],v[80:81],v[40:41]
v_pk_add_f32 v[38:39],v[38:39],v[82:83]
v_pk_fma_f32 v[42:43],v[82:83],v[82:83],v[42:43]
v_smfmac_f32_16x16x64_bf16 v[92:95],a[140:143],a[8:15],v10
s_waitcnt lgkmcnt(4)
ds_read_b64 a[8:9],v26 offset:128
ds_read_b64 a[10:11],v26 offset:320
ds_read_b64 a[12:13],v27 offset:128
ds_read_b64 a[14:15],v27 offset:320
v_xad_u32 v24,v11,32,v28
v_xad_u32 v25,v11,40,v29
v_pk_add_f32 v[36:37],v[36:37],v[84:85]
v_pk_fma_f32 v[40:41],v[84:85],v[84:85],v[40:41]
v_pk_add_f32 v[38:39],v[38:39],v[86:87]
v_pk_fma_f32 v[42:43],v[86:87],v[86:87],v[42:43]
v_smfmac_f32_16x16x64_bf16 v[96:99],a[160:163],a[16:23],v10
s_waitcnt lgkmcnt(4)
ds_read_b64 a[16:17],v26 offset:136
ds_read_b64 a[18:19],v26 offset:328
ds_read_b64 a[20:21],v27 offset:136
ds_read_b64 a[22:23],v27 offset:328
v_add_u32 v250,3536,v251
v_pk_add_f32 v[36:37],v[36:37],v[88:89]
v_pk_fma_f32 v[40:41],v[88:89],v[88:89],v[40:41]
v_pk_add_f32 v[38:39],v[38:39],v[90:91]
v_pk_fma_f32 v[42:43],v[90:91],v[90:91],v[42:43]
v_smfmac_f32_16x16x64_bf16 v[100:103],a[164:167],a[0:7],v10
s_waitcnt lgkmcnt(4)
ds_read_b64 a[0:1],v24 offset:0
ds_read_b64 a[2:3],v24 offset:192
ds_read_b64 a[4:5],v25 offset:0
ds_read_b64 a[6:7],v25 offset:192
v_xad_u32 v26,v11,48,v28
v_xad_u32 v27,v11,56,v29
v_pk_add_f32 v[36:37],v[36:37],v[92:93]
v_pk_fma_f32 v[40:41],v[92:93],v[92:93],v[40:41]
v_pk_add_f32 v[38:39],v[38:39],v[94:95]
v_pk_fma_f32 v[42:43],v[94:95],v[94:95],v[42:43]
v_smfmac_f32_16x16x64_bf16 v[104:107],a[168:171],a[8:15],v10
s_waitcnt lgkmcnt(4)
s_add_u32 m0,s57,63656
s_add_u32 s89,s56,1769472
buffer_load_dwordx4 v6,s[36:39],s89 offen lds
ds_read_b64 a[8:9],v24 offset:8
ds_read_b64 a[10:11],v24 offset:200
ds_read_b64 a[12:13],v25 offset:8
ds_read_b64 a[14:15],v25 offset:200
v_subrev_u32 v8,88400,v250
v_pk_add_f32 v[36:37],v[36:37],v[96:97]
v_pk_fma_f32 v[40:41],v[96:97],v[96:97],v[40:41]
v_pk_add_f32 v[38:39],v[38:39],v[98:99]
v_pk_fma_f32 v[42:43],v[98:99],v[98:99],v[42:43]
v_smfmac_f32_16x16x64_bf16 v[108:111],a[172:175],a[16:23],v10
s_waitcnt lgkmcnt(4)
s_add_u32 m0,s57,67192
s_add_u32 s89,s56,1867776
buffer_load_dwordx4 v6,s[36:39],s89 offen lds
ds_read_b64 a[16:17],v24 offset:64
ds_read_b64 a[18:19],v24 offset:256
ds_read_b64 a[20:21],v25 offset:64
ds_read_b64 a[22:23],v25 offset:256
v_min_u32 v250,v250,v8
v_pk_add_f32 v[36:37],v[36:37],v[100:101]
v_pk_fma_f32 v[40:41],v[100:101],v[100:101],v[40:41]
v_pk_add_f32 v[38:39],v[38:39],v[102:103]
v_pk_fma_f32 v[42:43],v[102:103],v[102:103],v[42:43]
v_smfmac_f32_16x16x64_bf16 v[112:115],a[112:115],a[0:7],v10
s_waitcnt lgkmcnt(4)
ds_read_b64 a[0:1],v24 offset:72
ds_read_b64 a[2:3],v24 offset:264
ds_read_b64 a[4:5],v25 offset:72
ds_read_b64 a[6:7],v25 offset:264
v_xor_b32 v9,0x80008,v12
v_pk_add_f32 v[36:37],v[36:37],v[104:105]
v_pk_fma_f32 v[40:41],v[104:105],v[104:105],v[40:41]
v_pk_add_f32 v[38:39],v[38:39],v[106:107]
v_pk_fma_f32 v[42:43],v[106:107],v[106:107],v[42:43]
v_smfmac_f32_16x16x64_bf16 v[116:119],a[116:119],a[8:15],v10
s_waitcnt lgkmcnt(4)
s_add_u32 m0,s57,70720
s_add_u32 s89,s56,1966080
buffer_load_dwordx4 v6,s[36:39],s89 offen lds
ds_read_b64 a[8:9],v24 offset:128
ds_read_b64 a[10:11],v24 offset:320
ds_read_b64 a[12:13],v25 offset:128
ds_read_b64 a[14:15],v25 offset:320
v_add_u32_sdwa v248,v12,v251 dst_sel:DWORD dst_unused:UNUSED_PAD src0_sel:WORD_0 src1_sel:DWORD
v_pk_add_f32 v[36:37],v[36:37],v[108:109]
v_pk_fma_f32 v[40:41],v[108:109],v[108:109],v[40:41]
v_pk_add_f32 v[38:39],v[38:39],v[110:111]
v_pk_fma_f32 v[42:43],v[110:111],v[110:111],v[42:43]
v_smfmac_f32_16x16x64_bf16 v[120:123],a[144:147],a[16:23],v10
s_waitcnt lgkmcnt(4)
ds_read_b64 a[16:17],v24 offset:136
ds_read_b64 a[18:19],v24 offset:328
ds_read_b64 a[20:21],v25 offset:136
ds_read_b64 a[22:23],v25 offset:328
v_add_u32_sdwa v249,v12,v251 dst_sel:DWORD dst_unused:UNUSED_PAD src0_sel:WORD_1 src1_sel:DWORD
v_pk_add_f32 v[36:37],v[36:37],v[112:113]
v_pk_fma_f32 v[40:41],v[112:113],v[112:113],v[40:41]
v_pk_add_f32 v[38:39],v[38:39],v[114:115]
v_pk_fma_f32 v[42:43],v[114:115],v[114:115],v[42:43]
v_smfmac_f32_16x16x64_bf16 v[124:127],a[148:151],a[0:7],v10
s_waitcnt lgkmcnt(4)
s_add_u32 m0,s57,74256
s_add_u32 s89,s56,2064384
buffer_load_dwordx4 v6,s[36:39],s89 offen lds
ds_read_b64 a[0:1],v26 offset:0
ds_read_b64 a[2:3],v26 offset:192
ds_read_b64 a[4:5],v27 offset:0
ds_read_b64 a[6:7],v27 offset:192
v_add_u32_sdwa v251,v9,v250 dst_sel:DWORD dst_unused:UNUSED_PAD src0_sel:WORD_1 src1_sel:DWORD
v_pk_add_f32 v[36:37],v[36:37],v[116:117]
v_pk_fma_f32 v[40:41],v[116:117],v[116:117],v[40:41]
v_pk_add_f32 v[38:39],v[38:39],v[118:119]
v_pk_fma_f32 v[42:43],v[118:119],v[118:119],v[42:43]
v_smfmac_f32_16x16x64_bf16 v[128:131],a[176:179],a[8:15],v10
s_waitcnt lgkmcnt(4)
s_nop 0
ds_read_b64 a[8:9],v26 offset:8
ds_read_b64 a[10:11],v26 offset:200
ds_read_b64 a[12:13],v27 offset:8
ds_read_b64 a[14:15],v27 offset:200
v_add_u32_sdwa v250,v9,v250 dst_sel:DWORD dst_unused:UNUSED_PAD src0_sel:WORD_0 src1_sel:DWORD
v_pk_add_f32 v[36:37],v[36:37],v[120:121]
v_pk_fma_f32 v[40:41],v[120:121],v[120:121],v[40:41]
v_pk_add_f32 v[38:39],v[38:39],v[122:123]
v_pk_fma_f32 v[42:43],v[122:123],v[122:123],v[42:43]
v_smfmac_f32_16x16x64_bf16 v[132:135],a[180:183],a[16:23],v10
s_waitcnt lgkmcnt(4)
s_add_u32 m0,s57,77800
s_add_u32 s89,s56,2162688
buffer_load_dwordx4 v6,s[36:39],s89 offen lds
ds_read_b64 a[16:17],v26 offset:64
ds_read_b64 a[18:19],v26 offset:256
ds_read_b64 a[20:21],v27 offset:64
ds_read_b64 a[22:23],v27 offset:256
v_pk_add_f32 v[36:37],v[36:37],v[124:125]
v_pk_fma_f32 v[40:41],v[124:125],v[124:125],v[40:41]
v_pk_add_f32 v[38:39],v[38:39],v[126:127]
v_pk_fma_f32 v[42:43],v[126:127],v[126:127],v[42:43]
v_smfmac_f32_16x16x64_bf16 v[136:139],a[120:123],a[0:7],v10
s_waitcnt lgkmcnt(4)
ds_read_b64 a[0:1],v26 offset:72
ds_read_b64 a[2:3],v26 offset:264
ds_read_b64 a[4:5],v27 offset:72
ds_read_b64 a[6:7],v27 offset:264
v_pk_add_f32 v[36:37],v[36:37],v[128:129]
v_pk_fma_f32 v[40:41],v[128:129],v[128:129],v[40:41]
v_pk_add_f32 v[38:39],v[38:39],v[130:131]
v_pk_fma_f32 v[42:43],v[130:131],v[130:131],v[42:43]
v_smfmac_f32_16x16x64_bf16 v[140:143],a[124:127],a[8:15],v10
s_waitcnt lgkmcnt(4)
s_add_u32 m0,s57,81336
s_add_u32 s89,s56,2260992
buffer_load_dwordx4 v6,s[36:39],s89 offen lds
ds_read_b64 a[8:9],v26 offset:128
ds_read_b64 a[10:11],v26 offset:320
ds_read_b64 a[12:13],v27 offset:128
ds_read_b64 a[14:15],v27 offset:320
v_pk_add_f32 v[36:37],v[36:37],v[132:133]
v_pk_fma_f32 v[40:41],v[132:133],v[132:133],v[40:41]
v_pk_add_f32 v[38:39],v[38:39],v[134:135]
v_pk_fma_f32 v[42:43],v[134:135],v[134:135],v[42:43]
v_smfmac_f32_16x16x64_bf16 v[144:147],a[152:155],a[16:23],v10
s_waitcnt lgkmcnt(4)
ds_read_b64 a[16:17],v26 offset:136
ds_read_b64 a[18:19],v26 offset:328
ds_read_b64 a[20:21],v27 offset:136
ds_read_b64 a[22:23],v27 offset:328
v_pk_add_f32 v[36:37],v[36:37],v[136:137]
v_pk_fma_f32 v[40:41],v[136:137],v[136:137],v[40:41]
v_pk_add_f32 v[38:39],v[38:39],v[138:139]
v_pk_fma_f32 v[42:43],v[138:139],v[138:139],v[42:43]
v_smfmac_f32_16x16x64_bf16 v[148:151],a[156:159],a[0:7],v10
s_waitcnt lgkmcnt(4)
s_add_u32 m0,s57,84864
s_add_u32 s89,s56,2359296
buffer_load_dwordx4 v6,s[36:39],s89 offen lds
ds_read_b64 a[24:25],v248 offset:57344
ds_read_b64 a[26:27],v248 offset:57352
ds_read_b64 a[36:37],v249 offset:57344
ds_read_b64 a[38:39],v249 offset:57352
v_pk_add_f32 v[36:37],v[36:37],v[140:141]
v_pk_fma_f32 v[40:41],v[140:141],v[140:141],v[40:41]
v_pk_add_f32 v[38:39],v[38:39],v[142:143]
v_pk_fma_f32 v[42:43],v[142:143],v[142:143],v[42:43]
v_smfmac_f32_16x16x64_bf16 v[152:155],a[184:187],a[8:15],v10
s_waitcnt lgkmcnt(4)
ds_read_b64 a[28:29],v248 offset:57408
ds_read_b64 a[30:31],v248 offset:57416
ds_read_b64 a[40:41],v249 offset:57408
ds_read_b64 a[42:43],v249 offset:57416
v_pk_add_f32 v[36:37],v[36:37],v[144:145]
v_pk_fma_f32 v[40:41],v[144:145],v[144:145],v[40:41]
v_pk_add_f32 v[38:39],v[38:39],v[146:147]
v_pk_fma_f32 v[42:43],v[146:147],v[146:147],v[42:43]
v_smfmac_f32_16x16x64_bf16 v[156:159],a[188:191],a[16:23],v10
ds_read_b64 a[32:33],v248 offset:57472
ds_read_b64 a[34:35],v248 offset:57480
ds_read_b64 a[44:45],v249 offset:57472
ds_read_b64 a[46:47],v249 offset:57480
v_pk_add_f32 v[36:37],v[36:37],v[148:149]
v_pk_fma_f32 v[40:41],v[148:149],v[148:149],v[40:41]
v_pk_add_f32 v[38:39],v[38:39],v[150:151]
v_pk_fma_f32 v[42:43],v[150:151],v[150:151],v[42:43]
v_pk_add_f32 v[36:37],v[36:37],v[152:153]
v_pk_fma_f32 v[40:41],v[152:153],v[152:153],v[40:41]
v_pk_add_f32 v[38:39],v[38:39],v[154:155]
v_pk_fma_f32 v[42:43],v[154:155],v[154:155],v[42:43]
v_pk_add_f32 v[36:37],v[36:37],v[156:157]
v_pk_fma_f32 v[40:41],v[156:157],v[156:157],v[40:41]
v_pk_add_f32 v[38:39],v[38:39],v[158:159]
v_pk_fma_f32 v[42:43],v[158:159],v[158:159],v[42:43]
s_nop 0
v_permlane32_swap_b32 v36,v38
v_permlane32_swap_b32 v37,v39
v_permlane32_swap_b32 v40,v42
v_permlane32_swap_b32_e64 v41,v43
v_pk_add_f32 v[36:37],v[36:37],v[38:39]
v_pk_add_f32 v[40:41],v[40:41],v[42:43]
s_add_u32 s60,s60,56576
s_sub_u32 s64,s60,88400
s_min_u32 s60,s60,s64
v_permlane16_swap_b32 v36,v37
v_permlane16_swap_b32 v40,v41
v_add_f32 v8,v36,v37
v_add_f32_e64 v9,v40,v41
v_mul_f32 v8,0x3c2aaaab,v8
v_mul_f32 v9,0x3c2aaaab,v9
v_cmp_ngt_f32 vcc,0x5d800000,v9
v_fma_f32 v9,-v8,v8,v9
v_max_f32 v9,0,v9
s_or_b64 s[90:91],s[90:91],vcc
v_add_f32 v9,0x3727c5ac,v9
v_rsq_f32 v9,v9
s_nop 0
v_mul_f32 v36,s96,v9
v_mul_f32 v37,s96,v9
v_mul_f32 v40,-v8,v36
v_mul_f32 v41,-v8,v36
v_permlane16_swap_b32 v36,v37
s_nop 0
v_permlane16_swap_b32 v40,v41
v_mov_b64 v[38:39],v[36:37]
v_mov_b64 v[42:43],v[40:41]
s_nop 0
v_permlane32_swap_b32 v36,v38
v_permlane32_swap_b32 v37,v39
v_permlane32_swap_b32 v40,v42
v_permlane32_swap_b32 v41,v43
s_waitcnt vmcnt(13)
s_barrier
ds_read_b64_tr_b16 a[192:193],v1 offset:18480
ds_read_b64_tr_b16 a[194:195],v1 offset:19504
ds_read_b64_tr_b16 a[196:197],v1 offset:20528
ds_read_b64_tr_b16 a[198:199],v1 offset:21552
v_pk_fma_f32 v[64:65],v[64:65],v[36:37],v[40:41]
v_pk_fma_f32 v[66:67],v[66:67],v[38:39],v[42:43]
v_pk_fma_f32 v[68:69],v[68:69],v[36:37],v[40:41]
v_pk_fma_f32 v[70:71],v[70:71],v[38:39],v[42:43]
v_pk_fma_f32 v[72:73],v[72:73],v[36:37],v[40:41]
v_pk_fma_f32 v[74:75],v[74:75],v[38:39],v[42:43]
v_pk_fma_f32 v[76:77],v[76:77],v[36:37],v[40:41]
v_pk_fma_f32 v[78:79],v[78:79],v[38:39],v[42:43]
v_pk_fma_f32 v[112:113],v[112:113],v[36:37],v[40:41]
v_pk_fma_f32 v[114:115],v[114:115],v[38:39],v[42:43]
ds_read_b64_tr_b16 a[200:201],v1 offset:22576
ds_read_b64_tr_b16 a[202:203],v1 offset:23600
ds_read_b64_tr_b16 a[204:205],v2 offset:22576
ds_read_b64_tr_b16 a[206:207],v2 offset:23600
v_pk_fma_f32 v[116:117],v[116:117],v[36:37],v[40:41]
v_pk_fma_f32 v[118:119],v[118:119],v[38:39],v[42:43]
v_pk_fma_f32 v[136:137],v[136:137],v[36:37],v[40:41]
v_pk_fma_f32 v[138:139],v[138:139],v[38:39],v[42:43]
v_pk_fma_f32 v[140:141],v[140:141],v[36:37],v[40:41]
v_pk_fma_f32 v[142:143],v[142:143],v[38:39],v[42:43]
v_pk_fma_f32 v[80:81],v[80:81],v[36:37],v[40:41]
v_pk_fma_f32 v[82:83],v[82:83],v[38:39],v[42:43]
v_pk_fma_f32 v[84:85],v[84:85],v[36:37],v[40:41]
v_pk_fma_f32 v[86:87],v[86:87],v[38:39],v[42:43]
ds_read_b64_tr_b16 a[208:209],v2 offset:20528
ds_read_b64_tr_b16 a[210:211],v2 offset:21552
ds_read_b64_tr_b16 a[212:213],v2 offset:18480
ds_read_b64_tr_b16 a[214:215],v2 offset:19504
v_pk_fma_f32 v[88:89],v[88:89],v[36:37],v[40:41]
v_pk_fma_f32 v[90:91],v[90:91],v[38:39],v[42:43]
v_pk_fma_f32 v[92:93],v[92:93],v[36:37],v[40:41]
v_pk_fma_f32 v[94:95],v[94:95],v[38:39],v[42:43]
v_pk_fma_f32 v[120:121],v[120:121],v[36:37],v[40:41]
v_pk_fma_f32 v[122:123],v[122:123],v[38:39],v[42:43]
v_pk_fma_f32 v[124:125],v[124:125],v[36:37],v[40:41]
v_pk_fma_f32 v[126:127],v[126:127],v[38:39],v[42:43]
v_pk_fma_f32 v[144:145],v[144:145],v[36:37],v[40:41]
v_pk_fma_f32 v[146:147],v[146:147],v[38:39],v[42:43]
ds_read_b128 v[16:19],v4 offset:0
ds_read_b128 v[20:23],v4 offset:32
ds_read_b64 v[232:233],v7 offset:18480
ds_read_b64 v[234:235],v7 offset:18992
v_pk_fma_f32 v[148:149],v[148:149],v[36:37],v[40:41]
v_pk_fma_f32 v[150:151],v[150:151],v[38:39],v[42:43]
v_pk_fma_f32 v[96:97],v[96:97],v[36:37],v[40:41]
v_pk_fma_f32 v[98:99],v[98:99],v[38:39],v[42:43]
v_pk_fma_f32 v[100:101],v[100:101],v[36:37],v[40:41]
v_pk_fma_f32 v[102:103],v[102:103],v[38:39],v[42:43]
v_pk_fma_f32 v[104:105],v[104:105],v[36:37],v[40:41]
v_pk_fma_f32 v[106:107],v[106:107],v[38:39],v[42:43]
v_pk_fma_f32 v[108:109],v[108:109],v[36:37],v[40:41]
v_pk_fma_f32 v[110:111],v[110:111],v[38:39],v[42:43]
ds_read_b64 v[236:237],v7 offset:19504
v_pk_fma_f32 v[128:129],v[128:129],v[36:37],v[40:41]
v_pk_fma_f32 v[130:131],v[130:131],v[38:39],v[42:43]
v_pk_fma_f32 v[132:133],v[132:133],v[36:37],v[40:41]
v_pk_fma_f32 v[134:135],v[134:135],v[38:39],v[42:43]
v_pk_fma_f32 v[152:153],v[152:153],v[36:37],v[40:41]
v_pk_fma_f32 v[154:155],v[154:155],v[38:39],v[42:43]
v_pk_fma_f32 v[156:157],v[156:157],v[36:37],v[40:41]
v_pk_fma_f32 v[158:159],v[158:159],v[38:39],v[42:43]
v_cvt_pk_bf16_f32 v160,v64,v68
v_cvt_pk_bf16_f32 v161,v72,v76
v_cvt_pk_bf16_f32 v162,v112,v116
v_cvt_pk_bf16_f32 v163,v136,v140
v_cvt_pk_bf16_f32 v164,v65,v69
v_cvt_pk_bf16_f32 v165,v73,v77
v_cvt_pk_bf16_f32 v166,v113,v117
v_cvt_pk_bf16_f32 v167,v137,v141
v_cvt_pk_bf16_f32 v176,v80,v84
v_cvt_pk_bf16_f32 v177,v88,v92
v_cvt_pk_bf16_f32 v178,v120,v124
v_cvt_pk_bf16_f32 v179,v144,v148
v_cvt_pk_bf16_f32 v180,v81,v85
v_cvt_pk_bf16_f32 v181,v89,v93
v_cvt_pk_bf16_f32 v182,v121,v125
v_cvt_pk_bf16_f32 v183,v145,v149
v_cvt_pk_bf16_f32 v192,v96,v100
v_cvt_pk_bf16_f32 v193,v104,v108
v_cvt_pk_bf16_f32 v194,v128,v132
v_cvt_pk_bf16_f32 v195,v152,v156
v_cvt_pk_bf16_f32 v196,v97,v101
v_cvt_pk_bf16_f32 v197,v105,v109
v_cvt_pk_bf16_f32 v198,v129,v133
v_cvt_pk_bf16_f32 v199,v153,v157
s_waitcnt lgkmcnt(0)
v_or3_b32 v252,v232,v233,0
v_or3_b32 v252,v234,v235,v252
v_or3_b32 v252,v236,v237,v252
v_mfma_f32_16x16x32_bf16 v[44:47],a[192:195],v[160:163],v[16:19]
ds_read_b128 a[48:51],v13 offset:0
v_cvt_pk_bf16_f32 v168,v66,v70
v_cvt_pk_bf16_f32 v169,v74,v78
v_mfma_f32_16x16x32_bf16 v[44:47],a[196:199],v[176:179],v[44:47]
ds_read_b128 v[208:211],v4 offset:3584
v_cvt_pk_bf16_f32 v170,v114,v118
v_cvt_pk_bf16_f32 v171,v138,v142
v_mfma_f32_16x16x32_bf16 v[44:47],a[200:203],v[192:195],v[44:47]
ds_read_b128 a[52:55],v13 offset:256
v_cvt_pk_bf16_f32 v172,v67,v71
v_cvt_pk_bf16_f32 v173,v75,v79
v_mfma_f32_16x16x32_bf16 v[48:51],a[204:207],v[192:195],v[20:23]
ds_read_b128 v[212:215],v4 offset:3616
v_cvt_pk_bf16_f32 v174,v115,v119
v_cvt_pk_bf16_f32 v175,v139,v143
v_mfma_f32_16x16x32_bf16 v[48:51],a[208:211],v[176:179],v[48:51]
ds_read_b128 a[56:59],v13 offset:512
v_cvt_pk_bf16_f32 v184,v82,v86
v_cvt_pk_bf16_f32 v185,v90,v94
v_mfma_f32_16x16x32_bf16 v[48:51],a[212:215],v[160:163],v[48:51]
ds_read_b128 v[216:219],v4 offset:3712
v_cvt_pk_bf16_f32 v186,v122,v126
v_cvt_pk_bf16_f32 v187,v146,v150
v_mfma_f32_16x16x32_bf16 v[52:55],a[212:215],v[164:167],v[20:23]
ds_read_b128 a[60:63],v13 offset:768
v_cvt_pk_bf16_f32 v188,v83,v87
v_cvt_pk_bf16_f32 v189,v91,v95
v_mfma_f32_16x16x32_bf16 v[52:55],a[208:211],v[180:183],v[52:55]
TE01_%=:
s_cbranch_execz TP3281_%=
ds_read_b128 v[220:223],v4 offset:3744
v_cvt_pk_bf16_f32 v190,v123,v127
v_cvt_pk_bf16_f32 v191,v147,v151
v_mfma_f32_16x16x32_bf16 v[52:55],a[204:207],v[196:199],v[52:55]
ds_read_b128 a[64:67],v13 offset:1024
v_cvt_pk_bf16_f32 v200,v98,v102
v_cvt_pk_bf16_f32 v201,v106,v110
v_mfma_f32_16x16x32_bf16 v[56:59],a[200:203],v[196:199],v[16:19]
ds_read_b128 v[224:227],v4 offset:3840
v_cvt_pk_bf16_f32 v202,v130,v134
v_cvt_pk_bf16_f32 v203,v154,v158
v_mfma_f32_16x16x32_bf16 v[56:59],a[196:199],v[180:183],v[56:59]
ds_read_b128 a[68:71],v13 offset:1280
v_cvt_pk_bf16_f32 v204,v99,v103
v_cvt_pk_bf16_f32 v205,v107,v111
v_mfma_f32_16x16x32_bf16 v[56:59],a[192:195],v[164:167],v[56:59]
ds_read_b128 v[228:231],v4 offset:3872
v_cvt_pk_bf16_f32 v206,v131,v135
v_cvt_pk_bf16_f32 v207,v155,v159
v_mfma_f32_16x16x32_bf16 v[60:63],a[192:195],v[168:171],v[16:19]
ds_read_b64 v[14:15],v7 offset:0
ds_read_b64 a[0:1],v250 offset:57344
v_lshlrev_b32_sdwa v44,1,v44 dst_sel:DWORD dst_unused:UNUSED_PAD src0_sel:DWORD src1_sel:WORD_1
v_mfma_f32_16x16x32_bf16 v[60:63],a[196:199],v[184:187],v[60:63]
ds_read_b64 a[2:3],v250 offset:57352
ds_read_b64 a[12:13],v251 offset:57344
v_fma_mixlo_f16 v8,v53,s99,0
v_mfma_f32_16x16x32_bf16 v[60:63],a[200:203],v[200:203],v[60:63]
ds_read_b64 a[14:15],v251 offset:57352
ds_read_b64 a[4:5],v250 offset:57408
v_lshlrev_b32_sdwa v46,1,v46 dst_sel:DWORD dst_unused:UNUSED_PAD src0_sel:DWORD src1_sel:WORD_1
v_mfma_f32_16x16x32_bf16 v[32:35],a[204:207],v[200:203],v[20:23]
ds_read_b64 a[6:7],v250 offset:57416
ds_read_b64 a[16:17],v251 offset:57408
v_fma_mixhi_f16 v8,v55,s99,0
v_mfma_f32_16x16x32_bf16 v[32:35],a[208:211],v[184:187],v[32:35]
ds_read_b64 a[18:19],v251 offset:57416
ds_read_b64 a[8:9],v250 offset:57472
v_lshlrev_b32_sdwa v45,1,v45 dst_sel:DWORD dst_unused:UNUSED_PAD src0_sel:DWORD src1_sel:WORD_1
v_pk_fma_f16 v9,v8,v8,s54
v_mfma_f32_16x16x32_bf16 v[32:35],a[212:215],v[168:171],v[32:35]
s_waitcnt lgkmcnt(9)
ds_read_b64 a[10:11],v250 offset:57480
ds_read_b64 a[20:21],v251 offset:57472
v_or3_b32 v252,v14,v15,v252
v_lshlrev_b32_sdwa v47,1,v47 dst_sel:DWORD dst_unused:UNUSED_PAD src0_sel:DWORD src1_sel:WORD_1
v_mfma_f32_16x16x32_bf16 v[36:39],a[212:215],v[172:175],v[20:23]
ds_read_b64 v[14:15],v7 offset:512
ds_read_b128 v[20:23],v4 offset:128
v_pk_mul_f16 v9,v8,v9
v_mfma_f32_16x16x32_bf16 v[36:39],a[208:211],v[188:191],v[36:39]
ds_read_b64 a[22:23],v251 offset:57480
v_exp_f16_e64 v8,v9
v_lshlrev_b32_sdwa v48,1,v48 dst_sel:DWORD dst_unused:UNUSED_PAD src0_sel:DWORD src1_sel:WORD_1
v_mfma_f32_16x16x32_bf16 v[36:39],a[204:207],v[204:207],v[36:39]
ds_read_b64_tr_b16 a[216:217],v1 offset:0
ds_read_b64_tr_b16 a[218:219],v1 offset:1024
v_mfma_f32_16x16x32_bf16 v[40:43],a[200:203],v[204:207],v[16:19]
ds_read_b128 v[16:19],v4 offset:160
ds_read_b64_tr_b16 a[220:221],v1 offset:2048
v_lshlrev_b32_sdwa v50,1,v50 dst_sel:DWORD dst_unused:UNUSED_PAD src0_sel:DWORD src1_sel:WORD_1
v_mfma_f32_16x16x32_bf16 v[40:43],a[196:199],v[188:191],v[40:43]
ds_read_b64_tr_b16 a[222:223],v1 offset:3072
ds_read_u16 v44,v44
v_lshlrev_b32_sdwa v49,1,v49 dst_sel:DWORD dst_unused:UNUSED_PAD src0_sel:DWORD src1_sel:WORD_1
v_mfma_f32_16x16x32_bf16 v[40:43],a[192:195],v[172:175],v[40:43]
s_waitcnt lgkmcnt(8)
ds_read_u16_d16_hi v46,v46
ds_read_u16 v45,v45
v_or3_b32 v252,v14,v15,v252
v_mfma_f32_16x16x32_bf16 v[136:139],a[48:51],a[24:27],v[208:211]
ds_read_b64 v[14:15],v7 offset:1024
ds_read_u16_d16_hi v47,v47
v_exp_f16_sdwa v8,v9 dst_sel:WORD_1 dst_unused:UNUSED_PRESERVE src0_sel:WORD_1
v_lshlrev_b32_sdwa v51,1,v51 dst_sel:DWORD dst_unused:UNUSED_PAD src0_sel:DWORD src1_sel:WORD_1
v_mfma_f32_16x16x32_bf16 v[140:143],a[48:51],a[36:39],v[208:211]
ds_read_b64_tr_b16 a[72:73],v1 offset:4096
ds_read_b64_tr_b16 a[74:75],v1 offset:5120
v_lshlrev_b32_sdwa v56,1,v56 dst_sel:DWORD dst_unused:UNUSED_PAD src0_sel:DWORD src1_sel:WORD_1
v_mfma_f32_16x16x32_bf16 v[68:71],a[52:55],a[36:39],v[212:215]
ds_read_u16 v48,v48
ds_read_u16_d16_hi v50,v50
v_pk_fma_f16 v8,v8,s55,s55
v_mfma_f32_16x16x32_bf16 v[64:67],a[52:55],a[24:27],v[212:215]
ds_read_u16 v49,v49
v_rcp_f16 v9,v8
v_lshlrev_b32_sdwa v58,1,v58 dst_sel:DWORD dst_unused:UNUSED_PAD src0_sel:DWORD src1_sel:WORD_1
v_mfma_f32_16x16x32_bf16 v[144:147],a[56:59],a[28:31],v[216:219]
ds_read_u16_d16_hi v51,v51
ds_read_b64_tr_b16 a[76:77],v2 offset:4096
v_mfma_f32_16x16x32_bf16 v[148:151],a[56:59],a[40:43],v[216:219]
s_waitcnt lgkmcnt(8)
ds_read_b64_tr_b16 a[78:79],v2 offset:5120
ds_read_b64_tr_b16 a[80:81],v2 offset:2048
v_or3_b32 v252,v14,v15,v252
v_lshlrev_b32_sdwa v57,1,v57 dst_sel:DWORD dst_unused:UNUSED_PAD src0_sel:DWORD src1_sel:WORD_1
v_mfma_f32_16x16x32_bf16 v[84:87],a[60:63],a[40:43],v[220:223]
ds_read_b64_tr_b16 a[82:83],v2 offset:3072
ds_read_u16 v56,v56
v_lshlrev_b32_sdwa v59,1,v59 dst_sel:DWORD dst_unused:UNUSED_PAD src0_sel:DWORD src1_sel:WORD_1
v_mfma_f32_16x16x32_bf16 v[80:83],a[60:63],a[28:31],v[220:223]
ds_read_u16_d16_hi v58,v58
v_rcp_f16_sdwa v9,v8 dst_sel:WORD_1 dst_unused:UNUSED_PRESERVE src0_sel:WORD_1
v_mfma_f32_16x16x32_bf16 v[152:155],a[64:67],a[32:35],v[224:227]
ds_read_u16 v57,v57
ds_read_u16_d16_hi v59,v59
v_lshlrev_b32_sdwa v52,1,v52 dst_sel:DWORD dst_unused:UNUSED_PAD src0_sel:DWORD src1_sel:WORD_1
v_mfma_f32_16x16x32_bf16 v[156:159],a[64:67],a[44:47],v[224:227]
ds_read_b64_tr_b16 a[84:85],v2 offset:0
ds_read_b64_tr_b16 a[86:87],v2 offset:1024
v_lshlrev_b32_sdwa v54,1,v54 dst_sel:DWORD dst_unused:UNUSED_PAD src0_sel:DWORD src1_sel:WORD_1
v_mfma_f32_16x16x32_bf16 v[100:103],a[68:71],a[44:47],v[228:231]
s_waitcnt vmcnt(10)
ds_read_u16 v52,v52
ds_read_u16_d16_hi v54,v54
v_fma_mix_f32 v53,v53,v9,0 op_sel_hi:[0,1,0]
v_mfma_f32_16x16x32_bf16 v[96:99],a[68:71],a[32:35],v[228:231]
s_barrier
ds_read_b64_tr_b16 a[224:225],v3 offset:0
ds_read_b64_tr_b16 a[226:227],v3 offset:1536
v_fma_mix_f32 v55,v55,v9,0 op_sel:[0,1,0] op_sel_hi:[0,1,0]
v_mfma_f32_16x16x32_bf16 v[24:27],a[216:219],v[160:163],v[20:23]
s_add_u32 m0,s57,0
ds_read_b64_tr_b16 a[228:229],v3 offset:8
ds_read_b64_tr_b16 a[230:231],v3 offset:1544
v_lshlrev_b32_sdwa v60,1,v60 dst_sel:DWORD dst_unused:UNUSED_PAD src0_sel:DWORD src1_sel:WORD_1
v_mfma_f32_16x16x32_bf16 v[24:27],a[220:223],v[176:179],v[24:27]
s_waitcnt lgkmcnt(15)
ds_read_b64_tr_b16 a[232:233],v3 offset:64
ds_read_b64_tr_b16 a[234:235],v3 offset:1600
v_lshl_add_u64 v[44:45],v[44:45],0,v[46:47]
v_mfma_f32_16x16x32_bf16 v[24:27],a[72:75],v[192:195],v[24:27]
s_waitcnt lgkmcnt(15)
ds_read_b64_tr_b16 a[236:237],v3 offset:72
ds_read_b64_tr_b16 a[238:239],v3 offset:1608
v_lshl_add_u64 v[46:47],v[48:49],0,v[50:51]
v_mfma_f32_16x16x32_bf16 v[28:31],a[76:79],v[192:195],v[16:19]
s_waitcnt lgkmcnt(15)
ds_read_b64_tr_b16 a[240:241],v3 offset:128
ds_read_b64_tr_b16 a[242:243],v3 offset:1664
v_lshlrev_b32_sdwa v62,1,v62 dst_sel:DWORD dst_unused:UNUSED_PAD src0_sel:DWORD src1_sel:WORD_1
v_mfma_f32_16x16x32_bf16 v[28:31],a[80:83],v[176:179],v[28:31]
s_waitcnt lgkmcnt(12)
ds_read_b64_tr_b16 a[244:245],v3 offset:136
v_lshl_add_u64 v[48:49],v[56:57],0,v[58:59]
v_mfma_f32_16x16x32_bf16 v[28:31],a[84:87],v[160:163],v[28:31]
s_add_u32 s89,s56,2457600
ds_read_b64_tr_b16 a[246:247],v3 offset:1672
v_lshlrev_b32_sdwa v61,1,v61 dst_sel:DWORD dst_unused:UNUSED_PAD src0_sel:DWORD src1_sel:WORD_1
v_mfma_f32_16x16x32_bf16 v[56:59],a[84:87],v[164:167],v[16:19]
s_waitcnt lgkmcnt(12)
buffer_load_dwordx4 v6,s[36:39],s89 offen lds
v_or_b32_e64 v50,v52,v54
v_mfma_f32_16x16x32_bf16 v[56:59],a[80:83],v[180:183],v[56:59]
s_add_u32 m0,s58,18480
ds_read_u16 v60,v60
v_cvt_pk_bf16_f32 v51,v53,v55
v_lshlrev_b32_sdwa v63,1,v63 dst_sel:DWORD dst_unused:UNUSED_PAD src0_sel:DWORD src1_sel:WORD_1
v_mfma_f32_16x16x32_bf16 v[56:59],a[76:79],v[196:199],v[56:59]
s_add_u32 s69,s69,s59
ds_read_u16_d16_hi v62,v62
v_lshlrev_b32_sdwa v32,1,v32 dst_sel:DWORD dst_unused:UNUSED_PAD src0_sel:DWORD src1_sel:WORD_1
v_mfma_f32_16x16x32_bf16 v[52:55],a[72:75],v[196:199],v[20:23]
buffer_load_dwordx4 v5,s[44:47],s69 offen offset:0 lds
v_lshlrev_b32_sdwa v34,1,v34 dst_sel:DWORD dst_unused:UNUSED_PAD src0_sel:DWORD src1_sel:WORD_1
v_mfma_f32_16x16x32_bf16 v[52:55],a[220:223],v[180:183],v[52:55]
s_add_u32 s69,s69,s53
ds_read_u16 v61,v61
v_lshlrev_b32_sdwa v33,1,v33 dst_sel:DWORD dst_unused:UNUSED_PAD src0_sel:DWORD src1_sel:WORD_1
v_mfma_f32_16x16x32_bf16 v[52:55],a[216:219],v[164:167],v[52:55]
s_waitcnt lgkmcnt(7)
buffer_load_dwordx4 v5,s[44:47],s69 offen offset:1024 lds
ds_read_u16_d16_hi v63,v63
v_mfma_f32_16x16x32_bf16 v[136:139],a[224:227],v[44:47],v[136:139]
s_add_u32 s69,s69,s53
ds_read_u16 v32,v32
ds_read_u16_d16_hi v34,v34
v_mfma_f32_16x16x32_bf16 v[140:143],a[224:227],v[48:51],v[140:143]
buffer_load_dwordx4 v5,s[44:47],s69 offen offset:2048 lds
v_lshlrev_b32_sdwa v35,1,v35 dst_sel:DWORD dst_unused:UNUSED_PAD src0_sel:DWORD src1_sel:WORD_1
v_mfma_f32_16x16x32_bf16 v[68:71],a[228:231],v[48:51],v[68:71]
ds_read_u16 v33,v33
ds_read_u16_d16_hi v35,v35
v_mfma_f32_16x16x32_bf16 v[64:67],a[228:231],v[44:47],v[64:67]
ds_read_b64_tr_b16 a[248:249],v3 offset:6160
v_lshlrev_b32_sdwa v40,1,v40 dst_sel:DWORD dst_unused:UNUSED_PAD src0_sel:DWORD src1_sel:WORD_1
v_mfma_f32_16x16x32_bf16 v[144:147],a[232:235],v[44:47],v[144:147]
s_add_u32 m0,s57,3544
ds_read_u16 v40,v40
ds_read_b64_tr_b16 a[250:251],v3 offset:7696
v_mfma_f32_16x16x32_bf16 v[148:151],a[232:235],v[48:51],v[148:151]
s_add_u32 s89,s56,2555904
ds_read_b64_tr_b16 a[252:253],v3 offset:6168
v_lshlrev_b32_sdwa v42,1,v42 dst_sel:DWORD dst_unused:UNUSED_PAD src0_sel:DWORD src1_sel:WORD_1
v_mfma_f32_16x16x32_bf16 v[84:87],a[236:239],v[48:51],v[84:87]
buffer_load_dwordx4 v6,s[36:39],s89 offen lds
ds_read_u16_d16_hi v42,v42
v_mfma_f32_16x16x32_bf16 v[80:83],a[236:239],v[44:47],v[80:83]
s_waitcnt lgkmcnt(10)
ds_read_b64_tr_b16 a[254:255],v3 offset:7704
v_lshlrev_b32_sdwa v41,1,v41 dst_sel:DWORD dst_unused:UNUSED_PAD src0_sel:DWORD src1_sel:WORD_1
v_mfma_f32_16x16x32_bf16 v[152:155],a[240:243],v[44:47],v[152:155]
ds_read_u16 v41,v41
v_lshlrev_b32_sdwa v43,1,v43 dst_sel:DWORD dst_unused:UNUSED_PAD src0_sel:DWORD src1_sel:WORD_1
v_mfma_f32_16x16x32_bf16 v[156:159],a[240:243],v[48:51],v[156:159]
ds_read_u16_d16_hi v43,v43
v_lshlrev_b32_sdwa v36,1,v36 dst_sel:DWORD dst_unused:UNUSED_PAD src0_sel:DWORD src1_sel:WORD_1
v_mfma_f32_16x16x32_bf16 v[100:103],a[244:247],v[48:51],v[100:103]
ds_read_u16 v36,v36
v_lshlrev_b32_sdwa v38,1,v38 dst_sel:DWORD dst_unused:UNUSED_PAD src0_sel:DWORD src1_sel:WORD_1
v_mfma_f32_16x16x32_bf16 v[96:99],a[244:247],v[44:47],v[96:99]
ds_read_u16_d16_hi v38,v38
v_lshlrev_b32_sdwa v37,1,v37 dst_sel:DWORD dst_unused:UNUSED_PAD src0_sel:DWORD src1_sel:WORD_1
v_mfma_f32_16x16x32_bf16 v[112:115],a[48:51],a[0:3],v[208:211]
ds_read_u16 v37,v37
v_lshlrev_b32_sdwa v39,1,v39 dst_sel:DWORD dst_unused:UNUSED_PAD src0_sel:DWORD src1_sel:WORD_1
v_mfma_f32_16x16x32_bf16 v[116:119],a[48:51],a[12:15],v[208:211]
ds_read_u16_d16_hi v39,v39
ds_read_b64_tr_b16 a[92:93],v3 offset:6224
v_mfma_f32_16x16x32_bf16 v[76:79],a[52:55],a[12:15],v[212:215]
ds_read_b64_tr_b16 a[94:95],v3 offset:7760
v_mfma_f32_16x16x32_bf16 v[72:75],a[52:55],a[0:3],v[212:215]
ds_read_b64_tr_b16 a[48:49],v3 offset:6232
v_mfma_f32_16x16x32_bf16 v[120:123],a[56:59],a[4:7],v[216:219]
ds_read_b64_tr_b16 a[50:51],v3 offset:7768
v_mfma_f32_16x16x32_bf16 v[124:127],a[56:59],a[16:19],v[216:219]
ds_read_b64_tr_b16 a[56:57],v3 offset:6288
v_mfma_f32_16x16x32_bf16 v[92:95],a[60:63],a[16:19],v[220:223]
ds_read_b64_tr_b16 a[58:59],v3 offset:7824
v_mfma_f32_16x16x32_bf16 v[88:91],a[60:63],a[4:7],v[220:223]
v_mfma_f32_16x16x32_bf16 v[128:131],a[64:67],a[8:11],v[224:227]
v_mfma_f32_16x16x32_bf16 v[132:135],a[64:67],a[20:23],v[224:227]
ds_read_b64_tr_b16 a[64:65],v3 offset:6296
v_mfma_f32_16x16x32_bf16 v[108:111],a[68:71],a[20:23],v[228:231]
ds_read_b64_tr_b16 a[66:67],v3 offset:7832
v_mfma_f32_16x16x32_bf16 v[104:107],a[68:71],a[8:11],v[228:231]
s_add_u32 m0,s57,7080
v_mfma_f32_16x16x32_bf16 v[48:51],a[216:219],v[168:171],v[20:23]
s_add_u32 s89,s56,2654208
ds_read_b64 v[14:15],v7 offset:6160
ds_read_b64_tr_b16 a[212:213],v1 offset:6160
v_lshlrev_b32_sdwa v24,1,v24 dst_sel:DWORD dst_unused:UNUSED_PAD src0_sel:DWORD src1_sel:WORD_1
v_mfma_f32_16x16x32_bf16 v[48:51],a[220:223],v[184:187],v[48:51]
s_waitcnt lgkmcnt(6)
buffer_load_dwordx4 v6,s[36:39],s89 offen lds
ds_read_b64_tr_b16 a[214:215],v1 offset:7184
v_lshl_add_u64 v[60:61],v[60:61],0,v[62:63]
v_fma_mixlo_f16 v8,v57,s99,0
v_mfma_f32_16x16x32_bf16 v[48:51],a[72:75],v[200:203],v[48:51]
ds_read_b64_tr_b16 a[208:209],v1 offset:8208
ds_read_b64_tr_b16 a[210:211],v1 offset:9232
v_lshl_add_u64 v[62:63],v[32:33],0,v[34:35]
v_lshlrev_b32_sdwa v26,1,v26 dst_sel:DWORD dst_unused:UNUSED_PAD src0_sel:DWORD src1_sel:WORD_1
v_mfma_f32_16x16x32_bf16 v[44:47],a[76:79],v[200:203],v[16:19]
ds_read_u16 v24,v24
ds_read_u16_d16_hi v26,v26
v_fma_mixhi_f16 v8,v59,s99,0
v_mfma_f32_16x16x32_bf16 v[44:47],a[80:83],v[184:187],v[44:47]
ds_read_b64_tr_b16 a[204:205],v1 offset:10256
ds_read_b64_tr_b16 a[206:207],v1 offset:11280
v_lshl_add_u64 v[32:33],v[40:41],0,v[42:43]
v_lshlrev_b32_sdwa v25,1,v25 dst_sel:DWORD dst_unused:UNUSED_PAD src0_sel:DWORD src1_sel:WORD_1
v_pk_fma_f16 v9,v8,v8,s54
v_mfma_f32_16x16x32_bf16 v[44:47],a[84:87],v[168:171],v[44:47]
s_waitcnt lgkmcnt(8)
ds_read_u16 v25,v25
ds_read_b64_tr_b16 a[200:201],v2 offset:10256
v_or3_b32 v252,v14,v15,v252
v_lshlrev_b32_sdwa v27,1,v27 dst_sel:DWORD dst_unused:UNUSED_PAD src0_sel:DWORD src1_sel:WORD_1
v_mfma_f32_16x16x32_bf16 v[40:43],a[84:87],v[172:175],v[16:19]
s_nop 0
ds_read_b64 v[14:15],v7 offset:6672
ds_read_b128 v[16:19],v4 offset:256
v_pk_mul_f16 v9,v8,v9
v_mfma_f32_16x16x32_bf16 v[40:43],a[80:83],v[188:191],v[40:43]
ds_read_u16_d16_hi v27,v27
ds_read_b64_tr_b16 a[202:203],v2 offset:11280
v_lshl_add_u64 v[34:35],v[36:37],0,v[38:39]
v_lshlrev_b32_sdwa v28,1,v28 dst_sel:DWORD dst_unused:UNUSED_PAD src0_sel:DWORD src1_sel:WORD_1
v_mfma_f32_16x16x32_bf16 v[40:43],a[76:79],v[204:207],v[40:43]
ds_read_u16 v28,v28
v_exp_f16 v8,v9
v_mfma_f32_16x16x32_bf16 v[36:39],a[72:75],v[204:207],v[20:23]
ds_read_b128 v[20:23],v4 offset:288
ds_read_b64_tr_b16 a[196:197],v2 offset:8208
v_lshlrev_b32_sdwa v30,1,v30 dst_sel:DWORD dst_unused:UNUSED_PAD src0_sel:DWORD src1_sel:WORD_1
v_mfma_f32_16x16x32_bf16 v[36:39],a[220:223],v[188:191],v[36:39]
ds_read_u16_d16_hi v30,v30
v_lshlrev_b32_sdwa v29,1,v29 dst_sel:DWORD dst_unused:UNUSED_PAD src0_sel:DWORD src1_sel:WORD_1
v_mfma_f32_16x16x32_bf16 v[36:39],a[216:219],v[172:175],v[36:39]
s_waitcnt lgkmcnt(7)
ds_read_u16 v29,v29
v_or3_b32 v252,v14,v15,v252
v_mfma_f32_16x16x32_bf16 v[112:115],a[224:227],v[60:63],v[112:115]
s_add_u32 m0,s57,10608
ds_read_b64 v[14:15],v7 offset:7184
v_exp_f16_sdwa v8,v9 dst_sel:WORD_1 dst_unused:UNUSED_PRESERVE src0_sel:WORD_1
v_lshlrev_b32_sdwa v31,1,v31 dst_sel:DWORD dst_unused:UNUSED_PAD src0_sel:DWORD src1_sel:WORD_1
v_mfma_f32_16x16x32_bf16 v[116:119],a[224:227],v[32:35],v[116:119]
s_add_u32 s89,s56,2752512
ds_read_u16_d16_hi v31,v31
v_lshlrev_b32_sdwa v52,1,v52 dst_sel:DWORD dst_unused:UNUSED_PAD src0_sel:DWORD src1_sel:WORD_1
v_mfma_f32_16x16x32_bf16 v[76:79],a[228:231],v[32:35],v[76:79]
buffer_load_dwordx4 v6,s[36:39],s89 offen lds
ds_read_b64_tr_b16 a[198:199],v2 offset:9232
v_pk_fma_f16 v8,v8,s55,s55
v_mfma_f32_16x16x32_bf16 v[72:75],a[228:231],v[60:63],v[72:75]
v_rcp_f16 v9,v8
v_lshlrev_b32_sdwa v54,1,v54 dst_sel:DWORD dst_unused:UNUSED_PAD src0_sel:DWORD src1_sel:WORD_1
v_mfma_f32_16x16x32_bf16 v[120:123],a[232:235],v[60:63],v[120:123]
ds_read_u16 v52,v52
v_lshlrev_b32_sdwa v53,1,v53 dst_sel:DWORD dst_unused:UNUSED_PAD src0_sel:DWORD src1_sel:WORD_1
v_mfma_f32_16x16x32_bf16 v[124:127],a[232:235],v[32:35],v[124:127]
s_waitcnt lgkmcnt(3)
ds_read_u16_d16_hi v54,v54
v_or3_b32 v252,v14,v15,v252
v_mfma_f32_16x16x32_bf16 v[92:95],a[236:239],v[32:35],v[92:95]
ds_read_u16 v53,v53
v_lshlrev_b32_sdwa v55,1,v55 dst_sel:DWORD dst_unused:UNUSED_PAD src0_sel:DWORD src1_sel:WORD_1
v_mfma_f32_16x16x32_bf16 v[88:91],a[236:239],v[60:63],v[88:91]
v_rcp_f16_sdwa v9,v8 dst_sel:WORD_1 dst_unused:UNUSED_PRESERVE src0_sel:WORD_1
v_mfma_f32_16x16x32_bf16 v[128:131],a[240:243],v[60:63],v[128:131]
ds_read_u16_d16_hi v55,v55
v_lshlrev_b32_sdwa v56,1,v56 dst_sel:DWORD dst_unused:UNUSED_PAD src0_sel:DWORD src1_sel:WORD_1
v_mfma_f32_16x16x32_bf16 v[132:135],a[240:243],v[32:35],v[132:135]
ds_read_b64_tr_b16 a[192:193],v2 offset:6160
ds_read_b64_tr_b16 a[194:195],v2 offset:7184
v_lshlrev_b32_sdwa v58,1,v58 dst_sel:DWORD dst_unused:UNUSED_PAD src0_sel:DWORD src1_sel:WORD_1
v_mfma_f32_16x16x32_bf16 v[108:111],a[244:247],v[32:35],v[108:111]
ds_read_u16 v56,v56
v_fma_mix_f32 v57,v57,v9,0 op_sel_hi:[0,1,0]
v_mfma_f32_16x16x32_bf16 v[104:107],a[244:247],v[60:63],v[104:107]
ds_read_u16_d16_hi v58,v58
v_fma_mix_f32 v59,v59,v9,0 op_sel:[0,1,0] op_sel_hi:[0,1,0]
v_mfma_f32_16x16x32_bf16 v[32:35],a[212:215],v[160:163],v[16:19]
v_lshlrev_b32_sdwa v48,1,v48 dst_sel:DWORD dst_unused:UNUSED_PAD src0_sel:DWORD src1_sel:WORD_1
v_mfma_f32_32x32x16_bf16 v[208:223],a[88:91],a[88:91],0
v_lshlrev_b32_sdwa v50,1,v50 dst_sel:DWORD dst_unused:UNUSED_PAD src0_sel:DWORD src1_sel:WORD_1
v_lshlrev_b32_sdwa v49,1,v49 dst_sel:DWORD dst_unused:UNUSED_PAD src0_sel:DWORD src1_sel:WORD_1
v_lshlrev_b32_sdwa v51,1,v51 dst_sel:DWORD dst_unused:UNUSED_PAD src0_sel:DWORD src1_sel:WORD_1
v_lshlrev_b32_sdwa v44,1,v44 dst_sel:DWORD dst_unused:UNUSED_PAD src0_sel:DWORD src1_sel:WORD_1
v_lshlrev_b32_sdwa v46,1,v46 dst_sel:DWORD dst_unused:UNUSED_PAD src0_sel:DWORD src1_sel:WORD_1
v_mfma_f32_16x16x32_bf16 v[32:35],a[208:211],v[176:179],v[32:35]
s_add_u32 m0,s57,14144
ds_read_u16 v48,v48
ds_read_u16_d16_hi v50,v50
v_lshl_add_u64 v[24:25],v[24:25],0,v[26:27]
v_mfma_f32_16x16x32_bf16 v[32:35],a[204:207],v[192:195],v[32:35]
s_waitcnt lgkmcnt(8)
ds_read_u16 v49,v49
v_lshl_add_u64 v[26:27],v[28:29],0,v[30:31]
v_mfma_f32_16x16x32_bf16 v[60:63],a[200:203],v[192:195],v[20:23]
s_add_u32 s89,s56,2850816
ds_read_u16_d16_hi v51,v51
v_mfma_f32_32x32x16_bf16 v[224:239],a[88:91],a[88:91],0
v_accvgpr_write_b32 a0,v160
v_accvgpr_write_b32 a1,v161
v_accvgpr_write_b32 a2,v162
v_accvgpr_write_b32 a3,v163
v_lshlrev_b32_sdwa v45,1,v45 dst_sel:DWORD dst_unused:UNUSED_PAD src0_sel:DWORD src1_sel:WORD_1
v_mfma_f32_16x16x32_bf16 v[60:63],a[196:199],v[176:179],v[60:63]
s_waitcnt lgkmcnt(6)
buffer_load_dwordx4 v6,s[36:39],s89 offen lds
v_lshl_add_u64 v[28:29],v[52:53],0,v[54:55]
v_mfma_f32_16x16x32_bf16 v[60:63],a[192:195],a[0:3],v[60:63]
ds_read_u16 v44,v44
v_lshlrev_b32_sdwa v47,1,v47 dst_sel:DWORD dst_unused:UNUSED_PAD src0_sel:DWORD src1_sel:WORD_1
v_mfma_f32_16x16x32_bf16 v[52:55],a[192:195],v[164:167],v[20:23]
s_waitcnt lgkmcnt(5)
ds_read_u16_d16_hi v46,v46
v_or_b32_e64 v30,v56,v58
v_mfma_f32_16x16x32_bf16 v[52:55],a[196:199],v[180:183],v[52:55]
s_waitcnt vmcnt(15)
ds_read_u16 v45,v45
v_cvt_pk_bf16_f32 v31,v57,v59
v_mfma_f32_16x16x32_bf16 v[52:55],a[200:203],v[196:199],v[52:55]
s_barrier
ds_read_u16_d16_hi v47,v47
v_accvgpr_write_b32 a4,v164
v_mfma_f32_16x16x32_bf16 v[56:59],a[204:207],v[196:199],v[16:19]
ds_read_b64_tr_b16 a[224:225],v3 offset:12320
v_lshlrev_b32_sdwa v36,1,v36 dst_sel:DWORD dst_unused:UNUSED_PAD src0_sel:DWORD src1_sel:WORD_1
v_mfma_f32_16x16x32_bf16 v[56:59],a[208:211],v[180:183],v[56:59]
ds_read_u16 v36,v36
v_accvgpr_write_b32 a5,v165
v_mfma_f32_16x16x32_bf16 v[56:59],a[212:215],v[164:167],v[56:59]
s_add_u32 m0,s58,0
ds_read_b64_tr_b16 a[226:227],v3 offset:13856
v_lshlrev_b32_sdwa v38,1,v38 dst_sel:DWORD dst_unused:UNUSED_PAD src0_sel:DWORD src1_sel:WORD_1
v_mfma_f32_16x16x32_bf16 v[136:139],a[248:251],v[24:27],v[136:139]
s_add_u32 s69,s69,s59
ds_read_u16_d16_hi v38,v38
v_accvgpr_write_b32 a6,v166
v_mfma_f32_16x16x32_bf16 v[140:143],a[248:251],v[28:31],v[140:143]
buffer_load_dwordx4 v5,s[44:47],s69 offen offset:0 lds
v_lshlrev_b32_sdwa v37,1,v37 dst_sel:DWORD dst_unused:UNUSED_PAD src0_sel:DWORD src1_sel:WORD_1
v_mfma_f32_16x16x32_bf16 v[68:71],a[252:255],v[28:31],v[68:71]
ds_read_u16 v37,v37
v_lshlrev_b32_sdwa v39,1,v39 dst_sel:DWORD dst_unused:UNUSED_PAD src0_sel:DWORD src1_sel:WORD_1
v_mfma_f32_16x16x32_bf16 v[64:67],a[252:255],v[24:27],v[64:67]
ds_read_u16_d16_hi v39,v39
v_accvgpr_write_b32 a7,v167
v_mfma_f32_16x16x32_bf16 v[144:147],a[92:95],v[24:27],v[144:147]
s_add_u32 s69,s69,s53
ds_read_b64_tr_b16 a[228:229],v3 offset:12328
v_lshlrev_b32_sdwa v40,1,v40 dst_sel:DWORD dst_unused:UNUSED_PAD src0_sel:DWORD src1_sel:WORD_1
v_mfma_f32_16x16x32_bf16 v[148:151],a[92:95],v[28:31],v[148:151]
buffer_load_dwordx4 v5,s[44:47],s69 offen offset:1024 lds
ds_read_u16 v40,v40
v_mfma_f32_16x16x32_bf16 v[84:87],a[48:51],v[28:31],v[84:87]
s_add_u32 m0,s57,17688
ds_read_b64_tr_b16 a[230:231],v3 offset:13864
ds_read_b64_tr_b16 a[232:233],v3 offset:12384
v_accvgpr_write_b32 a8,v168
v_mfma_f32_16x16x32_bf16 v[80:83],a[48:51],v[24:27],v[80:83]
s_add_u32 s89,s56,2949120
ds_read_b64_tr_b16 a[234:235],v3 offset:13920
v_lshlrev_b32_sdwa v42,1,v42 dst_sel:DWORD dst_unused:UNUSED_PAD src0_sel:DWORD src1_sel:WORD_1
v_mfma_f32_16x16x32_bf16 v[152:155],a[56:59],v[24:27],v[152:155]
buffer_load_dwordx4 v6,s[36:39],s89 offen lds
ds_read_u16_d16_hi v42,v42
v_lshlrev_b32_sdwa v41,1,v41 dst_sel:DWORD dst_unused:UNUSED_PAD src0_sel:DWORD src1_sel:WORD_1
v_accvgpr_write_b32 a9,v169
v_mfma_f32_16x16x32_bf16 v[156:159],a[56:59],v[28:31],v[156:159]
s_add_u32 m0,s58,0
ds_read_u16 v41,v41
ds_read_b64_tr_b16 a[236:237],v3 offset:12392
v_lshlrev_b32_sdwa v43,1,v43 dst_sel:DWORD dst_unused:UNUSED_PAD src0_sel:DWORD src1_sel:WORD_1
v_mfma_f32_16x16x32_bf16 v[100:103],a[64:67],v[28:31],v[100:103]
s_add_u32 s69,s69,s53
ds_read_u16_d16_hi v43,v43
ds_read_b64_tr_b16 a[238:239],v3 offset:13928
v_accvgpr_write_b32 a10,v170
v_mfma_f32_16x16x32_bf16 v[96:99],a[64:67],v[24:27],v[96:99]
buffer_load_dwordx4 v5,s[44:47],s69 offen offset:2048 lds
ds_read_b64_tr_b16 a[240:241],v3 offset:12448
v_accvgpr_write_b32 a11,v171
s_nop 1
v_mfma_f32_16x16x32_bf16 v[28:31],a[212:215],a[8:11],v[16:19]
ds_read_b64 v[14:15],v7 offset:12320
ds_read_b64_tr_b16 a[52:53],v1 offset:12320
v_lshlrev_b32_sdwa v32,1,v32 dst_sel:DWORD dst_unused:UNUSED_PAD src0_sel:DWORD src1_sel:WORD_1
v_mfma_f32_16x16x32_bf16 v[28:31],a[208:211],v[184:187],v[28:31]
s_waitcnt lgkmcnt(12)
ds_read_b64_tr_b16 a[54:55],v1 offset:13344
ds_read_b64_tr_b16 a[60:61],v1 offset:14368
v_lshl_add_u64 v[48:49],v[48:49],0,v[50:51]
v_fma_mixlo_f16 v8,v53,s99,0
v_mfma_f32_16x16x32_bf16 v[28:31],a[204:207],v[200:203],v[28:31]
s_add_u32 m0,s58,6160
ds_read_b64_tr_b16 a[62:63],v1 offset:15392
ds_read_u16 v32,v32
v_lshl_add_u64 v[50:51],v[44:45],0,v[46:47]
v_lshlrev_b32_sdwa v34,1,v34 dst_sel:DWORD dst_unused:UNUSED_PAD src0_sel:DWORD src1_sel:WORD_1
v_mfma_f32_16x16x32_bf16 v[24:27],a[200:203],v[200:203],v[20:23]
s_add_u32 s69,s69,s59
ds_read_u16_d16_hi v34,v34
ds_read_b64_tr_b16 a[68:69],v1 offset:16416
v_fma_mixhi_f16 v8,v55,s99,0
v_mfma_f32_16x16x32_bf16 v[24:27],a[196:199],v[184:187],v[24:27]
buffer_load_dwordx4 v5,s[44:47],s69 offen offset:0 lds
ds_read_b64_tr_b16 a[70:71],v1 offset:17440
v_lshl_add_u64 v[44:45],v[36:37],0,v[38:39]
v_lshlrev_b32_sdwa v33,1,v33 dst_sel:DWORD dst_unused:UNUSED_PAD src0_sel:DWORD src1_sel:WORD_1
v_pk_fma_f16 v9,v8,v8,s54
v_mfma_f32_16x16x32_bf16 v[24:27],a[192:195],a[8:11],v[24:27]
s_waitcnt lgkmcnt(8)
ds_read_u16 v33,v33
ds_read_b64_tr_b16 a[84:85],v2 offset:16416
v_or3_b32 v252,v14,v15,v252
v_lshlrev_b32_sdwa v35,1,v35 dst_sel:DWORD dst_unused:UNUSED_PAD src0_sel:DWORD src1_sel:WORD_1
v_accvgpr_write_b32 a12,v172
v_mfma_f32_16x16x32_bf16 v[36:39],a[192:195],v[172:175],v[20:23]
s_add_u32 s69,s69,s53
ds_read_b64 v[14:15],v7 offset:12832
ds_read_b128 v[20:23],v4 offset:384
v_pk_mul_f16 v9,v8,v9
v_mfma_f32_16x16x32_bf16 v[36:39],a[196:199],v[188:191],v[36:39]
buffer_load_dwordx4 v5,s[44:47],s69 offen offset:1024 lds
ds_read_u16_d16_hi v35,v35
v_lshl_add_u64 v[46:47],v[40:41],0,v[42:43]
v_lshlrev_b32_sdwa v60,1,v60 dst_sel:DWORD dst_unused:UNUSED_PAD src0_sel:DWORD src1_sel:WORD_1
v_mfma_f32_16x16x32_bf16 v[36:39],a[200:203],v[204:207],v[36:39]
s_add_u32 m0,s57,21224
ds_read_u16 v60,v60
v_exp_f16_e64 v8,v9
v_mfma_f32_16x16x32_bf16 v[40:43],a[204:207],v[204:207],v[16:19]
s_add_u32 s89,s56,3047424
ds_read_b128 v[16:19],v4 offset:416
ds_read_b64_tr_b16 a[86:87],v2 offset:17440
v_lshlrev_b32_sdwa v62,1,v62 dst_sel:DWORD dst_unused:UNUSED_PAD src0_sel:DWORD src1_sel:WORD_1
v_mfma_f32_16x16x32_bf16 v[40:43],a[208:211],v[188:191],v[40:43]
buffer_load_dwordx4 v6,s[36:39],s89 offen lds
ds_read_u16_d16_hi v62,v62
v_lshlrev_b32_sdwa v61,1,v61 dst_sel:DWORD dst_unused:UNUSED_PAD src0_sel:DWORD src1_sel:WORD_1
v_mfma_f32_16x16x32_bf16 v[40:43],a[212:215],v[172:175],v[40:43]
s_waitcnt lgkmcnt(6)
ds_read_u16 v61,v61
ds_read_b64_tr_b16 a[80:81],v2 offset:14368
v_or3_b32 v252,v14,v15,v252
v_accvgpr_write_b32 a13,v173
v_mfma_f32_16x16x32_bf16 v[112:115],a[248:251],v[48:51],v[112:115]
s_add_u32 m0,s58,6160
ds_read_b64 v[14:15],v7 offset:13344
ds_read_b64_tr_b16 a[82:83],v2 offset:15392
v_exp_f16_sdwa v8,v9 dst_sel:WORD_1 dst_unused:UNUSED_PRESERVE src0_sel:WORD_1
v_lshlrev_b32_sdwa v63,1,v63 dst_sel:DWORD dst_unused:UNUSED_PAD src0_sel:DWORD src1_sel:WORD_1
v_mfma_f32_16x16x32_bf16 v[116:119],a[248:251],v[44:47],v[116:119]
s_add_u32 s69,s69,s53
ds_read_u16_d16_hi v63,v63
ds_read_b64_tr_b16 a[76:77],v2 offset:12320
v_lshlrev_b32_sdwa v56,1,v56 dst_sel:DWORD dst_unused:UNUSED_PAD src0_sel:DWORD src1_sel:WORD_1
v_mfma_f32_16x16x32_bf16 v[76:79],a[252:255],v[44:47],v[76:79]
buffer_load_dwordx4 v5,s[44:47],s69 offen offset:2048 lds
ds_read_u16 v56,v56
v_pk_fma_f16 v8,v8,s55,s55
v_mfma_f32_16x16x32_bf16 v[72:75],a[252:255],v[48:51],v[72:75]
ds_read_b64_tr_b16 a[78:79],v2 offset:13344
v_rcp_f16_e64 v9,v8
v_lshlrev_b32_sdwa v58,1,v58 dst_sel:DWORD dst_unused:UNUSED_PAD src0_sel:DWORD src1_sel:WORD_1
v_mfma_f32_16x16x32_bf16 v[120:123],a[92:95],v[48:51],v[120:123]
ds_read_u16_d16_hi v58,v58
v_lshlrev_b32_sdwa v57,1,v57 dst_sel:DWORD dst_unused:UNUSED_PAD src0_sel:DWORD src1_sel:WORD_1
v_mfma_f32_16x16x32_bf16 v[124:127],a[92:95],v[44:47],v[124:127]
s_waitcnt lgkmcnt(6)
ds_read_u16 v57,v57
v_or3_b32 v252,v14,v15,v252
v_accvgpr_write_b32 a14,v174
v_mfma_f32_16x16x32_bf16 v[92:95],a[48:51],v[44:47],v[92:95]
s_add_u32 m0,s57,24752
ds_read_b64_tr_b16 a[242:243],v3 offset:13984
v_lshlrev_b32_sdwa v59,1,v59 dst_sel:DWORD dst_unused:UNUSED_PAD src0_sel:DWORD src1_sel:WORD_1
v_mfma_f32_16x16x32_bf16 v[88:91],a[48:51],v[48:51],v[88:91]
s_add_u32 s89,s56,3145728
v_rcp_f16_sdwa v9,v8 dst_sel:WORD_1 dst_unused:UNUSED_PRESERVE src0_sel:WORD_1
v_mfma_f32_16x16x32_bf16 v[128:131],a[56:59],v[48:51],v[128:131]
TE02_%=:
s_cbranch_execz TP3812_%=
buffer_load_dwordx4 v6,s[36:39],s89 offen lds
ds_read_u16_d16_hi v59,v59
v_lshlrev_b32_sdwa v52,1,v52 dst_sel:DWORD dst_unused:UNUSED_PAD src0_sel:DWORD src1_sel:WORD_1
v_mfma_f32_16x16x32_bf16 v[132:135],a[56:59],v[44:47],v[132:135]
s_nop 0
ds_read_u16 v52,v52
v_lshlrev_b32_sdwa v54,1,v54 dst_sel:DWORD dst_unused:UNUSED_PAD src0_sel:DWORD src1_sel:WORD_1
v_mfma_f32_16x16x32_bf16 v[108:111],a[64:67],v[44:47],v[108:111]
ds_read_u16_d16_hi v54,v54
v_fma_mix_f32 v53,v53,v9,0 op_sel_hi:[0,1,0]
v_mfma_f32_16x16x32_bf16 v[104:107],a[64:67],v[48:51],v[104:107]
ds_read_b64_tr_b16 a[244:245],v3 offset:12456
v_accvgpr_write_b32 a15,v175
v_fma_mix_f32 v55,v55,v9,0 op_sel:[0,1,0] op_sel_hi:[0,1,0]
v_mfma_f32_16x16x32_bf16 v[44:47],a[52:55],a[0:3],v[20:23]
ds_read_b64_tr_b16 a[246:247],v3 offset:13992
v_lshlrev_b32_sdwa v28,1,v28 dst_sel:DWORD dst_unused:UNUSED_PAD src0_sel:DWORD src1_sel:WORD_1
v_mfma_f32_16x16x32_bf16 v[44:47],a[60:63],v[176:179],v[44:47]
ds_read_u16 v28,v28
v_lshl_add_u64 v[32:33],v[32:33],0,v[34:35]
v_mfma_f32_32x32x16_bf16 v[160:175],a[88:91],a[88:91],0
v_lshlrev_b32_sdwa v30,1,v30 dst_sel:DWORD dst_unused:UNUSED_PAD src0_sel:DWORD src1_sel:WORD_1
v_accvgpr_write_b32 a16,v176
v_lshlrev_b32_sdwa v29,1,v29 dst_sel:DWORD dst_unused:UNUSED_PAD src0_sel:DWORD src1_sel:WORD_1
v_accvgpr_write_b32 a17,v177
v_lshlrev_b32_sdwa v31,1,v31 dst_sel:DWORD dst_unused:UNUSED_PAD src0_sel:DWORD src1_sel:WORD_1
v_mfma_f32_16x16x32_bf16 v[44:47],a[68:71],v[192:195],v[44:47]
s_waitcnt lgkmcnt(7)
ds_read_u16_d16_hi v30,v30
v_lshl_add_u64 v[34:35],v[60:61],0,v[62:63]
v_mfma_f32_16x16x32_bf16 v[48:51],a[84:87],v[192:195],v[16:19]
ds_read_u16 v29,v29
v_accvgpr_write_b32 a18,v178
v_mfma_f32_16x16x32_bf16 v[48:51],a[80:83],v[176:179],v[48:51]
s_waitcnt lgkmcnt(7)
ds_read_u16_d16_hi v31,v31
v_lshl_add_u64 v[60:61],v[56:57],0,v[58:59]
v_mfma_f32_16x16x32_bf16 v[48:51],a[76:79],a[0:3],v[48:51]
v_lshlrev_b32_sdwa v24,1,v24 dst_sel:DWORD dst_unused:UNUSED_PAD src0_sel:DWORD src1_sel:WORD_1
v_lshlrev_b32_sdwa v26,1,v26 dst_sel:DWORD dst_unused:UNUSED_PAD src0_sel:DWORD src1_sel:WORD_1
v_mfma_f32_16x16x32_bf16 v[56:59],a[76:79],a[4:7],v[16:19]
s_waitcnt lgkmcnt(6)
ds_read_u16 v24,v24
v_or_b32 v62,v52,v54
v_mfma_f32_16x16x32_bf16 v[56:59],a[80:83],v[180:183],v[56:59]
s_waitcnt vmcnt(13)
ds_read_u16_d16_hi v26,v26
v_cvt_pk_bf16_f32 v63,v53,v55
v_mfma_f32_16x16x32_bf16 v[56:59],a[84:87],v[196:199],v[56:59]
s_barrier
v_accvgpr_write_b32 a19,v179
v_lshlrev_b32_sdwa v25,1,v25 dst_sel:DWORD dst_unused:UNUSED_PAD src0_sel:DWORD src1_sel:WORD_1
v_mfma_f32_16x16x32_bf16 v[52:55],a[68:71],v[196:199],v[20:23]
s_add_u32 m0,s57,28288
ds_read_u16 v25,v25
v_accvgpr_write_b32 a20,v180
v_mfma_f32_16x16x32_bf16 v[52:55],a[60:63],v[180:183],v[52:55]
s_add_u32 s89,s56,3244032
ds_read_b64_tr_b16 a[92:93],v3 offset:18480
v_lshlrev_b32_sdwa v27,1,v27 dst_sel:DWORD dst_unused:UNUSED_PAD src0_sel:DWORD src1_sel:WORD_1
v_mfma_f32_16x16x32_bf16 v[52:55],a[52:55],a[4:7],v[52:55]
buffer_load_dwordx4 v6,s[36:39],s89 offen lds
v_accvgpr_write_b32 a21,v181
v_mfma_f32_16x16x32_bf16 v[136:139],a[224:227],v[32:35],v[136:139]
ds_read_u16_d16_hi v27,v27
v_lshlrev_b32_sdwa v40,1,v40 dst_sel:DWORD dst_unused:UNUSED_PAD src0_sel:DWORD src1_sel:WORD_1
v_mfma_f32_16x16x32_bf16 v[140:143],a[224:227],v[60:63],v[140:143]
ds_read_u16 v40,v40
v_lshlrev_b32_sdwa v42,1,v42 dst_sel:DWORD dst_unused:UNUSED_PAD src0_sel:DWORD src1_sel:WORD_1
v_accvgpr_write_b32 a22,v182
v_mfma_f32_16x16x32_bf16 v[68:71],a[228:231],v[60:63],v[68:71]
ds_read_u16_d16_hi v42,v42
v_lshlrev_b32_sdwa v41,1,v41 dst_sel:DWORD dst_unused:UNUSED_PAD src0_sel:DWORD src1_sel:WORD_1
v_mfma_f32_16x16x32_bf16 v[64:67],a[228:231],v[32:35],v[64:67]
ds_read_u16 v41,v41
v_accvgpr_write_b32 a23,v183
v_mfma_f32_16x16x32_bf16 v[144:147],a[232:235],v[32:35],v[144:147]
ds_read_b64_tr_b16 a[94:95],v3 offset:20016
ds_read_b64_tr_b16 a[48:49],v3 offset:18488
v_lshlrev_b32_sdwa v43,1,v43 dst_sel:DWORD dst_unused:UNUSED_PAD src0_sel:DWORD src1_sel:WORD_1
v_mfma_f32_16x16x32_bf16 v[148:151],a[232:235],v[60:63],v[148:151]
ds_read_u16_d16_hi v43,v43
v_accvgpr_write_b32 a24,v184
v_lshlrev_b32_sdwa v36,1,v36 dst_sel:DWORD dst_unused:UNUSED_PAD src0_sel:DWORD src1_sel:WORD_1
v_mfma_f32_16x16x32_bf16 v[84:87],a[236:239],v[60:63],v[84:87]
ds_read_u16 v36,v36
ds_read_b64_tr_b16 a[50:51],v3 offset:20024
v_accvgpr_write_b32 a25,v185
v_mfma_f32_16x16x32_bf16 v[80:83],a[236:239],v[32:35],v[80:83]
ds_read_b64_tr_b16 a[56:57],v3 offset:18544
v_lshlrev_b32_sdwa v38,1,v38 dst_sel:DWORD dst_unused:UNUSED_PAD src0_sel:DWORD src1_sel:WORD_1
v_mfma_f32_16x16x32_bf16 v[152:155],a[240:243],v[32:35],v[152:155]
ds_read_u16_d16_hi v38,v38
ds_read_b64_tr_b16 a[58:59],v3 offset:20080
v_lshlrev_b32_sdwa v37,1,v37 dst_sel:DWORD dst_unused:UNUSED_PAD src0_sel:DWORD src1_sel:WORD_1
v_accvgpr_write_b32 a26,v186
v_mfma_f32_16x16x32_bf16 v[156:159],a[240:243],v[60:63],v[156:159]
s_waitcnt lgkmcnt(11)
ds_read_u16 v37,v37
ds_read_b64_tr_b16 a[64:65],v3 offset:18552
v_lshlrev_b32_sdwa v39,1,v39 dst_sel:DWORD dst_unused:UNUSED_PAD src0_sel:DWORD src1_sel:WORD_1
v_mfma_f32_16x16x32_bf16 v[100:103],a[244:247],v[60:63],v[100:103]
s_nop 0
ds_read_u16_d16_hi v39,v39
ds_read_b64_tr_b16 a[66:67],v3 offset:20088
v_accvgpr_write_b32 a27,v187
v_mfma_f32_16x16x32_bf16 v[96:99],a[244:247],v[32:35],v[96:99]
s_add_u32 m0,s57,31832
ds_read_b64_tr_b16 a[248:249],v3 offset:18608
v_accvgpr_write_b32 a28,v188
v_mfma_f32_16x16x32_bf16 v[60:63],a[52:55],a[8:11],v[20:23]
s_add_u32 s89,s56,3342336
ds_read_b64 v[14:15],v7 offset:18480
ds_read_b64_tr_b16 a[72:73],v1 offset:18480
v_lshlrev_b32_sdwa v44,1,v44 dst_sel:DWORD dst_unused:UNUSED_PAD src0_sel:DWORD src1_sel:WORD_1
v_mfma_f32_16x16x32_bf16 v[60:63],a[60:63],a[24:27],v[60:63]
buffer_load_dwordx4 v6,s[36:39],s89 offen lds
ds_read_b64_tr_b16 a[74:75],v1 offset:19504
v_lshl_add_u64 v[28:29],v[28:29],0,v[30:31]
v_fma_mixlo_f16 v8,v57,s99,0
v_mfma_f32_16x16x32_bf16 v[60:63],a[68:71],v[200:203],v[60:63]
ds_read_b64_tr_b16 a[220:221],v1 offset:20528
ds_read_b64_tr_b16 a[222:223],v1 offset:21552
v_lshl_add_u64 v[30:31],v[24:25],0,v[26:27]
v_lshlrev_b32_sdwa v46,1,v46 dst_sel:DWORD dst_unused:UNUSED_PAD src0_sel:DWORD src1_sel:WORD_1
v_mfma_f32_16x16x32_bf16 v[32:35],a[84:87],v[200:203],v[16:19]
ds_read_u16 v44,v44
ds_read_u16_d16_hi v46,v46
v_fma_mixhi_f16 v8,v59,s99,0
v_mfma_f32_16x16x32_bf16 v[32:35],a[80:83],a[24:27],v[32:35]
s_waitcnt lgkmcnt(14)
ds_read_b64_tr_b16 a[216:217],v1 offset:22576
ds_read_b64_tr_b16 a[218:219],v1 offset:23600
v_lshl_add_u64 v[24:25],v[40:41],0,v[42:43]
v_lshlrev_b32_sdwa v45,1,v45 dst_sel:DWORD dst_unused:UNUSED_PAD src0_sel:DWORD src1_sel:WORD_1
v_pk_fma_f16 v9,v8,v8,s54
v_mfma_f32_16x16x32_bf16 v[32:35],a[76:79],a[8:11],v[32:35]
s_waitcnt lgkmcnt(8)
ds_read_u16 v45,v45
ds_read_b64_tr_b16 a[192:193],v2 offset:22576
v_or3_b32 v252,v14,v15,v252
v_lshlrev_b32_sdwa v47,1,v47 dst_sel:DWORD dst_unused:UNUSED_PAD src0_sel:DWORD src1_sel:WORD_1
v_accvgpr_write_b32 a29,v189
v_mfma_f32_16x16x32_bf16 v[40:43],a[76:79],a[12:15],v[16:19]
ds_read_b64 v[14:15],v7 offset:18992
ds_read_b128 v[16:19],v4 offset:512
v_pk_mul_f16 v9,v8,v9
v_mfma_f32_16x16x32_bf16 v[40:43],a[80:83],v[188:191],v[40:43]
ds_read_u16_d16_hi v47,v47
ds_read_b64_tr_b16 a[194:195],v2 offset:23600
v_lshl_add_u64 v[26:27],v[36:37],0,v[38:39]
v_lshlrev_b32_sdwa v48,1,v48 dst_sel:DWORD dst_unused:UNUSED_PAD src0_sel:DWORD src1_sel:WORD_1
v_mfma_f32_16x16x32_bf16 v[40:43],a[84:87],v[204:207],v[40:43]
ds_read_u16 v48,v48
v_exp_f16 v8,v9
v_mfma_f32_16x16x32_bf16 v[36:39],a[68:71],v[204:207],v[20:23]
ds_read_b128 v[20:23],v4 offset:544
ds_read_b64_tr_b16 a[196:197],v2 offset:20528
v_lshlrev_b32_sdwa v50,1,v50 dst_sel:DWORD dst_unused:UNUSED_PAD src0_sel:DWORD src1_sel:WORD_1
v_mfma_f32_16x16x32_bf16 v[36:39],a[60:63],v[188:191],v[36:39]
ds_read_u16_d16_hi v50,v50
ds_read_b64_tr_b16 a[198:199],v2 offset:21552
v_lshlrev_b32_sdwa v49,1,v49 dst_sel:DWORD dst_unused:UNUSED_PAD src0_sel:DWORD src1_sel:WORD_1
v_mfma_f32_16x16x32_bf16 v[36:39],a[52:55],a[12:15],v[36:39]
s_waitcnt lgkmcnt(8)
ds_read_u16 v49,v49
ds_read_b64_tr_b16 a[200:201],v2 offset:18480
v_or3_b32 v252,v14,v15,v252
v_accvgpr_write_b32 a30,v190
v_mfma_f32_16x16x32_bf16 v[112:115],a[224:227],v[28:31],v[112:115]
s_add_u32 m0,s57,35368
ds_read_b64 v[14:15],v7 offset:19504
ds_read_b64_tr_b16 a[202:203],v2 offset:19504
v_exp_f16_sdwa v8,v9 dst_sel:WORD_1 dst_unused:UNUSED_PRESERVE src0_sel:WORD_1
v_lshlrev_b32_sdwa v51,1,v51 dst_sel:DWORD dst_unused:UNUSED_PAD src0_sel:DWORD src1_sel:WORD_1
v_mfma_f32_16x16x32_bf16 v[116:119],a[224:227],v[24:27],v[116:119]
s_add_u32 s89,s56,3440640
ds_read_u16_d16_hi v51,v51
v_lshlrev_b32_sdwa v52,1,v52 dst_sel:DWORD dst_unused:UNUSED_PAD src0_sel:DWORD src1_sel:WORD_1
v_mfma_f32_16x16x32_bf16 v[76:79],a[228:231],v[24:27],v[76:79]
buffer_load_dwordx4 v6,s[36:39],s89 offen lds
ds_read_u16 v52,v52
v_pk_fma_f16 v8,v8,s55,s55
v_mfma_f32_16x16x32_bf16 v[72:75],a[228:231],v[28:31],v[72:75]
v_rcp_f16 v9,v8
v_lshlrev_b32_sdwa v54,1,v54 dst_sel:DWORD dst_unused:UNUSED_PAD src0_sel:DWORD src1_sel:WORD_1
v_mfma_f32_16x16x32_bf16 v[120:123],a[232:235],v[28:31],v[120:123]
ds_read_u16_d16_hi v54,v54
v_lshlrev_b32_sdwa v53,1,v53 dst_sel:DWORD dst_unused:UNUSED_PAD src0_sel:DWORD src1_sel:WORD_1
v_mfma_f32_16x16x32_bf16 v[124:127],a[232:235],v[24:27],v[124:127]
s_waitcnt lgkmcnt(4)
ds_read_u16 v53,v53
v_or3_b32 v252,v14,v15,v252
v_accvgpr_write_b32 a31,v191
v_mfma_f32_16x16x32_bf16 v[92:95],a[236:239],v[24:27],v[92:95]
ds_read_b64_tr_b16 a[250:251],v3 offset:20144
v_lshlrev_b32_sdwa v55,1,v55 dst_sel:DWORD dst_unused:UNUSED_PAD src0_sel:DWORD src1_sel:WORD_1
v_mfma_f32_16x16x32_bf16 v[88:91],a[236:239],v[28:31],v[88:91]
v_rcp_f16_sdwa v9,v8 dst_sel:WORD_1 dst_unused:UNUSED_PRESERVE src0_sel:WORD_1
v_mfma_f32_16x16x32_bf16 v[128:131],a[240:243],v[28:31],v[128:131]
ds_read_u16_d16_hi v55,v55
v_lshlrev_b32_sdwa v56,1,v56 dst_sel:DWORD dst_unused:UNUSED_PAD src0_sel:DWORD src1_sel:WORD_1
v_mfma_f32_16x16x32_bf16 v[132:135],a[240:243],v[24:27],v[132:135]
ds_read_u16 v56,v56
v_lshlrev_b32_sdwa v58,1,v58 dst_sel:DWORD dst_unused:UNUSED_PAD src0_sel:DWORD src1_sel:WORD_1
v_mfma_f32_16x16x32_bf16 v[108:111],a[244:247],v[24:27],v[108:111]
ds_read_u16_d16_hi v58,v58
v_fma_mix_f32 v57,v57,v9,0 op_sel_hi:[0,1,0]
v_mfma_f32_16x16x32_bf16 v[104:107],a[244:247],v[28:31],v[104:107]
ds_read_b64_tr_b16 a[252:253],v3 offset:18616
v_mfma_f32_32x32x16_bf16 v[176:191],a[88:91],a[88:91],0
v_fma_mix_f32 v59,v59,v9,0 op_sel:[0,1,0] op_sel_hi:[0,1,0]
v_mfma_f32_16x16x32_bf16 v[24:27],a[72:75],a[0:3],v[16:19]
s_add_u32 m0,s57,38896
ds_read_b64_tr_b16 a[254:255],v3 offset:20152
v_lshlrev_b32_sdwa v60,1,v60 dst_sel:DWORD dst_unused:UNUSED_PAD src0_sel:DWORD src1_sel:WORD_1
v_mfma_f32_16x16x32_bf16 v[24:27],a[220:223],a[16:19],v[24:27]
s_add_u32 s89,s56,3538944
ds_read_u16 v60,v60
v_lshl_add_u64 v[44:45],v[44:45],0,v[46:47]
v_accvgpr_write_b32 a32,v192
v_mfma_f32_16x16x32_bf16 v[24:27],a[216:219],v[192:195],v[24:27]
s_waitcnt lgkmcnt(7)
buffer_load_dwordx4 v6,s[36:39],s89 offen lds
v_lshl_add_u64 v[46:47],v[48:49],0,v[50:51]
v_lshlrev_b32_sdwa v62,1,v62 dst_sel:DWORD dst_unused:UNUSED_PAD src0_sel:DWORD src1_sel:WORD_1
v_mfma_f32_16x16x32_bf16 v[28:31],a[192:195],v[192:195],v[20:23]
ds_read_u16_d16_hi v62,v62
v_accvgpr_write_b32 a33,v193
v_lshlrev_b32_sdwa v61,1,v61 dst_sel:DWORD dst_unused:UNUSED_PAD src0_sel:DWORD src1_sel:WORD_1
v_mfma_f32_16x16x32_bf16 v[28:31],a[196:199],a[16:19],v[28:31]
s_waitcnt lgkmcnt(6)
ds_read_u16 v61,v61
v_lshl_add_u64 v[48:49],v[52:53],0,v[54:55]
v_mfma_f32_16x16x32_bf16 v[28:31],a[200:203],a[0:3],v[28:31]
v_lshlrev_b32_sdwa v63,1,v63 dst_sel:DWORD dst_unused:UNUSED_PAD src0_sel:DWORD src1_sel:WORD_1
v_accvgpr_write_b32 a34,v194
v_mfma_f32_16x16x32_bf16 v[52:55],a[200:203],a[4:7],v[20:23]
s_waitcnt lgkmcnt(5)
ds_read_u16_d16_hi v63,v63
v_or_b32 v50,v56,v58
v_lshlrev_b32_sdwa v32,1,v32 dst_sel:DWORD dst_unused:UNUSED_PAD src0_sel:DWORD src1_sel:WORD_1
v_accvgpr_write_b32 a35,v195
v_mfma_f32_16x16x32_bf16 v[52:55],a[196:199],a[20:23],v[52:55]
s_waitcnt vmcnt(9)
ds_read_u16 v32,v32
v_cvt_pk_bf16_f32 v51,v57,v59
v_lshlrev_b32_sdwa v34,1,v34 dst_sel:DWORD dst_unused:UNUSED_PAD src0_sel:DWORD src1_sel:WORD_1
v_mfma_f32_16x16x32_bf16 v[52:55],a[192:195],v[196:199],v[52:55]
s_barrier
ds_read_u16_d16_hi v34,v34
v_accvgpr_write_b32 a36,v196
v_mfma_f32_16x16x32_bf16 v[56:59],a[216:219],v[196:199],v[16:19]
ds_read_b64_tr_b16 a[224:225],v3 offset:0
v_lshlrev_b32_sdwa v33,1,v33 dst_sel:DWORD dst_unused:UNUSED_PAD src0_sel:DWORD src1_sel:WORD_1
v_mfma_f32_16x16x32_bf16 v[56:59],a[220:223],a[20:23],v[56:59]
s_add_u32 m0,s58,12320
ds_read_u16 v33,v33
v_accvgpr_write_b32 a37,v197
v_lshlrev_b32_sdwa v35,1,v35 dst_sel:DWORD dst_unused:UNUSED_PAD src0_sel:DWORD src1_sel:WORD_1
v_mfma_f32_16x16x32_bf16 v[56:59],a[72:75],a[4:7],v[56:59]
s_add_u32 s69,s69,s59
ds_read_u16_d16_hi v35,v35
v_accvgpr_write_b32 a38,v198
v_mfma_f32_16x16x32_bf16 v[136:139],a[92:95],v[44:47],v[136:139]
buffer_load_dwordx4 v5,s[44:47],s69 offen offset:0 lds
v_lshlrev_b32_sdwa v36,1,v36 dst_sel:DWORD dst_unused:UNUSED_PAD src0_sel:DWORD src1_sel:WORD_1
v_mfma_f32_16x16x32_bf16 v[140:143],a[92:95],v[48:51],v[140:143]
ds_read_u16 v36,v36
v_lshlrev_b32_sdwa v38,1,v38 dst_sel:DWORD dst_unused:UNUSED_PAD src0_sel:DWORD src1_sel:WORD_1
v_accvgpr_write_b32 a39,v199
v_mfma_f32_16x16x32_bf16 v[68:71],a[48:51],v[48:51],v[68:71]
ds_read_u16_d16_hi v38,v38
v_lshlrev_b32_sdwa v37,1,v37 dst_sel:DWORD dst_unused:UNUSED_PAD src0_sel:DWORD src1_sel:WORD_1
v_mfma_f32_16x16x32_bf16 v[64:67],a[48:51],v[44:47],v[64:67]
s_add_u32 m0,s57,42432
ds_read_u16 v37,v37
v_accvgpr_write_b32 a40,v200
v_mfma_f32_16x16x32_bf16 v[144:147],a[56:59],v[44:47],v[144:147]
s_add_u32 s89,s56,3637248
ds_read_b64_tr_b16 a[226:227],v3 offset:1536
ds_read_b64_tr_b16 a[228:229],v3 offset:8
v_lshlrev_b32_sdwa v39,1,v39 dst_sel:DWORD dst_unused:UNUSED_PAD src0_sel:DWORD src1_sel:WORD_1
v_mfma_f32_16x16x32_bf16 v[148:151],a[56:59],v[48:51],v[148:151]
buffer_load_dwordx4 v6,s[36:39],s89 offen lds
ds_read_u16_d16_hi v39,v39
v_accvgpr_write_b32 a41,v201
v_lshlrev_b32_sdwa v40,1,v40 dst_sel:DWORD dst_unused:UNUSED_PAD src0_sel:DWORD src1_sel:WORD_1
v_mfma_f32_16x16x32_bf16 v[84:87],a[64:67],v[48:51],v[84:87]
s_add_u32 m0,s58,12320
ds_read_u16 v40,v40
ds_read_b64_tr_b16 a[230:231],v3 offset:1544
v_accvgpr_write_b32 a42,v202
v_mfma_f32_16x16x32_bf16 v[80:83],a[64:67],v[44:47],v[80:83]
s_add_u32 s69,s69,s53
ds_read_b64_tr_b16 a[232:233],v3 offset:64
v_lshlrev_b32_sdwa v42,1,v42 dst_sel:DWORD dst_unused:UNUSED_PAD src0_sel:DWORD src1_sel:WORD_1
v_mfma_f32_16x16x32_bf16 v[152:155],a[248:251],v[44:47],v[152:155]
buffer_load_dwordx4 v5,s[44:47],s69 offen offset:1024 lds
ds_read_u16_d16_hi v42,v42
v_lshlrev_b32_sdwa v41,1,v41 dst_sel:DWORD dst_unused:UNUSED_PAD src0_sel:DWORD src1_sel:WORD_1
v_accvgpr_write_b32 a43,v203
v_mfma_f32_16x16x32_bf16 v[156:159],a[248:251],v[48:51],v[156:159]
s_waitcnt lgkmcnt(10)
ds_read_u16 v41,v41
ds_read_b64_tr_b16 a[234:235],v3 offset:1600
v_lshlrev_b32_sdwa v43,1,v43 dst_sel:DWORD dst_unused:UNUSED_PAD src0_sel:DWORD src1_sel:WORD_1
v_mfma_f32_16x16x32_bf16 v[100:103],a[252:255],v[48:51],v[100:103]
s_add_u32 s69,s69,s53
ds_read_u16_d16_hi v43,v43
ds_read_b64_tr_b16 a[236:237],v3 offset:72
v_accvgpr_write_b32 a44,v204
v_mfma_f32_16x16x32_bf16 v[96:99],a[252:255],v[44:47],v[96:99]
buffer_load_dwordx4 v5,s[44:47],s69 offen offset:2048 lds
ds_read_b64_tr_b16 a[238:239],v3 offset:1608
v_accvgpr_write_b32 a45,v205
v_mfma_f32_16x16x32_bf16 v[48:51],a[72:75],a[8:11],v[16:19]
ds_read_b64 v[14:15],v7 offset:0
ds_read_b64_tr_b16 a[204:205],v1 offset:0
v_lshlrev_b32_sdwa v24,1,v24 dst_sel:DWORD dst_unused:UNUSED_PAD src0_sel:DWORD src1_sel:WORD_1
v_mfma_f32_16x16x32_bf16 v[48:51],a[220:223],a[24:27],v[48:51]
ds_read_b64_tr_b16 a[206:207],v1 offset:1024
ds_read_b64_tr_b16 a[208:209],v1 offset:2048
v_lshl_add_u64 v[60:61],v[60:61],0,v[62:63]
v_fma_mixlo_f16 v8,v53,s99,0
v_mfma_f32_16x16x32_bf16 v[48:51],a[216:219],a[40:43],v[48:51]
ds_read_b64_tr_b16 a[210:211],v1 offset:3072
ds_read_u16 v24,v24
v_lshl_add_u64 v[62:63],v[32:33],0,v[34:35]
v_lshlrev_b32_sdwa v26,1,v26 dst_sel:DWORD dst_unused:UNUSED_PAD src0_sel:DWORD src1_sel:WORD_1
v_mfma_f32_16x16x32_bf16 v[44:47],a[192:195],a[40:43],v[20:23]
ds_read_u16_d16_hi v26,v26
ds_read_b64_tr_b16 a[212:213],v1 offset:4096
v_fma_mixhi_f16 v8,v55,s99,0
v_mfma_f32_16x16x32_bf16 v[44:47],a[196:199],a[24:27],v[44:47]
s_waitcnt lgkmcnt(14)
ds_read_b64_tr_b16 a[214:215],v1 offset:5120
ds_read_b64_tr_b16 a[240:241],v3 offset:128
v_lshl_add_u64 v[32:33],v[36:37],0,v[38:39]
v_lshlrev_b32_sdwa v25,1,v25 dst_sel:DWORD dst_unused:UNUSED_PAD src0_sel:DWORD src1_sel:WORD_1
v_pk_fma_f16 v9,v8,v8,s54
v_mfma_f32_16x16x32_bf16 v[44:47],a[200:203],a[8:11],v[44:47]
s_waitcnt lgkmcnt(9)
ds_read_u16 v25,v25
v_or3_b32 v252,v14,v15,v252
v_lshlrev_b32_sdwa v27,1,v27 dst_sel:DWORD dst_unused:UNUSED_PAD src0_sel:DWORD src1_sel:WORD_1
v_mfma_f32_16x16x32_bf16 v[36:39],a[200:203],a[12:15],v[20:23]
s_add_u32 m0,s57,45976
ds_read_b64 v[14:15],v7 offset:512
ds_read_b128 v[20:23],v4 offset:640
v_pk_mul_f16 v9,v8,v9
v_mfma_f32_16x16x32_bf16 v[36:39],a[196:199],a[28:31],v[36:39]
s_add_u32 s89,s56,3735552
ds_read_u16_d16_hi v27,v27
ds_read_b64_tr_b16 a[200:201],v2 offset:4096
v_lshl_add_u64 v[34:35],v[40:41],0,v[42:43]
v_lshlrev_b32_sdwa v28,1,v28 dst_sel:DWORD dst_unused:UNUSED_PAD src0_sel:DWORD src1_sel:WORD_1
v_accvgpr_write_b32 a46,v206
v_mfma_f32_16x16x32_bf16 v[36:39],a[192:195],v[204:207],v[36:39]
ds_read_u16 v28,v28
v_exp_f16 v8,v9
v_mfma_f32_16x16x32_bf16 v[40:43],a[216:219],v[204:207],v[16:19]
buffer_load_dwordx4 v6,s[36:39],s89 offen lds
ds_read_b128 v[16:19],v4 offset:672
v_lshlrev_b32_sdwa v30,1,v30 dst_sel:DWORD dst_unused:UNUSED_PAD src0_sel:DWORD src1_sel:WORD_1
v_mfma_f32_16x16x32_bf16 v[40:43],a[220:223],a[28:31],v[40:43]
ds_read_u16_d16_hi v30,v30
ds_read_b64_tr_b16 a[202:203],v2 offset:5120
v_lshlrev_b32_sdwa v29,1,v29 dst_sel:DWORD dst_unused:UNUSED_PAD src0_sel:DWORD src1_sel:WORD_1
v_mfma_f32_16x16x32_bf16 v[40:43],a[72:75],a[12:15],v[40:43]
s_waitcnt lgkmcnt(7)
ds_read_u16 v29,v29
ds_read_b64_tr_b16 a[196:197],v2 offset:2048
v_or3_b32 v252,v14,v15,v252
v_mfma_f32_16x16x32_bf16 v[112:115],a[92:95],v[60:63],v[112:115]
ds_read_b64 v[14:15],v7 offset:1024
ds_read_b64_tr_b16 a[198:199],v2 offset:3072
v_exp_f16_sdwa v8,v9 dst_sel:WORD_1 dst_unused:UNUSED_PRESERVE src0_sel:WORD_1
v_lshlrev_b32_sdwa v31,1,v31 dst_sel:DWORD dst_unused:UNUSED_PAD src0_sel:DWORD src1_sel:WORD_1
v_mfma_f32_16x16x32_bf16 v[116:119],a[92:95],v[32:35],v[116:119]
ds_read_u16_d16_hi v31,v31
ds_read_b64_tr_b16 a[192:193],v2 offset:0
v_lshlrev_b32_sdwa v56,1,v56 dst_sel:DWORD dst_unused:UNUSED_PAD src0_sel:DWORD src1_sel:WORD_1
v_mfma_f32_16x16x32_bf16 v[76:79],a[48:51],v[32:35],v[76:79]
ds_read_u16 v56,v56
ds_read_b64_tr_b16 a[194:195],v2 offset:1024
v_pk_fma_f16 v8,v8,s55,s55
v_mfma_f32_16x16x32_bf16 v[72:75],a[48:51],v[60:63],v[72:75]
v_rcp_f16_e64 v9,v8
v_lshlrev_b32_sdwa v58,1,v58 dst_sel:DWORD dst_unused:UNUSED_PAD src0_sel:DWORD src1_sel:WORD_1
v_accvgpr_write_b32 a47,v207
v_mfma_f32_16x16x32_bf16 v[120:123],a[56:59],v[60:63],v[120:123]
s_add_u32 m0,s57,49512
ds_read_u16_d16_hi v58,v58
v_lshlrev_b32_sdwa v57,1,v57 dst_sel:DWORD dst_unused:UNUSED_PAD src0_sel:DWORD src1_sel:WORD_1
v_mfma_f32_16x16x32_bf16 v[124:127],a[56:59],v[32:35],v[124:127]
s_waitcnt lgkmcnt(6)
ds_read_u16 v57,v57
v_or3_b32 v252,v14,v15,v252
v_mfma_f32_16x16x32_bf16 v[92:95],a[64:67],v[32:35],v[92:95]
s_add_u32 s89,s56,3833856
ds_read_b64_tr_b16 a[242:243],v3 offset:1664
v_and_b32 v252,0x40004000,v252
v_lshlrev_b32_sdwa v59,1,v59 dst_sel:DWORD dst_unused:UNUSED_PAD src0_sel:DWORD src1_sel:WORD_1
v_mfma_f32_16x16x32_bf16 v[88:91],a[64:67],v[60:63],v[88:91]
buffer_load_dwordx4 v6,s[36:39],s89 offen lds
v_cmp_ne_u32_e32 vcc,0,v252
s_nop 0
s_or_b64 s[90:91],s[90:91],vcc
v_mfma_f32_16x16x32_bf16 v[128:131],a[248:251],v[60:63],v[128:131]
ds_read_u16_d16_hi v59,v59
v_rcp_f16_sdwa v9,v8 dst_sel:WORD_1 dst_unused:UNUSED_PRESERVE src0_sel:WORD_1
v_lshlrev_b32_sdwa v52,1,v52 dst_sel:DWORD dst_unused:UNUSED_PAD src0_sel:DWORD src1_sel:WORD_1
v_mfma_f32_16x16x32_bf16 v[132:135],a[248:251],v[32:35],v[132:135]
ds_read_u16 v52,v52
v_lshlrev_b32_sdwa v54,1,v54 dst_sel:DWORD dst_unused:UNUSED_PAD src0_sel:DWORD src1_sel:WORD_1
v_mfma_f32_16x16x32_bf16 v[108:111],a[252:255],v[32:35],v[108:111]
ds_read_u16_d16_hi v54,v54
v_fma_mix_f32 v53,v53,v9,0 op_sel_hi:[0,1,0]
v_mfma_f32_16x16x32_bf16 v[104:107],a[252:255],v[60:63],v[104:107]
ds_read_b64_tr_b16 a[244:245],v3 offset:136
v_mfma_f32_32x32x16_bf16 v[192:207],a[88:91],a[88:91],0
v_fma_mix_f32 v55,v55,v9,0 op_sel:[0,1,0] op_sel_hi:[0,1,0]
v_mfma_f32_16x16x32_bf16 v[32:35],a[204:207],a[0:3],v[20:23]
ds_read_b64_tr_b16 a[246:247],v3 offset:1672
v_lshlrev_b32_sdwa v48,1,v48 dst_sel:DWORD dst_unused:UNUSED_PAD src0_sel:DWORD src1_sel:WORD_1
v_mfma_f32_16x16x32_bf16 v[32:35],a[208:211],a[16:19],v[32:35]
ds_read_u16 v48,v48
v_lshl_add_u64 v[24:25],v[24:25],0,v[26:27]
v_mfma_f32_16x16x32_bf16 v[32:35],a[212:215],a[32:35],v[32:35]
s_waitcnt lgkmcnt(7)
v_lshl_add_u64 v[26:27],v[28:29],0,v[30:31]
v_lshlrev_b32_sdwa v50,1,v50 dst_sel:DWORD dst_unused:UNUSED_PAD src0_sel:DWORD src1_sel:WORD_1
v_mfma_f32_16x16x32_bf16 v[60:63],a[200:203],a[32:35],v[16:19]
ds_read_u16_d16_hi v50,v50
v_lshlrev_b32_sdwa v49,1,v49 dst_sel:DWORD dst_unused:UNUSED_PAD src0_sel:DWORD src1_sel:WORD_1
v_mfma_f32_16x16x32_bf16 v[60:63],a[196:199],a[16:19],v[60:63]
s_waitcnt lgkmcnt(6)
ds_read_u16 v49,v49
v_lshl_add_u64 v[28:29],v[56:57],0,v[58:59]
v_mfma_f32_16x16x32_bf16 v[60:63],a[192:195],a[0:3],v[60:63]
v_lshlrev_b32_sdwa v51,1,v51 dst_sel:DWORD dst_unused:UNUSED_PAD src0_sel:DWORD src1_sel:WORD_1
v_lshlrev_b32_sdwa v44,1,v44 dst_sel:DWORD dst_unused:UNUSED_PAD src0_sel:DWORD src1_sel:WORD_1
v_mfma_f32_16x16x32_bf16 v[56:59],a[192:195],a[4:7],v[16:19]
s_waitcnt lgkmcnt(5)
ds_read_u16_d16_hi v51,v51
v_or_b32 v30,v52,v54
v_mfma_f32_16x16x32_bf16 v[56:59],a[196:199],a[20:23],v[56:59]
s_waitcnt vmcnt(11)
ds_read_u16 v44,v44
v_cvt_pk_bf16_f32 v31,v53,v55
v_lshlrev_b32_sdwa v46,1,v46 dst_sel:DWORD dst_unused:UNUSED_PAD src0_sel:DWORD src1_sel:WORD_1
v_mfma_f32_16x16x32_bf16 v[56:59],a[200:203],a[36:39],v[56:59]
s_barrier
ds_read_u16_d16_hi v46,v46
v_lshlrev_b32_sdwa v45,1,v45 dst_sel:DWORD dst_unused:UNUSED_PAD src0_sel:DWORD src1_sel:WORD_1
v_mfma_f32_16x16x32_bf16 v[52:55],a[212:215],a[36:39],v[20:23]
s_add_u32 m0,s57,53040
ds_read_u16 v45,v45
v_lshlrev_b32_sdwa v47,1,v47 dst_sel:DWORD dst_unused:UNUSED_PAD src0_sel:DWORD src1_sel:WORD_1
v_mfma_f32_16x16x32_bf16 v[52:55],a[208:211],a[20:23],v[52:55]
s_add_u32 s89,s56,3932160
ds_read_u16_d16_hi v47,v47
v_mfma_f32_32x32x16_bf16 v[240:255],a[88:91],a[88:91],0
v_lshlrev_b32_sdwa v40,1,v40 dst_sel:DWORD dst_unused:UNUSED_PAD src0_sel:DWORD src1_sel:WORD_1
v_lshlrev_b32_sdwa v42,1,v42 dst_sel:DWORD dst_unused:UNUSED_PAD src0_sel:DWORD src1_sel:WORD_1
v_lshlrev_b32_sdwa v41,1,v41 dst_sel:DWORD dst_unused:UNUSED_PAD src0_sel:DWORD src1_sel:WORD_1
v_lshlrev_b32_sdwa v43,1,v43 dst_sel:DWORD dst_unused:UNUSED_PAD src0_sel:DWORD src1_sel:WORD_1
v_lshlrev_b32_sdwa v36,1,v36 dst_sel:DWORD dst_unused:UNUSED_PAD src0_sel:DWORD src1_sel:WORD_1
v_mfma_f32_16x16x32_bf16 v[52:55],a[204:207],a[4:7],v[52:55]
buffer_load_dwordx4 v6,s[36:39],s89 offen lds
ds_read_u16 v40,v40
v_mfma_f32_16x16x32_bf16 v[136:139],a[224:227],v[24:27],v[136:139]
s_add_u32 m0,s58,18480
ds_read_u16_d16_hi v42,v42
ds_read_u16 v41,v41
v_mfma_f32_16x16x32_bf16 v[140:143],a[224:227],v[28:31],v[140:143]
s_add_u32 s69,s69,s59
ds_read_u16_d16_hi v43,v43
v_lshlrev_b32_sdwa v38,1,v38 dst_sel:DWORD dst_unused:UNUSED_PAD src0_sel:DWORD src1_sel:WORD_1
v_mfma_f32_16x16x32_bf16 v[68:71],a[228:231],v[28:31],v[68:71]
buffer_load_dwordx4 v5,s[44:47],s69 offen offset:0 lds
ds_read_u16 v36,v36
v_mfma_f32_16x16x32_bf16 v[64:67],a[228:231],v[24:27],v[64:67]
s_add_u32 s69,s69,s53
ds_read_u16_d16_hi v38,v38
ds_read_b64_tr_b16 a[248:249],v3 offset:6160
v_mfma_f32_16x16x32_bf16 v[144:147],a[232:235],v[24:27],v[144:147]
buffer_load_dwordx4 v5,s[44:47],s69 offen offset:1024 lds
v_lshlrev_b32_sdwa v37,1,v37 dst_sel:DWORD dst_unused:UNUSED_PAD src0_sel:DWORD src1_sel:WORD_1
v_mfma_f32_16x16x32_bf16 v[148:151],a[232:235],v[28:31],v[148:151]
ds_read_u16 v37,v37
v_lshlrev_b32_sdwa v39,1,v39 dst_sel:DWORD dst_unused:UNUSED_PAD src0_sel:DWORD src1_sel:WORD_1
v_mfma_f32_16x16x32_bf16 v[84:87],a[236:239],v[28:31],v[84:87]
ds_read_u16_d16_hi v39,v39
v_mfma_f32_16x16x32_bf16 v[80:83],a[236:239],v[24:27],v[80:83]
ds_read_b64_tr_b16 a[250:251],v3 offset:7696
v_mfma_f32_16x16x32_bf16 v[152:155],a[240:243],v[24:27],v[152:155]
s_add_u32 s69,s69,s53
ds_read_b64_tr_b16 a[252:253],v3 offset:6168
v_mfma_f32_16x16x32_bf16 v[156:159],a[240:243],v[28:31],v[156:159]
s_waitcnt lgkmcnt(8)
buffer_load_dwordx4 v5,s[44:47],s69 offen offset:2048 lds
v_mfma_f32_16x16x32_bf16 v[100:103],a[244:247],v[28:31],v[100:103]
ds_read_b64_tr_b16 a[254:255],v3 offset:7704
v_mfma_f32_16x16x32_bf16 v[96:99],a[244:247],v[24:27],v[96:99]
s_waitcnt vmcnt(13)
s_barrier
v_add_u32_sdwa v24,s60,v10 dst_sel:DWORD dst_unused:UNUSED_PAD src0_sel:DWORD src1_sel:WORD_1
v_subrev_u32 v8,88400,v24
v_min_u32_e64 v24,v24,v8
v_add_u32 v25,7072,v24
v_subrev_u32 v8,88400,v25
v_min_u32_e64 v25,v25,v8
v_lshl_add_u64 v[48:49],v[48:49],0,v[50:51]
v_lshl_add_u64 v[50:51],v[44:45],0,v[46:47]
s_waitcnt lgkmcnt(8)
v_lshl_add_u64 v[44:45],v[40:41],0,v[42:43]
s_waitcnt lgkmcnt(3)
v_lshl_add_u64 v[46:47],v[36:37],0,v[38:39]
v_mfma_f32_16x16x32_bf16 v[112:115],a[224:227],v[48:51],v[112:115]
s_add_u32 m0,s58,0
s_add_u32 s69,s69,s59
buffer_load_dwordx4 v5,s[44:47],s69 offen offset:0 lds
v_xad_u32 v28,v11,0,v24
v_xad_u32 v29,v11,8,v25
v_mfma_f32_16x16x32_bf16 v[116:119],a[224:227],v[44:47],v[116:119]
v_xad_u32 v30,v11,16,v24
v_xad_u32 v31,v11,24,v25
v_mfma_f32_16x16x32_bf16 v[76:79],a[228:231],v[44:47],v[76:79]
ds_read_b64 a[48:49],v28 offset:0
ds_read_b64 a[50:51],v28 offset:192
v_mfma_f32_16x16x32_bf16 v[72:75],a[228:231],v[48:51],v[72:75]
TE03_%=:
s_cbranch_execz TP4345_%=
ds_read_b64 a[52:53],v29 offset:0
ds_read_b64 a[54:55],v29 offset:192
v_mfma_f32_16x16x32_bf16 v[120:123],a[232:235],v[48:51],v[120:123]
ds_read_b64 a[56:57],v28 offset:8
ds_read_b64 a[58:59],v28 offset:200
v_mfma_f32_16x16x32_bf16 v[124:127],a[232:235],v[44:47],v[124:127]
s_add_u32 s69,s69,s53
buffer_load_dwordx4 v5,s[44:47],s69 offen offset:1024 lds
ds_read_b64 a[60:61],v29 offset:8
ds_read_b64 a[62:63],v29 offset:200
v_mfma_f32_16x16x32_bf16 v[92:95],a[236:239],v[44:47],v[92:95]
v_mfma_f32_16x16x32_bf16 v[88:91],a[236:239],v[48:51],v[88:91]
v_mfma_f32_16x16x32_bf16 v[128:131],a[240:243],v[48:51],v[128:131]
v_mfma_f32_16x16x32_bf16 v[132:135],a[240:243],v[44:47],v[132:135]
v_mfma_f32_16x16x32_bf16 v[108:111],a[244:247],v[44:47],v[108:111]
v_mfma_f32_16x16x32_bf16 v[104:107],a[244:247],v[48:51],v[104:107]
s_nop 0
ds_read_b64 a[64:65],v30 offset:0
ds_read_b64 a[66:67],v30 offset:192
ds_read_b64 a[68:69],v31 offset:0
ds_read_b64 a[70:71],v31 offset:192
s_waitcnt lgkmcnt(8)
v_smfmac_f32_16x16x64_bf16 v[160:163],a[96:99],a[48:55],v10
s_waitcnt lgkmcnt(4)
s_add_u32 s69,s69,s53
buffer_load_dwordx4 v5,s[44:47],s69 offen offset:2048 lds
ds_read_b64 a[48:49],v30 offset:8
ds_read_b64 a[50:51],v30 offset:200
ds_read_b64 a[52:53],v31 offset:8
ds_read_b64 a[54:55],v31 offset:200
v_smfmac_f32_16x16x64_bf16 v[164:167],a[100:103],a[56:63],v10
s_waitcnt lgkmcnt(4)
ds_read_b64 a[56:57],v28 offset:64
ds_read_b64 a[58:59],v28 offset:256
ds_read_b64 a[60:61],v29 offset:64
ds_read_b64 a[62:63],v29 offset:256
v_smfmac_f32_16x16x64_bf16 v[168:171],a[104:107],a[64:71],v10
s_waitcnt lgkmcnt(4)
ds_read_b64 a[64:65],v28 offset:72
ds_read_b64 a[66:67],v28 offset:264
ds_read_b64 a[68:69],v29 offset:72
ds_read_b64 a[70:71],v29 offset:264
v_and_b32 v48,8,v11
v_pk_mul_f32 v[44:45],v[160:161],v[160:161]
v_pk_mul_f32 v[46:47],v[162:163],v[162:163]
v_smfmac_f32_16x16x64_bf16 v[172:175],a[108:111],a[48:55],v10
s_waitcnt lgkmcnt(4)
ds_read_b64 a[48:49],v30 offset:64
ds_read_b64 a[50:51],v30 offset:256
ds_read_b64 a[52:53],v31 offset:64
ds_read_b64 a[54:55],v31 offset:256
v_mul_u32_u24 v48,884,v48
s_add_u32 s64,s60,3536
v_pk_add_f32 v[36:37],v[160:161],v[164:165]
v_pk_fma_f32 v[44:45],v[164:165],v[164:165],v[44:45]
v_pk_add_f32 v[38:39],v[162:163],v[166:167]
v_pk_fma_f32 v[46:47],v[166:167],v[166:167],v[46:47]
v_smfmac_f32_16x16x64_bf16 v[176:179],a[128:131],a[56:63],v10
s_waitcnt lgkmcnt(4)
ds_read_b64 a[56:57],v30 offset:72
ds_read_b64 a[58:59],v30 offset:264
ds_read_b64 a[60:61],v31 offset:72
ds_read_b64 a[62:63],v31 offset:264
v_add_u32 v51,s64,v48
v_pk_add_f32 v[36:37],v[36:37],v[168:169]
v_pk_fma_f32 v[44:45],v[168:169],v[168:169],v[44:45]
v_pk_add_f32 v[38:39],v[38:39],v[170:171]
v_pk_fma_f32 v[46:47],v[170:171],v[170:171],v[46:47]
v_smfmac_f32_16x16x64_bf16 v[180:183],a[132:135],a[64:71],v10
s_waitcnt lgkmcnt(4)
ds_read_b64 a[64:65],v28 offset:128
ds_read_b64 a[66:67],v28 offset:320
ds_read_b64 a[68:69],v29 offset:128
ds_read_b64 a[70:71],v29 offset:320
v_subrev_u32 v8,88400,v51
v_pk_add_f32 v[36:37],v[36:37],v[172:173]
v_pk_fma_f32 v[44:45],v[172:173],v[172:173],v[44:45]
v_pk_add_f32 v[38:39],v[38:39],v[174:175]
v_pk_fma_f32 v[46:47],v[174:175],v[174:175],v[46:47]
v_smfmac_f32_16x16x64_bf16 v[184:187],a[136:139],a[48:55],v10
s_waitcnt lgkmcnt(4)
ds_read_b64 a[48:49],v28 offset:136
ds_read_b64 a[50:51],v28 offset:328
ds_read_b64 a[52:53],v29 offset:136
ds_read_b64 a[54:55],v29 offset:328
v_min_u32 v51,v51,v8
v_pk_add_f32 v[36:37],v[36:37],v[176:177]
v_pk_fma_f32 v[44:45],v[176:177],v[176:177],v[44:45]
v_pk_add_f32 v[38:39],v[38:39],v[178:179]
v_pk_fma_f32 v[46:47],v[178:179],v[178:179],v[46:47]
v_smfmac_f32_16x16x64_bf16 v[188:191],a[140:143],a[56:63],v10
s_waitcnt lgkmcnt(4)
ds_read_b64 a[56:57],v30 offset:128
ds_read_b64 a[58:59],v30 offset:320
ds_read_b64 a[60:61],v31 offset:128
ds_read_b64 a[62:63],v31 offset:320
v_xad_u32 v28,v11,32,v24
v_xad_u32 v29,v11,40,v25
v_pk_add_f32 v[36:37],v[36:37],v[180:181]
v_pk_fma_f32 v[44:45],v[180:181],v[180:181],v[44:45]
v_pk_add_f32 v[38:39],v[38:39],v[182:183]
v_pk_fma_f32 v[46:47],v[182:183],v[182:183],v[46:47]
v_smfmac_f32_16x16x64_bf16 v[192:195],a[160:163],a[64:71],v10
s_waitcnt lgkmcnt(4)
ds_read_b64 a[64:65],v30 offset:136
ds_read_b64 a[66:67],v30 offset:328
ds_read_b64 a[68:69],v31 offset:136
ds_read_b64 a[70:71],v31 offset:328
v_add_u32 v50,3536,v51
v_pk_add_f32 v[36:37],v[36:37],v[184:185]
v_pk_fma_f32 v[44:45],v[184:185],v[184:185],v[44:45]
v_pk_add_f32 v[38:39],v[38:39],v[186:187]
v_pk_fma_f32 v[46:47],v[186:187],v[186:187],v[46:47]
v_smfmac_f32_16x16x64_bf16 v[196:199],a[164:167],a[48:55],v10
s_waitcnt lgkmcnt(4)
ds_read_b64 a[48:49],v28 offset:0
ds_read_b64 a[50:51],v28 offset:192
ds_read_b64 a[52:53],v29 offset:0
ds_read_b64 a[54:55],v29 offset:192
v_xad_u32 v30,v11,48,v24
v_xad_u32 v31,v11,56,v25
v_pk_add_f32 v[36:37],v[36:37],v[188:189]
v_pk_fma_f32 v[44:45],v[188:189],v[188:189],v[44:45]
v_pk_add_f32 v[38:39],v[38:39],v[190:191]
v_pk_fma_f32 v[46:47],v[190:191],v[190:191],v[46:47]
v_smfmac_f32_16x16x64_bf16 v[200:203],a[168:171],a[56:63],v10
s_waitcnt lgkmcnt(4)
ds_read_b64 a[56:57],v28 offset:8
ds_read_b64 a[58:59],v28 offset:200
ds_read_b64 a[60:61],v29 offset:8
ds_read_b64 a[62:63],v29 offset:200
v_subrev_u32 v8,88400,v50
v_pk_add_f32 v[36:37],v[36:37],v[192:193]
v_pk_fma_f32 v[44:45],v[192:193],v[192:193],v[44:45]
v_pk_add_f32 v[38:39],v[38:39],v[194:195]
v_pk_fma_f32 v[46:47],v[194:195],v[194:195],v[46:47]
v_smfmac_f32_16x16x64_bf16 v[204:207],a[172:175],a[64:71],v10
s_waitcnt lgkmcnt(4)
ds_read_b64 a[64:65],v28 offset:64
ds_read_b64 a[66:67],v28 offset:256
ds_read_b64 a[68:69],v29 offset:64
ds_read_b64 a[70:71],v29 offset:256
v_min_u32 v50,v50,v8
v_pk_add_f32 v[36:37],v[36:37],v[196:197]
v_pk_fma_f32 v[44:45],v[196:197],v[196:197],v[44:45]
v_pk_add_f32 v[38:39],v[38:39],v[198:199]
v_pk_fma_f32 v[46:47],v[198:199],v[198:199],v[46:47]
v_smfmac_f32_16x16x64_bf16 v[208:211],a[112:115],a[48:55],v10
s_waitcnt lgkmcnt(4)
ds_read_b64 a[48:49],v28 offset:72
ds_read_b64 a[50:51],v28 offset:264
ds_read_b64 a[52:53],v29 offset:72
ds_read_b64 a[54:55],v29 offset:264
v_xor_b32 v9,0x80008,v12
v_pk_add_f32 v[36:37],v[36:37],v[200:201]
v_pk_fma_f32 v[44:45],v[200:201],v[200:201],v[44:45]
v_pk_add_f32 v[38:39],v[38:39],v[202:203]
v_pk_fma_f32 v[46:47],v[202:203],v[202:203],v[46:47]
v_smfmac_f32_16x16x64_bf16 v[212:215],a[116:119],a[56:63],v10
s_waitcnt lgkmcnt(4)
ds_read_b64 a[56:57],v28 offset:128
ds_read_b64 a[58:59],v28 offset:320
ds_read_b64 a[60:61],v29 offset:128
ds_read_b64 a[62:63],v29 offset:320
v_add_u32_sdwa v48,v12,v51 dst_sel:DWORD dst_unused:UNUSED_PAD src0_sel:WORD_0 src1_sel:DWORD
v_pk_add_f32 v[36:37],v[36:37],v[204:205]
v_pk_fma_f32 v[44:45],v[204:205],v[204:205],v[44:45]
v_pk_add_f32 v[38:39],v[38:39],v[206:207]
v_pk_fma_f32 v[46:47],v[206:207],v[206:207],v[46:47]
v_smfmac_f32_16x16x64_bf16 v[216:219],a[144:147],a[64:71],v10
s_waitcnt lgkmcnt(4)
ds_read_b64 a[64:65],v28 offset:136
ds_read_b64 a[66:67],v28 offset:328
ds_read_b64 a[68:69],v29 offset:136
ds_read_b64 a[70:71],v29 offset:328
v_add_u32_sdwa v49,v12,v51 dst_sel:DWORD dst_unused:UNUSED_PAD src0_sel:WORD_1 src1_sel:DWORD
v_pk_add_f32 v[36:37],v[36:37],v[208:209]
v_pk_fma_f32 v[44:45],v[208:209],v[208:209],v[44:45]
v_pk_add_f32 v[38:39],v[38:39],v[210:211]
v_pk_fma_f32 v[46:47],v[210:211],v[210:211],v[46:47]
v_smfmac_f32_16x16x64_bf16 v[220:223],a[148:151],a[48:55],v10
s_waitcnt lgkmcnt(4)
ds_read_b64 a[48:49],v30 offset:0
ds_read_b64 a[50:51],v30 offset:192
ds_read_b64 a[52:53],v31 offset:0
ds_read_b64 a[54:55],v31 offset:192
v_add_u32_sdwa v51,v9,v50 dst_sel:DWORD dst_unused:UNUSED_PAD src0_sel:WORD_1 src1_sel:DWORD
v_pk_add_f32 v[36:37],v[36:37],v[212:213]
v_pk_fma_f32 v[44:45],v[212:213],v[212:213],v[44:45]
v_pk_add_f32 v[38:39],v[38:39],v[214:215]
v_pk_fma_f32 v[46:47],v[214:215],v[214:215],v[46:47]
v_smfmac_f32_16x16x64_bf16 v[224:227],a[176:179],a[56:63],v10
s_waitcnt lgkmcnt(4)
ds_read_b64 a[56:57],v30 offset:8
ds_read_b64 a[58:59],v30 offset:200
ds_read_b64 a[60:61],v31 offset:8
ds_read_b64 a[62:63],v31 offset:200
v_add_u32_sdwa v50,v9,v50 dst_sel:DWORD dst_unused:UNUSED_PAD src0_sel:WORD_0 src1_sel:DWORD
v_pk_add_f32 v[36:37],v[36:37],v[216:217]
v_pk_fma_f32 v[44:45],v[216:217],v[216:217],v[44:45]
v_pk_add_f32 v[38:39],v[38:39],v[218:219]
v_pk_fma_f32 v[46:47],v[218:219],v[218:219],v[46:47]
v_smfmac_f32_16x16x64_bf16 v[228:231],a[180:183],a[64:71],v10
s_waitcnt lgkmcnt(4)
ds_read_b64 a[64:65],v30 offset:64
ds_read_b64 a[66:67],v30 offset:256
ds_read_b64 a[68:69],v31 offset:64
ds_read_b64 a[70:71],v31 offset:256
v_pk_add_f32 v[36:37],v[36:37],v[220:221]
v_pk_fma_f32 v[44:45],v[220:221],v[220:221],v[44:45]
v_pk_add_f32 v[38:39],v[38:39],v[222:223]
v_pk_fma_f32 v[46:47],v[222:223],v[222:223],v[46:47]
v_smfmac_f32_16x16x64_bf16 v[232:235],a[120:123],a[48:55],v10
s_waitcnt lgkmcnt(4)
ds_read_b64 a[48:49],v30 offset:72
ds_read_b64 a[50:51],v30 offset:264
ds_read_b64 a[52:53],v31 offset:72
ds_read_b64 a[54:55],v31 offset:264
v_pk_add_f32 v[36:37],v[36:37],v[224:225]
v_pk_fma_f32 v[44:45],v[224:225],v[224:225],v[44:45]
v_pk_add_f32 v[38:39],v[38:39],v[226:227]
v_pk_fma_f32 v[46:47],v[226:227],v[226:227],v[46:47]
v_smfmac_f32_16x16x64_bf16 v[236:239],a[124:127],a[56:63],v10
s_waitcnt lgkmcnt(4)
ds_read_b64 a[56:57],v30 offset:128
ds_read_b64 a[58:59],v30 offset:320
ds_read_b64 a[60:61],v31 offset:128
ds_read_b64 a[62:63],v31 offset:320
v_pk_add_f32 v[36:37],v[36:37],v[228:229]
v_pk_fma_f32 v[44:45],v[228:229],v[228:229],v[44:45]
v_pk_add_f32 v[38:39],v[38:39],v[230:231]
v_pk_fma_f32 v[46:47],v[230:231],v[230:231],v[46:47]
v_smfmac_f32_16x16x64_bf16 v[240:243],a[152:155],a[64:71],v10
s_waitcnt lgkmcnt(4)
ds_read_b64 a[64:65],v30 offset:136
ds_read_b64 a[66:67],v30 offset:328
ds_read_b64 a[68:69],v31 offset:136
ds_read_b64 a[70:71],v31 offset:328
v_pk_add_f32 v[36:37],v[36:37],v[232:233]
v_pk_fma_f32 v[44:45],v[232:233],v[232:233],v[44:45]
v_pk_add_f32 v[38:39],v[38:39],v[234:235]
v_pk_fma_f32 v[46:47],v[234:235],v[234:235],v[46:47]
v_smfmac_f32_16x16x64_bf16 v[244:247],a[156:159],a[48:55],v10
s_waitcnt lgkmcnt(4)
ds_read_b64 a[224:225],v48 offset:57344
ds_read_b64 a[226:227],v48 offset:57352
ds_read_b64 a[228:229],v49 offset:57344
ds_read_b64 a[230:231],v49 offset:57352
ds_read_b128 a[240:243],v13 offset:0
v_pk_add_f32 v[36:37],v[36:37],v[236:237]
v_pk_fma_f32 v[44:45],v[236:237],v[236:237],v[44:45]
v_pk_add_f32 v[38:39],v[38:39],v[238:239]
v_pk_fma_f32 v[46:47],v[238:239],v[238:239],v[46:47]
v_smfmac_f32_16x16x64_bf16 v[248:251],a[184:187],a[56:63],v10
s_waitcnt lgkmcnt(5)
ds_read_b64 a[232:233],v50 offset:57344
ds_read_b64 a[234:235],v50 offset:57352
ds_read_b64 a[236:237],v51 offset:57344
ds_read_b64 a[238:239],v51 offset:57352
ds_read_b128 a[244:247],v13 offset:256
v_pk_add_f32 v[36:37],v[36:37],v[240:241]
v_pk_fma_f32 v[44:45],v[240:241],v[240:241],v[44:45]
v_pk_add_f32 v[38:39],v[38:39],v[242:243]
v_pk_fma_f32 v[46:47],v[242:243],v[242:243],v[46:47]
v_smfmac_f32_16x16x64_bf16 v[252:255],a[188:191],a[64:71],v10
ds_read_b128 v[28:31],v4 offset:3584
ds_read_b128 v[24:27],v4 offset:3616
v_pk_add_f32 v[36:37],v[36:37],v[244:245]
v_pk_fma_f32 v[44:45],v[244:245],v[244:245],v[44:45]
v_pk_add_f32 v[38:39],v[38:39],v[246:247]
v_pk_fma_f32 v[46:47],v[246:247],v[246:247],v[46:47]
v_pk_add_f32 v[36:37],v[36:37],v[248:249]
v_pk_fma_f32 v[44:45],v[248:249],v[248:249],v[44:45]
v_pk_add_f32 v[38:39],v[38:39],v[250:251]
v_pk_fma_f32 v[46:47],v[250:251],v[250:251],v[46:47]
v_pk_add_f32 v[36:37],v[36:37],v[252:253]
v_pk_fma_f32 v[44:45],v[252:253],v[252:253],v[44:45]
v_pk_add_f32 v[38:39],v[38:39],v[254:255]
v_pk_fma_f32 v[46:47],v[254:255],v[254:255],v[46:47]
s_nop 0
v_permlane32_swap_b32 v36,v38
v_permlane32_swap_b32 v37,v39
v_permlane32_swap_b32 v44,v46
v_permlane32_swap_b32_e64 v45,v47
v_pk_add_f32 v[36:37],v[36:37],v[38:39]
v_pk_add_f32 v[44:45],v[44:45],v[46:47]
s_add_u32 s60,s60,56576
s_sub_u32 s64,s60,88400
s_min_u32 s60,s60,s64
v_permlane16_swap_b32 v36,v37
v_permlane16_swap_b32 v44,v45
v_add_f32 v8,v36,v37
v_add_f32_e64 v9,v44,v45
v_mul_f32 v8,0x3c2aaaab,v8
v_mul_f32 v9,0x3c2aaaab,v9
v_cmp_ngt_f32 vcc,0x5d800000,v9
v_fma_f32 v9,-v8,v8,v9
v_max_f32 v9,0,v9
s_or_b64 s[90:91],s[90:91],vcc
v_add_f32 v9,0x3727c5ac,v9
v_rsq_f32 v9,v9
s_nop 0
v_mul_f32 v36,s96,v9
v_mul_f32 v37,s96,v9
v_mul_f32 v44,-v8,v36
v_mul_f32 v45,-v8,v36
v_permlane16_swap_b32 v36,v37
s_nop 0
v_permlane16_swap_b32 v44,v45
v_mov_b64 v[38:39],v[36:37]
v_mov_b64 v[46:47],v[44:45]
s_nop 0
v_permlane32_swap_b32 v36,v38
v_permlane32_swap_b32 v37,v39
v_permlane32_swap_b32 v44,v46
v_permlane32_swap_b32 v45,v47
v_pk_fma_f32 v[160:161],v[160:161],v[36:37],v[44:45]
v_pk_fma_f32 v[162:163],v[162:163],v[38:39],v[46:47]
v_pk_fma_f32 v[164:165],v[164:165],v[36:37],v[44:45]
v_pk_fma_f32 v[166:167],v[166:167],v[38:39],v[46:47]
v_pk_fma_f32 v[168:169],v[168:169],v[36:37],v[44:45]
v_pk_fma_f32 v[170:171],v[170:171],v[38:39],v[46:47]
v_pk_fma_f32 v[172:173],v[172:173],v[36:37],v[44:45]
v_pk_fma_f32 v[174:175],v[174:175],v[38:39],v[46:47]
v_pk_fma_f32 v[208:209],v[208:209],v[36:37],v[44:45]
v_pk_fma_f32 v[210:211],v[210:211],v[38:39],v[46:47]
v_pk_fma_f32 v[212:213],v[212:213],v[36:37],v[44:45]
v_pk_fma_f32 v[214:215],v[214:215],v[38:39],v[46:47]
v_pk_fma_f32 v[232:233],v[232:233],v[36:37],v[44:45]
v_pk_fma_f32 v[234:235],v[234:235],v[38:39],v[46:47]
v_pk_fma_f32 v[236:237],v[236:237],v[36:37],v[44:45]
v_pk_fma_f32 v[238:239],v[238:239],v[38:39],v[46:47]
v_pk_fma_f32 v[176:177],v[176:177],v[36:37],v[44:45]
v_pk_fma_f32 v[178:179],v[178:179],v[38:39],v[46:47]
v_pk_fma_f32 v[180:181],v[180:181],v[36:37],v[44:45]
v_pk_fma_f32 v[182:183],v[182:183],v[38:39],v[46:47]
v_pk_fma_f32 v[184:185],v[184:185],v[36:37],v[44:45]
v_pk_fma_f32 v[186:187],v[186:187],v[38:39],v[46:47]
v_pk_fma_f32 v[188:189],v[188:189],v[36:37],v[44:45]
v_pk_fma_f32 v[190:191],v[190:191],v[38:39],v[46:47]
v_pk_fma_f32 v[216:217],v[216:217],v[36:37],v[44:45]
v_pk_fma_f32 v[218:219],v[218:219],v[38:39],v[46:47]
v_pk_fma_f32 v[220:221],v[220:221],v[36:37],v[44:45]
v_pk_fma_f32 v[222:223],v[222:223],v[38:39],v[46:47]
v_pk_fma_f32 v[240:241],v[240:241],v[36:37],v[44:45]
v_pk_fma_f32 v[242:243],v[242:243],v[38:39],v[46:47]
v_pk_fma_f32 v[244:245],v[244:245],v[36:37],v[44:45]
v_pk_fma_f32 v[246:247],v[246:247],v[38:39],v[46:47]
v_pk_fma_f32 v[192:193],v[192:193],v[36:37],v[44:45]
v_pk_fma_f32 v[194:195],v[194:195],v[38:39],v[46:47]
v_pk_fma_f32 v[196:197],v[196:197],v[36:37],v[44:45]
v_pk_fma_f32 v[198:199],v[198:199],v[38:39],v[46:47]
v_pk_fma_f32 v[200:201],v[200:201],v[36:37],v[44:45]
v_pk_fma_f32 v[202:203],v[202:203],v[38:39],v[46:47]
v_pk_fma_f32 v[204:205],v[204:205],v[36:37],v[44:45]
v_pk_fma_f32 v[206:207],v[206:207],v[38:39],v[46:47]
v_pk_fma_f32 v[224:225],v[224:225],v[36:37],v[44:45]
v_pk_fma_f32 v[226:227],v[226:227],v[38:39],v[46:47]
v_pk_fma_f32 v[228:229],v[228:229],v[36:37],v[44:45]
v_pk_fma_f32 v[230:231],v[230:231],v[38:39],v[46:47]
v_pk_fma_f32 v[248:249],v[248:249],v[36:37],v[44:45]
v_pk_fma_f32 v[250:251],v[250:251],v[38:39],v[46:47]
v_pk_fma_f32 v[252:253],v[252:253],v[36:37],v[44:45]
v_pk_fma_f32 v[254:255],v[254:255],v[38:39],v[46:47]
v_mfma_f32_16x16x32_bf16 v[40:43],a[204:207],a[8:11],v[20:23]
ds_read_b64_tr_b16 a[216:217],v1 offset:6160
v_cvt_pk_bf16_f32 v160,v160,v164
v_cvt_pk_bf16_f32 v168,v168,v172
v_cvt_pk_bf16_f32 v208,v208,v212
v_lshlrev_b32_sdwa v32,1,v32 dst_sel:DWORD dst_unused:UNUSED_PAD src0_sel:DWORD src1_sel:WORD_1
v_mfma_f32_16x16x32_bf16 v[40:43],a[208:211],a[24:27],v[40:43]
ds_read_u16 v32,v32
v_accvgpr_write_b32 a48,v160
v_accvgpr_write_b32 a49,v168
v_cvt_pk_bf16_f32 v232,v232,v236
v_accvgpr_write_b32 a50,v208
v_mfma_f32_16x16x32_bf16 v[40:43],a[212:215],a[40:43],v[40:43]
ds_read_b64_tr_b16 a[218:219],v1 offset:7184
v_cvt_pk_bf16_f32 v161,v161,v165
v_accvgpr_write_b32 a51,v232
v_cvt_pk_bf16_f32 v169,v169,v173
v_accvgpr_write_b32 a52,v161
v_mfma_f32_16x16x32_bf16 v[36:39],a[200:203],a[40:43],v[16:19]
ds_read_b64_tr_b16 a[220:221],v1 offset:8208
v_cvt_pk_bf16_f32 v209,v209,v213
v_accvgpr_write_b32 a53,v169
v_cvt_pk_bf16_f32 v233,v233,v237
v_lshlrev_b32_sdwa v34,1,v34 dst_sel:DWORD dst_unused:UNUSED_PAD src0_sel:DWORD src1_sel:WORD_1
v_accvgpr_write_b32 a54,v209
v_mfma_f32_16x16x32_bf16 v[36:39],a[196:199],a[24:27],v[36:39]
ds_read_u16_d16_hi v34,v34
v_cvt_pk_bf16_f32 v162,v162,v166
v_accvgpr_write_b32 a55,v233
v_cvt_pk_bf16_f32 v170,v170,v174
v_accvgpr_write_b32 a56,v162
v_mfma_f32_16x16x32_bf16 v[36:39],a[192:195],a[8:11],v[36:39]
ds_read_b64_tr_b16 a[222:223],v1 offset:9232
v_accvgpr_write_b32 a57,v170
v_cvt_pk_bf16_f32 v210,v210,v214
v_accvgpr_write_b32 a58,v210
v_lshlrev_b32_sdwa v33,1,v33 dst_sel:DWORD dst_unused:UNUSED_PAD src0_sel:DWORD src1_sel:WORD_1
v_cvt_pk_bf16_f32 v234,v234,v238
v_mfma_f32_16x16x32_bf16 v[44:47],a[192:195],a[12:15],v[16:19]
ds_read_u16 v33,v33
v_accvgpr_write_b32 a59,v234
v_cvt_pk_bf16_f32 v163,v163,v167
v_accvgpr_write_b32 a60,v163
v_mfma_f32_16x16x32_bf16 v[44:47],a[196:199],a[28:31],v[44:47]
v_cvt_pk_bf16_f32 v171,v171,v175
v_accvgpr_write_b32 a61,v171
v_cvt_pk_bf16_f32 v211,v211,v215
v_accvgpr_write_b32 a62,v211
v_lshlrev_b32_sdwa v35,1,v35 dst_sel:DWORD dst_unused:UNUSED_PAD src0_sel:DWORD src1_sel:WORD_1
v_mfma_f32_16x16x32_bf16 v[44:47],a[200:203],a[44:47],v[44:47]
s_waitcnt lgkmcnt(7)
ds_read_u16_d16_hi v35,v35
v_cvt_pk_bf16_f32 v235,v235,v239
v_accvgpr_write_b32 a63,v235
v_cvt_pk_bf16_f32 v176,v176,v180
v_cvt_pk_bf16_f32 v184,v184,v188
v_mfma_f32_16x16x32_bf16 v[232:235],a[240:243],a[224:227],v[28:31]
s_nop 0
v_cvt_pk_bf16_f32 v216,v216,v220
v_accvgpr_write_b32 a64,v176
v_accvgpr_write_b32 a65,v184
v_cvt_pk_bf16_f32 v240,v240,v244
v_lshlrev_b32_sdwa v60,1,v60 dst_sel:DWORD dst_unused:UNUSED_PAD src0_sel:DWORD src1_sel:WORD_1
v_mfma_f32_16x16x32_bf16 v[236:239],a[240:243],a[228:231],v[28:31]
ds_read_u16 v60,v60
v_accvgpr_write_b32 a66,v216
v_cvt_pk_bf16_f32 v177,v177,v181
v_accvgpr_write_b32 a67,v240
v_cvt_pk_bf16_f32 v185,v185,v189
v_mfma_f32_16x16x32_bf16 v[208:211],a[240:243],a[232:235],v[28:31]
v_accvgpr_write_b32 a68,v177
v_cvt_pk_bf16_f32 v217,v217,v221
v_accvgpr_write_b32 a69,v185
v_lshlrev_b32_sdwa v62,1,v62 dst_sel:DWORD dst_unused:UNUSED_PAD src0_sel:DWORD src1_sel:WORD_1
v_cvt_pk_bf16_f32 v241,v241,v245
v_mfma_f32_16x16x32_bf16 v[212:215],a[240:243],a[236:239],v[28:31]
ds_read_b128 a[240:243],v13 offset:512
ds_read_b128 v[28:31],v4 offset:3712
v_accvgpr_write_b32 a70,v217
v_cvt_pk_bf16_f32 v178,v178,v182
v_accvgpr_write_b32 a71,v241
v_mfma_f32_16x16x32_bf16 v[160:163],a[244:247],a[224:227],v[24:27]
ds_read_b64 a[224:225],v48 offset:57408
ds_read_b64 a[226:227],v48 offset:57416
v_cvt_pk_bf16_f32 v186,v186,v190
v_accvgpr_write_b32 a72,v178
v_accvgpr_write_b32 a73,v186
v_cvt_pk_bf16_f32 v218,v218,v222
v_lshlrev_b32_sdwa v61,1,v61 dst_sel:DWORD dst_unused:UNUSED_PAD src0_sel:DWORD src1_sel:WORD_1
v_mfma_f32_16x16x32_bf16 v[164:167],a[244:247],a[228:231],v[24:27]
ds_read_b64 a[228:229],v49 offset:57408
ds_read_b64 a[230:231],v49 offset:57416
v_accvgpr_write_b32 a74,v218
v_cvt_pk_bf16_f32 v242,v242,v246
v_accvgpr_write_b32 a75,v242
v_cvt_pk_bf16_f32 v179,v179,v183
v_mfma_f32_16x16x32_bf16 v[168:171],a[244:247],a[232:235],v[24:27]
ds_read_b64 a[232:233],v50 offset:57408
ds_read_b64 a[234:235],v50 offset:57416
v_accvgpr_write_b32 a76,v179
v_cvt_pk_bf16_f32 v187,v187,v191
v_accvgpr_write_b32 a77,v187
v_cvt_pk_bf16_f32 v219,v219,v223
v_lshlrev_b32_sdwa v63,1,v63 dst_sel:DWORD dst_unused:UNUSED_PAD src0_sel:DWORD src1_sel:WORD_1
v_mfma_f32_16x16x32_bf16 v[172:175],a[244:247],a[236:239],v[24:27]
s_waitcnt lgkmcnt(4)
ds_read_b64 a[236:237],v51 offset:57408
ds_read_b64 a[238:239],v51 offset:57416
v_accvgpr_write_b32 a78,v219
v_cvt_pk_bf16_f32 v243,v243,v247
v_accvgpr_write_b32 a79,v243
v_cvt_pk_bf16_f32 v192,v192,v196
v_mfma_f32_16x16x32_bf16 v[240:243],a[240:243],a[224:227],v[28:31]
s_waitcnt lgkmcnt(4)
ds_read_b128 a[244:247],v13 offset:768
ds_read_b128 v[24:27],v4 offset:3744
v_cvt_pk_bf16_f32 v200,v200,v204
v_cvt_pk_bf16_f32 v224,v224,v228
v_accvgpr_write_b32 a80,v192
v_accvgpr_write_b32 a81,v200
v_lshlrev_b32_sdwa v52,1,v52 dst_sel:DWORD dst_unused:UNUSED_PAD src0_sel:DWORD src1_sel:WORD_1
v_mfma_f32_16x16x32_bf16 v[244:247],a[240:243],a[228:231],v[28:31]
s_waitcnt lgkmcnt(4)
ds_read_u16_d16_hi v62,v62
v_cvt_pk_bf16_f32 v248,v248,v252
v_accvgpr_write_b32 a82,v224
v_cvt_pk_bf16_f32 v193,v193,v197
v_mfma_f32_16x16x32_bf16 v[216:219],a[240:243],a[232:235],v[28:31]
s_waitcnt lgkmcnt(3)
ds_read_u16 v61,v61
v_accvgpr_write_b32 a83,v248
v_cvt_pk_bf16_f32 v201,v201,v205
v_accvgpr_write_b32 a84,v193
v_cvt_pk_bf16_f32 v225,v225,v229
v_lshlrev_b32_sdwa v54,1,v54 dst_sel:DWORD dst_unused:UNUSED_PAD src0_sel:DWORD src1_sel:WORD_1
v_mfma_f32_16x16x32_bf16 v[220:223],a[240:243],a[236:239],v[28:31]
s_waitcnt lgkmcnt(2)
ds_read_b128 a[240:243],v13 offset:1024
ds_read_b128 v[28:31],v4 offset:3840
v_accvgpr_write_b32 a85,v201
v_cvt_pk_bf16_f32 v249,v249,v253
v_accvgpr_write_b32 a86,v225
v_cvt_pk_bf16_f32 v194,v194,v198
v_mfma_f32_16x16x32_bf16 v[176:179],a[244:247],a[224:227],v[24:27]
s_nop 0
ds_read_b64 a[224:225],v48 offset:57472
ds_read_b64 a[226:227],v48 offset:57480
v_accvgpr_write_b32 a87,v249
v_cvt_pk_bf16_f32 v202,v202,v206
v_accvgpr_write_b32 a88,v194
v_accvgpr_write_b32 a89,v202
v_lshlrev_b32_sdwa v53,1,v53 dst_sel:DWORD dst_unused:UNUSED_PAD src0_sel:DWORD src1_sel:WORD_1
v_mfma_f32_16x16x32_bf16 v[180:183],a[244:247],a[228:231],v[24:27]
ds_read_b64 a[228:229],v49 offset:57472
ds_read_b64 a[230:231],v49 offset:57480
v_cvt_pk_bf16_f32 v226,v226,v230
v_accvgpr_write_b32 a90,v226
v_cvt_pk_bf16_f32 v250,v250,v254
v_accvgpr_write_b32 a91,v250
v_mfma_f32_16x16x32_bf16 v[184:187],a[244:247],a[232:235],v[24:27]
s_nop 0
ds_read_b64 a[232:233],v50 offset:57472
ds_read_b64 a[234:235],v50 offset:57480
v_cvt_pk_bf16_f32 v195,v195,v199
v_accvgpr_write_b32 a92,v195
v_cvt_pk_bf16_f32 v203,v203,v207
v_accvgpr_write_b32 a93,v203
v_mfma_f32_16x16x32_bf16 v[188:191],a[244:247],a[236:239],v[24:27]
s_waitcnt lgkmcnt(4)
ds_read_b64 a[236:237],v51 offset:57472
ds_read_b64 a[238:239],v51 offset:57480
v_cvt_pk_bf16_f32 v227,v227,v231
v_accvgpr_write_b32 a94,v227
v_cvt_pk_bf16_f32 v251,v251,v255
v_lshlrev_b32_sdwa v55,1,v55 dst_sel:DWORD dst_unused:UNUSED_PAD src0_sel:DWORD src1_sel:WORD_1
v_accvgpr_write_b32 a95,v251
v_mfma_f32_16x16x32_bf16 v[248:251],a[240:243],a[224:227],v[28:31]
s_waitcnt lgkmcnt(4)
ds_read_b128 a[244:247],v13 offset:1280
TP2752_%=:
s_cbranch_execz TP4880_%=
ds_read_b128 v[24:27],v4 offset:3872
v_mfma_f32_16x16x32_bf16 v[252:255],a[240:243],a[228:231],v[28:31]
s_waitcnt lgkmcnt(4)
ds_read_u16_d16_hi v63,v63
ds_read_u16 v52,v52
v_lshlrev_b32_sdwa v56,1,v56 dst_sel:DWORD dst_unused:UNUSED_PAD src0_sel:DWORD src1_sel:WORD_1
v_mfma_f32_16x16x32_bf16 v[224:227],a[240:243],a[232:235],v[28:31]
s_waitcnt lgkmcnt(4)
ds_read_u16_d16_hi v54,v54
v_lshlrev_b32_sdwa v58,1,v58 dst_sel:DWORD dst_unused:UNUSED_PAD src0_sel:DWORD src1_sel:WORD_1
v_mfma_f32_16x16x32_bf16 v[228:231],a[240:243],a[236:239],v[28:31]
s_waitcnt lgkmcnt(3)
ds_read_b64_tr_b16 a[240:241],v3 offset:6224
ds_read_b64_tr_b16 a[242:243],v3 offset:7760
v_mfma_f32_16x16x32_bf16 v[192:195],a[244:247],a[224:227],v[24:27]
ds_read_b64_tr_b16 a[224:225],v3 offset:6232
ds_read_b64_tr_b16 a[226:227],v3 offset:7768
v_mfma_f32_16x16x32_bf16 v[196:199],a[244:247],a[228:231],v[24:27]
ds_read_b64_tr_b16 a[228:229],v3 offset:6288
ds_read_b64_tr_b16 a[230:231],v3 offset:7824
v_lshlrev_b32_sdwa v57,1,v57 dst_sel:DWORD dst_unused:UNUSED_PAD src0_sel:DWORD src1_sel:WORD_1
v_mfma_f32_16x16x32_bf16 v[200:203],a[244:247],a[232:235],v[24:27]
ds_read_b64_tr_b16 a[232:233],v3 offset:6296
ds_read_b64_tr_b16 a[234:235],v3 offset:7832
v_mfma_f32_16x16x32_bf16 v[204:207],a[244:247],a[236:239],v[24:27]
ds_read_u16 v53,v53
ds_read_u16_d16_hi v55,v55
v_lshlrev_b32_sdwa v59,1,v59 dst_sel:DWORD dst_unused:UNUSED_PAD src0_sel:DWORD src1_sel:WORD_1
v_mfma_f32_16x16x32_bf16 v[48:51],a[212:215],a[44:47],v[20:23]
ds_read_u16 v56,v56
v_mfma_f32_16x16x32_bf16 v[48:51],a[208:211],a[28:31],v[48:51]
s_waitcnt vmcnt(9)
ds_read_u16_d16_hi v58,v58
v_mfma_f32_16x16x32_bf16 v[48:51],a[204:207],a[12:15],v[48:51]
s_barrier
ds_read_u16 v57,v57
ds_read_u16_d16_hi v59,v59
v_mfma_f32_16x16x32_bf16 v[28:31],a[204:207],a[48:51],v[20:23]
s_add_u32 m0,s57,56576
ds_read_b64 v[14:15],v7 offset:6160
v_lshlrev_b32_sdwa v40,1,v40 dst_sel:DWORD dst_unused:UNUSED_PAD src0_sel:DWORD src1_sel:WORD_1
v_mfma_f32_16x16x32_bf16 v[28:31],a[208:211],a[64:67],v[28:31]
s_add_u32 s89,s56,4030464
ds_read_u16 v40,v40
v_lshl_add_u64 v[32:33],v[32:33],0,v[34:35]
v_mfma_f32_16x16x32_bf16 v[28:31],a[212:215],a[80:83],v[28:31]
s_waitcnt lgkmcnt(12)
buffer_load_dwordx4 v6,s[36:39],s89 offen lds
v_lshl_add_u64 v[34:35],v[60:61],0,v[62:63]
v_lshlrev_b32_sdwa v42,1,v42 dst_sel:DWORD dst_unused:UNUSED_PAD src0_sel:DWORD src1_sel:WORD_1
v_mfma_f32_16x16x32_bf16 v[24:27],a[200:203],a[80:83],v[16:19]
ds_read_u16_d16_hi v42,v42
v_lshlrev_b32_sdwa v41,1,v41 dst_sel:DWORD dst_unused:UNUSED_PAD src0_sel:DWORD src1_sel:WORD_1
v_mfma_f32_16x16x32_bf16 v[24:27],a[196:199],a[64:67],v[24:27]
s_waitcnt lgkmcnt(7)
ds_read_u16 v41,v41
v_lshl_add_u64 v[60:61],v[52:53],0,v[54:55]
v_mfma_f32_16x16x32_bf16 v[24:27],a[192:195],a[48:51],v[24:27]
s_waitcnt lgkmcnt(3)
v_or3_b32 v13,v14,v15,0
v_lshlrev_b32_sdwa v43,1,v43 dst_sel:DWORD dst_unused:UNUSED_PAD src0_sel:DWORD src1_sel:WORD_1
v_mfma_f32_16x16x32_bf16 v[52:55],a[192:195],a[52:55],v[16:19]
ds_read_b64 v[14:15],v7 offset:6672
v_lshlrev_b32_sdwa v36,1,v36 dst_sel:DWORD dst_unused:UNUSED_PAD src0_sel:DWORD src1_sel:WORD_1
v_mfma_f32_16x16x32_bf16 v[52:55],a[196:199],a[68:71],v[52:55]
ds_read_u16_d16_hi v43,v43
v_lshl_add_u64 v[62:63],v[56:57],0,v[58:59]
v_lshlrev_b32_sdwa v38,1,v38 dst_sel:DWORD dst_unused:UNUSED_PAD src0_sel:DWORD src1_sel:WORD_1
v_mfma_f32_16x16x32_bf16 v[52:55],a[200:203],a[84:87],v[52:55]
ds_read_u16 v36,v36
v_lshlrev_b32_sdwa v37,1,v37 dst_sel:DWORD dst_unused:UNUSED_PAD src0_sel:DWORD src1_sel:WORD_1
v_mfma_f32_16x16x32_bf16 v[56:59],a[212:215],a[84:87],v[20:23]
ds_read_u16_d16_hi v38,v38
v_lshlrev_b32_sdwa v39,1,v39 dst_sel:DWORD dst_unused:UNUSED_PAD src0_sel:DWORD src1_sel:WORD_1
v_mfma_f32_16x16x32_bf16 v[56:59],a[208:211],a[68:71],v[56:59]
ds_read_u16 v37,v37
v_lshlrev_b32_sdwa v48,1,v48 dst_sel:DWORD dst_unused:UNUSED_PAD src0_sel:DWORD src1_sel:WORD_1
v_mfma_f32_16x16x32_bf16 v[56:59],a[204:207],a[52:55],v[56:59]
s_waitcnt lgkmcnt(4)
ds_read_u16_d16_hi v39,v39
v_or3_b32 v13,v14,v15,v13
v_mfma_f32_16x16x32_bf16 v[136:139],a[248:251],v[32:35],v[136:139]
ds_read_b64 v[14:15],v7 offset:7184
v_lshlrev_b32_sdwa v50,1,v50 dst_sel:DWORD dst_unused:UNUSED_PAD src0_sel:DWORD src1_sel:WORD_1
v_mfma_f32_16x16x32_bf16 v[140:143],a[248:251],v[60:63],v[140:143]
ds_read_u16 v48,v48
v_lshlrev_b32_sdwa v49,1,v49 dst_sel:DWORD dst_unused:UNUSED_PAD src0_sel:DWORD src1_sel:WORD_1
v_mfma_f32_16x16x32_bf16 v[68:71],a[252:255],v[60:63],v[68:71]
ds_read_u16_d16_hi v50,v50
v_lshlrev_b32_sdwa v51,1,v51 dst_sel:DWORD dst_unused:UNUSED_PAD src0_sel:DWORD src1_sel:WORD_1
v_mfma_f32_16x16x32_bf16 v[64:67],a[252:255],v[32:35],v[64:67]
ds_read_u16 v49,v49
v_lshlrev_b32_sdwa v44,1,v44 dst_sel:DWORD dst_unused:UNUSED_PAD src0_sel:DWORD src1_sel:WORD_1
v_mfma_f32_16x16x32_bf16 v[144:147],a[240:243],v[32:35],v[144:147]
ds_read_u16_d16_hi v51,v51
v_lshlrev_b32_sdwa v46,1,v46 dst_sel:DWORD dst_unused:UNUSED_PAD src0_sel:DWORD src1_sel:WORD_1
v_mfma_f32_16x16x32_bf16 v[148:151],a[240:243],v[60:63],v[148:151]
s_waitcnt lgkmcnt(4)
ds_read_u16 v44,v44
v_or3_b32 v13,v14,v15,v13
v_mfma_f32_16x16x32_bf16 v[84:87],a[224:227],v[60:63],v[84:87]
ds_read_u16_d16_hi v46,v46
v_lshlrev_b32_sdwa v45,1,v45 dst_sel:DWORD dst_unused:UNUSED_PAD src0_sel:DWORD src1_sel:WORD_1
v_mfma_f32_16x16x32_bf16 v[80:83],a[224:227],v[32:35],v[80:83]
ds_read_u16 v45,v45
v_lshlrev_b32_sdwa v47,1,v47 dst_sel:DWORD dst_unused:UNUSED_PAD src0_sel:DWORD src1_sel:WORD_1
v_mfma_f32_16x16x32_bf16 v[152:155],a[228:231],v[32:35],v[152:155]
ds_read_u16_d16_hi v47,v47
v_mfma_f32_16x16x32_bf16 v[156:159],a[228:231],v[60:63],v[156:159]
v_mfma_f32_16x16x32_bf16 v[100:103],a[232:235],v[60:63],v[100:103]
v_mfma_f32_16x16x32_bf16 v[96:99],a[232:235],v[32:35],v[96:99]
s_add_u32 m0,s57,60120
v_mfma_f32_16x16x32_bf16 v[60:63],a[204:207],a[56:59],v[20:23]
s_add_u32 s89,s56,4128768
v_lshlrev_b32_sdwa v28,1,v28 dst_sel:DWORD dst_unused:UNUSED_PAD src0_sel:DWORD src1_sel:WORD_1
v_fma_mixlo_f16 v8,v53,s99,0
v_mfma_f32_16x16x32_bf16 v[60:63],a[208:211],a[72:75],v[60:63]
buffer_load_dwordx4 v6,s[36:39],s89 offen lds
v_lshl_add_u64 v[40:41],v[40:41],0,v[42:43]
v_mfma_f32_16x16x32_bf16 v[60:63],a[212:215],a[88:91],v[60:63]
ds_read_u16 v28,v28
v_lshl_add_u64 v[42:43],v[36:37],0,v[38:39]
v_lshlrev_b32_sdwa v30,1,v30 dst_sel:DWORD dst_unused:UNUSED_PAD src0_sel:DWORD src1_sel:WORD_1
v_mfma_f32_16x16x32_bf16 v[32:35],a[200:203],a[88:91],v[16:19]
ds_read_u16_d16_hi v30,v30
v_fma_mixhi_f16 v8,v55,s99,0
v_mfma_f32_16x16x32_bf16 v[32:35],a[196:199],a[72:75],v[32:35]
s_waitcnt lgkmcnt(3)
v_lshl_add_u64 v[36:37],v[48:49],0,v[50:51]
v_lshlrev_b32_sdwa v29,1,v29 dst_sel:DWORD dst_unused:UNUSED_PAD src0_sel:DWORD src1_sel:WORD_1
v_pk_fma_f16 v9,v8,v8,s54
v_mfma_f32_16x16x32_bf16 v[32:35],a[192:195],a[56:59],v[32:35]
ds_read_u16 v29,v29
v_lshlrev_b32_sdwa v31,1,v31 dst_sel:DWORD dst_unused:UNUSED_PAD src0_sel:DWORD src1_sel:WORD_1
v_mfma_f32_16x16x32_bf16 v[48:51],a[192:195],a[60:63],v[16:19]
ds_read_b128 v[16:19],v4 offset:768
ds_read_b64_tr_b16 a[192:193],v1 offset:10256
v_pk_mul_f16 v9,v8,v9
v_mfma_f32_16x16x32_bf16 v[48:51],a[196:199],a[76:79],v[48:51]
s_waitcnt lgkmcnt(5)
ds_read_b64_tr_b16 a[194:195],v1 offset:11280
ds_read_b64_tr_b16 a[196:197],v2 offset:10256
v_lshl_add_u64 v[38:39],v[44:45],0,v[46:47]
v_lshlrev_b32_sdwa v24,1,v24 dst_sel:DWORD dst_unused:UNUSED_PAD src0_sel:DWORD src1_sel:WORD_1
v_mfma_f32_16x16x32_bf16 v[48:51],a[200:203],a[92:95],v[48:51]
ds_read_b64_tr_b16 a[198:199],v2 offset:11280
ds_read_b64_tr_b16 a[200:201],v2 offset:8208
v_exp_f16 v8,v9
v_mfma_f32_16x16x32_bf16 v[44:47],a[212:215],a[92:95],v[20:23]
ds_read_b128 v[20:23],v4 offset:800
ds_read_b64_tr_b16 a[202:203],v2 offset:9232
v_lshlrev_b32_sdwa v26,1,v26 dst_sel:DWORD dst_unused:UNUSED_PAD src0_sel:DWORD src1_sel:WORD_1
v_mfma_f32_16x16x32_bf16 v[44:47],a[208:211],a[76:79],v[44:47]
ds_read_b64_tr_b16 a[212:213],v2 offset:6160
ds_read_b64_tr_b16 a[214:215],v2 offset:7184
v_lshlrev_b32_sdwa v25,1,v25 dst_sel:DWORD dst_unused:UNUSED_PAD src0_sel:DWORD src1_sel:WORD_1
v_mfma_f32_16x16x32_bf16 v[44:47],a[204:207],a[60:63],v[44:47]
ds_read_u16_d16_hi v31,v31
v_exp_f16_sdwa v8,v9 dst_sel:WORD_1 dst_unused:UNUSED_PRESERVE src0_sel:WORD_1
v_mfma_f32_16x16x32_bf16 v[112:115],a[248:251],v[40:43],v[112:115]
ds_read_u16 v24,v24
v_lshlrev_b32_sdwa v27,1,v27 dst_sel:DWORD dst_unused:UNUSED_PAD src0_sel:DWORD src1_sel:WORD_1
v_mfma_f32_16x16x32_bf16 v[116:119],a[248:251],v[36:39],v[116:119]
ds_read_u16_d16_hi v26,v26
v_lshlrev_b32_sdwa v56,1,v56 dst_sel:DWORD dst_unused:UNUSED_PAD src0_sel:DWORD src1_sel:WORD_1
v_mfma_f32_16x16x32_bf16 v[76:79],a[252:255],v[36:39],v[76:79]
ds_read_u16 v25,v25
v_pk_fma_f16 v8,v8,s55,s55
v_mfma_f32_16x16x32_bf16 v[72:75],a[252:255],v[40:43],v[72:75]
v_rcp_f16 v9,v8
v_lshlrev_b32_sdwa v58,1,v58 dst_sel:DWORD dst_unused:UNUSED_PAD src0_sel:DWORD src1_sel:WORD_1
v_mfma_f32_16x16x32_bf16 v[120:123],a[240:243],v[40:43],v[120:123]
ds_read_u16_d16_hi v27,v27
v_lshlrev_b32_sdwa v57,1,v57 dst_sel:DWORD dst_unused:UNUSED_PAD src0_sel:DWORD src1_sel:WORD_1
v_mfma_f32_16x16x32_bf16 v[124:127],a[240:243],v[36:39],v[124:127]
ds_read_u16 v56,v56
v_lshlrev_b32_sdwa v59,1,v59 dst_sel:DWORD dst_unused:UNUSED_PAD src0_sel:DWORD src1_sel:WORD_1
v_mfma_f32_16x16x32_bf16 v[92:95],a[224:227],v[36:39],v[92:95]
ds_read_u16_d16_hi v58,v58
v_lshlrev_b32_sdwa v52,1,v52 dst_sel:DWORD dst_unused:UNUSED_PAD src0_sel:DWORD src1_sel:WORD_1
v_mfma_f32_16x16x32_bf16 v[88:91],a[224:227],v[40:43],v[88:91]
v_rcp_f16_sdwa v9,v8 dst_sel:WORD_1 dst_unused:UNUSED_PRESERVE src0_sel:WORD_1
v_mfma_f32_16x16x32_bf16 v[128:131],a[228:231],v[40:43],v[128:131]
ds_read_u16 v57,v57
ds_read_u16_d16_hi v59,v59
v_mfma_f32_16x16x32_bf16 v[132:135],a[228:231],v[36:39],v[132:135]
ds_read_u16 v52,v52
v_lshlrev_b32_sdwa v54,1,v54 dst_sel:DWORD dst_unused:UNUSED_PAD src0_sel:DWORD src1_sel:WORD_1
v_mfma_f32_16x16x32_bf16 v[108:111],a[232:235],v[36:39],v[108:111]
ds_read_u16_d16_hi v54,v54
v_fma_mix_f32 v53,v53,v9,0 op_sel_hi:[0,1,0]
v_mfma_f32_16x16x32_bf16 v[104:107],a[232:235],v[40:43],v[104:107]
s_waitcnt lgkmcnt(7)
v_fma_mix_f32 v55,v55,v9,0 op_sel:[0,1,0] op_sel_hi:[0,1,0]
v_mfma_f32_16x16x32_bf16 v[36:39],a[216:219],a[0:3],v[16:19]
s_add_u32 m0,s57,63656
v_lshlrev_b32_sdwa v60,1,v60 dst_sel:DWORD dst_unused:UNUSED_PAD src0_sel:DWORD src1_sel:WORD_1
v_lshlrev_b32_sdwa v62,1,v62 dst_sel:DWORD dst_unused:UNUSED_PAD src0_sel:DWORD src1_sel:WORD_1
v_mfma_f32_16x16x32_bf16 v[36:39],a[220:223],a[16:19],v[36:39]
s_add_u32 s89,s56,4227072
ds_read_u16 v60,v60
v_lshl_add_u64 v[28:29],v[28:29],0,v[30:31]
v_mfma_f32_16x16x32_bf16 v[36:39],a[192:195],a[32:35],v[36:39]
s_waitcnt lgkmcnt(6)
buffer_load_dwordx4 v6,s[36:39],s89 offen lds
v_lshl_add_u64 v[30:31],v[24:25],0,v[26:27]
v_mfma_f32_16x16x32_bf16 v[40:43],a[196:199],a[32:35],v[20:23]
ds_read_u16_d16_hi v62,v62
v_lshlrev_b32_sdwa v61,1,v61 dst_sel:DWORD dst_unused:UNUSED_PAD src0_sel:DWORD src1_sel:WORD_1
v_mfma_f32_16x16x32_bf16 v[40:43],a[200:203],a[16:19],v[40:43]
s_waitcnt lgkmcnt(4)
ds_read_u16 v61,v61
v_lshl_add_u64 v[24:25],v[56:57],0,v[58:59]
v_mfma_f32_16x16x32_bf16 v[40:43],a[212:215],a[0:3],v[40:43]
v_lshlrev_b32_sdwa v63,1,v63 dst_sel:DWORD dst_unused:UNUSED_PAD src0_sel:DWORD src1_sel:WORD_1
v_lshlrev_b32_sdwa v32,1,v32 dst_sel:DWORD dst_unused:UNUSED_PAD src0_sel:DWORD src1_sel:WORD_1
v_mfma_f32_16x16x32_bf16 v[56:59],a[212:215],a[4:7],v[20:23]
s_waitcnt lgkmcnt(3)
ds_read_u16_d16_hi v63,v63
v_or_b32 v26,v52,v54
v_mfma_f32_16x16x32_bf16 v[56:59],a[200:203],a[20:23],v[56:59]
ds_read_u16 v32,v32
v_cvt_pk_bf16_f32 v27,v53,v55
v_lshlrev_b32_sdwa v34,1,v34 dst_sel:DWORD dst_unused:UNUSED_PAD src0_sel:DWORD src1_sel:WORD_1
v_mfma_f32_16x16x32_bf16 v[56:59],a[196:199],a[36:39],v[56:59]
ds_read_u16_d16_hi v34,v34
v_lshlrev_b32_sdwa v33,1,v33 dst_sel:DWORD dst_unused:UNUSED_PAD src0_sel:DWORD src1_sel:WORD_1
v_mfma_f32_16x16x32_bf16 v[52:55],a[192:195],a[36:39],v[16:19]
ds_read_u16 v33,v33
v_lshlrev_b32_sdwa v35,1,v35 dst_sel:DWORD dst_unused:UNUSED_PAD src0_sel:DWORD src1_sel:WORD_1
v_mfma_f32_16x16x32_bf16 v[52:55],a[220:223],a[20:23],v[52:55]
ds_read_u16_d16_hi v35,v35
v_lshlrev_b32_sdwa v44,1,v44 dst_sel:DWORD dst_unused:UNUSED_PAD src0_sel:DWORD src1_sel:WORD_1
v_mfma_f32_16x16x32_bf16 v[52:55],a[216:219],a[4:7],v[52:55]
ds_read_u16 v44,v44
v_lshlrev_b32_sdwa v46,1,v46 dst_sel:DWORD dst_unused:UNUSED_PAD src0_sel:DWORD src1_sel:WORD_1
v_mfma_f32_16x16x32_bf16 v[232:235],a[248:251],v[28:31],v[232:235]
ds_read_u16_d16_hi v46,v46
v_lshlrev_b32_sdwa v45,1,v45 dst_sel:DWORD dst_unused:UNUSED_PAD src0_sel:DWORD src1_sel:WORD_1
v_mfma_f32_16x16x32_bf16 v[236:239],a[248:251],v[24:27],v[236:239]
ds_read_u16 v45,v45
v_lshlrev_b32_sdwa v47,1,v47 dst_sel:DWORD dst_unused:UNUSED_PAD src0_sel:DWORD src1_sel:WORD_1
v_mfma_f32_16x16x32_bf16 v[164:167],a[252:255],v[24:27],v[164:167]
ds_read_u16_d16_hi v47,v47
ds_read_b64_tr_b16 a[236:237],v3 offset:12320
v_mfma_f32_16x16x32_bf16 v[160:163],a[252:255],v[28:31],v[160:163]
ds_read_b64_tr_b16 a[238:239],v3 offset:13856
v_lshlrev_b32_sdwa v48,1,v48 dst_sel:DWORD dst_unused:UNUSED_PAD src0_sel:DWORD src1_sel:WORD_1
v_mfma_f32_16x16x32_bf16 v[240:243],a[240:243],v[28:31],v[240:243]
ds_read_u16 v48,v48
v_lshlrev_b32_sdwa v50,1,v50 dst_sel:DWORD dst_unused:UNUSED_PAD src0_sel:DWORD src1_sel:WORD_1
v_mfma_f32_16x16x32_bf16 v[244:247],a[240:243],v[24:27],v[244:247]
ds_read_u16_d16_hi v50,v50
v_lshlrev_b32_sdwa v49,1,v49 dst_sel:DWORD dst_unused:UNUSED_PAD src0_sel:DWORD src1_sel:WORD_1
v_mfma_f32_16x16x32_bf16 v[180:183],a[224:227],v[24:27],v[180:183]
s_add_u32 m0,s57,67184
ds_read_u16 v49,v49
v_lshlrev_b32_sdwa v51,1,v51 dst_sel:DWORD dst_unused:UNUSED_PAD src0_sel:DWORD src1_sel:WORD_1
v_mfma_f32_16x16x32_bf16 v[176:179],a[224:227],v[28:31],v[176:179]
s_add_u32 s89,s56,4325376
ds_read_u16_d16_hi v51,v51
v_mfma_f32_16x16x32_bf16 v[248:251],a[228:231],v[28:31],v[248:251]
buffer_load_dwordx4 v6,s[36:39],s89 offen lds
v_mfma_f32_16x16x32_bf16 v[252:255],a[228:231],v[24:27],v[252:255]
ds_read_b64_tr_b16 a[244:245],v3 offset:12328
v_mfma_f32_16x16x32_bf16 v[196:199],a[232:235],v[24:27],v[196:199]
ds_read_b64_tr_b16 a[246:247],v3 offset:13864
v_mfma_f32_16x16x32_bf16 v[192:195],a[232:235],v[28:31],v[192:195]
v_mfma_f32_16x16x32_bf16 v[24:27],a[216:219],a[8:11],v[16:19]
ds_read_b64_tr_b16 a[208:209],v1 offset:12320
v_lshlrev_b32_sdwa v36,1,v36 dst_sel:DWORD dst_unused:UNUSED_PAD src0_sel:DWORD src1_sel:WORD_1
v_mfma_f32_16x16x32_bf16 v[24:27],a[220:223],a[24:27],v[24:27]
s_waitcnt lgkmcnt(6)
ds_read_u16 v36,v36
v_lshl_add_u64 v[60:61],v[60:61],0,v[62:63]
v_fma_mixlo_f16 v8,v57,s99,0
v_mfma_f32_16x16x32_bf16 v[24:27],a[192:195],a[40:43],v[24:27]
ds_read_b64_tr_b16 a[210:211],v1 offset:13344
v_lshl_add_u64 v[62:63],v[32:33],0,v[34:35]
v_lshlrev_b32_sdwa v38,1,v38 dst_sel:DWORD dst_unused:UNUSED_PAD src0_sel:DWORD src1_sel:WORD_1
v_mfma_f32_16x16x32_bf16 v[28:31],a[196:199],a[40:43],v[20:23]
ds_read_u16_d16_hi v38,v38
v_fma_mixhi_f16 v8,v59,s99,0
v_mfma_f32_16x16x32_bf16 v[28:31],a[200:203],a[24:27],v[28:31]
ds_read_b64_tr_b16 a[204:205],v1 offset:14368
v_lshl_add_u64 v[32:33],v[44:45],0,v[46:47]
v_lshlrev_b32_sdwa v37,1,v37 dst_sel:DWORD dst_unused:UNUSED_PAD src0_sel:DWORD src1_sel:WORD_1
v_pk_fma_f16 v9,v8,v8,s54
v_mfma_f32_16x16x32_bf16 v[28:31],a[212:215],a[8:11],v[28:31]
ds_read_u16 v37,v37
v_lshlrev_b32_sdwa v39,1,v39 dst_sel:DWORD dst_unused:UNUSED_PAD src0_sel:DWORD src1_sel:WORD_1
v_mfma_f32_16x16x32_bf16 v[44:47],a[212:215],a[12:15],v[20:23]
ds_read_u16_d16_hi v39,v39
v_pk_mul_f16 v9,v8,v9
v_mfma_f32_16x16x32_bf16 v[44:47],a[200:203],a[28:31],v[44:47]
s_waitcnt lgkmcnt(7)
ds_read_b64_tr_b16 a[206:207],v1 offset:15392
v_lshl_add_u64 v[34:35],v[48:49],0,v[50:51]
v_lshlrev_b32_sdwa v40,1,v40 dst_sel:DWORD dst_unused:UNUSED_PAD src0_sel:DWORD src1_sel:WORD_1
v_mfma_f32_16x16x32_bf16 v[44:47],a[196:199],a[44:47],v[44:47]
v_exp_f16 v8,v9
v_mfma_f32_16x16x32_bf16 v[48:51],a[192:195],a[44:47],v[16:19]
ds_read_u16 v40,v40
v_lshlrev_b32_sdwa v42,1,v42 dst_sel:DWORD dst_unused:UNUSED_PAD src0_sel:DWORD src1_sel:WORD_1
v_mfma_f32_16x16x32_bf16 v[48:51],a[220:223],a[28:31],v[48:51]
ds_read_u16_d16_hi v42,v42
v_lshlrev_b32_sdwa v41,1,v41 dst_sel:DWORD dst_unused:UNUSED_PAD src0_sel:DWORD src1_sel:WORD_1
v_mfma_f32_16x16x32_bf16 v[48:51],a[216:219],a[12:15],v[48:51]
v_exp_f16_sdwa v8,v9 dst_sel:WORD_1 dst_unused:UNUSED_PRESERVE src0_sel:WORD_1
v_mfma_f32_16x16x32_bf16 v[208:211],a[248:251],v[60:63],v[208:211]
ds_read_u16 v41,v41
v_lshlrev_b32_sdwa v43,1,v43 dst_sel:DWORD dst_unused:UNUSED_PAD src0_sel:DWORD src1_sel:WORD_1
v_mfma_f32_16x16x32_bf16 v[212:215],a[248:251],v[32:35],v[212:215]
ds_read_u16_d16_hi v43,v43
v_lshlrev_b32_sdwa v52,1,v52 dst_sel:DWORD dst_unused:UNUSED_PAD src0_sel:DWORD src1_sel:WORD_1
v_mfma_f32_16x16x32_bf16 v[172:175],a[252:255],v[32:35],v[172:175]
ds_read_u16 v52,v52
v_pk_fma_f16 v8,v8,s55,s55
v_mfma_f32_16x16x32_bf16 v[168:171],a[252:255],v[60:63],v[168:171]
ds_read_b64_tr_b16 a[248:249],v3 offset:12384
v_rcp_f16 v9,v8
v_lshlrev_b32_sdwa v54,1,v54 dst_sel:DWORD dst_unused:UNUSED_PAD src0_sel:DWORD src1_sel:WORD_1
v_mfma_f32_16x16x32_bf16 v[216:219],a[240:243],v[60:63],v[216:219]
ds_read_u16_d16_hi v54,v54
v_lshlrev_b32_sdwa v53,1,v53 dst_sel:DWORD dst_unused:UNUSED_PAD src0_sel:DWORD src1_sel:WORD_1
v_mfma_f32_16x16x32_bf16 v[220:223],a[240:243],v[32:35],v[220:223]
ds_read_u16 v53,v53
ds_read_b64_tr_b16 a[250:251],v3 offset:13920
v_mfma_f32_16x16x32_bf16 v[188:191],a[224:227],v[32:35],v[188:191]
ds_read_b64_tr_b16 a[252:253],v3 offset:12392
v_lshlrev_b32_sdwa v55,1,v55 dst_sel:DWORD dst_unused:UNUSED_PAD src0_sel:DWORD src1_sel:WORD_1
v_mfma_f32_16x16x32_bf16 v[184:187],a[224:227],v[60:63],v[184:187]
ds_read_u16_d16_hi v55,v55
v_rcp_f16_sdwa v9,v8 dst_sel:WORD_1 dst_unused:UNUSED_PRESERVE src0_sel:WORD_1
v_mfma_f32_16x16x32_bf16 v[224:227],a[228:231],v[60:63],v[224:227]
ds_read_b64_tr_b16 a[254:255],v3 offset:13928
ds_read_b64_tr_b16 a[240:241],v3 offset:12448
v_lshlrev_b32_sdwa v56,1,v56 dst_sel:DWORD dst_unused:UNUSED_PAD src0_sel:DWORD src1_sel:WORD_1
v_mfma_f32_16x16x32_bf16 v[228:231],a[228:231],v[32:35],v[228:231]
ds_read_u16 v56,v56
v_lshlrev_b32_sdwa v58,1,v58 dst_sel:DWORD dst_unused:UNUSED_PAD src0_sel:DWORD src1_sel:WORD_1
v_mfma_f32_16x16x32_bf16 v[204:207],a[232:235],v[32:35],v[204:207]
ds_read_u16_d16_hi v58,v58
v_fma_mix_f32 v57,v57,v9,0 op_sel_hi:[0,1,0]
v_mfma_f32_16x16x32_bf16 v[200:203],a[232:235],v[60:63],v[200:203]
ds_read_b64_tr_b16 a[242:243],v3 offset:13984
v_fma_mix_f32 v59,v59,v9,0 op_sel:[0,1,0] op_sel_hi:[0,1,0]
v_mfma_f32_16x16x32_bf16 v[32:35],a[216:219],a[48:51],v[16:19]
s_add_u32 m0,s57,70720
ds_read_b64 v[14:15],v7 offset:12320
v_lshlrev_b32_sdwa v24,1,v24 dst_sel:DWORD dst_unused:UNUSED_PAD src0_sel:DWORD src1_sel:WORD_1
v_mfma_f32_16x16x32_bf16 v[32:35],a[220:223],a[64:67],v[32:35]
s_waitcnt lgkmcnt(10)
ds_read_b64_tr_b16 a[224:225],v3 offset:12456
v_lshl_add_u64 v[36:37],v[36:37],0,v[38:39]
v_mfma_f32_16x16x32_bf16 v[32:35],a[192:195],a[80:83],v[32:35]
s_add_u32 s89,s56,4423680
ds_read_b64_tr_b16 a[226:227],v3 offset:13992
v_lshl_add_u64 v[38:39],v[40:41],0,v[42:43]
v_lshlrev_b32_sdwa v26,1,v26 dst_sel:DWORD dst_unused:UNUSED_PAD src0_sel:DWORD src1_sel:WORD_1
v_mfma_f32_16x16x32_bf16 v[60:63],a[196:199],a[80:83],v[20:23]
buffer_load_dwordx4 v6,s[36:39],s89 offen lds
v_lshlrev_b32_sdwa v25,1,v25 dst_sel:DWORD dst_unused:UNUSED_PAD src0_sel:DWORD src1_sel:WORD_1
v_mfma_f32_16x16x32_bf16 v[60:63],a[200:203],a[64:67],v[60:63]
s_waitcnt lgkmcnt(8)
ds_read_u16 v24,v24
v_lshl_add_u64 v[40:41],v[52:53],0,v[54:55]
v_mfma_f32_16x16x32_bf16 v[60:63],a[212:215],a[48:51],v[60:63]
s_waitcnt lgkmcnt(3)
ds_read_u16_d16_hi v26,v26
v_or3_b32 v13,v14,v15,v13
v_lshlrev_b32_sdwa v27,1,v27 dst_sel:DWORD dst_unused:UNUSED_PAD src0_sel:DWORD src1_sel:WORD_1
v_mfma_f32_16x16x32_bf16 v[52:55],a[212:215],a[52:55],v[20:23]
ds_read_b64 v[14:15],v7 offset:12832
v_or_b32 v42,v56,v58
v_lshlrev_b32_sdwa v28,1,v28 dst_sel:DWORD dst_unused:UNUSED_PAD src0_sel:DWORD src1_sel:WORD_1
v_mfma_f32_16x16x32_bf16 v[52:55],a[200:203],a[68:71],v[52:55]
ds_read_u16 v25,v25
v_cvt_pk_bf16_f32 v43,v57,v59
v_lshlrev_b32_sdwa v30,1,v30 dst_sel:DWORD dst_unused:UNUSED_PAD src0_sel:DWORD src1_sel:WORD_1
v_mfma_f32_16x16x32_bf16 v[52:55],a[196:199],a[84:87],v[52:55]
ds_read_u16_d16_hi v27,v27
v_lshlrev_b32_sdwa v29,1,v29 dst_sel:DWORD dst_unused:UNUSED_PAD src0_sel:DWORD src1_sel:WORD_1
v_mfma_f32_16x16x32_bf16 v[56:59],a[192:195],a[84:87],v[16:19]
ds_read_u16 v28,v28
v_lshlrev_b32_sdwa v31,1,v31 dst_sel:DWORD dst_unused:UNUSED_PAD src0_sel:DWORD src1_sel:WORD_1
v_mfma_f32_16x16x32_bf16 v[56:59],a[220:223],a[68:71],v[56:59]
ds_read_u16_d16_hi v30,v30
v_lshlrev_b32_sdwa v48,1,v48 dst_sel:DWORD dst_unused:UNUSED_PAD src0_sel:DWORD src1_sel:WORD_1
v_mfma_f32_16x16x32_bf16 v[56:59],a[216:219],a[52:55],v[56:59]
s_waitcnt lgkmcnt(4)
ds_read_u16 v29,v29
v_or3_b32 v13,v14,v15,v13
v_mfma_f32_16x16x32_bf16 v[136:139],a[236:239],v[36:39],v[136:139]
ds_read_b64 v[14:15],v7 offset:13344
v_lshlrev_b32_sdwa v50,1,v50 dst_sel:DWORD dst_unused:UNUSED_PAD src0_sel:DWORD src1_sel:WORD_1
v_mfma_f32_16x16x32_bf16 v[140:143],a[236:239],v[40:43],v[140:143]
s_add_u32 m0,s57,74264
ds_read_u16_d16_hi v31,v31
v_lshlrev_b32_sdwa v49,1,v49 dst_sel:DWORD dst_unused:UNUSED_PAD src0_sel:DWORD src1_sel:WORD_1
v_mfma_f32_16x16x32_bf16 v[68:71],a[244:247],v[40:43],v[68:71]
s_add_u32 s89,s56,4521984
ds_read_u16 v48,v48
v_lshlrev_b32_sdwa v51,1,v51 dst_sel:DWORD dst_unused:UNUSED_PAD src0_sel:DWORD src1_sel:WORD_1
v_mfma_f32_16x16x32_bf16 v[64:67],a[244:247],v[36:39],v[64:67]
buffer_load_dwordx4 v6,s[36:39],s89 offen lds
v_lshlrev_b32_sdwa v44,1,v44 dst_sel:DWORD dst_unused:UNUSED_PAD src0_sel:DWORD src1_sel:WORD_1
v_mfma_f32_16x16x32_bf16 v[144:147],a[248:251],v[36:39],v[144:147]
ds_read_u16_d16_hi v50,v50
v_lshlrev_b32_sdwa v46,1,v46 dst_sel:DWORD dst_unused:UNUSED_PAD src0_sel:DWORD src1_sel:WORD_1
v_mfma_f32_16x16x32_bf16 v[148:151],a[248:251],v[40:43],v[148:151]
s_waitcnt lgkmcnt(3)
ds_read_u16 v49,v49
v_or3_b32 v13,v14,v15,v13
v_mfma_f32_16x16x32_bf16 v[84:87],a[252:255],v[40:43],v[84:87]
ds_read_u16_d16_hi v51,v51
v_lshlrev_b32_sdwa v45,1,v45 dst_sel:DWORD dst_unused:UNUSED_PAD src0_sel:DWORD src1_sel:WORD_1
v_mfma_f32_16x16x32_bf16 v[80:83],a[252:255],v[36:39],v[80:83]
ds_read_u16 v44,v44
v_lshlrev_b32_sdwa v47,1,v47 dst_sel:DWORD dst_unused:UNUSED_PAD src0_sel:DWORD src1_sel:WORD_1
v_mfma_f32_16x16x32_bf16 v[152:155],a[240:243],v[36:39],v[152:155]
ds_read_u16_d16_hi v46,v46
v_mfma_f32_16x16x32_bf16 v[156:159],a[240:243],v[40:43],v[156:159]
ds_read_u16 v45,v45
v_mfma_f32_16x16x32_bf16 v[100:103],a[224:227],v[40:43],v[100:103]
ds_read_u16_d16_hi v47,v47
v_mfma_f32_16x16x32_bf16 v[96:99],a[224:227],v[36:39],v[96:99]
v_mfma_f32_16x16x32_bf16 v[40:43],a[216:219],a[56:59],v[16:19]
v_lshlrev_b32_sdwa v32,1,v32 dst_sel:DWORD dst_unused:UNUSED_PAD src0_sel:DWORD src1_sel:WORD_1
v_fma_mixlo_f16 v8,v53,s99,0
v_mfma_f32_16x16x32_bf16 v[40:43],a[220:223],a[72:75],v[40:43]
ds_read_u16 v32,v32
v_lshl_add_u64 v[24:25],v[24:25],0,v[26:27]
v_mfma_f32_16x16x32_bf16 v[40:43],a[192:195],a[88:91],v[40:43]
s_waitcnt lgkmcnt(6)
v_lshl_add_u64 v[26:27],v[28:29],0,v[30:31]
v_lshlrev_b32_sdwa v34,1,v34 dst_sel:DWORD dst_unused:UNUSED_PAD src0_sel:DWORD src1_sel:WORD_1
v_mfma_f32_16x16x32_bf16 v[36:39],a[196:199],a[88:91],v[20:23]
ds_read_u16_d16_hi v34,v34
v_fma_mixhi_f16 v8,v55,s99,0
v_mfma_f32_16x16x32_bf16 v[36:39],a[200:203],a[72:75],v[36:39]
s_waitcnt lgkmcnt(5)
v_lshl_add_u64 v[28:29],v[48:49],0,v[50:51]
v_lshlrev_b32_sdwa v33,1,v33 dst_sel:DWORD dst_unused:UNUSED_PAD src0_sel:DWORD src1_sel:WORD_1
v_pk_fma_f16 v9,v8,v8,s54
v_mfma_f32_16x16x32_bf16 v[36:39],a[212:215],a[56:59],v[36:39]
ds_read_u16 v33,v33
v_lshlrev_b32_sdwa v35,1,v35 dst_sel:DWORD dst_unused:UNUSED_PAD src0_sel:DWORD src1_sel:WORD_1
v_mfma_f32_16x16x32_bf16 v[48:51],a[212:215],a[60:63],v[20:23]
ds_read_b128 v[20:23],v4 offset:896
v_pk_mul_f16 v9,v8,v9
v_mfma_f32_16x16x32_bf16 v[48:51],a[200:203],a[76:79],v[48:51]
s_waitcnt lgkmcnt(4)
ds_read_u16_d16_hi v35,v35
ds_read_b64_tr_b16 a[212:213],v1 offset:16416
v_lshl_add_u64 v[30:31],v[44:45],0,v[46:47]
v_lshlrev_b32_sdwa v60,1,v60 dst_sel:DWORD dst_unused:UNUSED_PAD src0_sel:DWORD src1_sel:WORD_1
v_mfma_f32_16x16x32_bf16 v[48:51],a[196:199],a[92:95],v[48:51]
ds_read_b64_tr_b16 a[214:215],v1 offset:17440
v_exp_f16 v8,v9
v_mfma_f32_16x16x32_bf16 v[44:47],a[192:195],a[92:95],v[16:19]
ds_read_b128 v[16:19],v4 offset:928
ds_read_u16 v60,v60
v_lshlrev_b32_sdwa v62,1,v62 dst_sel:DWORD dst_unused:UNUSED_PAD src0_sel:DWORD src1_sel:WORD_1
v_mfma_f32_16x16x32_bf16 v[44:47],a[220:223],a[76:79],v[44:47]
ds_read_u16_d16_hi v62,v62
ds_read_b64_tr_b16 a[200:201],v2 offset:16416
v_lshlrev_b32_sdwa v61,1,v61 dst_sel:DWORD dst_unused:UNUSED_PAD src0_sel:DWORD src1_sel:WORD_1
v_mfma_f32_16x16x32_bf16 v[44:47],a[216:219],a[60:63],v[44:47]
ds_read_u16 v61,v61
v_exp_f16_sdwa v8,v9 dst_sel:WORD_1 dst_unused:UNUSED_PRESERVE src0_sel:WORD_1
v_mfma_f32_16x16x32_bf16 v[112:115],a[236:239],v[24:27],v[112:115]
ds_read_b64_tr_b16 a[202:203],v2 offset:17440
ds_read_b64_tr_b16 a[196:197],v2 offset:14368
v_lshlrev_b32_sdwa v63,1,v63 dst_sel:DWORD dst_unused:UNUSED_PAD src0_sel:DWORD src1_sel:WORD_1
v_mfma_f32_16x16x32_bf16 v[116:119],a[236:239],v[28:31],v[116:119]
ds_read_u16_d16_hi v63,v63
v_lshlrev_b32_sdwa v56,1,v56 dst_sel:DWORD dst_unused:UNUSED_PAD src0_sel:DWORD src1_sel:WORD_1
v_mfma_f32_16x16x32_bf16 v[76:79],a[244:247],v[28:31],v[76:79]
ds_read_b64_tr_b16 a[198:199],v2 offset:15392
v_pk_fma_f16 v8,v8,s55,s55
v_mfma_f32_16x16x32_bf16 v[72:75],a[244:247],v[24:27],v[72:75]
v_rcp_f16 v9,v8
v_lshlrev_b32_sdwa v58,1,v58 dst_sel:DWORD dst_unused:UNUSED_PAD src0_sel:DWORD src1_sel:WORD_1
v_mfma_f32_16x16x32_bf16 v[120:123],a[248:251],v[24:27],v[120:123]
ds_read_u16 v56,v56
v_lshlrev_b32_sdwa v57,1,v57 dst_sel:DWORD dst_unused:UNUSED_PAD src0_sel:DWORD src1_sel:WORD_1
v_mfma_f32_16x16x32_bf16 v[124:127],a[248:251],v[28:31],v[124:127]
ds_read_u16_d16_hi v58,v58
v_lshlrev_b32_sdwa v59,1,v59 dst_sel:DWORD dst_unused:UNUSED_PAD src0_sel:DWORD src1_sel:WORD_1
v_mfma_f32_16x16x32_bf16 v[92:95],a[252:255],v[28:31],v[92:95]
ds_read_u16 v57,v57
v_lshlrev_b32_sdwa v52,1,v52 dst_sel:DWORD dst_unused:UNUSED_PAD src0_sel:DWORD src1_sel:WORD_1
v_mfma_f32_16x16x32_bf16 v[88:91],a[252:255],v[24:27],v[88:91]
ds_read_u16_d16_hi v59,v59
v_rcp_f16_sdwa v9,v8 dst_sel:WORD_1 dst_unused:UNUSED_PRESERVE src0_sel:WORD_1
v_mfma_f32_16x16x32_bf16 v[128:131],a[240:243],v[24:27],v[128:131]
ds_read_b64_tr_b16 a[192:193],v2 offset:12320
v_lshlrev_b32_sdwa v54,1,v54 dst_sel:DWORD dst_unused:UNUSED_PAD src0_sel:DWORD src1_sel:WORD_1
v_mfma_f32_16x16x32_bf16 v[132:135],a[240:243],v[28:31],v[132:135]
ds_read_b64_tr_b16 a[194:195],v2 offset:13344
v_fma_mix_f32 v53,v53,v9,0 op_sel_hi:[0,1,0]
v_mfma_f32_16x16x32_bf16 v[108:111],a[224:227],v[28:31],v[108:111]
ds_read_u16 v52,v52
v_fma_mix_f32 v55,v55,v9,0 op_sel:[0,1,0] op_sel_hi:[0,1,0]
v_mfma_f32_16x16x32_bf16 v[104:107],a[224:227],v[24:27],v[104:107]
s_waitcnt lgkmcnt(7)
ds_read_u16_d16_hi v54,v54
v_mfma_f32_16x16x32_bf16 v[28:31],a[208:211],a[0:3],v[20:23]
s_add_u32 m0,s57,77800
v_lshlrev_b32_sdwa v40,1,v40 dst_sel:DWORD dst_unused:UNUSED_PAD src0_sel:DWORD src1_sel:WORD_1
v_lshlrev_b32_sdwa v42,1,v42 dst_sel:DWORD dst_unused:UNUSED_PAD src0_sel:DWORD src1_sel:WORD_1
v_mfma_f32_16x16x32_bf16 v[28:31],a[204:207],a[16:19],v[28:31]
s_add_u32 s89,s56,4620288
ds_read_u16 v40,v40
v_lshl_add_u64 v[32:33],v[32:33],0,v[34:35]
v_mfma_f32_16x16x32_bf16 v[28:31],a[212:215],a[32:35],v[28:31]
buffer_load_dwordx4 v6,s[36:39],s89 offen lds
v_lshl_add_u64 v[34:35],v[60:61],0,v[62:63]
v_mfma_f32_16x16x32_bf16 v[24:27],a[200:203],a[32:35],v[16:19]
ds_read_u16_d16_hi v42,v42
v_lshlrev_b32_sdwa v41,1,v41 dst_sel:DWORD dst_unused:UNUSED_PAD src0_sel:DWORD src1_sel:WORD_1
v_mfma_f32_16x16x32_bf16 v[24:27],a[196:199],a[16:19],v[24:27]
s_waitcnt lgkmcnt(4)
ds_read_u16 v41,v41
v_lshl_add_u64 v[60:61],v[56:57],0,v[58:59]
v_mfma_f32_16x16x32_bf16 v[24:27],a[192:195],a[0:3],v[24:27]
v_lshlrev_b32_sdwa v43,1,v43 dst_sel:DWORD dst_unused:UNUSED_PAD src0_sel:DWORD src1_sel:WORD_1
v_lshlrev_b32_sdwa v36,1,v36 dst_sel:DWORD dst_unused:UNUSED_PAD src0_sel:DWORD src1_sel:WORD_1
TP3281_%=:
s_cbranch_execz TP5417_%=
v_mfma_f32_16x16x32_bf16 v[56:59],a[192:195],a[4:7],v[16:19]
s_waitcnt lgkmcnt(3)
ds_read_u16_d16_hi v43,v43
v_or_b32_e64 v62,v52,v54
v_mfma_f32_16x16x32_bf16 v[56:59],a[196:199],a[20:23],v[56:59]
s_waitcnt vmcnt(7)
ds_read_u16 v36,v36
v_cvt_pk_bf16_f32 v63,v53,v55
v_lshlrev_b32_sdwa v38,1,v38 dst_sel:DWORD dst_unused:UNUSED_PAD src0_sel:DWORD src1_sel:WORD_1
v_mfma_f32_16x16x32_bf16 v[56:59],a[200:203],a[36:39],v[56:59]
s_barrier
ds_read_u16_d16_hi v38,v38
v_lshlrev_b32_sdwa v37,1,v37 dst_sel:DWORD dst_unused:UNUSED_PAD src0_sel:DWORD src1_sel:WORD_1
v_mfma_f32_16x16x32_bf16 v[52:55],a[212:215],a[36:39],v[20:23]
s_add_u32 m0,s57,81328
ds_read_u16 v37,v37
v_lshlrev_b32_sdwa v39,1,v39 dst_sel:DWORD dst_unused:UNUSED_PAD src0_sel:DWORD src1_sel:WORD_1
v_mfma_f32_16x16x32_bf16 v[52:55],a[204:207],a[20:23],v[52:55]
s_add_u32 s89,s56,4718592
ds_read_u16_d16_hi v39,v39
v_lshlrev_b32_sdwa v44,1,v44 dst_sel:DWORD dst_unused:UNUSED_PAD src0_sel:DWORD src1_sel:WORD_1
v_mfma_f32_16x16x32_bf16 v[52:55],a[208:211],a[4:7],v[52:55]
buffer_load_dwordx4 v6,s[36:39],s89 offen lds
v_lshlrev_b32_sdwa v46,1,v46 dst_sel:DWORD dst_unused:UNUSED_PAD src0_sel:DWORD src1_sel:WORD_1
v_mfma_f32_16x16x32_bf16 v[232:235],a[236:239],v[32:35],v[232:235]
s_add_u32 m0,s58,6160
ds_read_u16 v44,v44
v_lshlrev_b32_sdwa v45,1,v45 dst_sel:DWORD dst_unused:UNUSED_PAD src0_sel:DWORD src1_sel:WORD_1
v_mfma_f32_16x16x32_bf16 v[236:239],a[236:239],v[60:63],v[236:239]
s_add_u32 s69,s69,s59
ds_read_u16_d16_hi v46,v46
v_lshlrev_b32_sdwa v47,1,v47 dst_sel:DWORD dst_unused:UNUSED_PAD src0_sel:DWORD src1_sel:WORD_1
v_mfma_f32_16x16x32_bf16 v[164:167],a[244:247],v[60:63],v[164:167]
buffer_load_dwordx4 v5,s[44:47],s69 offen offset:0 lds
ds_read_u16 v45,v45
v_mfma_f32_16x16x32_bf16 v[160:163],a[244:247],v[32:35],v[160:163]
ds_read_u16_d16_hi v47,v47
ds_read_b64_tr_b16 a[228:229],v3 offset:18480
v_mfma_f32_16x16x32_bf16 v[240:243],a[248:251],v[32:35],v[240:243]
s_add_u32 s69,s69,s53
ds_read_b64_tr_b16 a[230:231],v3 offset:20016
v_lshlrev_b32_sdwa v48,1,v48 dst_sel:DWORD dst_unused:UNUSED_PAD src0_sel:DWORD src1_sel:WORD_1
v_mfma_f32_16x16x32_bf16 v[244:247],a[248:251],v[60:63],v[244:247]
buffer_load_dwordx4 v5,s[44:47],s69 offen offset:1024 lds
v_lshlrev_b32_sdwa v50,1,v50 dst_sel:DWORD dst_unused:UNUSED_PAD src0_sel:DWORD src1_sel:WORD_1
v_mfma_f32_16x16x32_bf16 v[180:183],a[252:255],v[60:63],v[180:183]
ds_read_u16 v48,v48
v_lshlrev_b32_sdwa v49,1,v49 dst_sel:DWORD dst_unused:UNUSED_PAD src0_sel:DWORD src1_sel:WORD_1
v_mfma_f32_16x16x32_bf16 v[176:179],a[252:255],v[32:35],v[176:179]
ds_read_u16_d16_hi v50,v50
v_lshlrev_b32_sdwa v51,1,v51 dst_sel:DWORD dst_unused:UNUSED_PAD src0_sel:DWORD src1_sel:WORD_1
v_mfma_f32_16x16x32_bf16 v[248:251],a[240:243],v[32:35],v[248:251]
ds_read_u16 v49,v49
v_mfma_f32_16x16x32_bf16 v[252:255],a[240:243],v[60:63],v[252:255]
s_add_u32 s69,s69,s53
ds_read_u16_d16_hi v51,v51
v_mfma_f32_16x16x32_bf16 v[196:199],a[224:227],v[60:63],v[196:199]
buffer_load_dwordx4 v5,s[44:47],s69 offen offset:2048 lds
v_mfma_f32_16x16x32_bf16 v[192:195],a[224:227],v[32:35],v[192:195]
ds_read_b64_tr_b16 a[232:233],v3 offset:18488
v_mfma_f32_16x16x32_bf16 v[60:63],a[208:211],a[8:11],v[20:23]
ds_read_b64_tr_b16 a[234:235],v3 offset:20024
v_lshlrev_b32_sdwa v28,1,v28 dst_sel:DWORD dst_unused:UNUSED_PAD src0_sel:DWORD src1_sel:WORD_1
v_mfma_f32_16x16x32_bf16 v[60:63],a[204:207],a[24:27],v[60:63]
s_waitcnt lgkmcnt(6)
ds_read_u16 v28,v28
v_lshl_add_u64 v[40:41],v[40:41],0,v[42:43]
v_fma_mixlo_f16 v8,v57,s99,0
v_mfma_f32_16x16x32_bf16 v[60:63],a[212:215],a[40:43],v[60:63]
ds_read_b64_tr_b16 a[220:221],v1 offset:18480
v_lshl_add_u64 v[42:43],v[36:37],0,v[38:39]
v_lshlrev_b32_sdwa v30,1,v30 dst_sel:DWORD dst_unused:UNUSED_PAD src0_sel:DWORD src1_sel:WORD_1
v_mfma_f32_16x16x32_bf16 v[32:35],a[200:203],a[40:43],v[16:19]
ds_read_u16_d16_hi v30,v30
v_fma_mixhi_f16 v8,v59,s99,0
v_mfma_f32_16x16x32_bf16 v[32:35],a[196:199],a[24:27],v[32:35]
ds_read_b64_tr_b16 a[222:223],v1 offset:19504
v_lshl_add_u64 v[36:37],v[44:45],0,v[46:47]
v_lshlrev_b32_sdwa v29,1,v29 dst_sel:DWORD dst_unused:UNUSED_PAD src0_sel:DWORD src1_sel:WORD_1
v_pk_fma_f16 v9,v8,v8,s54
v_mfma_f32_16x16x32_bf16 v[32:35],a[192:195],a[8:11],v[32:35]
ds_read_u16 v29,v29
v_lshlrev_b32_sdwa v31,1,v31 dst_sel:DWORD dst_unused:UNUSED_PAD src0_sel:DWORD src1_sel:WORD_1
v_mfma_f32_16x16x32_bf16 v[44:47],a[192:195],a[12:15],v[16:19]
ds_read_u16_d16_hi v31,v31
v_pk_mul_f16 v9,v8,v9
v_mfma_f32_16x16x32_bf16 v[44:47],a[196:199],a[28:31],v[44:47]
s_waitcnt lgkmcnt(8)
ds_read_b64_tr_b16 a[216:217],v1 offset:20528
v_lshl_add_u64 v[38:39],v[48:49],0,v[50:51]
v_lshlrev_b32_sdwa v24,1,v24 dst_sel:DWORD dst_unused:UNUSED_PAD src0_sel:DWORD src1_sel:WORD_1
v_mfma_f32_16x16x32_bf16 v[44:47],a[200:203],a[44:47],v[44:47]
v_exp_f16 v8,v9
v_mfma_f32_16x16x32_bf16 v[48:51],a[212:215],a[44:47],v[20:23]
ds_read_u16 v24,v24
v_lshlrev_b32_sdwa v26,1,v26 dst_sel:DWORD dst_unused:UNUSED_PAD src0_sel:DWORD src1_sel:WORD_1
v_mfma_f32_16x16x32_bf16 v[48:51],a[204:207],a[28:31],v[48:51]
ds_read_u16_d16_hi v26,v26
v_lshlrev_b32_sdwa v25,1,v25 dst_sel:DWORD dst_unused:UNUSED_PAD src0_sel:DWORD src1_sel:WORD_1
v_mfma_f32_16x16x32_bf16 v[48:51],a[208:211],a[12:15],v[48:51]
v_exp_f16_sdwa v8,v9 dst_sel:WORD_1 dst_unused:UNUSED_PRESERVE src0_sel:WORD_1
v_mfma_f32_16x16x32_bf16 v[208:211],a[236:239],v[40:43],v[208:211]
ds_read_u16 v25,v25
v_lshlrev_b32_sdwa v27,1,v27 dst_sel:DWORD dst_unused:UNUSED_PAD src0_sel:DWORD src1_sel:WORD_1
v_mfma_f32_16x16x32_bf16 v[212:215],a[236:239],v[36:39],v[212:215]
ds_read_u16_d16_hi v27,v27
v_lshlrev_b32_sdwa v52,1,v52 dst_sel:DWORD dst_unused:UNUSED_PAD src0_sel:DWORD src1_sel:WORD_1
v_mfma_f32_16x16x32_bf16 v[172:175],a[244:247],v[36:39],v[172:175]
ds_read_u16 v52,v52
v_pk_fma_f16 v8,v8,s55,s55
v_mfma_f32_16x16x32_bf16 v[168:171],a[244:247],v[40:43],v[168:171]
ds_read_b64_tr_b16 a[236:237],v3 offset:18544
v_rcp_f16 v9,v8
v_lshlrev_b32_sdwa v54,1,v54 dst_sel:DWORD dst_unused:UNUSED_PAD src0_sel:DWORD src1_sel:WORD_1
v_mfma_f32_16x16x32_bf16 v[216:219],a[248:251],v[40:43],v[216:219]
ds_read_u16_d16_hi v54,v54
v_lshlrev_b32_sdwa v53,1,v53 dst_sel:DWORD dst_unused:UNUSED_PAD src0_sel:DWORD src1_sel:WORD_1
v_mfma_f32_16x16x32_bf16 v[220:223],a[248:251],v[36:39],v[220:223]
ds_read_u16 v53,v53
ds_read_b64_tr_b16 a[238:239],v3 offset:20080
v_mfma_f32_16x16x32_bf16 v[188:191],a[252:255],v[36:39],v[188:191]
ds_read_b64_tr_b16 a[244:245],v3 offset:18552
v_lshlrev_b32_sdwa v55,1,v55 dst_sel:DWORD dst_unused:UNUSED_PAD src0_sel:DWORD src1_sel:WORD_1
v_mfma_f32_16x16x32_bf16 v[184:187],a[252:255],v[40:43],v[184:187]
ds_read_u16_d16_hi v55,v55
v_rcp_f16_sdwa v9,v8 dst_sel:WORD_1 dst_unused:UNUSED_PRESERVE src0_sel:WORD_1
v_mfma_f32_16x16x32_bf16 v[224:227],a[240:243],v[40:43],v[224:227]
ds_read_b64_tr_b16 a[246:247],v3 offset:20088
ds_read_b64_tr_b16 a[248:249],v3 offset:18608
v_lshlrev_b32_sdwa v56,1,v56 dst_sel:DWORD dst_unused:UNUSED_PAD src0_sel:DWORD src1_sel:WORD_1
v_mfma_f32_16x16x32_bf16 v[228:231],a[240:243],v[36:39],v[228:231]
ds_read_u16 v56,v56
v_lshlrev_b32_sdwa v58,1,v58 dst_sel:DWORD dst_unused:UNUSED_PAD src0_sel:DWORD src1_sel:WORD_1
v_mfma_f32_16x16x32_bf16 v[204:207],a[224:227],v[36:39],v[204:207]
ds_read_u16_d16_hi v58,v58
v_fma_mix_f32 v57,v57,v9,0 op_sel_hi:[0,1,0]
v_mfma_f32_16x16x32_bf16 v[200:203],a[224:227],v[40:43],v[200:203]
ds_read_b64_tr_b16 a[250:251],v3 offset:20144
v_fma_mix_f32 v59,v59,v9,0 op_sel:[0,1,0] op_sel_hi:[0,1,0]
v_mfma_f32_16x16x32_bf16 v[36:39],a[208:211],a[48:51],v[20:23]
s_add_u32 m0,s58,12320
ds_read_b64 v[14:15],v7 offset:18480
v_lshlrev_b32_sdwa v60,1,v60 dst_sel:DWORD dst_unused:UNUSED_PAD src0_sel:DWORD src1_sel:WORD_1
v_mfma_f32_16x16x32_bf16 v[36:39],a[204:207],a[64:67],v[36:39]
s_waitcnt lgkmcnt(10)
ds_read_b64_tr_b16 a[252:253],v3 offset:18616
v_lshl_add_u64 v[28:29],v[28:29],0,v[30:31]
v_mfma_f32_16x16x32_bf16 v[36:39],a[212:215],a[80:83],v[36:39]
s_add_u32 s69,s69,s59
ds_read_b64_tr_b16 a[254:255],v3 offset:20152
v_lshl_add_u64 v[30:31],v[24:25],0,v[26:27]
v_lshlrev_b32_sdwa v62,1,v62 dst_sel:DWORD dst_unused:UNUSED_PAD src0_sel:DWORD src1_sel:WORD_1
v_mfma_f32_16x16x32_bf16 v[40:43],a[200:203],a[80:83],v[16:19]
buffer_load_dwordx4 v5,s[44:47],s69 offen offset:0 lds
v_lshlrev_b32_sdwa v61,1,v61 dst_sel:DWORD dst_unused:UNUSED_PAD src0_sel:DWORD src1_sel:WORD_1
v_mfma_f32_16x16x32_bf16 v[40:43],a[196:199],a[64:67],v[40:43]
s_waitcnt lgkmcnt(8)
ds_read_u16 v60,v60
v_lshl_add_u64 v[24:25],v[52:53],0,v[54:55]
v_mfma_f32_16x16x32_bf16 v[40:43],a[192:195],a[48:51],v[40:43]
s_waitcnt lgkmcnt(3)
ds_read_u16_d16_hi v62,v62
v_or3_b32 v13,v14,v15,v13
v_lshlrev_b32_sdwa v63,1,v63 dst_sel:DWORD dst_unused:UNUSED_PAD src0_sel:DWORD src1_sel:WORD_1
v_mfma_f32_16x16x32_bf16 v[52:55],a[192:195],a[52:55],v[16:19]
s_add_u32 s69,s69,s53
ds_read_b64 v[14:15],v7 offset:18992
v_or_b32 v26,v56,v58
v_lshlrev_b32_sdwa v32,1,v32 dst_sel:DWORD dst_unused:UNUSED_PAD src0_sel:DWORD src1_sel:WORD_1
v_mfma_f32_16x16x32_bf16 v[52:55],a[196:199],a[68:71],v[52:55]
buffer_load_dwordx4 v5,s[44:47],s69 offen offset:1024 lds
ds_read_u16 v61,v61
v_cvt_pk_bf16_f32 v27,v57,v59
v_lshlrev_b32_sdwa v34,1,v34 dst_sel:DWORD dst_unused:UNUSED_PAD src0_sel:DWORD src1_sel:WORD_1
v_mfma_f32_16x16x32_bf16 v[52:55],a[200:203],a[84:87],v[52:55]
s_add_u32 m0,s57,84864
ds_read_u16_d16_hi v63,v63
v_lshlrev_b32_sdwa v33,1,v33 dst_sel:DWORD dst_unused:UNUSED_PAD src0_sel:DWORD src1_sel:WORD_1
v_mfma_f32_16x16x32_bf16 v[56:59],a[212:215],a[84:87],v[20:23]
s_add_u32 s89,s56,4816896
ds_read_u16 v32,v32
v_lshlrev_b32_sdwa v35,1,v35 dst_sel:DWORD dst_unused:UNUSED_PAD src0_sel:DWORD src1_sel:WORD_1
v_mfma_f32_16x16x32_bf16 v[56:59],a[204:207],a[68:71],v[56:59]
buffer_load_dwordx4 v6,s[36:39],s89 offen lds
ds_read_u16_d16_hi v34,v34
v_mfma_f32_16x16x32_bf16 v[56:59],a[208:211],a[52:55],v[56:59]
s_waitcnt lgkmcnt(4)
ds_read_u16 v33,v33
v_or3_b32 v13,v14,v15,v13
v_mfma_f32_16x16x32_bf16 v[136:139],a[228:231],v[28:31],v[136:139]
s_add_u32 m0,s58,12320
ds_read_b64 v[14:15],v7 offset:19504
v_lshlrev_b32_sdwa v48,1,v48 dst_sel:DWORD dst_unused:UNUSED_PAD src0_sel:DWORD src1_sel:WORD_1
v_mfma_f32_16x16x32_bf16 v[140:143],a[228:231],v[24:27],v[140:143]
s_add_u32 s69,s69,s53
ds_read_u16_d16_hi v35,v35
v_lshlrev_b32_sdwa v50,1,v50 dst_sel:DWORD dst_unused:UNUSED_PAD src0_sel:DWORD src1_sel:WORD_1
v_mfma_f32_16x16x32_bf16 v[68:71],a[232:235],v[24:27],v[68:71]
buffer_load_dwordx4 v5,s[44:47],s69 offen offset:2048 lds
v_lshlrev_b32_sdwa v49,1,v49 dst_sel:DWORD dst_unused:UNUSED_PAD src0_sel:DWORD src1_sel:WORD_1
v_mfma_f32_16x16x32_bf16 v[64:67],a[232:235],v[28:31],v[64:67]
s_add_u32 m0,s57,8
ds_read_u16 v48,v48
v_lshlrev_b32_sdwa v51,1,v51 dst_sel:DWORD dst_unused:UNUSED_PAD src0_sel:DWORD src1_sel:WORD_1
v_mfma_f32_16x16x32_bf16 v[144:147],a[236:239],v[28:31],v[144:147]
s_add_u32 s89,s56,4915200
ds_read_u16_d16_hi v50,v50
v_lshlrev_b32_sdwa v44,1,v44 dst_sel:DWORD dst_unused:UNUSED_PAD src0_sel:DWORD src1_sel:WORD_1
v_mfma_f32_16x16x32_bf16 v[148:151],a[236:239],v[24:27],v[148:151]
s_waitcnt lgkmcnt(3)
buffer_load_dwordx4 v6,s[36:39],s89 offen lds
ds_read_u16 v49,v49
v_or3_b32 v13,v14,v15,v13
v_mfma_f32_16x16x32_bf16 v[84:87],a[244:247],v[24:27],v[84:87]
ds_read_u16_d16_hi v51,v51
v_lshlrev_b32_sdwa v46,1,v46 dst_sel:DWORD dst_unused:UNUSED_PAD src0_sel:DWORD src1_sel:WORD_1
v_mfma_f32_16x16x32_bf16 v[80:83],a[244:247],v[28:31],v[80:83]
ds_read_u16 v44,v44
v_lshlrev_b32_sdwa v45,1,v45 dst_sel:DWORD dst_unused:UNUSED_PAD src0_sel:DWORD src1_sel:WORD_1
v_mfma_f32_16x16x32_bf16 v[152:155],a[248:251],v[28:31],v[152:155]
ds_read_u16_d16_hi v46,v46
v_lshlrev_b32_sdwa v47,1,v47 dst_sel:DWORD dst_unused:UNUSED_PAD src0_sel:DWORD src1_sel:WORD_1
v_mfma_f32_16x16x32_bf16 v[156:159],a[248:251],v[24:27],v[156:159]
ds_read_u16 v45,v45
v_mfma_f32_16x16x32_bf16 v[100:103],a[252:255],v[24:27],v[100:103]
ds_read_u16_d16_hi v47,v47
v_mfma_f32_16x16x32_bf16 v[96:99],a[252:255],v[28:31],v[96:99]
ds_read_b64_tr_b16 a[218:219],v1 offset:21552
v_mfma_f32_16x16x32_bf16 v[24:27],a[208:211],a[56:59],v[20:23]
v_lshlrev_b32_sdwa v36,1,v36 dst_sel:DWORD dst_unused:UNUSED_PAD src0_sel:DWORD src1_sel:WORD_1
v_fma_mixlo_f16 v8,v53,s99,0
v_mfma_f32_16x16x32_bf16 v[24:27],a[204:207],a[72:75],v[24:27]
ds_read_u16 v36,v36
v_lshl_add_u64 v[60:61],v[60:61],0,v[62:63]
v_mfma_f32_16x16x32_bf16 v[24:27],a[212:215],a[88:91],v[24:27]
s_waitcnt lgkmcnt(7)
v_lshl_add_u64 v[62:63],v[32:33],0,v[34:35]
v_lshlrev_b32_sdwa v38,1,v38 dst_sel:DWORD dst_unused:UNUSED_PAD src0_sel:DWORD src1_sel:WORD_1
v_mfma_f32_16x16x32_bf16 v[28:31],a[200:203],a[88:91],v[16:19]
ds_read_u16_d16_hi v38,v38
v_fma_mixhi_f16 v8,v55,s99,0
v_mfma_f32_16x16x32_bf16 v[28:31],a[196:199],a[72:75],v[28:31]
s_waitcnt lgkmcnt(6)
v_lshl_add_u64 v[32:33],v[48:49],0,v[50:51]
v_lshlrev_b32_sdwa v37,1,v37 dst_sel:DWORD dst_unused:UNUSED_PAD src0_sel:DWORD src1_sel:WORD_1
v_pk_fma_f16 v9,v8,v8,s54
v_mfma_f32_16x16x32_bf16 v[28:31],a[192:195],a[56:59],v[28:31]
ds_read_u16 v37,v37
v_lshlrev_b32_sdwa v39,1,v39 dst_sel:DWORD dst_unused:UNUSED_PAD src0_sel:DWORD src1_sel:WORD_1
v_mfma_f32_16x16x32_bf16 v[48:51],a[192:195],a[60:63],v[16:19]
ds_read_b128 v[16:19],v4 offset:1024
v_pk_mul_f16 v9,v8,v9
v_mfma_f32_16x16x32_bf16 v[48:51],a[196:199],a[76:79],v[48:51]
s_waitcnt lgkmcnt(5)
ds_read_u16_d16_hi v39,v39
ds_read_b64_tr_b16 a[192:193],v1 offset:22576
v_lshl_add_u64 v[34:35],v[44:45],0,v[46:47]
v_lshlrev_b32_sdwa v40,1,v40 dst_sel:DWORD dst_unused:UNUSED_PAD src0_sel:DWORD src1_sel:WORD_1
v_mfma_f32_16x16x32_bf16 v[48:51],a[200:203],a[92:95],v[48:51]
ds_read_b64_tr_b16 a[194:195],v1 offset:23600
v_exp_f16_e64 v8,v9
v_mfma_f32_16x16x32_bf16 v[44:47],a[212:215],a[92:95],v[20:23]
ds_read_b128 v[20:23],v4 offset:1056
ds_read_u16 v40,v40
v_lshlrev_b32_sdwa v42,1,v42 dst_sel:DWORD dst_unused:UNUSED_PAD src0_sel:DWORD src1_sel:WORD_1
v_mfma_f32_16x16x32_bf16 v[44:47],a[204:207],a[76:79],v[44:47]
ds_read_u16_d16_hi v42,v42
ds_read_b64_tr_b16 a[196:197],v2 offset:22576
v_lshlrev_b32_sdwa v41,1,v41 dst_sel:DWORD dst_unused:UNUSED_PAD src0_sel:DWORD src1_sel:WORD_1
v_mfma_f32_16x16x32_bf16 v[44:47],a[208:211],a[60:63],v[44:47]
ds_read_u16 v41,v41
v_exp_f16_sdwa v8,v9 dst_sel:WORD_1 dst_unused:UNUSED_PRESERVE src0_sel:WORD_1
v_mfma_f32_16x16x32_bf16 v[112:115],a[228:231],v[60:63],v[112:115]
ds_read_b64_tr_b16 a[198:199],v2 offset:23600
ds_read_b64_tr_b16 a[200:201],v2 offset:20528
v_lshlrev_b32_sdwa v43,1,v43 dst_sel:DWORD dst_unused:UNUSED_PAD src0_sel:DWORD src1_sel:WORD_1
v_mfma_f32_16x16x32_bf16 v[116:119],a[228:231],v[32:35],v[116:119]
ds_read_u16_d16_hi v43,v43
v_lshlrev_b32_sdwa v56,1,v56 dst_sel:DWORD dst_unused:UNUSED_PAD src0_sel:DWORD src1_sel:WORD_1
v_mfma_f32_16x16x32_bf16 v[76:79],a[232:235],v[32:35],v[76:79]
ds_read_b64_tr_b16 a[202:203],v2 offset:21552
v_pk_fma_f16 v8,v8,s55,s55
v_mfma_f32_16x16x32_bf16 v[72:75],a[232:235],v[60:63],v[72:75]
v_rcp_f16 v9,v8
v_lshlrev_b32_sdwa v58,1,v58 dst_sel:DWORD dst_unused:UNUSED_PAD src0_sel:DWORD src1_sel:WORD_1
v_mfma_f32_16x16x32_bf16 v[120:123],a[236:239],v[60:63],v[120:123]
ds_read_u16 v56,v56
v_lshlrev_b32_sdwa v57,1,v57 dst_sel:DWORD dst_unused:UNUSED_PAD src0_sel:DWORD src1_sel:WORD_1
v_mfma_f32_16x16x32_bf16 v[124:127],a[236:239],v[32:35],v[124:127]
ds_read_u16_d16_hi v58,v58
v_lshlrev_b32_sdwa v59,1,v59 dst_sel:DWORD dst_unused:UNUSED_PAD src0_sel:DWORD src1_sel:WORD_1
v_mfma_f32_16x16x32_bf16 v[92:95],a[244:247],v[32:35],v[92:95]
ds_read_u16 v57,v57
v_lshlrev_b32_sdwa v52,1,v52 dst_sel:DWORD dst_unused:UNUSED_PAD src0_sel:DWORD src1_sel:WORD_1
v_mfma_f32_16x16x32_bf16 v[88:91],a[244:247],v[60:63],v[88:91]
ds_read_u16_d16_hi v59,v59
v_rcp_f16_sdwa v9,v8 dst_sel:WORD_1 dst_unused:UNUSED_PRESERVE src0_sel:WORD_1
v_mfma_f32_16x16x32_bf16 v[128:131],a[248:251],v[60:63],v[128:131]
ds_read_b64_tr_b16 a[212:213],v2 offset:18480
v_lshlrev_b32_sdwa v54,1,v54 dst_sel:DWORD dst_unused:UNUSED_PAD src0_sel:DWORD src1_sel:WORD_1
v_mfma_f32_16x16x32_bf16 v[132:135],a[248:251],v[32:35],v[132:135]
ds_read_b64_tr_b16 a[214:215],v2 offset:19504
v_fma_mix_f32 v53,v53,v9,0 op_sel_hi:[0,1,0]
v_mfma_f32_16x16x32_bf16 v[108:111],a[252:255],v[32:35],v[108:111]
ds_read_u16 v52,v52
v_fma_mix_f32 v55,v55,v9,0 op_sel:[0,1,0] op_sel_hi:[0,1,0]
v_mfma_f32_16x16x32_bf16 v[104:107],a[252:255],v[60:63],v[104:107]
s_waitcnt lgkmcnt(7)
ds_read_u16_d16_hi v54,v54
v_mfma_f32_16x16x32_bf16 v[32:35],a[220:223],a[0:3],v[16:19]
s_add_u32 m0,s57,3544
v_lshlrev_b32_sdwa v24,1,v24 dst_sel:DWORD dst_unused:UNUSED_PAD src0_sel:DWORD src1_sel:WORD_1
v_lshlrev_b32_sdwa v26,1,v26 dst_sel:DWORD dst_unused:UNUSED_PAD src0_sel:DWORD src1_sel:WORD_1
v_mfma_f32_16x16x32_bf16 v[32:35],a[216:219],a[16:19],v[32:35]
s_add_u32 s89,s56,5013504
ds_read_u16 v24,v24
v_lshl_add_u64 v[36:37],v[36:37],0,v[38:39]
v_mfma_f32_16x16x32_bf16 v[32:35],a[192:195],a[32:35],v[32:35]
buffer_load_dwordx4 v6,s[36:39],s89 offen lds
v_lshl_add_u64 v[38:39],v[40:41],0,v[42:43]
v_mfma_f32_16x16x32_bf16 v[60:63],a[196:199],a[32:35],v[20:23]
s_add_u32 m0,s57,7072
ds_read_u16_d16_hi v26,v26
v_lshlrev_b32_sdwa v25,1,v25 dst_sel:DWORD dst_unused:UNUSED_PAD src0_sel:DWORD src1_sel:WORD_1
v_mfma_f32_16x16x32_bf16 v[60:63],a[200:203],a[16:19],v[60:63]
s_waitcnt lgkmcnt(4)
ds_read_u16 v25,v25
v_lshl_add_u64 v[40:41],v[56:57],0,v[58:59]
v_mfma_f32_16x16x32_bf16 v[60:63],a[212:215],a[0:3],v[60:63]
s_add_u32 s89,s56,5111808
v_lshlrev_b32_sdwa v27,1,v27 dst_sel:DWORD dst_unused:UNUSED_PAD src0_sel:DWORD src1_sel:WORD_1
v_lshlrev_b32_sdwa v28,1,v28 dst_sel:DWORD dst_unused:UNUSED_PAD src0_sel:DWORD src1_sel:WORD_1
v_mfma_f32_16x16x32_bf16 v[56:59],a[212:215],a[4:7],v[20:23]
s_waitcnt lgkmcnt(3)
buffer_load_dwordx4 v6,s[36:39],s89 offen lds
v_or_b32 v42,v52,v54
v_mfma_f32_16x16x32_bf16 v[56:59],a[200:203],a[20:23],v[56:59]
ds_read_u16_d16_hi v27,v27
v_cvt_pk_bf16_f32 v43,v53,v55
v_lshlrev_b32_sdwa v30,1,v30 dst_sel:DWORD dst_unused:UNUSED_PAD src0_sel:DWORD src1_sel:WORD_1
v_mfma_f32_16x16x32_bf16 v[56:59],a[196:199],a[36:39],v[56:59]
ds_read_u16 v28,v28
v_lshlrev_b32_sdwa v29,1,v29 dst_sel:DWORD dst_unused:UNUSED_PAD src0_sel:DWORD src1_sel:WORD_1
v_mfma_f32_16x16x32_bf16 v[52:55],a[192:195],a[36:39],v[16:19]
ds_read_u16_d16_hi v30,v30
v_lshlrev_b32_sdwa v31,1,v31 dst_sel:DWORD dst_unused:UNUSED_PAD src0_sel:DWORD src1_sel:WORD_1
v_mfma_f32_16x16x32_bf16 v[52:55],a[216:219],a[20:23],v[52:55]
ds_read_u16 v29,v29
v_lshlrev_b32_sdwa v44,1,v44 dst_sel:DWORD dst_unused:UNUSED_PAD src0_sel:DWORD src1_sel:WORD_1
v_mfma_f32_16x16x32_bf16 v[52:55],a[220:223],a[4:7],v[52:55]
ds_read_u16_d16_hi v31,v31
v_lshlrev_b32_sdwa v46,1,v46 dst_sel:DWORD dst_unused:UNUSED_PAD src0_sel:DWORD src1_sel:WORD_1
v_mfma_f32_16x16x32_bf16 v[232:235],a[228:231],v[36:39],v[232:235]
ds_read_u16 v44,v44
v_lshlrev_b32_sdwa v45,1,v45 dst_sel:DWORD dst_unused:UNUSED_PAD src0_sel:DWORD src1_sel:WORD_1
v_mfma_f32_16x16x32_bf16 v[236:239],a[228:231],v[40:43],v[236:239]
ds_read_u16_d16_hi v46,v46
v_lshlrev_b32_sdwa v47,1,v47 dst_sel:DWORD dst_unused:UNUSED_PAD src0_sel:DWORD src1_sel:WORD_1
v_mfma_f32_16x16x32_bf16 v[164:167],a[232:235],v[40:43],v[164:167]
ds_read_u16 v45,v45
ds_read_u16_d16_hi v47,v47
v_mfma_f32_16x16x32_bf16 v[160:163],a[232:235],v[36:39],v[160:163]
ds_read_b64_tr_b16 a[240:241],v3 offset:0
v_lshlrev_b32_sdwa v48,1,v48 dst_sel:DWORD dst_unused:UNUSED_PAD src0_sel:DWORD src1_sel:WORD_1
v_mfma_f32_16x16x32_bf16 v[240:243],a[236:239],v[36:39],v[240:243]
ds_read_u16 v48,v48
v_lshlrev_b32_sdwa v50,1,v50 dst_sel:DWORD dst_unused:UNUSED_PAD src0_sel:DWORD src1_sel:WORD_1
v_mfma_f32_16x16x32_bf16 v[244:247],a[236:239],v[40:43],v[244:247]
ds_read_u16_d16_hi v50,v50
v_lshlrev_b32_sdwa v49,1,v49 dst_sel:DWORD dst_unused:UNUSED_PAD src0_sel:DWORD src1_sel:WORD_1
v_mfma_f32_16x16x32_bf16 v[180:183],a[244:247],v[40:43],v[180:183]
ds_read_u16 v49,v49
v_lshlrev_b32_sdwa v51,1,v51 dst_sel:DWORD dst_unused:UNUSED_PAD src0_sel:DWORD src1_sel:WORD_1
v_mfma_f32_16x16x32_bf16 v[176:179],a[244:247],v[36:39],v[176:179]
ds_read_u16_d16_hi v51,v51
v_mfma_f32_16x16x32_bf16 v[248:251],a[248:251],v[36:39],v[248:251]
ds_read_b64_tr_b16 a[242:243],v3 offset:1536
v_mfma_f32_16x16x32_bf16 v[252:255],a[248:251],v[40:43],v[252:255]
ds_read_b64_tr_b16 a[224:225],v3 offset:8
v_mfma_f32_16x16x32_bf16 v[196:199],a[252:255],v[40:43],v[196:199]
ds_read_b64_tr_b16 a[226:227],v3 offset:1544
v_mfma_f32_16x16x32_bf16 v[192:195],a[252:255],v[36:39],v[192:195]
v_mfma_f32_16x16x32_bf16 v[40:43],a[220:223],a[8:11],v[16:19]
ds_read_b64_tr_b16 a[204:205],v1 offset:0
v_lshlrev_b32_sdwa v32,1,v32 dst_sel:DWORD dst_unused:UNUSED_PAD src0_sel:DWORD src1_sel:WORD_1
v_mfma_f32_16x16x32_bf16 v[40:43],a[216:219],a[24:27],v[40:43]
s_waitcnt lgkmcnt(7)
ds_read_u16 v32,v32
v_lshl_add_u64 v[24:25],v[24:25],0,v[26:27]
v_fma_mixlo_f16 v8,v57,s99,0
v_mfma_f32_16x16x32_bf16 v[40:43],a[192:195],a[40:43],v[40:43]
ds_read_b64_tr_b16 a[206:207],v1 offset:1024
v_lshl_add_u64 v[26:27],v[28:29],0,v[30:31]
v_lshlrev_b32_sdwa v34,1,v34 dst_sel:DWORD dst_unused:UNUSED_PAD src0_sel:DWORD src1_sel:WORD_1
v_mfma_f32_16x16x32_bf16 v[36:39],a[196:199],a[40:43],v[20:23]
ds_read_u16_d16_hi v34,v34
v_fma_mixhi_f16 v8,v59,s99,0
v_mfma_f32_16x16x32_bf16 v[36:39],a[200:203],a[24:27],v[36:39]
ds_read_b64_tr_b16 a[208:209],v1 offset:2048
v_lshl_add_u64 v[28:29],v[44:45],0,v[46:47]
v_lshlrev_b32_sdwa v33,1,v33 dst_sel:DWORD dst_unused:UNUSED_PAD src0_sel:DWORD src1_sel:WORD_1
v_pk_fma_f16 v9,v8,v8,s54
v_mfma_f32_16x16x32_bf16 v[36:39],a[212:215],a[8:11],v[36:39]
ds_read_u16 v33,v33
v_lshlrev_b32_sdwa v35,1,v35 dst_sel:DWORD dst_unused:UNUSED_PAD src0_sel:DWORD src1_sel:WORD_1
v_mfma_f32_16x16x32_bf16 v[44:47],a[212:215],a[12:15],v[20:23]
ds_read_u16_d16_hi v35,v35
v_pk_mul_f16 v9,v8,v9
v_mfma_f32_16x16x32_bf16 v[44:47],a[200:203],a[28:31],v[44:47]
s_waitcnt lgkmcnt(7)
ds_read_b64_tr_b16 a[210:211],v1 offset:3072
v_lshl_add_u64 v[30:31],v[48:49],0,v[50:51]
v_lshlrev_b32_sdwa v60,1,v60 dst_sel:DWORD dst_unused:UNUSED_PAD src0_sel:DWORD src1_sel:WORD_1
v_mfma_f32_16x16x32_bf16 v[44:47],a[196:199],a[44:47],v[44:47]
v_exp_f16 v8,v9
v_mfma_f32_16x16x32_bf16 v[48:51],a[192:195],a[44:47],v[16:19]
ds_read_u16 v60,v60
v_lshlrev_b32_sdwa v62,1,v62 dst_sel:DWORD dst_unused:UNUSED_PAD src0_sel:DWORD src1_sel:WORD_1
v_mfma_f32_16x16x32_bf16 v[48:51],a[216:219],a[28:31],v[48:51]
ds_read_u16_d16_hi v62,v62
v_lshlrev_b32_sdwa v61,1,v61 dst_sel:DWORD dst_unused:UNUSED_PAD src0_sel:DWORD src1_sel:WORD_1
v_mfma_f32_16x16x32_bf16 v[48:51],a[220:223],a[12:15],v[48:51]
v_exp_f16_sdwa v8,v9 dst_sel:WORD_1 dst_unused:UNUSED_PRESERVE src0_sel:WORD_1
v_mfma_f32_16x16x32_bf16 v[208:211],a[228:231],v[24:27],v[208:211]
ds_read_u16 v61,v61
v_lshlrev_b32_sdwa v63,1,v63 dst_sel:DWORD dst_unused:UNUSED_PAD src0_sel:DWORD src1_sel:WORD_1
v_mfma_f32_16x16x32_bf16 v[212:215],a[228:231],v[28:31],v[212:215]
ds_read_u16_d16_hi v63,v63
v_lshlrev_b32_sdwa v52,1,v52 dst_sel:DWORD dst_unused:UNUSED_PAD src0_sel:DWORD src1_sel:WORD_1
v_mfma_f32_16x16x32_bf16 v[172:175],a[232:235],v[28:31],v[172:175]
ds_read_u16 v52,v52
v_pk_fma_f16 v8,v8,s55,s55
v_mfma_f32_16x16x32_bf16 v[168:171],a[232:235],v[24:27],v[168:171]
ds_read_b64_tr_b16 a[228:229],v3 offset:64
v_rcp_f16 v9,v8
v_lshlrev_b32_sdwa v54,1,v54 dst_sel:DWORD dst_unused:UNUSED_PAD src0_sel:DWORD src1_sel:WORD_1
v_mfma_f32_16x16x32_bf16 v[216:219],a[236:239],v[24:27],v[216:219]
ds_read_u16_d16_hi v54,v54
v_lshlrev_b32_sdwa v53,1,v53 dst_sel:DWORD dst_unused:UNUSED_PAD src0_sel:DWORD src1_sel:WORD_1
v_mfma_f32_16x16x32_bf16 v[220:223],a[236:239],v[28:31],v[220:223]
ds_read_u16 v53,v53
ds_read_b64_tr_b16 a[230:231],v3 offset:1600
v_mfma_f32_16x16x32_bf16 v[188:191],a[244:247],v[28:31],v[188:191]
ds_read_b64_tr_b16 a[232:233],v3 offset:72
v_lshlrev_b32_sdwa v55,1,v55 dst_sel:DWORD dst_unused:UNUSED_PAD src0_sel:DWORD src1_sel:WORD_1
v_mfma_f32_16x16x32_bf16 v[184:187],a[244:247],v[24:27],v[184:187]
ds_read_u16_d16_hi v55,v55
v_rcp_f16_sdwa v9,v8 dst_sel:WORD_1 dst_unused:UNUSED_PRESERVE src0_sel:WORD_1
v_mfma_f32_16x16x32_bf16 v[224:227],a[248:251],v[24:27],v[224:227]
ds_read_b64_tr_b16 a[234:235],v3 offset:1608
ds_read_b64_tr_b16 a[236:237],v3 offset:128
v_lshlrev_b32_sdwa v56,1,v56 dst_sel:DWORD dst_unused:UNUSED_PAD src0_sel:DWORD src1_sel:WORD_1
v_mfma_f32_16x16x32_bf16 v[228:231],a[248:251],v[28:31],v[228:231]
ds_read_u16 v56,v56
v_lshlrev_b32_sdwa v58,1,v58 dst_sel:DWORD dst_unused:UNUSED_PAD src0_sel:DWORD src1_sel:WORD_1
v_mfma_f32_16x16x32_bf16 v[204:207],a[252:255],v[28:31],v[204:207]
ds_read_u16_d16_hi v58,v58
v_fma_mix_f32 v57,v57,v9,0 op_sel_hi:[0,1,0]
v_mfma_f32_16x16x32_bf16 v[200:203],a[252:255],v[24:27],v[200:203]
ds_read_b64_tr_b16 a[238:239],v3 offset:1664
v_fma_mix_f32 v59,v59,v9,0 op_sel:[0,1,0] op_sel_hi:[0,1,0]
v_mfma_f32_16x16x32_bf16 v[28:31],a[220:223],a[48:51],v[16:19]
s_add_u32 m0,s57,10608
ds_read_b64 v[14:15],v7 offset:0
v_lshlrev_b32_sdwa v40,1,v40 dst_sel:DWORD dst_unused:UNUSED_PAD src0_sel:DWORD src1_sel:WORD_1
v_mfma_f32_16x16x32_bf16 v[28:31],a[216:219],a[64:67],v[28:31]
s_waitcnt lgkmcnt(10)
ds_read_b64_tr_b16 a[244:245],v3 offset:136
v_lshl_add_u64 v[32:33],v[32:33],0,v[34:35]
v_mfma_f32_16x16x32_bf16 v[28:31],a[192:195],a[80:83],v[28:31]
s_add_u32 s89,s56,5210112
ds_read_b64_tr_b16 a[246:247],v3 offset:1672
v_lshl_add_u64 v[34:35],v[60:61],0,v[62:63]
v_lshlrev_b32_sdwa v42,1,v42 dst_sel:DWORD dst_unused:UNUSED_PAD src0_sel:DWORD src1_sel:WORD_1
v_mfma_f32_16x16x32_bf16 v[24:27],a[196:199],a[80:83],v[20:23]
buffer_load_dwordx4 v6,s[36:39],s89 offen lds
v_lshlrev_b32_sdwa v41,1,v41 dst_sel:DWORD dst_unused:UNUSED_PAD src0_sel:DWORD src1_sel:WORD_1
v_mfma_f32_16x16x32_bf16 v[24:27],a[200:203],a[64:67],v[24:27]
s_waitcnt lgkmcnt(8)
ds_read_u16 v40,v40
v_lshl_add_u64 v[60:61],v[52:53],0,v[54:55]
v_mfma_f32_16x16x32_bf16 v[24:27],a[212:215],a[48:51],v[24:27]
s_waitcnt lgkmcnt(3)
ds_read_u16_d16_hi v42,v42
v_or3_b32 v13,v14,v15,v13
v_lshlrev_b32_sdwa v43,1,v43 dst_sel:DWORD dst_unused:UNUSED_PAD src0_sel:DWORD src1_sel:WORD_1
v_mfma_f32_16x16x32_bf16 v[52:55],a[212:215],a[52:55],v[20:23]
s_add_u32 m0,s57,14152
ds_read_b64 v[14:15],v7 offset:512
v_or_b32 v62,v56,v58
v_lshlrev_b32_sdwa v36,1,v36 dst_sel:DWORD dst_unused:UNUSED_PAD src0_sel:DWORD src1_sel:WORD_1
v_mfma_f32_16x16x32_bf16 v[52:55],a[200:203],a[68:71],v[52:55]
s_add_u32 s89,s56,5308416
ds_read_u16 v41,v41
v_cvt_pk_bf16_f32 v63,v57,v59
v_lshlrev_b32_sdwa v38,1,v38 dst_sel:DWORD dst_unused:UNUSED_PAD src0_sel:DWORD src1_sel:WORD_1
v_mfma_f32_16x16x32_bf16 v[52:55],a[196:199],a[84:87],v[52:55]
buffer_load_dwordx4 v6,s[36:39],s89 offen lds
v_lshlrev_b32_sdwa v37,1,v37 dst_sel:DWORD dst_unused:UNUSED_PAD src0_sel:DWORD src1_sel:WORD_1
v_mfma_f32_16x16x32_bf16 v[56:59],a[192:195],a[84:87],v[16:19]
ds_read_u16_d16_hi v43,v43
v_lshlrev_b32_sdwa v39,1,v39 dst_sel:DWORD dst_unused:UNUSED_PAD src0_sel:DWORD src1_sel:WORD_1
v_mfma_f32_16x16x32_bf16 v[56:59],a[216:219],a[68:71],v[56:59]
ds_read_u16 v36,v36
v_lshlrev_b32_sdwa v48,1,v48 dst_sel:DWORD dst_unused:UNUSED_PAD src0_sel:DWORD src1_sel:WORD_1
v_mfma_f32_16x16x32_bf16 v[56:59],a[220:223],a[52:55],v[56:59]
s_waitcnt lgkmcnt(3)
ds_read_u16_d16_hi v38,v38
v_or3_b32 v13,v14,v15,v13
v_mfma_f32_16x16x32_bf16 v[136:139],a[240:243],v[32:35],v[136:139]
ds_read_b64 v[14:15],v7 offset:1024
v_lshlrev_b32_sdwa v50,1,v50 dst_sel:DWORD dst_unused:UNUSED_PAD src0_sel:DWORD src1_sel:WORD_1
v_mfma_f32_16x16x32_bf16 v[140:143],a[240:243],v[60:63],v[140:143]
ds_read_u16 v37,v37
v_lshlrev_b32_sdwa v49,1,v49 dst_sel:DWORD dst_unused:UNUSED_PAD src0_sel:DWORD src1_sel:WORD_1
v_mfma_f32_16x16x32_bf16 v[68:71],a[224:227],v[60:63],v[68:71]
ds_read_u16_d16_hi v39,v39
v_lshlrev_b32_sdwa v51,1,v51 dst_sel:DWORD dst_unused:UNUSED_PAD src0_sel:DWORD src1_sel:WORD_1
v_mfma_f32_16x16x32_bf16 v[64:67],a[224:227],v[32:35],v[64:67]
ds_read_u16 v48,v48
v_lshlrev_b32_sdwa v44,1,v44 dst_sel:DWORD dst_unused:UNUSED_PAD src0_sel:DWORD src1_sel:WORD_1
v_mfma_f32_16x16x32_bf16 v[144:147],a[228:231],v[32:35],v[144:147]
ds_read_u16_d16_hi v50,v50
v_lshlrev_b32_sdwa v46,1,v46 dst_sel:DWORD dst_unused:UNUSED_PAD src0_sel:DWORD src1_sel:WORD_1
v_mfma_f32_16x16x32_bf16 v[148:151],a[228:231],v[60:63],v[148:151]
s_waitcnt lgkmcnt(4)
ds_read_u16 v49,v49
v_or3_b32 v13,v14,v15,v13
v_mfma_f32_16x16x32_bf16 v[84:87],a[232:235],v[60:63],v[84:87]
s_add_u32 m0,s57,17688
ds_read_u16_d16_hi v51,v51
TP3812_%=:
s_cbranch_execz TP5956_%=
v_lshlrev_b32_sdwa v45,1,v45 dst_sel:DWORD dst_unused:UNUSED_PAD src0_sel:DWORD src1_sel:WORD_1
v_mfma_f32_16x16x32_bf16 v[80:83],a[232:235],v[32:35],v[80:83]
s_add_u32 s89,s56,5406720
ds_read_u16 v44,v44
v_lshlrev_b32_sdwa v47,1,v47 dst_sel:DWORD dst_unused:UNUSED_PAD src0_sel:DWORD src1_sel:WORD_1
v_mfma_f32_16x16x32_bf16 v[152:155],a[236:239],v[32:35],v[152:155]
buffer_load_dwordx4 v6,s[36:39],s89 offen lds
v_mfma_f32_16x16x32_bf16 v[156:159],a[236:239],v[60:63],v[156:159]
ds_read_u16_d16_hi v46,v46
v_mfma_f32_16x16x32_bf16 v[100:103],a[244:247],v[60:63],v[100:103]
ds_read_u16 v45,v45
v_mfma_f32_16x16x32_bf16 v[96:99],a[244:247],v[32:35],v[96:99]
ds_read_u16_d16_hi v47,v47
v_mfma_f32_16x16x32_bf16 v[60:63],a[220:223],a[56:59],v[16:19]
v_lshlrev_b32_sdwa v28,1,v28 dst_sel:DWORD dst_unused:UNUSED_PAD src0_sel:DWORD src1_sel:WORD_1
v_fma_mixlo_f16 v8,v53,s99,0
v_mfma_f32_16x16x32_bf16 v[60:63],a[216:219],a[72:75],v[60:63]
ds_read_u16 v28,v28
v_lshl_add_u64 v[40:41],v[40:41],0,v[42:43]
v_mfma_f32_16x16x32_bf16 v[60:63],a[192:195],a[88:91],v[60:63]
s_waitcnt lgkmcnt(6)
v_lshl_add_u64 v[42:43],v[36:37],0,v[38:39]
v_lshlrev_b32_sdwa v30,1,v30 dst_sel:DWORD dst_unused:UNUSED_PAD src0_sel:DWORD src1_sel:WORD_1
v_mfma_f32_16x16x32_bf16 v[32:35],a[196:199],a[88:91],v[20:23]
ds_read_u16_d16_hi v30,v30
v_fma_mixhi_f16 v8,v55,s99,0
v_mfma_f32_16x16x32_bf16 v[32:35],a[200:203],a[72:75],v[32:35]
s_waitcnt lgkmcnt(5)
v_lshl_add_u64 v[36:37],v[48:49],0,v[50:51]
v_lshlrev_b32_sdwa v29,1,v29 dst_sel:DWORD dst_unused:UNUSED_PAD src0_sel:DWORD src1_sel:WORD_1
v_pk_fma_f16 v9,v8,v8,s54
v_mfma_f32_16x16x32_bf16 v[32:35],a[212:215],a[56:59],v[32:35]
ds_read_u16 v29,v29
v_lshlrev_b32_sdwa v31,1,v31 dst_sel:DWORD dst_unused:UNUSED_PAD src0_sel:DWORD src1_sel:WORD_1
v_mfma_f32_16x16x32_bf16 v[48:51],a[212:215],a[60:63],v[20:23]
s_nop 0
ds_read_b128 v[20:23],v4 offset:1152
v_pk_mul_f16 v9,v8,v9
v_mfma_f32_16x16x32_bf16 v[48:51],a[200:203],a[76:79],v[48:51]
s_waitcnt lgkmcnt(4)
ds_read_u16_d16_hi v31,v31
ds_read_b64_tr_b16 a[212:213],v1 offset:4096
v_lshl_add_u64 v[38:39],v[44:45],0,v[46:47]
v_lshlrev_b32_sdwa v24,1,v24 dst_sel:DWORD dst_unused:UNUSED_PAD src0_sel:DWORD src1_sel:WORD_1
v_mfma_f32_16x16x32_bf16 v[48:51],a[196:199],a[92:95],v[48:51]
ds_read_b64_tr_b16 a[214:215],v1 offset:5120
v_exp_f16 v8,v9
v_mfma_f32_16x16x32_bf16 v[44:47],a[192:195],a[92:95],v[16:19]
ds_read_b128 v[16:19],v4 offset:1184
ds_read_u16 v24,v24
v_lshlrev_b32_sdwa v26,1,v26 dst_sel:DWORD dst_unused:UNUSED_PAD src0_sel:DWORD src1_sel:WORD_1
v_mfma_f32_16x16x32_bf16 v[44:47],a[216:219],a[76:79],v[44:47]
ds_read_u16_d16_hi v26,v26
ds_read_b64_tr_b16 a[200:201],v2 offset:4096
v_lshlrev_b32_sdwa v25,1,v25 dst_sel:DWORD dst_unused:UNUSED_PAD src0_sel:DWORD src1_sel:WORD_1
v_mfma_f32_16x16x32_bf16 v[44:47],a[220:223],a[60:63],v[44:47]
ds_read_u16 v25,v25
v_exp_f16_sdwa v8,v9 dst_sel:WORD_1 dst_unused:UNUSED_PRESERVE src0_sel:WORD_1
v_mfma_f32_16x16x32_bf16 v[112:115],a[240:243],v[40:43],v[112:115]
ds_read_b64_tr_b16 a[202:203],v2 offset:5120
ds_read_b64_tr_b16 a[196:197],v2 offset:2048
v_lshlrev_b32_sdwa v27,1,v27 dst_sel:DWORD dst_unused:UNUSED_PAD src0_sel:DWORD src1_sel:WORD_1
v_mfma_f32_16x16x32_bf16 v[116:119],a[240:243],v[36:39],v[116:119]
ds_read_u16_d16_hi v27,v27
v_lshlrev_b32_sdwa v56,1,v56 dst_sel:DWORD dst_unused:UNUSED_PAD src0_sel:DWORD src1_sel:WORD_1
v_mfma_f32_16x16x32_bf16 v[76:79],a[224:227],v[36:39],v[76:79]
ds_read_b64_tr_b16 a[198:199],v2 offset:3072
v_pk_fma_f16 v8,v8,s55,s55
v_mfma_f32_16x16x32_bf16 v[72:75],a[224:227],v[40:43],v[72:75]
v_rcp_f16 v9,v8
v_lshlrev_b32_sdwa v58,1,v58 dst_sel:DWORD dst_unused:UNUSED_PAD src0_sel:DWORD src1_sel:WORD_1
v_mfma_f32_16x16x32_bf16 v[120:123],a[228:231],v[40:43],v[120:123]
ds_read_u16 v56,v56
v_lshlrev_b32_sdwa v57,1,v57 dst_sel:DWORD dst_unused:UNUSED_PAD src0_sel:DWORD src1_sel:WORD_1
v_mfma_f32_16x16x32_bf16 v[124:127],a[228:231],v[36:39],v[124:127]
ds_read_u16_d16_hi v58,v58
v_lshlrev_b32_sdwa v59,1,v59 dst_sel:DWORD dst_unused:UNUSED_PAD src0_sel:DWORD src1_sel:WORD_1
v_mfma_f32_16x16x32_bf16 v[92:95],a[232:235],v[36:39],v[92:95]
ds_read_u16 v57,v57
v_lshlrev_b32_sdwa v52,1,v52 dst_sel:DWORD dst_unused:UNUSED_PAD src0_sel:DWORD src1_sel:WORD_1
v_mfma_f32_16x16x32_bf16 v[88:91],a[232:235],v[40:43],v[88:91]
ds_read_u16_d16_hi v59,v59
v_rcp_f16_sdwa v9,v8 dst_sel:WORD_1 dst_unused:UNUSED_PRESERVE src0_sel:WORD_1
v_mfma_f32_16x16x32_bf16 v[128:131],a[236:239],v[40:43],v[128:131]
ds_read_b64_tr_b16 a[192:193],v2 offset:0
v_lshlrev_b32_sdwa v54,1,v54 dst_sel:DWORD dst_unused:UNUSED_PAD src0_sel:DWORD src1_sel:WORD_1
v_mfma_f32_16x16x32_bf16 v[132:135],a[236:239],v[36:39],v[132:135]
ds_read_b64_tr_b16 a[194:195],v2 offset:1024
v_fma_mix_f32 v53,v53,v9,0 op_sel_hi:[0,1,0]
v_mfma_f32_16x16x32_bf16 v[108:111],a[244:247],v[36:39],v[108:111]
ds_read_u16 v52,v52
v_fma_mix_f32 v55,v55,v9,0 op_sel:[0,1,0] op_sel_hi:[0,1,0]
v_mfma_f32_16x16x32_bf16 v[104:107],a[244:247],v[40:43],v[104:107]
s_waitcnt lgkmcnt(7)
ds_read_u16_d16_hi v54,v54
v_mfma_f32_16x16x32_bf16 v[36:39],a[204:207],a[0:3],v[20:23]
s_add_u32 m0,s57,21216
v_lshlrev_b32_sdwa v60,1,v60 dst_sel:DWORD dst_unused:UNUSED_PAD src0_sel:DWORD src1_sel:WORD_1
v_lshlrev_b32_sdwa v62,1,v62 dst_sel:DWORD dst_unused:UNUSED_PAD src0_sel:DWORD src1_sel:WORD_1
v_mfma_f32_16x16x32_bf16 v[36:39],a[208:211],a[16:19],v[36:39]
s_add_u32 s89,s56,5505024
ds_read_u16 v60,v60
v_lshl_add_u64 v[28:29],v[28:29],0,v[30:31]
v_mfma_f32_16x16x32_bf16 v[36:39],a[212:215],a[32:35],v[36:39]
buffer_load_dwordx4 v6,s[36:39],s89 offen lds
v_lshl_add_u64 v[30:31],v[24:25],0,v[26:27]
v_mfma_f32_16x16x32_bf16 v[40:43],a[200:203],a[32:35],v[16:19]
ds_read_u16_d16_hi v62,v62
v_lshlrev_b32_sdwa v61,1,v61 dst_sel:DWORD dst_unused:UNUSED_PAD src0_sel:DWORD src1_sel:WORD_1
v_mfma_f32_16x16x32_bf16 v[40:43],a[196:199],a[16:19],v[40:43]
s_waitcnt lgkmcnt(4)
ds_read_u16 v61,v61
v_lshl_add_u64 v[24:25],v[56:57],0,v[58:59]
v_mfma_f32_16x16x32_bf16 v[40:43],a[192:195],a[0:3],v[40:43]
v_lshlrev_b32_sdwa v63,1,v63 dst_sel:DWORD dst_unused:UNUSED_PAD src0_sel:DWORD src1_sel:WORD_1
v_lshlrev_b32_sdwa v32,1,v32 dst_sel:DWORD dst_unused:UNUSED_PAD src0_sel:DWORD src1_sel:WORD_1
v_mfma_f32_16x16x32_bf16 v[56:59],a[192:195],a[4:7],v[16:19]
s_waitcnt lgkmcnt(3)
ds_read_u16_d16_hi v63,v63
v_or_b32 v26,v52,v54
v_mfma_f32_16x16x32_bf16 v[56:59],a[196:199],a[20:23],v[56:59]
s_waitcnt vmcnt(7)
ds_read_u16 v32,v32
v_cvt_pk_bf16_f32 v27,v53,v55
v_lshlrev_b32_sdwa v34,1,v34 dst_sel:DWORD dst_unused:UNUSED_PAD src0_sel:DWORD src1_sel:WORD_1
v_mfma_f32_16x16x32_bf16 v[56:59],a[200:203],a[36:39],v[56:59]
s_barrier
ds_read_u16_d16_hi v34,v34
v_lshlrev_b32_sdwa v33,1,v33 dst_sel:DWORD dst_unused:UNUSED_PAD src0_sel:DWORD src1_sel:WORD_1
v_mfma_f32_16x16x32_bf16 v[52:55],a[212:215],a[36:39],v[20:23]
ds_read_u16 v33,v33
v_lshlrev_b32_sdwa v35,1,v35 dst_sel:DWORD dst_unused:UNUSED_PAD src0_sel:DWORD src1_sel:WORD_1
v_mfma_f32_16x16x32_bf16 v[52:55],a[208:211],a[20:23],v[52:55]
s_add_u32 m0,s58,18480
ds_read_u16_d16_hi v35,v35
v_lshlrev_b32_sdwa v44,1,v44 dst_sel:DWORD dst_unused:UNUSED_PAD src0_sel:DWORD src1_sel:WORD_1
v_mfma_f32_16x16x32_bf16 v[52:55],a[204:207],a[4:7],v[52:55]
s_mov_b32 s69,s88
ds_read_u16 v44,v44
v_lshlrev_b32_sdwa v46,1,v46 dst_sel:DWORD dst_unused:UNUSED_PAD src0_sel:DWORD src1_sel:WORD_1
v_mfma_f32_16x16x32_bf16 v[232:235],a[240:243],v[28:31],v[232:235]
buffer_load_dwordx4 v5,s[44:47],s69 offen offset:0 lds
v_lshlrev_b32_sdwa v45,1,v45 dst_sel:DWORD dst_unused:UNUSED_PAD src0_sel:DWORD src1_sel:WORD_1
v_mfma_f32_16x16x32_bf16 v[236:239],a[240:243],v[24:27],v[236:239]
ds_read_u16_d16_hi v46,v46
v_lshlrev_b32_sdwa v47,1,v47 dst_sel:DWORD dst_unused:UNUSED_PAD src0_sel:DWORD src1_sel:WORD_1
v_mfma_f32_16x16x32_bf16 v[164:167],a[224:227],v[24:27],v[164:167]
ds_read_u16 v45,v45
ds_read_u16_d16_hi v47,v47
v_mfma_f32_16x16x32_bf16 v[160:163],a[224:227],v[28:31],v[160:163]
ds_read_b64_tr_b16 a[248:249],v3 offset:6160
v_lshlrev_b32_sdwa v48,1,v48 dst_sel:DWORD dst_unused:UNUSED_PAD src0_sel:DWORD src1_sel:WORD_1
v_mfma_f32_16x16x32_bf16 v[240:243],a[228:231],v[28:31],v[240:243]
s_add_u32 s69,s69,s53
ds_read_u16 v48,v48
v_lshlrev_b32_sdwa v50,1,v50 dst_sel:DWORD dst_unused:UNUSED_PAD src0_sel:DWORD src1_sel:WORD_1
v_mfma_f32_16x16x32_bf16 v[244:247],a[228:231],v[24:27],v[244:247]
buffer_load_dwordx4 v5,s[44:47],s69 offen offset:1024 lds
ds_read_u16_d16_hi v50,v50
v_mfma_f32_16x16x32_bf16 v[180:183],a[232:235],v[24:27],v[180:183]
ds_read_b64_tr_b16 a[250:251],v3 offset:7696
v_lshlrev_b32_sdwa v49,1,v49 dst_sel:DWORD dst_unused:UNUSED_PAD src0_sel:DWORD src1_sel:WORD_1
v_mfma_f32_16x16x32_bf16 v[176:179],a[232:235],v[28:31],v[176:179]
ds_read_u16 v49,v49
v_lshlrev_b32_sdwa v51,1,v51 dst_sel:DWORD dst_unused:UNUSED_PAD src0_sel:DWORD src1_sel:WORD_1
v_mfma_f32_16x16x32_bf16 v[248:251],a[236:239],v[28:31],v[248:251]
ds_read_u16_d16_hi v51,v51
v_mfma_f32_16x16x32_bf16 v[252:255],a[236:239],v[24:27],v[252:255]
s_add_u32 s69,s69,s53
ds_read_b64_tr_b16 a[252:253],v3 offset:6168
v_mfma_f32_16x16x32_bf16 v[196:199],a[244:247],v[24:27],v[196:199]
buffer_load_dwordx4 v5,s[44:47],s69 offen offset:2048 lds
v_mfma_f32_16x16x32_bf16 v[192:195],a[244:247],v[28:31],v[192:195]
ds_read_b64_tr_b16 a[254:255],v3 offset:7704
v_mfma_f32_16x16x32_bf16 v[24:27],a[204:207],a[8:11],v[20:23]
ds_read_b64_tr_b16 a[216:217],v1 offset:6160
v_lshlrev_b32_sdwa v36,1,v36 dst_sel:DWORD dst_unused:UNUSED_PAD src0_sel:DWORD src1_sel:WORD_1
v_mfma_f32_16x16x32_bf16 v[24:27],a[208:211],a[24:27],v[24:27]
s_waitcnt lgkmcnt(7)
ds_read_u16 v36,v36
v_lshl_add_u64 v[60:61],v[60:61],0,v[62:63]
v_fma_mixlo_f16 v8,v57,s99,0
v_mfma_f32_16x16x32_bf16 v[24:27],a[212:215],a[40:43],v[24:27]
ds_read_b64_tr_b16 a[218:219],v1 offset:7184
v_lshl_add_u64 v[62:63],v[32:33],0,v[34:35]
v_lshlrev_b32_sdwa v38,1,v38 dst_sel:DWORD dst_unused:UNUSED_PAD src0_sel:DWORD src1_sel:WORD_1
v_mfma_f32_16x16x32_bf16 v[28:31],a[200:203],a[40:43],v[16:19]
ds_read_u16_d16_hi v38,v38
v_fma_mixhi_f16 v8,v59,s99,0
v_mfma_f32_16x16x32_bf16 v[28:31],a[196:199],a[24:27],v[28:31]
ds_read_b64_tr_b16 a[220:221],v1 offset:8208
v_lshl_add_u64 v[32:33],v[44:45],0,v[46:47]
v_lshlrev_b32_sdwa v37,1,v37 dst_sel:DWORD dst_unused:UNUSED_PAD src0_sel:DWORD src1_sel:WORD_1
v_pk_fma_f16 v9,v8,v8,s54
v_mfma_f32_16x16x32_bf16 v[28:31],a[192:195],a[8:11],v[28:31]
ds_read_u16 v37,v37
v_lshlrev_b32_sdwa v39,1,v39 dst_sel:DWORD dst_unused:UNUSED_PAD src0_sel:DWORD src1_sel:WORD_1
v_mfma_f32_16x16x32_bf16 v[44:47],a[192:195],a[12:15],v[16:19]
ds_read_u16_d16_hi v39,v39
v_pk_mul_f16 v9,v8,v9
v_mfma_f32_16x16x32_bf16 v[44:47],a[196:199],a[28:31],v[44:47]
s_waitcnt lgkmcnt(8)
ds_read_b64_tr_b16 a[222:223],v1 offset:9232
v_lshl_add_u64 v[34:35],v[48:49],0,v[50:51]
v_lshlrev_b32_sdwa v40,1,v40 dst_sel:DWORD dst_unused:UNUSED_PAD src0_sel:DWORD src1_sel:WORD_1
v_mfma_f32_16x16x32_bf16 v[44:47],a[200:203],a[44:47],v[44:47]
v_exp_f16 v8,v9
v_mfma_f32_16x16x32_bf16 v[48:51],a[212:215],a[44:47],v[20:23]
ds_read_u16 v40,v40
v_lshlrev_b32_sdwa v42,1,v42 dst_sel:DWORD dst_unused:UNUSED_PAD src0_sel:DWORD src1_sel:WORD_1
v_mfma_f32_16x16x32_bf16 v[48:51],a[208:211],a[28:31],v[48:51]
ds_read_u16_d16_hi v42,v42
v_lshlrev_b32_sdwa v41,1,v41 dst_sel:DWORD dst_unused:UNUSED_PAD src0_sel:DWORD src1_sel:WORD_1
v_mfma_f32_16x16x32_bf16 v[48:51],a[204:207],a[12:15],v[48:51]
v_exp_f16_sdwa v8,v9 dst_sel:WORD_1 dst_unused:UNUSED_PRESERVE src0_sel:WORD_1
v_mfma_f32_16x16x32_bf16 v[208:211],a[240:243],v[60:63],v[208:211]
ds_read_u16 v41,v41
v_lshlrev_b32_sdwa v43,1,v43 dst_sel:DWORD dst_unused:UNUSED_PAD src0_sel:DWORD src1_sel:WORD_1
v_mfma_f32_16x16x32_bf16 v[212:215],a[240:243],v[32:35],v[212:215]
ds_read_u16_d16_hi v43,v43
v_lshlrev_b32_sdwa v52,1,v52 dst_sel:DWORD dst_unused:UNUSED_PAD src0_sel:DWORD src1_sel:WORD_1
v_mfma_f32_16x16x32_bf16 v[172:175],a[224:227],v[32:35],v[172:175]
ds_read_u16 v52,v52
v_pk_fma_f16 v8,v8,s55,s55
v_mfma_f32_16x16x32_bf16 v[168:171],a[224:227],v[60:63],v[168:171]
ds_read_b64_tr_b16 a[240:241],v3 offset:6224
v_rcp_f16 v9,v8
v_lshlrev_b32_sdwa v54,1,v54 dst_sel:DWORD dst_unused:UNUSED_PAD src0_sel:DWORD src1_sel:WORD_1
v_mfma_f32_16x16x32_bf16 v[216:219],a[228:231],v[60:63],v[216:219]
ds_read_u16_d16_hi v54,v54
v_lshlrev_b32_sdwa v53,1,v53 dst_sel:DWORD dst_unused:UNUSED_PAD src0_sel:DWORD src1_sel:WORD_1
v_mfma_f32_16x16x32_bf16 v[220:223],a[228:231],v[32:35],v[220:223]
ds_read_u16 v53,v53
ds_read_b64_tr_b16 a[242:243],v3 offset:7760
v_mfma_f32_16x16x32_bf16 v[188:191],a[232:235],v[32:35],v[188:191]
ds_read_b64_tr_b16 a[224:225],v3 offset:6232
v_lshlrev_b32_sdwa v55,1,v55 dst_sel:DWORD dst_unused:UNUSED_PAD src0_sel:DWORD src1_sel:WORD_1
v_mfma_f32_16x16x32_bf16 v[184:187],a[232:235],v[60:63],v[184:187]
ds_read_u16_d16_hi v55,v55
v_rcp_f16_sdwa v9,v8 dst_sel:WORD_1 dst_unused:UNUSED_PRESERVE src0_sel:WORD_1
v_mfma_f32_16x16x32_bf16 v[224:227],a[236:239],v[60:63],v[224:227]
ds_read_b64_tr_b16 a[226:227],v3 offset:7768
ds_read_b64_tr_b16 a[228:229],v3 offset:6288
v_lshlrev_b32_sdwa v56,1,v56 dst_sel:DWORD dst_unused:UNUSED_PAD src0_sel:DWORD src1_sel:WORD_1
v_mfma_f32_16x16x32_bf16 v[228:231],a[236:239],v[32:35],v[228:231]
ds_read_u16 v56,v56
v_lshlrev_b32_sdwa v58,1,v58 dst_sel:DWORD dst_unused:UNUSED_PAD src0_sel:DWORD src1_sel:WORD_1
v_mfma_f32_16x16x32_bf16 v[204:207],a[244:247],v[32:35],v[204:207]
ds_read_u16_d16_hi v58,v58
v_fma_mix_f32 v57,v57,v9,0 op_sel_hi:[0,1,0]
v_mfma_f32_16x16x32_bf16 v[200:203],a[244:247],v[60:63],v[200:203]
ds_read_b64_tr_b16 a[230:231],v3 offset:7824
v_fma_mix_f32 v59,v59,v9,0 op_sel:[0,1,0] op_sel_hi:[0,1,0]
v_mfma_f32_16x16x32_bf16 v[32:35],a[204:207],a[48:51],v[20:23]
s_add_u32 m0,s58,0
ds_read_b64 v[14:15],v7 offset:6160
v_lshlrev_b32_sdwa v24,1,v24 dst_sel:DWORD dst_unused:UNUSED_PAD src0_sel:DWORD src1_sel:WORD_1
v_mfma_f32_16x16x32_bf16 v[32:35],a[208:211],a[64:67],v[32:35]
s_waitcnt lgkmcnt(10)
ds_read_b64_tr_b16 a[232:233],v3 offset:6296
v_lshl_add_u64 v[36:37],v[36:37],0,v[38:39]
v_mfma_f32_16x16x32_bf16 v[32:35],a[212:215],a[80:83],v[32:35]
s_mov_b32 s69,s87
ds_read_b64_tr_b16 a[234:235],v3 offset:7832
v_lshl_add_u64 v[38:39],v[40:41],0,v[42:43]
v_lshlrev_b32_sdwa v26,1,v26 dst_sel:DWORD dst_unused:UNUSED_PAD src0_sel:DWORD src1_sel:WORD_1
v_mfma_f32_16x16x32_bf16 v[60:63],a[200:203],a[80:83],v[16:19]
buffer_load_dwordx4 v5,s[44:47],s69 offen offset:0 lds
v_lshlrev_b32_sdwa v25,1,v25 dst_sel:DWORD dst_unused:UNUSED_PAD src0_sel:DWORD src1_sel:WORD_1
v_mfma_f32_16x16x32_bf16 v[60:63],a[196:199],a[64:67],v[60:63]
s_waitcnt lgkmcnt(8)
ds_read_u16 v24,v24
v_lshl_add_u64 v[40:41],v[52:53],0,v[54:55]
v_mfma_f32_16x16x32_bf16 v[60:63],a[192:195],a[48:51],v[60:63]
s_waitcnt lgkmcnt(3)
ds_read_u16_d16_hi v26,v26
v_or3_b32 v13,v14,v15,v13
v_lshlrev_b32_sdwa v27,1,v27 dst_sel:DWORD dst_unused:UNUSED_PAD src0_sel:DWORD src1_sel:WORD_1
v_mfma_f32_16x16x32_bf16 v[52:55],a[192:195],a[52:55],v[16:19]
s_add_u32 s69,s69,s53
ds_read_b64 v[14:15],v7 offset:6672
v_or_b32 v42,v56,v58
v_lshlrev_b32_sdwa v28,1,v28 dst_sel:DWORD dst_unused:UNUSED_PAD src0_sel:DWORD src1_sel:WORD_1
v_mfma_f32_16x16x32_bf16 v[52:55],a[196:199],a[68:71],v[52:55]
buffer_load_dwordx4 v5,s[44:47],s69 offen offset:1024 lds
v_cvt_pk_bf16_f32 v43,v57,v59
v_lshlrev_b32_sdwa v30,1,v30 dst_sel:DWORD dst_unused:UNUSED_PAD src0_sel:DWORD src1_sel:WORD_1
v_mfma_f32_16x16x32_bf16 v[52:55],a[200:203],a[84:87],v[52:55]
s_add_u32 s69,s69,s53
ds_read_u16 v25,v25
v_lshlrev_b32_sdwa v29,1,v29 dst_sel:DWORD dst_unused:UNUSED_PAD src0_sel:DWORD src1_sel:WORD_1
v_mfma_f32_16x16x32_bf16 v[56:59],a[212:215],a[84:87],v[20:23]
buffer_load_dwordx4 v5,s[44:47],s69 offen offset:2048 lds
ds_read_u16_d16_hi v27,v27
v_mfma_f32_16x16x32_bf16 v[56:59],a[208:211],a[68:71],v[56:59]
ds_read_u16 v28,v28
v_lshlrev_b32_sdwa v31,1,v31 dst_sel:DWORD dst_unused:UNUSED_PAD src0_sel:DWORD src1_sel:WORD_1
v_mfma_f32_16x16x32_bf16 v[56:59],a[204:207],a[52:55],v[56:59]
s_waitcnt lgkmcnt(3)
ds_read_u16_d16_hi v30,v30
v_or3_b32 v13,v14,v15,v13
v_mfma_f32_16x16x32_bf16 v[136:139],a[248:251],v[36:39],v[136:139]
ds_read_b64 v[14:15],v7 offset:7184
v_lshlrev_b32_sdwa v48,1,v48 dst_sel:DWORD dst_unused:UNUSED_PAD src0_sel:DWORD src1_sel:WORD_1
v_mfma_f32_16x16x32_bf16 v[140:143],a[248:251],v[40:43],v[140:143]
ds_read_u16 v29,v29
v_lshlrev_b32_sdwa v50,1,v50 dst_sel:DWORD dst_unused:UNUSED_PAD src0_sel:DWORD src1_sel:WORD_1
v_mfma_f32_16x16x32_bf16 v[68:71],a[252:255],v[40:43],v[68:71]
ds_read_u16_d16_hi v31,v31
v_lshlrev_b32_sdwa v49,1,v49 dst_sel:DWORD dst_unused:UNUSED_PAD src0_sel:DWORD src1_sel:WORD_1
v_mfma_f32_16x16x32_bf16 v[64:67],a[252:255],v[36:39],v[64:67]
ds_read_u16 v48,v48
v_lshlrev_b32_sdwa v51,1,v51 dst_sel:DWORD dst_unused:UNUSED_PAD src0_sel:DWORD src1_sel:WORD_1
v_mfma_f32_16x16x32_bf16 v[144:147],a[240:243],v[36:39],v[144:147]
ds_read_u16_d16_hi v50,v50
v_lshlrev_b32_sdwa v44,1,v44 dst_sel:DWORD dst_unused:UNUSED_PAD src0_sel:DWORD src1_sel:WORD_1
v_mfma_f32_16x16x32_bf16 v[148:151],a[240:243],v[40:43],v[148:151]
s_waitcnt lgkmcnt(4)
ds_read_u16 v49,v49
v_or3_b32 v13,v14,v15,v13
v_mfma_f32_16x16x32_bf16 v[84:87],a[224:227],v[40:43],v[84:87]
ds_read_u16_d16_hi v51,v51
v_lshlrev_b32_sdwa v46,1,v46 dst_sel:DWORD dst_unused:UNUSED_PAD src0_sel:DWORD src1_sel:WORD_1
v_mfma_f32_16x16x32_bf16 v[80:83],a[224:227],v[36:39],v[80:83]
ds_read_u16 v44,v44
v_lshlrev_b32_sdwa v45,1,v45 dst_sel:DWORD dst_unused:UNUSED_PAD src0_sel:DWORD src1_sel:WORD_1
v_mfma_f32_16x16x32_bf16 v[152:155],a[228:231],v[36:39],v[152:155]
ds_read_u16_d16_hi v46,v46
v_lshlrev_b32_sdwa v47,1,v47 dst_sel:DWORD dst_unused:UNUSED_PAD src0_sel:DWORD src1_sel:WORD_1
v_mfma_f32_16x16x32_bf16 v[156:159],a[228:231],v[40:43],v[156:159]
ds_read_u16 v45,v45
v_mfma_f32_16x16x32_bf16 v[100:103],a[232:235],v[40:43],v[100:103]
ds_read_u16_d16_hi v47,v47
v_mfma_f32_16x16x32_bf16 v[96:99],a[232:235],v[36:39],v[96:99]
v_mfma_f32_16x16x32_bf16 v[40:43],a[204:207],a[56:59],v[20:23]
v_lshlrev_b32_sdwa v32,1,v32 dst_sel:DWORD dst_unused:UNUSED_PAD src0_sel:DWORD src1_sel:WORD_1
v_fma_mixlo_f16 v8,v53,s99,0
v_mfma_f32_16x16x32_bf16 v[40:43],a[208:211],a[72:75],v[40:43]
ds_read_u16 v32,v32
v_lshl_add_u64 v[24:25],v[24:25],0,v[26:27]
v_mfma_f32_16x16x32_bf16 v[40:43],a[212:215],a[88:91],v[40:43]
s_waitcnt lgkmcnt(6)
v_lshl_add_u64 v[26:27],v[28:29],0,v[30:31]
v_lshlrev_b32_sdwa v34,1,v34 dst_sel:DWORD dst_unused:UNUSED_PAD src0_sel:DWORD src1_sel:WORD_1
v_mfma_f32_16x16x32_bf16 v[36:39],a[200:203],a[88:91],v[16:19]
ds_read_u16_d16_hi v34,v34
v_fma_mixhi_f16 v8,v55,s99,0
v_mfma_f32_16x16x32_bf16 v[36:39],a[196:199],a[72:75],v[36:39]
s_waitcnt lgkmcnt(5)
v_lshl_add_u64 v[28:29],v[48:49],0,v[50:51]
v_lshlrev_b32_sdwa v33,1,v33 dst_sel:DWORD dst_unused:UNUSED_PAD src0_sel:DWORD src1_sel:WORD_1
v_pk_fma_f16 v9,v8,v8,s54
v_mfma_f32_16x16x32_bf16 v[36:39],a[192:195],a[56:59],v[36:39]
ds_read_u16 v33,v33
v_lshlrev_b32_sdwa v35,1,v35 dst_sel:DWORD dst_unused:UNUSED_PAD src0_sel:DWORD src1_sel:WORD_1
v_mfma_f32_16x16x32_bf16 v[48:51],a[192:195],a[60:63],v[16:19]
ds_read_b128 v[16:19],v4 offset:1280
v_pk_mul_f16 v9,v8,v9
v_mfma_f32_16x16x32_bf16 v[48:51],a[196:199],a[76:79],v[48:51]
s_waitcnt lgkmcnt(4)
ds_read_u16_d16_hi v35,v35
ds_read_b64_tr_b16 a[192:193],v1 offset:10256
v_lshl_add_u64 v[30:31],v[44:45],0,v[46:47]
v_lshlrev_b32_sdwa v60,1,v60 dst_sel:DWORD dst_unused:UNUSED_PAD src0_sel:DWORD src1_sel:WORD_1
v_mfma_f32_16x16x32_bf16 v[48:51],a[200:203],a[92:95],v[48:51]
ds_read_b64_tr_b16 a[194:195],v1 offset:11280
v_exp_f16_e64 v8,v9
v_mfma_f32_16x16x32_bf16 v[44:47],a[212:215],a[92:95],v[20:23]
ds_read_b128 v[20:23],v4 offset:1312
ds_read_u16 v60,v60
v_lshlrev_b32_sdwa v62,1,v62 dst_sel:DWORD dst_unused:UNUSED_PAD src0_sel:DWORD src1_sel:WORD_1
v_mfma_f32_16x16x32_bf16 v[44:47],a[208:211],a[76:79],v[44:47]
ds_read_u16_d16_hi v62,v62
ds_read_b64_tr_b16 a[196:197],v2 offset:10256
v_lshlrev_b32_sdwa v61,1,v61 dst_sel:DWORD dst_unused:UNUSED_PAD src0_sel:DWORD src1_sel:WORD_1
v_mfma_f32_16x16x32_bf16 v[44:47],a[204:207],a[60:63],v[44:47]
ds_read_u16 v61,v61
v_exp_f16_sdwa v8,v9 dst_sel:WORD_1 dst_unused:UNUSED_PRESERVE src0_sel:WORD_1
v_mfma_f32_16x16x32_bf16 v[112:115],a[248:251],v[24:27],v[112:115]
ds_read_b64_tr_b16 a[198:199],v2 offset:11280
ds_read_b64_tr_b16 a[200:201],v2 offset:8208
v_lshlrev_b32_sdwa v63,1,v63 dst_sel:DWORD dst_unused:UNUSED_PAD src0_sel:DWORD src1_sel:WORD_1
v_mfma_f32_16x16x32_bf16 v[116:119],a[248:251],v[28:31],v[116:119]
ds_read_u16_d16_hi v63,v63
v_lshlrev_b32_sdwa v56,1,v56 dst_sel:DWORD dst_unused:UNUSED_PAD src0_sel:DWORD src1_sel:WORD_1
v_mfma_f32_16x16x32_bf16 v[76:79],a[252:255],v[28:31],v[76:79]
ds_read_b64_tr_b16 a[202:203],v2 offset:9232
v_pk_fma_f16 v8,v8,s55,s55
v_mfma_f32_16x16x32_bf16 v[72:75],a[252:255],v[24:27],v[72:75]
v_rcp_f16 v9,v8
v_lshlrev_b32_sdwa v58,1,v58 dst_sel:DWORD dst_unused:UNUSED_PAD src0_sel:DWORD src1_sel:WORD_1
v_mfma_f32_16x16x32_bf16 v[120:123],a[240:243],v[24:27],v[120:123]
ds_read_u16 v56,v56
v_lshlrev_b32_sdwa v57,1,v57 dst_sel:DWORD dst_unused:UNUSED_PAD src0_sel:DWORD src1_sel:WORD_1
v_mfma_f32_16x16x32_bf16 v[124:127],a[240:243],v[28:31],v[124:127]
ds_read_u16_d16_hi v58,v58
v_lshlrev_b32_sdwa v59,1,v59 dst_sel:DWORD dst_unused:UNUSED_PAD src0_sel:DWORD src1_sel:WORD_1
v_mfma_f32_16x16x32_bf16 v[92:95],a[224:227],v[28:31],v[92:95]
ds_read_u16 v57,v57
v_lshlrev_b32_sdwa v52,1,v52 dst_sel:DWORD dst_unused:UNUSED_PAD src0_sel:DWORD src1_sel:WORD_1
v_mfma_f32_16x16x32_bf16 v[88:91],a[224:227],v[24:27],v[88:91]
ds_read_u16_d16_hi v59,v59
v_rcp_f16_sdwa v9,v8 dst_sel:WORD_1 dst_unused:UNUSED_PRESERVE src0_sel:WORD_1
v_mfma_f32_16x16x32_bf16 v[128:131],a[228:231],v[24:27],v[128:131]
ds_read_b64_tr_b16 a[212:213],v2 offset:6160
v_lshlrev_b32_sdwa v54,1,v54 dst_sel:DWORD dst_unused:UNUSED_PAD src0_sel:DWORD src1_sel:WORD_1
v_mfma_f32_16x16x32_bf16 v[132:135],a[228:231],v[28:31],v[132:135]
ds_read_b64_tr_b16 a[214:215],v2 offset:7184
v_fma_mix_f32 v53,v53,v9,0 op_sel_hi:[0,1,0]
v_mfma_f32_16x16x32_bf16 v[108:111],a[232:235],v[28:31],v[108:111]
ds_read_u16 v52,v52
v_fma_mix_f32 v55,v55,v9,0 op_sel:[0,1,0] op_sel_hi:[0,1,0]
v_mfma_f32_16x16x32_bf16 v[104:107],a[232:235],v[24:27],v[104:107]
s_waitcnt lgkmcnt(7)
ds_read_u16_d16_hi v54,v54
v_mfma_f32_16x16x32_bf16 v[28:31],a[216:219],a[0:3],v[16:19]
v_lshlrev_b32_sdwa v40,1,v40 dst_sel:DWORD dst_unused:UNUSED_PAD src0_sel:DWORD src1_sel:WORD_1
v_lshlrev_b32_sdwa v42,1,v42 dst_sel:DWORD dst_unused:UNUSED_PAD src0_sel:DWORD src1_sel:WORD_1
v_mfma_f32_16x16x32_bf16 v[28:31],a[220:223],a[16:19],v[28:31]
ds_read_u16 v40,v40
v_lshl_add_u64 v[32:33],v[32:33],0,v[34:35]
v_mfma_f32_16x16x32_bf16 v[28:31],a[192:195],a[32:35],v[28:31]
ds_read_u16_d16_hi v42,v42
v_lshl_add_u64 v[34:35],v[60:61],0,v[62:63]
v_mfma_f32_16x16x32_bf16 v[24:27],a[196:199],a[32:35],v[20:23]
v_lshlrev_b32_sdwa v41,1,v41 dst_sel:DWORD dst_unused:UNUSED_PAD src0_sel:DWORD src1_sel:WORD_1
v_lshlrev_b32_sdwa v43,1,v43 dst_sel:DWORD dst_unused:UNUSED_PAD src0_sel:DWORD src1_sel:WORD_1
v_mfma_f32_16x16x32_bf16 v[24:27],a[200:203],a[16:19],v[24:27]
s_waitcnt lgkmcnt(4)
ds_read_u16 v41,v41
v_lshl_add_u64 v[60:61],v[56:57],0,v[58:59]
v_mfma_f32_16x16x32_bf16 v[24:27],a[212:215],a[0:3],v[24:27]
ds_read_u16_d16_hi v43,v43
v_lshlrev_b32_sdwa v36,1,v36 dst_sel:DWORD dst_unused:UNUSED_PAD src0_sel:DWORD src1_sel:WORD_1
v_mfma_f32_16x16x32_bf16 v[56:59],a[212:215],a[4:7],v[20:23]
s_waitcnt lgkmcnt(4)
ds_read_u16 v36,v36
v_or_b32 v62,v52,v54
v_mfma_f32_16x16x32_bf16 v[56:59],a[200:203],a[20:23],v[56:59]
v_cvt_pk_bf16_f32 v63,v53,v55
v_lshlrev_b32_sdwa v38,1,v38 dst_sel:DWORD dst_unused:UNUSED_PAD src0_sel:DWORD src1_sel:WORD_1
v_mfma_f32_16x16x32_bf16 v[56:59],a[196:199],a[36:39],v[56:59]
ds_read_u16_d16_hi v38,v38
v_lshlrev_b32_sdwa v37,1,v37 dst_sel:DWORD dst_unused:UNUSED_PAD src0_sel:DWORD src1_sel:WORD_1
v_mfma_f32_16x16x32_bf16 v[52:55],a[192:195],a[36:39],v[16:19]
ds_read_u16 v37,v37
v_lshlrev_b32_sdwa v39,1,v39 dst_sel:DWORD dst_unused:UNUSED_PAD src0_sel:DWORD src1_sel:WORD_1
v_mfma_f32_16x16x32_bf16 v[52:55],a[220:223],a[20:23],v[52:55]
ds_read_u16_d16_hi v39,v39
v_lshlrev_b32_sdwa v44,1,v44 dst_sel:DWORD dst_unused:UNUSED_PAD src0_sel:DWORD src1_sel:WORD_1
v_mfma_f32_16x16x32_bf16 v[52:55],a[216:219],a[4:7],v[52:55]
ds_read_u16 v44,v44
v_lshlrev_b32_sdwa v46,1,v46 dst_sel:DWORD dst_unused:UNUSED_PAD src0_sel:DWORD src1_sel:WORD_1
v_mfma_f32_16x16x32_bf16 v[232:235],a[248:251],v[32:35],v[232:235]
ds_read_u16_d16_hi v46,v46
v_lshlrev_b32_sdwa v45,1,v45 dst_sel:DWORD dst_unused:UNUSED_PAD src0_sel:DWORD src1_sel:WORD_1
v_mfma_f32_16x16x32_bf16 v[236:239],a[248:251],v[60:63],v[236:239]
ds_read_u16 v45,v45
v_lshlrev_b32_sdwa v47,1,v47 dst_sel:DWORD dst_unused:UNUSED_PAD src0_sel:DWORD src1_sel:WORD_1
v_mfma_f32_16x16x32_bf16 v[164:167],a[252:255],v[60:63],v[164:167]
ds_read_u16_d16_hi v47,v47
ds_read_b64_tr_b16 a[236:237],v3 offset:12320
v_mfma_f32_16x16x32_bf16 v[160:163],a[252:255],v[32:35],v[160:163]
ds_read_b64_tr_b16 a[238:239],v3 offset:13856
v_lshlrev_b32_sdwa v48,1,v48 dst_sel:DWORD dst_unused:UNUSED_PAD src0_sel:DWORD src1_sel:WORD_1
v_mfma_f32_16x16x32_bf16 v[240:243],a[240:243],v[32:35],v[240:243]
ds_read_u16 v48,v48
v_lshlrev_b32_sdwa v50,1,v50 dst_sel:DWORD dst_unused:UNUSED_PAD src0_sel:DWORD src1_sel:WORD_1
v_mfma_f32_16x16x32_bf16 v[244:247],a[240:243],v[60:63],v[244:247]
ds_read_u16_d16_hi v50,v50
v_lshlrev_b32_sdwa v49,1,v49 dst_sel:DWORD dst_unused:UNUSED_PAD src0_sel:DWORD src1_sel:WORD_1
v_mfma_f32_16x16x32_bf16 v[180:183],a[224:227],v[60:63],v[180:183]
ds_read_u16 v49,v49
v_lshlrev_b32_sdwa v51,1,v51 dst_sel:DWORD dst_unused:UNUSED_PAD src0_sel:DWORD src1_sel:WORD_1
v_mfma_f32_16x16x32_bf16 v[176:179],a[224:227],v[32:35],v[176:179]
ds_read_u16_d16_hi v51,v51
v_mfma_f32_16x16x32_bf16 v[248:251],a[228:231],v[32:35],v[248:251]
ds_read_b64_tr_b16 a[244:245],v3 offset:12328
v_mfma_f32_16x16x32_bf16 v[252:255],a[228:231],v[60:63],v[252:255]
ds_read_b64_tr_b16 a[246:247],v3 offset:13864
v_mfma_f32_16x16x32_bf16 v[196:199],a[232:235],v[60:63],v[196:199]
v_mfma_f32_16x16x32_bf16 v[192:195],a[232:235],v[32:35],v[192:195]
v_mfma_f32_16x16x32_bf16 v[60:63],a[216:219],a[8:11],v[16:19]
ds_read_b64_tr_b16 a[208:209],v1 offset:12320
v_lshlrev_b32_sdwa v28,1,v28 dst_sel:DWORD dst_unused:UNUSED_PAD src0_sel:DWORD src1_sel:WORD_1
v_mfma_f32_16x16x32_bf16 v[60:63],a[220:223],a[24:27],v[60:63]
s_waitcnt lgkmcnt(6)
ds_read_u16 v28,v28
v_lshl_add_u64 v[40:41],v[40:41],0,v[42:43]
v_fma_mixlo_f16 v8,v57,s99,0
v_mfma_f32_16x16x32_bf16 v[60:63],a[192:195],a[40:43],v[60:63]
ds_read_b64_tr_b16 a[210:211],v1 offset:13344
v_lshl_add_u64 v[42:43],v[36:37],0,v[38:39]
v_lshlrev_b32_sdwa v30,1,v30 dst_sel:DWORD dst_unused:UNUSED_PAD src0_sel:DWORD src1_sel:WORD_1
v_mfma_f32_16x16x32_bf16 v[32:35],a[196:199],a[40:43],v[20:23]
ds_read_u16_d16_hi v30,v30
v_fma_mixhi_f16 v8,v59,s99,0
v_mfma_f32_16x16x32_bf16 v[32:35],a[200:203],a[24:27],v[32:35]
ds_read_b64_tr_b16 a[204:205],v1 offset:14368
v_lshl_add_u64 v[36:37],v[44:45],0,v[46:47]
v_lshlrev_b32_sdwa v29,1,v29 dst_sel:DWORD dst_unused:UNUSED_PAD src0_sel:DWORD src1_sel:WORD_1
v_pk_fma_f16 v9,v8,v8,s54
v_mfma_f32_16x16x32_bf16 v[32:35],a[212:215],a[8:11],v[32:35]
ds_read_u16 v29,v29
v_lshlrev_b32_sdwa v31,1,v31 dst_sel:DWORD dst_unused:UNUSED_PAD src0_sel:DWORD src1_sel:WORD_1
v_mfma_f32_16x16x32_bf16 v[44:47],a[212:215],a[12:15],v[20:23]
ds_read_u16_d16_hi v31,v31
v_pk_mul_f16 v9,v8,v9
v_mfma_f32_16x16x32_bf16 v[44:47],a[200:203],a[28:31],v[44:47]
s_waitcnt lgkmcnt(7)
ds_read_b64_tr_b16 a[206:207],v1 offset:15392
v_lshl_add_u64 v[38:39],v[48:49],0,v[50:51]
v_lshlrev_b32_sdwa v24,1,v24 dst_sel:DWORD dst_unused:UNUSED_PAD src0_sel:DWORD src1_sel:WORD_1
v_mfma_f32_16x16x32_bf16 v[44:47],a[196:199],a[44:47],v[44:47]
v_exp_f16 v8,v9
v_mfma_f32_16x16x32_bf16 v[48:51],a[192:195],a[44:47],v[16:19]
ds_read_u16 v24,v24
v_lshlrev_b32_sdwa v26,1,v26 dst_sel:DWORD dst_unused:UNUSED_PAD src0_sel:DWORD src1_sel:WORD_1
v_mfma_f32_16x16x32_bf16 v[48:51],a[220:223],a[28:31],v[48:51]
ds_read_u16_d16_hi v26,v26
v_lshlrev_b32_sdwa v25,1,v25 dst_sel:DWORD dst_unused:UNUSED_PAD src0_sel:DWORD src1_sel:WORD_1
v_mfma_f32_16x16x32_bf16 v[48:51],a[216:219],a[12:15],v[48:51]
v_exp_f16_sdwa v8,v9 dst_sel:WORD_1 dst_unused:UNUSED_PRESERVE src0_sel:WORD_1
v_mfma_f32_16x16x32_bf16 v[208:211],a[248:251],v[40:43],v[208:211]
ds_read_u16 v25,v25
v_lshlrev_b32_sdwa v27,1,v27 dst_sel:DWORD dst_unused:UNUSED_PAD src0_sel:DWORD src1_sel:WORD_1
v_mfma_f32_16x16x32_bf16 v[212:215],a[248:251],v[36:39],v[212:215]
ds_read_u16_d16_hi v27,v27
v_lshlrev_b32_sdwa v52,1,v52 dst_sel:DWORD dst_unused:UNUSED_PAD src0_sel:DWORD src1_sel:WORD_1
v_mfma_f32_16x16x32_bf16 v[172:175],a[252:255],v[36:39],v[172:175]
ds_read_u16 v52,v52
v_pk_fma_f16 v8,v8,s55,s55
v_mfma_f32_16x16x32_bf16 v[168:171],a[252:255],v[40:43],v[168:171]
TP4345_%=:
s_cbranch_execz TP6487_%=
ds_read_b64_tr_b16 a[248:249],v3 offset:12384
v_rcp_f16 v9,v8
v_lshlrev_b32_sdwa v54,1,v54 dst_sel:DWORD dst_unused:UNUSED_PAD src0_sel:DWORD src1_sel:WORD_1
v_mfma_f32_16x16x32_bf16 v[216:219],a[240:243],v[40:43],v[216:219]
ds_read_u16_d16_hi v54,v54
v_lshlrev_b32_sdwa v53,1,v53 dst_sel:DWORD dst_unused:UNUSED_PAD src0_sel:DWORD src1_sel:WORD_1
v_mfma_f32_16x16x32_bf16 v[220:223],a[240:243],v[36:39],v[220:223]
ds_read_u16 v53,v53
ds_read_b64_tr_b16 a[250:251],v3 offset:13920
v_mfma_f32_16x16x32_bf16 v[188:191],a[224:227],v[36:39],v[188:191]
ds_read_b64_tr_b16 a[252:253],v3 offset:12392
v_lshlrev_b32_sdwa v55,1,v55 dst_sel:DWORD dst_unused:UNUSED_PAD src0_sel:DWORD src1_sel:WORD_1
v_mfma_f32_16x16x32_bf16 v[184:187],a[224:227],v[40:43],v[184:187]
ds_read_u16_d16_hi v55,v55
v_rcp_f16_sdwa v9,v8 dst_sel:WORD_1 dst_unused:UNUSED_PRESERVE src0_sel:WORD_1
v_mfma_f32_16x16x32_bf16 v[224:227],a[228:231],v[40:43],v[224:227]
ds_read_b64_tr_b16 a[254:255],v3 offset:13928
ds_read_b64_tr_b16 a[240:241],v3 offset:12448
v_lshlrev_b32_sdwa v56,1,v56 dst_sel:DWORD dst_unused:UNUSED_PAD src0_sel:DWORD src1_sel:WORD_1
v_mfma_f32_16x16x32_bf16 v[228:231],a[228:231],v[36:39],v[228:231]
ds_read_u16 v56,v56
v_lshlrev_b32_sdwa v58,1,v58 dst_sel:DWORD dst_unused:UNUSED_PAD src0_sel:DWORD src1_sel:WORD_1
v_mfma_f32_16x16x32_bf16 v[204:207],a[232:235],v[36:39],v[204:207]
ds_read_u16_d16_hi v58,v58
v_fma_mix_f32 v57,v57,v9,0 op_sel_hi:[0,1,0]
v_mfma_f32_16x16x32_bf16 v[200:203],a[232:235],v[40:43],v[200:203]
ds_read_b64_tr_b16 a[242:243],v3 offset:13984
v_fma_mix_f32 v59,v59,v9,0 op_sel:[0,1,0] op_sel_hi:[0,1,0]
v_mbcnt_lo_u32_b32 v8,-1,0
N_qs10_%=:
s_add_u32 s64,s16,N_qs10_%=-N_pf_%=+0
v_and_b32_e64 v8,3,v8
s_mov_b32 m0,12288
v_lshl_add_u32 v8,v8,7,s64
v_min_u32 v8,N_mainend_%=-N_pf_%=+2048,v8
global_load_lds_dword v8,s[14:15]
v_mfma_f32_16x16x32_bf16 v[36:39],a[216:219],a[48:51],v[16:19]
ds_read_b64 v[14:15],v7 offset:12320
v_lshlrev_b32_sdwa v60,1,v60 dst_sel:DWORD dst_unused:UNUSED_PAD src0_sel:DWORD src1_sel:WORD_1
v_mfma_f32_16x16x32_bf16 v[36:39],a[220:223],a[64:67],v[36:39]
s_waitcnt lgkmcnt(10)
ds_read_b64_tr_b16 a[224:225],v3 offset:12456
v_lshl_add_u64 v[28:29],v[28:29],0,v[30:31]
v_mfma_f32_16x16x32_bf16 v[36:39],a[192:195],a[80:83],v[36:39]
ds_read_b64_tr_b16 a[226:227],v3 offset:13992
v_lshl_add_u64 v[30:31],v[24:25],0,v[26:27]
v_lshlrev_b32_sdwa v62,1,v62 dst_sel:DWORD dst_unused:UNUSED_PAD src0_sel:DWORD src1_sel:WORD_1
v_mfma_f32_16x16x32_bf16 v[40:43],a[196:199],a[80:83],v[20:23]
ds_read_u16 v60,v60
v_lshlrev_b32_sdwa v61,1,v61 dst_sel:DWORD dst_unused:UNUSED_PAD src0_sel:DWORD src1_sel:WORD_1
v_mfma_f32_16x16x32_bf16 v[40:43],a[200:203],a[64:67],v[40:43]
s_waitcnt lgkmcnt(9)
ds_read_u16_d16_hi v62,v62
v_lshl_add_u64 v[24:25],v[52:53],0,v[54:55]
v_mfma_f32_16x16x32_bf16 v[40:43],a[212:215],a[48:51],v[40:43]
s_waitcnt lgkmcnt(4)
ds_read_u16 v61,v61
v_or3_b32 v13,v14,v15,v13
v_lshlrev_b32_sdwa v63,1,v63 dst_sel:DWORD dst_unused:UNUSED_PAD src0_sel:DWORD src1_sel:WORD_1
v_mfma_f32_16x16x32_bf16 v[52:55],a[212:215],a[52:55],v[20:23]
ds_read_b64 v[14:15],v7 offset:12832
v_or_b32 v26,v56,v58
v_lshlrev_b32_sdwa v32,1,v32 dst_sel:DWORD dst_unused:UNUSED_PAD src0_sel:DWORD src1_sel:WORD_1
v_mfma_f32_16x16x32_bf16 v[52:55],a[200:203],a[68:71],v[52:55]
ds_read_u16_d16_hi v63,v63
v_cvt_pk_bf16_f32 v27,v57,v59
v_lshlrev_b32_sdwa v34,1,v34 dst_sel:DWORD dst_unused:UNUSED_PAD src0_sel:DWORD src1_sel:WORD_1
v_mfma_f32_16x16x32_bf16 v[52:55],a[196:199],a[84:87],v[52:55]
ds_read_u16 v32,v32
v_lshlrev_b32_sdwa v33,1,v33 dst_sel:DWORD dst_unused:UNUSED_PAD src0_sel:DWORD src1_sel:WORD_1
v_mfma_f32_16x16x32_bf16 v[56:59],a[192:195],a[84:87],v[16:19]
ds_read_u16_d16_hi v34,v34
v_lshlrev_b32_sdwa v35,1,v35 dst_sel:DWORD dst_unused:UNUSED_PAD src0_sel:DWORD src1_sel:WORD_1
v_mfma_f32_16x16x32_bf16 v[56:59],a[220:223],a[68:71],v[56:59]
ds_read_u16 v33,v33
v_lshlrev_b32_sdwa v48,1,v48 dst_sel:DWORD dst_unused:UNUSED_PAD src0_sel:DWORD src1_sel:WORD_1
v_mfma_f32_16x16x32_bf16 v[56:59],a[216:219],a[52:55],v[56:59]
s_waitcnt lgkmcnt(4)
ds_read_u16_d16_hi v35,v35
v_or3_b32 v13,v14,v15,v13
v_mfma_f32_16x16x32_bf16 v[136:139],a[236:239],v[28:31],v[136:139]
ds_read_b64 v[14:15],v7 offset:13344
v_lshlrev_b32_sdwa v50,1,v50 dst_sel:DWORD dst_unused:UNUSED_PAD src0_sel:DWORD src1_sel:WORD_1
v_mfma_f32_16x16x32_bf16 v[140:143],a[236:239],v[24:27],v[140:143]
ds_read_u16 v48,v48
v_lshlrev_b32_sdwa v49,1,v49 dst_sel:DWORD dst_unused:UNUSED_PAD src0_sel:DWORD src1_sel:WORD_1
v_mfma_f32_16x16x32_bf16 v[68:71],a[244:247],v[24:27],v[68:71]
ds_read_u16_d16_hi v50,v50
v_lshlrev_b32_sdwa v51,1,v51 dst_sel:DWORD dst_unused:UNUSED_PAD src0_sel:DWORD src1_sel:WORD_1
v_mfma_f32_16x16x32_bf16 v[64:67],a[244:247],v[28:31],v[64:67]
ds_read_u16 v49,v49
v_lshlrev_b32_sdwa v44,1,v44 dst_sel:DWORD dst_unused:UNUSED_PAD src0_sel:DWORD src1_sel:WORD_1
v_mfma_f32_16x16x32_bf16 v[144:147],a[248:251],v[28:31],v[144:147]
ds_read_u16_d16_hi v51,v51
v_lshlrev_b32_sdwa v46,1,v46 dst_sel:DWORD dst_unused:UNUSED_PAD src0_sel:DWORD src1_sel:WORD_1
v_mfma_f32_16x16x32_bf16 v[148:151],a[248:251],v[24:27],v[148:151]
s_waitcnt lgkmcnt(4)
ds_read_u16 v44,v44
v_or3_b32 v13,v14,v15,v13
v_mfma_f32_16x16x32_bf16 v[84:87],a[252:255],v[24:27],v[84:87]
ds_read_u16_d16_hi v46,v46
v_and_b32 v13,0x40004000,v13
v_mfma_f32_16x16x32_bf16 v[80:83],a[252:255],v[28:31],v[80:83]
v_cmp_ne_u32_e32 vcc,0,v13
s_nop 0
s_or_b64 s[90:91],s[90:91],vcc
v_lshlrev_b32_sdwa v45,1,v45 dst_sel:DWORD dst_unused:UNUSED_PAD src0_sel:DWORD src1_sel:WORD_1
v_mfma_f32_16x16x32_bf16 v[152:155],a[240:243],v[28:31],v[152:155]
ds_read_u16 v45,v45
v_mbcnt_lo_u32_b32 v15,-1,0
v_mfma_f32_16x16x32_bf16 v[156:159],a[240:243],v[24:27],v[156:159]
v_mbcnt_hi_u32_b32 v15,-1,v15
v_lshlrev_b32_sdwa v47,1,v47 dst_sel:DWORD dst_unused:UNUSED_PAD src0_sel:DWORD src1_sel:WORD_1
v_mfma_f32_16x16x32_bf16 v[100:103],a[224:227],v[24:27],v[100:103]
ds_read_u16_d16_hi v47,v47
v_lshlrev_b32 v15,2,v15
v_mfma_f32_16x16x32_bf16 v[96:99],a[224:227],v[28:31],v[96:99]
ds_read_b32 v7,v15 offset:14848
ds_read_b32 v13,v15 offset:15104
v_mfma_f32_16x16x32_bf16 v[24:27],a[216:219],a[56:59],v[16:19]
ds_read_b32 v14,v15 offset:14336
ds_read_b32 v15,v15 offset:14592
v_lshlrev_b32_sdwa v36,1,v36 dst_sel:DWORD dst_unused:UNUSED_PAD src0_sel:DWORD src1_sel:WORD_1
v_mfma_f32_16x16x32_bf16 v[24:27],a[220:223],a[72:75],v[24:27]
ds_read_u16 v36,v36
v_lshl_add_u64 v[60:61],v[60:61],0,v[62:63]
v_fma_mixlo_f16 v8,v53,s99,0
v_mfma_f32_16x16x32_bf16 v[24:27],a[192:195],a[88:91],v[24:27]
v_lshl_add_u64 v[62:63],v[32:33],0,v[34:35]
v_lshlrev_b32_sdwa v38,1,v38 dst_sel:DWORD dst_unused:UNUSED_PAD src0_sel:DWORD src1_sel:WORD_1
v_mfma_f32_16x16x32_bf16 v[28:31],a[196:199],a[88:91],v[20:23]
ds_read_u16_d16_hi v38,v38
v_fma_mixhi_f16 v8,v55,s99,0
v_mfma_f32_16x16x32_bf16 v[28:31],a[200:203],a[72:75],v[28:31]
s_waitcnt lgkmcnt(8)
v_lshl_add_u64 v[32:33],v[48:49],0,v[50:51]
v_lshlrev_b32_sdwa v37,1,v37 dst_sel:DWORD dst_unused:UNUSED_PAD src0_sel:DWORD src1_sel:WORD_1
v_pk_fma_f16 v9,v8,v8,s54
v_mfma_f32_16x16x32_bf16 v[28:31],a[212:215],a[56:59],v[28:31]
ds_read_u16 v37,v37
v_lshlrev_b32_sdwa v39,1,v39 dst_sel:DWORD dst_unused:UNUSED_PAD src0_sel:DWORD src1_sel:WORD_1
v_mfma_f32_16x16x32_bf16 v[48:51],a[212:215],a[60:63],v[20:23]
ds_read_b128 v[20:23],v4 offset:1408
v_pk_mul_f16 v9,v8,v9
v_mfma_f32_16x16x32_bf16 v[48:51],a[200:203],a[76:79],v[48:51]
s_waitcnt lgkmcnt(8)
ds_read_u16_d16_hi v39,v39
ds_read_b64_tr_b16 a[212:213],v1 offset:16416
v_lshl_add_u64 v[34:35],v[44:45],0,v[46:47]
v_lshlrev_b32_sdwa v40,1,v40 dst_sel:DWORD dst_unused:UNUSED_PAD src0_sel:DWORD src1_sel:WORD_1
v_mfma_f32_16x16x32_bf16 v[48:51],a[196:199],a[92:95],v[48:51]
ds_read_b64_tr_b16 a[214:215],v1 offset:17440
v_exp_f16 v8,v9
v_mfma_f32_16x16x32_bf16 v[44:47],a[192:195],a[92:95],v[16:19]
ds_read_b128 v[16:19],v4 offset:1440
ds_read_u16 v40,v40
v_lshlrev_b32_sdwa v42,1,v42 dst_sel:DWORD dst_unused:UNUSED_PAD src0_sel:DWORD src1_sel:WORD_1
v_mfma_f32_16x16x32_bf16 v[44:47],a[220:223],a[76:79],v[44:47]
ds_read_u16_d16_hi v42,v42
ds_read_b64_tr_b16 a[200:201],v2 offset:16416
v_lshlrev_b32_sdwa v41,1,v41 dst_sel:DWORD dst_unused:UNUSED_PAD src0_sel:DWORD src1_sel:WORD_1
v_mfma_f32_16x16x32_bf16 v[44:47],a[216:219],a[60:63],v[44:47]
ds_read_u16 v41,v41
v_exp_f16_sdwa v8,v9 dst_sel:WORD_1 dst_unused:UNUSED_PRESERVE src0_sel:WORD_1
v_mfma_f32_16x16x32_bf16 v[112:115],a[236:239],v[60:63],v[112:115]
ds_read_b64_tr_b16 a[202:203],v2 offset:17440
ds_read_b64_tr_b16 a[196:197],v2 offset:14368
v_lshlrev_b32_sdwa v43,1,v43 dst_sel:DWORD dst_unused:UNUSED_PAD src0_sel:DWORD src1_sel:WORD_1
v_mfma_f32_16x16x32_bf16 v[116:119],a[236:239],v[32:35],v[116:119]
ds_read_u16_d16_hi v43,v43
v_lshlrev_b32_sdwa v56,1,v56 dst_sel:DWORD dst_unused:UNUSED_PAD src0_sel:DWORD src1_sel:WORD_1
v_mfma_f32_16x16x32_bf16 v[76:79],a[244:247],v[32:35],v[76:79]
ds_read_b64_tr_b16 a[198:199],v2 offset:15392
v_pk_fma_f16 v8,v8,s55,s55
v_mfma_f32_16x16x32_bf16 v[72:75],a[244:247],v[60:63],v[72:75]
v_rcp_f16 v9,v8
v_lshlrev_b32_sdwa v58,1,v58 dst_sel:DWORD dst_unused:UNUSED_PAD src0_sel:DWORD src1_sel:WORD_1
v_mfma_f32_16x16x32_bf16 v[120:123],a[248:251],v[60:63],v[120:123]
ds_read_u16 v56,v56
v_lshlrev_b32_sdwa v57,1,v57 dst_sel:DWORD dst_unused:UNUSED_PAD src0_sel:DWORD src1_sel:WORD_1
v_mfma_f32_16x16x32_bf16 v[124:127],a[248:251],v[32:35],v[124:127]
ds_read_u16_d16_hi v58,v58
v_lshlrev_b32_sdwa v59,1,v59 dst_sel:DWORD dst_unused:UNUSED_PAD src0_sel:DWORD src1_sel:WORD_1
v_mfma_f32_16x16x32_bf16 v[92:95],a[252:255],v[32:35],v[92:95]
ds_read_u16 v57,v57
v_lshlrev_b32_sdwa v52,1,v52 dst_sel:DWORD dst_unused:UNUSED_PAD src0_sel:DWORD src1_sel:WORD_1
v_mfma_f32_16x16x32_bf16 v[88:91],a[252:255],v[60:63],v[88:91]
ds_read_u16_d16_hi v59,v59
v_rcp_f16_sdwa v9,v8 dst_sel:WORD_1 dst_unused:UNUSED_PRESERVE src0_sel:WORD_1
v_mfma_f32_16x16x32_bf16 v[128:131],a[240:243],v[60:63],v[128:131]
ds_read_b64_tr_b16 a[192:193],v2 offset:12320
v_lshlrev_b32_sdwa v54,1,v54 dst_sel:DWORD dst_unused:UNUSED_PAD src0_sel:DWORD src1_sel:WORD_1
v_mfma_f32_16x16x32_bf16 v[132:135],a[240:243],v[32:35],v[132:135]
ds_read_b64_tr_b16 a[194:195],v2 offset:13344
v_fma_mix_f32 v53,v53,v9,0 op_sel_hi:[0,1,0]
v_mfma_f32_16x16x32_bf16 v[108:111],a[224:227],v[32:35],v[108:111]
ds_read_u16 v52,v52
v_fma_mix_f32 v55,v55,v9,0 op_sel:[0,1,0] op_sel_hi:[0,1,0]
v_mfma_f32_16x16x32_bf16 v[104:107],a[224:227],v[60:63],v[104:107]
s_waitcnt lgkmcnt(7)
ds_read_u16_d16_hi v54,v54
v_mfma_f32_16x16x32_bf16 v[32:35],a[208:211],a[0:3],v[20:23]
v_lshlrev_b32_sdwa v24,1,v24 dst_sel:DWORD dst_unused:UNUSED_PAD src0_sel:DWORD src1_sel:WORD_1
v_lshlrev_b32_sdwa v26,1,v26 dst_sel:DWORD dst_unused:UNUSED_PAD src0_sel:DWORD src1_sel:WORD_1
v_mfma_f32_16x16x32_bf16 v[32:35],a[204:207],a[16:19],v[32:35]
ds_read_u16 v24,v24
v_lshl_add_u64 v[36:37],v[36:37],0,v[38:39]
v_mfma_f32_16x16x32_bf16 v[32:35],a[212:215],a[32:35],v[32:35]
ds_read_u16_d16_hi v26,v26
v_lshl_add_u64 v[38:39],v[40:41],0,v[42:43]
v_mfma_f32_16x16x32_bf16 v[60:63],a[200:203],a[32:35],v[16:19]
v_lshlrev_b32_sdwa v25,1,v25 dst_sel:DWORD dst_unused:UNUSED_PAD src0_sel:DWORD src1_sel:WORD_1
v_lshlrev_b32_sdwa v27,1,v27 dst_sel:DWORD dst_unused:UNUSED_PAD src0_sel:DWORD src1_sel:WORD_1
v_mfma_f32_16x16x32_bf16 v[60:63],a[196:199],a[16:19],v[60:63]
s_waitcnt lgkmcnt(4)
ds_read_u16 v25,v25
v_lshl_add_u64 v[40:41],v[56:57],0,v[58:59]
v_mfma_f32_16x16x32_bf16 v[60:63],a[192:195],a[0:3],v[60:63]
ds_read_u16_d16_hi v27,v27
v_lshlrev_b32_sdwa v28,1,v28 dst_sel:DWORD dst_unused:UNUSED_PAD src0_sel:DWORD src1_sel:WORD_1
v_mfma_f32_16x16x32_bf16 v[56:59],a[192:195],a[4:7],v[16:19]
s_waitcnt lgkmcnt(4)
ds_read_u16 v28,v28
v_or_b32 v42,v52,v54
v_mfma_f32_16x16x32_bf16 v[56:59],a[196:199],a[20:23],v[56:59]
s_waitcnt vmcnt(4)
v_cvt_pk_bf16_f32 v43,v53,v55
v_lshlrev_b32_sdwa v30,1,v30 dst_sel:DWORD dst_unused:UNUSED_PAD src0_sel:DWORD src1_sel:WORD_1
v_mfma_f32_16x16x32_bf16 v[56:59],a[200:203],a[36:39],v[56:59]
s_barrier
ds_read_u16_d16_hi v30,v30
v_lshlrev_b32_sdwa v29,1,v29 dst_sel:DWORD dst_unused:UNUSED_PAD src0_sel:DWORD src1_sel:WORD_1
v_mfma_f32_16x16x32_bf16 v[52:55],a[212:215],a[36:39],v[20:23]
ds_read_u16 v29,v29
v_lshlrev_b32_sdwa v31,1,v31 dst_sel:DWORD dst_unused:UNUSED_PAD src0_sel:DWORD src1_sel:WORD_1
v_mfma_f32_16x16x32_bf16 v[52:55],a[204:207],a[20:23],v[52:55]
s_add_u32 m0,s58,6160
ds_read_u16_d16_hi v31,v31
v_lshlrev_b32_sdwa v44,1,v44 dst_sel:DWORD dst_unused:UNUSED_PAD src0_sel:DWORD src1_sel:WORD_1
v_mfma_f32_16x16x32_bf16 v[52:55],a[208:211],a[4:7],v[52:55]
s_add_u32 s69,s69,s59
ds_read_u16 v44,v44
v_lshlrev_b32_sdwa v46,1,v46 dst_sel:DWORD dst_unused:UNUSED_PAD src0_sel:DWORD src1_sel:WORD_1
v_mfma_f32_16x16x32_bf16 v[232:235],a[236:239],v[36:39],v[232:235]
buffer_load_dwordx4 v5,s[44:47],s69 offen offset:0 lds
v_lshlrev_b32_sdwa v45,1,v45 dst_sel:DWORD dst_unused:UNUSED_PAD src0_sel:DWORD src1_sel:WORD_1
v_mfma_f32_16x16x32_bf16 v[236:239],a[236:239],v[40:43],v[236:239]
ds_read_u16_d16_hi v46,v46
v_lshlrev_b32_sdwa v47,1,v47 dst_sel:DWORD dst_unused:UNUSED_PAD src0_sel:DWORD src1_sel:WORD_1
v_mfma_f32_16x16x32_bf16 v[164:167],a[244:247],v[40:43],v[164:167]
ds_read_u16 v45,v45
ds_read_u16_d16_hi v47,v47
v_mfma_f32_16x16x32_bf16 v[160:163],a[244:247],v[36:39],v[160:163]
ds_read_b64_tr_b16 a[228:229],v3 offset:18480
v_lshlrev_b32_sdwa v48,1,v48 dst_sel:DWORD dst_unused:UNUSED_PAD src0_sel:DWORD src1_sel:WORD_1
v_mfma_f32_16x16x32_bf16 v[240:243],a[248:251],v[36:39],v[240:243]
s_add_u32 s69,s69,s53
ds_read_u16 v48,v48
v_lshlrev_b32_sdwa v50,1,v50 dst_sel:DWORD dst_unused:UNUSED_PAD src0_sel:DWORD src1_sel:WORD_1
v_mfma_f32_16x16x32_bf16 v[244:247],a[248:251],v[40:43],v[244:247]
buffer_load_dwordx4 v5,s[44:47],s69 offen offset:1024 lds
ds_read_u16_d16_hi v50,v50
v_mfma_f32_16x16x32_bf16 v[180:183],a[252:255],v[40:43],v[180:183]
ds_read_b64_tr_b16 a[230:231],v3 offset:20016
v_lshlrev_b32_sdwa v49,1,v49 dst_sel:DWORD dst_unused:UNUSED_PAD src0_sel:DWORD src1_sel:WORD_1
v_mfma_f32_16x16x32_bf16 v[176:179],a[252:255],v[36:39],v[176:179]
ds_read_u16 v49,v49
v_lshlrev_b32_sdwa v51,1,v51 dst_sel:DWORD dst_unused:UNUSED_PAD src0_sel:DWORD src1_sel:WORD_1
v_mfma_f32_16x16x32_bf16 v[248:251],a[240:243],v[36:39],v[248:251]
ds_read_u16_d16_hi v51,v51
v_mfma_f32_16x16x32_bf16 v[252:255],a[240:243],v[40:43],v[252:255]
s_add_u32 s69,s69,s53
ds_read_b64_tr_b16 a[232:233],v3 offset:18488
v_mfma_f32_16x16x32_bf16 v[196:199],a[224:227],v[40:43],v[196:199]
buffer_load_dwordx4 v5,s[44:47],s69 offen offset:2048 lds
v_mfma_f32_16x16x32_bf16 v[192:195],a[224:227],v[36:39],v[192:195]
ds_read_b64_tr_b16 a[234:235],v3 offset:20024
v_mfma_f32_16x16x32_bf16 v[40:43],a[208:211],a[8:11],v[20:23]
ds_read_b64_tr_b16 a[220:221],v1 offset:18480
v_lshlrev_b32_sdwa v32,1,v32 dst_sel:DWORD dst_unused:UNUSED_PAD src0_sel:DWORD src1_sel:WORD_1
v_mfma_f32_16x16x32_bf16 v[40:43],a[204:207],a[24:27],v[40:43]
s_waitcnt lgkmcnt(7)
ds_read_u16 v32,v32
v_lshl_add_u64 v[24:25],v[24:25],0,v[26:27]
v_fma_mixlo_f16 v8,v57,s99,0
v_mfma_f32_16x16x32_bf16 v[40:43],a[212:215],a[40:43],v[40:43]
ds_read_b64_tr_b16 a[222:223],v1 offset:19504
v_lshl_add_u64 v[26:27],v[28:29],0,v[30:31]
v_lshlrev_b32_sdwa v34,1,v34 dst_sel:DWORD dst_unused:UNUSED_PAD src0_sel:DWORD src1_sel:WORD_1
v_mfma_f32_16x16x32_bf16 v[36:39],a[200:203],a[40:43],v[16:19]
ds_read_u16_d16_hi v34,v34
v_fma_mixhi_f16 v8,v59,s99,0
v_mfma_f32_16x16x32_bf16 v[36:39],a[196:199],a[24:27],v[36:39]
ds_read_b64_tr_b16 a[216:217],v1 offset:20528
v_lshl_add_u64 v[28:29],v[44:45],0,v[46:47]
v_lshlrev_b32_sdwa v33,1,v33 dst_sel:DWORD dst_unused:UNUSED_PAD src0_sel:DWORD src1_sel:WORD_1
v_pk_fma_f16 v9,v8,v8,s54
v_mfma_f32_16x16x32_bf16 v[36:39],a[192:195],a[8:11],v[36:39]
ds_read_u16 v33,v33
v_lshlrev_b32_sdwa v35,1,v35 dst_sel:DWORD dst_unused:UNUSED_PAD src0_sel:DWORD src1_sel:WORD_1
v_mfma_f32_16x16x32_bf16 v[44:47],a[192:195],a[12:15],v[16:19]
ds_read_u16_d16_hi v35,v35
v_pk_mul_f16 v9,v8,v9
v_mfma_f32_16x16x32_bf16 v[44:47],a[196:199],a[28:31],v[44:47]
s_waitcnt lgkmcnt(8)
ds_read_b64_tr_b16 a[218:219],v1 offset:21552
v_lshl_add_u64 v[30:31],v[48:49],0,v[50:51]
v_lshlrev_b32_sdwa v60,1,v60 dst_sel:DWORD dst_unused:UNUSED_PAD src0_sel:DWORD src1_sel:WORD_1
v_mfma_f32_16x16x32_bf16 v[44:47],a[200:203],a[44:47],v[44:47]
v_exp_f16 v8,v9
v_mfma_f32_16x16x32_bf16 v[48:51],a[212:215],a[44:47],v[20:23]
ds_read_u16 v60,v60
v_lshlrev_b32_sdwa v62,1,v62 dst_sel:DWORD dst_unused:UNUSED_PAD src0_sel:DWORD src1_sel:WORD_1
v_mfma_f32_16x16x32_bf16 v[48:51],a[204:207],a[28:31],v[48:51]
ds_read_u16_d16_hi v62,v62
v_lshlrev_b32_sdwa v61,1,v61 dst_sel:DWORD dst_unused:UNUSED_PAD src0_sel:DWORD src1_sel:WORD_1
v_mfma_f32_16x16x32_bf16 v[48:51],a[208:211],a[12:15],v[48:51]
v_exp_f16_sdwa v8,v9 dst_sel:WORD_1 dst_unused:UNUSED_PRESERVE src0_sel:WORD_1
v_mfma_f32_16x16x32_bf16 v[208:211],a[236:239],v[24:27],v[208:211]
ds_read_u16 v61,v61
v_lshlrev_b32_sdwa v63,1,v63 dst_sel:DWORD dst_unused:UNUSED_PAD src0_sel:DWORD src1_sel:WORD_1
v_mfma_f32_16x16x32_bf16 v[212:215],a[236:239],v[28:31],v[212:215]
ds_read_u16_d16_hi v63,v63
v_lshlrev_b32_sdwa v52,1,v52 dst_sel:DWORD dst_unused:UNUSED_PAD src0_sel:DWORD src1_sel:WORD_1
v_mfma_f32_16x16x32_bf16 v[172:175],a[244:247],v[28:31],v[172:175]
ds_read_u16 v52,v52
v_pk_fma_f16 v8,v8,s55,s55
v_mfma_f32_16x16x32_bf16 v[168:171],a[244:247],v[24:27],v[168:171]
ds_read_b64_tr_b16 a[236:237],v3 offset:18544
v_rcp_f16 v9,v8
v_lshlrev_b32_sdwa v54,1,v54 dst_sel:DWORD dst_unused:UNUSED_PAD src0_sel:DWORD src1_sel:WORD_1
v_mfma_f32_16x16x32_bf16 v[216:219],a[248:251],v[24:27],v[216:219]
ds_read_u16_d16_hi v54,v54
v_lshlrev_b32_sdwa v53,1,v53 dst_sel:DWORD dst_unused:UNUSED_PAD src0_sel:DWORD src1_sel:WORD_1
v_mfma_f32_16x16x32_bf16 v[220:223],a[248:251],v[28:31],v[220:223]
ds_read_u16 v53,v53
ds_read_b64_tr_b16 a[238:239],v3 offset:20080
v_mfma_f32_16x16x32_bf16 v[188:191],a[252:255],v[28:31],v[188:191]
ds_read_b64_tr_b16 a[244:245],v3 offset:18552
v_lshlrev_b32_sdwa v55,1,v55 dst_sel:DWORD dst_unused:UNUSED_PAD src0_sel:DWORD src1_sel:WORD_1
v_mfma_f32_16x16x32_bf16 v[184:187],a[252:255],v[24:27],v[184:187]
ds_read_u16_d16_hi v55,v55
v_rcp_f16_sdwa v9,v8 dst_sel:WORD_1 dst_unused:UNUSED_PRESERVE src0_sel:WORD_1
v_mfma_f32_16x16x32_bf16 v[224:227],a[240:243],v[24:27],v[224:227]
ds_read_b64_tr_b16 a[246:247],v3 offset:20088
ds_read_b64_tr_b16 a[248:249],v3 offset:18608
v_lshlrev_b32_sdwa v56,1,v56 dst_sel:DWORD dst_unused:UNUSED_PAD src0_sel:DWORD src1_sel:WORD_1
v_mfma_f32_16x16x32_bf16 v[228:231],a[240:243],v[28:31],v[228:231]
ds_read_u16 v56,v56
v_lshlrev_b32_sdwa v58,1,v58 dst_sel:DWORD dst_unused:UNUSED_PAD src0_sel:DWORD src1_sel:WORD_1
v_mfma_f32_16x16x32_bf16 v[204:207],a[224:227],v[28:31],v[204:207]
ds_read_u16_d16_hi v58,v58
v_fma_mix_f32 v57,v57,v9,0 op_sel_hi:[0,1,0]
v_mfma_f32_16x16x32_bf16 v[200:203],a[224:227],v[24:27],v[200:203]
ds_read_b64_tr_b16 a[250:251],v3 offset:20144
v_fma_mix_f32 v59,v59,v9,0 op_sel:[0,1,0] op_sel_hi:[0,1,0]
v_mfma_f32_16x16x32_bf16 v[28:31],a[208:211],a[48:51],v[20:23]
s_add_u32 m0,s58,12320
ds_read_b64_tr_b16 a[252:253],v3 offset:18616
v_lshlrev_b32_sdwa v40,1,v40 dst_sel:DWORD dst_unused:UNUSED_PAD src0_sel:DWORD src1_sel:WORD_1
v_mfma_f32_16x16x32_bf16 v[28:31],a[204:207],a[64:67],v[28:31]
s_waitcnt lgkmcnt(10)
ds_read_b64_tr_b16 a[254:255],v3 offset:20152
v_lshl_add_u64 v[32:33],v[32:33],0,v[34:35]
v_mfma_f32_16x16x32_bf16 v[28:31],a[212:215],a[80:83],v[28:31]
s_add_u32 s69,s69,s59
ds_read_u16 v40,v40
v_lshl_add_u64 v[34:35],v[60:61],0,v[62:63]
v_lshlrev_b32_sdwa v42,1,v42 dst_sel:DWORD dst_unused:UNUSED_PAD src0_sel:DWORD src1_sel:WORD_1
v_mfma_f32_16x16x32_bf16 v[24:27],a[200:203],a[80:83],v[16:19]
buffer_load_dwordx4 v5,s[44:47],s69 offen offset:0 lds
v_lshlrev_b32_sdwa v41,1,v41 dst_sel:DWORD dst_unused:UNUSED_PAD src0_sel:DWORD src1_sel:WORD_1
v_mfma_f32_16x16x32_bf16 v[24:27],a[196:199],a[64:67],v[24:27]
s_waitcnt lgkmcnt(8)
ds_read_u16_d16_hi v42,v42
v_lshl_add_u64 v[60:61],v[52:53],0,v[54:55]
v_mfma_f32_16x16x32_bf16 v[24:27],a[192:195],a[48:51],v[24:27]
s_add_u32 s69,s69,s53
ds_read_u16 v41,v41
v_lshlrev_b32_sdwa v43,1,v43 dst_sel:DWORD dst_unused:UNUSED_PAD src0_sel:DWORD src1_sel:WORD_1
v_mfma_f32_16x16x32_bf16 v[52:55],a[192:195],a[52:55],v[16:19]
s_waitcnt lgkmcnt(6)
buffer_load_dwordx4 v5,s[44:47],s69 offen offset:1024 lds
v_or_b32 v62,v56,v58
v_lshlrev_b32_sdwa v36,1,v36 dst_sel:DWORD dst_unused:UNUSED_PAD src0_sel:DWORD src1_sel:WORD_1
v_mfma_f32_16x16x32_bf16 v[52:55],a[196:199],a[68:71],v[52:55]
s_add_u32 s69,s69,s53
ds_read_u16_d16_hi v43,v43
v_cvt_pk_bf16_f32 v63,v57,v59
v_lshlrev_b32_sdwa v38,1,v38 dst_sel:DWORD dst_unused:UNUSED_PAD src0_sel:DWORD src1_sel:WORD_1
v_mfma_f32_16x16x32_bf16 v[52:55],a[200:203],a[84:87],v[52:55]
buffer_load_dwordx4 v5,s[44:47],s69 offen offset:2048 lds
v_lshlrev_b32_sdwa v37,1,v37 dst_sel:DWORD dst_unused:UNUSED_PAD src0_sel:DWORD src1_sel:WORD_1
v_mfma_f32_16x16x32_bf16 v[56:59],a[212:215],a[84:87],v[20:23]
ds_read_u16 v36,v36
v_lshlrev_b32_sdwa v39,1,v39 dst_sel:DWORD dst_unused:UNUSED_PAD src0_sel:DWORD src1_sel:WORD_1
v_mfma_f32_16x16x32_bf16 v[56:59],a[204:207],a[68:71],v[56:59]
ds_read_u16_d16_hi v38,v38
v_lshlrev_b32_sdwa v48,1,v48 dst_sel:DWORD dst_unused:UNUSED_PAD src0_sel:DWORD src1_sel:WORD_1
v_mfma_f32_16x16x32_bf16 v[56:59],a[208:211],a[52:55],v[56:59]
ds_read_u16 v37,v37
v_lshlrev_b32_sdwa v50,1,v50 dst_sel:DWORD dst_unused:UNUSED_PAD src0_sel:DWORD src1_sel:WORD_1
v_mfma_f32_16x16x32_bf16 v[136:139],a[228:231],v[32:35],v[136:139]
ds_read_u16_d16_hi v39,v39
v_lshlrev_b32_sdwa v49,1,v49 dst_sel:DWORD dst_unused:UNUSED_PAD src0_sel:DWORD src1_sel:WORD_1
v_mfma_f32_16x16x32_bf16 v[140:143],a[228:231],v[60:63],v[140:143]
ds_read_u16 v48,v48
v_lshlrev_b32_sdwa v51,1,v51 dst_sel:DWORD dst_unused:UNUSED_PAD src0_sel:DWORD src1_sel:WORD_1
v_mfma_f32_16x16x32_bf16 v[68:71],a[232:235],v[60:63],v[68:71]
ds_read_u16_d16_hi v50,v50
v_lshlrev_b32_sdwa v44,1,v44 dst_sel:DWORD dst_unused:UNUSED_PAD src0_sel:DWORD src1_sel:WORD_1
v_mfma_f32_16x16x32_bf16 v[64:67],a[232:235],v[32:35],v[64:67]
ds_read_u16 v49,v49
v_lshlrev_b32_sdwa v46,1,v46 dst_sel:DWORD dst_unused:UNUSED_PAD src0_sel:DWORD src1_sel:WORD_1
v_mfma_f32_16x16x32_bf16 v[144:147],a[236:239],v[32:35],v[144:147]
ds_read_u16_d16_hi v51,v51
v_lshlrev_b32_sdwa v45,1,v45 dst_sel:DWORD dst_unused:UNUSED_PAD src0_sel:DWORD src1_sel:WORD_1
v_mfma_f32_16x16x32_bf16 v[148:151],a[236:239],v[60:63],v[148:151]
ds_read_u16 v44,v44
v_lshlrev_b32_sdwa v47,1,v47 dst_sel:DWORD dst_unused:UNUSED_PAD src0_sel:DWORD src1_sel:WORD_1
v_mfma_f32_16x16x32_bf16 v[84:87],a[244:247],v[60:63],v[84:87]
ds_read_u16_d16_hi v46,v46
v_mfma_f32_16x16x32_bf16 v[80:83],a[244:247],v[32:35],v[80:83]
s_waitcnt lgkmcnt(8)
ds_read_u16 v45,v45
v_mfma_f32_16x16x32_bf16 v[152:155],a[248:251],v[32:35],v[152:155]
ds_read_u16_d16_hi v47,v47
v_mfma_f32_16x16x32_bf16 v[156:159],a[248:251],v[60:63],v[156:159]
v_mfma_f32_16x16x32_bf16 v[100:103],a[252:255],v[60:63],v[100:103]
v_mfma_f32_16x16x32_bf16 v[96:99],a[252:255],v[32:35],v[96:99]
v_mfma_f32_16x16x32_bf16 v[60:63],a[208:211],a[56:59],v[20:23]
v_mul_f32_dpp v136,v14,v136 row_newbcast:0 row_mask:0xf bank_mask:0xf
s_add_u32 s8,s61,0
v_mul_f32_dpp v137,v14,v137 row_newbcast:1 row_mask:0xf bank_mask:0xf
v_mul_f32_dpp v138,v14,v138 row_newbcast:2 row_mask:0xf bank_mask:0xf
v_lshlrev_b32_sdwa v28,1,v28 dst_sel:DWORD dst_unused:UNUSED_PAD src0_sel:DWORD src1_sel:WORD_1
v_mfma_f32_16x16x32_bf16 v[60:63],a[204:207],a[72:75],v[60:63]
s_nop 0
ds_read_u16 v28,v28
v_lshl_add_u64 v[40:41],v[40:41],0,v[42:43]
v_mul_f32_dpp v139,v14,v139 row_newbcast:3 row_mask:0xf bank_mask:0xf
v_mul_f32_dpp v64,v14,v64 row_newbcast:4 row_mask:0xf bank_mask:0xf
v_mul_f32_dpp v65,v14,v65 row_newbcast:5 row_mask:0xf bank_mask:0xf
v_mfma_f32_16x16x32_bf16 v[60:63],a[212:215],a[88:91],v[60:63]
s_waitcnt lgkmcnt(4)
v_lshl_add_u64 v[42:43],v[36:37],0,v[38:39]
v_mul_f32_dpp v66,v14,v66 row_newbcast:6 row_mask:0xf bank_mask:0xf
v_mul_f32_dpp v67,v14,v67 row_newbcast:7 row_mask:0xf bank_mask:0xf
v_lshlrev_b32_sdwa v30,1,v30 dst_sel:DWORD dst_unused:UNUSED_PAD src0_sel:DWORD src1_sel:WORD_1
v_cvt_pk_bf16_f32 v136,v136,v137
v_mfma_f32_16x16x32_bf16 v[32:35],a[200:203],a[88:91],v[16:19]
ds_read_u16_d16_hi v30,v30
v_cvt_pk_bf16_f32 v137,v138,v139
v_cvt_pk_bf16_f32 v138,v64,v65
v_lshlrev_b32_sdwa v29,1,v29 dst_sel:DWORD dst_unused:UNUSED_PAD src0_sel:DWORD src1_sel:WORD_1
v_cvt_pk_bf16_f32 v139,v66,v67
v_mfma_f32_16x16x32_bf16 v[32:35],a[196:199],a[72:75],v[32:35]
buffer_store_dwordx4 v[136:139],v7,s[40:43],s8 offen offset:0 sc1
v_lshl_add_u64 v[36:37],v[48:49],0,v[50:51]
v_mul_f32_dpp v144,v14,v144 row_newbcast:8 row_mask:0xf bank_mask:0xf
v_mul_f32_dpp v145,v14,v145 row_newbcast:9 row_mask:0xf bank_mask:0xf
v_mul_f32_dpp v146,v14,v146 row_newbcast:10 row_mask:0xf bank_mask:0xf
v_mfma_f32_16x16x32_bf16 v[32:35],a[192:195],a[56:59],v[32:35]
ds_read_u16 v29,v29
v_mul_f32_dpp v147,v14,v147 row_newbcast:11 row_mask:0xf bank_mask:0xf
v_mul_f32_dpp v80,v14,v80 row_newbcast:12 row_mask:0xf bank_mask:0xf
v_lshlrev_b32_sdwa v31,1,v31 dst_sel:DWORD dst_unused:UNUSED_PAD src0_sel:DWORD src1_sel:WORD_1
v_mul_f32_dpp v81,v14,v81 row_newbcast:13 row_mask:0xf bank_mask:0xf
v_mfma_f32_16x16x32_bf16 v[48:51],a[192:195],a[60:63],v[16:19]
s_nop 0
ds_read_b128 v[16:19],v4 offset:0
v_mul_f32_dpp v82,v14,v82 row_newbcast:14 row_mask:0xf bank_mask:0xf
v_mul_f32_dpp v83,v14,v83 row_newbcast:15 row_mask:0xf bank_mask:0xf
v_lshlrev_b32_sdwa v24,1,v24 dst_sel:DWORD dst_unused:UNUSED_PAD src0_sel:DWORD src1_sel:WORD_1
v_cvt_pk_bf16_f32 v144,v144,v145
v_mfma_f32_16x16x32_bf16 v[48:51],a[196:199],a[76:79],v[48:51]
s_waitcnt lgkmcnt(4)
ds_read_u16_d16_hi v31,v31
ds_read_b64_tr_b16 a[192:193],v1 offset:22576
v_lshl_add_u64 v[38:39],v[44:45],0,v[46:47]
v_cvt_pk_bf16_f32 v145,v146,v147
v_cvt_pk_bf16_f32 v146,v80,v81
v_cvt_pk_bf16_f32 v147,v82,v83
v_lshlrev_b32_sdwa v26,1,v26 dst_sel:DWORD dst_unused:UNUSED_PAD src0_sel:DWORD src1_sel:WORD_1
v_mfma_f32_16x16x32_bf16 v[48:51],a[200:203],a[92:95],v[48:51]
ds_read_b64_tr_b16 a[194:195],v1 offset:23600
ds_read_u16 v24,v24
v_mul_f32_dpp v152,v15,v152 row_newbcast:0 row_mask:0xf bank_mask:0xf
v_mul_f32_dpp v153,v15,v153 row_newbcast:1 row_mask:0xf bank_mask:0xf
v_mul_f32_dpp v154,v15,v154 row_newbcast:2 row_mask:0xf bank_mask:0xf
v_mfma_f32_16x16x32_bf16 v[44:47],a[212:215],a[92:95],v[20:23]
ds_read_b128 v[20:23],v4 offset:32
ds_read_u16_d16_hi v26,v26
v_mul_f32_dpp v155,v15,v155 row_newbcast:3 row_mask:0xf bank_mask:0xf
v_mul_f32_dpp v96,v15,v96 row_newbcast:4 row_mask:0xf bank_mask:0xf
v_lshlrev_b32_sdwa v25,1,v25 dst_sel:DWORD dst_unused:UNUSED_PAD src0_sel:DWORD src1_sel:WORD_1
v_mul_f32_dpp v97,v15,v97 row_newbcast:5 row_mask:0xf bank_mask:0xf
v_mfma_f32_16x16x32_bf16 v[44:47],a[204:207],a[76:79],v[44:47]
ds_read_u16 v25,v25
ds_read_b64_tr_b16 a[196:197],v2 offset:22576
v_mul_f32_dpp v98,v15,v98 row_newbcast:6 row_mask:0xf bank_mask:0xf
v_mul_f32_dpp v99,v15,v99 row_newbcast:7 row_mask:0xf bank_mask:0xf
v_lshlrev_b32_sdwa v27,1,v27 dst_sel:DWORD dst_unused:UNUSED_PAD src0_sel:DWORD src1_sel:WORD_1
v_cvt_pk_bf16_f32 v152,v152,v153
v_mfma_f32_16x16x32_bf16 v[44:47],a[208:211],a[60:63],v[44:47]
ds_read_u16_d16_hi v27,v27
ds_read_b64_tr_b16 a[198:199],v2 offset:23600
v_cvt_pk_bf16_f32 v153,v154,v155
v_cvt_pk_bf16_f32 v154,v96,v97
v_cvt_pk_bf16_f32 v155,v98,v99
v_mfma_f32_16x16x32_bf16 v[112:115],a[228:231],v[40:43],v[112:115]
ds_read_b64_tr_b16 a[200:201],v2 offset:20528
v_mul_f32_dpp v140,v14,v140 row_newbcast:0 row_mask:0xf bank_mask:0xf
v_mul_f32_dpp v141,v14,v141 row_newbcast:1 row_mask:0xf bank_mask:0xf
v_lshlrev_b32_sdwa v56,1,v56 dst_sel:DWORD dst_unused:UNUSED_PAD src0_sel:DWORD src1_sel:WORD_1
v_mul_f32_dpp v142,v14,v142 row_newbcast:2 row_mask:0xf bank_mask:0xf
v_mfma_f32_16x16x32_bf16 v[116:119],a[228:231],v[36:39],v[116:119]
ds_read_b64_tr_b16 a[202:203],v2 offset:21552
v_mul_f32_dpp v143,v14,v143 row_newbcast:3 row_mask:0xf bank_mask:0xf
v_mul_f32_dpp v68,v14,v68 row_newbcast:4 row_mask:0xf bank_mask:0xf
v_lshlrev_b32_sdwa v58,1,v58 dst_sel:DWORD dst_unused:UNUSED_PAD src0_sel:DWORD src1_sel:WORD_1
v_mul_f32_dpp v69,v14,v69 row_newbcast:5 row_mask:0xf bank_mask:0xf
v_mfma_f32_16x16x32_bf16 v[76:79],a[232:235],v[36:39],v[76:79]
ds_read_u16 v56,v56
v_mul_f32_dpp v70,v14,v70 row_newbcast:6 row_mask:0xf bank_mask:0xf
v_mul_f32_dpp v71,v14,v71 row_newbcast:7 row_mask:0xf bank_mask:0xf
v_cvt_pk_bf16_f32 v140,v140,v141
v_lshlrev_b32_sdwa v57,1,v57 dst_sel:DWORD dst_unused:UNUSED_PAD src0_sel:DWORD src1_sel:WORD_1
v_mfma_f32_16x16x32_bf16 v[72:75],a[232:235],v[40:43],v[72:75]
buffer_store_dwordx4 v[144:147],v7,s[40:43],s8 offen offset:64 sc1
ds_read_u16_d16_hi v58,v58
v_cvt_pk_bf16_f32 v141,v142,v143
v_cvt_pk_bf16_f32 v142,v68,v69
v_cvt_pk_bf16_f32 v143,v70,v71
v_mfma_f32_16x16x32_bf16 v[120:123],a[236:239],v[40:43],v[120:123]
TP4880_%=:
s_cbranch_execz TP7013_%=
ds_read_u16 v57,v57
v_mul_f32_dpp v148,v14,v148 row_newbcast:8 row_mask:0xf bank_mask:0xf
v_mul_f32_dpp v149,v14,v149 row_newbcast:9 row_mask:0xf bank_mask:0xf
v_lshlrev_b32_sdwa v59,1,v59 dst_sel:DWORD dst_unused:UNUSED_PAD src0_sel:DWORD src1_sel:WORD_1
v_mul_f32_dpp v150,v14,v150 row_newbcast:10 row_mask:0xf bank_mask:0xf
v_mfma_f32_16x16x32_bf16 v[124:127],a[236:239],v[36:39],v[124:127]
s_nop 0
ds_read_u16_d16_hi v59,v59
v_mul_f32_dpp v151,v14,v151 row_newbcast:11 row_mask:0xf bank_mask:0xf
v_mul_f32_dpp v84,v14,v84 row_newbcast:12 row_mask:0xf bank_mask:0xf
v_lshlrev_b32_sdwa v52,1,v52 dst_sel:DWORD dst_unused:UNUSED_PAD src0_sel:DWORD src1_sel:WORD_1
v_mul_f32_dpp v85,v14,v85 row_newbcast:13 row_mask:0xf bank_mask:0xf
v_mfma_f32_16x16x32_bf16 v[92:95],a[244:247],v[36:39],v[92:95]
ds_read_b64_tr_b16 a[212:213],v2 offset:18480
v_mul_f32_dpp v86,v14,v86 row_newbcast:14 row_mask:0xf bank_mask:0xf
v_mul_f32_dpp v87,v14,v87 row_newbcast:15 row_mask:0xf bank_mask:0xf
v_cvt_pk_bf16_f32 v148,v148,v149
v_mfma_f32_16x16x32_bf16 v[88:91],a[244:247],v[40:43],v[88:91]
ds_read_b64_tr_b16 a[214:215],v2 offset:19504
v_cvt_pk_bf16_f32 v149,v150,v151
v_cvt_pk_bf16_f32 v150,v84,v85
v_lshlrev_b32_sdwa v54,1,v54 dst_sel:DWORD dst_unused:UNUSED_PAD src0_sel:DWORD src1_sel:WORD_1
v_cvt_pk_bf16_f32 v151,v86,v87
v_mfma_f32_16x16x32_bf16 v[128:131],a[248:251],v[40:43],v[128:131]
ds_read_u16 v52,v52
v_mul_f32_dpp v156,v15,v156 row_newbcast:0 row_mask:0xf bank_mask:0xf
v_mul_f32_dpp v157,v15,v157 row_newbcast:1 row_mask:0xf bank_mask:0xf
v_lshlrev_b32_sdwa v53,1,v53 dst_sel:DWORD dst_unused:UNUSED_PAD src0_sel:DWORD src1_sel:WORD_1
v_mul_f32_dpp v158,v15,v158 row_newbcast:2 row_mask:0xf bank_mask:0xf
v_mfma_f32_16x16x32_bf16 v[132:135],a[248:251],v[36:39],v[132:135]
ds_read_u16_d16_hi v54,v54
v_mul_f32_dpp v159,v15,v159 row_newbcast:3 row_mask:0xf bank_mask:0xf
v_mul_f32_dpp v100,v15,v100 row_newbcast:4 row_mask:0xf bank_mask:0xf
v_mul_f32_dpp v101,v15,v101 row_newbcast:5 row_mask:0xf bank_mask:0xf
v_lshlrev_b32_sdwa v55,1,v55 dst_sel:DWORD dst_unused:UNUSED_PAD src0_sel:DWORD src1_sel:WORD_1
v_mfma_f32_16x16x32_bf16 v[108:111],a[252:255],v[36:39],v[108:111]
ds_read_u16 v53,v53
v_mul_f32_dpp v102,v15,v102 row_newbcast:6 row_mask:0xf bank_mask:0xf
v_mul_f32_dpp v103,v15,v103 row_newbcast:7 row_mask:0xf bank_mask:0xf
v_cvt_pk_bf16_f32 v156,v156,v157
v_mfma_f32_16x16x32_bf16 v[104:107],a[252:255],v[40:43],v[104:107]
s_waitcnt lgkmcnt(8)
ds_read_u16_d16_hi v55,v55
v_cvt_pk_bf16_f32 v157,v158,v159
v_cvt_pk_bf16_f32 v158,v100,v101
v_cvt_pk_bf16_f32 v159,v102,v103
v_mfma_f32_16x16x32_bf16 v[36:39],a[220:223],a[48:51],v[16:19]
s_nop 0
v_mul_f32_dpp v112,v14,v112 row_newbcast:0 row_mask:0xf bank_mask:0xf
s_add_u32 s9,s61,98304
v_accvgpr_write_b32 a16,0
v_accvgpr_write_b32 a17,0
v_lshlrev_b32_sdwa v60,1,v60 dst_sel:DWORD dst_unused:UNUSED_PAD src0_sel:DWORD src1_sel:WORD_1
v_mfma_f32_16x16x32_bf16 v[36:39],a[216:219],a[64:67],v[36:39]
ds_read_u16 v60,v60
v_lshl_add_u64 v[28:29],v[28:29],0,v[30:31]
v_accvgpr_write_b32 a18,0
v_accvgpr_write_b32 a19,0
v_mul_f32_dpp v113,v14,v113 row_newbcast:1 row_mask:0xf bank_mask:0xf
v_mfma_f32_16x16x32_bf16 v[36:39],a[192:195],a[80:83],v[36:39]
v_lshl_add_u64 v[30:31],v[24:25],0,v[26:27]
v_mul_f32_dpp v114,v14,v114 row_newbcast:2 row_mask:0xf bank_mask:0xf
v_mul_f32_dpp v115,v14,v115 row_newbcast:3 row_mask:0xf bank_mask:0xf
v_lshlrev_b32_sdwa v62,1,v62 dst_sel:DWORD dst_unused:UNUSED_PAD src0_sel:DWORD src1_sel:WORD_1
v_mul_f32_dpp v72,v14,v72 row_newbcast:4 row_mask:0xf bank_mask:0xf
v_mfma_f32_16x16x32_bf16 v[40:43],a[196:199],a[80:83],v[20:23]
s_nop 0
buffer_store_dwordx4 v[152:155],v7,s[40:43],s8 offen offset:128 sc1
v_mul_f32_dpp v73,v14,v73 row_newbcast:5 row_mask:0xf bank_mask:0xf
v_mul_f32_dpp v74,v14,v74 row_newbcast:6 row_mask:0xf bank_mask:0xf
v_lshlrev_b32_sdwa v61,1,v61 dst_sel:DWORD dst_unused:UNUSED_PAD src0_sel:DWORD src1_sel:WORD_1
v_mul_f32_dpp v75,v14,v75 row_newbcast:7 row_mask:0xf bank_mask:0xf
v_mfma_f32_16x16x32_bf16 v[40:43],a[200:203],a[64:67],v[40:43]
s_waitcnt lgkmcnt(5)
ds_read_u16_d16_hi v62,v62
v_lshl_add_u64 v[24:25],v[56:57],0,v[58:59]
v_cvt_pk_bf16_f32 v112,v112,v113
v_cvt_pk_bf16_f32 v113,v114,v115
v_cvt_pk_bf16_f32 v114,v72,v73
v_mfma_f32_16x16x32_bf16 v[40:43],a[212:215],a[48:51],v[40:43]
ds_read_u16 v61,v61
v_cvt_pk_bf16_f32 v115,v74,v75
v_mul_f32_dpp v120,v14,v120 row_newbcast:8 row_mask:0xf bank_mask:0xf
v_lshlrev_b32_sdwa v63,1,v63 dst_sel:DWORD dst_unused:UNUSED_PAD src0_sel:DWORD src1_sel:WORD_1
v_accvgpr_write_b32 a24,v112
v_mfma_f32_16x16x32_bf16 v[56:59],a[212:215],a[52:55],v[20:23]
s_nop 0
ds_read_u16_d16_hi v63,v63
v_mul_f32_dpp v121,v14,v121 row_newbcast:9 row_mask:0xf bank_mask:0xf
v_mul_f32_dpp v122,v14,v122 row_newbcast:10 row_mask:0xf bank_mask:0xf
v_lshlrev_b32_sdwa v32,1,v32 dst_sel:DWORD dst_unused:UNUSED_PAD src0_sel:DWORD src1_sel:WORD_1
v_accvgpr_write_b32 a25,v113
v_mfma_f32_16x16x32_bf16 v[56:59],a[200:203],a[68:71],v[56:59]
s_waitcnt lgkmcnt(4)
ds_read_u16 v32,v32
v_lshl_add_u64 v[26:27],v[52:53],0,v[54:55]
v_mul_f32_dpp v123,v14,v123 row_newbcast:11 row_mask:0xf bank_mask:0xf
v_mul_f32_dpp v88,v14,v88 row_newbcast:12 row_mask:0xf bank_mask:0xf
v_accvgpr_write_b32 a26,v114
v_lshlrev_b32_sdwa v34,1,v34 dst_sel:DWORD dst_unused:UNUSED_PAD src0_sel:DWORD src1_sel:WORD_1
v_mfma_f32_16x16x32_bf16 v[56:59],a[196:199],a[84:87],v[56:59]
s_waitcnt vmcnt(10)
s_barrier
ds_read_u16_d16_hi v34,v34
v_mul_f32_dpp v89,v14,v89 row_newbcast:13 row_mask:0xf bank_mask:0xf
v_mul_f32_dpp v90,v14,v90 row_newbcast:14 row_mask:0xf bank_mask:0xf
v_accvgpr_write_b32 a27,v115
v_mfma_f32_16x16x32_bf16 v[52:55],a[192:195],a[84:87],v[16:19]
ds_read_b64_tr_b16 a[240:241],v3 offset:0
v_mul_f32_dpp v91,v14,v91 row_newbcast:15 row_mask:0xf bank_mask:0xf
v_cvt_pk_bf16_f32 v120,v120,v121
v_lshlrev_b32_sdwa v33,1,v33 dst_sel:DWORD dst_unused:UNUSED_PAD src0_sel:DWORD src1_sel:WORD_1
v_cvt_pk_bf16_f32 v121,v122,v123
v_mfma_f32_16x16x32_bf16 v[52:55],a[216:219],a[68:71],v[52:55]
s_add_u32 m0,s58,18480
ds_read_u16 v33,v33
v_cvt_pk_bf16_f32 v122,v88,v89
v_cvt_pk_bf16_f32 v123,v90,v91
v_lshlrev_b32_sdwa v35,1,v35 dst_sel:DWORD dst_unused:UNUSED_PAD src0_sel:DWORD src1_sel:WORD_1
v_mul_f32_dpp v128,v15,v128 row_newbcast:0 row_mask:0xf bank_mask:0xf
v_mfma_f32_16x16x32_bf16 v[52:55],a[220:223],a[52:55],v[52:55]
s_add_u32 s69,s69,s59
ds_read_u16_d16_hi v35,v35
v_accvgpr_write_b32 a28,v120
v_mul_f32_dpp v129,v15,v129 row_newbcast:1 row_mask:0xf bank_mask:0xf
v_mul_f32_dpp v130,v15,v130 row_newbcast:2 row_mask:0xf bank_mask:0xf
v_mfma_f32_16x16x32_bf16 v[232:235],a[228:231],v[28:31],v[232:235]
buffer_load_dwordx4 v5,s[44:47],s69 offen offset:0 lds
v_accvgpr_write_b32 a29,v121
v_mul_f32_dpp v131,v15,v131 row_newbcast:3 row_mask:0xf bank_mask:0xf
v_lshlrev_b32_sdwa v44,1,v44 dst_sel:DWORD dst_unused:UNUSED_PAD src0_sel:DWORD src1_sel:WORD_1
v_mul_f32_dpp v104,v15,v104 row_newbcast:4 row_mask:0xf bank_mask:0xf
v_mfma_f32_16x16x32_bf16 v[236:239],a[228:231],v[24:27],v[236:239]
s_add_u32 s69,s69,s53
ds_read_u16 v44,v44
v_accvgpr_write_b32 a30,v122
v_mul_f32_dpp v105,v15,v105 row_newbcast:5 row_mask:0xf bank_mask:0xf
v_lshlrev_b32_sdwa v46,1,v46 dst_sel:DWORD dst_unused:UNUSED_PAD src0_sel:DWORD src1_sel:WORD_1
v_mul_f32_dpp v106,v15,v106 row_newbcast:6 row_mask:0xf bank_mask:0xf
v_mfma_f32_16x16x32_bf16 v[164:167],a[232:235],v[24:27],v[164:167]
buffer_load_dwordx4 v5,s[44:47],s69 offen offset:1024 lds
ds_read_u16_d16_hi v46,v46
v_accvgpr_write_b32 a31,v123
v_mul_f32_dpp v107,v15,v107 row_newbcast:7 row_mask:0xf bank_mask:0xf
v_cvt_pk_bf16_f32 v128,v128,v129
v_lshlrev_b32_sdwa v45,1,v45 dst_sel:DWORD dst_unused:UNUSED_PAD src0_sel:DWORD src1_sel:WORD_1
v_mfma_f32_16x16x32_bf16 v[160:163],a[232:235],v[28:31],v[160:163]
s_nop 0
buffer_store_dwordx4 v[140:143],v7,s[40:43],s8 offen offset:192 sc1
ds_read_u16 v45,v45
v_cvt_pk_bf16_f32 v129,v130,v131
v_cvt_pk_bf16_f32 v130,v104,v105
v_cvt_pk_bf16_f32 v131,v106,v107
v_mfma_f32_16x16x32_bf16 v[240:243],a[236:239],v[28:31],v[240:243]
s_add_u32 s69,s69,s53
ds_read_b64_tr_b16 a[242:243],v3 offset:1536
v_mul_f32_dpp v116,v14,v116 row_newbcast:0 row_mask:0xf bank_mask:0xf
v_accvgpr_write_b32 a32,v128
v_lshlrev_b32_sdwa v47,1,v47 dst_sel:DWORD dst_unused:UNUSED_PAD src0_sel:DWORD src1_sel:WORD_1
v_mul_f32_dpp v117,v14,v117 row_newbcast:1 row_mask:0xf bank_mask:0xf
v_mfma_f32_16x16x32_bf16 v[244:247],a[236:239],v[24:27],v[244:247]
buffer_load_dwordx4 v5,s[44:47],s69 offen offset:2048 lds
v_mul_f32_dpp v118,v14,v118 row_newbcast:2 row_mask:0xf bank_mask:0xf
v_accvgpr_write_b32 a33,v129
v_lshlrev_b32_sdwa v48,1,v48 dst_sel:DWORD dst_unused:UNUSED_PAD src0_sel:DWORD src1_sel:WORD_1
v_mul_f32_dpp v119,v14,v119 row_newbcast:3 row_mask:0xf bank_mask:0xf
v_mfma_f32_16x16x32_bf16 v[180:183],a[244:247],v[24:27],v[180:183]
ds_read_u16_d16_hi v47,v47
v_mul_f32_dpp v76,v14,v76 row_newbcast:4 row_mask:0xf bank_mask:0xf
v_accvgpr_write_b32 a34,v130
v_mul_f32_dpp v77,v14,v77 row_newbcast:5 row_mask:0xf bank_mask:0xf
v_mfma_f32_16x16x32_bf16 v[176:179],a[244:247],v[28:31],v[176:179]
ds_read_u16 v48,v48
v_mul_f32_dpp v78,v14,v78 row_newbcast:6 row_mask:0xf bank_mask:0xf
v_accvgpr_write_b32 a35,v131
v_lshlrev_b32_sdwa v50,1,v50 dst_sel:DWORD dst_unused:UNUSED_PAD src0_sel:DWORD src1_sel:WORD_1
v_mul_f32_dpp v79,v14,v79 row_newbcast:7 row_mask:0xf bank_mask:0xf
v_mfma_f32_16x16x32_bf16 v[248:251],a[248:251],v[28:31],v[248:251]
ds_read_u16_d16_hi v50,v50
v_cvt_pk_bf16_f32 v116,v116,v117
v_cvt_pk_bf16_f32 v117,v118,v119
v_lshlrev_b32_sdwa v49,1,v49 dst_sel:DWORD dst_unused:UNUSED_PAD src0_sel:DWORD src1_sel:WORD_1
v_cvt_pk_bf16_f32 v118,v76,v77
v_mfma_f32_16x16x32_bf16 v[252:255],a[248:251],v[24:27],v[252:255]
ds_read_u16 v49,v49
v_cvt_pk_bf16_f32 v119,v78,v79
v_mfma_f32_32x32x16_bf16 v[64:79],a[16:19],a[16:19],0
v_mul_f32_dpp v124,v14,v124 row_newbcast:8 row_mask:0xf bank_mask:0xf
v_lshlrev_b32_sdwa v51,1,v51 dst_sel:DWORD dst_unused:UNUSED_PAD src0_sel:DWORD src1_sel:WORD_1
v_accvgpr_write_b32 a36,v116
v_mul_f32_dpp v125,v14,v125 row_newbcast:9 row_mask:0xf bank_mask:0xf
v_mul_f32_dpp v126,v14,v126 row_newbcast:10 row_mask:0xf bank_mask:0xf
v_mfma_f32_16x16x32_bf16 v[196:199],a[252:255],v[24:27],v[196:199]
ds_read_u16_d16_hi v51,v51
v_accvgpr_write_b32 a37,v117
v_mfma_f32_16x16x32_bf16 v[192:195],a[252:255],v[28:31],v[192:195]
s_nop 0
ds_read_b64_tr_b16 a[224:225],v3 offset:8
v_mul_f32_dpp v127,v14,v127 row_newbcast:11 row_mask:0xf bank_mask:0xf
v_mul_f32_dpp v92,v14,v92 row_newbcast:12 row_mask:0xf bank_mask:0xf
ds_read_b64_tr_b16 a[226:227],v3 offset:1544
s_mov_b64 exec,0
s_cmp_eq_u32 s52,0
s_cbranch_scc1 TE10_%=
s_cmp_eq_u32 s52,1
s_cbranch_scc1 TE11_%=
s_cmp_eq_u32 s52,2
s_cbranch_scc1 TE12_%=
s_branch TE13_%=
TR1_%=:
s_mov_b64 exec,-1
v_add_u32_sdwa v28,s60,v10 dst_sel:DWORD dst_unused:UNUSED_PAD src0_sel:DWORD src1_sel:WORD_1
v_subrev_u32 v8,88400,v28
v_min_u32_e64 v28,v28,v8
v_add_u32 v29,7072,v28
v_subrev_u32 v8,88400,v29
v_min_u32_e64 v29,v29,v8
s_waitcnt lgkmcnt(15)
v_lshl_add_u64 v[60:61],v[60:61],0,v[62:63]
s_waitcnt lgkmcnt(11)
v_lshl_add_u64 v[62:63],v[32:33],0,v[34:35]
s_waitcnt lgkmcnt(6)
v_lshl_add_u64 v[32:33],v[44:45],0,v[46:47]
s_waitcnt lgkmcnt(2)
v_lshl_add_u64 v[34:35],v[48:49],0,v[50:51]
v_mfma_f32_16x16x32_bf16 v[208:211],a[228:231],v[60:63],v[208:211]
v_xad_u32 v24,v11,0,v28
v_xad_u32 v25,v11,8,v29
v_xad_u32 v26,v11,16,v28
v_xad_u32 v27,v11,24,v29
v_mfma_f32_16x16x32_bf16 v[212:215],a[228:231],v[32:35],v[212:215]
ds_read_b64 a[0:1],v24 offset:0
v_accvgpr_write_b32 a38,v118
v_mul_f32_dpp v93,v14,v93 row_newbcast:13 row_mask:0xf bank_mask:0xf
v_mul_f32_dpp v94,v14,v94 row_newbcast:14 row_mask:0xf bank_mask:0xf
v_accvgpr_write_b32 a39,v119
v_mfma_f32_16x16x32_bf16 v[172:175],a[232:235],v[32:35],v[172:175]
buffer_store_dwordx4 v[148:151],v7,s[40:43],s8 offen offset:256 sc1
ds_read_b64 a[2:3],v24 offset:192
v_mul_f32_dpp v95,v14,v95 row_newbcast:15 row_mask:0xf bank_mask:0xf
v_cvt_pk_bf16_f32 v124,v124,v125
v_cvt_pk_bf16_f32 v125,v126,v127
v_mfma_f32_16x16x32_bf16 v[168:171],a[232:235],v[60:63],v[168:171]
ds_read_b64 a[4:5],v25 offset:0
v_cvt_pk_bf16_f32 v126,v92,v93
v_cvt_pk_bf16_f32 v127,v94,v95
v_mfma_f32_32x32x16_bf16 v[80:95],a[16:19],a[16:19],0
v_mfma_f32_16x16x32_bf16 v[216:219],a[236:239],v[60:63],v[216:219]
ds_read_b64 a[6:7],v25 offset:192
v_mul_f32_dpp v132,v15,v132 row_newbcast:0 row_mask:0xf bank_mask:0xf
v_accvgpr_write_b32 a40,v124
v_mul_f32_dpp v133,v15,v133 row_newbcast:1 row_mask:0xf bank_mask:0xf
v_mfma_f32_16x16x32_bf16 v[220:223],a[236:239],v[32:35],v[220:223]
ds_read_b64 a[8:9],v24 offset:8
v_mul_f32_dpp v134,v15,v134 row_newbcast:2 row_mask:0xf bank_mask:0xf
v_accvgpr_write_b32 a41,v125
v_mul_f32_dpp v135,v15,v135 row_newbcast:3 row_mask:0xf bank_mask:0xf
v_mfma_f32_16x16x32_bf16 v[188:191],a[244:247],v[32:35],v[188:191]
ds_read_b64 a[10:11],v24 offset:200
v_mul_f32_dpp v108,v15,v108 row_newbcast:4 row_mask:0xf bank_mask:0xf
v_accvgpr_write_b32 a42,v126
v_mul_f32_dpp v109,v15,v109 row_newbcast:5 row_mask:0xf bank_mask:0xf
v_mfma_f32_16x16x32_bf16 v[184:187],a[244:247],v[60:63],v[184:187]
ds_read_b64 a[12:13],v25 offset:8
v_mul_f32_dpp v110,v15,v110 row_newbcast:6 row_mask:0xf bank_mask:0xf
v_accvgpr_write_b32 a43,v127
v_mul_f32_dpp v111,v15,v111 row_newbcast:7 row_mask:0xf bank_mask:0xf
v_mfma_f32_16x16x32_bf16 v[224:227],a[248:251],v[60:63],v[224:227]
ds_read_b64 a[14:15],v25 offset:200
v_cvt_pk_bf16_f32 v132,v132,v133
v_cvt_pk_bf16_f32 v133,v134,v135
v_cvt_pk_bf16_f32 v134,v108,v109
v_mfma_f32_16x16x32_bf16 v[228:231],a[248:251],v[32:35],v[228:231]
v_cvt_pk_bf16_f32 v135,v110,v111
v_mfma_f32_32x32x16_bf16 v[96:111],a[16:19],a[16:19],0
v_accvgpr_write_b32 a44,v132
v_mfma_f32_16x16x32_bf16 v[204:207],a[252:255],v[32:35],v[204:207]
v_accvgpr_write_b32 a45,v133
v_accvgpr_write_b32 a46,v134
v_accvgpr_write_b32 a47,v135
v_mfma_f32_16x16x32_bf16 v[200:203],a[252:255],v[60:63],v[200:203]
v_mfma_f32_32x32x16_bf16 v[112:127],a[16:19],a[16:19],0
v_mov_b64 v[44:45],0
v_mov_b64 v[46:47],0
ds_read_b64 a[16:17],v26 offset:0
ds_read_b64 a[18:19],v26 offset:192
ds_read_b64 a[20:21],v27 offset:0
ds_read_b64 a[22:23],v27 offset:192
s_waitcnt lgkmcnt(8)
v_smfmac_f32_16x16x64_bf16 v[64:67],a[96:99],a[0:7],v10
s_waitcnt lgkmcnt(4)
ds_read_b64 a[0:1],v26 offset:8
ds_read_b64 a[2:3],v26 offset:200
ds_read_b64 a[4:5],v27 offset:8
ds_read_b64 a[6:7],v27 offset:200
v_smfmac_f32_16x16x64_bf16 v[68:71],a[100:103],a[8:15],v10
s_waitcnt lgkmcnt(4)
ds_read_b64 a[8:9],v24 offset:64
ds_read_b64 a[10:11],v24 offset:256
ds_read_b64 a[12:13],v25 offset:64
ds_read_b64 a[14:15],v25 offset:256
v_smfmac_f32_16x16x64_bf16 v[72:75],a[104:107],a[16:23],v10
s_waitcnt lgkmcnt(4)
buffer_store_dwordx4 v[156:159],v7,s[40:43],s8 offen offset:320 sc1
ds_read_b64 a[16:17],v24 offset:72
ds_read_b64 a[18:19],v24 offset:264
ds_read_b64 a[20:21],v25 offset:72
ds_read_b64 a[22:23],v25 offset:264
v_and_b32 v60,8,v11
v_pk_mul_f32 v[32:33],v[64:65],v[64:65]
v_pk_mul_f32 v[34:35],v[66:67],v[66:67]
v_smfmac_f32_16x16x64_bf16 v[76:79],a[108:111],a[0:7],v10
s_waitcnt lgkmcnt(4)
ds_read_b64 a[0:1],v26 offset:64
ds_read_b64 a[2:3],v26 offset:256
ds_read_b64 a[4:5],v27 offset:64
ds_read_b64 a[6:7],v27 offset:256
v_mul_u32_u24 v60,884,v60
s_add_u32 s64,s60,3536
v_pk_add_f32 v[48:49],v[64:65],v[68:69]
v_pk_fma_f32 v[32:33],v[68:69],v[68:69],v[32:33]
v_pk_add_f32 v[50:51],v[66:67],v[70:71]
v_pk_fma_f32 v[34:35],v[70:71],v[70:71],v[34:35]
v_smfmac_f32_16x16x64_bf16 v[80:83],a[128:131],a[8:15],v10
s_waitcnt lgkmcnt(4)
ds_read_b64 a[8:9],v26 offset:72
ds_read_b64 a[10:11],v26 offset:264
ds_read_b64 a[12:13],v27 offset:72
ds_read_b64 a[14:15],v27 offset:264
v_add_u32 v63,s64,v60
v_pk_add_f32 v[48:49],v[48:49],v[72:73]
v_pk_fma_f32 v[32:33],v[72:73],v[72:73],v[32:33]
v_pk_add_f32 v[50:51],v[50:51],v[74:75]
v_pk_fma_f32 v[34:35],v[74:75],v[74:75],v[34:35]
v_smfmac_f32_16x16x64_bf16 v[84:87],a[132:135],a[16:23],v10
s_waitcnt lgkmcnt(4)
ds_read_b64 a[16:17],v24 offset:128
ds_read_b64 a[18:19],v24 offset:320
ds_read_b64 a[20:21],v25 offset:128
ds_read_b64 a[22:23],v25 offset:320
v_subrev_u32 v8,88400,v63
v_pk_add_f32 v[48:49],v[48:49],v[76:77]
v_pk_fma_f32 v[32:33],v[76:77],v[76:77],v[32:33]
v_pk_add_f32 v[50:51],v[50:51],v[78:79]
v_pk_fma_f32 v[34:35],v[78:79],v[78:79],v[34:35]
v_smfmac_f32_16x16x64_bf16 v[88:91],a[136:139],a[0:7],v10
s_waitcnt lgkmcnt(4)
ds_read_b64 a[0:1],v24 offset:136
ds_read_b64 a[2:3],v24 offset:328
ds_read_b64 a[4:5],v25 offset:136
ds_read_b64 a[6:7],v25 offset:328
v_min_u32 v63,v63,v8
v_pk_add_f32 v[48:49],v[48:49],v[80:81]
v_pk_fma_f32 v[32:33],v[80:81],v[80:81],v[32:33]
v_pk_add_f32 v[50:51],v[50:51],v[82:83]
v_pk_fma_f32 v[34:35],v[82:83],v[82:83],v[34:35]
v_smfmac_f32_16x16x64_bf16 v[92:95],a[140:143],a[8:15],v10
s_waitcnt lgkmcnt(4)
ds_read_b64 a[8:9],v26 offset:128
ds_read_b64 a[10:11],v26 offset:320
ds_read_b64 a[12:13],v27 offset:128
ds_read_b64 a[14:15],v27 offset:320
v_xad_u32 v24,v11,32,v28
v_xad_u32 v25,v11,40,v29
v_pk_add_f32 v[48:49],v[48:49],v[84:85]
v_pk_fma_f32 v[32:33],v[84:85],v[84:85],v[32:33]
v_pk_add_f32 v[50:51],v[50:51],v[86:87]
v_pk_fma_f32 v[34:35],v[86:87],v[86:87],v[34:35]
v_smfmac_f32_16x16x64_bf16 v[96:99],a[160:163],a[16:23],v10
s_waitcnt lgkmcnt(4)
ds_read_b64 a[16:17],v26 offset:136
ds_read_b64 a[18:19],v26 offset:328
ds_read_b64 a[20:21],v27 offset:136
ds_read_b64 a[22:23],v27 offset:328
v_add_u32 v62,3536,v63
v_pk_add_f32 v[48:49],v[48:49],v[88:89]
v_pk_fma_f32 v[32:33],v[88:89],v[88:89],v[32:33]
v_pk_add_f32 v[50:51],v[50:51],v[90:91]
v_pk_fma_f32 v[34:35],v[90:91],v[90:91],v[34:35]
v_smfmac_f32_16x16x64_bf16 v[100:103],a[164:167],a[0:7],v10
s_waitcnt lgkmcnt(4)
buffer_store_dwordx4 a[24:27],v7,s[40:43],s9 offen offset:0 sc1
ds_read_b64 a[0:1],v24 offset:0
ds_read_b64 a[2:3],v24 offset:192
ds_read_b64 a[4:5],v25 offset:0
ds_read_b64 a[6:7],v25 offset:192
v_xad_u32 v26,v11,48,v28
v_xad_u32 v27,v11,56,v29
v_pk_add_f32 v[48:49],v[48:49],v[92:93]
v_pk_fma_f32 v[32:33],v[92:93],v[92:93],v[32:33]
v_pk_add_f32 v[50:51],v[50:51],v[94:95]
v_pk_fma_f32 v[34:35],v[94:95],v[94:95],v[34:35]
v_smfmac_f32_16x16x64_bf16 v[104:107],a[168:171],a[8:15],v10
s_waitcnt lgkmcnt(4)
ds_read_b64 a[8:9],v24 offset:8
ds_read_b64 a[10:11],v24 offset:200
ds_read_b64 a[12:13],v25 offset:8
ds_read_b64 a[14:15],v25 offset:200
v_subrev_u32 v8,88400,v62
v_pk_add_f32 v[48:49],v[48:49],v[96:97]
v_pk_fma_f32 v[32:33],v[96:97],v[96:97],v[32:33]
v_pk_add_f32 v[50:51],v[50:51],v[98:99]
v_pk_fma_f32 v[34:35],v[98:99],v[98:99],v[34:35]
v_smfmac_f32_16x16x64_bf16 v[108:111],a[172:175],a[16:23],v10
s_waitcnt lgkmcnt(4)
ds_read_b64 a[16:17],v24 offset:64
ds_read_b64 a[18:19],v24 offset:256
ds_read_b64 a[20:21],v25 offset:64
ds_read_b64 a[22:23],v25 offset:256
v_min_u32 v62,v62,v8
v_pk_add_f32 v[48:49],v[48:49],v[100:101]
v_pk_fma_f32 v[32:33],v[100:101],v[100:101],v[32:33]
v_pk_add_f32 v[50:51],v[50:51],v[102:103]
v_pk_fma_f32 v[34:35],v[102:103],v[102:103],v[34:35]
v_smfmac_f32_16x16x64_bf16 v[112:115],a[112:115],a[0:7],v10
s_waitcnt lgkmcnt(4)
ds_read_b64 a[0:1],v24 offset:72
ds_read_b64 a[2:3],v24 offset:264
ds_read_b64 a[4:5],v25 offset:72
ds_read_b64 a[6:7],v25 offset:264
v_xor_b32 v9,0x80008,v12
v_pk_add_f32 v[48:49],v[48:49],v[104:105]
v_pk_fma_f32 v[32:33],v[104:105],v[104:105],v[32:33]
v_pk_add_f32 v[50:51],v[50:51],v[106:107]
v_pk_fma_f32 v[34:35],v[106:107],v[106:107],v[34:35]
v_mfma_f32_32x32x16_bf16 v[128:143],v[44:47],v[44:47],0
v_smfmac_f32_16x16x64_bf16 v[116:119],a[116:119],a[8:15],v10
s_waitcnt lgkmcnt(4)
ds_read_b64 a[8:9],v24 offset:128
ds_read_b64 a[10:11],v24 offset:320
ds_read_b64 a[12:13],v25 offset:128
ds_read_b64 a[14:15],v25 offset:320
v_add_u32_sdwa v60,v12,v63 dst_sel:DWORD dst_unused:UNUSED_PAD src0_sel:WORD_0 src1_sel:DWORD
v_pk_add_f32 v[48:49],v[48:49],v[108:109]
v_pk_fma_f32 v[32:33],v[108:109],v[108:109],v[32:33]
v_pk_add_f32 v[50:51],v[50:51],v[110:111]
v_pk_fma_f32 v[34:35],v[110:111],v[110:111],v[34:35]
v_smfmac_f32_16x16x64_bf16 v[120:123],a[144:147],a[16:23],v10
s_waitcnt lgkmcnt(4)
ds_read_b64 a[16:17],v24 offset:136
ds_read_b64 a[18:19],v24 offset:328
ds_read_b64 a[20:21],v25 offset:136
ds_read_b64 a[22:23],v25 offset:328
v_add_u32_sdwa v61,v12,v63 dst_sel:DWORD dst_unused:UNUSED_PAD src0_sel:WORD_1 src1_sel:DWORD
v_pk_add_f32 v[48:49],v[48:49],v[112:113]
v_pk_fma_f32 v[32:33],v[112:113],v[112:113],v[32:33]
v_pk_add_f32 v[50:51],v[50:51],v[114:115]
v_pk_fma_f32 v[34:35],v[114:115],v[114:115],v[34:35]
v_smfmac_f32_16x16x64_bf16 v[124:127],a[148:151],a[0:7],v10
s_waitcnt lgkmcnt(4)
buffer_store_dwordx4 a[28:31],v7,s[40:43],s9 offen offset:64 sc1
ds_read_b64 a[0:1],v26 offset:0
ds_read_b64 a[2:3],v26 offset:192
ds_read_b64 a[4:5],v27 offset:0
ds_read_b64 a[6:7],v27 offset:192
v_add_u32_sdwa v63,v9,v62 dst_sel:DWORD dst_unused:UNUSED_PAD src0_sel:WORD_1 src1_sel:DWORD
v_pk_add_f32 v[48:49],v[48:49],v[116:117]
v_pk_fma_f32 v[32:33],v[116:117],v[116:117],v[32:33]
v_pk_add_f32 v[50:51],v[50:51],v[118:119]
v_pk_fma_f32 v[34:35],v[118:119],v[118:119],v[34:35]
v_smfmac_f32_16x16x64_bf16 v[128:131],a[176:179],a[8:15],v10
s_waitcnt lgkmcnt(4)
ds_read_b64 a[8:9],v26 offset:8
ds_read_b64 a[10:11],v26 offset:200
ds_read_b64 a[12:13],v27 offset:8
ds_read_b64 a[14:15],v27 offset:200
v_add_u32_sdwa v62,v9,v62 dst_sel:DWORD dst_unused:UNUSED_PAD src0_sel:WORD_0 src1_sel:DWORD
v_pk_add_f32 v[48:49],v[48:49],v[120:121]
v_pk_fma_f32 v[32:33],v[120:121],v[120:121],v[32:33]
v_pk_add_f32 v[50:51],v[50:51],v[122:123]
v_pk_fma_f32 v[34:35],v[122:123],v[122:123],v[34:35]
v_mfma_f32_32x32x16_bf16 v[144:159],v[44:47],v[44:47],0
v_smfmac_f32_16x16x64_bf16 v[132:135],a[180:183],a[16:23],v10
s_waitcnt lgkmcnt(4)
ds_read_b64 a[16:17],v26 offset:64
ds_read_b64 a[18:19],v26 offset:256
ds_read_b64 a[20:21],v27 offset:64
ds_read_b64 a[22:23],v27 offset:256
v_pk_add_f32 v[48:49],v[48:49],v[124:125]
v_pk_fma_f32 v[32:33],v[124:125],v[124:125],v[32:33]
v_pk_add_f32 v[50:51],v[50:51],v[126:127]
v_pk_fma_f32 v[34:35],v[126:127],v[126:127],v[34:35]
v_smfmac_f32_16x16x64_bf16 v[136:139],a[120:123],a[0:7],v10
s_waitcnt lgkmcnt(4)
ds_read_b64 a[0:1],v26 offset:72
ds_read_b64 a[2:3],v26 offset:264
ds_read_b64 a[4:5],v27 offset:72
ds_read_b64 a[6:7],v27 offset:264
v_pk_add_f32 v[48:49],v[48:49],v[128:129]
v_pk_fma_f32 v[32:33],v[128:129],v[128:129],v[32:33]
v_pk_add_f32 v[50:51],v[50:51],v[130:131]
v_pk_fma_f32 v[34:35],v[130:131],v[130:131],v[34:35]
v_smfmac_f32_16x16x64_bf16 v[140:143],a[124:127],a[8:15],v10
s_waitcnt lgkmcnt(4)
ds_read_b64 a[8:9],v26 offset:128
ds_read_b64 a[10:11],v26 offset:320
ds_read_b64 a[12:13],v27 offset:128
ds_read_b64 a[14:15],v27 offset:320
v_pk_add_f32 v[48:49],v[48:49],v[132:133]
v_pk_fma_f32 v[32:33],v[132:133],v[132:133],v[32:33]
v_pk_add_f32 v[50:51],v[50:51],v[134:135]
v_pk_fma_f32 v[34:35],v[134:135],v[134:135],v[34:35]
v_smfmac_f32_16x16x64_bf16 v[144:147],a[152:155],a[16:23],v10
s_waitcnt lgkmcnt(4)
ds_read_b64 a[16:17],v26 offset:136
ds_read_b64 a[18:19],v26 offset:328
ds_read_b64 a[20:21],v27 offset:136
ds_read_b64 a[22:23],v27 offset:328
v_pk_add_f32 v[48:49],v[48:49],v[136:137]
v_pk_fma_f32 v[32:33],v[136:137],v[136:137],v[32:33]
v_pk_add_f32 v[50:51],v[50:51],v[138:139]
v_pk_fma_f32 v[34:35],v[138:139],v[138:139],v[34:35]
v_smfmac_f32_16x16x64_bf16 v[148:151],a[156:159],a[0:7],v10
s_waitcnt lgkmcnt(4)
ds_read_b64 a[228:229],v60 offset:57344
ds_read_b64 a[230:231],v60 offset:57352
ds_read_b64 a[232:233],v61 offset:57344
ds_read_b64 a[234:235],v61 offset:57352
ds_read_b128 a[248:251],v13 offset:0
v_pk_add_f32 v[48:49],v[48:49],v[140:141]
v_pk_fma_f32 v[32:33],v[140:141],v[140:141],v[32:33]
v_pk_add_f32 v[50:51],v[50:51],v[142:143]
v_pk_fma_f32 v[34:35],v[142:143],v[142:143],v[34:35]
v_smfmac_f32_16x16x64_bf16 v[152:155],a[184:187],a[8:15],v10
s_waitcnt lgkmcnt(5)
buffer_store_dwordx4 a[32:35],v7,s[40:43],s9 offen offset:128 sc1
ds_read_b64 a[236:237],v62 offset:57344
ds_read_b64 a[238:239],v62 offset:57352
ds_read_b64 a[244:245],v63 offset:57344
ds_read_b64 a[246:247],v63 offset:57352
ds_read_b128 a[252:255],v13 offset:256
v_pk_add_f32 v[48:49],v[48:49],v[144:145]
v_pk_fma_f32 v[32:33],v[144:145],v[144:145],v[32:33]
v_pk_add_f32 v[50:51],v[50:51],v[146:147]
v_pk_fma_f32 v[34:35],v[146:147],v[146:147],v[34:35]
v_smfmac_f32_16x16x64_bf16 v[156:159],a[188:191],a[16:23],v10
ds_read_b128 v[24:27],v4 offset:3584
TP5417_%=:
s_cbranch_execz TR0_%=
s_nop 0
ds_read_b128 v[28:31],v4 offset:3616
v_pk_add_f32 v[48:49],v[48:49],v[148:149]
v_pk_fma_f32 v[32:33],v[148:149],v[148:149],v[32:33]
v_pk_add_f32 v[50:51],v[50:51],v[150:151]
v_pk_fma_f32 v[34:35],v[150:151],v[150:151],v[34:35]
v_pk_add_f32 v[48:49],v[48:49],v[152:153]
v_pk_fma_f32 v[32:33],v[152:153],v[152:153],v[32:33]
v_pk_add_f32 v[50:51],v[50:51],v[154:155]
v_pk_fma_f32 v[34:35],v[154:155],v[154:155],v[34:35]
v_pk_add_f32 v[48:49],v[48:49],v[156:157]
v_pk_fma_f32 v[32:33],v[156:157],v[156:157],v[32:33]
v_pk_add_f32 v[50:51],v[50:51],v[158:159]
v_pk_fma_f32 v[34:35],v[158:159],v[158:159],v[34:35]
s_nop 0
v_permlane32_swap_b32 v48,v50
v_permlane32_swap_b32 v49,v51
v_permlane32_swap_b32 v32,v34
v_permlane32_swap_b32_e64 v33,v35
v_pk_add_f32 v[48:49],v[48:49],v[50:51]
v_pk_add_f32 v[32:33],v[32:33],v[34:35]
s_add_u32 s60,s60,56576
s_sub_u32 s64,s60,88400
s_min_u32 s60,s60,s64
v_permlane16_swap_b32 v48,v49
v_permlane16_swap_b32 v32,v33
v_add_f32 v8,v48,v49
v_add_f32_e64 v9,v32,v33
v_mul_f32 v8,0x3c2aaaab,v8
v_mul_f32 v9,0x3c2aaaab,v9
v_cmp_ngt_f32 vcc,0x5d800000,v9
v_fma_f32 v9,-v8,v8,v9
v_max_f32 v9,0,v9
s_or_b64 s[90:91],s[90:91],vcc
v_add_f32 v9,0x3727c5ac,v9
v_rsq_f32 v9,v9
s_nop 0
v_mul_f32 v48,s96,v9
v_mul_f32 v49,s96,v9
v_mul_f32 v32,-v8,v48
v_mul_f32 v33,-v8,v48
v_permlane16_swap_b32 v48,v49
s_nop 0
v_permlane16_swap_b32 v32,v33
v_mov_b64 v[50:51],v[48:49]
v_mov_b64 v[34:35],v[32:33]
s_nop 0
v_permlane32_swap_b32 v48,v50
v_permlane32_swap_b32 v49,v51
v_permlane32_swap_b32 v32,v34
v_permlane32_swap_b32 v33,v35
v_pk_fma_f32 v[64:65],v[64:65],v[48:49],v[32:33]
v_pk_fma_f32 v[66:67],v[66:67],v[50:51],v[34:35]
v_pk_fma_f32 v[68:69],v[68:69],v[48:49],v[32:33]
v_pk_fma_f32 v[70:71],v[70:71],v[50:51],v[34:35]
v_pk_fma_f32 v[72:73],v[72:73],v[48:49],v[32:33]
v_pk_fma_f32 v[74:75],v[74:75],v[50:51],v[34:35]
v_pk_fma_f32 v[76:77],v[76:77],v[48:49],v[32:33]
v_pk_fma_f32 v[78:79],v[78:79],v[50:51],v[34:35]
v_pk_fma_f32 v[112:113],v[112:113],v[48:49],v[32:33]
v_pk_fma_f32 v[114:115],v[114:115],v[50:51],v[34:35]
v_pk_fma_f32 v[116:117],v[116:117],v[48:49],v[32:33]
v_pk_fma_f32 v[118:119],v[118:119],v[50:51],v[34:35]
v_pk_fma_f32 v[136:137],v[136:137],v[48:49],v[32:33]
v_pk_fma_f32 v[138:139],v[138:139],v[50:51],v[34:35]
v_pk_fma_f32 v[140:141],v[140:141],v[48:49],v[32:33]
v_pk_fma_f32 v[142:143],v[142:143],v[50:51],v[34:35]
v_pk_fma_f32 v[80:81],v[80:81],v[48:49],v[32:33]
v_pk_fma_f32 v[82:83],v[82:83],v[50:51],v[34:35]
v_pk_fma_f32 v[84:85],v[84:85],v[48:49],v[32:33]
v_pk_fma_f32 v[86:87],v[86:87],v[50:51],v[34:35]
v_pk_fma_f32 v[88:89],v[88:89],v[48:49],v[32:33]
v_pk_fma_f32 v[90:91],v[90:91],v[50:51],v[34:35]
v_pk_fma_f32 v[92:93],v[92:93],v[48:49],v[32:33]
v_pk_fma_f32 v[94:95],v[94:95],v[50:51],v[34:35]
v_pk_fma_f32 v[120:121],v[120:121],v[48:49],v[32:33]
v_pk_fma_f32 v[122:123],v[122:123],v[50:51],v[34:35]
v_pk_fma_f32 v[124:125],v[124:125],v[48:49],v[32:33]
v_pk_fma_f32 v[126:127],v[126:127],v[50:51],v[34:35]
v_pk_fma_f32 v[144:145],v[144:145],v[48:49],v[32:33]
v_pk_fma_f32 v[146:147],v[146:147],v[50:51],v[34:35]
v_pk_fma_f32 v[148:149],v[148:149],v[48:49],v[32:33]
v_pk_fma_f32 v[150:151],v[150:151],v[50:51],v[34:35]
v_pk_fma_f32 v[96:97],v[96:97],v[48:49],v[32:33]
v_pk_fma_f32 v[98:99],v[98:99],v[50:51],v[34:35]
v_pk_fma_f32 v[100:101],v[100:101],v[48:49],v[32:33]
v_pk_fma_f32 v[102:103],v[102:103],v[50:51],v[34:35]
v_pk_fma_f32 v[104:105],v[104:105],v[48:49],v[32:33]
v_pk_fma_f32 v[106:107],v[106:107],v[50:51],v[34:35]
v_pk_fma_f32 v[108:109],v[108:109],v[48:49],v[32:33]
v_pk_fma_f32 v[110:111],v[110:111],v[50:51],v[34:35]
v_pk_fma_f32 v[128:129],v[128:129],v[48:49],v[32:33]
v_pk_fma_f32 v[130:131],v[130:131],v[50:51],v[34:35]
v_pk_fma_f32 v[132:133],v[132:133],v[48:49],v[32:33]
v_pk_fma_f32 v[134:135],v[134:135],v[50:51],v[34:35]
v_pk_fma_f32 v[152:153],v[152:153],v[48:49],v[32:33]
v_pk_fma_f32 v[154:155],v[154:155],v[50:51],v[34:35]
v_pk_fma_f32 v[156:157],v[156:157],v[48:49],v[32:33]
v_pk_fma_f32 v[158:159],v[158:159],v[50:51],v[34:35]
v_mfma_f32_16x16x32_bf16 v[44:47],a[220:223],a[56:59],v[16:19]
buffer_store_dwordx4 a[36:39],v7,s[40:43],s9 offen offset:192 sc1
v_cvt_pk_bf16_f32 v64,v64,v68
v_cvt_pk_bf16_f32 v72,v72,v76
v_cvt_pk_bf16_f32 v112,v112,v116
v_lshlrev_b32_sdwa v36,1,v36 dst_sel:DWORD dst_unused:UNUSED_PAD src0_sel:DWORD src1_sel:WORD_1
v_mfma_f32_16x16x32_bf16 v[44:47],a[216:219],a[72:75],v[44:47]
ds_read_u16 v36,v36
v_accvgpr_write_b32 a0,v64
v_accvgpr_write_b32 a1,v72
v_cvt_pk_bf16_f32 v136,v136,v140
v_accvgpr_write_b32 a2,v112
v_mfma_f32_16x16x32_bf16 v[44:47],a[192:195],a[88:91],v[44:47]
ds_read_b64_tr_b16 a[204:205],v1 offset:0
v_cvt_pk_bf16_f32 v65,v65,v69
v_accvgpr_write_b32 a3,v136
v_cvt_pk_bf16_f32 v73,v73,v77
v_accvgpr_write_b32 a4,v65
v_mfma_f32_16x16x32_bf16 v[48:51],a[196:199],a[88:91],v[20:23]
ds_read_b64_tr_b16 a[206:207],v1 offset:1024
v_cvt_pk_bf16_f32 v113,v113,v117
v_accvgpr_write_b32 a5,v73
v_cvt_pk_bf16_f32 v137,v137,v141
v_lshlrev_b32_sdwa v38,1,v38 dst_sel:DWORD dst_unused:UNUSED_PAD src0_sel:DWORD src1_sel:WORD_1
v_accvgpr_write_b32 a6,v113
v_mfma_f32_16x16x32_bf16 v[48:51],a[200:203],a[72:75],v[48:51]
ds_read_u16_d16_hi v38,v38
v_cvt_pk_bf16_f32 v66,v66,v70
v_accvgpr_write_b32 a7,v137
v_cvt_pk_bf16_f32 v74,v74,v78
v_accvgpr_write_b32 a8,v66
v_mfma_f32_16x16x32_bf16 v[48:51],a[212:215],a[56:59],v[48:51]
ds_read_b64_tr_b16 a[208:209],v1 offset:2048
v_accvgpr_write_b32 a9,v74
v_cvt_pk_bf16_f32 v114,v114,v118
v_accvgpr_write_b32 a10,v114
v_lshlrev_b32_sdwa v37,1,v37 dst_sel:DWORD dst_unused:UNUSED_PAD src0_sel:DWORD src1_sel:WORD_1
v_cvt_pk_bf16_f32 v138,v138,v142
v_mfma_f32_16x16x32_bf16 v[32:35],a[212:215],a[60:63],v[20:23]
buffer_store_dwordx4 a[40:43],v7,s[40:43],s9 offen offset:256 sc1
v_accvgpr_write_b32 a11,v138
v_cvt_pk_bf16_f32 v67,v67,v71
v_accvgpr_write_b32 a12,v67
v_mfma_f32_16x16x32_bf16 v[32:35],a[200:203],a[76:79],v[32:35]
ds_read_u16 v37,v37
v_cvt_pk_bf16_f32 v75,v75,v79
v_accvgpr_write_b32 a13,v75
v_cvt_pk_bf16_f32 v115,v115,v119
v_accvgpr_write_b32 a14,v115
v_lshlrev_b32_sdwa v39,1,v39 dst_sel:DWORD dst_unused:UNUSED_PAD src0_sel:DWORD src1_sel:WORD_1
v_mfma_f32_16x16x32_bf16 v[32:35],a[196:199],a[92:95],v[32:35]
s_waitcnt lgkmcnt(6)
ds_read_u16_d16_hi v39,v39
v_cvt_pk_bf16_f32 v139,v139,v143
v_accvgpr_write_b32 a15,v139
v_cvt_pk_bf16_f32 v80,v80,v84
v_cvt_pk_bf16_f32 v88,v88,v92
v_mfma_f32_16x16x32_bf16 v[136:139],a[248:251],a[228:231],v[24:27]
s_nop 0
ds_read_b64_tr_b16 a[210:211],v1 offset:3072
v_cvt_pk_bf16_f32 v120,v120,v124
v_accvgpr_write_b32 a16,v80
v_accvgpr_write_b32 a17,v88
v_cvt_pk_bf16_f32 v144,v144,v148
v_lshlrev_b32_sdwa v40,1,v40 dst_sel:DWORD dst_unused:UNUSED_PAD src0_sel:DWORD src1_sel:WORD_1
v_mfma_f32_16x16x32_bf16 v[140:143],a[248:251],a[232:235],v[24:27]
ds_read_u16 v40,v40
v_accvgpr_write_b32 a18,v120
v_cvt_pk_bf16_f32 v81,v81,v85
v_accvgpr_write_b32 a19,v144
v_cvt_pk_bf16_f32 v89,v89,v93
v_mfma_f32_16x16x32_bf16 v[112:115],a[248:251],a[236:239],v[24:27]
v_accvgpr_write_b32 a20,v81
v_cvt_pk_bf16_f32 v121,v121,v125
v_accvgpr_write_b32 a21,v89
v_lshlrev_b32_sdwa v42,1,v42 dst_sel:DWORD dst_unused:UNUSED_PAD src0_sel:DWORD src1_sel:WORD_1
v_cvt_pk_bf16_f32 v145,v145,v149
v_mfma_f32_16x16x32_bf16 v[116:119],a[248:251],a[244:247],v[24:27]
ds_read_b128 a[248:251],v13 offset:512
ds_read_b128 v[24:27],v4 offset:3712
v_accvgpr_write_b32 a22,v121
v_cvt_pk_bf16_f32 v82,v82,v86
v_accvgpr_write_b32 a23,v145
v_mfma_f32_16x16x32_bf16 v[64:67],a[252:255],a[228:231],v[28:31]
ds_read_b64 a[228:229],v60 offset:57408
ds_read_b64 a[230:231],v60 offset:57416
v_cvt_pk_bf16_f32 v90,v90,v94
v_accvgpr_write_b32 a24,v82
v_accvgpr_write_b32 a25,v90
v_cvt_pk_bf16_f32 v122,v122,v126
v_lshlrev_b32_sdwa v41,1,v41 dst_sel:DWORD dst_unused:UNUSED_PAD src0_sel:DWORD src1_sel:WORD_1
v_mfma_f32_16x16x32_bf16 v[68:71],a[252:255],a[232:235],v[28:31]
ds_read_b64 a[232:233],v61 offset:57408
ds_read_b64 a[234:235],v61 offset:57416
v_accvgpr_write_b32 a26,v122
v_cvt_pk_bf16_f32 v146,v146,v150
v_accvgpr_write_b32 a27,v146
v_cvt_pk_bf16_f32 v83,v83,v87
v_mfma_f32_16x16x32_bf16 v[72:75],a[252:255],a[236:239],v[28:31]
ds_read_b64 a[236:237],v62 offset:57408
ds_read_b64 a[238:239],v62 offset:57416
v_accvgpr_write_b32 a28,v83
v_cvt_pk_bf16_f32 v91,v91,v95
v_accvgpr_write_b32 a29,v91
v_cvt_pk_bf16_f32 v123,v123,v127
v_lshlrev_b32_sdwa v43,1,v43 dst_sel:DWORD dst_unused:UNUSED_PAD src0_sel:DWORD src1_sel:WORD_1
v_mfma_f32_16x16x32_bf16 v[76:79],a[252:255],a[244:247],v[28:31]
s_waitcnt lgkmcnt(4)
buffer_store_dwordx4 a[44:47],v7,s[40:43],s9 offen offset:320 sc1
ds_read_b64 a[244:245],v63 offset:57408
v_accvgpr_write_b32 a30,v123
v_cvt_pk_bf16_f32 v147,v147,v151
v_accvgpr_write_b32 a31,v147
v_cvt_pk_bf16_f32 v96,v96,v100
v_mfma_f32_16x16x32_bf16 v[144:147],a[248:251],a[228:231],v[24:27]
s_waitcnt lgkmcnt(3)
ds_read_b64 a[246:247],v63 offset:57416
ds_read_b128 a[252:255],v13 offset:768
v_cvt_pk_bf16_f32 v104,v104,v108
v_cvt_pk_bf16_f32 v128,v128,v132
v_accvgpr_write_b32 a32,v96
v_accvgpr_write_b32 a33,v104
v_lshlrev_b32_sdwa v52,1,v52 dst_sel:DWORD dst_unused:UNUSED_PAD src0_sel:DWORD src1_sel:WORD_1
v_mfma_f32_16x16x32_bf16 v[148:151],a[248:251],a[232:235],v[24:27]
s_waitcnt lgkmcnt(3)
ds_read_b128 v[28:31],v4 offset:3744
ds_read_u16_d16_hi v42,v42
v_cvt_pk_bf16_f32 v152,v152,v156
v_accvgpr_write_b32 a34,v128
v_cvt_pk_bf16_f32 v97,v97,v101
v_mfma_f32_16x16x32_bf16 v[120:123],a[248:251],a[236:239],v[24:27]
s_waitcnt lgkmcnt(3)
ds_read_u16 v41,v41
v_accvgpr_write_b32 a35,v152
v_cvt_pk_bf16_f32 v105,v105,v109
v_accvgpr_write_b32 a36,v97
v_cvt_pk_bf16_f32 v129,v129,v133
v_lshlrev_b32_sdwa v54,1,v54 dst_sel:DWORD dst_unused:UNUSED_PAD src0_sel:DWORD src1_sel:WORD_1
v_mfma_f32_16x16x32_bf16 v[124:127],a[248:251],a[244:247],v[24:27]
s_waitcnt lgkmcnt(2)
ds_read_b128 a[248:251],v13 offset:1024
ds_read_b128 v[24:27],v4 offset:3840
v_accvgpr_write_b32 a37,v105
v_cvt_pk_bf16_f32 v153,v153,v157
v_accvgpr_write_b32 a38,v129
v_cvt_pk_bf16_f32 v98,v98,v102
v_mfma_f32_16x16x32_bf16 v[80:83],a[252:255],a[228:231],v[28:31]
s_nop 0
ds_read_b64 a[228:229],v60 offset:57472
ds_read_b64 a[230:231],v60 offset:57480
v_accvgpr_write_b32 a39,v153
v_cvt_pk_bf16_f32 v106,v106,v110
v_accvgpr_write_b32 a40,v98
v_accvgpr_write_b32 a41,v106
v_lshlrev_b32_sdwa v53,1,v53 dst_sel:DWORD dst_unused:UNUSED_PAD src0_sel:DWORD src1_sel:WORD_1
v_mfma_f32_16x16x32_bf16 v[84:87],a[252:255],a[232:235],v[28:31]
ds_read_b64 a[232:233],v61 offset:57472
ds_read_b64 a[234:235],v61 offset:57480
v_cvt_pk_bf16_f32 v130,v130,v134
v_accvgpr_write_b32 a42,v130
v_cvt_pk_bf16_f32 v154,v154,v158
v_accvgpr_write_b32 a43,v154
v_mfma_f32_16x16x32_bf16 v[88:91],a[252:255],a[236:239],v[28:31]
s_nop 0
ds_read_b64 a[236:237],v62 offset:57472
ds_read_b64 a[238:239],v62 offset:57480
v_cvt_pk_bf16_f32 v99,v99,v103
v_accvgpr_write_b32 a44,v99
v_cvt_pk_bf16_f32 v107,v107,v111
v_accvgpr_write_b32 a45,v107
v_mfma_f32_16x16x32_bf16 v[92:95],a[252:255],a[244:247],v[28:31]
s_waitcnt lgkmcnt(4)
ds_read_b64 a[244:245],v63 offset:57472
ds_read_b64 a[246:247],v63 offset:57480
v_cvt_pk_bf16_f32 v131,v131,v135
v_accvgpr_write_b32 a46,v131
v_cvt_pk_bf16_f32 v155,v155,v159
v_lshlrev_b32_sdwa v55,1,v55 dst_sel:DWORD dst_unused:UNUSED_PAD src0_sel:DWORD src1_sel:WORD_1
v_accvgpr_write_b32 a47,v155
v_mfma_f32_16x16x32_bf16 v[152:155],a[248:251],a[228:231],v[24:27]
s_waitcnt lgkmcnt(4)
ds_read_b128 a[252:255],v13 offset:1280
ds_read_b128 v[28:31],v4 offset:3872
v_mfma_f32_16x16x32_bf16 v[156:159],a[248:251],a[232:235],v[24:27]
s_waitcnt lgkmcnt(4)
ds_read_u16_d16_hi v43,v43
ds_read_u16 v52,v52
v_lshlrev_b32_sdwa v56,1,v56 dst_sel:DWORD dst_unused:UNUSED_PAD src0_sel:DWORD src1_sel:WORD_1
v_mfma_f32_16x16x32_bf16 v[128:131],a[248:251],a[236:239],v[24:27]
s_waitcnt lgkmcnt(4)
ds_read_u16_d16_hi v54,v54
v_lshlrev_b32_sdwa v58,1,v58 dst_sel:DWORD dst_unused:UNUSED_PAD src0_sel:DWORD src1_sel:WORD_1
v_mfma_f32_16x16x32_bf16 v[132:135],a[248:251],a[244:247],v[24:27]
s_waitcnt lgkmcnt(3)
ds_read_b64_tr_b16 a[248:249],v3 offset:64
ds_read_b64_tr_b16 a[250:251],v3 offset:1600
v_mfma_f32_16x16x32_bf16 v[96:99],a[252:255],a[228:231],v[28:31]
ds_read_b64_tr_b16 a[228:229],v3 offset:72
ds_read_b64_tr_b16 a[230:231],v3 offset:1608
v_mfma_f32_16x16x32_bf16 v[100:103],a[252:255],a[232:235],v[28:31]
ds_read_b64_tr_b16 a[232:233],v3 offset:128
ds_read_b64_tr_b16 a[234:235],v3 offset:1664
v_lshlrev_b32_sdwa v57,1,v57 dst_sel:DWORD dst_unused:UNUSED_PAD src0_sel:DWORD src1_sel:WORD_1
v_mfma_f32_16x16x32_bf16 v[104:107],a[252:255],a[236:239],v[28:31]
ds_read_b64_tr_b16 a[236:237],v3 offset:136
ds_read_b64_tr_b16 a[238:239],v3 offset:1672
v_mfma_f32_16x16x32_bf16 v[108:111],a[252:255],a[244:247],v[28:31]
ds_read_u16 v53,v53
ds_read_u16_d16_hi v55,v55
v_lshlrev_b32_sdwa v59,1,v59 dst_sel:DWORD dst_unused:UNUSED_PAD src0_sel:DWORD src1_sel:WORD_1
v_mfma_f32_16x16x32_bf16 v[60:63],a[192:195],a[92:95],v[16:19]
ds_read_u16 v56,v56
v_mfma_f32_16x16x32_bf16 v[60:63],a[216:219],a[76:79],v[60:63]
s_waitcnt vmcnt(18)
ds_read_u16_d16_hi v58,v58
v_mfma_f32_16x16x32_bf16 v[60:63],a[220:223],a[60:63],v[60:63]
s_barrier
ds_read_u16 v57,v57
ds_read_u16_d16_hi v59,v59
v_mfma_f32_16x16x32_bf16 v[24:27],a[220:223],a[0:3],v[16:19]
s_add_u32 m0,s57,24752
v_lshlrev_b32_sdwa v44,1,v44 dst_sel:DWORD dst_unused:UNUSED_PAD src0_sel:DWORD src1_sel:WORD_1
v_lshlrev_b32_sdwa v46,1,v46 dst_sel:DWORD dst_unused:UNUSED_PAD src0_sel:DWORD src1_sel:WORD_1
v_mfma_f32_16x16x32_bf16 v[24:27],a[216:219],a[16:19],v[24:27]
s_add_u32 s89,s56,5603328
ds_read_u16 v44,v44
v_lshl_add_u64 v[36:37],v[36:37],0,v[38:39]
v_mfma_f32_16x16x32_bf16 v[24:27],a[192:195],a[32:35],v[24:27]
s_waitcnt lgkmcnt(11)
buffer_load_dwordx4 v6,s[36:39],s89 offen lds
v_lshl_add_u64 v[38:39],v[40:41],0,v[42:43]
v_mfma_f32_16x16x32_bf16 v[28:31],a[196:199],a[32:35],v[20:23]
ds_read_u16_d16_hi v46,v46
v_lshlrev_b32_sdwa v45,1,v45 dst_sel:DWORD dst_unused:UNUSED_PAD src0_sel:DWORD src1_sel:WORD_1
v_mfma_f32_16x16x32_bf16 v[28:31],a[200:203],a[16:19],v[28:31]
s_waitcnt lgkmcnt(6)
ds_read_u16 v45,v45
v_lshl_add_u64 v[40:41],v[52:53],0,v[54:55]
v_mfma_f32_16x16x32_bf16 v[28:31],a[212:215],a[0:3],v[28:31]
v_lshlrev_b32_sdwa v47,1,v47 dst_sel:DWORD dst_unused:UNUSED_PAD src0_sel:DWORD src1_sel:WORD_1
v_lshlrev_b32_sdwa v48,1,v48 dst_sel:DWORD dst_unused:UNUSED_PAD src0_sel:DWORD src1_sel:WORD_1
v_mfma_f32_16x16x32_bf16 v[52:55],a[212:215],a[4:7],v[20:23]
s_nop 0
ds_read_u16_d16_hi v47,v47
v_lshlrev_b32_sdwa v50,1,v50 dst_sel:DWORD dst_unused:UNUSED_PAD src0_sel:DWORD src1_sel:WORD_1
v_mfma_f32_16x16x32_bf16 v[52:55],a[200:203],a[20:23],v[52:55]
s_waitcnt lgkmcnt(4)
ds_read_u16 v48,v48
v_lshl_add_u64 v[42:43],v[56:57],0,v[58:59]
v_mfma_f32_16x16x32_bf16 v[52:55],a[196:199],a[36:39],v[52:55]
ds_read_u16_d16_hi v50,v50
v_lshlrev_b32_sdwa v49,1,v49 dst_sel:DWORD dst_unused:UNUSED_PAD src0_sel:DWORD src1_sel:WORD_1
v_mfma_f32_16x16x32_bf16 v[56:59],a[192:195],a[36:39],v[16:19]
ds_read_u16 v49,v49
v_lshlrev_b32_sdwa v51,1,v51 dst_sel:DWORD dst_unused:UNUSED_PAD src0_sel:DWORD src1_sel:WORD_1
v_mfma_f32_16x16x32_bf16 v[56:59],a[216:219],a[20:23],v[56:59]
ds_read_u16_d16_hi v51,v51
v_lshlrev_b32_sdwa v60,1,v60 dst_sel:DWORD dst_unused:UNUSED_PAD src0_sel:DWORD src1_sel:WORD_1
v_mfma_f32_16x16x32_bf16 v[56:59],a[220:223],a[4:7],v[56:59]
ds_read_u16 v60,v60
v_lshlrev_b32_sdwa v62,1,v62 dst_sel:DWORD dst_unused:UNUSED_PAD src0_sel:DWORD src1_sel:WORD_1
v_mfma_f32_16x16x32_bf16 v[232:235],a[240:243],v[36:39],v[232:235]
ds_read_u16_d16_hi v62,v62
v_lshlrev_b32_sdwa v61,1,v61 dst_sel:DWORD dst_unused:UNUSED_PAD src0_sel:DWORD src1_sel:WORD_1
v_mfma_f32_16x16x32_bf16 v[236:239],a[240:243],v[40:43],v[236:239]
ds_read_u16 v61,v61
v_lshlrev_b32_sdwa v63,1,v63 dst_sel:DWORD dst_unused:UNUSED_PAD src0_sel:DWORD src1_sel:WORD_1
v_mfma_f32_16x16x32_bf16 v[164:167],a[224:227],v[40:43],v[164:167]
ds_read_u16_d16_hi v63,v63
v_lshlrev_b32_sdwa v32,1,v32 dst_sel:DWORD dst_unused:UNUSED_PAD src0_sel:DWORD src1_sel:WORD_1
v_mfma_f32_16x16x32_bf16 v[160:163],a[224:227],v[36:39],v[160:163]
ds_read_u16 v32,v32
v_lshlrev_b32_sdwa v34,1,v34 dst_sel:DWORD dst_unused:UNUSED_PAD src0_sel:DWORD src1_sel:WORD_1
v_mfma_f32_16x16x32_bf16 v[240:243],a[248:251],v[36:39],v[240:243]
ds_read_u16_d16_hi v34,v34
v_lshlrev_b32_sdwa v33,1,v33 dst_sel:DWORD dst_unused:UNUSED_PAD src0_sel:DWORD src1_sel:WORD_1
v_mfma_f32_16x16x32_bf16 v[244:247],a[248:251],v[40:43],v[244:247]
ds_read_u16 v33,v33
v_lshlrev_b32_sdwa v35,1,v35 dst_sel:DWORD dst_unused:UNUSED_PAD src0_sel:DWORD src1_sel:WORD_1
v_mfma_f32_16x16x32_bf16 v[180:183],a[228:231],v[40:43],v[180:183]
ds_read_u16_d16_hi v35,v35
v_mfma_f32_16x16x32_bf16 v[176:179],a[228:231],v[36:39],v[176:179]
v_mfma_f32_16x16x32_bf16 v[248:251],a[232:235],v[36:39],v[248:251]
v_mfma_f32_16x16x32_bf16 v[252:255],a[232:235],v[40:43],v[252:255]
v_mfma_f32_16x16x32_bf16 v[196:199],a[236:239],v[40:43],v[196:199]
v_mfma_f32_16x16x32_bf16 v[192:195],a[236:239],v[36:39],v[192:195]
v_mfma_f32_16x16x32_bf16 v[40:43],a[220:223],a[8:11],v[16:19]
v_lshlrev_b32_sdwa v24,1,v24 dst_sel:DWORD dst_unused:UNUSED_PAD src0_sel:DWORD src1_sel:WORD_1
v_fma_mixlo_f16 v8,v53,s99,0
v_mfma_f32_16x16x32_bf16 v[40:43],a[216:219],a[24:27],v[40:43]
s_waitcnt lgkmcnt(2)
ds_read_u16 v24,v24
v_lshl_add_u64 v[44:45],v[44:45],0,v[46:47]
v_mfma_f32_16x16x32_bf16 v[40:43],a[192:195],a[40:43],v[40:43]
v_lshl_add_u64 v[46:47],v[48:49],0,v[50:51]
v_lshlrev_b32_sdwa v26,1,v26 dst_sel:DWORD dst_unused:UNUSED_PAD src0_sel:DWORD src1_sel:WORD_1
v_mfma_f32_16x16x32_bf16 v[36:39],a[196:199],a[40:43],v[20:23]
ds_read_u16_d16_hi v26,v26
v_fma_mixhi_f16 v8,v55,s99,0
v_mfma_f32_16x16x32_bf16 v[36:39],a[200:203],a[24:27],v[36:39]
v_lshl_add_u64 v[48:49],v[60:61],0,v[62:63]
v_lshlrev_b32_sdwa v25,1,v25 dst_sel:DWORD dst_unused:UNUSED_PAD src0_sel:DWORD src1_sel:WORD_1
v_pk_fma_f16 v9,v8,v8,s54
v_mfma_f32_16x16x32_bf16 v[36:39],a[212:215],a[8:11],v[36:39]
ds_read_u16 v25,v25
v_lshlrev_b32_sdwa v27,1,v27 dst_sel:DWORD dst_unused:UNUSED_PAD src0_sel:DWORD src1_sel:WORD_1
v_mfma_f32_16x16x32_bf16 v[60:63],a[212:215],a[12:15],v[20:23]
ds_read_b128 v[20:23],v4 offset:128
ds_read_b64_tr_b16 a[212:213],v1 offset:4096
v_pk_mul_f16 v9,v8,v9
v_mfma_f32_16x16x32_bf16 v[60:63],a[200:203],a[28:31],v[60:63]
s_waitcnt lgkmcnt(5)
ds_read_b64_tr_b16 a[214:215],v1 offset:5120
ds_read_b64_tr_b16 a[200:201],v2 offset:4096
v_lshl_add_u64 v[50:51],v[32:33],0,v[34:35]
v_lshlrev_b32_sdwa v28,1,v28 dst_sel:DWORD dst_unused:UNUSED_PAD src0_sel:DWORD src1_sel:WORD_1
v_mfma_f32_16x16x32_bf16 v[60:63],a[196:199],a[44:47],v[60:63]
ds_read_b64_tr_b16 a[202:203],v2 offset:5120
ds_read_b64_tr_b16 a[196:197],v2 offset:2048
v_exp_f16 v8,v9
v_mfma_f32_16x16x32_bf16 v[32:35],a[192:195],a[44:47],v[16:19]
ds_read_b128 v[16:19],v4 offset:160
ds_read_b64_tr_b16 a[198:199],v2 offset:3072
v_lshlrev_b32_sdwa v30,1,v30 dst_sel:DWORD dst_unused:UNUSED_PAD src0_sel:DWORD src1_sel:WORD_1
v_mfma_f32_16x16x32_bf16 v[32:35],a[216:219],a[28:31],v[32:35]
ds_read_b64_tr_b16 a[192:193],v2 offset:0
ds_read_b64_tr_b16 a[194:195],v2 offset:1024
v_lshlrev_b32_sdwa v29,1,v29 dst_sel:DWORD dst_unused:UNUSED_PAD src0_sel:DWORD src1_sel:WORD_1
v_mfma_f32_16x16x32_bf16 v[32:35],a[220:223],a[12:15],v[32:35]
ds_read_u16_d16_hi v27,v27
v_exp_f16_sdwa v8,v9 dst_sel:WORD_1 dst_unused:UNUSED_PRESERVE src0_sel:WORD_1
v_mfma_f32_16x16x32_bf16 v[208:211],a[240:243],v[44:47],v[208:211]
ds_read_u16 v28,v28
v_lshlrev_b32_sdwa v31,1,v31 dst_sel:DWORD dst_unused:UNUSED_PAD src0_sel:DWORD src1_sel:WORD_1
v_mfma_f32_16x16x32_bf16 v[212:215],a[240:243],v[48:51],v[212:215]
ds_read_u16_d16_hi v30,v30
v_lshlrev_b32_sdwa v56,1,v56 dst_sel:DWORD dst_unused:UNUSED_PAD src0_sel:DWORD src1_sel:WORD_1
v_mfma_f32_16x16x32_bf16 v[172:175],a[224:227],v[48:51],v[172:175]
ds_read_u16 v29,v29
v_pk_fma_f16 v8,v8,s55,s55
v_mfma_f32_16x16x32_bf16 v[168:171],a[224:227],v[44:47],v[168:171]
v_rcp_f16 v9,v8
v_lshlrev_b32_sdwa v58,1,v58 dst_sel:DWORD dst_unused:UNUSED_PAD src0_sel:DWORD src1_sel:WORD_1
v_mfma_f32_16x16x32_bf16 v[216:219],a[248:251],v[44:47],v[216:219]
ds_read_u16_d16_hi v31,v31
v_lshlrev_b32_sdwa v57,1,v57 dst_sel:DWORD dst_unused:UNUSED_PAD src0_sel:DWORD src1_sel:WORD_1
v_mfma_f32_16x16x32_bf16 v[220:223],a[248:251],v[48:51],v[220:223]
ds_read_u16 v56,v56
v_lshlrev_b32_sdwa v59,1,v59 dst_sel:DWORD dst_unused:UNUSED_PAD src0_sel:DWORD src1_sel:WORD_1
v_mfma_f32_16x16x32_bf16 v[188:191],a[228:231],v[48:51],v[188:191]
ds_read_u16_d16_hi v58,v58
v_lshlrev_b32_sdwa v52,1,v52 dst_sel:DWORD dst_unused:UNUSED_PAD src0_sel:DWORD src1_sel:WORD_1
v_mfma_f32_16x16x32_bf16 v[184:187],a[228:231],v[44:47],v[184:187]
v_rcp_f16_sdwa v9,v8 dst_sel:WORD_1 dst_unused:UNUSED_PRESERVE src0_sel:WORD_1
v_mfma_f32_16x16x32_bf16 v[224:227],a[232:235],v[44:47],v[224:227]
ds_read_u16 v57,v57
ds_read_u16_d16_hi v59,v59
v_mfma_f32_16x16x32_bf16 v[228:231],a[232:235],v[48:51],v[228:231]
ds_read_u16 v52,v52
v_lshlrev_b32_sdwa v54,1,v54 dst_sel:DWORD dst_unused:UNUSED_PAD src0_sel:DWORD src1_sel:WORD_1
v_mfma_f32_16x16x32_bf16 v[204:207],a[236:239],v[48:51],v[204:207]
ds_read_u16_d16_hi v54,v54
v_fma_mix_f32 v53,v53,v9,0 op_sel_hi:[0,1,0]
v_mfma_f32_16x16x32_bf16 v[200:203],a[236:239],v[44:47],v[200:203]
s_waitcnt lgkmcnt(7)
v_fma_mix_f32 v55,v55,v9,0 op_sel:[0,1,0] op_sel_hi:[0,1,0]
v_mfma_f32_16x16x32_bf16 v[48:51],a[204:207],a[48:51],v[20:23]
s_add_u32 m0,s57,28296
v_lshlrev_b32_sdwa v40,1,v40 dst_sel:DWORD dst_unused:UNUSED_PAD src0_sel:DWORD src1_sel:WORD_1
v_lshlrev_b32_sdwa v42,1,v42 dst_sel:DWORD dst_unused:UNUSED_PAD src0_sel:DWORD src1_sel:WORD_1
v_mfma_f32_16x16x32_bf16 v[48:51],a[208:211],a[64:67],v[48:51]
s_add_u32 s89,s56,5701632
ds_read_u16 v40,v40
v_lshl_add_u64 v[24:25],v[24:25],0,v[26:27]
v_mfma_f32_16x16x32_bf16 v[48:51],a[212:215],a[80:83],v[48:51]
s_waitcnt lgkmcnt(6)
buffer_load_dwordx4 v6,s[36:39],s89 offen lds
v_lshl_add_u64 v[26:27],v[28:29],0,v[30:31]
v_mfma_f32_16x16x32_bf16 v[44:47],a[200:203],a[80:83],v[16:19]
ds_read_u16_d16_hi v42,v42
v_lshlrev_b32_sdwa v41,1,v41 dst_sel:DWORD dst_unused:UNUSED_PAD src0_sel:DWORD src1_sel:WORD_1
v_mfma_f32_16x16x32_bf16 v[44:47],a[196:199],a[64:67],v[44:47]
s_waitcnt lgkmcnt(4)
ds_read_u16 v41,v41
v_lshl_add_u64 v[28:29],v[56:57],0,v[58:59]
v_mfma_f32_16x16x32_bf16 v[44:47],a[192:195],a[48:51],v[44:47]
v_lshlrev_b32_sdwa v43,1,v43 dst_sel:DWORD dst_unused:UNUSED_PAD src0_sel:DWORD src1_sel:WORD_1
v_lshlrev_b32_sdwa v36,1,v36 dst_sel:DWORD dst_unused:UNUSED_PAD src0_sel:DWORD src1_sel:WORD_1
v_mfma_f32_16x16x32_bf16 v[56:59],a[192:195],a[52:55],v[16:19]
s_waitcnt lgkmcnt(3)
ds_read_u16_d16_hi v43,v43
v_or_b32 v30,v52,v54
v_mfma_f32_16x16x32_bf16 v[56:59],a[196:199],a[68:71],v[56:59]
ds_read_u16 v36,v36
v_cvt_pk_bf16_f32 v31,v53,v55
v_lshlrev_b32_sdwa v38,1,v38 dst_sel:DWORD dst_unused:UNUSED_PAD src0_sel:DWORD src1_sel:WORD_1
v_mfma_f32_16x16x32_bf16 v[56:59],a[200:203],a[84:87],v[56:59]
ds_read_u16_d16_hi v38,v38
v_lshlrev_b32_sdwa v37,1,v37 dst_sel:DWORD dst_unused:UNUSED_PAD src0_sel:DWORD src1_sel:WORD_1
v_mfma_f32_16x16x32_bf16 v[52:55],a[212:215],a[84:87],v[20:23]
ds_read_u16 v37,v37
v_lshlrev_b32_sdwa v39,1,v39 dst_sel:DWORD dst_unused:UNUSED_PAD src0_sel:DWORD src1_sel:WORD_1
v_mfma_f32_16x16x32_bf16 v[52:55],a[208:211],a[68:71],v[52:55]
ds_read_u16_d16_hi v39,v39
v_lshlrev_b32_sdwa v32,1,v32 dst_sel:DWORD dst_unused:UNUSED_PAD src0_sel:DWORD src1_sel:WORD_1
v_mfma_f32_16x16x32_bf16 v[52:55],a[204:207],a[52:55],v[52:55]
s_add_u32 m0,s57,31832
ds_read_u16 v32,v32
v_lshlrev_b32_sdwa v34,1,v34 dst_sel:DWORD dst_unused:UNUSED_PAD src0_sel:DWORD src1_sel:WORD_1
v_mfma_f32_16x16x32_bf16 v[136:139],a[240:243],v[24:27],v[136:139]
s_add_u32 s89,s56,5799936
ds_read_u16_d16_hi v34,v34
v_lshlrev_b32_sdwa v33,1,v33 dst_sel:DWORD dst_unused:UNUSED_PAD src0_sel:DWORD src1_sel:WORD_1
v_mfma_f32_16x16x32_bf16 v[140:143],a[240:243],v[28:31],v[140:143]
buffer_load_dwordx4 v6,s[36:39],s89 offen lds
v_lshlrev_b32_sdwa v35,1,v35 dst_sel:DWORD dst_unused:UNUSED_PAD src0_sel:DWORD src1_sel:WORD_1
v_mfma_f32_16x16x32_bf16 v[68:71],a[224:227],v[28:31],v[68:71]
ds_read_u16 v33,v33
ds_read_u16_d16_hi v35,v35
v_mfma_f32_16x16x32_bf16 v[64:67],a[224:227],v[24:27],v[64:67]
ds_read_b64_tr_b16 a[244:245],v3 offset:6160
v_lshlrev_b32_sdwa v60,1,v60 dst_sel:DWORD dst_unused:UNUSED_PAD src0_sel:DWORD src1_sel:WORD_1
v_mfma_f32_16x16x32_bf16 v[144:147],a[248:251],v[24:27],v[144:147]
ds_read_u16 v60,v60
v_lshlrev_b32_sdwa v62,1,v62 dst_sel:DWORD dst_unused:UNUSED_PAD src0_sel:DWORD src1_sel:WORD_1
v_mfma_f32_16x16x32_bf16 v[148:151],a[248:251],v[28:31],v[148:151]
ds_read_u16_d16_hi v62,v62
v_lshlrev_b32_sdwa v61,1,v61 dst_sel:DWORD dst_unused:UNUSED_PAD src0_sel:DWORD src1_sel:WORD_1
v_mfma_f32_16x16x32_bf16 v[84:87],a[228:231],v[28:31],v[84:87]
ds_read_u16 v61,v61
v_lshlrev_b32_sdwa v63,1,v63 dst_sel:DWORD dst_unused:UNUSED_PAD src0_sel:DWORD src1_sel:WORD_1
v_mfma_f32_16x16x32_bf16 v[80:83],a[228:231],v[24:27],v[80:83]
ds_read_u16_d16_hi v63,v63
v_mfma_f32_16x16x32_bf16 v[152:155],a[232:235],v[24:27],v[152:155]
ds_read_b64_tr_b16 a[246:247],v3 offset:7696
v_mfma_f32_16x16x32_bf16 v[156:159],a[232:235],v[28:31],v[156:159]
ds_read_b64_tr_b16 a[252:253],v3 offset:6168
v_mfma_f32_16x16x32_bf16 v[100:103],a[236:239],v[28:31],v[100:103]
ds_read_b64_tr_b16 a[254:255],v3 offset:7704
v_mfma_f32_16x16x32_bf16 v[96:99],a[236:239],v[24:27],v[96:99]
TP5956_%=:
s_cbranch_execz TR0_%=
v_mfma_f32_16x16x32_bf16 v[28:31],a[204:207],a[56:59],v[20:23]
s_nop 0
ds_read_b64_tr_b16 a[216:217],v1 offset:6160
v_lshlrev_b32_sdwa v48,1,v48 dst_sel:DWORD dst_unused:UNUSED_PAD src0_sel:DWORD src1_sel:WORD_1
v_mfma_f32_16x16x32_bf16 v[28:31],a[208:211],a[72:75],v[28:31]
s_waitcnt lgkmcnt(7)
ds_read_u16 v48,v48
v_lshl_add_u64 v[40:41],v[40:41],0,v[42:43]
v_fma_mixlo_f16 v8,v57,s99,0
v_mfma_f32_16x16x32_bf16 v[28:31],a[212:215],a[88:91],v[28:31]
ds_read_b64_tr_b16 a[218:219],v1 offset:7184
v_lshl_add_u64 v[42:43],v[36:37],0,v[38:39]
v_lshlrev_b32_sdwa v50,1,v50 dst_sel:DWORD dst_unused:UNUSED_PAD src0_sel:DWORD src1_sel:WORD_1
v_mfma_f32_16x16x32_bf16 v[24:27],a[200:203],a[88:91],v[16:19]
ds_read_u16_d16_hi v50,v50
v_fma_mixhi_f16 v8,v59,s99,0
v_mfma_f32_16x16x32_bf16 v[24:27],a[196:199],a[72:75],v[24:27]
ds_read_b64_tr_b16 a[220:221],v1 offset:8208
v_lshl_add_u64 v[36:37],v[32:33],0,v[34:35]
v_lshlrev_b32_sdwa v49,1,v49 dst_sel:DWORD dst_unused:UNUSED_PAD src0_sel:DWORD src1_sel:WORD_1
v_pk_fma_f16 v9,v8,v8,s54
v_mfma_f32_16x16x32_bf16 v[24:27],a[192:195],a[56:59],v[24:27]
ds_read_u16 v49,v49
v_lshlrev_b32_sdwa v51,1,v51 dst_sel:DWORD dst_unused:UNUSED_PAD src0_sel:DWORD src1_sel:WORD_1
v_mfma_f32_16x16x32_bf16 v[32:35],a[192:195],a[60:63],v[16:19]
ds_read_u16_d16_hi v51,v51
v_pk_mul_f16 v9,v8,v9
v_mfma_f32_16x16x32_bf16 v[32:35],a[196:199],a[76:79],v[32:35]
s_waitcnt lgkmcnt(7)
ds_read_b64_tr_b16 a[222:223],v1 offset:9232
v_lshl_add_u64 v[38:39],v[60:61],0,v[62:63]
v_lshlrev_b32_sdwa v44,1,v44 dst_sel:DWORD dst_unused:UNUSED_PAD src0_sel:DWORD src1_sel:WORD_1
v_mfma_f32_16x16x32_bf16 v[32:35],a[200:203],a[92:95],v[32:35]
v_exp_f16 v8,v9
v_mfma_f32_16x16x32_bf16 v[60:63],a[212:215],a[92:95],v[20:23]
ds_read_u16 v44,v44
v_lshlrev_b32_sdwa v46,1,v46 dst_sel:DWORD dst_unused:UNUSED_PAD src0_sel:DWORD src1_sel:WORD_1
v_mfma_f32_16x16x32_bf16 v[60:63],a[208:211],a[76:79],v[60:63]
ds_read_u16_d16_hi v46,v46
v_lshlrev_b32_sdwa v45,1,v45 dst_sel:DWORD dst_unused:UNUSED_PAD src0_sel:DWORD src1_sel:WORD_1
v_mfma_f32_16x16x32_bf16 v[60:63],a[204:207],a[60:63],v[60:63]
v_exp_f16_sdwa v8,v9 dst_sel:WORD_1 dst_unused:UNUSED_PRESERVE src0_sel:WORD_1
v_mfma_f32_16x16x32_bf16 v[112:115],a[240:243],v[40:43],v[112:115]
ds_read_u16 v45,v45
v_lshlrev_b32_sdwa v47,1,v47 dst_sel:DWORD dst_unused:UNUSED_PAD src0_sel:DWORD src1_sel:WORD_1
v_mfma_f32_16x16x32_bf16 v[116:119],a[240:243],v[36:39],v[116:119]
ds_read_u16_d16_hi v47,v47
v_lshlrev_b32_sdwa v52,1,v52 dst_sel:DWORD dst_unused:UNUSED_PAD src0_sel:DWORD src1_sel:WORD_1
v_mfma_f32_16x16x32_bf16 v[76:79],a[224:227],v[36:39],v[76:79]
ds_read_u16 v52,v52
v_pk_fma_f16 v8,v8,s55,s55
v_mfma_f32_16x16x32_bf16 v[72:75],a[224:227],v[40:43],v[72:75]
ds_read_b64_tr_b16 a[240:241],v3 offset:6224
v_rcp_f16_e64 v9,v8
v_lshlrev_b32_sdwa v54,1,v54 dst_sel:DWORD dst_unused:UNUSED_PAD src0_sel:DWORD src1_sel:WORD_1
v_mfma_f32_16x16x32_bf16 v[120:123],a[248:251],v[40:43],v[120:123]
ds_read_u16_d16_hi v54,v54
v_lshlrev_b32_sdwa v53,1,v53 dst_sel:DWORD dst_unused:UNUSED_PAD src0_sel:DWORD src1_sel:WORD_1
v_mfma_f32_16x16x32_bf16 v[124:127],a[248:251],v[36:39],v[124:127]
ds_read_u16 v53,v53
ds_read_b64_tr_b16 a[242:243],v3 offset:7760
v_mfma_f32_16x16x32_bf16 v[92:95],a[228:231],v[36:39],v[92:95]
ds_read_b64_tr_b16 a[224:225],v3 offset:6232
v_lshlrev_b32_sdwa v55,1,v55 dst_sel:DWORD dst_unused:UNUSED_PAD src0_sel:DWORD src1_sel:WORD_1
v_mfma_f32_16x16x32_bf16 v[88:91],a[228:231],v[40:43],v[88:91]
ds_read_u16_d16_hi v55,v55
v_rcp_f16_sdwa v9,v8 dst_sel:WORD_1 dst_unused:UNUSED_PRESERVE src0_sel:WORD_1
v_mfma_f32_16x16x32_bf16 v[128:131],a[232:235],v[40:43],v[128:131]
ds_read_b64_tr_b16 a[226:227],v3 offset:7768
ds_read_b64_tr_b16 a[248:249],v3 offset:6288
v_lshlrev_b32_sdwa v56,1,v56 dst_sel:DWORD dst_unused:UNUSED_PAD src0_sel:DWORD src1_sel:WORD_1
v_mfma_f32_16x16x32_bf16 v[132:135],a[232:235],v[36:39],v[132:135]
ds_read_u16 v56,v56
v_lshlrev_b32_sdwa v58,1,v58 dst_sel:DWORD dst_unused:UNUSED_PAD src0_sel:DWORD src1_sel:WORD_1
v_mfma_f32_16x16x32_bf16 v[108:111],a[236:239],v[36:39],v[108:111]
ds_read_u16_d16_hi v58,v58
v_fma_mix_f32 v57,v57,v9,0 op_sel_hi:[0,1,0]
v_mfma_f32_16x16x32_bf16 v[104:107],a[236:239],v[40:43],v[104:107]
ds_read_b64_tr_b16 a[250:251],v3 offset:7824
v_fma_mix_f32 v59,v59,v9,0 op_sel:[0,1,0] op_sel_hi:[0,1,0]
v_mfma_f32_16x16x32_bf16 v[36:39],a[204:207],a[0:3],v[20:23]
s_add_u32 m0,s57,35360
ds_read_b64_tr_b16 a[228:229],v3 offset:6296
v_lshlrev_b32_sdwa v28,1,v28 dst_sel:DWORD dst_unused:UNUSED_PAD src0_sel:DWORD src1_sel:WORD_1
v_mfma_f32_16x16x32_bf16 v[36:39],a[208:211],a[16:19],v[36:39]
s_waitcnt lgkmcnt(10)
ds_read_b64_tr_b16 a[230:231],v3 offset:7832
v_lshl_add_u64 v[48:49],v[48:49],0,v[50:51]
v_mfma_f32_16x16x32_bf16 v[36:39],a[212:215],a[32:35],v[36:39]
s_add_u32 s89,s56,5898240
ds_read_u16 v28,v28
v_lshl_add_u64 v[50:51],v[44:45],0,v[46:47]
v_lshlrev_b32_sdwa v30,1,v30 dst_sel:DWORD dst_unused:UNUSED_PAD src0_sel:DWORD src1_sel:WORD_1
v_mfma_f32_16x16x32_bf16 v[40:43],a[200:203],a[32:35],v[16:19]
buffer_load_dwordx4 v6,s[36:39],s89 offen lds
v_lshlrev_b32_sdwa v29,1,v29 dst_sel:DWORD dst_unused:UNUSED_PAD src0_sel:DWORD src1_sel:WORD_1
v_mfma_f32_16x16x32_bf16 v[40:43],a[196:199],a[16:19],v[40:43]
s_waitcnt lgkmcnt(8)
ds_read_u16_d16_hi v30,v30
v_lshl_add_u64 v[44:45],v[52:53],0,v[54:55]
v_mfma_f32_16x16x32_bf16 v[40:43],a[192:195],a[0:3],v[40:43]
ds_read_u16 v29,v29
v_lshlrev_b32_sdwa v31,1,v31 dst_sel:DWORD dst_unused:UNUSED_PAD src0_sel:DWORD src1_sel:WORD_1
v_mfma_f32_16x16x32_bf16 v[52:55],a[192:195],a[4:7],v[16:19]
s_waitcnt lgkmcnt(6)
ds_read_u16_d16_hi v31,v31
v_or_b32 v46,v56,v58
v_lshlrev_b32_sdwa v24,1,v24 dst_sel:DWORD dst_unused:UNUSED_PAD src0_sel:DWORD src1_sel:WORD_1
v_mfma_f32_16x16x32_bf16 v[52:55],a[196:199],a[20:23],v[52:55]
ds_read_u16 v24,v24
v_cvt_pk_bf16_f32 v47,v57,v59
v_lshlrev_b32_sdwa v26,1,v26 dst_sel:DWORD dst_unused:UNUSED_PAD src0_sel:DWORD src1_sel:WORD_1
v_mfma_f32_16x16x32_bf16 v[52:55],a[200:203],a[36:39],v[52:55]
ds_read_u16_d16_hi v26,v26
v_lshlrev_b32_sdwa v25,1,v25 dst_sel:DWORD dst_unused:UNUSED_PAD src0_sel:DWORD src1_sel:WORD_1
v_mfma_f32_16x16x32_bf16 v[56:59],a[212:215],a[36:39],v[20:23]
ds_read_u16 v25,v25
v_lshlrev_b32_sdwa v27,1,v27 dst_sel:DWORD dst_unused:UNUSED_PAD src0_sel:DWORD src1_sel:WORD_1
v_mfma_f32_16x16x32_bf16 v[56:59],a[208:211],a[20:23],v[56:59]
ds_read_u16_d16_hi v27,v27
v_lshlrev_b32_sdwa v60,1,v60 dst_sel:DWORD dst_unused:UNUSED_PAD src0_sel:DWORD src1_sel:WORD_1
v_mfma_f32_16x16x32_bf16 v[56:59],a[204:207],a[4:7],v[56:59]
ds_read_u16 v60,v60
v_lshlrev_b32_sdwa v62,1,v62 dst_sel:DWORD dst_unused:UNUSED_PAD src0_sel:DWORD src1_sel:WORD_1
v_mfma_f32_16x16x32_bf16 v[232:235],a[244:247],v[48:51],v[232:235]
ds_read_u16_d16_hi v62,v62
v_lshlrev_b32_sdwa v61,1,v61 dst_sel:DWORD dst_unused:UNUSED_PAD src0_sel:DWORD src1_sel:WORD_1
v_mfma_f32_16x16x32_bf16 v[236:239],a[244:247],v[44:47],v[236:239]
ds_read_u16 v61,v61
v_lshlrev_b32_sdwa v63,1,v63 dst_sel:DWORD dst_unused:UNUSED_PAD src0_sel:DWORD src1_sel:WORD_1
v_mfma_f32_16x16x32_bf16 v[164:167],a[252:255],v[44:47],v[164:167]
ds_read_u16_d16_hi v63,v63
v_lshlrev_b32_sdwa v32,1,v32 dst_sel:DWORD dst_unused:UNUSED_PAD src0_sel:DWORD src1_sel:WORD_1
v_mfma_f32_16x16x32_bf16 v[160:163],a[252:255],v[48:51],v[160:163]
ds_read_u16 v32,v32
v_lshlrev_b32_sdwa v34,1,v34 dst_sel:DWORD dst_unused:UNUSED_PAD src0_sel:DWORD src1_sel:WORD_1
v_mfma_f32_16x16x32_bf16 v[240:243],a[240:243],v[48:51],v[240:243]
ds_read_u16_d16_hi v34,v34
v_lshlrev_b32_sdwa v33,1,v33 dst_sel:DWORD dst_unused:UNUSED_PAD src0_sel:DWORD src1_sel:WORD_1
v_mfma_f32_16x16x32_bf16 v[244:247],a[240:243],v[44:47],v[244:247]
ds_read_u16 v33,v33
v_lshlrev_b32_sdwa v35,1,v35 dst_sel:DWORD dst_unused:UNUSED_PAD src0_sel:DWORD src1_sel:WORD_1
v_mfma_f32_16x16x32_bf16 v[180:183],a[224:227],v[44:47],v[180:183]
ds_read_u16_d16_hi v35,v35
v_mfma_f32_16x16x32_bf16 v[176:179],a[224:227],v[48:51],v[176:179]
s_waitcnt lgkmcnt(8)
v_mfma_f32_16x16x32_bf16 v[248:251],a[248:251],v[48:51],v[248:251]
s_add_u32 m0,s57,38896
v_mfma_f32_16x16x32_bf16 v[252:255],a[248:251],v[44:47],v[252:255]
s_add_u32 s89,s56,5996544
v_mfma_f32_16x16x32_bf16 v[196:199],a[228:231],v[44:47],v[196:199]
buffer_load_dwordx4 v6,s[36:39],s89 offen lds
v_mfma_f32_16x16x32_bf16 v[192:195],a[228:231],v[48:51],v[192:195]
v_mfma_f32_16x16x32_bf16 v[44:47],a[204:207],a[8:11],v[20:23]
v_lshlrev_b32_sdwa v36,1,v36 dst_sel:DWORD dst_unused:UNUSED_PAD src0_sel:DWORD src1_sel:WORD_1
v_fma_mixlo_f16 v8,v53,s99,0
v_mfma_f32_16x16x32_bf16 v[44:47],a[208:211],a[24:27],v[44:47]
ds_read_u16 v36,v36
v_lshl_add_u64 v[28:29],v[28:29],0,v[30:31]
v_mfma_f32_16x16x32_bf16 v[44:47],a[212:215],a[40:43],v[44:47]
v_lshl_add_u64 v[30:31],v[24:25],0,v[26:27]
v_lshlrev_b32_sdwa v38,1,v38 dst_sel:DWORD dst_unused:UNUSED_PAD src0_sel:DWORD src1_sel:WORD_1
v_mfma_f32_16x16x32_bf16 v[48:51],a[200:203],a[40:43],v[16:19]
ds_read_u16_d16_hi v38,v38
v_fma_mixhi_f16 v8,v55,s99,0
v_mfma_f32_16x16x32_bf16 v[48:51],a[196:199],a[24:27],v[48:51]
s_waitcnt lgkmcnt(2)
v_lshl_add_u64 v[24:25],v[60:61],0,v[62:63]
v_lshlrev_b32_sdwa v37,1,v37 dst_sel:DWORD dst_unused:UNUSED_PAD src0_sel:DWORD src1_sel:WORD_1
v_pk_fma_f16 v9,v8,v8,s54
v_mfma_f32_16x16x32_bf16 v[48:51],a[192:195],a[8:11],v[48:51]
ds_read_u16 v37,v37
v_lshlrev_b32_sdwa v39,1,v39 dst_sel:DWORD dst_unused:UNUSED_PAD src0_sel:DWORD src1_sel:WORD_1
v_mfma_f32_16x16x32_bf16 v[60:63],a[192:195],a[12:15],v[16:19]
ds_read_b128 v[16:19],v4 offset:256
v_pk_mul_f16 v9,v8,v9
v_mfma_f32_16x16x32_bf16 v[60:63],a[196:199],a[28:31],v[60:63]
ds_read_u16_d16_hi v39,v39
ds_read_b64_tr_b16 a[192:193],v1 offset:10256
v_lshl_add_u64 v[26:27],v[32:33],0,v[34:35]
v_lshlrev_b32_sdwa v40,1,v40 dst_sel:DWORD dst_unused:UNUSED_PAD src0_sel:DWORD src1_sel:WORD_1
v_mfma_f32_16x16x32_bf16 v[60:63],a[200:203],a[44:47],v[60:63]
ds_read_b64_tr_b16 a[194:195],v1 offset:11280
v_exp_f16_e64 v8,v9
v_mfma_f32_16x16x32_bf16 v[32:35],a[212:215],a[44:47],v[20:23]
ds_read_b128 v[20:23],v4 offset:288
ds_read_u16 v40,v40
v_lshlrev_b32_sdwa v42,1,v42 dst_sel:DWORD dst_unused:UNUSED_PAD src0_sel:DWORD src1_sel:WORD_1
v_mfma_f32_16x16x32_bf16 v[32:35],a[208:211],a[28:31],v[32:35]
ds_read_u16_d16_hi v42,v42
ds_read_b64_tr_b16 a[196:197],v2 offset:10256
v_lshlrev_b32_sdwa v41,1,v41 dst_sel:DWORD dst_unused:UNUSED_PAD src0_sel:DWORD src1_sel:WORD_1
v_mfma_f32_16x16x32_bf16 v[32:35],a[204:207],a[12:15],v[32:35]
ds_read_u16 v41,v41
v_exp_f16_sdwa v8,v9 dst_sel:WORD_1 dst_unused:UNUSED_PRESERVE src0_sel:WORD_1
v_mfma_f32_16x16x32_bf16 v[208:211],a[244:247],v[28:31],v[208:211]
ds_read_b64_tr_b16 a[198:199],v2 offset:11280
ds_read_b64_tr_b16 a[200:201],v2 offset:8208
v_lshlrev_b32_sdwa v43,1,v43 dst_sel:DWORD dst_unused:UNUSED_PAD src0_sel:DWORD src1_sel:WORD_1
v_mfma_f32_16x16x32_bf16 v[212:215],a[244:247],v[24:27],v[212:215]
ds_read_u16_d16_hi v43,v43
v_lshlrev_b32_sdwa v56,1,v56 dst_sel:DWORD dst_unused:UNUSED_PAD src0_sel:DWORD src1_sel:WORD_1
v_mfma_f32_16x16x32_bf16 v[172:175],a[252:255],v[24:27],v[172:175]
ds_read_b64_tr_b16 a[202:203],v2 offset:9232
v_pk_fma_f16 v8,v8,s55,s55
v_mfma_f32_16x16x32_bf16 v[168:171],a[252:255],v[28:31],v[168:171]
v_rcp_f16 v9,v8
v_lshlrev_b32_sdwa v58,1,v58 dst_sel:DWORD dst_unused:UNUSED_PAD src0_sel:DWORD src1_sel:WORD_1
v_mfma_f32_16x16x32_bf16 v[216:219],a[240:243],v[28:31],v[216:219]
ds_read_u16 v56,v56
v_lshlrev_b32_sdwa v57,1,v57 dst_sel:DWORD dst_unused:UNUSED_PAD src0_sel:DWORD src1_sel:WORD_1
v_mfma_f32_16x16x32_bf16 v[220:223],a[240:243],v[24:27],v[220:223]
ds_read_u16_d16_hi v58,v58
v_lshlrev_b32_sdwa v59,1,v59 dst_sel:DWORD dst_unused:UNUSED_PAD src0_sel:DWORD src1_sel:WORD_1
v_mfma_f32_16x16x32_bf16 v[188:191],a[224:227],v[24:27],v[188:191]
ds_read_u16 v57,v57
v_lshlrev_b32_sdwa v52,1,v52 dst_sel:DWORD dst_unused:UNUSED_PAD src0_sel:DWORD src1_sel:WORD_1
v_mfma_f32_16x16x32_bf16 v[184:187],a[224:227],v[28:31],v[184:187]
ds_read_u16_d16_hi v59,v59
v_rcp_f16_sdwa v9,v8 dst_sel:WORD_1 dst_unused:UNUSED_PRESERVE src0_sel:WORD_1
v_mfma_f32_16x16x32_bf16 v[224:227],a[248:251],v[28:31],v[224:227]
ds_read_b64_tr_b16 a[212:213],v2 offset:6160
v_lshlrev_b32_sdwa v54,1,v54 dst_sel:DWORD dst_unused:UNUSED_PAD src0_sel:DWORD src1_sel:WORD_1
v_mfma_f32_16x16x32_bf16 v[228:231],a[248:251],v[24:27],v[228:231]
ds_read_b64_tr_b16 a[214:215],v2 offset:7184
v_fma_mix_f32 v53,v53,v9,0 op_sel_hi:[0,1,0]
v_mfma_f32_16x16x32_bf16 v[204:207],a[228:231],v[24:27],v[204:207]
ds_read_u16 v52,v52
v_fma_mix_f32 v55,v55,v9,0 op_sel:[0,1,0] op_sel_hi:[0,1,0]
v_mfma_f32_16x16x32_bf16 v[200:203],a[228:231],v[28:31],v[200:203]
s_waitcnt lgkmcnt(7)
ds_read_u16_d16_hi v54,v54
v_mfma_f32_16x16x32_bf16 v[24:27],a[216:219],a[48:51],v[16:19]
v_lshlrev_b32_sdwa v44,1,v44 dst_sel:DWORD dst_unused:UNUSED_PAD src0_sel:DWORD src1_sel:WORD_1
v_lshlrev_b32_sdwa v46,1,v46 dst_sel:DWORD dst_unused:UNUSED_PAD src0_sel:DWORD src1_sel:WORD_1
v_mfma_f32_16x16x32_bf16 v[24:27],a[220:223],a[64:67],v[24:27]
s_add_u32 m0,s57,42440
ds_read_u16 v44,v44
v_lshl_add_u64 v[36:37],v[36:37],0,v[38:39]
v_mfma_f32_16x16x32_bf16 v[24:27],a[192:195],a[80:83],v[24:27]
s_add_u32 s89,s56,6094848
ds_read_u16_d16_hi v46,v46
v_lshl_add_u64 v[38:39],v[40:41],0,v[42:43]
v_mfma_f32_16x16x32_bf16 v[28:31],a[196:199],a[80:83],v[20:23]
buffer_load_dwordx4 v6,s[36:39],s89 offen lds
v_lshlrev_b32_sdwa v45,1,v45 dst_sel:DWORD dst_unused:UNUSED_PAD src0_sel:DWORD src1_sel:WORD_1
v_mfma_f32_16x16x32_bf16 v[28:31],a[200:203],a[64:67],v[28:31]
s_waitcnt lgkmcnt(4)
ds_read_u16 v45,v45
v_lshl_add_u64 v[40:41],v[56:57],0,v[58:59]
v_mfma_f32_16x16x32_bf16 v[28:31],a[212:215],a[48:51],v[28:31]
v_lshlrev_b32_sdwa v47,1,v47 dst_sel:DWORD dst_unused:UNUSED_PAD src0_sel:DWORD src1_sel:WORD_1
v_lshlrev_b32_sdwa v48,1,v48 dst_sel:DWORD dst_unused:UNUSED_PAD src0_sel:DWORD src1_sel:WORD_1
v_mfma_f32_16x16x32_bf16 v[56:59],a[212:215],a[52:55],v[20:23]
s_waitcnt lgkmcnt(3)
ds_read_u16_d16_hi v47,v47
v_or_b32 v42,v52,v54
v_mfma_f32_16x16x32_bf16 v[56:59],a[200:203],a[68:71],v[56:59]
s_waitcnt vmcnt(14)
ds_read_u16 v48,v48
v_cvt_pk_bf16_f32 v43,v53,v55
v_lshlrev_b32_sdwa v50,1,v50 dst_sel:DWORD dst_unused:UNUSED_PAD src0_sel:DWORD src1_sel:WORD_1
v_mfma_f32_16x16x32_bf16 v[56:59],a[196:199],a[84:87],v[56:59]
s_barrier
ds_read_u16_d16_hi v50,v50
v_lshlrev_b32_sdwa v49,1,v49 dst_sel:DWORD dst_unused:UNUSED_PAD src0_sel:DWORD src1_sel:WORD_1
v_mfma_f32_16x16x32_bf16 v[52:55],a[192:195],a[84:87],v[16:19]
ds_read_u16 v49,v49
v_lshlrev_b32_sdwa v51,1,v51 dst_sel:DWORD dst_unused:UNUSED_PAD src0_sel:DWORD src1_sel:WORD_1
v_mfma_f32_16x16x32_bf16 v[52:55],a[220:223],a[68:71],v[52:55]
s_add_u32 m0,s58,0
ds_read_u16_d16_hi v51,v51
v_lshlrev_b32_sdwa v32,1,v32 dst_sel:DWORD dst_unused:UNUSED_PAD src0_sel:DWORD src1_sel:WORD_1
v_mfma_f32_16x16x32_bf16 v[52:55],a[216:219],a[52:55],v[52:55]
s_add_u32 s69,s69,s59
ds_read_u16 v32,v32
v_lshlrev_b32_sdwa v34,1,v34 dst_sel:DWORD dst_unused:UNUSED_PAD src0_sel:DWORD src1_sel:WORD_1
v_mfma_f32_16x16x32_bf16 v[136:139],a[244:247],v[36:39],v[136:139]
buffer_load_dwordx4 v5,s[44:47],s69 offen offset:0 lds
v_lshlrev_b32_sdwa v33,1,v33 dst_sel:DWORD dst_unused:UNUSED_PAD src0_sel:DWORD src1_sel:WORD_1
v_mfma_f32_16x16x32_bf16 v[140:143],a[244:247],v[40:43],v[140:143]
ds_read_u16_d16_hi v34,v34
v_lshlrev_b32_sdwa v35,1,v35 dst_sel:DWORD dst_unused:UNUSED_PAD src0_sel:DWORD src1_sel:WORD_1
v_mfma_f32_16x16x32_bf16 v[68:71],a[252:255],v[40:43],v[68:71]
ds_read_u16 v33,v33
ds_read_u16_d16_hi v35,v35
v_mfma_f32_16x16x32_bf16 v[64:67],a[252:255],v[36:39],v[64:67]
ds_read_b64_tr_b16 a[232:233],v3 offset:12320
v_lshlrev_b32_sdwa v60,1,v60 dst_sel:DWORD dst_unused:UNUSED_PAD src0_sel:DWORD src1_sel:WORD_1
v_mfma_f32_16x16x32_bf16 v[144:147],a[240:243],v[36:39],v[144:147]
s_add_u32 s69,s69,s53
ds_read_u16 v60,v60
v_lshlrev_b32_sdwa v62,1,v62 dst_sel:DWORD dst_unused:UNUSED_PAD src0_sel:DWORD src1_sel:WORD_1
v_mfma_f32_16x16x32_bf16 v[148:151],a[240:243],v[40:43],v[148:151]
buffer_load_dwordx4 v5,s[44:47],s69 offen offset:1024 lds
ds_read_u16_d16_hi v62,v62
v_mfma_f32_16x16x32_bf16 v[84:87],a[224:227],v[40:43],v[84:87]
ds_read_b64_tr_b16 a[234:235],v3 offset:13856
v_lshlrev_b32_sdwa v61,1,v61 dst_sel:DWORD dst_unused:UNUSED_PAD src0_sel:DWORD src1_sel:WORD_1
v_mfma_f32_16x16x32_bf16 v[80:83],a[224:227],v[36:39],v[80:83]
ds_read_u16 v61,v61
v_lshlrev_b32_sdwa v63,1,v63 dst_sel:DWORD dst_unused:UNUSED_PAD src0_sel:DWORD src1_sel:WORD_1
v_mfma_f32_16x16x32_bf16 v[152:155],a[248:251],v[36:39],v[152:155]
ds_read_u16_d16_hi v63,v63
v_mfma_f32_16x16x32_bf16 v[156:159],a[248:251],v[40:43],v[156:159]
s_add_u32 s69,s69,s53
ds_read_b64_tr_b16 a[236:237],v3 offset:12328
v_mfma_f32_16x16x32_bf16 v[100:103],a[228:231],v[40:43],v[100:103]
buffer_load_dwordx4 v5,s[44:47],s69 offen offset:2048 lds
v_mfma_f32_16x16x32_bf16 v[96:99],a[228:231],v[36:39],v[96:99]
ds_read_b64_tr_b16 a[238:239],v3 offset:13864
v_mfma_f32_16x16x32_bf16 v[40:43],a[216:219],a[56:59],v[16:19]
ds_read_b64_tr_b16 a[208:209],v1 offset:12320
v_lshlrev_b32_sdwa v24,1,v24 dst_sel:DWORD dst_unused:UNUSED_PAD src0_sel:DWORD src1_sel:WORD_1
v_mfma_f32_16x16x32_bf16 v[40:43],a[220:223],a[72:75],v[40:43]
s_waitcnt lgkmcnt(7)
ds_read_u16 v24,v24
v_lshl_add_u64 v[44:45],v[44:45],0,v[46:47]
v_fma_mixlo_f16 v8,v57,s99,0
v_mfma_f32_16x16x32_bf16 v[40:43],a[192:195],a[88:91],v[40:43]
ds_read_b64_tr_b16 a[210:211],v1 offset:13344
v_lshl_add_u64 v[46:47],v[48:49],0,v[50:51]
v_lshlrev_b32_sdwa v26,1,v26 dst_sel:DWORD dst_unused:UNUSED_PAD src0_sel:DWORD src1_sel:WORD_1
v_mfma_f32_16x16x32_bf16 v[36:39],a[196:199],a[88:91],v[20:23]
ds_read_u16_d16_hi v26,v26
v_fma_mixhi_f16 v8,v59,s99,0
v_mfma_f32_16x16x32_bf16 v[36:39],a[200:203],a[72:75],v[36:39]
ds_read_b64_tr_b16 a[204:205],v1 offset:14368
v_lshl_add_u64 v[48:49],v[32:33],0,v[34:35]
v_lshlrev_b32_sdwa v25,1,v25 dst_sel:DWORD dst_unused:UNUSED_PAD src0_sel:DWORD src1_sel:WORD_1
v_pk_fma_f16 v9,v8,v8,s54
v_mfma_f32_16x16x32_bf16 v[36:39],a[212:215],a[56:59],v[36:39]
ds_read_u16 v25,v25
v_lshlrev_b32_sdwa v27,1,v27 dst_sel:DWORD dst_unused:UNUSED_PAD src0_sel:DWORD src1_sel:WORD_1
v_mfma_f32_16x16x32_bf16 v[32:35],a[212:215],a[60:63],v[20:23]
ds_read_u16_d16_hi v27,v27
v_pk_mul_f16 v9,v8,v9
v_mfma_f32_16x16x32_bf16 v[32:35],a[200:203],a[76:79],v[32:35]
s_waitcnt lgkmcnt(8)
ds_read_b64_tr_b16 a[206:207],v1 offset:15392
v_lshl_add_u64 v[50:51],v[60:61],0,v[62:63]
v_lshlrev_b32_sdwa v28,1,v28 dst_sel:DWORD dst_unused:UNUSED_PAD src0_sel:DWORD src1_sel:WORD_1
v_mfma_f32_16x16x32_bf16 v[32:35],a[196:199],a[92:95],v[32:35]
v_exp_f16 v8,v9
v_mfma_f32_16x16x32_bf16 v[60:63],a[192:195],a[92:95],v[16:19]
ds_read_u16 v28,v28
v_lshlrev_b32_sdwa v30,1,v30 dst_sel:DWORD dst_unused:UNUSED_PAD src0_sel:DWORD src1_sel:WORD_1
v_mfma_f32_16x16x32_bf16 v[60:63],a[220:223],a[76:79],v[60:63]
ds_read_u16_d16_hi v30,v30
v_lshlrev_b32_sdwa v29,1,v29 dst_sel:DWORD dst_unused:UNUSED_PAD src0_sel:DWORD src1_sel:WORD_1
v_mfma_f32_16x16x32_bf16 v[60:63],a[216:219],a[60:63],v[60:63]
v_exp_f16_sdwa v8,v9 dst_sel:WORD_1 dst_unused:UNUSED_PRESERVE src0_sel:WORD_1
v_mfma_f32_16x16x32_bf16 v[112:115],a[244:247],v[44:47],v[112:115]
ds_read_u16 v29,v29
v_lshlrev_b32_sdwa v31,1,v31 dst_sel:DWORD dst_unused:UNUSED_PAD src0_sel:DWORD src1_sel:WORD_1
v_mfma_f32_16x16x32_bf16 v[116:119],a[244:247],v[48:51],v[116:119]
ds_read_u16_d16_hi v31,v31
v_lshlrev_b32_sdwa v52,1,v52 dst_sel:DWORD dst_unused:UNUSED_PAD src0_sel:DWORD src1_sel:WORD_1
v_mfma_f32_16x16x32_bf16 v[76:79],a[252:255],v[48:51],v[76:79]
ds_read_u16 v52,v52
v_pk_fma_f16 v8,v8,s55,s55
v_mfma_f32_16x16x32_bf16 v[72:75],a[252:255],v[44:47],v[72:75]
ds_read_b64_tr_b16 a[244:245],v3 offset:12384
v_rcp_f16_e64 v9,v8
v_lshlrev_b32_sdwa v54,1,v54 dst_sel:DWORD dst_unused:UNUSED_PAD src0_sel:DWORD src1_sel:WORD_1
v_mfma_f32_16x16x32_bf16 v[120:123],a[240:243],v[44:47],v[120:123]
ds_read_u16_d16_hi v54,v54
v_lshlrev_b32_sdwa v53,1,v53 dst_sel:DWORD dst_unused:UNUSED_PAD src0_sel:DWORD src1_sel:WORD_1
v_mfma_f32_16x16x32_bf16 v[124:127],a[240:243],v[48:51],v[124:127]
ds_read_u16 v53,v53
ds_read_b64_tr_b16 a[246:247],v3 offset:13920
v_mfma_f32_16x16x32_bf16 v[92:95],a[224:227],v[48:51],v[92:95]
ds_read_b64_tr_b16 a[252:253],v3 offset:12392
v_lshlrev_b32_sdwa v55,1,v55 dst_sel:DWORD dst_unused:UNUSED_PAD src0_sel:DWORD src1_sel:WORD_1
v_mfma_f32_16x16x32_bf16 v[88:91],a[224:227],v[44:47],v[88:91]
ds_read_u16_d16_hi v55,v55
v_rcp_f16_sdwa v9,v8 dst_sel:WORD_1 dst_unused:UNUSED_PRESERVE src0_sel:WORD_1
v_mfma_f32_16x16x32_bf16 v[128:131],a[248:251],v[44:47],v[128:131]
ds_read_b64_tr_b16 a[254:255],v3 offset:13928
ds_read_b64_tr_b16 a[240:241],v3 offset:12448
v_lshlrev_b32_sdwa v56,1,v56 dst_sel:DWORD dst_unused:UNUSED_PAD src0_sel:DWORD src1_sel:WORD_1
v_mfma_f32_16x16x32_bf16 v[132:135],a[248:251],v[48:51],v[132:135]
ds_read_u16 v56,v56
v_lshlrev_b32_sdwa v58,1,v58 dst_sel:DWORD dst_unused:UNUSED_PAD src0_sel:DWORD src1_sel:WORD_1
v_mfma_f32_16x16x32_bf16 v[108:111],a[228:231],v[48:51],v[108:111]
ds_read_u16_d16_hi v58,v58
v_fma_mix_f32 v57,v57,v9,0 op_sel_hi:[0,1,0]
v_mfma_f32_16x16x32_bf16 v[104:107],a[228:231],v[44:47],v[104:107]
ds_read_b64_tr_b16 a[242:243],v3 offset:13984
v_fma_mix_f32 v59,v59,v9,0 op_sel:[0,1,0] op_sel_hi:[0,1,0]
v_mfma_f32_16x16x32_bf16 v[48:51],a[216:219],a[0:3],v[16:19]
s_add_u32 m0,s58,6160
ds_read_b64_tr_b16 a[224:225],v3 offset:12456
v_lshlrev_b32_sdwa v40,1,v40 dst_sel:DWORD dst_unused:UNUSED_PAD src0_sel:DWORD src1_sel:WORD_1
v_mfma_f32_16x16x32_bf16 v[48:51],a[220:223],a[16:19],v[48:51]
s_waitcnt lgkmcnt(10)
ds_read_b64_tr_b16 a[226:227],v3 offset:13992
v_lshl_add_u64 v[24:25],v[24:25],0,v[26:27]
v_mfma_f32_16x16x32_bf16 v[48:51],a[192:195],a[32:35],v[48:51]
s_add_u32 s69,s69,s59
ds_read_u16 v40,v40
v_lshl_add_u64 v[26:27],v[28:29],0,v[30:31]
v_lshlrev_b32_sdwa v42,1,v42 dst_sel:DWORD dst_unused:UNUSED_PAD src0_sel:DWORD src1_sel:WORD_1
v_mfma_f32_16x16x32_bf16 v[44:47],a[196:199],a[32:35],v[20:23]
buffer_load_dwordx4 v5,s[44:47],s69 offen offset:0 lds
v_lshlrev_b32_sdwa v41,1,v41 dst_sel:DWORD dst_unused:UNUSED_PAD src0_sel:DWORD src1_sel:WORD_1
v_mfma_f32_16x16x32_bf16 v[44:47],a[200:203],a[16:19],v[44:47]
s_waitcnt lgkmcnt(8)
ds_read_u16_d16_hi v42,v42
v_lshl_add_u64 v[28:29],v[52:53],0,v[54:55]
v_mfma_f32_16x16x32_bf16 v[44:47],a[212:215],a[0:3],v[44:47]
s_add_u32 m0,s57,45976
ds_read_u16 v41,v41
v_lshlrev_b32_sdwa v43,1,v43 dst_sel:DWORD dst_unused:UNUSED_PAD src0_sel:DWORD src1_sel:WORD_1
v_mfma_f32_16x16x32_bf16 v[52:55],a[212:215],a[4:7],v[20:23]
s_waitcnt lgkmcnt(6)
ds_read_u16_d16_hi v43,v43
v_or_b32 v30,v56,v58
v_lshlrev_b32_sdwa v36,1,v36 dst_sel:DWORD dst_unused:UNUSED_PAD src0_sel:DWORD src1_sel:WORD_1
v_mfma_f32_16x16x32_bf16 v[52:55],a[200:203],a[20:23],v[52:55]
s_add_u32 s89,s56,6193152
ds_read_u16 v36,v36
v_cvt_pk_bf16_f32 v31,v57,v59
v_lshlrev_b32_sdwa v38,1,v38 dst_sel:DWORD dst_unused:UNUSED_PAD src0_sel:DWORD src1_sel:WORD_1
v_mfma_f32_16x16x32_bf16 v[52:55],a[196:199],a[36:39],v[52:55]
buffer_load_dwordx4 v6,s[36:39],s89 offen lds
v_lshlrev_b32_sdwa v37,1,v37 dst_sel:DWORD dst_unused:UNUSED_PAD src0_sel:DWORD src1_sel:WORD_1
v_mfma_f32_16x16x32_bf16 v[56:59],a[192:195],a[36:39],v[16:19]
s_add_u32 m0,s58,6160
ds_read_u16_d16_hi v38,v38
v_lshlrev_b32_sdwa v39,1,v39 dst_sel:DWORD dst_unused:UNUSED_PAD src0_sel:DWORD src1_sel:WORD_1
v_mfma_f32_16x16x32_bf16 v[56:59],a[220:223],a[20:23],v[56:59]
s_add_u32 s69,s69,s53
ds_read_u16 v37,v37
v_lshlrev_b32_sdwa v60,1,v60 dst_sel:DWORD dst_unused:UNUSED_PAD src0_sel:DWORD src1_sel:WORD_1
v_mfma_f32_16x16x32_bf16 v[56:59],a[216:219],a[4:7],v[56:59]
buffer_load_dwordx4 v5,s[44:47],s69 offen offset:1024 lds
v_lshlrev_b32_sdwa v62,1,v62 dst_sel:DWORD dst_unused:UNUSED_PAD src0_sel:DWORD src1_sel:WORD_1
v_mfma_f32_16x16x32_bf16 v[232:235],a[232:235],v[24:27],v[232:235]
s_add_u32 s69,s69,s53
ds_read_u16_d16_hi v39,v39
v_lshlrev_b32_sdwa v61,1,v61 dst_sel:DWORD dst_unused:UNUSED_PAD src0_sel:DWORD src1_sel:WORD_1
v_mfma_f32_16x16x32_bf16 v[236:239],a[232:235],v[28:31],v[236:239]
buffer_load_dwordx4 v5,s[44:47],s69 offen offset:2048 lds
v_lshlrev_b32_sdwa v63,1,v63 dst_sel:DWORD dst_unused:UNUSED_PAD src0_sel:DWORD src1_sel:WORD_1
v_mfma_f32_16x16x32_bf16 v[164:167],a[236:239],v[28:31],v[164:167]
s_add_u32 m0,s57,49504
ds_read_u16 v60,v60
v_lshlrev_b32_sdwa v32,1,v32 dst_sel:DWORD dst_unused:UNUSED_PAD src0_sel:DWORD src1_sel:WORD_1
v_mfma_f32_16x16x32_bf16 v[160:163],a[236:239],v[24:27],v[160:163]
s_add_u32 s89,s56,6291456
ds_read_u16_d16_hi v62,v62
v_lshlrev_b32_sdwa v34,1,v34 dst_sel:DWORD dst_unused:UNUSED_PAD src0_sel:DWORD src1_sel:WORD_1
v_mfma_f32_16x16x32_bf16 v[240:243],a[244:247],v[24:27],v[240:243]
buffer_load_dwordx4 v6,s[36:39],s89 offen lds
ds_read_u16 v61,v61
v_mfma_f32_16x16x32_bf16 v[244:247],a[244:247],v[28:31],v[244:247]
ds_read_u16_d16_hi v63,v63
v_lshlrev_b32_sdwa v33,1,v33 dst_sel:DWORD dst_unused:UNUSED_PAD src0_sel:DWORD src1_sel:WORD_1
v_mfma_f32_16x16x32_bf16 v[180:183],a[252:255],v[28:31],v[180:183]
ds_read_u16 v32,v32
v_lshlrev_b32_sdwa v35,1,v35 dst_sel:DWORD dst_unused:UNUSED_PAD src0_sel:DWORD src1_sel:WORD_1
v_mfma_f32_16x16x32_bf16 v[176:179],a[252:255],v[24:27],v[176:179]
s_waitcnt lgkmcnt(6)
ds_read_u16_d16_hi v34,v34
v_mfma_f32_16x16x32_bf16 v[248:251],a[240:243],v[24:27],v[248:251]
ds_read_u16 v33,v33
v_mfma_f32_16x16x32_bf16 v[252:255],a[240:243],v[28:31],v[252:255]
ds_read_u16_d16_hi v35,v35
v_mfma_f32_16x16x32_bf16 v[196:199],a[224:227],v[28:31],v[196:199]
v_mfma_f32_16x16x32_bf16 v[192:195],a[224:227],v[24:27],v[192:195]
v_mfma_f32_16x16x32_bf16 v[28:31],a[216:219],a[8:11],v[16:19]
v_lshlrev_b32_sdwa v48,1,v48 dst_sel:DWORD dst_unused:UNUSED_PAD src0_sel:DWORD src1_sel:WORD_1
v_fma_mixlo_f16 v8,v53,s99,0
v_mfma_f32_16x16x32_bf16 v[28:31],a[220:223],a[24:27],v[28:31]
ds_read_u16 v48,v48
v_lshl_add_u64 v[40:41],v[40:41],0,v[42:43]
v_mfma_f32_16x16x32_bf16 v[28:31],a[192:195],a[40:43],v[28:31]
s_waitcnt lgkmcnt(5)
v_lshl_add_u64 v[42:43],v[36:37],0,v[38:39]
v_lshlrev_b32_sdwa v50,1,v50 dst_sel:DWORD dst_unused:UNUSED_PAD src0_sel:DWORD src1_sel:WORD_1
v_mfma_f32_16x16x32_bf16 v[24:27],a[196:199],a[40:43],v[20:23]
ds_read_u16_d16_hi v50,v50
v_fma_mixhi_f16 v8,v55,s99,0
v_mfma_f32_16x16x32_bf16 v[24:27],a[200:203],a[24:27],v[24:27]
v_lshl_add_u64 v[36:37],v[60:61],0,v[62:63]
v_lshlrev_b32_sdwa v49,1,v49 dst_sel:DWORD dst_unused:UNUSED_PAD src0_sel:DWORD src1_sel:WORD_1
v_pk_fma_f16 v9,v8,v8,s54
v_mfma_f32_16x16x32_bf16 v[24:27],a[212:215],a[8:11],v[24:27]
ds_read_u16 v49,v49
v_lshlrev_b32_sdwa v51,1,v51 dst_sel:DWORD dst_unused:UNUSED_PAD src0_sel:DWORD src1_sel:WORD_1
v_mfma_f32_16x16x32_bf16 v[60:63],a[212:215],a[12:15],v[20:23]
ds_read_b128 v[20:23],v4 offset:384
v_pk_mul_f16 v9,v8,v9
v_mfma_f32_16x16x32_bf16 v[60:63],a[200:203],a[28:31],v[60:63]
s_waitcnt lgkmcnt(4)
ds_read_u16_d16_hi v51,v51
ds_read_b64_tr_b16 a[212:213],v1 offset:16416
v_lshl_add_u64 v[38:39],v[32:33],0,v[34:35]
v_lshlrev_b32_sdwa v44,1,v44 dst_sel:DWORD dst_unused:UNUSED_PAD src0_sel:DWORD src1_sel:WORD_1
v_mfma_f32_16x16x32_bf16 v[60:63],a[196:199],a[44:47],v[60:63]
ds_read_b64_tr_b16 a[214:215],v1 offset:17440
v_exp_f16_e64 v8,v9
v_mfma_f32_16x16x32_bf16 v[32:35],a[192:195],a[44:47],v[16:19]
ds_read_b128 v[16:19],v4 offset:416
ds_read_u16 v44,v44
v_lshlrev_b32_sdwa v46,1,v46 dst_sel:DWORD dst_unused:UNUSED_PAD src0_sel:DWORD src1_sel:WORD_1
v_mfma_f32_16x16x32_bf16 v[32:35],a[220:223],a[28:31],v[32:35]
ds_read_u16_d16_hi v46,v46
ds_read_b64_tr_b16 a[200:201],v2 offset:16416
v_lshlrev_b32_sdwa v45,1,v45 dst_sel:DWORD dst_unused:UNUSED_PAD src0_sel:DWORD src1_sel:WORD_1
v_mfma_f32_16x16x32_bf16 v[32:35],a[216:219],a[12:15],v[32:35]
ds_read_u16 v45,v45
v_exp_f16_sdwa v8,v9 dst_sel:WORD_1 dst_unused:UNUSED_PRESERVE src0_sel:WORD_1
v_mfma_f32_16x16x32_bf16 v[208:211],a[232:235],v[40:43],v[208:211]
ds_read_b64_tr_b16 a[202:203],v2 offset:17440
ds_read_b64_tr_b16 a[196:197],v2 offset:14368
v_lshlrev_b32_sdwa v47,1,v47 dst_sel:DWORD dst_unused:UNUSED_PAD src0_sel:DWORD src1_sel:WORD_1
v_mfma_f32_16x16x32_bf16 v[212:215],a[232:235],v[36:39],v[212:215]
ds_read_u16_d16_hi v47,v47
v_lshlrev_b32_sdwa v56,1,v56 dst_sel:DWORD dst_unused:UNUSED_PAD src0_sel:DWORD src1_sel:WORD_1
v_mfma_f32_16x16x32_bf16 v[172:175],a[236:239],v[36:39],v[172:175]
ds_read_b64_tr_b16 a[198:199],v2 offset:15392
v_pk_fma_f16 v8,v8,s55,s55
v_mfma_f32_16x16x32_bf16 v[168:171],a[236:239],v[40:43],v[168:171]
v_rcp_f16 v9,v8
v_lshlrev_b32_sdwa v58,1,v58 dst_sel:DWORD dst_unused:UNUSED_PAD src0_sel:DWORD src1_sel:WORD_1
v_mfma_f32_16x16x32_bf16 v[216:219],a[244:247],v[40:43],v[216:219]
ds_read_u16 v56,v56
v_lshlrev_b32_sdwa v57,1,v57 dst_sel:DWORD dst_unused:UNUSED_PAD src0_sel:DWORD src1_sel:WORD_1
v_mfma_f32_16x16x32_bf16 v[220:223],a[244:247],v[36:39],v[220:223]
TP6487_%=:
s_cbranch_execz TR0_%=
ds_read_u16_d16_hi v58,v58
v_lshlrev_b32_sdwa v59,1,v59 dst_sel:DWORD dst_unused:UNUSED_PAD src0_sel:DWORD src1_sel:WORD_1
v_mfma_f32_16x16x32_bf16 v[188:191],a[252:255],v[36:39],v[188:191]
ds_read_u16 v57,v57
v_lshlrev_b32_sdwa v52,1,v52 dst_sel:DWORD dst_unused:UNUSED_PAD src0_sel:DWORD src1_sel:WORD_1
v_mfma_f32_16x16x32_bf16 v[184:187],a[252:255],v[40:43],v[184:187]
ds_read_u16_d16_hi v59,v59
v_rcp_f16_sdwa v9,v8 dst_sel:WORD_1 dst_unused:UNUSED_PRESERVE src0_sel:WORD_1
v_mfma_f32_16x16x32_bf16 v[224:227],a[240:243],v[40:43],v[224:227]
ds_read_b64_tr_b16 a[192:193],v2 offset:12320
v_lshlrev_b32_sdwa v54,1,v54 dst_sel:DWORD dst_unused:UNUSED_PAD src0_sel:DWORD src1_sel:WORD_1
v_mfma_f32_16x16x32_bf16 v[228:231],a[240:243],v[36:39],v[228:231]
ds_read_b64_tr_b16 a[194:195],v2 offset:13344
v_fma_mix_f32 v53,v53,v9,0 op_sel_hi:[0,1,0]
v_mfma_f32_16x16x32_bf16 v[204:207],a[224:227],v[36:39],v[204:207]
ds_read_u16 v52,v52
v_fma_mix_f32 v55,v55,v9,0 op_sel:[0,1,0] op_sel_hi:[0,1,0]
v_mfma_f32_16x16x32_bf16 v[200:203],a[224:227],v[40:43],v[200:203]
s_waitcnt lgkmcnt(7)
ds_read_u16_d16_hi v54,v54
v_mfma_f32_16x16x32_bf16 v[36:39],a[208:211],a[48:51],v[20:23]
s_add_u32 m0,s57,53040
v_lshlrev_b32_sdwa v28,1,v28 dst_sel:DWORD dst_unused:UNUSED_PAD src0_sel:DWORD src1_sel:WORD_1
v_lshlrev_b32_sdwa v30,1,v30 dst_sel:DWORD dst_unused:UNUSED_PAD src0_sel:DWORD src1_sel:WORD_1
v_mfma_f32_16x16x32_bf16 v[36:39],a[204:207],a[64:67],v[36:39]
s_add_u32 s89,s56,6389760
ds_read_u16 v28,v28
v_lshl_add_u64 v[48:49],v[48:49],0,v[50:51]
v_mfma_f32_16x16x32_bf16 v[36:39],a[212:215],a[80:83],v[36:39]
buffer_load_dwordx4 v6,s[36:39],s89 offen lds
v_lshl_add_u64 v[50:51],v[44:45],0,v[46:47]
v_mfma_f32_16x16x32_bf16 v[40:43],a[200:203],a[80:83],v[16:19]
ds_read_u16_d16_hi v30,v30
v_lshlrev_b32_sdwa v29,1,v29 dst_sel:DWORD dst_unused:UNUSED_PAD src0_sel:DWORD src1_sel:WORD_1
v_mfma_f32_16x16x32_bf16 v[40:43],a[196:199],a[64:67],v[40:43]
s_waitcnt lgkmcnt(4)
ds_read_u16 v29,v29
v_lshl_add_u64 v[44:45],v[56:57],0,v[58:59]
v_mfma_f32_16x16x32_bf16 v[40:43],a[192:195],a[48:51],v[40:43]
v_lshlrev_b32_sdwa v31,1,v31 dst_sel:DWORD dst_unused:UNUSED_PAD src0_sel:DWORD src1_sel:WORD_1
v_lshlrev_b32_sdwa v24,1,v24 dst_sel:DWORD dst_unused:UNUSED_PAD src0_sel:DWORD src1_sel:WORD_1
v_mfma_f32_16x16x32_bf16 v[56:59],a[192:195],a[52:55],v[16:19]
s_waitcnt lgkmcnt(3)
ds_read_u16_d16_hi v31,v31
v_or_b32_e64 v46,v52,v54
v_mfma_f32_16x16x32_bf16 v[56:59],a[196:199],a[68:71],v[56:59]
ds_read_u16 v24,v24
v_cvt_pk_bf16_f32 v47,v53,v55
v_lshlrev_b32_sdwa v26,1,v26 dst_sel:DWORD dst_unused:UNUSED_PAD src0_sel:DWORD src1_sel:WORD_1
v_mfma_f32_16x16x32_bf16 v[56:59],a[200:203],a[84:87],v[56:59]
ds_read_u16_d16_hi v26,v26
v_lshlrev_b32_sdwa v25,1,v25 dst_sel:DWORD dst_unused:UNUSED_PAD src0_sel:DWORD src1_sel:WORD_1
v_mfma_f32_16x16x32_bf16 v[52:55],a[212:215],a[84:87],v[20:23]
ds_read_u16 v25,v25
v_lshlrev_b32_sdwa v27,1,v27 dst_sel:DWORD dst_unused:UNUSED_PAD src0_sel:DWORD src1_sel:WORD_1
v_mfma_f32_16x16x32_bf16 v[52:55],a[204:207],a[68:71],v[52:55]
ds_read_u16_d16_hi v27,v27
v_lshlrev_b32_sdwa v32,1,v32 dst_sel:DWORD dst_unused:UNUSED_PAD src0_sel:DWORD src1_sel:WORD_1
v_mfma_f32_16x16x32_bf16 v[52:55],a[208:211],a[52:55],v[52:55]
ds_read_u16 v32,v32
v_lshlrev_b32_sdwa v34,1,v34 dst_sel:DWORD dst_unused:UNUSED_PAD src0_sel:DWORD src1_sel:WORD_1
v_mfma_f32_16x16x32_bf16 v[136:139],a[232:235],v[48:51],v[136:139]
ds_read_u16_d16_hi v34,v34
v_lshlrev_b32_sdwa v33,1,v33 dst_sel:DWORD dst_unused:UNUSED_PAD src0_sel:DWORD src1_sel:WORD_1
v_mfma_f32_16x16x32_bf16 v[140:143],a[232:235],v[44:47],v[140:143]
ds_read_u16 v33,v33
v_lshlrev_b32_sdwa v35,1,v35 dst_sel:DWORD dst_unused:UNUSED_PAD src0_sel:DWORD src1_sel:WORD_1
v_mfma_f32_16x16x32_bf16 v[68:71],a[236:239],v[44:47],v[68:71]
ds_read_u16_d16_hi v35,v35
ds_read_b64_tr_b16 a[248:249],v3 offset:18480
v_mfma_f32_16x16x32_bf16 v[64:67],a[236:239],v[48:51],v[64:67]
ds_read_b64_tr_b16 a[250:251],v3 offset:20016
v_lshlrev_b32_sdwa v60,1,v60 dst_sel:DWORD dst_unused:UNUSED_PAD src0_sel:DWORD src1_sel:WORD_1
v_mfma_f32_16x16x32_bf16 v[144:147],a[244:247],v[48:51],v[144:147]
ds_read_u16 v60,v60
v_lshlrev_b32_sdwa v62,1,v62 dst_sel:DWORD dst_unused:UNUSED_PAD src0_sel:DWORD src1_sel:WORD_1
v_mfma_f32_16x16x32_bf16 v[148:151],a[244:247],v[44:47],v[148:151]
ds_read_u16_d16_hi v62,v62
v_lshlrev_b32_sdwa v61,1,v61 dst_sel:DWORD dst_unused:UNUSED_PAD src0_sel:DWORD src1_sel:WORD_1
v_mfma_f32_16x16x32_bf16 v[84:87],a[252:255],v[44:47],v[84:87]
ds_read_u16 v61,v61
v_lshlrev_b32_sdwa v63,1,v63 dst_sel:DWORD dst_unused:UNUSED_PAD src0_sel:DWORD src1_sel:WORD_1
v_mfma_f32_16x16x32_bf16 v[80:83],a[252:255],v[48:51],v[80:83]
ds_read_u16_d16_hi v63,v63
v_mfma_f32_16x16x32_bf16 v[152:155],a[240:243],v[48:51],v[152:155]
ds_read_b64_tr_b16 a[228:229],v3 offset:18488
v_mfma_f32_16x16x32_bf16 v[156:159],a[240:243],v[44:47],v[156:159]
ds_read_b64_tr_b16 a[230:231],v3 offset:20024
v_mfma_f32_16x16x32_bf16 v[100:103],a[224:227],v[44:47],v[100:103]
v_mfma_f32_16x16x32_bf16 v[96:99],a[224:227],v[48:51],v[96:99]
v_mfma_f32_16x16x32_bf16 v[44:47],a[208:211],a[56:59],v[20:23]
ds_read_b64_tr_b16 a[220:221],v1 offset:18480
v_lshlrev_b32_sdwa v36,1,v36 dst_sel:DWORD dst_unused:UNUSED_PAD src0_sel:DWORD src1_sel:WORD_1
v_mfma_f32_16x16x32_bf16 v[44:47],a[204:207],a[72:75],v[44:47]
s_waitcnt lgkmcnt(6)
ds_read_u16 v36,v36
v_lshl_add_u64 v[28:29],v[28:29],0,v[30:31]
v_fma_mixlo_f16 v8,v57,s99,0
v_mfma_f32_16x16x32_bf16 v[44:47],a[212:215],a[88:91],v[44:47]
ds_read_b64_tr_b16 a[222:223],v1 offset:19504
v_lshl_add_u64 v[30:31],v[24:25],0,v[26:27]
v_lshlrev_b32_sdwa v38,1,v38 dst_sel:DWORD dst_unused:UNUSED_PAD src0_sel:DWORD src1_sel:WORD_1
v_mfma_f32_16x16x32_bf16 v[48:51],a[200:203],a[88:91],v[16:19]
ds_read_u16_d16_hi v38,v38
v_fma_mixhi_f16 v8,v59,s99,0
v_mfma_f32_16x16x32_bf16 v[48:51],a[196:199],a[72:75],v[48:51]
ds_read_b64_tr_b16 a[216:217],v1 offset:20528
v_lshl_add_u64 v[24:25],v[32:33],0,v[34:35]
v_lshlrev_b32_sdwa v37,1,v37 dst_sel:DWORD dst_unused:UNUSED_PAD src0_sel:DWORD src1_sel:WORD_1
v_pk_fma_f16 v9,v8,v8,s54
v_mfma_f32_16x16x32_bf16 v[48:51],a[192:195],a[56:59],v[48:51]
ds_read_u16 v37,v37
v_lshlrev_b32_sdwa v39,1,v39 dst_sel:DWORD dst_unused:UNUSED_PAD src0_sel:DWORD src1_sel:WORD_1
v_mfma_f32_16x16x32_bf16 v[32:35],a[192:195],a[60:63],v[16:19]
ds_read_u16_d16_hi v39,v39
v_pk_mul_f16 v9,v8,v9
v_mfma_f32_16x16x32_bf16 v[32:35],a[196:199],a[76:79],v[32:35]
s_waitcnt lgkmcnt(7)
ds_read_b64_tr_b16 a[218:219],v1 offset:21552
v_lshl_add_u64 v[26:27],v[60:61],0,v[62:63]
v_lshlrev_b32_sdwa v40,1,v40 dst_sel:DWORD dst_unused:UNUSED_PAD src0_sel:DWORD src1_sel:WORD_1
v_mfma_f32_16x16x32_bf16 v[32:35],a[200:203],a[92:95],v[32:35]
v_exp_f16 v8,v9
v_mfma_f32_16x16x32_bf16 v[60:63],a[212:215],a[92:95],v[20:23]
ds_read_u16 v40,v40
v_lshlrev_b32_sdwa v42,1,v42 dst_sel:DWORD dst_unused:UNUSED_PAD src0_sel:DWORD src1_sel:WORD_1
v_mfma_f32_16x16x32_bf16 v[60:63],a[204:207],a[76:79],v[60:63]
ds_read_u16_d16_hi v42,v42
v_lshlrev_b32_sdwa v41,1,v41 dst_sel:DWORD dst_unused:UNUSED_PAD src0_sel:DWORD src1_sel:WORD_1
v_mfma_f32_16x16x32_bf16 v[60:63],a[208:211],a[60:63],v[60:63]
v_exp_f16_sdwa v8,v9 dst_sel:WORD_1 dst_unused:UNUSED_PRESERVE src0_sel:WORD_1
v_mfma_f32_16x16x32_bf16 v[112:115],a[232:235],v[28:31],v[112:115]
ds_read_u16 v41,v41
v_lshlrev_b32_sdwa v43,1,v43 dst_sel:DWORD dst_unused:UNUSED_PAD src0_sel:DWORD src1_sel:WORD_1
v_mfma_f32_16x16x32_bf16 v[116:119],a[232:235],v[24:27],v[116:119]
ds_read_u16_d16_hi v43,v43
v_lshlrev_b32_sdwa v52,1,v52 dst_sel:DWORD dst_unused:UNUSED_PAD src0_sel:DWORD src1_sel:WORD_1
v_mfma_f32_16x16x32_bf16 v[76:79],a[236:239],v[24:27],v[76:79]
ds_read_u16 v52,v52
v_pk_fma_f16 v8,v8,s55,s55
v_mfma_f32_16x16x32_bf16 v[72:75],a[236:239],v[28:31],v[72:75]
ds_read_b64_tr_b16 a[232:233],v3 offset:18544
v_rcp_f16_e64 v9,v8
v_lshlrev_b32_sdwa v54,1,v54 dst_sel:DWORD dst_unused:UNUSED_PAD src0_sel:DWORD src1_sel:WORD_1
v_mfma_f32_16x16x32_bf16 v[120:123],a[244:247],v[28:31],v[120:123]
ds_read_u16_d16_hi v54,v54
v_lshlrev_b32_sdwa v53,1,v53 dst_sel:DWORD dst_unused:UNUSED_PAD src0_sel:DWORD src1_sel:WORD_1
v_mfma_f32_16x16x32_bf16 v[124:127],a[244:247],v[24:27],v[124:127]
ds_read_u16 v53,v53
ds_read_b64_tr_b16 a[234:235],v3 offset:20080
v_mfma_f32_16x16x32_bf16 v[92:95],a[252:255],v[24:27],v[92:95]
ds_read_b64_tr_b16 a[236:237],v3 offset:18552
v_lshlrev_b32_sdwa v55,1,v55 dst_sel:DWORD dst_unused:UNUSED_PAD src0_sel:DWORD src1_sel:WORD_1
v_mfma_f32_16x16x32_bf16 v[88:91],a[252:255],v[28:31],v[88:91]
ds_read_u16_d16_hi v55,v55
v_rcp_f16_sdwa v9,v8 dst_sel:WORD_1 dst_unused:UNUSED_PRESERVE src0_sel:WORD_1
v_mfma_f32_16x16x32_bf16 v[128:131],a[240:243],v[28:31],v[128:131]
ds_read_b64_tr_b16 a[238:239],v3 offset:20088
ds_read_b64_tr_b16 a[244:245],v3 offset:18608
v_lshlrev_b32_sdwa v56,1,v56 dst_sel:DWORD dst_unused:UNUSED_PAD src0_sel:DWORD src1_sel:WORD_1
v_mfma_f32_16x16x32_bf16 v[132:135],a[240:243],v[24:27],v[132:135]
ds_read_u16 v56,v56
v_lshlrev_b32_sdwa v58,1,v58 dst_sel:DWORD dst_unused:UNUSED_PAD src0_sel:DWORD src1_sel:WORD_1
v_mfma_f32_16x16x32_bf16 v[108:111],a[224:227],v[24:27],v[108:111]
ds_read_u16_d16_hi v58,v58
v_fma_mix_f32 v57,v57,v9,0 op_sel_hi:[0,1,0]
v_mfma_f32_16x16x32_bf16 v[104:107],a[224:227],v[28:31],v[104:107]
ds_read_b64_tr_b16 a[246:247],v3 offset:20144
v_fma_mix_f32 v59,v59,v9,0 op_sel:[0,1,0] op_sel_hi:[0,1,0]
v_mfma_f32_16x16x32_bf16 v[24:27],a[208:211],a[0:3],v[20:23]
ds_read_b64_tr_b16 a[252:253],v3 offset:18616
v_lshlrev_b32_sdwa v44,1,v44 dst_sel:DWORD dst_unused:UNUSED_PAD src0_sel:DWORD src1_sel:WORD_1
v_mfma_f32_16x16x32_bf16 v[24:27],a[204:207],a[16:19],v[24:27]
s_waitcnt lgkmcnt(10)
ds_read_b64_tr_b16 a[254:255],v3 offset:20152
v_lshl_add_u64 v[36:37],v[36:37],0,v[38:39]
v_mfma_f32_16x16x32_bf16 v[24:27],a[212:215],a[32:35],v[24:27]
ds_read_u16 v44,v44
v_lshl_add_u64 v[38:39],v[40:41],0,v[42:43]
v_lshlrev_b32_sdwa v46,1,v46 dst_sel:DWORD dst_unused:UNUSED_PAD src0_sel:DWORD src1_sel:WORD_1
v_mfma_f32_16x16x32_bf16 v[28:31],a[200:203],a[32:35],v[16:19]
ds_read_u16_d16_hi v46,v46
v_lshlrev_b32_sdwa v45,1,v45 dst_sel:DWORD dst_unused:UNUSED_PAD src0_sel:DWORD src1_sel:WORD_1
v_mfma_f32_16x16x32_bf16 v[28:31],a[196:199],a[16:19],v[28:31]
s_waitcnt lgkmcnt(9)
ds_read_u16 v45,v45
v_lshl_add_u64 v[40:41],v[52:53],0,v[54:55]
v_mfma_f32_16x16x32_bf16 v[28:31],a[192:195],a[0:3],v[28:31]
v_lshlrev_b32_sdwa v47,1,v47 dst_sel:DWORD dst_unused:UNUSED_PAD src0_sel:DWORD src1_sel:WORD_1
v_lshlrev_b32_sdwa v48,1,v48 dst_sel:DWORD dst_unused:UNUSED_PAD src0_sel:DWORD src1_sel:WORD_1
v_mfma_f32_16x16x32_bf16 v[52:55],a[192:195],a[4:7],v[16:19]
s_waitcnt lgkmcnt(6)
ds_read_u16_d16_hi v47,v47
v_or_b32 v42,v56,v58
v_mfma_f32_16x16x32_bf16 v[52:55],a[196:199],a[20:23],v[52:55]
ds_read_u16 v48,v48
v_cvt_pk_bf16_f32 v43,v57,v59
v_lshlrev_b32_sdwa v50,1,v50 dst_sel:DWORD dst_unused:UNUSED_PAD src0_sel:DWORD src1_sel:WORD_1
v_mfma_f32_16x16x32_bf16 v[52:55],a[200:203],a[36:39],v[52:55]
ds_read_u16_d16_hi v50,v50
v_lshlrev_b32_sdwa v49,1,v49 dst_sel:DWORD dst_unused:UNUSED_PAD src0_sel:DWORD src1_sel:WORD_1
v_mfma_f32_16x16x32_bf16 v[56:59],a[212:215],a[36:39],v[20:23]
ds_read_u16 v49,v49
v_lshlrev_b32_sdwa v51,1,v51 dst_sel:DWORD dst_unused:UNUSED_PAD src0_sel:DWORD src1_sel:WORD_1
v_mfma_f32_16x16x32_bf16 v[56:59],a[204:207],a[20:23],v[56:59]
ds_read_u16_d16_hi v51,v51
v_lshlrev_b32_sdwa v60,1,v60 dst_sel:DWORD dst_unused:UNUSED_PAD src0_sel:DWORD src1_sel:WORD_1
v_mfma_f32_16x16x32_bf16 v[56:59],a[208:211],a[4:7],v[56:59]
ds_read_u16 v60,v60
v_lshlrev_b32_sdwa v62,1,v62 dst_sel:DWORD dst_unused:UNUSED_PAD src0_sel:DWORD src1_sel:WORD_1
v_mfma_f32_16x16x32_bf16 v[232:235],a[248:251],v[36:39],v[232:235]
ds_read_u16_d16_hi v62,v62
v_lshlrev_b32_sdwa v61,1,v61 dst_sel:DWORD dst_unused:UNUSED_PAD src0_sel:DWORD src1_sel:WORD_1
v_mfma_f32_16x16x32_bf16 v[236:239],a[248:251],v[40:43],v[236:239]
ds_read_u16 v61,v61
v_lshlrev_b32_sdwa v63,1,v63 dst_sel:DWORD dst_unused:UNUSED_PAD src0_sel:DWORD src1_sel:WORD_1
v_mfma_f32_16x16x32_bf16 v[164:167],a[228:231],v[40:43],v[164:167]
ds_read_u16_d16_hi v63,v63
v_lshlrev_b32_sdwa v32,1,v32 dst_sel:DWORD dst_unused:UNUSED_PAD src0_sel:DWORD src1_sel:WORD_1
v_mfma_f32_16x16x32_bf16 v[160:163],a[228:231],v[36:39],v[160:163]
ds_read_u16 v32,v32
v_lshlrev_b32_sdwa v34,1,v34 dst_sel:DWORD dst_unused:UNUSED_PAD src0_sel:DWORD src1_sel:WORD_1
v_mfma_f32_16x16x32_bf16 v[240:243],a[232:235],v[36:39],v[240:243]
ds_read_u16_d16_hi v34,v34
v_lshlrev_b32_sdwa v33,1,v33 dst_sel:DWORD dst_unused:UNUSED_PAD src0_sel:DWORD src1_sel:WORD_1
v_mfma_f32_16x16x32_bf16 v[244:247],a[232:235],v[40:43],v[244:247]
ds_read_u16 v33,v33
v_lshlrev_b32_sdwa v35,1,v35 dst_sel:DWORD dst_unused:UNUSED_PAD src0_sel:DWORD src1_sel:WORD_1
v_mfma_f32_16x16x32_bf16 v[180:183],a[236:239],v[40:43],v[180:183]
ds_read_u16_d16_hi v35,v35
v_mfma_f32_16x16x32_bf16 v[176:179],a[236:239],v[36:39],v[176:179]
s_waitcnt lgkmcnt(8)
v_mfma_f32_16x16x32_bf16 v[248:251],a[244:247],v[36:39],v[248:251]
v_mfma_f32_16x16x32_bf16 v[252:255],a[244:247],v[40:43],v[252:255]
v_mfma_f32_16x16x32_bf16 v[196:199],a[252:255],v[40:43],v[196:199]
v_mfma_f32_16x16x32_bf16 v[192:195],a[252:255],v[36:39],v[192:195]
v_mfma_f32_16x16x32_bf16 v[40:43],a[208:211],a[8:11],v[20:23]
v_lshlrev_b32_sdwa v24,1,v24 dst_sel:DWORD dst_unused:UNUSED_PAD src0_sel:DWORD src1_sel:WORD_1
v_fma_mixlo_f16 v8,v53,s99,0
v_mfma_f32_16x16x32_bf16 v[40:43],a[204:207],a[24:27],v[40:43]
ds_read_u16 v24,v24
v_lshl_add_u64 v[44:45],v[44:45],0,v[46:47]
v_mfma_f32_16x16x32_bf16 v[40:43],a[212:215],a[40:43],v[40:43]
v_lshl_add_u64 v[46:47],v[48:49],0,v[50:51]
v_lshlrev_b32_sdwa v26,1,v26 dst_sel:DWORD dst_unused:UNUSED_PAD src0_sel:DWORD src1_sel:WORD_1
v_mfma_f32_16x16x32_bf16 v[36:39],a[200:203],a[40:43],v[16:19]
ds_read_u16_d16_hi v26,v26
v_fma_mixhi_f16 v8,v55,s99,0
v_mfma_f32_16x16x32_bf16 v[36:39],a[196:199],a[24:27],v[36:39]
s_waitcnt lgkmcnt(2)
v_lshl_add_u64 v[48:49],v[60:61],0,v[62:63]
v_lshlrev_b32_sdwa v25,1,v25 dst_sel:DWORD dst_unused:UNUSED_PAD src0_sel:DWORD src1_sel:WORD_1
v_pk_fma_f16 v9,v8,v8,s54
v_mfma_f32_16x16x32_bf16 v[36:39],a[192:195],a[8:11],v[36:39]
ds_read_u16 v25,v25
v_lshlrev_b32_sdwa v27,1,v27 dst_sel:DWORD dst_unused:UNUSED_PAD src0_sel:DWORD src1_sel:WORD_1
v_mfma_f32_16x16x32_bf16 v[60:63],a[192:195],a[12:15],v[16:19]
ds_read_b128 v[16:19],v4 offset:512
v_pk_mul_f16 v9,v8,v9
v_mfma_f32_16x16x32_bf16 v[60:63],a[196:199],a[28:31],v[60:63]
ds_read_u16_d16_hi v27,v27
ds_read_b64_tr_b16 a[192:193],v1 offset:22576
v_lshl_add_u64 v[50:51],v[32:33],0,v[34:35]
v_lshlrev_b32_sdwa v28,1,v28 dst_sel:DWORD dst_unused:UNUSED_PAD src0_sel:DWORD src1_sel:WORD_1
v_mfma_f32_16x16x32_bf16 v[60:63],a[200:203],a[44:47],v[60:63]
ds_read_b64_tr_b16 a[194:195],v1 offset:23600
v_exp_f16 v8,v9
v_mfma_f32_16x16x32_bf16 v[32:35],a[212:215],a[44:47],v[20:23]
ds_read_b128 v[20:23],v4 offset:544
ds_read_u16 v28,v28
v_lshlrev_b32_sdwa v30,1,v30 dst_sel:DWORD dst_unused:UNUSED_PAD src0_sel:DWORD src1_sel:WORD_1
v_mfma_f32_16x16x32_bf16 v[32:35],a[204:207],a[28:31],v[32:35]
ds_read_u16_d16_hi v30,v30
ds_read_b64_tr_b16 a[196:197],v2 offset:22576
v_lshlrev_b32_sdwa v29,1,v29 dst_sel:DWORD dst_unused:UNUSED_PAD src0_sel:DWORD src1_sel:WORD_1
v_mfma_f32_16x16x32_bf16 v[32:35],a[208:211],a[12:15],v[32:35]
ds_read_u16 v29,v29
v_exp_f16_sdwa v8,v9 dst_sel:WORD_1 dst_unused:UNUSED_PRESERVE src0_sel:WORD_1
v_mfma_f32_16x16x32_bf16 v[208:211],a[248:251],v[44:47],v[208:211]
ds_read_b64_tr_b16 a[198:199],v2 offset:23600
ds_read_b64_tr_b16 a[200:201],v2 offset:20528
v_lshlrev_b32_sdwa v31,1,v31 dst_sel:DWORD dst_unused:UNUSED_PAD src0_sel:DWORD src1_sel:WORD_1
v_mfma_f32_16x16x32_bf16 v[212:215],a[248:251],v[48:51],v[212:215]
ds_read_u16_d16_hi v31,v31
v_lshlrev_b32_sdwa v56,1,v56 dst_sel:DWORD dst_unused:UNUSED_PAD src0_sel:DWORD src1_sel:WORD_1
v_mfma_f32_16x16x32_bf16 v[172:175],a[228:231],v[48:51],v[172:175]
ds_read_b64_tr_b16 a[202:203],v2 offset:21552
v_pk_fma_f16 v8,v8,s55,s55
v_mfma_f32_16x16x32_bf16 v[168:171],a[228:231],v[44:47],v[168:171]
v_rcp_f16 v9,v8
v_lshlrev_b32_sdwa v58,1,v58 dst_sel:DWORD dst_unused:UNUSED_PAD src0_sel:DWORD src1_sel:WORD_1
v_mfma_f32_16x16x32_bf16 v[216:219],a[232:235],v[44:47],v[216:219]
ds_read_u16 v56,v56
v_lshlrev_b32_sdwa v57,1,v57 dst_sel:DWORD dst_unused:UNUSED_PAD src0_sel:DWORD src1_sel:WORD_1
v_mfma_f32_16x16x32_bf16 v[220:223],a[232:235],v[48:51],v[220:223]
ds_read_u16_d16_hi v58,v58
v_lshlrev_b32_sdwa v59,1,v59 dst_sel:DWORD dst_unused:UNUSED_PAD src0_sel:DWORD src1_sel:WORD_1
v_mfma_f32_16x16x32_bf16 v[188:191],a[236:239],v[48:51],v[188:191]
ds_read_u16 v57,v57
v_lshlrev_b32_sdwa v52,1,v52 dst_sel:DWORD dst_unused:UNUSED_PAD src0_sel:DWORD src1_sel:WORD_1
v_mfma_f32_16x16x32_bf16 v[184:187],a[236:239],v[44:47],v[184:187]
ds_read_u16_d16_hi v59,v59
v_rcp_f16_sdwa v9,v8 dst_sel:WORD_1 dst_unused:UNUSED_PRESERVE src0_sel:WORD_1
v_mfma_f32_16x16x32_bf16 v[224:227],a[244:247],v[44:47],v[224:227]
ds_read_b64_tr_b16 a[212:213],v2 offset:18480
v_lshlrev_b32_sdwa v54,1,v54 dst_sel:DWORD dst_unused:UNUSED_PAD src0_sel:DWORD src1_sel:WORD_1
v_mfma_f32_16x16x32_bf16 v[228:231],a[244:247],v[48:51],v[228:231]
ds_read_b64_tr_b16 a[214:215],v2 offset:19504
v_fma_mix_f32 v53,v53,v9,0 op_sel_hi:[0,1,0]
v_mfma_f32_16x16x32_bf16 v[204:207],a[252:255],v[48:51],v[204:207]
ds_read_u16 v52,v52
v_fma_mix_f32 v55,v55,v9,0 op_sel:[0,1,0] op_sel_hi:[0,1,0]
v_mfma_f32_16x16x32_bf16 v[200:203],a[252:255],v[44:47],v[200:203]
s_waitcnt lgkmcnt(7)
ds_read_u16_d16_hi v54,v54
v_mfma_f32_16x16x32_bf16 v[48:51],a[220:223],a[48:51],v[16:19]
v_lshlrev_b32_sdwa v40,1,v40 dst_sel:DWORD dst_unused:UNUSED_PAD src0_sel:DWORD src1_sel:WORD_1
v_lshlrev_b32_sdwa v42,1,v42 dst_sel:DWORD dst_unused:UNUSED_PAD src0_sel:DWORD src1_sel:WORD_1
v_mfma_f32_16x16x32_bf16 v[48:51],a[216:219],a[64:67],v[48:51]
ds_read_u16 v40,v40
v_lshl_add_u64 v[24:25],v[24:25],0,v[26:27]
v_mfma_f32_16x16x32_bf16 v[48:51],a[192:195],a[80:83],v[48:51]
ds_read_u16_d16_hi v42,v42
v_lshl_add_u64 v[26:27],v[28:29],0,v[30:31]
v_mfma_f32_16x16x32_bf16 v[44:47],a[196:199],a[80:83],v[20:23]
v_lshlrev_b32_sdwa v41,1,v41 dst_sel:DWORD dst_unused:UNUSED_PAD src0_sel:DWORD src1_sel:WORD_1
v_lshlrev_b32_sdwa v43,1,v43 dst_sel:DWORD dst_unused:UNUSED_PAD src0_sel:DWORD src1_sel:WORD_1
v_mfma_f32_16x16x32_bf16 v[44:47],a[200:203],a[64:67],v[44:47]
s_waitcnt lgkmcnt(4)
ds_read_u16 v41,v41
v_lshl_add_u64 v[28:29],v[56:57],0,v[58:59]
v_mfma_f32_16x16x32_bf16 v[44:47],a[212:215],a[48:51],v[44:47]
ds_read_u16_d16_hi v43,v43
v_lshlrev_b32_sdwa v36,1,v36 dst_sel:DWORD dst_unused:UNUSED_PAD src0_sel:DWORD src1_sel:WORD_1
v_mfma_f32_16x16x32_bf16 v[56:59],a[212:215],a[52:55],v[20:23]
s_waitcnt lgkmcnt(4)
ds_read_u16 v36,v36
v_or_b32 v30,v52,v54
v_mfma_f32_16x16x32_bf16 v[56:59],a[200:203],a[68:71],v[56:59]
s_waitcnt vmcnt(6)
v_cvt_pk_bf16_f32 v31,v53,v55
v_lshlrev_b32_sdwa v38,1,v38 dst_sel:DWORD dst_unused:UNUSED_PAD src0_sel:DWORD src1_sel:WORD_1
v_mfma_f32_16x16x32_bf16 v[56:59],a[196:199],a[84:87],v[56:59]
s_barrier
ds_read_u16_d16_hi v38,v38
v_lshlrev_b32_sdwa v37,1,v37 dst_sel:DWORD dst_unused:UNUSED_PAD src0_sel:DWORD src1_sel:WORD_1
v_mfma_f32_16x16x32_bf16 v[52:55],a[192:195],a[84:87],v[16:19]
ds_read_u16 v37,v37
v_lshlrev_b32_sdwa v39,1,v39 dst_sel:DWORD dst_unused:UNUSED_PAD src0_sel:DWORD src1_sel:WORD_1
v_mfma_f32_16x16x32_bf16 v[52:55],a[216:219],a[68:71],v[52:55]
s_add_u32 m0,s58,12320
ds_read_u16_d16_hi v39,v39
v_lshlrev_b32_sdwa v32,1,v32 dst_sel:DWORD dst_unused:UNUSED_PAD src0_sel:DWORD src1_sel:WORD_1
v_mfma_f32_16x16x32_bf16 v[52:55],a[220:223],a[52:55],v[52:55]
s_add_u32 s69,s69,s59
ds_read_u16 v32,v32
v_lshlrev_b32_sdwa v34,1,v34 dst_sel:DWORD dst_unused:UNUSED_PAD src0_sel:DWORD src1_sel:WORD_1
v_mfma_f32_16x16x32_bf16 v[136:139],a[248:251],v[24:27],v[136:139]
buffer_load_dwordx4 v5,s[44:47],s69 offen offset:0 lds
v_lshlrev_b32_sdwa v33,1,v33 dst_sel:DWORD dst_unused:UNUSED_PAD src0_sel:DWORD src1_sel:WORD_1
v_mfma_f32_16x16x32_bf16 v[140:143],a[248:251],v[28:31],v[140:143]
ds_read_u16_d16_hi v34,v34
v_lshlrev_b32_sdwa v35,1,v35 dst_sel:DWORD dst_unused:UNUSED_PAD src0_sel:DWORD src1_sel:WORD_1
v_mfma_f32_16x16x32_bf16 v[68:71],a[228:231],v[28:31],v[68:71]
ds_read_u16 v33,v33
ds_read_u16_d16_hi v35,v35
v_mfma_f32_16x16x32_bf16 v[64:67],a[228:231],v[24:27],v[64:67]
ds_read_b64_tr_b16 a[240:241],v3 offset:0
v_lshlrev_b32_sdwa v60,1,v60 dst_sel:DWORD dst_unused:UNUSED_PAD src0_sel:DWORD src1_sel:WORD_1
v_mfma_f32_16x16x32_bf16 v[144:147],a[232:235],v[24:27],v[144:147]
s_add_u32 s69,s69,s53
ds_read_u16 v60,v60
v_lshlrev_b32_sdwa v62,1,v62 dst_sel:DWORD dst_unused:UNUSED_PAD src0_sel:DWORD src1_sel:WORD_1
v_mfma_f32_16x16x32_bf16 v[148:151],a[232:235],v[28:31],v[148:151]
buffer_load_dwordx4 v5,s[44:47],s69 offen offset:1024 lds
ds_read_u16_d16_hi v62,v62
v_mfma_f32_16x16x32_bf16 v[84:87],a[236:239],v[28:31],v[84:87]
ds_read_b64_tr_b16 a[242:243],v3 offset:1536
v_lshlrev_b32_sdwa v61,1,v61 dst_sel:DWORD dst_unused:UNUSED_PAD src0_sel:DWORD src1_sel:WORD_1
v_mfma_f32_16x16x32_bf16 v[80:83],a[236:239],v[24:27],v[80:83]
ds_read_u16 v61,v61
v_lshlrev_b32_sdwa v63,1,v63 dst_sel:DWORD dst_unused:UNUSED_PAD src0_sel:DWORD src1_sel:WORD_1
v_mfma_f32_16x16x32_bf16 v[152:155],a[244:247],v[24:27],v[152:155]
ds_read_u16_d16_hi v63,v63
v_mfma_f32_16x16x32_bf16 v[156:159],a[244:247],v[28:31],v[156:159]
s_add_u32 s69,s69,s53
ds_read_b64_tr_b16 a[224:225],v3 offset:8
v_mfma_f32_16x16x32_bf16 v[100:103],a[252:255],v[28:31],v[100:103]
buffer_load_dwordx4 v5,s[44:47],s69 offen offset:2048 lds
v_mfma_f32_16x16x32_bf16 v[96:99],a[252:255],v[24:27],v[96:99]
ds_read_b64_tr_b16 a[226:227],v3 offset:1544
v_mfma_f32_16x16x32_bf16 v[28:31],a[220:223],a[56:59],v[16:19]
ds_read_b64_tr_b16 a[204:205],v1 offset:0
v_lshlrev_b32_sdwa v48,1,v48 dst_sel:DWORD dst_unused:UNUSED_PAD src0_sel:DWORD src1_sel:WORD_1
v_mfma_f32_16x16x32_bf16 v[28:31],a[216:219],a[72:75],v[28:31]
s_waitcnt lgkmcnt(7)
ds_read_u16 v48,v48
v_lshl_add_u64 v[40:41],v[40:41],0,v[42:43]
v_fma_mixlo_f16 v8,v57,s99,0
v_mfma_f32_16x16x32_bf16 v[28:31],a[192:195],a[88:91],v[28:31]
ds_read_b64_tr_b16 a[206:207],v1 offset:1024
v_lshl_add_u64 v[42:43],v[36:37],0,v[38:39]
v_lshlrev_b32_sdwa v50,1,v50 dst_sel:DWORD dst_unused:UNUSED_PAD src0_sel:DWORD src1_sel:WORD_1
v_mfma_f32_16x16x32_bf16 v[24:27],a[196:199],a[88:91],v[20:23]
ds_read_u16_d16_hi v50,v50
v_fma_mixhi_f16 v8,v59,s99,0
v_mfma_f32_16x16x32_bf16 v[24:27],a[200:203],a[72:75],v[24:27]
ds_read_b64_tr_b16 a[208:209],v1 offset:2048
v_lshl_add_u64 v[36:37],v[32:33],0,v[34:35]
v_lshlrev_b32_sdwa v49,1,v49 dst_sel:DWORD dst_unused:UNUSED_PAD src0_sel:DWORD src1_sel:WORD_1
v_pk_fma_f16 v9,v8,v8,s54
v_mfma_f32_16x16x32_bf16 v[24:27],a[212:215],a[56:59],v[24:27]
ds_read_u16 v49,v49
v_lshlrev_b32_sdwa v51,1,v51 dst_sel:DWORD dst_unused:UNUSED_PAD src0_sel:DWORD src1_sel:WORD_1
v_mfma_f32_16x16x32_bf16 v[32:35],a[212:215],a[60:63],v[20:23]
ds_read_u16_d16_hi v51,v51
v_pk_mul_f16 v9,v8,v9
v_mfma_f32_16x16x32_bf16 v[32:35],a[200:203],a[76:79],v[32:35]
s_waitcnt lgkmcnt(8)
ds_read_b64_tr_b16 a[210:211],v1 offset:3072
v_lshl_add_u64 v[38:39],v[60:61],0,v[62:63]
v_lshlrev_b32_sdwa v44,1,v44 dst_sel:DWORD dst_unused:UNUSED_PAD src0_sel:DWORD src1_sel:WORD_1
v_mfma_f32_16x16x32_bf16 v[32:35],a[196:199],a[92:95],v[32:35]
v_exp_f16 v8,v9
v_mfma_f32_16x16x32_bf16 v[60:63],a[192:195],a[92:95],v[16:19]
ds_read_u16 v44,v44
v_lshlrev_b32_sdwa v46,1,v46 dst_sel:DWORD dst_unused:UNUSED_PAD src0_sel:DWORD src1_sel:WORD_1
v_mfma_f32_16x16x32_bf16 v[60:63],a[216:219],a[76:79],v[60:63]
ds_read_u16_d16_hi v46,v46
v_lshlrev_b32_sdwa v45,1,v45 dst_sel:DWORD dst_unused:UNUSED_PAD src0_sel:DWORD src1_sel:WORD_1
v_mfma_f32_16x16x32_bf16 v[60:63],a[220:223],a[60:63],v[60:63]
v_exp_f16_sdwa v8,v9 dst_sel:WORD_1 dst_unused:UNUSED_PRESERVE src0_sel:WORD_1
v_mfma_f32_16x16x32_bf16 v[112:115],a[248:251],v[40:43],v[112:115]
ds_read_u16 v45,v45
v_lshlrev_b32_sdwa v47,1,v47 dst_sel:DWORD dst_unused:UNUSED_PAD src0_sel:DWORD src1_sel:WORD_1
v_mfma_f32_16x16x32_bf16 v[116:119],a[248:251],v[36:39],v[116:119]
ds_read_u16_d16_hi v47,v47
v_lshlrev_b32_sdwa v52,1,v52 dst_sel:DWORD dst_unused:UNUSED_PAD src0_sel:DWORD src1_sel:WORD_1
v_mfma_f32_16x16x32_bf16 v[76:79],a[228:231],v[36:39],v[76:79]
ds_read_u16 v52,v52
v_pk_fma_f16 v8,v8,s55,s55
v_mfma_f32_16x16x32_bf16 v[72:75],a[228:231],v[40:43],v[72:75]
ds_read_b64_tr_b16 a[248:249],v3 offset:64
v_rcp_f16_e64 v9,v8
v_lshlrev_b32_sdwa v54,1,v54 dst_sel:DWORD dst_unused:UNUSED_PAD src0_sel:DWORD src1_sel:WORD_1
v_mfma_f32_16x16x32_bf16 v[120:123],a[232:235],v[40:43],v[120:123]
ds_read_u16_d16_hi v54,v54
v_lshlrev_b32_sdwa v53,1,v53 dst_sel:DWORD dst_unused:UNUSED_PAD src0_sel:DWORD src1_sel:WORD_1
v_mfma_f32_16x16x32_bf16 v[124:127],a[232:235],v[36:39],v[124:127]
ds_read_u16 v53,v53
ds_read_b64_tr_b16 a[250:251],v3 offset:1600
v_mfma_f32_16x16x32_bf16 v[92:95],a[236:239],v[36:39],v[92:95]
ds_read_b64_tr_b16 a[228:229],v3 offset:72
v_lshlrev_b32_sdwa v55,1,v55 dst_sel:DWORD dst_unused:UNUSED_PAD src0_sel:DWORD src1_sel:WORD_1
v_mfma_f32_16x16x32_bf16 v[88:91],a[236:239],v[40:43],v[88:91]
ds_read_u16_d16_hi v55,v55
v_rcp_f16_sdwa v9,v8 dst_sel:WORD_1 dst_unused:UNUSED_PRESERVE src0_sel:WORD_1
v_mfma_f32_16x16x32_bf16 v[128:131],a[244:247],v[40:43],v[128:131]
ds_read_b64_tr_b16 a[230:231],v3 offset:1608
ds_read_b64_tr_b16 a[232:233],v3 offset:128
v_lshlrev_b32_sdwa v56,1,v56 dst_sel:DWORD dst_unused:UNUSED_PAD src0_sel:DWORD src1_sel:WORD_1
v_mfma_f32_16x16x32_bf16 v[132:135],a[244:247],v[36:39],v[132:135]
ds_read_u16 v56,v56
v_lshlrev_b32_sdwa v58,1,v58 dst_sel:DWORD dst_unused:UNUSED_PAD src0_sel:DWORD src1_sel:WORD_1
v_mfma_f32_16x16x32_bf16 v[108:111],a[252:255],v[36:39],v[108:111]
ds_read_u16_d16_hi v58,v58
v_fma_mix_f32 v57,v57,v9,0 op_sel_hi:[0,1,0]
v_mfma_f32_16x16x32_bf16 v[104:107],a[252:255],v[40:43],v[104:107]
ds_read_b64_tr_b16 a[234:235],v3 offset:1664
v_fma_mix_f32 v59,v59,v9,0 op_sel:[0,1,0] op_sel_hi:[0,1,0]
v_mfma_f32_16x16x32_bf16 v[36:39],a[220:223],a[0:3],v[16:19]
s_add_u32 m0,s58,18480
ds_read_b64_tr_b16 a[236:237],v3 offset:136
v_lshlrev_b32_sdwa v28,1,v28 dst_sel:DWORD dst_unused:UNUSED_PAD src0_sel:DWORD src1_sel:WORD_1
v_mfma_f32_16x16x32_bf16 v[36:39],a[216:219],a[16:19],v[36:39]
s_waitcnt lgkmcnt(10)
ds_read_b64_tr_b16 a[238:239],v3 offset:1672
v_lshl_add_u64 v[48:49],v[48:49],0,v[50:51]
v_mfma_f32_16x16x32_bf16 v[36:39],a[192:195],a[32:35],v[36:39]
s_add_u32 s69,s69,s59
ds_read_u16 v28,v28
v_lshl_add_u64 v[50:51],v[44:45],0,v[46:47]
v_lshlrev_b32_sdwa v30,1,v30 dst_sel:DWORD dst_unused:UNUSED_PAD src0_sel:DWORD src1_sel:WORD_1
v_mfma_f32_16x16x32_bf16 v[40:43],a[196:199],a[32:35],v[20:23]
buffer_load_dwordx4 v5,s[44:47],s69 offen offset:0 lds
v_lshlrev_b32_sdwa v29,1,v29 dst_sel:DWORD dst_unused:UNUSED_PAD src0_sel:DWORD src1_sel:WORD_1
v_mfma_f32_16x16x32_bf16 v[40:43],a[200:203],a[16:19],v[40:43]
s_waitcnt lgkmcnt(8)
ds_read_u16_d16_hi v30,v30
v_lshl_add_u64 v[44:45],v[52:53],0,v[54:55]
v_mfma_f32_16x16x32_bf16 v[40:43],a[212:215],a[0:3],v[40:43]
s_add_u32 s69,s69,s53
ds_read_u16 v29,v29
v_lshlrev_b32_sdwa v31,1,v31 dst_sel:DWORD dst_unused:UNUSED_PAD src0_sel:DWORD src1_sel:WORD_1
v_mfma_f32_16x16x32_bf16 v[52:55],a[212:215],a[4:7],v[20:23]
s_waitcnt lgkmcnt(6)
buffer_load_dwordx4 v5,s[44:47],s69 offen offset:1024 lds
v_or_b32 v46,v56,v58
v_lshlrev_b32_sdwa v24,1,v24 dst_sel:DWORD dst_unused:UNUSED_PAD src0_sel:DWORD src1_sel:WORD_1
v_mfma_f32_16x16x32_bf16 v[52:55],a[200:203],a[20:23],v[52:55]
s_add_u32 s69,s69,s53
ds_read_u16_d16_hi v31,v31
v_cvt_pk_bf16_f32 v47,v57,v59
v_lshlrev_b32_sdwa v26,1,v26 dst_sel:DWORD dst_unused:UNUSED_PAD src0_sel:DWORD src1_sel:WORD_1
v_mfma_f32_16x16x32_bf16 v[52:55],a[196:199],a[36:39],v[52:55]
buffer_load_dwordx4 v5,s[44:47],s69 offen offset:2048 lds
v_lshlrev_b32_sdwa v25,1,v25 dst_sel:DWORD dst_unused:UNUSED_PAD src0_sel:DWORD src1_sel:WORD_1
v_mfma_f32_16x16x32_bf16 v[56:59],a[192:195],a[36:39],v[16:19]
ds_read_u16 v24,v24
v_lshlrev_b32_sdwa v27,1,v27 dst_sel:DWORD dst_unused:UNUSED_PAD src0_sel:DWORD src1_sel:WORD_1
v_mfma_f32_16x16x32_bf16 v[56:59],a[216:219],a[20:23],v[56:59]
ds_read_u16_d16_hi v26,v26
v_lshlrev_b32_sdwa v60,1,v60 dst_sel:DWORD dst_unused:UNUSED_PAD src0_sel:DWORD src1_sel:WORD_1
v_mfma_f32_16x16x32_bf16 v[56:59],a[220:223],a[4:7],v[56:59]
ds_read_u16 v25,v25
v_lshlrev_b32_sdwa v62,1,v62 dst_sel:DWORD dst_unused:UNUSED_PAD src0_sel:DWORD src1_sel:WORD_1
v_mfma_f32_16x16x32_bf16 v[232:235],a[240:243],v[48:51],v[232:235]
ds_read_u16_d16_hi v27,v27
v_lshlrev_b32_sdwa v61,1,v61 dst_sel:DWORD dst_unused:UNUSED_PAD src0_sel:DWORD src1_sel:WORD_1
v_mfma_f32_16x16x32_bf16 v[236:239],a[240:243],v[44:47],v[236:239]
ds_read_u16 v60,v60
v_lshlrev_b32_sdwa v63,1,v63 dst_sel:DWORD dst_unused:UNUSED_PAD src0_sel:DWORD src1_sel:WORD_1
v_mfma_f32_16x16x32_bf16 v[164:167],a[224:227],v[44:47],v[164:167]
ds_read_u16_d16_hi v62,v62
v_lshlrev_b32_sdwa v32,1,v32 dst_sel:DWORD dst_unused:UNUSED_PAD src0_sel:DWORD src1_sel:WORD_1
v_mfma_f32_16x16x32_bf16 v[160:163],a[224:227],v[48:51],v[160:163]
TP7013_%=:
s_cbranch_execz TR0_%=
ds_read_u16 v61,v61
v_lshlrev_b32_sdwa v34,1,v34 dst_sel:DWORD dst_unused:UNUSED_PAD src0_sel:DWORD src1_sel:WORD_1
v_mfma_f32_16x16x32_bf16 v[240:243],a[248:251],v[48:51],v[240:243]
ds_read_u16_d16_hi v63,v63
v_lshlrev_b32_sdwa v33,1,v33 dst_sel:DWORD dst_unused:UNUSED_PAD src0_sel:DWORD src1_sel:WORD_1
v_mfma_f32_16x16x32_bf16 v[244:247],a[248:251],v[44:47],v[244:247]
ds_read_u16 v32,v32
v_lshlrev_b32_sdwa v35,1,v35 dst_sel:DWORD dst_unused:UNUSED_PAD src0_sel:DWORD src1_sel:WORD_1
v_mfma_f32_16x16x32_bf16 v[180:183],a[228:231],v[44:47],v[180:183]
s_nop 0
ds_read_u16_d16_hi v34,v34
v_mfma_f32_16x16x32_bf16 v[176:179],a[228:231],v[48:51],v[176:179]
s_waitcnt lgkmcnt(8)
ds_read_u16 v33,v33
v_mfma_f32_16x16x32_bf16 v[248:251],a[232:235],v[48:51],v[248:251]
ds_read_u16_d16_hi v35,v35
v_mfma_f32_16x16x32_bf16 v[252:255],a[232:235],v[44:47],v[252:255]
v_mfma_f32_16x16x32_bf16 v[196:199],a[236:239],v[44:47],v[196:199]
v_mfma_f32_16x16x32_bf16 v[192:195],a[236:239],v[48:51],v[192:195]
v_mfma_f32_16x16x32_bf16 v[44:47],a[220:223],a[8:11],v[16:19]
v_mul_f32_dpp v232,v14,v232 row_newbcast:0 row_mask:0xf bank_mask:0xf
s_add_u32 s10,s61,1572864
v_mul_f32_dpp v233,v14,v233 row_newbcast:1 row_mask:0xf bank_mask:0xf
v_mul_f32_dpp v234,v14,v234 row_newbcast:2 row_mask:0xf bank_mask:0xf
v_lshlrev_b32_sdwa v36,1,v36 dst_sel:DWORD dst_unused:UNUSED_PAD src0_sel:DWORD src1_sel:WORD_1
v_mfma_f32_16x16x32_bf16 v[44:47],a[216:219],a[24:27],v[44:47]
s_nop 0
ds_read_u16 v36,v36
v_lshl_add_u64 v[28:29],v[28:29],0,v[30:31]
v_mul_f32_dpp v235,v14,v235 row_newbcast:3 row_mask:0xf bank_mask:0xf
v_mul_f32_dpp v160,v14,v160 row_newbcast:4 row_mask:0xf bank_mask:0xf
v_mul_f32_dpp v161,v14,v161 row_newbcast:5 row_mask:0xf bank_mask:0xf
v_mfma_f32_16x16x32_bf16 v[44:47],a[192:195],a[40:43],v[44:47]
s_waitcnt lgkmcnt(4)
v_lshl_add_u64 v[30:31],v[24:25],0,v[26:27]
v_mul_f32_dpp v162,v14,v162 row_newbcast:6 row_mask:0xf bank_mask:0xf
v_mul_f32_dpp v163,v14,v163 row_newbcast:7 row_mask:0xf bank_mask:0xf
v_lshlrev_b32_sdwa v38,1,v38 dst_sel:DWORD dst_unused:UNUSED_PAD src0_sel:DWORD src1_sel:WORD_1
v_cvt_pk_bf16_f32 v232,v232,v233
v_mfma_f32_16x16x32_bf16 v[48:51],a[196:199],a[40:43],v[20:23]
ds_read_u16_d16_hi v38,v38
v_cvt_pk_bf16_f32 v233,v234,v235
v_cvt_pk_bf16_f32 v234,v160,v161
v_lshlrev_b32_sdwa v37,1,v37 dst_sel:DWORD dst_unused:UNUSED_PAD src0_sel:DWORD src1_sel:WORD_1
v_cvt_pk_bf16_f32 v235,v162,v163
v_mfma_f32_16x16x32_bf16 v[48:51],a[200:203],a[24:27],v[48:51]
buffer_store_dwordx4 v[232:235],v7,s[40:43],s10 offen offset:0 sc1
v_lshl_add_u64 v[24:25],v[60:61],0,v[62:63]
v_mul_f32_dpp v240,v14,v240 row_newbcast:8 row_mask:0xf bank_mask:0xf
v_mul_f32_dpp v241,v14,v241 row_newbcast:9 row_mask:0xf bank_mask:0xf
v_mul_f32_dpp v242,v14,v242 row_newbcast:10 row_mask:0xf bank_mask:0xf
v_mfma_f32_16x16x32_bf16 v[48:51],a[212:215],a[8:11],v[48:51]
ds_read_u16 v37,v37
v_mul_f32_dpp v243,v14,v243 row_newbcast:11 row_mask:0xf bank_mask:0xf
v_mul_f32_dpp v176,v14,v176 row_newbcast:12 row_mask:0xf bank_mask:0xf
v_lshlrev_b32_sdwa v39,1,v39 dst_sel:DWORD dst_unused:UNUSED_PAD src0_sel:DWORD src1_sel:WORD_1
v_mul_f32_dpp v177,v14,v177 row_newbcast:13 row_mask:0xf bank_mask:0xf
v_mfma_f32_16x16x32_bf16 v[60:63],a[212:215],a[12:15],v[20:23]
s_nop 0
ds_read_b128 v[20:23],v4 offset:640
v_mul_f32_dpp v178,v14,v178 row_newbcast:14 row_mask:0xf bank_mask:0xf
v_mul_f32_dpp v179,v14,v179 row_newbcast:15 row_mask:0xf bank_mask:0xf
v_lshlrev_b32_sdwa v40,1,v40 dst_sel:DWORD dst_unused:UNUSED_PAD src0_sel:DWORD src1_sel:WORD_1
v_cvt_pk_bf16_f32 v240,v240,v241
v_mfma_f32_16x16x32_bf16 v[60:63],a[200:203],a[28:31],v[60:63]
s_waitcnt lgkmcnt(4)
ds_read_u16_d16_hi v39,v39
ds_read_b64_tr_b16 a[212:213],v1 offset:4096
v_lshl_add_u64 v[26:27],v[32:33],0,v[34:35]
v_cvt_pk_bf16_f32 v241,v242,v243
v_cvt_pk_bf16_f32 v242,v176,v177
v_cvt_pk_bf16_f32 v243,v178,v179
v_lshlrev_b32_sdwa v42,1,v42 dst_sel:DWORD dst_unused:UNUSED_PAD src0_sel:DWORD src1_sel:WORD_1
v_mfma_f32_16x16x32_bf16 v[60:63],a[196:199],a[44:47],v[60:63]
ds_read_b64_tr_b16 a[214:215],v1 offset:5120
ds_read_u16 v40,v40
v_mul_f32_dpp v248,v15,v248 row_newbcast:0 row_mask:0xf bank_mask:0xf
v_mul_f32_dpp v249,v15,v249 row_newbcast:1 row_mask:0xf bank_mask:0xf
v_mul_f32_dpp v250,v15,v250 row_newbcast:2 row_mask:0xf bank_mask:0xf
v_mfma_f32_16x16x32_bf16 v[32:35],a[192:195],a[44:47],v[16:19]
ds_read_b128 v[16:19],v4 offset:672
ds_read_u16_d16_hi v42,v42
v_mul_f32_dpp v251,v15,v251 row_newbcast:3 row_mask:0xf bank_mask:0xf
v_mul_f32_dpp v192,v15,v192 row_newbcast:4 row_mask:0xf bank_mask:0xf
v_lshlrev_b32_sdwa v41,1,v41 dst_sel:DWORD dst_unused:UNUSED_PAD src0_sel:DWORD src1_sel:WORD_1
v_mul_f32_dpp v193,v15,v193 row_newbcast:5 row_mask:0xf bank_mask:0xf
v_mfma_f32_16x16x32_bf16 v[32:35],a[216:219],a[28:31],v[32:35]
ds_read_u16 v41,v41
ds_read_b64_tr_b16 a[200:201],v2 offset:4096
v_mul_f32_dpp v194,v15,v194 row_newbcast:6 row_mask:0xf bank_mask:0xf
v_mul_f32_dpp v195,v15,v195 row_newbcast:7 row_mask:0xf bank_mask:0xf
v_lshlrev_b32_sdwa v43,1,v43 dst_sel:DWORD dst_unused:UNUSED_PAD src0_sel:DWORD src1_sel:WORD_1
v_cvt_pk_bf16_f32 v248,v248,v249
v_mfma_f32_16x16x32_bf16 v[32:35],a[220:223],a[12:15],v[32:35]
ds_read_u16_d16_hi v43,v43
ds_read_b64_tr_b16 a[202:203],v2 offset:5120
v_cvt_pk_bf16_f32 v249,v250,v251
v_cvt_pk_bf16_f32 v250,v192,v193
v_cvt_pk_bf16_f32 v251,v194,v195
v_mfma_f32_16x16x32_bf16 v[208:211],a[240:243],v[28:31],v[208:211]
ds_read_b64_tr_b16 a[196:197],v2 offset:2048
v_mul_f32_dpp v236,v14,v236 row_newbcast:0 row_mask:0xf bank_mask:0xf
v_mul_f32_dpp v237,v14,v237 row_newbcast:1 row_mask:0xf bank_mask:0xf
v_lshlrev_b32_sdwa v56,1,v56 dst_sel:DWORD dst_unused:UNUSED_PAD src0_sel:DWORD src1_sel:WORD_1
v_mul_f32_dpp v238,v14,v238 row_newbcast:2 row_mask:0xf bank_mask:0xf
v_mfma_f32_16x16x32_bf16 v[212:215],a[240:243],v[24:27],v[212:215]
ds_read_b64_tr_b16 a[198:199],v2 offset:3072
v_mul_f32_dpp v239,v14,v239 row_newbcast:3 row_mask:0xf bank_mask:0xf
v_mul_f32_dpp v164,v14,v164 row_newbcast:4 row_mask:0xf bank_mask:0xf
v_lshlrev_b32_sdwa v58,1,v58 dst_sel:DWORD dst_unused:UNUSED_PAD src0_sel:DWORD src1_sel:WORD_1
v_mul_f32_dpp v165,v14,v165 row_newbcast:5 row_mask:0xf bank_mask:0xf
v_mfma_f32_16x16x32_bf16 v[172:175],a[224:227],v[24:27],v[172:175]
ds_read_u16 v56,v56
v_mul_f32_dpp v166,v14,v166 row_newbcast:6 row_mask:0xf bank_mask:0xf
v_mul_f32_dpp v167,v14,v167 row_newbcast:7 row_mask:0xf bank_mask:0xf
v_cvt_pk_bf16_f32 v236,v236,v237
v_lshlrev_b32_sdwa v57,1,v57 dst_sel:DWORD dst_unused:UNUSED_PAD src0_sel:DWORD src1_sel:WORD_1
v_mfma_f32_16x16x32_bf16 v[168:171],a[224:227],v[28:31],v[168:171]
buffer_store_dwordx4 v[240:243],v7,s[40:43],s10 offen offset:64 sc1
ds_read_u16_d16_hi v58,v58
v_cvt_pk_bf16_f32 v237,v238,v239
v_cvt_pk_bf16_f32 v238,v164,v165
v_cvt_pk_bf16_f32 v239,v166,v167
v_mfma_f32_16x16x32_bf16 v[216:219],a[248:251],v[28:31],v[216:219]
ds_read_u16 v57,v57
v_mul_f32_dpp v244,v14,v244 row_newbcast:8 row_mask:0xf bank_mask:0xf
v_mul_f32_dpp v245,v14,v245 row_newbcast:9 row_mask:0xf bank_mask:0xf
v_lshlrev_b32_sdwa v59,1,v59 dst_sel:DWORD dst_unused:UNUSED_PAD src0_sel:DWORD src1_sel:WORD_1
v_mul_f32_dpp v246,v14,v246 row_newbcast:10 row_mask:0xf bank_mask:0xf
v_mfma_f32_16x16x32_bf16 v[220:223],a[248:251],v[24:27],v[220:223]
ds_read_u16_d16_hi v59,v59
v_mul_f32_dpp v247,v14,v247 row_newbcast:11 row_mask:0xf bank_mask:0xf
v_mul_f32_dpp v180,v14,v180 row_newbcast:12 row_mask:0xf bank_mask:0xf
v_lshlrev_b32_sdwa v52,1,v52 dst_sel:DWORD dst_unused:UNUSED_PAD src0_sel:DWORD src1_sel:WORD_1
v_mul_f32_dpp v181,v14,v181 row_newbcast:13 row_mask:0xf bank_mask:0xf
v_mfma_f32_16x16x32_bf16 v[188:191],a[228:231],v[24:27],v[188:191]
ds_read_b64_tr_b16 a[192:193],v2 offset:0
v_mul_f32_dpp v182,v14,v182 row_newbcast:14 row_mask:0xf bank_mask:0xf
v_mul_f32_dpp v183,v14,v183 row_newbcast:15 row_mask:0xf bank_mask:0xf
v_cvt_pk_bf16_f32 v244,v244,v245
v_mfma_f32_16x16x32_bf16 v[184:187],a[228:231],v[28:31],v[184:187]
ds_read_b64_tr_b16 a[194:195],v2 offset:1024
v_cvt_pk_bf16_f32 v245,v246,v247
v_cvt_pk_bf16_f32 v246,v180,v181
v_lshlrev_b32_sdwa v54,1,v54 dst_sel:DWORD dst_unused:UNUSED_PAD src0_sel:DWORD src1_sel:WORD_1
v_cvt_pk_bf16_f32 v247,v182,v183
v_mfma_f32_16x16x32_bf16 v[224:227],a[232:235],v[28:31],v[224:227]
ds_read_u16 v52,v52
v_mul_f32_dpp v252,v15,v252 row_newbcast:0 row_mask:0xf bank_mask:0xf
v_mul_f32_dpp v253,v15,v253 row_newbcast:1 row_mask:0xf bank_mask:0xf
v_lshlrev_b32_sdwa v53,1,v53 dst_sel:DWORD dst_unused:UNUSED_PAD src0_sel:DWORD src1_sel:WORD_1
v_mul_f32_dpp v254,v15,v254 row_newbcast:2 row_mask:0xf bank_mask:0xf
v_mfma_f32_16x16x32_bf16 v[228:231],a[232:235],v[24:27],v[228:231]
ds_read_u16_d16_hi v54,v54
v_mul_f32_dpp v255,v15,v255 row_newbcast:3 row_mask:0xf bank_mask:0xf
v_mul_f32_dpp v196,v15,v196 row_newbcast:4 row_mask:0xf bank_mask:0xf
v_mul_f32_dpp v197,v15,v197 row_newbcast:5 row_mask:0xf bank_mask:0xf
v_lshlrev_b32_sdwa v55,1,v55 dst_sel:DWORD dst_unused:UNUSED_PAD src0_sel:DWORD src1_sel:WORD_1
v_mfma_f32_16x16x32_bf16 v[204:207],a[236:239],v[24:27],v[204:207]
s_nop 0
ds_read_u16 v53,v53
v_mul_f32_dpp v198,v15,v198 row_newbcast:6 row_mask:0xf bank_mask:0xf
v_mul_f32_dpp v199,v15,v199 row_newbcast:7 row_mask:0xf bank_mask:0xf
v_cvt_pk_bf16_f32 v252,v252,v253
v_mfma_f32_16x16x32_bf16 v[200:203],a[236:239],v[28:31],v[200:203]
s_waitcnt lgkmcnt(8)
ds_read_u16_d16_hi v55,v55
v_cvt_pk_bf16_f32 v253,v254,v255
v_cvt_pk_bf16_f32 v254,v196,v197
v_cvt_pk_bf16_f32 v255,v198,v199
v_mfma_f32_16x16x32_bf16 v[24:27],a[204:207],a[0:3],v[20:23]
v_mul_f32_dpp v208,v14,v208 row_newbcast:0 row_mask:0xf bank_mask:0xf
s_add_u32 s11,s61,1671168
v_accvgpr_write_b32 a64,0
v_accvgpr_write_b32 a65,0
v_lshlrev_b32_sdwa v44,1,v44 dst_sel:DWORD dst_unused:UNUSED_PAD src0_sel:DWORD src1_sel:WORD_1
v_mfma_f32_16x16x32_bf16 v[24:27],a[208:211],a[16:19],v[24:27]
ds_read_u16 v44,v44
v_lshl_add_u64 v[36:37],v[36:37],0,v[38:39]
v_accvgpr_write_b32 a66,0
v_accvgpr_write_b32 a67,0
v_mul_f32_dpp v209,v14,v209 row_newbcast:1 row_mask:0xf bank_mask:0xf
v_mfma_f32_16x16x32_bf16 v[24:27],a[212:215],a[32:35],v[24:27]
v_lshl_add_u64 v[38:39],v[40:41],0,v[42:43]
v_mul_f32_dpp v210,v14,v210 row_newbcast:2 row_mask:0xf bank_mask:0xf
v_mul_f32_dpp v211,v14,v211 row_newbcast:3 row_mask:0xf bank_mask:0xf
v_lshlrev_b32_sdwa v46,1,v46 dst_sel:DWORD dst_unused:UNUSED_PAD src0_sel:DWORD src1_sel:WORD_1
v_mul_f32_dpp v168,v14,v168 row_newbcast:4 row_mask:0xf bank_mask:0xf
v_mfma_f32_16x16x32_bf16 v[28:31],a[200:203],a[32:35],v[16:19]
s_nop 0
buffer_store_dwordx4 v[248:251],v7,s[40:43],s10 offen offset:128 sc1
v_mul_f32_dpp v169,v14,v169 row_newbcast:5 row_mask:0xf bank_mask:0xf
v_mul_f32_dpp v170,v14,v170 row_newbcast:6 row_mask:0xf bank_mask:0xf
v_lshlrev_b32_sdwa v45,1,v45 dst_sel:DWORD dst_unused:UNUSED_PAD src0_sel:DWORD src1_sel:WORD_1
v_mul_f32_dpp v171,v14,v171 row_newbcast:7 row_mask:0xf bank_mask:0xf
v_mfma_f32_16x16x32_bf16 v[28:31],a[196:199],a[16:19],v[28:31]
s_waitcnt lgkmcnt(5)
ds_read_u16_d16_hi v46,v46
v_lshl_add_u64 v[40:41],v[56:57],0,v[58:59]
v_cvt_pk_bf16_f32 v208,v208,v209
v_cvt_pk_bf16_f32 v209,v210,v211
v_cvt_pk_bf16_f32 v210,v168,v169
v_mfma_f32_16x16x32_bf16 v[28:31],a[192:195],a[0:3],v[28:31]
ds_read_u16 v45,v45
v_cvt_pk_bf16_f32 v211,v170,v171
v_mul_f32_dpp v216,v14,v216 row_newbcast:8 row_mask:0xf bank_mask:0xf
v_lshlrev_b32_sdwa v47,1,v47 dst_sel:DWORD dst_unused:UNUSED_PAD src0_sel:DWORD src1_sel:WORD_1
v_accvgpr_write_b32 a72,v208
v_mfma_f32_16x16x32_bf16 v[56:59],a[192:195],a[4:7],v[16:19]
s_nop 0
ds_read_u16_d16_hi v47,v47
v_mul_f32_dpp v217,v14,v217 row_newbcast:9 row_mask:0xf bank_mask:0xf
v_mul_f32_dpp v218,v14,v218 row_newbcast:10 row_mask:0xf bank_mask:0xf
v_lshlrev_b32_sdwa v48,1,v48 dst_sel:DWORD dst_unused:UNUSED_PAD src0_sel:DWORD src1_sel:WORD_1
v_accvgpr_write_b32 a73,v209
v_mfma_f32_16x16x32_bf16 v[56:59],a[196:199],a[20:23],v[56:59]
s_waitcnt lgkmcnt(4)
ds_read_u16 v48,v48
v_lshl_add_u64 v[42:43],v[52:53],0,v[54:55]
v_mul_f32_dpp v219,v14,v219 row_newbcast:11 row_mask:0xf bank_mask:0xf
v_mul_f32_dpp v184,v14,v184 row_newbcast:12 row_mask:0xf bank_mask:0xf
v_accvgpr_write_b32 a74,v210
v_lshlrev_b32_sdwa v50,1,v50 dst_sel:DWORD dst_unused:UNUSED_PAD src0_sel:DWORD src1_sel:WORD_1
v_mfma_f32_16x16x32_bf16 v[56:59],a[200:203],a[36:39],v[56:59]
s_waitcnt vmcnt(9)
s_barrier
ds_read_u16_d16_hi v50,v50
v_mul_f32_dpp v185,v14,v185 row_newbcast:13 row_mask:0xf bank_mask:0xf
v_mul_f32_dpp v186,v14,v186 row_newbcast:14 row_mask:0xf bank_mask:0xf
v_accvgpr_write_b32 a75,v211
v_mfma_f32_16x16x32_bf16 v[52:55],a[212:215],a[36:39],v[20:23]
ds_read_b64_tr_b16 a[244:245],v3 offset:6160
v_mul_f32_dpp v187,v14,v187 row_newbcast:15 row_mask:0xf bank_mask:0xf
v_cvt_pk_bf16_f32 v216,v216,v217
v_lshlrev_b32_sdwa v49,1,v49 dst_sel:DWORD dst_unused:UNUSED_PAD src0_sel:DWORD src1_sel:WORD_1
v_cvt_pk_bf16_f32 v217,v218,v219
v_mfma_f32_16x16x32_bf16 v[52:55],a[208:211],a[20:23],v[52:55]
s_add_u32 m0,s58,0
ds_read_u16 v49,v49
v_cvt_pk_bf16_f32 v218,v184,v185
v_cvt_pk_bf16_f32 v219,v186,v187
v_lshlrev_b32_sdwa v51,1,v51 dst_sel:DWORD dst_unused:UNUSED_PAD src0_sel:DWORD src1_sel:WORD_1
v_mul_f32_dpp v224,v15,v224 row_newbcast:0 row_mask:0xf bank_mask:0xf
v_mfma_f32_16x16x32_bf16 v[52:55],a[204:207],a[4:7],v[52:55]
s_add_u32 s69,s69,s59
ds_read_u16_d16_hi v51,v51
v_accvgpr_write_b32 a76,v216
v_mul_f32_dpp v225,v15,v225 row_newbcast:1 row_mask:0xf bank_mask:0xf
v_mul_f32_dpp v226,v15,v226 row_newbcast:2 row_mask:0xf bank_mask:0xf
v_mfma_f32_16x16x32_bf16 v[136:139],a[240:243],v[36:39],v[136:139]
s_nop 0
buffer_load_dwordx4 v5,s[44:47],s69 offen offset:0 lds
v_accvgpr_write_b32 a77,v217
v_mul_f32_dpp v227,v15,v227 row_newbcast:3 row_mask:0xf bank_mask:0xf
v_lshlrev_b32_sdwa v32,1,v32 dst_sel:DWORD dst_unused:UNUSED_PAD src0_sel:DWORD src1_sel:WORD_1
v_mul_f32_dpp v200,v15,v200 row_newbcast:4 row_mask:0xf bank_mask:0xf
v_mfma_f32_16x16x32_bf16 v[140:143],a[240:243],v[40:43],v[140:143]
s_add_u32 s69,s69,s53
ds_read_u16 v32,v32
v_accvgpr_write_b32 a78,v218
v_mul_f32_dpp v201,v15,v201 row_newbcast:5 row_mask:0xf bank_mask:0xf
v_lshlrev_b32_sdwa v34,1,v34 dst_sel:DWORD dst_unused:UNUSED_PAD src0_sel:DWORD src1_sel:WORD_1
v_mul_f32_dpp v202,v15,v202 row_newbcast:6 row_mask:0xf bank_mask:0xf
v_mfma_f32_16x16x32_bf16 v[68:71],a[224:227],v[40:43],v[68:71]
buffer_load_dwordx4 v5,s[44:47],s69 offen offset:1024 lds
ds_read_u16_d16_hi v34,v34
v_accvgpr_write_b32 a79,v219
v_mul_f32_dpp v203,v15,v203 row_newbcast:7 row_mask:0xf bank_mask:0xf
v_cvt_pk_bf16_f32 v224,v224,v225
v_lshlrev_b32_sdwa v33,1,v33 dst_sel:DWORD dst_unused:UNUSED_PAD src0_sel:DWORD src1_sel:WORD_1
v_mfma_f32_16x16x32_bf16 v[64:67],a[224:227],v[36:39],v[64:67]
s_nop 0
buffer_store_dwordx4 v[236:239],v7,s[40:43],s10 offen offset:192 sc1
ds_read_u16 v33,v33
v_cvt_pk_bf16_f32 v225,v226,v227
v_cvt_pk_bf16_f32 v226,v200,v201
v_cvt_pk_bf16_f32 v227,v202,v203
v_mfma_f32_16x16x32_bf16 v[144:147],a[248:251],v[36:39],v[144:147]
s_add_u32 s69,s69,s53
ds_read_b64_tr_b16 a[246:247],v3 offset:7696
v_mul_f32_dpp v212,v14,v212 row_newbcast:0 row_mask:0xf bank_mask:0xf
v_accvgpr_write_b32 a80,v224
v_lshlrev_b32_sdwa v35,1,v35 dst_sel:DWORD dst_unused:UNUSED_PAD src0_sel:DWORD src1_sel:WORD_1
v_mul_f32_dpp v213,v14,v213 row_newbcast:1 row_mask:0xf bank_mask:0xf
v_mfma_f32_16x16x32_bf16 v[148:151],a[248:251],v[40:43],v[148:151]
buffer_load_dwordx4 v5,s[44:47],s69 offen offset:2048 lds
v_mul_f32_dpp v214,v14,v214 row_newbcast:2 row_mask:0xf bank_mask:0xf
v_accvgpr_write_b32 a81,v225
v_lshlrev_b32_sdwa v60,1,v60 dst_sel:DWORD dst_unused:UNUSED_PAD src0_sel:DWORD src1_sel:WORD_1
v_mul_f32_dpp v215,v14,v215 row_newbcast:3 row_mask:0xf bank_mask:0xf
v_mfma_f32_16x16x32_bf16 v[84:87],a[228:231],v[40:43],v[84:87]
ds_read_u16_d16_hi v35,v35
v_mul_f32_dpp v172,v14,v172 row_newbcast:4 row_mask:0xf bank_mask:0xf
v_accvgpr_write_b32 a82,v226
v_mul_f32_dpp v173,v14,v173 row_newbcast:5 row_mask:0xf bank_mask:0xf
v_mfma_f32_16x16x32_bf16 v[80:83],a[228:231],v[36:39],v[80:83]
ds_read_u16 v60,v60
v_mul_f32_dpp v174,v14,v174 row_newbcast:6 row_mask:0xf bank_mask:0xf
v_accvgpr_write_b32 a83,v227
v_lshlrev_b32_sdwa v62,1,v62 dst_sel:DWORD dst_unused:UNUSED_PAD src0_sel:DWORD src1_sel:WORD_1
v_mul_f32_dpp v175,v14,v175 row_newbcast:7 row_mask:0xf bank_mask:0xf
v_mfma_f32_16x16x32_bf16 v[152:155],a[232:235],v[36:39],v[152:155]
ds_read_u16_d16_hi v62,v62
v_cvt_pk_bf16_f32 v212,v212,v213
v_cvt_pk_bf16_f32 v213,v214,v215
v_lshlrev_b32_sdwa v61,1,v61 dst_sel:DWORD dst_unused:UNUSED_PAD src0_sel:DWORD src1_sel:WORD_1
v_cvt_pk_bf16_f32 v214,v172,v173
v_mfma_f32_16x16x32_bf16 v[156:159],a[232:235],v[40:43],v[156:159]
ds_read_u16 v61,v61
v_cvt_pk_bf16_f32 v215,v174,v175
v_mfma_f32_32x32x16_bf16 v[160:175],a[64:67],a[64:67],0
v_mul_f32_dpp v220,v14,v220 row_newbcast:8 row_mask:0xf bank_mask:0xf
v_lshlrev_b32_sdwa v63,1,v63 dst_sel:DWORD dst_unused:UNUSED_PAD src0_sel:DWORD src1_sel:WORD_1
v_accvgpr_write_b32 a84,v212
v_mul_f32_dpp v221,v14,v221 row_newbcast:9 row_mask:0xf bank_mask:0xf
v_mul_f32_dpp v222,v14,v222 row_newbcast:10 row_mask:0xf bank_mask:0xf
v_mfma_f32_16x16x32_bf16 v[100:103],a[236:239],v[40:43],v[100:103]
ds_read_u16_d16_hi v63,v63
v_accvgpr_write_b32 a85,v213
v_mfma_f32_16x16x32_bf16 v[96:99],a[236:239],v[36:39],v[96:99]
ds_read_b64_tr_b16 a[252:253],v3 offset:6168
v_mul_f32_dpp v223,v14,v223 row_newbcast:11 row_mask:0xf bank_mask:0xf
v_mul_f32_dpp v188,v14,v188 row_newbcast:12 row_mask:0xf bank_mask:0xf
ds_read_b64_tr_b16 a[254:255],v3 offset:7704
v_add_u32_sdwa v36,s60,v10 dst_sel:DWORD dst_unused:UNUSED_PAD src0_sel:DWORD src1_sel:WORD_1
v_subrev_u32 v8,88400,v36
v_min_u32_e64 v36,v36,v8
v_add_u32 v37,7072,v36
v_subrev_u32 v8,88400,v37
v_min_u32_e64 v37,v37,v8
s_waitcnt lgkmcnt(15)
v_lshl_add_u64 v[44:45],v[44:45],0,v[46:47]
s_waitcnt lgkmcnt(11)
v_lshl_add_u64 v[46:47],v[48:49],0,v[50:51]
s_waitcnt lgkmcnt(6)
v_lshl_add_u64 v[48:49],v[32:33],0,v[34:35]
s_waitcnt lgkmcnt(2)
v_lshl_add_u64 v[50:51],v[60:61],0,v[62:63]
v_mfma_f32_16x16x32_bf16 v[112:115],a[240:243],v[44:47],v[112:115]
v_xad_u32 v40,v11,0,v36
v_xad_u32 v41,v11,8,v37
v_xad_u32 v42,v11,16,v36
v_xad_u32 v43,v11,24,v37
v_mfma_f32_16x16x32_bf16 v[116:119],a[240:243],v[48:51],v[116:119]
ds_read_b64 a[48:49],v40 offset:0
v_accvgpr_write_b32 a86,v214
v_mul_f32_dpp v189,v14,v189 row_newbcast:13 row_mask:0xf bank_mask:0xf
v_mul_f32_dpp v190,v14,v190 row_newbcast:14 row_mask:0xf bank_mask:0xf
v_accvgpr_write_b32 a87,v215
v_mfma_f32_16x16x32_bf16 v[76:79],a[224:227],v[48:51],v[76:79]
buffer_store_dwordx4 v[244:247],v7,s[40:43],s10 offen offset:256 sc1
ds_read_b64 a[50:51],v40 offset:192
v_mul_f32_dpp v191,v14,v191 row_newbcast:15 row_mask:0xf bank_mask:0xf
v_cvt_pk_bf16_f32 v220,v220,v221
v_cvt_pk_bf16_f32 v221,v222,v223
v_mfma_f32_16x16x32_bf16 v[72:75],a[224:227],v[44:47],v[72:75]
ds_read_b64 a[52:53],v41 offset:0
v_cvt_pk_bf16_f32 v222,v188,v189
v_cvt_pk_bf16_f32 v223,v190,v191
v_mfma_f32_32x32x16_bf16 v[176:191],a[64:67],a[64:67],0
v_mfma_f32_16x16x32_bf16 v[120:123],a[248:251],v[44:47],v[120:123]
ds_read_b64 a[54:55],v41 offset:192
v_mul_f32_dpp v228,v15,v228 row_newbcast:0 row_mask:0xf bank_mask:0xf
v_accvgpr_write_b32 a88,v220
v_mul_f32_dpp v229,v15,v229 row_newbcast:1 row_mask:0xf bank_mask:0xf
v_mfma_f32_16x16x32_bf16 v[124:127],a[248:251],v[48:51],v[124:127]
ds_read_b64 a[56:57],v40 offset:8
v_mul_f32_dpp v230,v15,v230 row_newbcast:2 row_mask:0xf bank_mask:0xf
v_accvgpr_write_b32 a89,v221
v_mul_f32_dpp v231,v15,v231 row_newbcast:3 row_mask:0xf bank_mask:0xf
v_mfma_f32_16x16x32_bf16 v[92:95],a[228:231],v[48:51],v[92:95]
ds_read_b64 a[58:59],v40 offset:200
v_mul_f32_dpp v204,v15,v204 row_newbcast:4 row_mask:0xf bank_mask:0xf
v_accvgpr_write_b32 a90,v222
v_mul_f32_dpp v205,v15,v205 row_newbcast:5 row_mask:0xf bank_mask:0xf
v_mfma_f32_16x16x32_bf16 v[88:91],a[228:231],v[44:47],v[88:91]
ds_read_b64 a[60:61],v41 offset:8
v_mul_f32_dpp v206,v15,v206 row_newbcast:6 row_mask:0xf bank_mask:0xf
v_accvgpr_write_b32 a91,v223
v_mul_f32_dpp v207,v15,v207 row_newbcast:7 row_mask:0xf bank_mask:0xf
v_mfma_f32_16x16x32_bf16 v[128:131],a[232:235],v[44:47],v[128:131]
ds_read_b64 a[62:63],v41 offset:200
v_cvt_pk_bf16_f32 v228,v228,v229
v_cvt_pk_bf16_f32 v229,v230,v231
v_cvt_pk_bf16_f32 v230,v204,v205
v_mfma_f32_16x16x32_bf16 v[132:135],a[232:235],v[48:51],v[132:135]
v_cvt_pk_bf16_f32 v231,v206,v207
v_mfma_f32_32x32x16_bf16 v[192:207],a[64:67],a[64:67],0
v_accvgpr_write_b32 a92,v228
v_mfma_f32_16x16x32_bf16 v[108:111],a[236:239],v[48:51],v[108:111]
v_accvgpr_write_b32 a93,v229
v_accvgpr_write_b32 a94,v230
v_accvgpr_write_b32 a95,v231
v_mfma_f32_16x16x32_bf16 v[104:107],a[236:239],v[44:47],v[104:107]
v_mfma_f32_32x32x16_bf16 v[208:223],a[64:67],a[64:67],0
v_mov_b64 v[32:33],0
v_mov_b64 v[34:35],0
ds_read_b64 a[64:65],v42 offset:0
ds_read_b64 a[66:67],v42 offset:192
ds_read_b64 a[68:69],v43 offset:0
ds_read_b64 a[70:71],v43 offset:192
s_waitcnt lgkmcnt(8)
v_smfmac_f32_16x16x64_bf16 v[160:163],a[96:99],a[48:55],v10
s_waitcnt lgkmcnt(4)
ds_read_b64 a[48:49],v42 offset:8
ds_read_b64 a[50:51],v42 offset:200
ds_read_b64 a[52:53],v43 offset:8
ds_read_b64 a[54:55],v43 offset:200
v_smfmac_f32_16x16x64_bf16 v[164:167],a[100:103],a[56:63],v10
s_waitcnt lgkmcnt(4)
ds_read_b64 a[56:57],v40 offset:64
ds_read_b64 a[58:59],v40 offset:256
ds_read_b64 a[60:61],v41 offset:64
ds_read_b64 a[62:63],v41 offset:256
v_smfmac_f32_16x16x64_bf16 v[168:171],a[104:107],a[64:71],v10
s_waitcnt lgkmcnt(4)
buffer_store_dwordx4 v[252:255],v7,s[40:43],s10 offen offset:320 sc1
ds_read_b64 a[64:65],v40 offset:72
ds_read_b64 a[66:67],v40 offset:264
ds_read_b64 a[68:69],v41 offset:72
ds_read_b64 a[70:71],v41 offset:264
v_and_b32 v44,8,v11
v_pk_mul_f32 v[48:49],v[160:161],v[160:161]
v_pk_mul_f32 v[50:51],v[162:163],v[162:163]
v_smfmac_f32_16x16x64_bf16 v[172:175],a[108:111],a[48:55],v10
s_waitcnt lgkmcnt(4)
ds_read_b64 a[48:49],v42 offset:64
ds_read_b64 a[50:51],v42 offset:256
ds_read_b64 a[52:53],v43 offset:64
ds_read_b64 a[54:55],v43 offset:256
v_mul_u32_u24 v44,884,v44
s_add_u32 s64,s60,3536
v_pk_add_f32 v[60:61],v[160:161],v[164:165]
v_pk_fma_f32 v[48:49],v[164:165],v[164:165],v[48:49]
v_pk_add_f32 v[62:63],v[162:163],v[166:167]
v_pk_fma_f32 v[50:51],v[166:167],v[166:167],v[50:51]
v_smfmac_f32_16x16x64_bf16 v[176:179],a[128:131],a[56:63],v10
s_waitcnt lgkmcnt(4)
ds_read_b64 a[56:57],v42 offset:72
ds_read_b64 a[58:59],v42 offset:264
ds_read_b64 a[60:61],v43 offset:72
ds_read_b64 a[62:63],v43 offset:264
v_add_u32 v47,s64,v44
v_pk_add_f32 v[60:61],v[60:61],v[168:169]
v_pk_fma_f32 v[48:49],v[168:169],v[168:169],v[48:49]
v_pk_add_f32 v[62:63],v[62:63],v[170:171]
v_pk_fma_f32 v[50:51],v[170:171],v[170:171],v[50:51]
v_smfmac_f32_16x16x64_bf16 v[180:183],a[132:135],a[64:71],v10
s_waitcnt lgkmcnt(4)
ds_read_b64 a[64:65],v40 offset:128
ds_read_b64 a[66:67],v40 offset:320
ds_read_b64 a[68:69],v41 offset:128
ds_read_b64 a[70:71],v41 offset:320
v_subrev_u32 v8,88400,v47
v_pk_add_f32 v[60:61],v[60:61],v[172:173]
v_pk_fma_f32 v[48:49],v[172:173],v[172:173],v[48:49]
v_pk_add_f32 v[62:63],v[62:63],v[174:175]
v_pk_fma_f32 v[50:51],v[174:175],v[174:175],v[50:51]
v_smfmac_f32_16x16x64_bf16 v[184:187],a[136:139],a[48:55],v10
s_waitcnt lgkmcnt(4)
ds_read_b64 a[48:49],v40 offset:136
ds_read_b64 a[50:51],v40 offset:328
ds_read_b64 a[52:53],v41 offset:136
ds_read_b64 a[54:55],v41 offset:328
v_min_u32 v47,v47,v8
v_pk_add_f32 v[60:61],v[60:61],v[176:177]
v_pk_fma_f32 v[48:49],v[176:177],v[176:177],v[48:49]
v_pk_add_f32 v[62:63],v[62:63],v[178:179]
v_pk_fma_f32 v[50:51],v[178:179],v[178:179],v[50:51]
v_smfmac_f32_16x16x64_bf16 v[188:191],a[140:143],a[56:63],v10
s_waitcnt lgkmcnt(4)
ds_read_b64 a[56:57],v42 offset:128
ds_read_b64 a[58:59],v42 offset:320
ds_read_b64 a[60:61],v43 offset:128
ds_read_b64 a[62:63],v43 offset:320
v_xad_u32 v40,v11,32,v36
v_xad_u32 v41,v11,40,v37
v_pk_add_f32 v[60:61],v[60:61],v[180:181]
v_pk_fma_f32 v[48:49],v[180:181],v[180:181],v[48:49]
v_pk_add_f32 v[62:63],v[62:63],v[182:183]
v_pk_fma_f32 v[50:51],v[182:183],v[182:183],v[50:51]
v_smfmac_f32_16x16x64_bf16 v[192:195],a[160:163],a[64:71],v10
s_waitcnt lgkmcnt(4)
ds_read_b64 a[64:65],v42 offset:136
ds_read_b64 a[66:67],v42 offset:328
ds_read_b64 a[68:69],v43 offset:136
ds_read_b64 a[70:71],v43 offset:328
v_add_u32 v46,3536,v47
v_pk_add_f32 v[60:61],v[60:61],v[184:185]
v_pk_fma_f32 v[48:49],v[184:185],v[184:185],v[48:49]
v_pk_add_f32 v[62:63],v[62:63],v[186:187]
v_pk_fma_f32 v[50:51],v[186:187],v[186:187],v[50:51]
v_smfmac_f32_16x16x64_bf16 v[196:199],a[164:167],a[48:55],v10
s_waitcnt lgkmcnt(4)
buffer_store_dwordx4 a[72:75],v7,s[40:43],s11 offen offset:0 sc1
ds_read_b64 a[48:49],v40 offset:0
ds_read_b64 a[50:51],v40 offset:192
ds_read_b64 a[52:53],v41 offset:0
ds_read_b64 a[54:55],v41 offset:192
v_xad_u32 v42,v11,48,v36
v_xad_u32 v43,v11,56,v37
v_pk_add_f32 v[60:61],v[60:61],v[188:189]
v_pk_fma_f32 v[48:49],v[188:189],v[188:189],v[48:49]
v_pk_add_f32 v[62:63],v[62:63],v[190:191]
v_pk_fma_f32 v[50:51],v[190:191],v[190:191],v[50:51]
v_smfmac_f32_16x16x64_bf16 v[200:203],a[168:171],a[56:63],v10
s_waitcnt lgkmcnt(4)
ds_read_b64 a[56:57],v40 offset:8
ds_read_b64 a[58:59],v40 offset:200
ds_read_b64 a[60:61],v41 offset:8
ds_read_b64 a[62:63],v41 offset:200
v_subrev_u32 v8,88400,v46
v_pk_add_f32 v[60:61],v[60:61],v[192:193]
v_pk_fma_f32 v[48:49],v[192:193],v[192:193],v[48:49]
v_pk_add_f32 v[62:63],v[62:63],v[194:195]
v_pk_fma_f32 v[50:51],v[194:195],v[194:195],v[50:51]
v_smfmac_f32_16x16x64_bf16 v[204:207],a[172:175],a[64:71],v10
s_waitcnt lgkmcnt(4)
ds_read_b64 a[64:65],v40 offset:64
ds_read_b64 a[66:67],v40 offset:256
ds_read_b64 a[68:69],v41 offset:64
ds_read_b64 a[70:71],v41 offset:256
v_min_u32 v46,v46,v8
v_pk_add_f32 v[60:61],v[60:61],v[196:197]
v_pk_fma_f32 v[48:49],v[196:197],v[196:197],v[48:49]
v_pk_add_f32 v[62:63],v[62:63],v[198:199]
v_pk_fma_f32 v[50:51],v[198:199],v[198:199],v[50:51]
v_smfmac_f32_16x16x64_bf16 v[208:211],a[112:115],a[48:55],v10
s_waitcnt lgkmcnt(4)
ds_read_b64 a[48:49],v40 offset:72
ds_read_b64 a[50:51],v40 offset:264
ds_read_b64 a[52:53],v41 offset:72
ds_read_b64 a[54:55],v41 offset:264
v_xor_b32 v9,0x80008,v12
v_pk_add_f32 v[60:61],v[60:61],v[200:201]
v_pk_fma_f32 v[48:49],v[200:201],v[200:201],v[48:49]
v_pk_add_f32 v[62:63],v[62:63],v[202:203]
v_pk_fma_f32 v[50:51],v[202:203],v[202:203],v[50:51]
v_mfma_f32_32x32x16_bf16 v[224:239],v[32:35],v[32:35],0
TE10_%=:
s_cbranch_execz TP9682_%=
v_smfmac_f32_16x16x64_bf16 v[212:215],a[116:119],a[56:63],v10
s_waitcnt lgkmcnt(4)
ds_read_b64 a[56:57],v40 offset:128
ds_read_b64 a[58:59],v40 offset:320
ds_read_b64 a[60:61],v41 offset:128
ds_read_b64 a[62:63],v41 offset:320
v_add_u32_sdwa v44,v12,v47 dst_sel:DWORD dst_unused:UNUSED_PAD src0_sel:WORD_0 src1_sel:DWORD
v_pk_add_f32 v[60:61],v[60:61],v[204:205]
v_pk_fma_f32 v[48:49],v[204:205],v[204:205],v[48:49]
v_pk_add_f32 v[62:63],v[62:63],v[206:207]
v_pk_fma_f32 v[50:51],v[206:207],v[206:207],v[50:51]
v_smfmac_f32_16x16x64_bf16 v[216:219],a[144:147],a[64:71],v10
s_waitcnt lgkmcnt(4)
ds_read_b64 a[64:65],v40 offset:136
ds_read_b64 a[66:67],v40 offset:328
ds_read_b64 a[68:69],v41 offset:136
ds_read_b64 a[70:71],v41 offset:328
v_add_u32_sdwa v45,v12,v47 dst_sel:DWORD dst_unused:UNUSED_PAD src0_sel:WORD_1 src1_sel:DWORD
v_pk_add_f32 v[60:61],v[60:61],v[208:209]
v_pk_fma_f32 v[48:49],v[208:209],v[208:209],v[48:49]
v_pk_add_f32 v[62:63],v[62:63],v[210:211]
v_pk_fma_f32 v[50:51],v[210:211],v[210:211],v[50:51]
v_smfmac_f32_16x16x64_bf16 v[220:223],a[148:151],a[48:55],v10
s_waitcnt lgkmcnt(4)
buffer_store_dwordx4 a[76:79],v7,s[40:43],s11 offen offset:64 sc1
ds_read_b64 a[48:49],v42 offset:0
ds_read_b64 a[50:51],v42 offset:192
ds_read_b64 a[52:53],v43 offset:0
ds_read_b64 a[54:55],v43 offset:192
v_add_u32_sdwa v47,v9,v46 dst_sel:DWORD dst_unused:UNUSED_PAD src0_sel:WORD_1 src1_sel:DWORD
v_pk_add_f32 v[60:61],v[60:61],v[212:213]
v_pk_fma_f32 v[48:49],v[212:213],v[212:213],v[48:49]
v_pk_add_f32 v[62:63],v[62:63],v[214:215]
v_pk_fma_f32 v[50:51],v[214:215],v[214:215],v[50:51]
v_smfmac_f32_16x16x64_bf16 v[224:227],a[176:179],a[56:63],v10
s_waitcnt lgkmcnt(4)
s_nop 0
ds_read_b64 a[56:57],v42 offset:8
ds_read_b64 a[58:59],v42 offset:200
ds_read_b64 a[60:61],v43 offset:8
ds_read_b64 a[62:63],v43 offset:200
v_add_u32_sdwa v46,v9,v46 dst_sel:DWORD dst_unused:UNUSED_PAD src0_sel:WORD_0 src1_sel:DWORD
v_pk_add_f32 v[60:61],v[60:61],v[216:217]
v_pk_fma_f32 v[48:49],v[216:217],v[216:217],v[48:49]
v_pk_add_f32 v[62:63],v[62:63],v[218:219]
v_pk_fma_f32 v[50:51],v[218:219],v[218:219],v[50:51]
v_mfma_f32_32x32x16_bf16 v[240:255],v[32:35],v[32:35],0
v_smfmac_f32_16x16x64_bf16 v[228:231],a[180:183],a[64:71],v10
s_waitcnt lgkmcnt(4)
ds_read_b64 a[64:65],v42 offset:64
ds_read_b64 a[66:67],v42 offset:256
ds_read_b64 a[68:69],v43 offset:64
ds_read_b64 a[70:71],v43 offset:256
v_pk_add_f32 v[60:61],v[60:61],v[220:221]
v_pk_fma_f32 v[48:49],v[220:221],v[220:221],v[48:49]
v_pk_add_f32 v[62:63],v[62:63],v[222:223]
v_pk_fma_f32 v[50:51],v[222:223],v[222:223],v[50:51]
v_smfmac_f32_16x16x64_bf16 v[232:235],a[120:123],a[48:55],v10
s_waitcnt lgkmcnt(4)
ds_read_b64 a[48:49],v42 offset:72
ds_read_b64 a[50:51],v42 offset:264
ds_read_b64 a[52:53],v43 offset:72
ds_read_b64 a[54:55],v43 offset:264
v_pk_add_f32 v[60:61],v[60:61],v[224:225]
v_pk_fma_f32 v[48:49],v[224:225],v[224:225],v[48:49]
v_pk_add_f32 v[62:63],v[62:63],v[226:227]
v_pk_fma_f32 v[50:51],v[226:227],v[226:227],v[50:51]
v_smfmac_f32_16x16x64_bf16 v[236:239],a[124:127],a[56:63],v10
s_waitcnt lgkmcnt(4)
ds_read_b64 a[56:57],v42 offset:128
ds_read_b64 a[58:59],v42 offset:320
ds_read_b64 a[60:61],v43 offset:128
ds_read_b64 a[62:63],v43 offset:320
v_pk_add_f32 v[60:61],v[60:61],v[228:229]
v_pk_fma_f32 v[48:49],v[228:229],v[228:229],v[48:49]
v_pk_add_f32 v[62:63],v[62:63],v[230:231]
v_pk_fma_f32 v[50:51],v[230:231],v[230:231],v[50:51]
v_smfmac_f32_16x16x64_bf16 v[240:243],a[152:155],a[64:71],v10
s_waitcnt lgkmcnt(4)
ds_read_b64 a[64:65],v42 offset:136
ds_read_b64 a[66:67],v42 offset:328
ds_read_b64 a[68:69],v43 offset:136
ds_read_b64 a[70:71],v43 offset:328
v_pk_add_f32 v[60:61],v[60:61],v[232:233]
v_pk_fma_f32 v[48:49],v[232:233],v[232:233],v[48:49]
v_pk_add_f32 v[62:63],v[62:63],v[234:235]
v_pk_fma_f32 v[50:51],v[234:235],v[234:235],v[50:51]
v_smfmac_f32_16x16x64_bf16 v[244:247],a[156:159],a[48:55],v10
s_waitcnt lgkmcnt(4)
ds_read_b64 a[240:241],v44 offset:57344
ds_read_b64 a[242:243],v44 offset:57352
ds_read_b64 a[224:225],v45 offset:57344
ds_read_b64 a[226:227],v45 offset:57352
ds_read_b128 a[232:235],v13 offset:0
v_pk_add_f32 v[60:61],v[60:61],v[236:237]
v_pk_fma_f32 v[48:49],v[236:237],v[236:237],v[48:49]
v_pk_add_f32 v[62:63],v[62:63],v[238:239]
v_pk_fma_f32 v[50:51],v[238:239],v[238:239],v[50:51]
v_smfmac_f32_16x16x64_bf16 v[248:251],a[184:187],a[56:63],v10
s_waitcnt lgkmcnt(5)
buffer_store_dwordx4 a[80:83],v7,s[40:43],s11 offen offset:128 sc1
ds_read_b64 a[248:249],v46 offset:57344
ds_read_b64 a[250:251],v46 offset:57352
ds_read_b64 a[228:229],v47 offset:57344
ds_read_b64 a[230:231],v47 offset:57352
ds_read_b128 a[236:239],v13 offset:256
v_pk_add_f32 v[60:61],v[60:61],v[240:241]
v_pk_fma_f32 v[48:49],v[240:241],v[240:241],v[48:49]
v_pk_add_f32 v[62:63],v[62:63],v[242:243]
v_pk_fma_f32 v[50:51],v[242:243],v[242:243],v[50:51]
v_smfmac_f32_16x16x64_bf16 v[252:255],a[188:191],a[64:71],v10
ds_read_b128 v[40:43],v4 offset:3584
ds_read_b128 v[36:39],v4 offset:3616
v_pk_add_f32 v[60:61],v[60:61],v[244:245]
v_pk_fma_f32 v[48:49],v[244:245],v[244:245],v[48:49]
v_pk_add_f32 v[62:63],v[62:63],v[246:247]
v_pk_fma_f32 v[50:51],v[246:247],v[246:247],v[50:51]
v_pk_add_f32 v[60:61],v[60:61],v[248:249]
v_pk_fma_f32 v[48:49],v[248:249],v[248:249],v[48:49]
v_pk_add_f32 v[62:63],v[62:63],v[250:251]
v_pk_fma_f32 v[50:51],v[250:251],v[250:251],v[50:51]
v_pk_add_f32 v[60:61],v[60:61],v[252:253]
v_pk_fma_f32 v[48:49],v[252:253],v[252:253],v[48:49]
v_pk_add_f32 v[62:63],v[62:63],v[254:255]
v_pk_fma_f32 v[50:51],v[254:255],v[254:255],v[50:51]
s_nop 0
v_permlane32_swap_b32 v60,v62
v_permlane32_swap_b32 v61,v63
v_permlane32_swap_b32 v48,v50
v_permlane32_swap_b32_e64 v49,v51
v_pk_add_f32 v[60:61],v[60:61],v[62:63]
v_pk_add_f32 v[48:49],v[48:49],v[50:51]
s_nop 0
v_permlane16_swap_b32 v60,v61
v_permlane16_swap_b32 v48,v49
v_add_f32 v8,v60,v61
v_add_f32_e64 v9,v48,v49
v_mul_f32 v8,0x3c2aaaab,v8
v_mul_f32 v9,0x3c2aaaab,v9
v_cmp_ngt_f32 vcc,0x5d800000,v9
v_fma_f32 v9,-v8,v8,v9
v_max_f32 v9,0,v9
s_or_b64 s[90:91],s[90:91],vcc
v_add_f32 v9,0x3727c5ac,v9
v_rsq_f32 v9,v9
s_nop 0
v_mul_f32 v60,s96,v9
v_mul_f32 v61,s96,v9
v_mul_f32 v48,-v8,v60
v_mul_f32 v49,-v8,v60
v_permlane16_swap_b32 v60,v61
s_nop 0
v_permlane16_swap_b32 v48,v49
v_mov_b64 v[62:63],v[60:61]
v_mov_b64 v[50:51],v[48:49]
s_nop 0
v_permlane32_swap_b32 v60,v62
v_permlane32_swap_b32 v61,v63
v_permlane32_swap_b32 v48,v50
v_permlane32_swap_b32 v49,v51
s_or_b32 s64,s90,s91
s_lshl_b32 s65,s52,2
v_mov_b32 v8,s64
v_mov_b32 v9,s65
ds_write_b32 v9,v8 offset:11648
v_pk_fma_f32 v[160:161],v[160:161],v[60:61],v[48:49]
v_pk_fma_f32 v[162:163],v[162:163],v[62:63],v[50:51]
v_pk_fma_f32 v[164:165],v[164:165],v[60:61],v[48:49]
v_pk_fma_f32 v[166:167],v[166:167],v[62:63],v[50:51]
v_pk_fma_f32 v[168:169],v[168:169],v[60:61],v[48:49]
v_pk_fma_f32 v[170:171],v[170:171],v[62:63],v[50:51]
v_pk_fma_f32 v[172:173],v[172:173],v[60:61],v[48:49]
v_pk_fma_f32 v[174:175],v[174:175],v[62:63],v[50:51]
v_pk_fma_f32 v[208:209],v[208:209],v[60:61],v[48:49]
v_pk_fma_f32 v[210:211],v[210:211],v[62:63],v[50:51]
v_pk_fma_f32 v[212:213],v[212:213],v[60:61],v[48:49]
v_pk_fma_f32 v[214:215],v[214:215],v[62:63],v[50:51]
v_pk_fma_f32 v[232:233],v[232:233],v[60:61],v[48:49]
v_pk_fma_f32 v[234:235],v[234:235],v[62:63],v[50:51]
v_pk_fma_f32 v[236:237],v[236:237],v[60:61],v[48:49]
v_pk_fma_f32 v[238:239],v[238:239],v[62:63],v[50:51]
v_pk_fma_f32 v[176:177],v[176:177],v[60:61],v[48:49]
v_pk_fma_f32 v[178:179],v[178:179],v[62:63],v[50:51]
v_pk_fma_f32 v[180:181],v[180:181],v[60:61],v[48:49]
v_pk_fma_f32 v[182:183],v[182:183],v[62:63],v[50:51]
v_pk_fma_f32 v[184:185],v[184:185],v[60:61],v[48:49]
v_pk_fma_f32 v[186:187],v[186:187],v[62:63],v[50:51]
v_pk_fma_f32 v[188:189],v[188:189],v[60:61],v[48:49]
v_pk_fma_f32 v[190:191],v[190:191],v[62:63],v[50:51]
v_pk_fma_f32 v[216:217],v[216:217],v[60:61],v[48:49]
v_pk_fma_f32 v[218:219],v[218:219],v[62:63],v[50:51]
v_pk_fma_f32 v[220:221],v[220:221],v[60:61],v[48:49]
v_pk_fma_f32 v[222:223],v[222:223],v[62:63],v[50:51]
v_pk_fma_f32 v[240:241],v[240:241],v[60:61],v[48:49]
v_pk_fma_f32 v[242:243],v[242:243],v[62:63],v[50:51]
v_pk_fma_f32 v[244:245],v[244:245],v[60:61],v[48:49]
v_pk_fma_f32 v[246:247],v[246:247],v[62:63],v[50:51]
v_pk_fma_f32 v[192:193],v[192:193],v[60:61],v[48:49]
v_pk_fma_f32 v[194:195],v[194:195],v[62:63],v[50:51]
v_pk_fma_f32 v[196:197],v[196:197],v[60:61],v[48:49]
v_pk_fma_f32 v[198:199],v[198:199],v[62:63],v[50:51]
v_pk_fma_f32 v[200:201],v[200:201],v[60:61],v[48:49]
v_pk_fma_f32 v[202:203],v[202:203],v[62:63],v[50:51]
v_pk_fma_f32 v[204:205],v[204:205],v[60:61],v[48:49]
v_pk_fma_f32 v[206:207],v[206:207],v[62:63],v[50:51]
v_pk_fma_f32 v[224:225],v[224:225],v[60:61],v[48:49]
v_pk_fma_f32 v[226:227],v[226:227],v[62:63],v[50:51]
v_pk_fma_f32 v[228:229],v[228:229],v[60:61],v[48:49]
v_pk_fma_f32 v[230:231],v[230:231],v[62:63],v[50:51]
v_pk_fma_f32 v[248:249],v[248:249],v[60:61],v[48:49]
v_pk_fma_f32 v[250:251],v[250:251],v[62:63],v[50:51]
v_pk_fma_f32 v[252:253],v[252:253],v[60:61],v[48:49]
v_pk_fma_f32 v[254:255],v[254:255],v[62:63],v[50:51]
v_mfma_f32_16x16x32_bf16 v[32:35],a[204:207],a[8:11],v[20:23]
buffer_store_dwordx4 a[84:87],v7,s[40:43],s11 offen offset:192 sc1
v_cvt_pk_bf16_f32 v160,v160,v164
v_cvt_pk_bf16_f32 v168,v168,v172
v_cvt_pk_bf16_f32 v208,v208,v212
v_lshlrev_b32_sdwa v24,1,v24 dst_sel:DWORD dst_unused:UNUSED_PAD src0_sel:DWORD src1_sel:WORD_1
v_mfma_f32_16x16x32_bf16 v[32:35],a[208:211],a[24:27],v[32:35]
ds_read_u16 v24,v24
v_accvgpr_write_b32 a48,v160
v_accvgpr_write_b32 a49,v168
v_cvt_pk_bf16_f32 v232,v232,v236
v_accvgpr_write_b32 a50,v208
v_mfma_f32_16x16x32_bf16 v[32:35],a[212:215],a[40:43],v[32:35]
ds_read_b64_tr_b16 a[216:217],v1 offset:6160
v_cvt_pk_bf16_f32 v161,v161,v165
v_accvgpr_write_b32 a51,v232
v_cvt_pk_bf16_f32 v169,v169,v173
v_accvgpr_write_b32 a52,v161
v_mfma_f32_16x16x32_bf16 v[60:63],a[200:203],a[40:43],v[16:19]
ds_read_b64_tr_b16 a[218:219],v1 offset:7184
v_cvt_pk_bf16_f32 v209,v209,v213
v_accvgpr_write_b32 a53,v169
v_cvt_pk_bf16_f32 v233,v233,v237
v_lshlrev_b32_sdwa v26,1,v26 dst_sel:DWORD dst_unused:UNUSED_PAD src0_sel:DWORD src1_sel:WORD_1
v_accvgpr_write_b32 a54,v209
v_mfma_f32_16x16x32_bf16 v[60:63],a[196:199],a[24:27],v[60:63]
ds_read_u16_d16_hi v26,v26
v_cvt_pk_bf16_f32 v162,v162,v166
v_accvgpr_write_b32 a55,v233
v_cvt_pk_bf16_f32 v170,v170,v174
v_accvgpr_write_b32 a56,v162
v_mfma_f32_16x16x32_bf16 v[60:63],a[192:195],a[8:11],v[60:63]
buffer_store_dwordx4 a[88:91],v7,s[40:43],s11 offen offset:256 sc1
v_accvgpr_write_b32 a57,v170
v_cvt_pk_bf16_f32 v210,v210,v214
v_accvgpr_write_b32 a58,v210
v_lshlrev_b32_sdwa v25,1,v25 dst_sel:DWORD dst_unused:UNUSED_PAD src0_sel:DWORD src1_sel:WORD_1
v_cvt_pk_bf16_f32 v234,v234,v238
v_mfma_f32_16x16x32_bf16 v[48:51],a[192:195],a[12:15],v[16:19]
ds_read_u16 v25,v25
v_accvgpr_write_b32 a59,v234
v_cvt_pk_bf16_f32 v163,v163,v167
v_accvgpr_write_b32 a60,v163
v_mfma_f32_16x16x32_bf16 v[48:51],a[196:199],a[28:31],v[48:51]
ds_read_b64_tr_b16 a[220:221],v1 offset:8208
v_cvt_pk_bf16_f32 v171,v171,v175
v_accvgpr_write_b32 a61,v171
v_cvt_pk_bf16_f32 v211,v211,v215
v_accvgpr_write_b32 a62,v211
v_lshlrev_b32_sdwa v27,1,v27 dst_sel:DWORD dst_unused:UNUSED_PAD src0_sel:DWORD src1_sel:WORD_1
v_mfma_f32_16x16x32_bf16 v[48:51],a[200:203],a[44:47],v[48:51]
s_waitcnt lgkmcnt(6)
ds_read_u16_d16_hi v27,v27
v_cvt_pk_bf16_f32 v235,v235,v239
v_accvgpr_write_b32 a63,v235
v_cvt_pk_bf16_f32 v176,v176,v180
v_cvt_pk_bf16_f32 v184,v184,v188
v_mfma_f32_16x16x32_bf16 v[232:235],a[232:235],a[240:243],v[40:43]
s_nop 0
ds_read_b64_tr_b16 a[222:223],v1 offset:9232
v_cvt_pk_bf16_f32 v216,v216,v220
v_accvgpr_write_b32 a64,v176
v_accvgpr_write_b32 a65,v184
v_cvt_pk_bf16_f32 v240,v240,v244
v_lshlrev_b32_sdwa v28,1,v28 dst_sel:DWORD dst_unused:UNUSED_PAD src0_sel:DWORD src1_sel:WORD_1
v_mfma_f32_16x16x32_bf16 v[236:239],a[232:235],a[224:227],v[40:43]
ds_read_u16 v28,v28
v_accvgpr_write_b32 a66,v216
v_cvt_pk_bf16_f32 v177,v177,v181
v_accvgpr_write_b32 a67,v240
v_cvt_pk_bf16_f32 v185,v185,v189
v_mfma_f32_16x16x32_bf16 v[208:211],a[232:235],a[248:251],v[40:43]
v_accvgpr_write_b32 a68,v177
v_cvt_pk_bf16_f32 v217,v217,v221
v_accvgpr_write_b32 a69,v185
v_lshlrev_b32_sdwa v30,1,v30 dst_sel:DWORD dst_unused:UNUSED_PAD src0_sel:DWORD src1_sel:WORD_1
v_cvt_pk_bf16_f32 v241,v241,v245
v_mfma_f32_16x16x32_bf16 v[212:215],a[232:235],a[228:231],v[40:43]
ds_read_b128 a[232:235],v13 offset:512
ds_read_b128 v[40:43],v4 offset:3712
v_accvgpr_write_b32 a70,v217
v_cvt_pk_bf16_f32 v178,v178,v182
v_accvgpr_write_b32 a71,v241
v_mfma_f32_16x16x32_bf16 v[160:163],a[236:239],a[240:243],v[36:39]
ds_read_b64 a[240:241],v44 offset:57408
ds_read_b64 a[242:243],v44 offset:57416
v_cvt_pk_bf16_f32 v186,v186,v190
v_accvgpr_write_b32 a72,v178
v_accvgpr_write_b32 a73,v186
v_cvt_pk_bf16_f32 v218,v218,v222
v_lshlrev_b32_sdwa v29,1,v29 dst_sel:DWORD dst_unused:UNUSED_PAD src0_sel:DWORD src1_sel:WORD_1
v_mfma_f32_16x16x32_bf16 v[164:167],a[236:239],a[224:227],v[36:39]
ds_read_b64 a[224:225],v45 offset:57408
ds_read_b64 a[226:227],v45 offset:57416
v_accvgpr_write_b32 a74,v218
v_cvt_pk_bf16_f32 v242,v242,v246
v_accvgpr_write_b32 a75,v242
v_cvt_pk_bf16_f32 v179,v179,v183
v_mfma_f32_16x16x32_bf16 v[168:171],a[236:239],a[248:251],v[36:39]
ds_read_b64 a[248:249],v46 offset:57408
ds_read_b64 a[250:251],v46 offset:57416
v_accvgpr_write_b32 a76,v179
v_cvt_pk_bf16_f32 v187,v187,v191
v_accvgpr_write_b32 a77,v187
v_cvt_pk_bf16_f32 v219,v219,v223
v_lshlrev_b32_sdwa v31,1,v31 dst_sel:DWORD dst_unused:UNUSED_PAD src0_sel:DWORD src1_sel:WORD_1
v_mfma_f32_16x16x32_bf16 v[172:175],a[236:239],a[228:231],v[36:39]
s_waitcnt lgkmcnt(4)
buffer_store_dwordx4 a[92:95],v7,s[40:43],s11 offen offset:320 sc1
ds_read_b64 a[228:229],v47 offset:57408
v_accvgpr_write_b32 a78,v219
v_cvt_pk_bf16_f32 v243,v243,v247
v_accvgpr_write_b32 a79,v243
v_cvt_pk_bf16_f32 v192,v192,v196
v_mfma_f32_16x16x32_bf16 v[240:243],a[232:235],a[240:243],v[40:43]
s_waitcnt lgkmcnt(3)
ds_read_b64 a[230:231],v47 offset:57416
ds_read_b128 a[236:239],v13 offset:768
v_cvt_pk_bf16_f32 v200,v200,v204
v_cvt_pk_bf16_f32 v224,v224,v228
v_accvgpr_write_b32 a80,v192
v_accvgpr_write_b32 a81,v200
v_lshlrev_b32_sdwa v52,1,v52 dst_sel:DWORD dst_unused:UNUSED_PAD src0_sel:DWORD src1_sel:WORD_1
v_mfma_f32_16x16x32_bf16 v[244:247],a[232:235],a[224:227],v[40:43]
s_waitcnt lgkmcnt(3)
ds_read_b128 v[36:39],v4 offset:3744
ds_read_u16_d16_hi v30,v30
v_cvt_pk_bf16_f32 v248,v248,v252
v_accvgpr_write_b32 a82,v224
v_cvt_pk_bf16_f32 v193,v193,v197
v_mfma_f32_16x16x32_bf16 v[216:219],a[232:235],a[248:251],v[40:43]
s_waitcnt lgkmcnt(3)
ds_read_u16 v29,v29
v_accvgpr_write_b32 a83,v248
v_cvt_pk_bf16_f32 v201,v201,v205
v_accvgpr_write_b32 a84,v193
v_cvt_pk_bf16_f32 v225,v225,v229
v_lshlrev_b32_sdwa v54,1,v54 dst_sel:DWORD dst_unused:UNUSED_PAD src0_sel:DWORD src1_sel:WORD_1
v_mfma_f32_16x16x32_bf16 v[220:223],a[232:235],a[228:231],v[40:43]
s_waitcnt lgkmcnt(2)
ds_read_b128 a[232:235],v13 offset:1024
ds_read_b128 v[40:43],v4 offset:3840
v_accvgpr_write_b32 a85,v201
v_cvt_pk_bf16_f32 v249,v249,v253
v_accvgpr_write_b32 a86,v225
v_cvt_pk_bf16_f32 v194,v194,v198
v_mfma_f32_16x16x32_bf16 v[176:179],a[236:239],a[240:243],v[36:39]
s_nop 0
ds_read_b64 a[240:241],v44 offset:57472
ds_read_b64 a[242:243],v44 offset:57480
v_accvgpr_write_b32 a87,v249
v_cvt_pk_bf16_f32 v202,v202,v206
v_accvgpr_write_b32 a88,v194
v_accvgpr_write_b32 a89,v202
v_lshlrev_b32_sdwa v53,1,v53 dst_sel:DWORD dst_unused:UNUSED_PAD src0_sel:DWORD src1_sel:WORD_1
v_mfma_f32_16x16x32_bf16 v[180:183],a[236:239],a[224:227],v[36:39]
ds_read_b64 a[224:225],v45 offset:57472
ds_read_b64 a[226:227],v45 offset:57480
v_cvt_pk_bf16_f32 v226,v226,v230
v_accvgpr_write_b32 a90,v226
v_cvt_pk_bf16_f32 v250,v250,v254
v_accvgpr_write_b32 a91,v250
v_mfma_f32_16x16x32_bf16 v[184:187],a[236:239],a[248:251],v[36:39]
s_nop 0
ds_read_b64 a[248:249],v46 offset:57472
ds_read_b64 a[250:251],v46 offset:57480
v_cvt_pk_bf16_f32 v195,v195,v199
v_accvgpr_write_b32 a92,v195
v_cvt_pk_bf16_f32 v203,v203,v207
v_accvgpr_write_b32 a93,v203
v_mfma_f32_16x16x32_bf16 v[188:191],a[236:239],a[228:231],v[36:39]
s_waitcnt lgkmcnt(4)
ds_read_b64 a[228:229],v47 offset:57472
ds_read_b64 a[230:231],v47 offset:57480
v_cvt_pk_bf16_f32 v227,v227,v231
v_accvgpr_write_b32 a94,v227
v_cvt_pk_bf16_f32 v251,v251,v255
v_lshlrev_b32_sdwa v55,1,v55 dst_sel:DWORD dst_unused:UNUSED_PAD src0_sel:DWORD src1_sel:WORD_1
v_accvgpr_write_b32 a95,v251
v_mfma_f32_16x16x32_bf16 v[248:251],a[232:235],a[240:243],v[40:43]
s_waitcnt lgkmcnt(4)
ds_read_b128 a[236:239],v13 offset:1280
ds_read_b128 v[36:39],v4 offset:3872
v_mfma_f32_16x16x32_bf16 v[252:255],a[232:235],a[224:227],v[40:43]
s_waitcnt lgkmcnt(4)
ds_read_u16_d16_hi v31,v31
ds_read_u16 v52,v52
v_lshlrev_b32_sdwa v56,1,v56 dst_sel:DWORD dst_unused:UNUSED_PAD src0_sel:DWORD src1_sel:WORD_1
v_mfma_f32_16x16x32_bf16 v[224:227],a[232:235],a[248:251],v[40:43]
s_waitcnt lgkmcnt(4)
ds_read_u16_d16_hi v54,v54
v_lshlrev_b32_sdwa v58,1,v58 dst_sel:DWORD dst_unused:UNUSED_PAD src0_sel:DWORD src1_sel:WORD_1
v_mfma_f32_16x16x32_bf16 v[228:231],a[232:235],a[228:231],v[40:43]
s_waitcnt lgkmcnt(3)
ds_read_b64_tr_b16 a[232:233],v3 offset:6224
ds_read_b64_tr_b16 a[234:235],v3 offset:7760
v_mfma_f32_16x16x32_bf16 v[192:195],a[236:239],a[240:243],v[36:39]
ds_read_b64_tr_b16 a[240:241],v3 offset:6232
ds_read_b64_tr_b16 a[242:243],v3 offset:7768
v_mfma_f32_16x16x32_bf16 v[196:199],a[236:239],a[224:227],v[36:39]
ds_read_b64_tr_b16 a[224:225],v3 offset:6288
ds_read_b64_tr_b16 a[226:227],v3 offset:7824
v_lshlrev_b32_sdwa v57,1,v57 dst_sel:DWORD dst_unused:UNUSED_PAD src0_sel:DWORD src1_sel:WORD_1
v_mfma_f32_16x16x32_bf16 v[200:203],a[236:239],a[248:251],v[36:39]
ds_read_b64_tr_b16 a[248:249],v3 offset:6296
ds_read_b64_tr_b16 a[250:251],v3 offset:7832
v_mfma_f32_16x16x32_bf16 v[204:207],a[236:239],a[228:231],v[36:39]
ds_read_u16 v53,v53
ds_read_u16_d16_hi v55,v55
v_lshlrev_b32_sdwa v59,1,v59 dst_sel:DWORD dst_unused:UNUSED_PAD src0_sel:DWORD src1_sel:WORD_1
v_mfma_f32_16x16x32_bf16 v[44:47],a[212:215],a[44:47],v[20:23]
ds_read_u16 v56,v56
v_mfma_f32_16x16x32_bf16 v[44:47],a[208:211],a[28:31],v[44:47]
s_waitcnt vmcnt(18)
ds_read_u16_d16_hi v58,v58
v_mfma_f32_16x16x32_bf16 v[44:47],a[204:207],a[12:15],v[44:47]
s_barrier
ds_read_u16 v57,v57
ds_read_u16_d16_hi v59,v59
v_mfma_f32_16x16x32_bf16 v[40:43],a[204:207],a[48:51],v[20:23]
v_lshlrev_b32_sdwa v32,1,v32 dst_sel:DWORD dst_unused:UNUSED_PAD src0_sel:DWORD src1_sel:WORD_1
v_lshlrev_b32_sdwa v34,1,v34 dst_sel:DWORD dst_unused:UNUSED_PAD src0_sel:DWORD src1_sel:WORD_1
v_mfma_f32_16x16x32_bf16 v[40:43],a[208:211],a[64:67],v[40:43]
ds_read_u16 v32,v32
v_lshl_add_u64 v[24:25],v[24:25],0,v[26:27]
v_mfma_f32_16x16x32_bf16 v[40:43],a[212:215],a[80:83],v[40:43]
s_waitcnt lgkmcnt(11)
ds_read_u16_d16_hi v34,v34
v_lshl_add_u64 v[26:27],v[28:29],0,v[30:31]
v_mfma_f32_16x16x32_bf16 v[36:39],a[200:203],a[80:83],v[16:19]
v_lshlrev_b32_sdwa v33,1,v33 dst_sel:DWORD dst_unused:UNUSED_PAD src0_sel:DWORD src1_sel:WORD_1
v_lshlrev_b32_sdwa v35,1,v35 dst_sel:DWORD dst_unused:UNUSED_PAD src0_sel:DWORD src1_sel:WORD_1
v_mfma_f32_16x16x32_bf16 v[36:39],a[196:199],a[64:67],v[36:39]
s_waitcnt lgkmcnt(6)
ds_read_u16 v33,v33
v_lshl_add_u64 v[28:29],v[52:53],0,v[54:55]
v_mfma_f32_16x16x32_bf16 v[36:39],a[192:195],a[48:51],v[36:39]
ds_read_u16_d16_hi v35,v35
v_lshlrev_b32_sdwa v60,1,v60 dst_sel:DWORD dst_unused:UNUSED_PAD src0_sel:DWORD src1_sel:WORD_1
v_mfma_f32_16x16x32_bf16 v[52:55],a[192:195],a[52:55],v[16:19]
ds_read_u16 v60,v60
v_lshlrev_b32_sdwa v62,1,v62 dst_sel:DWORD dst_unused:UNUSED_PAD src0_sel:DWORD src1_sel:WORD_1
v_mfma_f32_16x16x32_bf16 v[52:55],a[196:199],a[68:71],v[52:55]
s_waitcnt lgkmcnt(5)
ds_read_u16_d16_hi v62,v62
v_lshl_add_u64 v[30:31],v[56:57],0,v[58:59]
v_mfma_f32_16x16x32_bf16 v[52:55],a[200:203],a[84:87],v[52:55]
s_nop 0
v_lshlrev_b32_sdwa v61,1,v61 dst_sel:DWORD dst_unused:UNUSED_PAD src0_sel:DWORD src1_sel:WORD_1
v_lshlrev_b32_sdwa v63,1,v63 dst_sel:DWORD dst_unused:UNUSED_PAD src0_sel:DWORD src1_sel:WORD_1
v_mfma_f32_16x16x32_bf16 v[56:59],a[212:215],a[84:87],v[20:23]
ds_read_u16 v61,v61
v_lshlrev_b32_sdwa v44,1,v44 dst_sel:DWORD dst_unused:UNUSED_PAD src0_sel:DWORD src1_sel:WORD_1
v_mfma_f32_16x16x32_bf16 v[56:59],a[208:211],a[68:71],v[56:59]
ds_read_u16_d16_hi v63,v63
v_lshlrev_b32_sdwa v46,1,v46 dst_sel:DWORD dst_unused:UNUSED_PAD src0_sel:DWORD src1_sel:WORD_1
v_mfma_f32_16x16x32_bf16 v[56:59],a[204:207],a[52:55],v[56:59]
ds_read_u16 v44,v44
v_lshlrev_b32_sdwa v45,1,v45 dst_sel:DWORD dst_unused:UNUSED_PAD src0_sel:DWORD src1_sel:WORD_1
v_mfma_f32_16x16x32_bf16 v[136:139],a[244:247],v[24:27],v[136:139]
ds_read_u16_d16_hi v46,v46
v_lshlrev_b32_sdwa v47,1,v47 dst_sel:DWORD dst_unused:UNUSED_PAD src0_sel:DWORD src1_sel:WORD_1
v_mfma_f32_16x16x32_bf16 v[140:143],a[244:247],v[28:31],v[140:143]
ds_read_u16 v45,v45
v_lshlrev_b32_sdwa v48,1,v48 dst_sel:DWORD dst_unused:UNUSED_PAD src0_sel:DWORD src1_sel:WORD_1
v_mfma_f32_16x16x32_bf16 v[68:71],a[252:255],v[28:31],v[68:71]
ds_read_u16_d16_hi v47,v47
v_lshlrev_b32_sdwa v50,1,v50 dst_sel:DWORD dst_unused:UNUSED_PAD src0_sel:DWORD src1_sel:WORD_1
v_mfma_f32_16x16x32_bf16 v[64:67],a[252:255],v[24:27],v[64:67]
ds_read_u16 v48,v48
v_lshlrev_b32_sdwa v49,1,v49 dst_sel:DWORD dst_unused:UNUSED_PAD src0_sel:DWORD src1_sel:WORD_1
v_mfma_f32_16x16x32_bf16 v[144:147],a[232:235],v[24:27],v[144:147]
ds_read_u16_d16_hi v50,v50
v_lshlrev_b32_sdwa v51,1,v51 dst_sel:DWORD dst_unused:UNUSED_PAD src0_sel:DWORD src1_sel:WORD_1
v_mfma_f32_16x16x32_bf16 v[148:151],a[232:235],v[28:31],v[148:151]
ds_read_u16 v49,v49
v_mfma_f32_16x16x32_bf16 v[84:87],a[240:243],v[28:31],v[84:87]
ds_read_u16_d16_hi v51,v51
v_mfma_f32_16x16x32_bf16 v[80:83],a[240:243],v[24:27],v[80:83]
v_mfma_f32_16x16x32_bf16 v[152:155],a[224:227],v[24:27],v[152:155]
v_mfma_f32_16x16x32_bf16 v[156:159],a[224:227],v[28:31],v[156:159]
v_mfma_f32_16x16x32_bf16 v[100:103],a[248:251],v[28:31],v[100:103]
v_mfma_f32_16x16x32_bf16 v[96:99],a[248:251],v[24:27],v[96:99]
v_mfma_f32_16x16x32_bf16 v[28:31],a[204:207],a[56:59],v[20:23]
v_lshlrev_b32_sdwa v40,1,v40 dst_sel:DWORD dst_unused:UNUSED_PAD src0_sel:DWORD src1_sel:WORD_1
v_fma_mixlo_f16 v8,v53,s99,0
v_mfma_f32_16x16x32_bf16 v[28:31],a[208:211],a[72:75],v[28:31]
s_waitcnt lgkmcnt(2)
ds_read_u16 v40,v40
v_lshl_add_u64 v[32:33],v[32:33],0,v[34:35]
v_mfma_f32_16x16x32_bf16 v[28:31],a[212:215],a[88:91],v[28:31]
v_lshl_add_u64 v[34:35],v[60:61],0,v[62:63]
v_lshlrev_b32_sdwa v42,1,v42 dst_sel:DWORD dst_unused:UNUSED_PAD src0_sel:DWORD src1_sel:WORD_1
v_mfma_f32_16x16x32_bf16 v[24:27],a[200:203],a[88:91],v[16:19]
ds_read_u16_d16_hi v42,v42
v_fma_mixhi_f16 v8,v55,s99,0
v_mfma_f32_16x16x32_bf16 v[24:27],a[196:199],a[72:75],v[24:27]
v_lshl_add_u64 v[60:61],v[44:45],0,v[46:47]
v_lshlrev_b32_sdwa v41,1,v41 dst_sel:DWORD dst_unused:UNUSED_PAD src0_sel:DWORD src1_sel:WORD_1
v_pk_fma_f16 v9,v8,v8,s54
v_mfma_f32_16x16x32_bf16 v[24:27],a[192:195],a[56:59],v[24:27]
ds_read_u16 v41,v41
v_lshlrev_b32_sdwa v43,1,v43 dst_sel:DWORD dst_unused:UNUSED_PAD src0_sel:DWORD src1_sel:WORD_1
v_mfma_f32_16x16x32_bf16 v[44:47],a[192:195],a[60:63],v[16:19]
ds_read_b128 v[16:19],v4 offset:768
ds_read_b64_tr_b16 a[192:193],v1 offset:10256
v_pk_mul_f16 v9,v8,v9
v_mfma_f32_16x16x32_bf16 v[44:47],a[196:199],a[76:79],v[44:47]
s_waitcnt lgkmcnt(5)
ds_read_b64_tr_b16 a[194:195],v1 offset:11280
ds_read_b64_tr_b16 a[196:197],v2 offset:10256
v_lshl_add_u64 v[62:63],v[48:49],0,v[50:51]
v_lshlrev_b32_sdwa v36,1,v36 dst_sel:DWORD dst_unused:UNUSED_PAD src0_sel:DWORD src1_sel:WORD_1
v_mfma_f32_16x16x32_bf16 v[44:47],a[200:203],a[92:95],v[44:47]
ds_read_b64_tr_b16 a[198:199],v2 offset:11280
ds_read_b64_tr_b16 a[200:201],v2 offset:8208
v_exp_f16 v8,v9
v_mfma_f32_16x16x32_bf16 v[48:51],a[212:215],a[92:95],v[20:23]
ds_read_b128 v[20:23],v4 offset:800
ds_read_b64_tr_b16 a[202:203],v2 offset:9232
v_lshlrev_b32_sdwa v38,1,v38 dst_sel:DWORD dst_unused:UNUSED_PAD src0_sel:DWORD src1_sel:WORD_1
v_mfma_f32_16x16x32_bf16 v[48:51],a[208:211],a[76:79],v[48:51]
ds_read_b64_tr_b16 a[212:213],v2 offset:6160
ds_read_b64_tr_b16 a[214:215],v2 offset:7184
v_lshlrev_b32_sdwa v37,1,v37 dst_sel:DWORD dst_unused:UNUSED_PAD src0_sel:DWORD src1_sel:WORD_1
v_mfma_f32_16x16x32_bf16 v[48:51],a[204:207],a[60:63],v[48:51]
TE11_%=:
s_cbranch_execz TP10205_%=
ds_read_u16_d16_hi v43,v43
v_exp_f16_sdwa v8,v9 dst_sel:WORD_1 dst_unused:UNUSED_PRESERVE src0_sel:WORD_1
v_mfma_f32_16x16x32_bf16 v[112:115],a[244:247],v[32:35],v[112:115]
ds_read_u16 v36,v36
v_lshlrev_b32_sdwa v39,1,v39 dst_sel:DWORD dst_unused:UNUSED_PAD src0_sel:DWORD src1_sel:WORD_1
v_mfma_f32_16x16x32_bf16 v[116:119],a[244:247],v[60:63],v[116:119]
ds_read_u16_d16_hi v38,v38
v_lshlrev_b32_sdwa v56,1,v56 dst_sel:DWORD dst_unused:UNUSED_PAD src0_sel:DWORD src1_sel:WORD_1
v_mfma_f32_16x16x32_bf16 v[76:79],a[252:255],v[60:63],v[76:79]
ds_read_u16 v37,v37
v_pk_fma_f16 v8,v8,s55,s55
v_mfma_f32_16x16x32_bf16 v[72:75],a[252:255],v[32:35],v[72:75]
v_rcp_f16_e64 v9,v8
v_lshlrev_b32_sdwa v58,1,v58 dst_sel:DWORD dst_unused:UNUSED_PAD src0_sel:DWORD src1_sel:WORD_1
v_mfma_f32_16x16x32_bf16 v[120:123],a[232:235],v[32:35],v[120:123]
ds_read_u16_d16_hi v39,v39
v_lshlrev_b32_sdwa v57,1,v57 dst_sel:DWORD dst_unused:UNUSED_PAD src0_sel:DWORD src1_sel:WORD_1
v_mfma_f32_16x16x32_bf16 v[124:127],a[232:235],v[60:63],v[124:127]
ds_read_u16 v56,v56
v_lshlrev_b32_sdwa v59,1,v59 dst_sel:DWORD dst_unused:UNUSED_PAD src0_sel:DWORD src1_sel:WORD_1
v_mfma_f32_16x16x32_bf16 v[92:95],a[240:243],v[60:63],v[92:95]
ds_read_u16_d16_hi v58,v58
v_lshlrev_b32_sdwa v52,1,v52 dst_sel:DWORD dst_unused:UNUSED_PAD src0_sel:DWORD src1_sel:WORD_1
v_mfma_f32_16x16x32_bf16 v[88:91],a[240:243],v[32:35],v[88:91]
v_rcp_f16_sdwa v9,v8 dst_sel:WORD_1 dst_unused:UNUSED_PRESERVE src0_sel:WORD_1
v_mfma_f32_16x16x32_bf16 v[128:131],a[224:227],v[32:35],v[128:131]
ds_read_u16 v57,v57
ds_read_u16_d16_hi v59,v59
v_mfma_f32_16x16x32_bf16 v[132:135],a[224:227],v[60:63],v[132:135]
ds_read_u16 v52,v52
v_lshlrev_b32_sdwa v54,1,v54 dst_sel:DWORD dst_unused:UNUSED_PAD src0_sel:DWORD src1_sel:WORD_1
v_mfma_f32_16x16x32_bf16 v[108:111],a[248:251],v[60:63],v[108:111]
ds_read_u16_d16_hi v54,v54
v_fma_mix_f32 v53,v53,v9,0 op_sel_hi:[0,1,0]
v_mfma_f32_16x16x32_bf16 v[104:107],a[248:251],v[32:35],v[104:107]
s_waitcnt lgkmcnt(7)
v_fma_mix_f32 v55,v55,v9,0 op_sel:[0,1,0] op_sel_hi:[0,1,0]
v_mfma_f32_16x16x32_bf16 v[60:63],a[216:219],a[0:3],v[16:19]
s_add_u32 m0,s62,0
v_lshlrev_b32_sdwa v28,1,v28 dst_sel:DWORD dst_unused:UNUSED_PAD src0_sel:DWORD src1_sel:WORD_1
v_lshlrev_b32_sdwa v30,1,v30 dst_sel:DWORD dst_unused:UNUSED_PAD src0_sel:DWORD src1_sel:WORD_1
v_mfma_f32_16x16x32_bf16 v[60:63],a[220:223],a[16:19],v[60:63]
s_mov_b32 s68,s87
ds_read_u16 v28,v28
v_lshl_add_u64 v[40:41],v[40:41],0,v[42:43]
v_mfma_f32_16x16x32_bf16 v[60:63],a[192:195],a[32:35],v[60:63]
s_waitcnt lgkmcnt(6)
buffer_load_dwordx4 v5,s[44:47],s68 offen offset:0 lds
v_lshl_add_u64 v[42:43],v[36:37],0,v[38:39]
v_mfma_f32_16x16x32_bf16 v[32:35],a[196:199],a[32:35],v[20:23]
s_add_u32 s68,s68,s53
ds_read_u16_d16_hi v30,v30
v_lshlrev_b32_sdwa v29,1,v29 dst_sel:DWORD dst_unused:UNUSED_PAD src0_sel:DWORD src1_sel:WORD_1
v_mfma_f32_16x16x32_bf16 v[32:35],a[200:203],a[16:19],v[32:35]
s_waitcnt lgkmcnt(4)
buffer_load_dwordx4 v5,s[44:47],s68 offen offset:1024 lds
v_lshl_add_u64 v[36:37],v[56:57],0,v[58:59]
v_mfma_f32_16x16x32_bf16 v[32:35],a[212:215],a[0:3],v[32:35]
ds_read_u16 v29,v29
v_lshlrev_b32_sdwa v31,1,v31 dst_sel:DWORD dst_unused:UNUSED_PAD src0_sel:DWORD src1_sel:WORD_1
v_mfma_f32_16x16x32_bf16 v[56:59],a[212:215],a[4:7],v[20:23]
s_waitcnt lgkmcnt(3)
ds_read_u16_d16_hi v31,v31
v_or_b32 v38,v52,v54
v_lshlrev_b32_sdwa v24,1,v24 dst_sel:DWORD dst_unused:UNUSED_PAD src0_sel:DWORD src1_sel:WORD_1
v_mfma_f32_16x16x32_bf16 v[56:59],a[200:203],a[20:23],v[56:59]
ds_read_u16 v24,v24
v_cvt_pk_bf16_f32 v39,v53,v55
v_lshlrev_b32_sdwa v26,1,v26 dst_sel:DWORD dst_unused:UNUSED_PAD src0_sel:DWORD src1_sel:WORD_1
v_mfma_f32_16x16x32_bf16 v[56:59],a[196:199],a[36:39],v[56:59]
ds_read_u16_d16_hi v26,v26
v_lshlrev_b32_sdwa v25,1,v25 dst_sel:DWORD dst_unused:UNUSED_PAD src0_sel:DWORD src1_sel:WORD_1
v_mfma_f32_16x16x32_bf16 v[52:55],a[192:195],a[36:39],v[16:19]
ds_read_u16 v25,v25
v_lshlrev_b32_sdwa v27,1,v27 dst_sel:DWORD dst_unused:UNUSED_PAD src0_sel:DWORD src1_sel:WORD_1
v_mfma_f32_16x16x32_bf16 v[52:55],a[220:223],a[20:23],v[52:55]
ds_read_u16_d16_hi v27,v27
v_lshlrev_b32_sdwa v48,1,v48 dst_sel:DWORD dst_unused:UNUSED_PAD src0_sel:DWORD src1_sel:WORD_1
v_mfma_f32_16x16x32_bf16 v[52:55],a[216:219],a[4:7],v[52:55]
ds_read_u16 v48,v48
v_lshlrev_b32_sdwa v50,1,v50 dst_sel:DWORD dst_unused:UNUSED_PAD src0_sel:DWORD src1_sel:WORD_1
v_mfma_f32_16x16x32_bf16 v[232:235],a[244:247],v[40:43],v[232:235]
ds_read_u16_d16_hi v50,v50
v_lshlrev_b32_sdwa v49,1,v49 dst_sel:DWORD dst_unused:UNUSED_PAD src0_sel:DWORD src1_sel:WORD_1
v_mfma_f32_16x16x32_bf16 v[236:239],a[244:247],v[36:39],v[236:239]
ds_read_u16 v49,v49
v_lshlrev_b32_sdwa v51,1,v51 dst_sel:DWORD dst_unused:UNUSED_PAD src0_sel:DWORD src1_sel:WORD_1
v_mfma_f32_16x16x32_bf16 v[164:167],a[252:255],v[36:39],v[164:167]
ds_read_u16_d16_hi v51,v51
ds_read_b64_tr_b16 a[228:229],v3 offset:12320
v_mfma_f32_16x16x32_bf16 v[160:163],a[252:255],v[40:43],v[160:163]
s_add_u32 s68,s68,s53
ds_read_b64_tr_b16 a[230:231],v3 offset:13856
v_lshlrev_b32_sdwa v44,1,v44 dst_sel:DWORD dst_unused:UNUSED_PAD src0_sel:DWORD src1_sel:WORD_1
v_mfma_f32_16x16x32_bf16 v[240:243],a[232:235],v[40:43],v[240:243]
buffer_load_dwordx4 v5,s[44:47],s68 offen offset:2048 lds
v_lshlrev_b32_sdwa v46,1,v46 dst_sel:DWORD dst_unused:UNUSED_PAD src0_sel:DWORD src1_sel:WORD_1
v_mfma_f32_16x16x32_bf16 v[244:247],a[232:235],v[36:39],v[244:247]
ds_read_u16 v44,v44
v_lshlrev_b32_sdwa v45,1,v45 dst_sel:DWORD dst_unused:UNUSED_PAD src0_sel:DWORD src1_sel:WORD_1
v_mfma_f32_16x16x32_bf16 v[180:183],a[240:243],v[36:39],v[180:183]
ds_read_u16_d16_hi v46,v46
v_lshlrev_b32_sdwa v47,1,v47 dst_sel:DWORD dst_unused:UNUSED_PAD src0_sel:DWORD src1_sel:WORD_1
v_mfma_f32_16x16x32_bf16 v[176:179],a[240:243],v[40:43],v[176:179]
ds_read_u16 v45,v45
v_mfma_f32_16x16x32_bf16 v[248:251],a[224:227],v[40:43],v[248:251]
ds_read_u16_d16_hi v47,v47
v_mfma_f32_16x16x32_bf16 v[252:255],a[224:227],v[36:39],v[252:255]
ds_read_b64_tr_b16 a[236:237],v3 offset:12328
v_mfma_f32_16x16x32_bf16 v[196:199],a[248:251],v[36:39],v[196:199]
ds_read_b64_tr_b16 a[238:239],v3 offset:13864
v_mfma_f32_16x16x32_bf16 v[192:195],a[248:251],v[40:43],v[192:195]
v_mfma_f32_16x16x32_bf16 v[36:39],a[216:219],a[8:11],v[16:19]
ds_read_b64_tr_b16 a[208:209],v1 offset:12320
v_lshlrev_b32_sdwa v60,1,v60 dst_sel:DWORD dst_unused:UNUSED_PAD src0_sel:DWORD src1_sel:WORD_1
v_mfma_f32_16x16x32_bf16 v[36:39],a[220:223],a[24:27],v[36:39]
s_waitcnt lgkmcnt(7)
ds_read_u16 v60,v60
v_lshl_add_u64 v[28:29],v[28:29],0,v[30:31]
v_fma_mixlo_f16 v8,v57,s99,0
v_mfma_f32_16x16x32_bf16 v[36:39],a[192:195],a[40:43],v[36:39]
ds_read_b64_tr_b16 a[210:211],v1 offset:13344
v_lshl_add_u64 v[30:31],v[24:25],0,v[26:27]
v_lshlrev_b32_sdwa v62,1,v62 dst_sel:DWORD dst_unused:UNUSED_PAD src0_sel:DWORD src1_sel:WORD_1
v_mfma_f32_16x16x32_bf16 v[40:43],a[196:199],a[40:43],v[20:23]
ds_read_u16_d16_hi v62,v62
v_fma_mixhi_f16 v8,v59,s99,0
v_mfma_f32_16x16x32_bf16 v[40:43],a[200:203],a[24:27],v[40:43]
ds_read_b64_tr_b16 a[204:205],v1 offset:14368
v_lshl_add_u64 v[24:25],v[48:49],0,v[50:51]
v_lshlrev_b32_sdwa v61,1,v61 dst_sel:DWORD dst_unused:UNUSED_PAD src0_sel:DWORD src1_sel:WORD_1
v_pk_fma_f16 v9,v8,v8,s54
v_mfma_f32_16x16x32_bf16 v[40:43],a[212:215],a[8:11],v[40:43]
ds_read_u16 v61,v61
v_lshlrev_b32_sdwa v63,1,v63 dst_sel:DWORD dst_unused:UNUSED_PAD src0_sel:DWORD src1_sel:WORD_1
v_mfma_f32_16x16x32_bf16 v[48:51],a[212:215],a[12:15],v[20:23]
ds_read_u16_d16_hi v63,v63
v_pk_mul_f16 v9,v8,v9
v_mfma_f32_16x16x32_bf16 v[48:51],a[200:203],a[28:31],v[48:51]
s_waitcnt lgkmcnt(7)
ds_read_b64_tr_b16 a[206:207],v1 offset:15392
v_lshl_add_u64 v[26:27],v[44:45],0,v[46:47]
v_lshlrev_b32_sdwa v32,1,v32 dst_sel:DWORD dst_unused:UNUSED_PAD src0_sel:DWORD src1_sel:WORD_1
v_mfma_f32_16x16x32_bf16 v[48:51],a[196:199],a[44:47],v[48:51]
v_exp_f16 v8,v9
v_mfma_f32_16x16x32_bf16 v[44:47],a[192:195],a[44:47],v[16:19]
ds_read_u16 v32,v32
v_lshlrev_b32_sdwa v34,1,v34 dst_sel:DWORD dst_unused:UNUSED_PAD src0_sel:DWORD src1_sel:WORD_1
v_mfma_f32_16x16x32_bf16 v[44:47],a[220:223],a[28:31],v[44:47]
ds_read_u16_d16_hi v34,v34
v_lshlrev_b32_sdwa v33,1,v33 dst_sel:DWORD dst_unused:UNUSED_PAD src0_sel:DWORD src1_sel:WORD_1
v_mfma_f32_16x16x32_bf16 v[44:47],a[216:219],a[12:15],v[44:47]
v_exp_f16_sdwa v8,v9 dst_sel:WORD_1 dst_unused:UNUSED_PRESERVE src0_sel:WORD_1
v_mfma_f32_16x16x32_bf16 v[208:211],a[244:247],v[28:31],v[208:211]
ds_read_u16 v33,v33
v_lshlrev_b32_sdwa v35,1,v35 dst_sel:DWORD dst_unused:UNUSED_PAD src0_sel:DWORD src1_sel:WORD_1
v_mfma_f32_16x16x32_bf16 v[212:215],a[244:247],v[24:27],v[212:215]
ds_read_u16_d16_hi v35,v35
v_lshlrev_b32_sdwa v52,1,v52 dst_sel:DWORD dst_unused:UNUSED_PAD src0_sel:DWORD src1_sel:WORD_1
v_mfma_f32_16x16x32_bf16 v[172:175],a[252:255],v[24:27],v[172:175]
ds_read_u16 v52,v52
v_pk_fma_f16 v8,v8,s55,s55
v_mfma_f32_16x16x32_bf16 v[168:171],a[252:255],v[28:31],v[168:171]
ds_read_b64_tr_b16 a[244:245],v3 offset:12384
v_rcp_f16 v9,v8
v_lshlrev_b32_sdwa v54,1,v54 dst_sel:DWORD dst_unused:UNUSED_PAD src0_sel:DWORD src1_sel:WORD_1
v_mfma_f32_16x16x32_bf16 v[216:219],a[232:235],v[28:31],v[216:219]
ds_read_u16_d16_hi v54,v54
v_lshlrev_b32_sdwa v53,1,v53 dst_sel:DWORD dst_unused:UNUSED_PAD src0_sel:DWORD src1_sel:WORD_1
v_mfma_f32_16x16x32_bf16 v[220:223],a[232:235],v[24:27],v[220:223]
ds_read_u16 v53,v53
ds_read_b64_tr_b16 a[246:247],v3 offset:13920
v_mfma_f32_16x16x32_bf16 v[188:191],a[240:243],v[24:27],v[188:191]
ds_read_b64_tr_b16 a[252:253],v3 offset:12392
v_lshlrev_b32_sdwa v55,1,v55 dst_sel:DWORD dst_unused:UNUSED_PAD src0_sel:DWORD src1_sel:WORD_1
v_mfma_f32_16x16x32_bf16 v[184:187],a[240:243],v[28:31],v[184:187]
ds_read_u16_d16_hi v55,v55
v_rcp_f16_sdwa v9,v8 dst_sel:WORD_1 dst_unused:UNUSED_PRESERVE src0_sel:WORD_1
v_mfma_f32_16x16x32_bf16 v[224:227],a[224:227],v[28:31],v[224:227]
ds_read_b64_tr_b16 a[254:255],v3 offset:13928
ds_read_b64_tr_b16 a[232:233],v3 offset:12448
v_lshlrev_b32_sdwa v56,1,v56 dst_sel:DWORD dst_unused:UNUSED_PAD src0_sel:DWORD src1_sel:WORD_1
v_mfma_f32_16x16x32_bf16 v[228:231],a[224:227],v[24:27],v[228:231]
ds_read_u16 v56,v56
v_lshlrev_b32_sdwa v58,1,v58 dst_sel:DWORD dst_unused:UNUSED_PAD src0_sel:DWORD src1_sel:WORD_1
v_mfma_f32_16x16x32_bf16 v[204:207],a[248:251],v[24:27],v[204:207]
ds_read_u16_d16_hi v58,v58
v_fma_mix_f32 v57,v57,v9,0 op_sel_hi:[0,1,0]
v_mfma_f32_16x16x32_bf16 v[200:203],a[248:251],v[28:31],v[200:203]
ds_read_b64_tr_b16 a[234:235],v3 offset:13984
v_fma_mix_f32 v59,v59,v9,0 op_sel:[0,1,0] op_sel_hi:[0,1,0]
v_mfma_f32_16x16x32_bf16 v[24:27],a[216:219],a[48:51],v[16:19]
s_add_u32 m0,s62,6160
ds_read_b64_tr_b16 a[240:241],v3 offset:12456
v_lshlrev_b32_sdwa v36,1,v36 dst_sel:DWORD dst_unused:UNUSED_PAD src0_sel:DWORD src1_sel:WORD_1
v_mfma_f32_16x16x32_bf16 v[24:27],a[220:223],a[64:67],v[24:27]
s_waitcnt lgkmcnt(10)
ds_read_b64_tr_b16 a[242:243],v3 offset:13992
v_lshl_add_u64 v[60:61],v[60:61],0,v[62:63]
v_mfma_f32_16x16x32_bf16 v[24:27],a[192:195],a[80:83],v[24:27]
s_add_u32 s68,s68,s59
ds_read_u16 v36,v36
v_lshl_add_u64 v[62:63],v[32:33],0,v[34:35]
v_lshlrev_b32_sdwa v38,1,v38 dst_sel:DWORD dst_unused:UNUSED_PAD src0_sel:DWORD src1_sel:WORD_1
v_mfma_f32_16x16x32_bf16 v[28:31],a[196:199],a[80:83],v[20:23]
buffer_load_dwordx4 v5,s[44:47],s68 offen offset:0 lds
v_lshlrev_b32_sdwa v37,1,v37 dst_sel:DWORD dst_unused:UNUSED_PAD src0_sel:DWORD src1_sel:WORD_1
v_mfma_f32_16x16x32_bf16 v[28:31],a[200:203],a[64:67],v[28:31]
s_waitcnt lgkmcnt(8)
ds_read_u16_d16_hi v38,v38
v_lshl_add_u64 v[32:33],v[52:53],0,v[54:55]
v_mfma_f32_16x16x32_bf16 v[28:31],a[212:215],a[48:51],v[28:31]
s_add_u32 s68,s68,s53
ds_read_u16 v37,v37
v_lshlrev_b32_sdwa v39,1,v39 dst_sel:DWORD dst_unused:UNUSED_PAD src0_sel:DWORD src1_sel:WORD_1
v_mfma_f32_16x16x32_bf16 v[52:55],a[212:215],a[52:55],v[20:23]
s_waitcnt lgkmcnt(6)
buffer_load_dwordx4 v5,s[44:47],s68 offen offset:1024 lds
v_or_b32 v34,v56,v58
v_lshlrev_b32_sdwa v40,1,v40 dst_sel:DWORD dst_unused:UNUSED_PAD src0_sel:DWORD src1_sel:WORD_1
v_mfma_f32_16x16x32_bf16 v[52:55],a[200:203],a[68:71],v[52:55]
s_add_u32 s68,s68,s53
ds_read_u16_d16_hi v39,v39
v_cvt_pk_bf16_f32 v35,v57,v59
v_lshlrev_b32_sdwa v42,1,v42 dst_sel:DWORD dst_unused:UNUSED_PAD src0_sel:DWORD src1_sel:WORD_1
v_mfma_f32_16x16x32_bf16 v[52:55],a[196:199],a[84:87],v[52:55]
buffer_load_dwordx4 v5,s[44:47],s68 offen offset:2048 lds
v_lshlrev_b32_sdwa v41,1,v41 dst_sel:DWORD dst_unused:UNUSED_PAD src0_sel:DWORD src1_sel:WORD_1
v_mfma_f32_16x16x32_bf16 v[56:59],a[192:195],a[84:87],v[16:19]
ds_read_u16 v40,v40
v_lshlrev_b32_sdwa v43,1,v43 dst_sel:DWORD dst_unused:UNUSED_PAD src0_sel:DWORD src1_sel:WORD_1
v_mfma_f32_16x16x32_bf16 v[56:59],a[220:223],a[68:71],v[56:59]
ds_read_u16_d16_hi v42,v42
v_lshlrev_b32_sdwa v44,1,v44 dst_sel:DWORD dst_unused:UNUSED_PAD src0_sel:DWORD src1_sel:WORD_1
v_mfma_f32_16x16x32_bf16 v[56:59],a[216:219],a[52:55],v[56:59]
ds_read_u16 v41,v41
v_lshlrev_b32_sdwa v46,1,v46 dst_sel:DWORD dst_unused:UNUSED_PAD src0_sel:DWORD src1_sel:WORD_1
v_mfma_f32_16x16x32_bf16 v[136:139],a[228:231],v[60:63],v[136:139]
ds_read_u16_d16_hi v43,v43
v_lshlrev_b32_sdwa v45,1,v45 dst_sel:DWORD dst_unused:UNUSED_PAD src0_sel:DWORD src1_sel:WORD_1
v_mfma_f32_16x16x32_bf16 v[140:143],a[228:231],v[32:35],v[140:143]
ds_read_u16 v44,v44
v_lshlrev_b32_sdwa v47,1,v47 dst_sel:DWORD dst_unused:UNUSED_PAD src0_sel:DWORD src1_sel:WORD_1
v_mfma_f32_16x16x32_bf16 v[68:71],a[236:239],v[32:35],v[68:71]
ds_read_u16_d16_hi v46,v46
v_lshlrev_b32_sdwa v48,1,v48 dst_sel:DWORD dst_unused:UNUSED_PAD src0_sel:DWORD src1_sel:WORD_1
v_mfma_f32_16x16x32_bf16 v[64:67],a[236:239],v[60:63],v[64:67]
ds_read_u16 v45,v45
v_lshlrev_b32_sdwa v50,1,v50 dst_sel:DWORD dst_unused:UNUSED_PAD src0_sel:DWORD src1_sel:WORD_1
v_mfma_f32_16x16x32_bf16 v[144:147],a[244:247],v[60:63],v[144:147]
s_add_u32 m0,s62,12320
ds_read_u16_d16_hi v47,v47
v_lshlrev_b32_sdwa v49,1,v49 dst_sel:DWORD dst_unused:UNUSED_PAD src0_sel:DWORD src1_sel:WORD_1
v_mfma_f32_16x16x32_bf16 v[148:151],a[244:247],v[32:35],v[148:151]
s_add_u32 s68,s68,s59
ds_read_u16 v48,v48
v_lshlrev_b32_sdwa v51,1,v51 dst_sel:DWORD dst_unused:UNUSED_PAD src0_sel:DWORD src1_sel:WORD_1
v_mfma_f32_16x16x32_bf16 v[84:87],a[252:255],v[32:35],v[84:87]
buffer_load_dwordx4 v5,s[44:47],s68 offen offset:0 lds
v_mfma_f32_16x16x32_bf16 v[80:83],a[252:255],v[60:63],v[80:83]
s_waitcnt lgkmcnt(7)
ds_read_u16_d16_hi v50,v50
v_mfma_f32_16x16x32_bf16 v[152:155],a[232:235],v[60:63],v[152:155]
ds_read_u16 v49,v49
v_mfma_f32_16x16x32_bf16 v[156:159],a[232:235],v[32:35],v[156:159]
ds_read_u16_d16_hi v51,v51
v_mfma_f32_16x16x32_bf16 v[100:103],a[240:243],v[32:35],v[100:103]
v_mfma_f32_16x16x32_bf16 v[96:99],a[240:243],v[60:63],v[96:99]
v_mfma_f32_16x16x32_bf16 v[32:35],a[216:219],a[56:59],v[16:19]
v_lshlrev_b32_sdwa v24,1,v24 dst_sel:DWORD dst_unused:UNUSED_PAD src0_sel:DWORD src1_sel:WORD_1
v_fma_mixlo_f16 v8,v53,s99,0
v_mfma_f32_16x16x32_bf16 v[32:35],a[220:223],a[72:75],v[32:35]
ds_read_u16 v24,v24
v_lshl_add_u64 v[36:37],v[36:37],0,v[38:39]
v_mfma_f32_16x16x32_bf16 v[32:35],a[192:195],a[88:91],v[32:35]
s_waitcnt lgkmcnt(4)
v_lshl_add_u64 v[38:39],v[40:41],0,v[42:43]
v_lshlrev_b32_sdwa v26,1,v26 dst_sel:DWORD dst_unused:UNUSED_PAD src0_sel:DWORD src1_sel:WORD_1
v_mfma_f32_16x16x32_bf16 v[60:63],a[196:199],a[88:91],v[20:23]
ds_read_u16_d16_hi v26,v26
v_fma_mixhi_f16 v8,v55,s99,0
v_mfma_f32_16x16x32_bf16 v[60:63],a[200:203],a[72:75],v[60:63]
v_lshl_add_u64 v[40:41],v[44:45],0,v[46:47]
v_lshlrev_b32_sdwa v25,1,v25 dst_sel:DWORD dst_unused:UNUSED_PAD src0_sel:DWORD src1_sel:WORD_1
v_pk_fma_f16 v9,v8,v8,s54
v_mfma_f32_16x16x32_bf16 v[60:63],a[212:215],a[56:59],v[60:63]
ds_read_u16 v25,v25
v_lshlrev_b32_sdwa v27,1,v27 dst_sel:DWORD dst_unused:UNUSED_PAD src0_sel:DWORD src1_sel:WORD_1
v_mfma_f32_16x16x32_bf16 v[44:47],a[212:215],a[60:63],v[20:23]
ds_read_b128 v[20:23],v4 offset:896
v_pk_mul_f16 v9,v8,v9
v_mfma_f32_16x16x32_bf16 v[44:47],a[200:203],a[76:79],v[44:47]
s_waitcnt lgkmcnt(4)
ds_read_u16_d16_hi v27,v27
ds_read_b64_tr_b16 a[212:213],v1 offset:16416
v_lshl_add_u64 v[42:43],v[48:49],0,v[50:51]
v_lshlrev_b32_sdwa v28,1,v28 dst_sel:DWORD dst_unused:UNUSED_PAD src0_sel:DWORD src1_sel:WORD_1
v_mfma_f32_16x16x32_bf16 v[44:47],a[196:199],a[92:95],v[44:47]
ds_read_b64_tr_b16 a[214:215],v1 offset:17440
v_exp_f16 v8,v9
v_mfma_f32_16x16x32_bf16 v[48:51],a[192:195],a[92:95],v[16:19]
ds_read_b128 v[16:19],v4 offset:928
ds_read_u16 v28,v28
v_lshlrev_b32_sdwa v30,1,v30 dst_sel:DWORD dst_unused:UNUSED_PAD src0_sel:DWORD src1_sel:WORD_1
v_mfma_f32_16x16x32_bf16 v[48:51],a[220:223],a[76:79],v[48:51]
ds_read_u16_d16_hi v30,v30
ds_read_b64_tr_b16 a[200:201],v2 offset:16416
v_lshlrev_b32_sdwa v29,1,v29 dst_sel:DWORD dst_unused:UNUSED_PAD src0_sel:DWORD src1_sel:WORD_1
v_mfma_f32_16x16x32_bf16 v[48:51],a[216:219],a[60:63],v[48:51]
ds_read_u16 v29,v29
v_exp_f16_sdwa v8,v9 dst_sel:WORD_1 dst_unused:UNUSED_PRESERVE src0_sel:WORD_1
v_mfma_f32_16x16x32_bf16 v[112:115],a[228:231],v[36:39],v[112:115]
ds_read_b64_tr_b16 a[202:203],v2 offset:17440
ds_read_b64_tr_b16 a[196:197],v2 offset:14368
v_lshlrev_b32_sdwa v31,1,v31 dst_sel:DWORD dst_unused:UNUSED_PAD src0_sel:DWORD src1_sel:WORD_1
v_mfma_f32_16x16x32_bf16 v[116:119],a[228:231],v[40:43],v[116:119]
ds_read_u16_d16_hi v31,v31
v_lshlrev_b32_sdwa v56,1,v56 dst_sel:DWORD dst_unused:UNUSED_PAD src0_sel:DWORD src1_sel:WORD_1
v_mfma_f32_16x16x32_bf16 v[76:79],a[236:239],v[40:43],v[76:79]
ds_read_b64_tr_b16 a[198:199],v2 offset:15392
v_pk_fma_f16 v8,v8,s55,s55
v_mfma_f32_16x16x32_bf16 v[72:75],a[236:239],v[36:39],v[72:75]
v_rcp_f16 v9,v8
v_lshlrev_b32_sdwa v58,1,v58 dst_sel:DWORD dst_unused:UNUSED_PAD src0_sel:DWORD src1_sel:WORD_1
v_mfma_f32_16x16x32_bf16 v[120:123],a[244:247],v[36:39],v[120:123]
ds_read_u16 v56,v56
v_lshlrev_b32_sdwa v57,1,v57 dst_sel:DWORD dst_unused:UNUSED_PAD src0_sel:DWORD src1_sel:WORD_1
v_mfma_f32_16x16x32_bf16 v[124:127],a[244:247],v[40:43],v[124:127]
ds_read_u16_d16_hi v58,v58
v_lshlrev_b32_sdwa v59,1,v59 dst_sel:DWORD dst_unused:UNUSED_PAD src0_sel:DWORD src1_sel:WORD_1
v_mfma_f32_16x16x32_bf16 v[92:95],a[252:255],v[40:43],v[92:95]
ds_read_u16 v57,v57
v_lshlrev_b32_sdwa v52,1,v52 dst_sel:DWORD dst_unused:UNUSED_PAD src0_sel:DWORD src1_sel:WORD_1
v_mfma_f32_16x16x32_bf16 v[88:91],a[252:255],v[36:39],v[88:91]
ds_read_u16_d16_hi v59,v59
v_rcp_f16_sdwa v9,v8 dst_sel:WORD_1 dst_unused:UNUSED_PRESERVE src0_sel:WORD_1
v_mfma_f32_16x16x32_bf16 v[128:131],a[232:235],v[36:39],v[128:131]
ds_read_b64_tr_b16 a[192:193],v2 offset:12320
v_lshlrev_b32_sdwa v54,1,v54 dst_sel:DWORD dst_unused:UNUSED_PAD src0_sel:DWORD src1_sel:WORD_1
v_mfma_f32_16x16x32_bf16 v[132:135],a[232:235],v[40:43],v[132:135]
ds_read_b64_tr_b16 a[194:195],v2 offset:13344
v_fma_mix_f32 v53,v53,v9,0 op_sel_hi:[0,1,0]
v_mfma_f32_16x16x32_bf16 v[108:111],a[240:243],v[40:43],v[108:111]
ds_read_u16 v52,v52
v_fma_mix_f32 v55,v55,v9,0 op_sel:[0,1,0] op_sel_hi:[0,1,0]
v_mfma_f32_16x16x32_bf16 v[104:107],a[240:243],v[36:39],v[104:107]
s_waitcnt lgkmcnt(7)
ds_read_u16_d16_hi v54,v54
v_mfma_f32_16x16x32_bf16 v[40:43],a[208:211],a[0:3],v[20:23]
s_add_u32 s68,s68,s53
v_lshlrev_b32_sdwa v32,1,v32 dst_sel:DWORD dst_unused:UNUSED_PAD src0_sel:DWORD src1_sel:WORD_1
v_lshlrev_b32_sdwa v34,1,v34 dst_sel:DWORD dst_unused:UNUSED_PAD src0_sel:DWORD src1_sel:WORD_1
v_mfma_f32_16x16x32_bf16 v[40:43],a[204:207],a[16:19],v[40:43]
buffer_load_dwordx4 v5,s[44:47],s68 offen offset:1024 lds
v_lshl_add_u64 v[24:25],v[24:25],0,v[26:27]
v_mfma_f32_16x16x32_bf16 v[40:43],a[212:215],a[32:35],v[40:43]
s_add_u32 s68,s68,s53
ds_read_u16 v32,v32
v_lshl_add_u64 v[26:27],v[28:29],0,v[30:31]
v_mfma_f32_16x16x32_bf16 v[36:39],a[200:203],a[32:35],v[16:19]
buffer_load_dwordx4 v5,s[44:47],s68 offen offset:2048 lds
v_lshlrev_b32_sdwa v33,1,v33 dst_sel:DWORD dst_unused:UNUSED_PAD src0_sel:DWORD src1_sel:WORD_1
v_mfma_f32_16x16x32_bf16 v[36:39],a[196:199],a[16:19],v[36:39]
s_waitcnt lgkmcnt(3)
ds_read_u16_d16_hi v34,v34
v_lshl_add_u64 v[28:29],v[56:57],0,v[58:59]
v_mfma_f32_16x16x32_bf16 v[36:39],a[192:195],a[0:3],v[36:39]
ds_read_u16 v33,v33
v_lshlrev_b32_sdwa v35,1,v35 dst_sel:DWORD dst_unused:UNUSED_PAD src0_sel:DWORD src1_sel:WORD_1
v_mfma_f32_16x16x32_bf16 v[56:59],a[192:195],a[4:7],v[16:19]
s_waitcnt lgkmcnt(3)
ds_read_u16_d16_hi v35,v35
v_or_b32 v30,v52,v54
v_lshlrev_b32_sdwa v60,1,v60 dst_sel:DWORD dst_unused:UNUSED_PAD src0_sel:DWORD src1_sel:WORD_1
v_mfma_f32_16x16x32_bf16 v[56:59],a[196:199],a[20:23],v[56:59]
s_waitcnt vmcnt(17)
ds_read_u16 v60,v60
v_cvt_pk_bf16_f32 v31,v53,v55
v_lshlrev_b32_sdwa v62,1,v62 dst_sel:DWORD dst_unused:UNUSED_PAD src0_sel:DWORD src1_sel:WORD_1
v_mfma_f32_16x16x32_bf16 v[56:59],a[200:203],a[36:39],v[56:59]
s_barrier
ds_read_u16_d16_hi v62,v62
v_lshlrev_b32_sdwa v61,1,v61 dst_sel:DWORD dst_unused:UNUSED_PAD src0_sel:DWORD src1_sel:WORD_1
v_mfma_f32_16x16x32_bf16 v[52:55],a[212:215],a[36:39],v[20:23]
s_add_u32 m0,s62,18480
ds_read_u16 v61,v61
v_lshlrev_b32_sdwa v63,1,v63 dst_sel:DWORD dst_unused:UNUSED_PAD src0_sel:DWORD src1_sel:WORD_1
v_mfma_f32_16x16x32_bf16 v[52:55],a[204:207],a[20:23],v[52:55]
s_add_u32 s68,s68,s59
ds_read_u16_d16_hi v63,v63
v_lshlrev_b32_sdwa v48,1,v48 dst_sel:DWORD dst_unused:UNUSED_PAD src0_sel:DWORD src1_sel:WORD_1
v_mfma_f32_16x16x32_bf16 v[52:55],a[208:211],a[4:7],v[52:55]
buffer_load_dwordx4 v5,s[44:47],s68 offen offset:0 lds
v_lshlrev_b32_sdwa v50,1,v50 dst_sel:DWORD dst_unused:UNUSED_PAD src0_sel:DWORD src1_sel:WORD_1
v_mfma_f32_16x16x32_bf16 v[232:235],a[228:231],v[24:27],v[232:235]
s_add_u32 m0,s58,6160
ds_read_u16 v48,v48
v_lshlrev_b32_sdwa v49,1,v49 dst_sel:DWORD dst_unused:UNUSED_PAD src0_sel:DWORD src1_sel:WORD_1
v_mfma_f32_16x16x32_bf16 v[236:239],a[228:231],v[28:31],v[236:239]
s_add_u32 s69,s69,s59
ds_read_u16_d16_hi v50,v50
v_lshlrev_b32_sdwa v51,1,v51 dst_sel:DWORD dst_unused:UNUSED_PAD src0_sel:DWORD src1_sel:WORD_1
v_mfma_f32_16x16x32_bf16 v[164:167],a[236:239],v[28:31],v[164:167]
buffer_load_dwordx4 v5,s[44:47],s69 offen offset:0 lds
ds_read_u16 v49,v49
v_mfma_f32_16x16x32_bf16 v[160:163],a[236:239],v[24:27],v[160:163]
ds_read_u16_d16_hi v51,v51
ds_read_b64_tr_b16 a[224:225],v3 offset:18480
v_mfma_f32_16x16x32_bf16 v[240:243],a[244:247],v[24:27],v[240:243]
s_add_u32 s69,s69,s53
ds_read_b64_tr_b16 a[226:227],v3 offset:20016
v_lshlrev_b32_sdwa v44,1,v44 dst_sel:DWORD dst_unused:UNUSED_PAD src0_sel:DWORD src1_sel:WORD_1
v_mfma_f32_16x16x32_bf16 v[244:247],a[244:247],v[28:31],v[244:247]
buffer_load_dwordx4 v5,s[44:47],s69 offen offset:1024 lds
v_lshlrev_b32_sdwa v46,1,v46 dst_sel:DWORD dst_unused:UNUSED_PAD src0_sel:DWORD src1_sel:WORD_1
v_mfma_f32_16x16x32_bf16 v[180:183],a[252:255],v[28:31],v[180:183]
ds_read_u16 v44,v44
v_lshlrev_b32_sdwa v45,1,v45 dst_sel:DWORD dst_unused:UNUSED_PAD src0_sel:DWORD src1_sel:WORD_1
v_mfma_f32_16x16x32_bf16 v[176:179],a[252:255],v[24:27],v[176:179]
s_add_u32 m0,s62,18480
ds_read_u16_d16_hi v46,v46
v_lshlrev_b32_sdwa v47,1,v47 dst_sel:DWORD dst_unused:UNUSED_PAD src0_sel:DWORD src1_sel:WORD_1
v_mfma_f32_16x16x32_bf16 v[248:251],a[232:235],v[24:27],v[248:251]
s_add_u32 s68,s68,s53
ds_read_u16 v45,v45
v_mfma_f32_16x16x32_bf16 v[252:255],a[232:235],v[28:31],v[252:255]
buffer_load_dwordx4 v5,s[44:47],s68 offen offset:1024 lds
v_mfma_f32_16x16x32_bf16 v[196:199],a[240:243],v[28:31],v[196:199]
s_add_u32 m0,s58,6160
ds_read_u16_d16_hi v47,v47
v_mfma_f32_16x16x32_bf16 v[192:195],a[240:243],v[24:27],v[192:195]
s_add_u32 s69,s69,s53
ds_read_b64_tr_b16 a[248:249],v3 offset:18488
v_mfma_f32_16x16x32_bf16 v[28:31],a[208:211],a[8:11],v[20:23]
buffer_load_dwordx4 v5,s[44:47],s69 offen offset:2048 lds
v_lshlrev_b32_sdwa v40,1,v40 dst_sel:DWORD dst_unused:UNUSED_PAD src0_sel:DWORD src1_sel:WORD_1
v_mfma_f32_16x16x32_bf16 v[28:31],a[204:207],a[24:27],v[28:31]
s_waitcnt lgkmcnt(5)
ds_read_u16 v40,v40
v_lshl_add_u64 v[32:33],v[32:33],0,v[34:35]
v_fma_mixlo_f16 v8,v57,s99,0
v_mfma_f32_16x16x32_bf16 v[28:31],a[212:215],a[40:43],v[28:31]
ds_read_b64_tr_b16 a[250:251],v3 offset:20024
v_lshl_add_u64 v[34:35],v[60:61],0,v[62:63]
v_lshlrev_b32_sdwa v42,1,v42 dst_sel:DWORD dst_unused:UNUSED_PAD src0_sel:DWORD src1_sel:WORD_1
v_mfma_f32_16x16x32_bf16 v[24:27],a[200:203],a[40:43],v[16:19]
ds_read_u16_d16_hi v42,v42
v_fma_mixhi_f16 v8,v59,s99,0
v_mfma_f32_16x16x32_bf16 v[24:27],a[196:199],a[24:27],v[24:27]
ds_read_b64_tr_b16 a[220:221],v1 offset:18480
v_lshl_add_u64 v[60:61],v[48:49],0,v[50:51]
v_lshlrev_b32_sdwa v41,1,v41 dst_sel:DWORD dst_unused:UNUSED_PAD src0_sel:DWORD src1_sel:WORD_1
v_pk_fma_f16 v9,v8,v8,s54
v_mfma_f32_16x16x32_bf16 v[24:27],a[192:195],a[8:11],v[24:27]
ds_read_u16 v41,v41
v_lshlrev_b32_sdwa v43,1,v43 dst_sel:DWORD dst_unused:UNUSED_PAD src0_sel:DWORD src1_sel:WORD_1
v_mfma_f32_16x16x32_bf16 v[48:51],a[192:195],a[12:15],v[16:19]
ds_read_u16_d16_hi v43,v43
v_pk_mul_f16 v9,v8,v9
v_mfma_f32_16x16x32_bf16 v[48:51],a[196:199],a[28:31],v[48:51]
s_waitcnt lgkmcnt(7)
ds_read_b64_tr_b16 a[222:223],v1 offset:19504
v_lshl_add_u64 v[62:63],v[44:45],0,v[46:47]
v_lshlrev_b32_sdwa v36,1,v36 dst_sel:DWORD dst_unused:UNUSED_PAD src0_sel:DWORD src1_sel:WORD_1
v_mfma_f32_16x16x32_bf16 v[48:51],a[200:203],a[44:47],v[48:51]
v_exp_f16 v8,v9
v_mfma_f32_16x16x32_bf16 v[44:47],a[212:215],a[44:47],v[20:23]
ds_read_u16 v36,v36
v_lshlrev_b32_sdwa v38,1,v38 dst_sel:DWORD dst_unused:UNUSED_PAD src0_sel:DWORD src1_sel:WORD_1
v_mfma_f32_16x16x32_bf16 v[44:47],a[204:207],a[28:31],v[44:47]
ds_read_u16_d16_hi v38,v38
v_lshlrev_b32_sdwa v37,1,v37 dst_sel:DWORD dst_unused:UNUSED_PAD src0_sel:DWORD src1_sel:WORD_1
v_mfma_f32_16x16x32_bf16 v[44:47],a[208:211],a[12:15],v[44:47]
v_exp_f16_sdwa v8,v9 dst_sel:WORD_1 dst_unused:UNUSED_PRESERVE src0_sel:WORD_1
v_mfma_f32_16x16x32_bf16 v[208:211],a[228:231],v[32:35],v[208:211]
ds_read_u16 v37,v37
v_lshlrev_b32_sdwa v39,1,v39 dst_sel:DWORD dst_unused:UNUSED_PAD src0_sel:DWORD src1_sel:WORD_1
v_mfma_f32_16x16x32_bf16 v[212:215],a[228:231],v[60:63],v[212:215]
ds_read_u16_d16_hi v39,v39
v_lshlrev_b32_sdwa v52,1,v52 dst_sel:DWORD dst_unused:UNUSED_PAD src0_sel:DWORD src1_sel:WORD_1
v_mfma_f32_16x16x32_bf16 v[172:175],a[236:239],v[60:63],v[172:175]
ds_read_u16 v52,v52
v_pk_fma_f16 v8,v8,s55,s55
v_mfma_f32_16x16x32_bf16 v[168:171],a[236:239],v[32:35],v[168:171]
ds_read_b64_tr_b16 a[228:229],v3 offset:18544
v_rcp_f16_e64 v9,v8
v_lshlrev_b32_sdwa v54,1,v54 dst_sel:DWORD dst_unused:UNUSED_PAD src0_sel:DWORD src1_sel:WORD_1
v_mfma_f32_16x16x32_bf16 v[216:219],a[244:247],v[32:35],v[216:219]
ds_read_u16_d16_hi v54,v54
v_lshlrev_b32_sdwa v53,1,v53 dst_sel:DWORD dst_unused:UNUSED_PAD src0_sel:DWORD src1_sel:WORD_1
v_mfma_f32_16x16x32_bf16 v[220:223],a[244:247],v[60:63],v[220:223]
ds_read_u16 v53,v53
ds_read_b64_tr_b16 a[230:231],v3 offset:20080
v_mfma_f32_16x16x32_bf16 v[188:191],a[252:255],v[60:63],v[188:191]
ds_read_b64_tr_b16 a[236:237],v3 offset:18552
v_lshlrev_b32_sdwa v55,1,v55 dst_sel:DWORD dst_unused:UNUSED_PAD src0_sel:DWORD src1_sel:WORD_1
v_mfma_f32_16x16x32_bf16 v[184:187],a[252:255],v[32:35],v[184:187]
ds_read_u16_d16_hi v55,v55
v_rcp_f16_sdwa v9,v8 dst_sel:WORD_1 dst_unused:UNUSED_PRESERVE src0_sel:WORD_1
v_mfma_f32_16x16x32_bf16 v[224:227],a[232:235],v[32:35],v[224:227]
ds_read_b64_tr_b16 a[238:239],v3 offset:20088
ds_read_b64_tr_b16 a[244:245],v3 offset:18608
v_lshlrev_b32_sdwa v56,1,v56 dst_sel:DWORD dst_unused:UNUSED_PAD src0_sel:DWORD src1_sel:WORD_1
v_mfma_f32_16x16x32_bf16 v[228:231],a[232:235],v[60:63],v[228:231]
ds_read_u16 v56,v56
v_lshlrev_b32_sdwa v58,1,v58 dst_sel:DWORD dst_unused:UNUSED_PAD src0_sel:DWORD src1_sel:WORD_1
v_mfma_f32_16x16x32_bf16 v[204:207],a[240:243],v[60:63],v[204:207]
ds_read_u16_d16_hi v58,v58
v_fma_mix_f32 v57,v57,v9,0 op_sel_hi:[0,1,0]
v_mfma_f32_16x16x32_bf16 v[200:203],a[240:243],v[32:35],v[200:203]
ds_read_b64_tr_b16 a[246:247],v3 offset:20144
v_fma_mix_f32 v59,v59,v9,0 op_sel:[0,1,0] op_sel_hi:[0,1,0]
v_mfma_f32_16x16x32_bf16 v[60:63],a[208:211],a[48:51],v[20:23]
s_add_u32 m0,s58,12320
ds_read_b64_tr_b16 a[252:253],v3 offset:18616
v_lshlrev_b32_sdwa v28,1,v28 dst_sel:DWORD dst_unused:UNUSED_PAD src0_sel:DWORD src1_sel:WORD_1
v_mfma_f32_16x16x32_bf16 v[60:63],a[204:207],a[64:67],v[60:63]
s_waitcnt lgkmcnt(10)
ds_read_b64_tr_b16 a[254:255],v3 offset:20152
v_lshl_add_u64 v[40:41],v[40:41],0,v[42:43]
v_mfma_f32_16x16x32_bf16 v[60:63],a[212:215],a[80:83],v[60:63]
s_add_u32 s69,s69,s59
ds_read_u16 v28,v28
v_lshl_add_u64 v[42:43],v[36:37],0,v[38:39]
v_lshlrev_b32_sdwa v30,1,v30 dst_sel:DWORD dst_unused:UNUSED_PAD src0_sel:DWORD src1_sel:WORD_1
v_mfma_f32_16x16x32_bf16 v[32:35],a[200:203],a[80:83],v[16:19]
buffer_load_dwordx4 v5,s[44:47],s69 offen offset:0 lds
v_lshlrev_b32_sdwa v29,1,v29 dst_sel:DWORD dst_unused:UNUSED_PAD src0_sel:DWORD src1_sel:WORD_1
v_mfma_f32_16x16x32_bf16 v[32:35],a[196:199],a[64:67],v[32:35]
s_waitcnt lgkmcnt(8)
ds_read_u16_d16_hi v30,v30
v_lshl_add_u64 v[36:37],v[52:53],0,v[54:55]
TE12_%=:
s_cbranch_execz TP10726_%=
v_mfma_f32_16x16x32_bf16 v[32:35],a[192:195],a[48:51],v[32:35]
s_add_u32 s69,s69,s53
ds_read_u16 v29,v29
v_lshlrev_b32_sdwa v31,1,v31 dst_sel:DWORD dst_unused:UNUSED_PAD src0_sel:DWORD src1_sel:WORD_1
v_mfma_f32_16x16x32_bf16 v[52:55],a[192:195],a[52:55],v[16:19]
s_waitcnt lgkmcnt(6)
buffer_load_dwordx4 v5,s[44:47],s69 offen offset:1024 lds
v_or_b32_e64 v38,v56,v58
v_lshlrev_b32_sdwa v24,1,v24 dst_sel:DWORD dst_unused:UNUSED_PAD src0_sel:DWORD src1_sel:WORD_1
v_mfma_f32_16x16x32_bf16 v[52:55],a[196:199],a[68:71],v[52:55]
s_add_u32 m0,s62,18480
ds_read_u16_d16_hi v31,v31
v_cvt_pk_bf16_f32 v39,v57,v59
v_lshlrev_b32_sdwa v26,1,v26 dst_sel:DWORD dst_unused:UNUSED_PAD src0_sel:DWORD src1_sel:WORD_1
v_mfma_f32_16x16x32_bf16 v[52:55],a[200:203],a[84:87],v[52:55]
s_add_u32 s68,s68,s53
ds_read_u16 v24,v24
v_lshlrev_b32_sdwa v25,1,v25 dst_sel:DWORD dst_unused:UNUSED_PAD src0_sel:DWORD src1_sel:WORD_1
v_mfma_f32_16x16x32_bf16 v[56:59],a[212:215],a[84:87],v[20:23]
buffer_load_dwordx4 v5,s[44:47],s68 offen offset:2048 lds
ds_read_u16_d16_hi v26,v26
v_mfma_f32_16x16x32_bf16 v[56:59],a[204:207],a[68:71],v[56:59]
s_add_u32 m0,s58,12320
ds_read_u16 v25,v25
v_lshlrev_b32_sdwa v27,1,v27 dst_sel:DWORD dst_unused:UNUSED_PAD src0_sel:DWORD src1_sel:WORD_1
v_mfma_f32_16x16x32_bf16 v[56:59],a[208:211],a[52:55],v[56:59]
s_add_u32 s69,s69,s53
ds_read_u16_d16_hi v27,v27
v_lshlrev_b32_sdwa v44,1,v44 dst_sel:DWORD dst_unused:UNUSED_PAD src0_sel:DWORD src1_sel:WORD_1
v_mfma_f32_16x16x32_bf16 v[136:139],a[224:227],v[40:43],v[136:139]
buffer_load_dwordx4 v5,s[44:47],s69 offen offset:2048 lds
v_lshlrev_b32_sdwa v46,1,v46 dst_sel:DWORD dst_unused:UNUSED_PAD src0_sel:DWORD src1_sel:WORD_1
v_mfma_f32_16x16x32_bf16 v[140:143],a[224:227],v[36:39],v[140:143]
s_add_u32 m0,s62,24640
ds_read_u16 v44,v44
v_lshlrev_b32_sdwa v45,1,v45 dst_sel:DWORD dst_unused:UNUSED_PAD src0_sel:DWORD src1_sel:WORD_1
v_mfma_f32_16x16x32_bf16 v[68:71],a[248:251],v[36:39],v[68:71]
s_add_u32 s68,s68,s59
ds_read_u16_d16_hi v46,v46
v_lshlrev_b32_sdwa v47,1,v47 dst_sel:DWORD dst_unused:UNUSED_PAD src0_sel:DWORD src1_sel:WORD_1
v_mfma_f32_16x16x32_bf16 v[64:67],a[248:251],v[40:43],v[64:67]
buffer_load_dwordx4 v5,s[44:47],s68 offen offset:0 lds
ds_read_u16 v45,v45
v_mfma_f32_16x16x32_bf16 v[144:147],a[228:231],v[40:43],v[144:147]
s_add_u32 s68,s68,s53
ds_read_u16_d16_hi v47,v47
v_lshlrev_b32_sdwa v48,1,v48 dst_sel:DWORD dst_unused:UNUSED_PAD src0_sel:DWORD src1_sel:WORD_1
v_mfma_f32_16x16x32_bf16 v[148:151],a[228:231],v[36:39],v[148:151]
buffer_load_dwordx4 v5,s[44:47],s68 offen offset:1024 lds
v_lshlrev_b32_sdwa v50,1,v50 dst_sel:DWORD dst_unused:UNUSED_PAD src0_sel:DWORD src1_sel:WORD_1
v_mfma_f32_16x16x32_bf16 v[84:87],a[236:239],v[36:39],v[84:87]
ds_read_u16 v48,v48
v_lshlrev_b32_sdwa v49,1,v49 dst_sel:DWORD dst_unused:UNUSED_PAD src0_sel:DWORD src1_sel:WORD_1
v_mfma_f32_16x16x32_bf16 v[80:83],a[236:239],v[40:43],v[80:83]
s_waitcnt lgkmcnt(6)
ds_read_u16_d16_hi v50,v50
v_lshlrev_b32_sdwa v51,1,v51 dst_sel:DWORD dst_unused:UNUSED_PAD src0_sel:DWORD src1_sel:WORD_1
v_mfma_f32_16x16x32_bf16 v[152:155],a[244:247],v[40:43],v[152:155]
ds_read_u16 v49,v49
v_mfma_f32_16x16x32_bf16 v[156:159],a[244:247],v[36:39],v[156:159]
s_add_u32 s68,s68,s53
ds_read_u16_d16_hi v51,v51
v_mfma_f32_16x16x32_bf16 v[100:103],a[252:255],v[36:39],v[100:103]
buffer_load_dwordx4 v5,s[44:47],s68 offen offset:2048 lds
v_mfma_f32_16x16x32_bf16 v[96:99],a[252:255],v[40:43],v[96:99]
ds_read_b64_tr_b16 a[216:217],v1 offset:20528
v_mfma_f32_16x16x32_bf16 v[36:39],a[208:211],a[56:59],v[20:23]
ds_read_b64_tr_b16 a[218:219],v1 offset:21552
v_lshlrev_b32_sdwa v60,1,v60 dst_sel:DWORD dst_unused:UNUSED_PAD src0_sel:DWORD src1_sel:WORD_1
v_mfma_f32_16x16x32_bf16 v[36:39],a[204:207],a[72:75],v[36:39]
ds_read_u16 v60,v60
v_lshl_add_u64 v[28:29],v[28:29],0,v[30:31]
v_fma_mixlo_f16 v8,v53,s99,0
v_mfma_f32_16x16x32_bf16 v[36:39],a[212:215],a[88:91],v[36:39]
s_waitcnt lgkmcnt(7)
v_lshl_add_u64 v[30:31],v[24:25],0,v[26:27]
v_lshlrev_b32_sdwa v62,1,v62 dst_sel:DWORD dst_unused:UNUSED_PAD src0_sel:DWORD src1_sel:WORD_1
v_mfma_f32_16x16x32_bf16 v[40:43],a[200:203],a[88:91],v[16:19]
ds_read_u16_d16_hi v62,v62
v_fma_mixhi_f16 v8,v55,s99,0
v_mfma_f32_16x16x32_bf16 v[40:43],a[196:199],a[72:75],v[40:43]
v_lshl_add_u64 v[24:25],v[44:45],0,v[46:47]
v_lshlrev_b32_sdwa v61,1,v61 dst_sel:DWORD dst_unused:UNUSED_PAD src0_sel:DWORD src1_sel:WORD_1
v_pk_fma_f16 v9,v8,v8,s54
v_mfma_f32_16x16x32_bf16 v[40:43],a[192:195],a[56:59],v[40:43]
ds_read_u16 v61,v61
v_lshlrev_b32_sdwa v63,1,v63 dst_sel:DWORD dst_unused:UNUSED_PAD src0_sel:DWORD src1_sel:WORD_1
v_mfma_f32_16x16x32_bf16 v[44:47],a[192:195],a[60:63],v[16:19]
ds_read_b128 v[16:19],v4 offset:1024
v_pk_mul_f16 v9,v8,v9
v_mfma_f32_16x16x32_bf16 v[44:47],a[196:199],a[76:79],v[44:47]
s_waitcnt lgkmcnt(6)
ds_read_u16_d16_hi v63,v63
ds_read_b64_tr_b16 a[192:193],v1 offset:22576
v_lshl_add_u64 v[26:27],v[48:49],0,v[50:51]
v_lshlrev_b32_sdwa v32,1,v32 dst_sel:DWORD dst_unused:UNUSED_PAD src0_sel:DWORD src1_sel:WORD_1
v_mfma_f32_16x16x32_bf16 v[44:47],a[200:203],a[92:95],v[44:47]
ds_read_b64_tr_b16 a[194:195],v1 offset:23600
v_exp_f16_e64 v8,v9
v_mfma_f32_16x16x32_bf16 v[48:51],a[212:215],a[92:95],v[20:23]
ds_read_b128 v[20:23],v4 offset:1056
ds_read_u16 v32,v32
v_lshlrev_b32_sdwa v34,1,v34 dst_sel:DWORD dst_unused:UNUSED_PAD src0_sel:DWORD src1_sel:WORD_1
v_mfma_f32_16x16x32_bf16 v[48:51],a[204:207],a[76:79],v[48:51]
ds_read_u16_d16_hi v34,v34
ds_read_b64_tr_b16 a[196:197],v2 offset:22576
v_lshlrev_b32_sdwa v33,1,v33 dst_sel:DWORD dst_unused:UNUSED_PAD src0_sel:DWORD src1_sel:WORD_1
v_mfma_f32_16x16x32_bf16 v[48:51],a[208:211],a[60:63],v[48:51]
ds_read_u16 v33,v33
v_exp_f16_sdwa v8,v9 dst_sel:WORD_1 dst_unused:UNUSED_PRESERVE src0_sel:WORD_1
v_mfma_f32_16x16x32_bf16 v[112:115],a[224:227],v[28:31],v[112:115]
ds_read_b64_tr_b16 a[198:199],v2 offset:23600
ds_read_b64_tr_b16 a[200:201],v2 offset:20528
v_lshlrev_b32_sdwa v35,1,v35 dst_sel:DWORD dst_unused:UNUSED_PAD src0_sel:DWORD src1_sel:WORD_1
v_mfma_f32_16x16x32_bf16 v[116:119],a[224:227],v[24:27],v[116:119]
ds_read_u16_d16_hi v35,v35
v_lshlrev_b32_sdwa v56,1,v56 dst_sel:DWORD dst_unused:UNUSED_PAD src0_sel:DWORD src1_sel:WORD_1
v_mfma_f32_16x16x32_bf16 v[76:79],a[248:251],v[24:27],v[76:79]
ds_read_b64_tr_b16 a[202:203],v2 offset:21552
v_pk_fma_f16 v8,v8,s55,s55
v_mfma_f32_16x16x32_bf16 v[72:75],a[248:251],v[28:31],v[72:75]
v_rcp_f16 v9,v8
v_lshlrev_b32_sdwa v58,1,v58 dst_sel:DWORD dst_unused:UNUSED_PAD src0_sel:DWORD src1_sel:WORD_1
v_mfma_f32_16x16x32_bf16 v[120:123],a[228:231],v[28:31],v[120:123]
ds_read_u16 v56,v56
v_lshlrev_b32_sdwa v57,1,v57 dst_sel:DWORD dst_unused:UNUSED_PAD src0_sel:DWORD src1_sel:WORD_1
v_mfma_f32_16x16x32_bf16 v[124:127],a[228:231],v[24:27],v[124:127]
ds_read_u16_d16_hi v58,v58
v_lshlrev_b32_sdwa v59,1,v59 dst_sel:DWORD dst_unused:UNUSED_PAD src0_sel:DWORD src1_sel:WORD_1
v_mfma_f32_16x16x32_bf16 v[92:95],a[236:239],v[24:27],v[92:95]
ds_read_u16 v57,v57
v_lshlrev_b32_sdwa v52,1,v52 dst_sel:DWORD dst_unused:UNUSED_PAD src0_sel:DWORD src1_sel:WORD_1
v_mfma_f32_16x16x32_bf16 v[88:91],a[236:239],v[28:31],v[88:91]
ds_read_u16_d16_hi v59,v59
v_rcp_f16_sdwa v9,v8 dst_sel:WORD_1 dst_unused:UNUSED_PRESERVE src0_sel:WORD_1
v_mfma_f32_16x16x32_bf16 v[128:131],a[244:247],v[28:31],v[128:131]
ds_read_b64_tr_b16 a[212:213],v2 offset:18480
v_lshlrev_b32_sdwa v54,1,v54 dst_sel:DWORD dst_unused:UNUSED_PAD src0_sel:DWORD src1_sel:WORD_1
v_mfma_f32_16x16x32_bf16 v[132:135],a[244:247],v[24:27],v[132:135]
ds_read_b64_tr_b16 a[214:215],v2 offset:19504
v_fma_mix_f32 v53,v53,v9,0 op_sel_hi:[0,1,0]
v_mfma_f32_16x16x32_bf16 v[108:111],a[252:255],v[24:27],v[108:111]
ds_read_u16 v52,v52
v_fma_mix_f32 v55,v55,v9,0 op_sel:[0,1,0] op_sel_hi:[0,1,0]
v_mfma_f32_16x16x32_bf16 v[104:107],a[252:255],v[28:31],v[104:107]
s_waitcnt lgkmcnt(7)
ds_read_u16_d16_hi v54,v54
v_mfma_f32_16x16x32_bf16 v[24:27],a[220:223],a[0:3],v[16:19]
v_lshlrev_b32_sdwa v36,1,v36 dst_sel:DWORD dst_unused:UNUSED_PAD src0_sel:DWORD src1_sel:WORD_1
v_lshlrev_b32_sdwa v38,1,v38 dst_sel:DWORD dst_unused:UNUSED_PAD src0_sel:DWORD src1_sel:WORD_1
v_mfma_f32_16x16x32_bf16 v[24:27],a[216:219],a[16:19],v[24:27]
ds_read_u16 v36,v36
v_lshl_add_u64 v[60:61],v[60:61],0,v[62:63]
v_mfma_f32_16x16x32_bf16 v[24:27],a[192:195],a[32:35],v[24:27]
ds_read_u16_d16_hi v38,v38
v_lshl_add_u64 v[62:63],v[32:33],0,v[34:35]
v_mfma_f32_16x16x32_bf16 v[28:31],a[196:199],a[32:35],v[20:23]
v_lshlrev_b32_sdwa v37,1,v37 dst_sel:DWORD dst_unused:UNUSED_PAD src0_sel:DWORD src1_sel:WORD_1
v_lshlrev_b32_sdwa v39,1,v39 dst_sel:DWORD dst_unused:UNUSED_PAD src0_sel:DWORD src1_sel:WORD_1
v_mfma_f32_16x16x32_bf16 v[28:31],a[200:203],a[16:19],v[28:31]
s_waitcnt lgkmcnt(4)
ds_read_u16 v37,v37
v_lshl_add_u64 v[32:33],v[56:57],0,v[58:59]
v_mfma_f32_16x16x32_bf16 v[28:31],a[212:215],a[0:3],v[28:31]
ds_read_u16_d16_hi v39,v39
v_lshlrev_b32_sdwa v40,1,v40 dst_sel:DWORD dst_unused:UNUSED_PAD src0_sel:DWORD src1_sel:WORD_1
v_mfma_f32_16x16x32_bf16 v[56:59],a[212:215],a[4:7],v[20:23]
s_waitcnt lgkmcnt(4)
ds_read_u16 v40,v40
v_or_b32 v34,v52,v54
v_mfma_f32_16x16x32_bf16 v[56:59],a[200:203],a[20:23],v[56:59]
v_cvt_pk_bf16_f32 v35,v53,v55
v_lshlrev_b32_sdwa v42,1,v42 dst_sel:DWORD dst_unused:UNUSED_PAD src0_sel:DWORD src1_sel:WORD_1
v_mfma_f32_16x16x32_bf16 v[56:59],a[196:199],a[36:39],v[56:59]
ds_read_u16_d16_hi v42,v42
v_lshlrev_b32_sdwa v41,1,v41 dst_sel:DWORD dst_unused:UNUSED_PAD src0_sel:DWORD src1_sel:WORD_1
v_mfma_f32_16x16x32_bf16 v[52:55],a[192:195],a[36:39],v[16:19]
ds_read_u16 v41,v41
v_lshlrev_b32_sdwa v43,1,v43 dst_sel:DWORD dst_unused:UNUSED_PAD src0_sel:DWORD src1_sel:WORD_1
v_mfma_f32_16x16x32_bf16 v[52:55],a[216:219],a[20:23],v[52:55]
ds_read_u16_d16_hi v43,v43
v_lshlrev_b32_sdwa v48,1,v48 dst_sel:DWORD dst_unused:UNUSED_PAD src0_sel:DWORD src1_sel:WORD_1
v_mfma_f32_16x16x32_bf16 v[52:55],a[220:223],a[4:7],v[52:55]
ds_read_u16 v48,v48
v_lshlrev_b32_sdwa v50,1,v50 dst_sel:DWORD dst_unused:UNUSED_PAD src0_sel:DWORD src1_sel:WORD_1
v_mfma_f32_16x16x32_bf16 v[232:235],a[224:227],v[60:63],v[232:235]
ds_read_u16_d16_hi v50,v50
v_lshlrev_b32_sdwa v49,1,v49 dst_sel:DWORD dst_unused:UNUSED_PAD src0_sel:DWORD src1_sel:WORD_1
v_mfma_f32_16x16x32_bf16 v[236:239],a[224:227],v[32:35],v[236:239]
ds_read_u16 v49,v49
v_lshlrev_b32_sdwa v51,1,v51 dst_sel:DWORD dst_unused:UNUSED_PAD src0_sel:DWORD src1_sel:WORD_1
v_mfma_f32_16x16x32_bf16 v[164:167],a[248:251],v[32:35],v[164:167]
ds_read_u16_d16_hi v51,v51
ds_read_b64_tr_b16 a[232:233],v3 offset:0
v_mfma_f32_16x16x32_bf16 v[160:163],a[248:251],v[60:63],v[160:163]
ds_read_b64_tr_b16 a[234:235],v3 offset:1536
v_lshlrev_b32_sdwa v44,1,v44 dst_sel:DWORD dst_unused:UNUSED_PAD src0_sel:DWORD src1_sel:WORD_1
v_mfma_f32_16x16x32_bf16 v[240:243],a[228:231],v[60:63],v[240:243]
ds_read_u16 v44,v44
v_lshlrev_b32_sdwa v46,1,v46 dst_sel:DWORD dst_unused:UNUSED_PAD src0_sel:DWORD src1_sel:WORD_1
v_mfma_f32_16x16x32_bf16 v[244:247],a[228:231],v[32:35],v[244:247]
ds_read_u16_d16_hi v46,v46
v_lshlrev_b32_sdwa v45,1,v45 dst_sel:DWORD dst_unused:UNUSED_PAD src0_sel:DWORD src1_sel:WORD_1
v_mfma_f32_16x16x32_bf16 v[180:183],a[236:239],v[32:35],v[180:183]
ds_read_u16 v45,v45
v_lshlrev_b32_sdwa v47,1,v47 dst_sel:DWORD dst_unused:UNUSED_PAD src0_sel:DWORD src1_sel:WORD_1
v_mfma_f32_16x16x32_bf16 v[176:179],a[236:239],v[60:63],v[176:179]
ds_read_u16_d16_hi v47,v47
v_mfma_f32_16x16x32_bf16 v[248:251],a[244:247],v[60:63],v[248:251]
ds_read_b64_tr_b16 a[240:241],v3 offset:8
v_mfma_f32_16x16x32_bf16 v[252:255],a[244:247],v[32:35],v[252:255]
ds_read_b64_tr_b16 a[242:243],v3 offset:1544
v_mfma_f32_16x16x32_bf16 v[196:199],a[252:255],v[32:35],v[196:199]
v_mfma_f32_16x16x32_bf16 v[192:195],a[252:255],v[60:63],v[192:195]
v_mfma_f32_16x16x32_bf16 v[32:35],a[220:223],a[8:11],v[16:19]
ds_read_b64_tr_b16 a[204:205],v1 offset:0
v_lshlrev_b32_sdwa v24,1,v24 dst_sel:DWORD dst_unused:UNUSED_PAD src0_sel:DWORD src1_sel:WORD_1
v_mfma_f32_16x16x32_bf16 v[32:35],a[216:219],a[24:27],v[32:35]
s_waitcnt lgkmcnt(6)
ds_read_u16 v24,v24
v_lshl_add_u64 v[36:37],v[36:37],0,v[38:39]
v_fma_mixlo_f16 v8,v57,s99,0
v_mfma_f32_16x16x32_bf16 v[32:35],a[192:195],a[40:43],v[32:35]
ds_read_b64_tr_b16 a[206:207],v1 offset:1024
v_lshl_add_u64 v[38:39],v[40:41],0,v[42:43]
v_lshlrev_b32_sdwa v26,1,v26 dst_sel:DWORD dst_unused:UNUSED_PAD src0_sel:DWORD src1_sel:WORD_1
v_mfma_f32_16x16x32_bf16 v[60:63],a[196:199],a[40:43],v[20:23]
ds_read_u16_d16_hi v26,v26
v_fma_mixhi_f16 v8,v59,s99,0
v_mfma_f32_16x16x32_bf16 v[60:63],a[200:203],a[24:27],v[60:63]
ds_read_b64_tr_b16 a[208:209],v1 offset:2048
v_lshl_add_u64 v[40:41],v[48:49],0,v[50:51]
v_lshlrev_b32_sdwa v25,1,v25 dst_sel:DWORD dst_unused:UNUSED_PAD src0_sel:DWORD src1_sel:WORD_1
v_pk_fma_f16 v9,v8,v8,s54
v_mfma_f32_16x16x32_bf16 v[60:63],a[212:215],a[8:11],v[60:63]
ds_read_u16 v25,v25
v_lshlrev_b32_sdwa v27,1,v27 dst_sel:DWORD dst_unused:UNUSED_PAD src0_sel:DWORD src1_sel:WORD_1
v_mfma_f32_16x16x32_bf16 v[48:51],a[212:215],a[12:15],v[20:23]
ds_read_u16_d16_hi v27,v27
v_pk_mul_f16 v9,v8,v9
v_mfma_f32_16x16x32_bf16 v[48:51],a[200:203],a[28:31],v[48:51]
s_waitcnt lgkmcnt(7)
ds_read_b64_tr_b16 a[210:211],v1 offset:3072
v_lshl_add_u64 v[42:43],v[44:45],0,v[46:47]
v_lshlrev_b32_sdwa v28,1,v28 dst_sel:DWORD dst_unused:UNUSED_PAD src0_sel:DWORD src1_sel:WORD_1
v_mfma_f32_16x16x32_bf16 v[48:51],a[196:199],a[44:47],v[48:51]
v_exp_f16 v8,v9
v_mfma_f32_16x16x32_bf16 v[44:47],a[192:195],a[44:47],v[16:19]
ds_read_u16 v28,v28
v_lshlrev_b32_sdwa v30,1,v30 dst_sel:DWORD dst_unused:UNUSED_PAD src0_sel:DWORD src1_sel:WORD_1
v_mfma_f32_16x16x32_bf16 v[44:47],a[216:219],a[28:31],v[44:47]
ds_read_u16_d16_hi v30,v30
v_lshlrev_b32_sdwa v29,1,v29 dst_sel:DWORD dst_unused:UNUSED_PAD src0_sel:DWORD src1_sel:WORD_1
v_mfma_f32_16x16x32_bf16 v[44:47],a[220:223],a[12:15],v[44:47]
v_exp_f16_sdwa v8,v9 dst_sel:WORD_1 dst_unused:UNUSED_PRESERVE src0_sel:WORD_1
v_mfma_f32_16x16x32_bf16 v[208:211],a[224:227],v[36:39],v[208:211]
ds_read_u16 v29,v29
v_lshlrev_b32_sdwa v31,1,v31 dst_sel:DWORD dst_unused:UNUSED_PAD src0_sel:DWORD src1_sel:WORD_1
v_mfma_f32_16x16x32_bf16 v[212:215],a[224:227],v[40:43],v[212:215]
ds_read_u16_d16_hi v31,v31
v_lshlrev_b32_sdwa v52,1,v52 dst_sel:DWORD dst_unused:UNUSED_PAD src0_sel:DWORD src1_sel:WORD_1
v_mfma_f32_16x16x32_bf16 v[172:175],a[248:251],v[40:43],v[172:175]
ds_read_u16 v52,v52
v_pk_fma_f16 v8,v8,s55,s55
v_mfma_f32_16x16x32_bf16 v[168:171],a[248:251],v[36:39],v[168:171]
ds_read_b64_tr_b16 a[224:225],v3 offset:64
v_rcp_f16 v9,v8
v_lshlrev_b32_sdwa v54,1,v54 dst_sel:DWORD dst_unused:UNUSED_PAD src0_sel:DWORD src1_sel:WORD_1
v_mfma_f32_16x16x32_bf16 v[216:219],a[228:231],v[36:39],v[216:219]
ds_read_u16_d16_hi v54,v54
v_lshlrev_b32_sdwa v53,1,v53 dst_sel:DWORD dst_unused:UNUSED_PAD src0_sel:DWORD src1_sel:WORD_1
v_mfma_f32_16x16x32_bf16 v[220:223],a[228:231],v[40:43],v[220:223]
ds_read_u16 v53,v53
ds_read_b64_tr_b16 a[226:227],v3 offset:1600
v_mfma_f32_16x16x32_bf16 v[188:191],a[236:239],v[40:43],v[188:191]
ds_read_b64_tr_b16 a[248:249],v3 offset:72
v_lshlrev_b32_sdwa v55,1,v55 dst_sel:DWORD dst_unused:UNUSED_PAD src0_sel:DWORD src1_sel:WORD_1
v_mfma_f32_16x16x32_bf16 v[184:187],a[236:239],v[36:39],v[184:187]
ds_read_u16_d16_hi v55,v55
v_rcp_f16_sdwa v9,v8 dst_sel:WORD_1 dst_unused:UNUSED_PRESERVE src0_sel:WORD_1
v_mfma_f32_16x16x32_bf16 v[224:227],a[244:247],v[36:39],v[224:227]
ds_read_b64_tr_b16 a[250:251],v3 offset:1608
ds_read_b64_tr_b16 a[228:229],v3 offset:128
v_lshlrev_b32_sdwa v56,1,v56 dst_sel:DWORD dst_unused:UNUSED_PAD src0_sel:DWORD src1_sel:WORD_1
v_mfma_f32_16x16x32_bf16 v[228:231],a[244:247],v[40:43],v[228:231]
ds_read_u16 v56,v56
v_lshlrev_b32_sdwa v58,1,v58 dst_sel:DWORD dst_unused:UNUSED_PAD src0_sel:DWORD src1_sel:WORD_1
v_mfma_f32_16x16x32_bf16 v[204:207],a[252:255],v[40:43],v[204:207]
ds_read_u16_d16_hi v58,v58
v_fma_mix_f32 v57,v57,v9,0 op_sel_hi:[0,1,0]
v_mfma_f32_16x16x32_bf16 v[200:203],a[252:255],v[36:39],v[200:203]
ds_read_b64_tr_b16 a[230:231],v3 offset:1664
v_fma_mix_f32 v59,v59,v9,0 op_sel:[0,1,0] op_sel_hi:[0,1,0]
v_mbcnt_lo_u32_b32 v8,-1,0
N_qs20_%=:
s_add_u32 s64,s16,N_qs20_%=-N_pf_%=+0
v_and_b32 v8,3,v8
s_mov_b32 m0,12288
v_lshl_add_u32 v8,v8,7,s64
v_min_u32 v8,N_mainend_%=-N_pf_%=+2048,v8
global_load_lds_dword v8,s[14:15]
v_mfma_f32_16x16x32_bf16 v[40:43],a[220:223],a[48:51],v[16:19]
ds_read_b64_tr_b16 a[236:237],v3 offset:136
v_lshlrev_b32_sdwa v32,1,v32 dst_sel:DWORD dst_unused:UNUSED_PAD src0_sel:DWORD src1_sel:WORD_1
v_mfma_f32_16x16x32_bf16 v[40:43],a[216:219],a[64:67],v[40:43]
s_waitcnt lgkmcnt(10)
ds_read_b64_tr_b16 a[238:239],v3 offset:1672
v_lshl_add_u64 v[24:25],v[24:25],0,v[26:27]
v_mfma_f32_16x16x32_bf16 v[40:43],a[192:195],a[80:83],v[40:43]
ds_read_u16 v32,v32
v_lshl_add_u64 v[26:27],v[28:29],0,v[30:31]
v_lshlrev_b32_sdwa v34,1,v34 dst_sel:DWORD dst_unused:UNUSED_PAD src0_sel:DWORD src1_sel:WORD_1
v_mfma_f32_16x16x32_bf16 v[36:39],a[196:199],a[80:83],v[20:23]
ds_read_u16_d16_hi v34,v34
v_lshlrev_b32_sdwa v33,1,v33 dst_sel:DWORD dst_unused:UNUSED_PAD src0_sel:DWORD src1_sel:WORD_1
v_mfma_f32_16x16x32_bf16 v[36:39],a[200:203],a[64:67],v[36:39]
s_waitcnt lgkmcnt(9)
ds_read_u16 v33,v33
v_lshl_add_u64 v[28:29],v[52:53],0,v[54:55]
v_mfma_f32_16x16x32_bf16 v[36:39],a[212:215],a[48:51],v[36:39]
v_lshlrev_b32_sdwa v35,1,v35 dst_sel:DWORD dst_unused:UNUSED_PAD src0_sel:DWORD src1_sel:WORD_1
v_lshlrev_b32_sdwa v60,1,v60 dst_sel:DWORD dst_unused:UNUSED_PAD src0_sel:DWORD src1_sel:WORD_1
v_mfma_f32_16x16x32_bf16 v[52:55],a[212:215],a[52:55],v[20:23]
s_waitcnt lgkmcnt(6)
ds_read_u16_d16_hi v35,v35
v_or_b32 v30,v56,v58
v_mfma_f32_16x16x32_bf16 v[52:55],a[200:203],a[68:71],v[52:55]
ds_read_u16 v60,v60
v_cvt_pk_bf16_f32 v31,v57,v59
v_lshlrev_b32_sdwa v62,1,v62 dst_sel:DWORD dst_unused:UNUSED_PAD src0_sel:DWORD src1_sel:WORD_1
v_mfma_f32_16x16x32_bf16 v[52:55],a[196:199],a[84:87],v[52:55]
ds_read_u16_d16_hi v62,v62
v_lshlrev_b32_sdwa v61,1,v61 dst_sel:DWORD dst_unused:UNUSED_PAD src0_sel:DWORD src1_sel:WORD_1
v_mfma_f32_16x16x32_bf16 v[56:59],a[192:195],a[84:87],v[16:19]
ds_read_u16 v61,v61
v_lshlrev_b32_sdwa v63,1,v63 dst_sel:DWORD dst_unused:UNUSED_PAD src0_sel:DWORD src1_sel:WORD_1
v_mfma_f32_16x16x32_bf16 v[56:59],a[216:219],a[68:71],v[56:59]
ds_read_u16_d16_hi v63,v63
v_lshlrev_b32_sdwa v44,1,v44 dst_sel:DWORD dst_unused:UNUSED_PAD src0_sel:DWORD src1_sel:WORD_1
v_mfma_f32_16x16x32_bf16 v[56:59],a[220:223],a[52:55],v[56:59]
ds_read_u16 v44,v44
v_lshlrev_b32_sdwa v46,1,v46 dst_sel:DWORD dst_unused:UNUSED_PAD src0_sel:DWORD src1_sel:WORD_1
v_mfma_f32_16x16x32_bf16 v[136:139],a[232:235],v[24:27],v[136:139]
ds_read_u16_d16_hi v46,v46
v_lshlrev_b32_sdwa v45,1,v45 dst_sel:DWORD dst_unused:UNUSED_PAD src0_sel:DWORD src1_sel:WORD_1
v_mfma_f32_16x16x32_bf16 v[140:143],a[232:235],v[28:31],v[140:143]
ds_read_u16 v45,v45
v_lshlrev_b32_sdwa v47,1,v47 dst_sel:DWORD dst_unused:UNUSED_PAD src0_sel:DWORD src1_sel:WORD_1
v_mfma_f32_16x16x32_bf16 v[68:71],a[240:243],v[28:31],v[68:71]
ds_read_u16_d16_hi v47,v47
v_lshlrev_b32_sdwa v48,1,v48 dst_sel:DWORD dst_unused:UNUSED_PAD src0_sel:DWORD src1_sel:WORD_1
v_mfma_f32_16x16x32_bf16 v[64:67],a[240:243],v[24:27],v[64:67]
ds_read_u16 v48,v48
v_lshlrev_b32_sdwa v50,1,v50 dst_sel:DWORD dst_unused:UNUSED_PAD src0_sel:DWORD src1_sel:WORD_1
v_mfma_f32_16x16x32_bf16 v[144:147],a[224:227],v[24:27],v[144:147]
ds_read_u16_d16_hi v50,v50
v_lshlrev_b32_sdwa v49,1,v49 dst_sel:DWORD dst_unused:UNUSED_PAD src0_sel:DWORD src1_sel:WORD_1
v_mfma_f32_16x16x32_bf16 v[148:151],a[224:227],v[28:31],v[148:151]
ds_read_u16 v49,v49
v_lshlrev_b32_sdwa v51,1,v51 dst_sel:DWORD dst_unused:UNUSED_PAD src0_sel:DWORD src1_sel:WORD_1
v_mfma_f32_16x16x32_bf16 v[84:87],a[248:251],v[28:31],v[84:87]
ds_read_u16_d16_hi v51,v51
v_mfma_f32_16x16x32_bf16 v[80:83],a[248:251],v[24:27],v[80:83]
s_waitcnt lgkmcnt(8)
v_mfma_f32_16x16x32_bf16 v[152:155],a[228:231],v[24:27],v[152:155]
v_mfma_f32_16x16x32_bf16 v[156:159],a[228:231],v[28:31],v[156:159]
v_mfma_f32_16x16x32_bf16 v[100:103],a[236:239],v[28:31],v[100:103]
v_mfma_f32_16x16x32_bf16 v[96:99],a[236:239],v[24:27],v[96:99]
v_mfma_f32_16x16x32_bf16 v[28:31],a[220:223],a[56:59],v[16:19]
v_lshlrev_b32_sdwa v40,1,v40 dst_sel:DWORD dst_unused:UNUSED_PAD src0_sel:DWORD src1_sel:WORD_1
v_fma_mixlo_f16 v8,v53,s99,0
v_mfma_f32_16x16x32_bf16 v[28:31],a[216:219],a[72:75],v[28:31]
ds_read_u16 v40,v40
v_lshl_add_u64 v[32:33],v[32:33],0,v[34:35]
v_mfma_f32_16x16x32_bf16 v[28:31],a[192:195],a[88:91],v[28:31]
v_lshl_add_u64 v[34:35],v[60:61],0,v[62:63]
v_lshlrev_b32_sdwa v42,1,v42 dst_sel:DWORD dst_unused:UNUSED_PAD src0_sel:DWORD src1_sel:WORD_1
v_mfma_f32_16x16x32_bf16 v[24:27],a[196:199],a[88:91],v[20:23]
ds_read_u16_d16_hi v42,v42
v_fma_mixhi_f16 v8,v55,s99,0
v_mfma_f32_16x16x32_bf16 v[24:27],a[200:203],a[72:75],v[24:27]
s_waitcnt lgkmcnt(2)
v_lshl_add_u64 v[60:61],v[44:45],0,v[46:47]
v_lshlrev_b32_sdwa v41,1,v41 dst_sel:DWORD dst_unused:UNUSED_PAD src0_sel:DWORD src1_sel:WORD_1
v_pk_fma_f16 v9,v8,v8,s54
v_mfma_f32_16x16x32_bf16 v[24:27],a[212:215],a[56:59],v[24:27]
ds_read_u16 v41,v41
v_lshlrev_b32_sdwa v43,1,v43 dst_sel:DWORD dst_unused:UNUSED_PAD src0_sel:DWORD src1_sel:WORD_1
v_mfma_f32_16x16x32_bf16 v[44:47],a[212:215],a[60:63],v[20:23]
ds_read_b128 v[20:23],v4 offset:1152
v_pk_mul_f16 v9,v8,v9
v_mfma_f32_16x16x32_bf16 v[44:47],a[200:203],a[76:79],v[44:47]
ds_read_u16_d16_hi v43,v43
ds_read_b64_tr_b16 a[212:213],v1 offset:4096
v_lshl_add_u64 v[62:63],v[48:49],0,v[50:51]
v_lshlrev_b32_sdwa v36,1,v36 dst_sel:DWORD dst_unused:UNUSED_PAD src0_sel:DWORD src1_sel:WORD_1
v_mfma_f32_16x16x32_bf16 v[44:47],a[196:199],a[92:95],v[44:47]
ds_read_b64_tr_b16 a[214:215],v1 offset:5120
v_exp_f16 v8,v9
v_mfma_f32_16x16x32_bf16 v[48:51],a[192:195],a[92:95],v[16:19]
ds_read_b128 v[16:19],v4 offset:1184
ds_read_u16 v36,v36
v_lshlrev_b32_sdwa v38,1,v38 dst_sel:DWORD dst_unused:UNUSED_PAD src0_sel:DWORD src1_sel:WORD_1
v_mfma_f32_16x16x32_bf16 v[48:51],a[216:219],a[76:79],v[48:51]
ds_read_u16_d16_hi v38,v38
ds_read_b64_tr_b16 a[200:201],v2 offset:4096
v_lshlrev_b32_sdwa v37,1,v37 dst_sel:DWORD dst_unused:UNUSED_PAD src0_sel:DWORD src1_sel:WORD_1
v_mfma_f32_16x16x32_bf16 v[48:51],a[220:223],a[60:63],v[48:51]
ds_read_u16 v37,v37
v_exp_f16_sdwa v8,v9 dst_sel:WORD_1 dst_unused:UNUSED_PRESERVE src0_sel:WORD_1
v_mfma_f32_16x16x32_bf16 v[112:115],a[232:235],v[32:35],v[112:115]
ds_read_b64_tr_b16 a[202:203],v2 offset:5120
ds_read_b64_tr_b16 a[196:197],v2 offset:2048
v_lshlrev_b32_sdwa v39,1,v39 dst_sel:DWORD dst_unused:UNUSED_PAD src0_sel:DWORD src1_sel:WORD_1
v_mfma_f32_16x16x32_bf16 v[116:119],a[232:235],v[60:63],v[116:119]
ds_read_u16_d16_hi v39,v39
v_lshlrev_b32_sdwa v56,1,v56 dst_sel:DWORD dst_unused:UNUSED_PAD src0_sel:DWORD src1_sel:WORD_1
v_mfma_f32_16x16x32_bf16 v[76:79],a[240:243],v[60:63],v[76:79]
ds_read_b64_tr_b16 a[198:199],v2 offset:3072
v_pk_fma_f16 v8,v8,s55,s55
v_mfma_f32_16x16x32_bf16 v[72:75],a[240:243],v[32:35],v[72:75]
v_rcp_f16 v9,v8
v_lshlrev_b32_sdwa v58,1,v58 dst_sel:DWORD dst_unused:UNUSED_PAD src0_sel:DWORD src1_sel:WORD_1
v_mfma_f32_16x16x32_bf16 v[120:123],a[224:227],v[32:35],v[120:123]
ds_read_u16 v56,v56
v_lshlrev_b32_sdwa v57,1,v57 dst_sel:DWORD dst_unused:UNUSED_PAD src0_sel:DWORD src1_sel:WORD_1
v_mfma_f32_16x16x32_bf16 v[124:127],a[224:227],v[60:63],v[124:127]
ds_read_u16_d16_hi v58,v58
v_lshlrev_b32_sdwa v59,1,v59 dst_sel:DWORD dst_unused:UNUSED_PAD src0_sel:DWORD src1_sel:WORD_1
v_mfma_f32_16x16x32_bf16 v[92:95],a[248:251],v[60:63],v[92:95]
ds_read_u16 v57,v57
v_lshlrev_b32_sdwa v52,1,v52 dst_sel:DWORD dst_unused:UNUSED_PAD src0_sel:DWORD src1_sel:WORD_1
v_mfma_f32_16x16x32_bf16 v[88:91],a[248:251],v[32:35],v[88:91]
ds_read_u16_d16_hi v59,v59
v_rcp_f16_sdwa v9,v8 dst_sel:WORD_1 dst_unused:UNUSED_PRESERVE src0_sel:WORD_1
v_mfma_f32_16x16x32_bf16 v[128:131],a[228:231],v[32:35],v[128:131]
ds_read_b64_tr_b16 a[192:193],v2 offset:0
v_lshlrev_b32_sdwa v54,1,v54 dst_sel:DWORD dst_unused:UNUSED_PAD src0_sel:DWORD src1_sel:WORD_1
v_mfma_f32_16x16x32_bf16 v[132:135],a[228:231],v[60:63],v[132:135]
ds_read_b64_tr_b16 a[194:195],v2 offset:1024
v_fma_mix_f32 v53,v53,v9,0 op_sel_hi:[0,1,0]
v_mfma_f32_16x16x32_bf16 v[108:111],a[236:239],v[60:63],v[108:111]
ds_read_u16 v52,v52
v_fma_mix_f32 v55,v55,v9,0 op_sel:[0,1,0] op_sel_hi:[0,1,0]
v_mfma_f32_16x16x32_bf16 v[104:107],a[236:239],v[32:35],v[104:107]
s_waitcnt lgkmcnt(7)
ds_read_u16_d16_hi v54,v54
v_mfma_f32_16x16x32_bf16 v[60:63],a[204:207],a[0:3],v[20:23]
v_lshlrev_b32_sdwa v28,1,v28 dst_sel:DWORD dst_unused:UNUSED_PAD src0_sel:DWORD src1_sel:WORD_1
v_lshlrev_b32_sdwa v30,1,v30 dst_sel:DWORD dst_unused:UNUSED_PAD src0_sel:DWORD src1_sel:WORD_1
v_mfma_f32_16x16x32_bf16 v[60:63],a[208:211],a[16:19],v[60:63]
ds_read_u16 v28,v28
v_lshl_add_u64 v[40:41],v[40:41],0,v[42:43]
v_mfma_f32_16x16x32_bf16 v[60:63],a[212:215],a[32:35],v[60:63]
ds_read_u16_d16_hi v30,v30
v_lshl_add_u64 v[42:43],v[36:37],0,v[38:39]
v_mfma_f32_16x16x32_bf16 v[32:35],a[200:203],a[32:35],v[16:19]
v_lshlrev_b32_sdwa v29,1,v29 dst_sel:DWORD dst_unused:UNUSED_PAD src0_sel:DWORD src1_sel:WORD_1
v_lshlrev_b32_sdwa v31,1,v31 dst_sel:DWORD dst_unused:UNUSED_PAD src0_sel:DWORD src1_sel:WORD_1
v_mfma_f32_16x16x32_bf16 v[32:35],a[196:199],a[16:19],v[32:35]
s_waitcnt lgkmcnt(4)
ds_read_u16 v29,v29
v_lshl_add_u64 v[36:37],v[56:57],0,v[58:59]
v_mfma_f32_16x16x32_bf16 v[32:35],a[192:195],a[0:3],v[32:35]
ds_read_u16_d16_hi v31,v31
v_lshlrev_b32_sdwa v24,1,v24 dst_sel:DWORD dst_unused:UNUSED_PAD src0_sel:DWORD src1_sel:WORD_1
v_mfma_f32_16x16x32_bf16 v[56:59],a[192:195],a[4:7],v[16:19]
s_waitcnt lgkmcnt(4)
ds_read_u16 v24,v24
v_or_b32 v38,v52,v54
v_mfma_f32_16x16x32_bf16 v[56:59],a[196:199],a[20:23],v[56:59]
s_waitcnt vmcnt(4)
v_cvt_pk_bf16_f32 v39,v53,v55
v_lshlrev_b32_sdwa v26,1,v26 dst_sel:DWORD dst_unused:UNUSED_PAD src0_sel:DWORD src1_sel:WORD_1
v_mfma_f32_16x16x32_bf16 v[56:59],a[200:203],a[36:39],v[56:59]
s_barrier
ds_read_u16_d16_hi v26,v26
v_lshlrev_b32_sdwa v25,1,v25 dst_sel:DWORD dst_unused:UNUSED_PAD src0_sel:DWORD src1_sel:WORD_1
v_mfma_f32_16x16x32_bf16 v[52:55],a[212:215],a[36:39],v[20:23]
ds_read_u16 v25,v25
v_lshlrev_b32_sdwa v27,1,v27 dst_sel:DWORD dst_unused:UNUSED_PAD src0_sel:DWORD src1_sel:WORD_1
v_mfma_f32_16x16x32_bf16 v[52:55],a[208:211],a[20:23],v[52:55]
s_add_u32 m0,s58,18480
ds_read_u16_d16_hi v27,v27
v_lshlrev_b32_sdwa v48,1,v48 dst_sel:DWORD dst_unused:UNUSED_PAD src0_sel:DWORD src1_sel:WORD_1
v_mfma_f32_16x16x32_bf16 v[52:55],a[204:207],a[4:7],v[52:55]
s_mov_b32 s69,s88
ds_read_u16 v48,v48
v_lshlrev_b32_sdwa v50,1,v50 dst_sel:DWORD dst_unused:UNUSED_PAD src0_sel:DWORD src1_sel:WORD_1
v_mfma_f32_16x16x32_bf16 v[232:235],a[232:235],v[40:43],v[232:235]
buffer_load_dwordx4 v5,s[44:47],s69 offen offset:0 lds
v_lshlrev_b32_sdwa v49,1,v49 dst_sel:DWORD dst_unused:UNUSED_PAD src0_sel:DWORD src1_sel:WORD_1
v_mfma_f32_16x16x32_bf16 v[236:239],a[232:235],v[36:39],v[236:239]
ds_read_u16_d16_hi v50,v50
v_lshlrev_b32_sdwa v51,1,v51 dst_sel:DWORD dst_unused:UNUSED_PAD src0_sel:DWORD src1_sel:WORD_1
v_mfma_f32_16x16x32_bf16 v[164:167],a[240:243],v[36:39],v[164:167]
ds_read_u16 v49,v49
ds_read_u16_d16_hi v51,v51
v_mfma_f32_16x16x32_bf16 v[160:163],a[240:243],v[40:43],v[160:163]
ds_read_b64_tr_b16 a[244:245],v3 offset:6160
v_lshlrev_b32_sdwa v44,1,v44 dst_sel:DWORD dst_unused:UNUSED_PAD src0_sel:DWORD src1_sel:WORD_1
v_mfma_f32_16x16x32_bf16 v[240:243],a[224:227],v[40:43],v[240:243]
s_add_u32 s69,s69,s53
ds_read_u16 v44,v44
v_lshlrev_b32_sdwa v46,1,v46 dst_sel:DWORD dst_unused:UNUSED_PAD src0_sel:DWORD src1_sel:WORD_1
v_mfma_f32_16x16x32_bf16 v[244:247],a[224:227],v[36:39],v[244:247]
buffer_load_dwordx4 v5,s[44:47],s69 offen offset:1024 lds
ds_read_u16_d16_hi v46,v46
v_mfma_f32_16x16x32_bf16 v[180:183],a[248:251],v[36:39],v[180:183]
ds_read_b64_tr_b16 a[246:247],v3 offset:7696
v_lshlrev_b32_sdwa v45,1,v45 dst_sel:DWORD dst_unused:UNUSED_PAD src0_sel:DWORD src1_sel:WORD_1
v_mfma_f32_16x16x32_bf16 v[176:179],a[248:251],v[40:43],v[176:179]
ds_read_u16 v45,v45
v_lshlrev_b32_sdwa v47,1,v47 dst_sel:DWORD dst_unused:UNUSED_PAD src0_sel:DWORD src1_sel:WORD_1
v_mfma_f32_16x16x32_bf16 v[248:251],a[228:231],v[40:43],v[248:251]
ds_read_u16_d16_hi v47,v47
v_mfma_f32_16x16x32_bf16 v[252:255],a[228:231],v[36:39],v[252:255]
s_add_u32 s69,s69,s53
ds_read_b64_tr_b16 a[252:253],v3 offset:6168
v_mfma_f32_16x16x32_bf16 v[196:199],a[236:239],v[36:39],v[196:199]
buffer_load_dwordx4 v5,s[44:47],s69 offen offset:2048 lds
v_mfma_f32_16x16x32_bf16 v[192:195],a[236:239],v[40:43],v[192:195]
ds_read_b64_tr_b16 a[254:255],v3 offset:7704
v_add_u32 v11,88272,v1
v_add_u32 v12,88272,v2
v_mfma_f32_16x16x32_bf16 v[36:39],a[204:207],a[8:11],v[20:23]
ds_read_b64_tr_b16 a[96:97],v11 offset:0
ds_read_b64_tr_b16 a[98:99],v11 offset:1024
v_lshlrev_b32_sdwa v60,1,v60 dst_sel:DWORD dst_unused:UNUSED_PAD src0_sel:DWORD src1_sel:WORD_1
v_mfma_f32_16x16x32_bf16 v[36:39],a[208:211],a[24:27],v[36:39]
s_waitcnt lgkmcnt(8)
ds_read_b64_tr_b16 a[100:101],v11 offset:2048
ds_read_b64_tr_b16 a[102:103],v11 offset:3072
v_lshl_add_u64 v[28:29],v[28:29],0,v[30:31]
v_fma_mixlo_f16 v8,v57,s99,0
v_mfma_f32_16x16x32_bf16 v[36:39],a[212:215],a[40:43],v[36:39]
TE13_%=:
s_cbranch_execz TR1_%=
ds_read_u16 v60,v60
ds_read_b64_tr_b16 a[104:105],v11 offset:4096
v_lshl_add_u64 v[30:31],v[24:25],0,v[26:27]
v_lshlrev_b32_sdwa v62,1,v62 dst_sel:DWORD dst_unused:UNUSED_PAD src0_sel:DWORD src1_sel:WORD_1
v_mfma_f32_16x16x32_bf16 v[40:43],a[200:203],a[40:43],v[16:19]
s_nop 0
ds_read_u16_d16_hi v62,v62
ds_read_b64_tr_b16 a[106:107],v11 offset:5120
v_fma_mixhi_f16 v8,v59,s99,0
v_mfma_f32_16x16x32_bf16 v[40:43],a[196:199],a[24:27],v[40:43]
ds_read_b64_tr_b16 a[108:109],v12 offset:4096
v_lshl_add_u64 v[24:25],v[48:49],0,v[50:51]
v_lshlrev_b32_sdwa v61,1,v61 dst_sel:DWORD dst_unused:UNUSED_PAD src0_sel:DWORD src1_sel:WORD_1
v_pk_fma_f16 v9,v8,v8,s54
v_mfma_f32_16x16x32_bf16 v[40:43],a[192:195],a[8:11],v[40:43]
ds_read_u16 v61,v61
v_lshlrev_b32_sdwa v63,1,v63 dst_sel:DWORD dst_unused:UNUSED_PAD src0_sel:DWORD src1_sel:WORD_1
v_mfma_f32_16x16x32_bf16 v[48:51],a[192:195],a[12:15],v[16:19]
s_nop 0
ds_read_u16_d16_hi v63,v63
v_pk_mul_f16 v9,v8,v9
v_mfma_f32_16x16x32_bf16 v[48:51],a[196:199],a[28:31],v[48:51]
s_waitcnt lgkmcnt(12)
ds_read_b64_tr_b16 a[110:111],v12 offset:5120
ds_read_b64_tr_b16 a[112:113],v12 offset:2048
v_lshl_add_u64 v[26:27],v[44:45],0,v[46:47]
v_lshlrev_b32_sdwa v32,1,v32 dst_sel:DWORD dst_unused:UNUSED_PAD src0_sel:DWORD src1_sel:WORD_1
v_mfma_f32_16x16x32_bf16 v[48:51],a[200:203],a[44:47],v[48:51]
ds_read_u16 v32,v32
v_exp_f16 v8,v9
v_mfma_f32_16x16x32_bf16 v[44:47],a[212:215],a[44:47],v[20:23]
ds_read_b64_tr_b16 a[114:115],v12 offset:3072
v_lshlrev_b32_sdwa v34,1,v34 dst_sel:DWORD dst_unused:UNUSED_PAD src0_sel:DWORD src1_sel:WORD_1
v_mfma_f32_16x16x32_bf16 v[44:47],a[208:211],a[28:31],v[44:47]
ds_read_u16_d16_hi v34,v34
v_lshlrev_b32_sdwa v33,1,v33 dst_sel:DWORD dst_unused:UNUSED_PAD src0_sel:DWORD src1_sel:WORD_1
v_mfma_f32_16x16x32_bf16 v[44:47],a[204:207],a[12:15],v[44:47]
v_exp_f16_sdwa v8,v9 dst_sel:WORD_1 dst_unused:UNUSED_PRESERVE src0_sel:WORD_1
v_mfma_f32_16x16x32_bf16 v[208:211],a[232:235],v[28:31],v[208:211]
ds_read_u16 v33,v33
v_lshlrev_b32_sdwa v35,1,v35 dst_sel:DWORD dst_unused:UNUSED_PAD src0_sel:DWORD src1_sel:WORD_1
v_mfma_f32_16x16x32_bf16 v[212:215],a[232:235],v[24:27],v[212:215]
ds_read_u16_d16_hi v35,v35
ds_read_b64_tr_b16 a[232:233],v3 offset:6224
v_lshlrev_b32_sdwa v52,1,v52 dst_sel:DWORD dst_unused:UNUSED_PAD src0_sel:DWORD src1_sel:WORD_1
v_mfma_f32_16x16x32_bf16 v[172:175],a[240:243],v[24:27],v[172:175]
ds_read_u16 v52,v52
v_pk_fma_f16 v8,v8,s55,s55
v_mfma_f32_16x16x32_bf16 v[168:171],a[240:243],v[28:31],v[168:171]
ds_read_b64_tr_b16 a[234:235],v3 offset:7760
v_rcp_f16 v9,v8
v_lshlrev_b32_sdwa v54,1,v54 dst_sel:DWORD dst_unused:UNUSED_PAD src0_sel:DWORD src1_sel:WORD_1
v_mfma_f32_16x16x32_bf16 v[216:219],a[224:227],v[28:31],v[216:219]
ds_read_u16_d16_hi v54,v54
ds_read_b64_tr_b16 a[240:241],v3 offset:6232
v_mfma_f32_16x16x32_bf16 v[220:223],a[224:227],v[24:27],v[220:223]
ds_read_b64_tr_b16 a[242:243],v3 offset:7768
ds_read_b64_tr_b16 a[224:225],v3 offset:6288
v_lshlrev_b32_sdwa v53,1,v53 dst_sel:DWORD dst_unused:UNUSED_PAD src0_sel:DWORD src1_sel:WORD_1
v_mfma_f32_16x16x32_bf16 v[188:191],a[248:251],v[24:27],v[188:191]
ds_read_u16 v53,v53
ds_read_b64_tr_b16 a[226:227],v3 offset:7824
v_lshlrev_b32_sdwa v55,1,v55 dst_sel:DWORD dst_unused:UNUSED_PAD src0_sel:DWORD src1_sel:WORD_1
v_mfma_f32_16x16x32_bf16 v[184:187],a[248:251],v[28:31],v[184:187]
ds_read_u16_d16_hi v55,v55
v_rcp_f16_sdwa v9,v8 dst_sel:WORD_1 dst_unused:UNUSED_PRESERVE src0_sel:WORD_1
v_mfma_f32_16x16x32_bf16 v[224:227],a[228:231],v[28:31],v[224:227]
ds_read_b64_tr_b16 a[248:249],v3 offset:6296
ds_read_b64_tr_b16 a[250:251],v3 offset:7832
v_lshlrev_b32_sdwa v56,1,v56 dst_sel:DWORD dst_unused:UNUSED_PAD src0_sel:DWORD src1_sel:WORD_1
v_mfma_f32_16x16x32_bf16 v[228:231],a[228:231],v[24:27],v[228:231]
ds_read_u16 v56,v56
ds_read_b64_tr_b16 a[116:117],v12 offset:0
v_lshlrev_b32_sdwa v58,1,v58 dst_sel:DWORD dst_unused:UNUSED_PAD src0_sel:DWORD src1_sel:WORD_1
v_mfma_f32_16x16x32_bf16 v[204:207],a[236:239],v[24:27],v[204:207]
ds_read_u16_d16_hi v58,v58
v_fma_mix_f32 v57,v57,v9,0 op_sel_hi:[0,1,0]
v_mfma_f32_16x16x32_bf16 v[200:203],a[236:239],v[28:31],v[200:203]
ds_read_b64_tr_b16 a[118:119],v12 offset:1024
ds_read_b64_tr_b16 a[120:121],v12 offset:6160
v_fma_mix_f32 v59,v59,v9,0 op_sel:[0,1,0] op_sel_hi:[0,1,0]
v_mfma_f32_16x16x32_bf16 v[24:27],a[204:207],a[48:51],v[20:23]
ds_read_b64_tr_b16 a[122:123],v12 offset:7184
ds_read_b64_tr_b16 a[216:217],v1 offset:6160
v_lshlrev_b32_sdwa v36,1,v36 dst_sel:DWORD dst_unused:UNUSED_PAD src0_sel:DWORD src1_sel:WORD_1
v_mfma_f32_16x16x32_bf16 v[24:27],a[208:211],a[64:67],v[24:27]
s_waitcnt lgkmcnt(14)
ds_read_u16 v36,v36
ds_read_b64_tr_b16 a[218:219],v1 offset:7184
v_lshl_add_u64 v[60:61],v[60:61],0,v[62:63]
v_mfma_f32_16x16x32_bf16 v[24:27],a[212:215],a[80:83],v[24:27]
ds_read_b64_tr_b16 a[124:125],v12 offset:8208
v_lshl_add_u64 v[62:63],v[32:33],0,v[34:35]
v_lshlrev_b32_sdwa v38,1,v38 dst_sel:DWORD dst_unused:UNUSED_PAD src0_sel:DWORD src1_sel:WORD_1
v_mfma_f32_16x16x32_bf16 v[28:31],a[200:203],a[80:83],v[16:19]
ds_read_u16_d16_hi v38,v38
v_lshlrev_b32_sdwa v37,1,v37 dst_sel:DWORD dst_unused:UNUSED_PAD src0_sel:DWORD src1_sel:WORD_1
v_mfma_f32_16x16x32_bf16 v[28:31],a[196:199],a[64:67],v[28:31]
s_waitcnt lgkmcnt(13)
ds_read_u16 v37,v37
v_lshl_add_u64 v[32:33],v[52:53],0,v[54:55]
v_mfma_f32_16x16x32_bf16 v[28:31],a[192:195],a[48:51],v[28:31]
ds_read_b64_tr_b16 a[126:127],v12 offset:9232
v_lshlrev_b32_sdwa v39,1,v39 dst_sel:DWORD dst_unused:UNUSED_PAD src0_sel:DWORD src1_sel:WORD_1
v_mfma_f32_16x16x32_bf16 v[52:55],a[192:195],a[52:55],v[16:19]
s_waitcnt lgkmcnt(10)
ds_read_u16_d16_hi v39,v39
v_or_b32 v34,v56,v58
v_lshlrev_b32_sdwa v40,1,v40 dst_sel:DWORD dst_unused:UNUSED_PAD src0_sel:DWORD src1_sel:WORD_1
v_mfma_f32_16x16x32_bf16 v[52:55],a[196:199],a[68:71],v[52:55]
ds_read_u16 v40,v40
ds_read_b64_tr_b16 a[220:221],v1 offset:8208
v_cvt_pk_bf16_f32 v35,v57,v59
v_lshlrev_b32_sdwa v42,1,v42 dst_sel:DWORD dst_unused:UNUSED_PAD src0_sel:DWORD src1_sel:WORD_1
v_mfma_f32_16x16x32_bf16 v[52:55],a[200:203],a[84:87],v[52:55]
ds_read_u16_d16_hi v42,v42
v_lshlrev_b32_sdwa v41,1,v41 dst_sel:DWORD dst_unused:UNUSED_PAD src0_sel:DWORD src1_sel:WORD_1
v_mfma_f32_16x16x32_bf16 v[56:59],a[212:215],a[84:87],v[20:23]
ds_read_u16 v41,v41
v_lshlrev_b32_sdwa v43,1,v43 dst_sel:DWORD dst_unused:UNUSED_PAD src0_sel:DWORD src1_sel:WORD_1
v_mfma_f32_16x16x32_bf16 v[56:59],a[208:211],a[68:71],v[56:59]
ds_read_u16_d16_hi v43,v43
v_lshlrev_b32_sdwa v44,1,v44 dst_sel:DWORD dst_unused:UNUSED_PAD src0_sel:DWORD src1_sel:WORD_1
v_mfma_f32_16x16x32_bf16 v[56:59],a[204:207],a[52:55],v[56:59]
ds_read_u16 v44,v44
v_lshlrev_b32_sdwa v46,1,v46 dst_sel:DWORD dst_unused:UNUSED_PAD src0_sel:DWORD src1_sel:WORD_1
v_mfma_f32_16x16x32_bf16 v[136:139],a[244:247],v[60:63],v[136:139]
ds_read_u16_d16_hi v46,v46
v_lshlrev_b32_sdwa v45,1,v45 dst_sel:DWORD dst_unused:UNUSED_PAD src0_sel:DWORD src1_sel:WORD_1
v_mfma_f32_16x16x32_bf16 v[140:143],a[244:247],v[32:35],v[140:143]
ds_read_u16 v45,v45
v_lshlrev_b32_sdwa v47,1,v47 dst_sel:DWORD dst_unused:UNUSED_PAD src0_sel:DWORD src1_sel:WORD_1
v_mfma_f32_16x16x32_bf16 v[68:71],a[252:255],v[32:35],v[68:71]
ds_read_u16_d16_hi v47,v47
v_lshlrev_b32_sdwa v48,1,v48 dst_sel:DWORD dst_unused:UNUSED_PAD src0_sel:DWORD src1_sel:WORD_1
v_mfma_f32_16x16x32_bf16 v[64:67],a[252:255],v[60:63],v[64:67]
ds_read_u16 v48,v48
ds_read_b64_tr_b16 a[222:223],v1 offset:9232
v_mfma_f32_16x16x32_bf16 v[144:147],a[232:235],v[60:63],v[144:147]
ds_read_b64_tr_b16 a[128:129],v12 offset:10256
v_lshlrev_b32_sdwa v50,1,v50 dst_sel:DWORD dst_unused:UNUSED_PAD src0_sel:DWORD src1_sel:WORD_1
v_mfma_f32_16x16x32_bf16 v[148:151],a[232:235],v[32:35],v[148:151]
ds_read_u16_d16_hi v50,v50
v_lshlrev_b32_sdwa v49,1,v49 dst_sel:DWORD dst_unused:UNUSED_PAD src0_sel:DWORD src1_sel:WORD_1
v_mfma_f32_16x16x32_bf16 v[84:87],a[240:243],v[32:35],v[84:87]
ds_read_u16 v49,v49
v_lshlrev_b32_sdwa v51,1,v51 dst_sel:DWORD dst_unused:UNUSED_PAD src0_sel:DWORD src1_sel:WORD_1
v_mfma_f32_16x16x32_bf16 v[80:83],a[240:243],v[60:63],v[80:83]
ds_read_u16_d16_hi v51,v51
v_mfma_f32_16x16x32_bf16 v[152:155],a[224:227],v[60:63],v[152:155]
ds_read_b64_tr_b16 a[130:131],v12 offset:11280
v_mfma_f32_16x16x32_bf16 v[156:159],a[224:227],v[32:35],v[156:159]
ds_read_b64_tr_b16 a[132:133],v11 offset:10256
v_mfma_f32_16x16x32_bf16 v[100:103],a[248:251],v[32:35],v[100:103]
ds_read_b64_tr_b16 a[134:135],v11 offset:11280
v_mfma_f32_16x16x32_bf16 v[96:99],a[248:251],v[60:63],v[96:99]
ds_read_b64_tr_b16 a[136:137],v11 offset:8208
v_mfma_f32_16x16x32_bf16 v[32:35],a[204:207],a[56:59],v[20:23]
s_nop 0
ds_read_b64_tr_b16 a[138:139],v11 offset:9232
v_lshlrev_b32_sdwa v24,1,v24 dst_sel:DWORD dst_unused:UNUSED_PAD src0_sel:DWORD src1_sel:WORD_1
v_mfma_f32_16x16x32_bf16 v[32:35],a[208:211],a[72:75],v[32:35]
s_waitcnt lgkmcnt(8)
ds_read_u16 v24,v24
v_lshl_add_u64 v[36:37],v[36:37],0,v[38:39]
v_fma_mixlo_f16 v8,v53,s99,0
v_mfma_f32_16x16x32_bf16 v[32:35],a[212:215],a[88:91],v[32:35]
ds_read_b64_tr_b16 a[140:141],v11 offset:6160
v_lshl_add_u64 v[38:39],v[40:41],0,v[42:43]
v_lshlrev_b32_sdwa v26,1,v26 dst_sel:DWORD dst_unused:UNUSED_PAD src0_sel:DWORD src1_sel:WORD_1
v_mfma_f32_16x16x32_bf16 v[60:63],a[200:203],a[88:91],v[16:19]
ds_read_u16_d16_hi v26,v26
v_fma_mixhi_f16 v8,v55,s99,0
v_mfma_f32_16x16x32_bf16 v[60:63],a[196:199],a[72:75],v[60:63]
ds_read_b64_tr_b16 a[142:143],v11 offset:7184
v_lshl_add_u64 v[40:41],v[44:45],0,v[46:47]
v_lshlrev_b32_sdwa v25,1,v25 dst_sel:DWORD dst_unused:UNUSED_PAD src0_sel:DWORD src1_sel:WORD_1
v_pk_fma_f16 v9,v8,v8,s54
v_mfma_f32_16x16x32_bf16 v[60:63],a[192:195],a[56:59],v[60:63]
ds_read_u16 v25,v25
v_lshlrev_b32_sdwa v27,1,v27 dst_sel:DWORD dst_unused:UNUSED_PAD src0_sel:DWORD src1_sel:WORD_1
v_mfma_f32_16x16x32_bf16 v[44:47],a[192:195],a[60:63],v[16:19]
s_nop 0
ds_read_b128 v[16:19],v4 offset:1280
v_pk_mul_f16 v9,v8,v9
v_mfma_f32_16x16x32_bf16 v[44:47],a[196:199],a[76:79],v[44:47]
s_waitcnt lgkmcnt(8)
ds_read_u16_d16_hi v27,v27
ds_read_b64_tr_b16 a[192:193],v1 offset:10256
v_lshl_add_u64 v[42:43],v[48:49],0,v[50:51]
v_lshlrev_b32_sdwa v28,1,v28 dst_sel:DWORD dst_unused:UNUSED_PAD src0_sel:DWORD src1_sel:WORD_1
v_mfma_f32_16x16x32_bf16 v[44:47],a[200:203],a[92:95],v[44:47]
ds_read_b64_tr_b16 a[194:195],v1 offset:11280
v_exp_f16 v8,v9
v_mfma_f32_16x16x32_bf16 v[48:51],a[212:215],a[92:95],v[20:23]
ds_read_b128 v[20:23],v4 offset:1312
ds_read_u16 v28,v28
v_lshlrev_b32_sdwa v30,1,v30 dst_sel:DWORD dst_unused:UNUSED_PAD src0_sel:DWORD src1_sel:WORD_1
v_mfma_f32_16x16x32_bf16 v[48:51],a[208:211],a[76:79],v[48:51]
ds_read_u16_d16_hi v30,v30
ds_read_b64_tr_b16 a[196:197],v2 offset:10256
v_lshlrev_b32_sdwa v29,1,v29 dst_sel:DWORD dst_unused:UNUSED_PAD src0_sel:DWORD src1_sel:WORD_1
v_mfma_f32_16x16x32_bf16 v[48:51],a[204:207],a[60:63],v[48:51]
ds_read_u16 v29,v29
v_exp_f16_sdwa v8,v9 dst_sel:WORD_1 dst_unused:UNUSED_PRESERVE src0_sel:WORD_1
v_mfma_f32_16x16x32_bf16 v[112:115],a[244:247],v[36:39],v[112:115]
ds_read_b64_tr_b16 a[198:199],v2 offset:11280
ds_read_b64_tr_b16 a[200:201],v2 offset:8208
v_lshlrev_b32_sdwa v31,1,v31 dst_sel:DWORD dst_unused:UNUSED_PAD src0_sel:DWORD src1_sel:WORD_1
v_mfma_f32_16x16x32_bf16 v[116:119],a[244:247],v[40:43],v[116:119]
ds_read_u16_d16_hi v31,v31
v_lshlrev_b32_sdwa v56,1,v56 dst_sel:DWORD dst_unused:UNUSED_PAD src0_sel:DWORD src1_sel:WORD_1
v_mfma_f32_16x16x32_bf16 v[76:79],a[252:255],v[40:43],v[76:79]
ds_read_b64_tr_b16 a[202:203],v2 offset:9232
v_pk_fma_f16 v8,v8,s55,s55
v_mfma_f32_16x16x32_bf16 v[72:75],a[252:255],v[36:39],v[72:75]
ds_read_u16 v56,v56
v_rcp_f16 v9,v8
v_lshlrev_b32_sdwa v58,1,v58 dst_sel:DWORD dst_unused:UNUSED_PAD src0_sel:DWORD src1_sel:WORD_1
v_mfma_f32_16x16x32_bf16 v[120:123],a[232:235],v[36:39],v[120:123]
ds_read_u16_d16_hi v58,v58
ds_read_b64_tr_b16 a[212:213],v2 offset:6160
v_mfma_f32_16x16x32_bf16 v[124:127],a[232:235],v[40:43],v[124:127]
ds_read_b64_tr_b16 a[214:215],v2 offset:7184
v_lshlrev_b32_sdwa v57,1,v57 dst_sel:DWORD dst_unused:UNUSED_PAD src0_sel:DWORD src1_sel:WORD_1
v_mfma_f32_16x16x32_bf16 v[92:95],a[240:243],v[40:43],v[92:95]
ds_read_u16 v57,v57
v_lshlrev_b32_sdwa v59,1,v59 dst_sel:DWORD dst_unused:UNUSED_PAD src0_sel:DWORD src1_sel:WORD_1
v_mfma_f32_16x16x32_bf16 v[88:91],a[240:243],v[36:39],v[88:91]
v_rcp_f16_sdwa v9,v8 dst_sel:WORD_1 dst_unused:UNUSED_PRESERVE src0_sel:WORD_1
v_mfma_f32_16x16x32_bf16 v[128:131],a[224:227],v[36:39],v[128:131]
ds_read_u16_d16_hi v59,v59
v_lshlrev_b32_sdwa v52,1,v52 dst_sel:DWORD dst_unused:UNUSED_PAD src0_sel:DWORD src1_sel:WORD_1
v_mfma_f32_16x16x32_bf16 v[132:135],a[224:227],v[40:43],v[132:135]
ds_read_u16 v52,v52
v_lshlrev_b32_sdwa v54,1,v54 dst_sel:DWORD dst_unused:UNUSED_PAD src0_sel:DWORD src1_sel:WORD_1
v_mfma_f32_16x16x32_bf16 v[108:111],a[248:251],v[40:43],v[108:111]
ds_read_u16_d16_hi v54,v54
v_fma_mix_f32 v53,v53,v9,0 op_sel_hi:[0,1,0]
v_mfma_f32_16x16x32_bf16 v[104:107],a[248:251],v[36:39],v[104:107]
s_waitcnt lgkmcnt(8)
ds_read_b64_tr_b16 a[144:145],v11 offset:12320
v_fma_mix_f32 v55,v55,v9,0 op_sel:[0,1,0] op_sel_hi:[0,1,0]
v_mfma_f32_16x16x32_bf16 v[40:43],a[216:219],a[0:3],v[16:19]
ds_read_b64_tr_b16 a[146:147],v11 offset:13344
v_lshlrev_b32_sdwa v32,1,v32 dst_sel:DWORD dst_unused:UNUSED_PAD src0_sel:DWORD src1_sel:WORD_1
v_mfma_f32_16x16x32_bf16 v[40:43],a[220:223],a[16:19],v[40:43]
ds_read_u16 v32,v32
v_lshl_add_u64 v[24:25],v[24:25],0,v[26:27]
v_mfma_f32_16x16x32_bf16 v[40:43],a[192:195],a[32:35],v[40:43]
ds_read_b64_tr_b16 a[148:149],v11 offset:14368
v_lshl_add_u64 v[26:27],v[28:29],0,v[30:31]
v_lshlrev_b32_sdwa v34,1,v34 dst_sel:DWORD dst_unused:UNUSED_PAD src0_sel:DWORD src1_sel:WORD_1
v_mfma_f32_16x16x32_bf16 v[36:39],a[196:199],a[32:35],v[20:23]
ds_read_u16_d16_hi v34,v34
v_lshlrev_b32_sdwa v33,1,v33 dst_sel:DWORD dst_unused:UNUSED_PAD src0_sel:DWORD src1_sel:WORD_1
v_mfma_f32_16x16x32_bf16 v[36:39],a[200:203],a[16:19],v[36:39]
s_waitcnt lgkmcnt(7)
ds_read_u16 v33,v33
v_lshl_add_u64 v[28:29],v[56:57],0,v[58:59]
v_mfma_f32_16x16x32_bf16 v[36:39],a[212:215],a[0:3],v[36:39]
ds_read_b64_tr_b16 a[150:151],v11 offset:15392
v_lshlrev_b32_sdwa v35,1,v35 dst_sel:DWORD dst_unused:UNUSED_PAD src0_sel:DWORD src1_sel:WORD_1
v_mfma_f32_16x16x32_bf16 v[56:59],a[212:215],a[4:7],v[20:23]
s_waitcnt lgkmcnt(7)
ds_read_u16_d16_hi v35,v35
v_or_b32 v30,v52,v54
v_lshlrev_b32_sdwa v60,1,v60 dst_sel:DWORD dst_unused:UNUSED_PAD src0_sel:DWORD src1_sel:WORD_1
v_mfma_f32_16x16x32_bf16 v[56:59],a[200:203],a[20:23],v[56:59]
ds_read_u16 v60,v60
v_cvt_pk_bf16_f32 v31,v53,v55
v_lshlrev_b32_sdwa v62,1,v62 dst_sel:DWORD dst_unused:UNUSED_PAD src0_sel:DWORD src1_sel:WORD_1
v_mfma_f32_16x16x32_bf16 v[56:59],a[196:199],a[36:39],v[56:59]
ds_read_u16_d16_hi v62,v62
v_lshlrev_b32_sdwa v61,1,v61 dst_sel:DWORD dst_unused:UNUSED_PAD src0_sel:DWORD src1_sel:WORD_1
v_mfma_f32_16x16x32_bf16 v[52:55],a[192:195],a[36:39],v[16:19]
ds_read_u16 v61,v61
v_lshlrev_b32_sdwa v63,1,v63 dst_sel:DWORD dst_unused:UNUSED_PAD src0_sel:DWORD src1_sel:WORD_1
v_mfma_f32_16x16x32_bf16 v[52:55],a[220:223],a[20:23],v[52:55]
ds_read_u16_d16_hi v63,v63
v_lshlrev_b32_sdwa v48,1,v48 dst_sel:DWORD dst_unused:UNUSED_PAD src0_sel:DWORD src1_sel:WORD_1
v_mfma_f32_16x16x32_bf16 v[52:55],a[216:219],a[4:7],v[52:55]
ds_read_u16 v48,v48
v_lshlrev_b32_sdwa v50,1,v50 dst_sel:DWORD dst_unused:UNUSED_PAD src0_sel:DWORD src1_sel:WORD_1
v_mfma_f32_16x16x32_bf16 v[232:235],a[244:247],v[24:27],v[232:235]
ds_read_u16_d16_hi v50,v50
ds_read_b64_tr_b16 a[152:153],v11 offset:16416
v_mfma_f32_16x16x32_bf16 v[236:239],a[244:247],v[28:31],v[236:239]
ds_read_b64_tr_b16 a[154:155],v11 offset:17440
v_lshlrev_b32_sdwa v49,1,v49 dst_sel:DWORD dst_unused:UNUSED_PAD src0_sel:DWORD src1_sel:WORD_1
v_mfma_f32_16x16x32_bf16 v[164:167],a[252:255],v[28:31],v[164:167]
ds_read_u16 v49,v49
ds_read_b64_tr_b16 a[228:229],v3 offset:12320
v_mfma_f32_16x16x32_bf16 v[160:163],a[252:255],v[24:27],v[160:163]
ds_read_b64_tr_b16 a[230:231],v3 offset:13856
ds_read_b64_tr_b16 a[236:237],v3 offset:12328
v_mfma_f32_16x16x32_bf16 v[240:243],a[232:235],v[24:27],v[240:243]
ds_read_b64_tr_b16 a[238:239],v3 offset:13864
v_lshlrev_b32_sdwa v51,1,v51 dst_sel:DWORD dst_unused:UNUSED_PAD src0_sel:DWORD src1_sel:WORD_1
v_mfma_f32_16x16x32_bf16 v[244:247],a[232:235],v[28:31],v[244:247]
ds_read_u16_d16_hi v51,v51
v_lshlrev_b32_sdwa v44,1,v44 dst_sel:DWORD dst_unused:UNUSED_PAD src0_sel:DWORD src1_sel:WORD_1
v_mfma_f32_16x16x32_bf16 v[180:183],a[240:243],v[28:31],v[180:183]
ds_read_u16 v44,v44
v_lshlrev_b32_sdwa v46,1,v46 dst_sel:DWORD dst_unused:UNUSED_PAD src0_sel:DWORD src1_sel:WORD_1
v_mfma_f32_16x16x32_bf16 v[176:179],a[240:243],v[24:27],v[176:179]
ds_read_u16_d16_hi v46,v46
v_lshlrev_b32_sdwa v45,1,v45 dst_sel:DWORD dst_unused:UNUSED_PAD src0_sel:DWORD src1_sel:WORD_1
v_mfma_f32_16x16x32_bf16 v[248:251],a[224:227],v[24:27],v[248:251]
ds_read_u16 v45,v45
v_lshlrev_b32_sdwa v47,1,v47 dst_sel:DWORD dst_unused:UNUSED_PAD src0_sel:DWORD src1_sel:WORD_1
v_mfma_f32_16x16x32_bf16 v[252:255],a[224:227],v[28:31],v[252:255]
ds_read_u16_d16_hi v47,v47
v_mfma_f32_16x16x32_bf16 v[196:199],a[248:251],v[28:31],v[196:199]
ds_read_b64_tr_b16 a[156:157],v12 offset:16416
v_mfma_f32_16x16x32_bf16 v[192:195],a[248:251],v[24:27],v[192:195]
ds_read_b64_tr_b16 a[158:159],v12 offset:17440
v_mfma_f32_16x16x32_bf16 v[28:31],a[216:219],a[8:11],v[16:19]
ds_read_b64_tr_b16 a[160:161],v12 offset:14368
v_lshlrev_b32_sdwa v40,1,v40 dst_sel:DWORD dst_unused:UNUSED_PAD src0_sel:DWORD src1_sel:WORD_1
v_mfma_f32_16x16x32_bf16 v[28:31],a[220:223],a[24:27],v[28:31]
s_waitcnt lgkmcnt(8)
ds_read_u16 v40,v40
v_lshl_add_u64 v[32:33],v[32:33],0,v[34:35]
v_fma_mixlo_f16 v8,v57,s99,0
v_mfma_f32_16x16x32_bf16 v[28:31],a[192:195],a[40:43],v[28:31]
ds_read_b64_tr_b16 a[162:163],v12 offset:15392
v_lshl_add_u64 v[34:35],v[60:61],0,v[62:63]
v_lshlrev_b32_sdwa v42,1,v42 dst_sel:DWORD dst_unused:UNUSED_PAD src0_sel:DWORD src1_sel:WORD_1
v_mfma_f32_16x16x32_bf16 v[24:27],a[196:199],a[40:43],v[20:23]
ds_read_u16_d16_hi v42,v42
v_fma_mixhi_f16 v8,v59,s99,0
v_mfma_f32_16x16x32_bf16 v[24:27],a[200:203],a[24:27],v[24:27]
s_waitcnt lgkmcnt(8)
ds_read_b64_tr_b16 a[164:165],v12 offset:12320
v_lshl_add_u64 v[60:61],v[48:49],0,v[50:51]
v_lshlrev_b32_sdwa v41,1,v41 dst_sel:DWORD dst_unused:UNUSED_PAD src0_sel:DWORD src1_sel:WORD_1
v_pk_fma_f16 v9,v8,v8,s54
v_mfma_f32_16x16x32_bf16 v[24:27],a[212:215],a[8:11],v[24:27]
ds_read_u16 v41,v41
v_lshlrev_b32_sdwa v43,1,v43 dst_sel:DWORD dst_unused:UNUSED_PAD src0_sel:DWORD src1_sel:WORD_1
v_mfma_f32_16x16x32_bf16 v[48:51],a[212:215],a[12:15],v[20:23]
ds_read_u16_d16_hi v43,v43
v_pk_mul_f16 v9,v8,v9
v_mfma_f32_16x16x32_bf16 v[48:51],a[200:203],a[28:31],v[48:51]
s_waitcnt lgkmcnt(8)
ds_read_b64_tr_b16 a[166:167],v12 offset:13344
v_lshl_add_u64 v[62:63],v[44:45],0,v[46:47]
v_lshlrev_b32_sdwa v36,1,v36 dst_sel:DWORD dst_unused:UNUSED_PAD src0_sel:DWORD src1_sel:WORD_1
v_mfma_f32_16x16x32_bf16 v[48:51],a[196:199],a[44:47],v[48:51]
v_exp_f16 v8,v9
v_mfma_f32_16x16x32_bf16 v[44:47],a[192:195],a[44:47],v[16:19]
ds_read_u16 v36,v36
v_lshlrev_b32_sdwa v38,1,v38 dst_sel:DWORD dst_unused:UNUSED_PAD src0_sel:DWORD src1_sel:WORD_1
v_mfma_f32_16x16x32_bf16 v[44:47],a[220:223],a[28:31],v[44:47]
ds_read_u16_d16_hi v38,v38
v_lshlrev_b32_sdwa v37,1,v37 dst_sel:DWORD dst_unused:UNUSED_PAD src0_sel:DWORD src1_sel:WORD_1
v_mfma_f32_16x16x32_bf16 v[44:47],a[216:219],a[12:15],v[44:47]
v_exp_f16_sdwa v8,v9 dst_sel:WORD_1 dst_unused:UNUSED_PRESERVE src0_sel:WORD_1
v_mfma_f32_16x16x32_bf16 v[208:211],a[244:247],v[32:35],v[208:211]
ds_read_u16 v37,v37
v_lshlrev_b32_sdwa v39,1,v39 dst_sel:DWORD dst_unused:UNUSED_PAD src0_sel:DWORD src1_sel:WORD_1
v_mfma_f32_16x16x32_bf16 v[212:215],a[244:247],v[60:63],v[212:215]
ds_read_u16_d16_hi v39,v39
v_lshlrev_b32_sdwa v52,1,v52 dst_sel:DWORD dst_unused:UNUSED_PAD src0_sel:DWORD src1_sel:WORD_1
v_mfma_f32_16x16x32_bf16 v[172:175],a[252:255],v[60:63],v[172:175]
ds_read_u16 v52,v52
v_pk_fma_f16 v8,v8,s55,s55
v_mfma_f32_16x16x32_bf16 v[168:171],a[252:255],v[32:35],v[168:171]
ds_read_b64_tr_b16 a[244:245],v3 offset:12384
v_rcp_f16_e64 v9,v8
v_lshlrev_b32_sdwa v54,1,v54 dst_sel:DWORD dst_unused:UNUSED_PAD src0_sel:DWORD src1_sel:WORD_1
v_mfma_f32_16x16x32_bf16 v[216:219],a[232:235],v[32:35],v[216:219]
ds_read_u16_d16_hi v54,v54
ds_read_b64_tr_b16 a[246:247],v3 offset:13920
v_mfma_f32_16x16x32_bf16 v[220:223],a[232:235],v[60:63],v[220:223]
ds_read_b64_tr_b16 a[252:253],v3 offset:12392
ds_read_b64_tr_b16 a[254:255],v3 offset:13928
v_lshlrev_b32_sdwa v53,1,v53 dst_sel:DWORD dst_unused:UNUSED_PAD src0_sel:DWORD src1_sel:WORD_1
v_mfma_f32_16x16x32_bf16 v[188:191],a[240:243],v[60:63],v[188:191]
ds_read_u16 v53,v53
ds_read_b64_tr_b16 a[232:233],v3 offset:12448
v_lshlrev_b32_sdwa v55,1,v55 dst_sel:DWORD dst_unused:UNUSED_PAD src0_sel:DWORD src1_sel:WORD_1
v_mfma_f32_16x16x32_bf16 v[184:187],a[240:243],v[32:35],v[184:187]
ds_read_u16_d16_hi v55,v55
v_rcp_f16_sdwa v9,v8 dst_sel:WORD_1 dst_unused:UNUSED_PRESERVE src0_sel:WORD_1
v_mfma_f32_16x16x32_bf16 v[224:227],a[224:227],v[32:35],v[224:227]
ds_read_b64_tr_b16 a[234:235],v3 offset:13984
ds_read_b64_tr_b16 a[240:241],v3 offset:12456
v_lshlrev_b32_sdwa v56,1,v56 dst_sel:DWORD dst_unused:UNUSED_PAD src0_sel:DWORD src1_sel:WORD_1
v_mfma_f32_16x16x32_bf16 v[228:231],a[224:227],v[60:63],v[228:231]
ds_read_u16 v56,v56
v_lshlrev_b32_sdwa v58,1,v58 dst_sel:DWORD dst_unused:UNUSED_PAD src0_sel:DWORD src1_sel:WORD_1
v_mfma_f32_16x16x32_bf16 v[204:207],a[248:251],v[60:63],v[204:207]
ds_read_u16_d16_hi v58,v58
v_fma_mix_f32 v57,v57,v9,0 op_sel_hi:[0,1,0]
v_mfma_f32_16x16x32_bf16 v[200:203],a[248:251],v[32:35],v[200:203]
ds_read_b64_tr_b16 a[242:243],v3 offset:13992
ds_read_b64_tr_b16 a[168:169],v12 offset:18480
v_fma_mix_f32 v59,v59,v9,0 op_sel:[0,1,0] op_sel_hi:[0,1,0]
v_mfma_f32_16x16x32_bf16 v[60:63],a[216:219],a[48:51],v[16:19]
ds_read_b64_tr_b16 a[170:171],v12 offset:19504
ds_read_b64_tr_b16 a[172:173],v12 offset:20528
v_lshlrev_b32_sdwa v28,1,v28 dst_sel:DWORD dst_unused:UNUSED_PAD src0_sel:DWORD src1_sel:WORD_1
v_mfma_f32_16x16x32_bf16 v[60:63],a[220:223],a[64:67],v[60:63]
s_waitcnt lgkmcnt(13)
ds_read_u16 v28,v28
ds_read_b64_tr_b16 a[174:175],v12 offset:21552
v_lshl_add_u64 v[40:41],v[40:41],0,v[42:43]
v_mfma_f32_16x16x32_bf16 v[60:63],a[192:195],a[80:83],v[60:63]
ds_read_b64_tr_b16 a[208:209],v1 offset:12320
v_lshl_add_u64 v[42:43],v[36:37],0,v[38:39]
v_lshlrev_b32_sdwa v30,1,v30 dst_sel:DWORD dst_unused:UNUSED_PAD src0_sel:DWORD src1_sel:WORD_1
v_mfma_f32_16x16x32_bf16 v[32:35],a[196:199],a[80:83],v[20:23]
ds_read_u16_d16_hi v30,v30
v_lshlrev_b32_sdwa v29,1,v29 dst_sel:DWORD dst_unused:UNUSED_PAD src0_sel:DWORD src1_sel:WORD_1
v_mfma_f32_16x16x32_bf16 v[32:35],a[200:203],a[64:67],v[32:35]
s_waitcnt lgkmcnt(12)
ds_read_u16 v29,v29
v_lshl_add_u64 v[36:37],v[52:53],0,v[54:55]
v_mfma_f32_16x16x32_bf16 v[32:35],a[212:215],a[48:51],v[32:35]
ds_read_b64_tr_b16 a[210:211],v1 offset:13344
v_lshlrev_b32_sdwa v31,1,v31 dst_sel:DWORD dst_unused:UNUSED_PAD src0_sel:DWORD src1_sel:WORD_1
v_mfma_f32_16x16x32_bf16 v[52:55],a[212:215],a[52:55],v[20:23]
s_waitcnt lgkmcnt(10)
ds_read_u16_d16_hi v31,v31
v_or_b32 v38,v56,v58
v_lshlrev_b32_sdwa v24,1,v24 dst_sel:DWORD dst_unused:UNUSED_PAD src0_sel:DWORD src1_sel:WORD_1
v_mfma_f32_16x16x32_bf16 v[52:55],a[200:203],a[68:71],v[52:55]
ds_read_u16 v24,v24
ds_read_b64_tr_b16 a[204:205],v1 offset:14368
v_cvt_pk_bf16_f32 v39,v57,v59
v_lshlrev_b32_sdwa v26,1,v26 dst_sel:DWORD dst_unused:UNUSED_PAD src0_sel:DWORD src1_sel:WORD_1
v_mfma_f32_16x16x32_bf16 v[52:55],a[196:199],a[84:87],v[52:55]
ds_read_u16_d16_hi v26,v26
v_lshlrev_b32_sdwa v25,1,v25 dst_sel:DWORD dst_unused:UNUSED_PAD src0_sel:DWORD src1_sel:WORD_1
v_mfma_f32_16x16x32_bf16 v[56:59],a[192:195],a[84:87],v[16:19]
ds_read_u16 v25,v25
v_lshlrev_b32_sdwa v27,1,v27 dst_sel:DWORD dst_unused:UNUSED_PAD src0_sel:DWORD src1_sel:WORD_1
v_mfma_f32_16x16x32_bf16 v[56:59],a[220:223],a[68:71],v[56:59]
ds_read_u16_d16_hi v27,v27
v_lshlrev_b32_sdwa v44,1,v44 dst_sel:DWORD dst_unused:UNUSED_PAD src0_sel:DWORD src1_sel:WORD_1
v_mfma_f32_16x16x32_bf16 v[56:59],a[216:219],a[52:55],v[56:59]
ds_read_u16 v44,v44
v_lshlrev_b32_sdwa v46,1,v46 dst_sel:DWORD dst_unused:UNUSED_PAD src0_sel:DWORD src1_sel:WORD_1
v_mfma_f32_16x16x32_bf16 v[136:139],a[228:231],v[40:43],v[136:139]
ds_read_u16_d16_hi v46,v46
v_lshlrev_b32_sdwa v45,1,v45 dst_sel:DWORD dst_unused:UNUSED_PAD src0_sel:DWORD src1_sel:WORD_1
v_mfma_f32_16x16x32_bf16 v[140:143],a[228:231],v[36:39],v[140:143]
ds_read_u16 v45,v45
v_lshlrev_b32_sdwa v47,1,v47 dst_sel:DWORD dst_unused:UNUSED_PAD src0_sel:DWORD src1_sel:WORD_1
v_mfma_f32_16x16x32_bf16 v[68:71],a[236:239],v[36:39],v[68:71]
ds_read_u16_d16_hi v47,v47
v_lshlrev_b32_sdwa v48,1,v48 dst_sel:DWORD dst_unused:UNUSED_PAD src0_sel:DWORD src1_sel:WORD_1
v_mfma_f32_16x16x32_bf16 v[64:67],a[236:239],v[40:43],v[64:67]
ds_read_u16 v48,v48
ds_read_b64_tr_b16 a[206:207],v1 offset:15392
v_mfma_f32_16x16x32_bf16 v[144:147],a[244:247],v[40:43],v[144:147]
ds_read_b64_tr_b16 a[176:177],v12 offset:22576
v_lshlrev_b32_sdwa v50,1,v50 dst_sel:DWORD dst_unused:UNUSED_PAD src0_sel:DWORD src1_sel:WORD_1
v_mfma_f32_16x16x32_bf16 v[148:151],a[244:247],v[36:39],v[148:151]
ds_read_u16_d16_hi v50,v50
v_lshlrev_b32_sdwa v49,1,v49 dst_sel:DWORD dst_unused:UNUSED_PAD src0_sel:DWORD src1_sel:WORD_1
v_mfma_f32_16x16x32_bf16 v[84:87],a[252:255],v[36:39],v[84:87]
ds_read_u16 v49,v49
v_lshlrev_b32_sdwa v51,1,v51 dst_sel:DWORD dst_unused:UNUSED_PAD src0_sel:DWORD src1_sel:WORD_1
v_mfma_f32_16x16x32_bf16 v[80:83],a[252:255],v[40:43],v[80:83]
ds_read_u16_d16_hi v51,v51
v_mfma_f32_16x16x32_bf16 v[152:155],a[232:235],v[40:43],v[152:155]
ds_read_b64_tr_b16 a[178:179],v12 offset:23600
v_mfma_f32_16x16x32_bf16 v[156:159],a[232:235],v[36:39],v[156:159]
s_waitcnt lgkmcnt(9)
ds_read_b64_tr_b16 a[180:181],v11 offset:22576
v_mfma_f32_16x16x32_bf16 v[100:103],a[240:243],v[36:39],v[100:103]
ds_read_b64_tr_b16 a[182:183],v11 offset:23600
v_mfma_f32_16x16x32_bf16 v[96:99],a[240:243],v[40:43],v[96:99]
ds_read_b64_tr_b16 a[184:185],v11 offset:20528
v_mfma_f32_16x16x32_bf16 v[36:39],a[216:219],a[56:59],v[16:19]
ds_read_b64_tr_b16 a[186:187],v11 offset:21552
v_lshlrev_b32_sdwa v60,1,v60 dst_sel:DWORD dst_unused:UNUSED_PAD src0_sel:DWORD src1_sel:WORD_1
v_mfma_f32_16x16x32_bf16 v[36:39],a[220:223],a[72:75],v[36:39]
ds_read_u16 v60,v60
v_lshl_add_u64 v[28:29],v[28:29],0,v[30:31]
v_fma_mixlo_f16 v8,v53,s99,0
v_mfma_f32_16x16x32_bf16 v[36:39],a[192:195],a[88:91],v[36:39]
ds_read_b64_tr_b16 a[188:189],v11 offset:18480
v_lshl_add_u64 v[30:31],v[24:25],0,v[26:27]
v_lshlrev_b32_sdwa v62,1,v62 dst_sel:DWORD dst_unused:UNUSED_PAD src0_sel:DWORD src1_sel:WORD_1
v_mfma_f32_16x16x32_bf16 v[40:43],a[196:199],a[88:91],v[20:23]
ds_read_u16_d16_hi v62,v62
v_fma_mixhi_f16 v8,v55,s99,0
v_mfma_f32_16x16x32_bf16 v[40:43],a[200:203],a[72:75],v[40:43]
s_waitcnt lgkmcnt(8)
ds_read_b64_tr_b16 a[190:191],v11 offset:19504
v_lshl_add_u64 v[24:25],v[44:45],0,v[46:47]
v_lshlrev_b32_sdwa v61,1,v61 dst_sel:DWORD dst_unused:UNUSED_PAD src0_sel:DWORD src1_sel:WORD_1
v_pk_fma_f16 v9,v8,v8,s54
v_mfma_f32_16x16x32_bf16 v[40:43],a[212:215],a[56:59],v[40:43]
ds_read_u16 v61,v61
v_lshlrev_b32_sdwa v63,1,v63 dst_sel:DWORD dst_unused:UNUSED_PAD src0_sel:DWORD src1_sel:WORD_1
v_mfma_f32_16x16x32_bf16 v[44:47],a[212:215],a[60:63],v[20:23]
ds_read_b128 v[20:23],v4 offset:1408
v_pk_mul_f16 v9,v8,v9
v_mfma_f32_16x16x32_bf16 v[44:47],a[200:203],a[76:79],v[44:47]
ds_read_u16_d16_hi v63,v63
ds_read_b64_tr_b16 a[212:213],v1 offset:16416
v_lshl_add_u64 v[26:27],v[48:49],0,v[50:51]
v_lshlrev_b32_sdwa v32,1,v32 dst_sel:DWORD dst_unused:UNUSED_PAD src0_sel:DWORD src1_sel:WORD_1
v_mfma_f32_16x16x32_bf16 v[44:47],a[196:199],a[92:95],v[44:47]
ds_read_b64_tr_b16 a[214:215],v1 offset:17440
v_exp_f16_e64 v8,v9
v_mfma_f32_16x16x32_bf16 v[48:51],a[192:195],a[92:95],v[16:19]
ds_read_b128 v[16:19],v4 offset:1440
ds_read_u16 v32,v32
v_lshlrev_b32_sdwa v34,1,v34 dst_sel:DWORD dst_unused:UNUSED_PAD src0_sel:DWORD src1_sel:WORD_1
v_mfma_f32_16x16x32_bf16 v[48:51],a[220:223],a[76:79],v[48:51]
ds_read_u16_d16_hi v34,v34
ds_read_b64_tr_b16 a[200:201],v2 offset:16416
v_lshlrev_b32_sdwa v33,1,v33 dst_sel:DWORD dst_unused:UNUSED_PAD src0_sel:DWORD src1_sel:WORD_1
v_mfma_f32_16x16x32_bf16 v[48:51],a[216:219],a[60:63],v[48:51]
ds_read_u16 v33,v33
v_exp_f16_sdwa v8,v9 dst_sel:WORD_1 dst_unused:UNUSED_PRESERVE src0_sel:WORD_1
v_mfma_f32_16x16x32_bf16 v[112:115],a[228:231],v[28:31],v[112:115]
TP9682_%=:
s_cbranch_execz TR1_%=
ds_read_b64_tr_b16 a[202:203],v2 offset:17440
ds_read_b64_tr_b16 a[196:197],v2 offset:14368
v_lshlrev_b32_sdwa v35,1,v35 dst_sel:DWORD dst_unused:UNUSED_PAD src0_sel:DWORD src1_sel:WORD_1
v_mfma_f32_16x16x32_bf16 v[116:119],a[228:231],v[24:27],v[116:119]
ds_read_u16_d16_hi v35,v35
v_lshlrev_b32_sdwa v56,1,v56 dst_sel:DWORD dst_unused:UNUSED_PAD src0_sel:DWORD src1_sel:WORD_1
v_mfma_f32_16x16x32_bf16 v[76:79],a[236:239],v[24:27],v[76:79]
ds_read_b64_tr_b16 a[198:199],v2 offset:15392
v_pk_fma_f16 v8,v8,s55,s55
v_mfma_f32_16x16x32_bf16 v[72:75],a[236:239],v[28:31],v[72:75]
v_rcp_f16 v9,v8
v_lshlrev_b32_sdwa v58,1,v58 dst_sel:DWORD dst_unused:UNUSED_PAD src0_sel:DWORD src1_sel:WORD_1
v_mfma_f32_16x16x32_bf16 v[120:123],a[244:247],v[28:31],v[120:123]
ds_read_u16 v56,v56
v_lshlrev_b32_sdwa v57,1,v57 dst_sel:DWORD dst_unused:UNUSED_PAD src0_sel:DWORD src1_sel:WORD_1
v_mfma_f32_16x16x32_bf16 v[124:127],a[244:247],v[24:27],v[124:127]
ds_read_u16_d16_hi v58,v58
v_lshlrev_b32_sdwa v59,1,v59 dst_sel:DWORD dst_unused:UNUSED_PAD src0_sel:DWORD src1_sel:WORD_1
v_mfma_f32_16x16x32_bf16 v[92:95],a[252:255],v[24:27],v[92:95]
ds_read_u16 v57,v57
v_lshlrev_b32_sdwa v52,1,v52 dst_sel:DWORD dst_unused:UNUSED_PAD src0_sel:DWORD src1_sel:WORD_1
v_mfma_f32_16x16x32_bf16 v[88:91],a[252:255],v[28:31],v[88:91]
ds_read_u16_d16_hi v59,v59
v_rcp_f16_sdwa v9,v8 dst_sel:WORD_1 dst_unused:UNUSED_PRESERVE src0_sel:WORD_1
v_mfma_f32_16x16x32_bf16 v[128:131],a[232:235],v[28:31],v[128:131]
ds_read_b64_tr_b16 a[192:193],v2 offset:12320
v_lshlrev_b32_sdwa v54,1,v54 dst_sel:DWORD dst_unused:UNUSED_PAD src0_sel:DWORD src1_sel:WORD_1
v_mfma_f32_16x16x32_bf16 v[132:135],a[232:235],v[24:27],v[132:135]
ds_read_b64_tr_b16 a[194:195],v2 offset:13344
v_fma_mix_f32 v53,v53,v9,0 op_sel_hi:[0,1,0]
v_mfma_f32_16x16x32_bf16 v[108:111],a[240:243],v[24:27],v[108:111]
ds_read_u16 v52,v52
v_fma_mix_f32 v55,v55,v9,0 op_sel:[0,1,0] op_sel_hi:[0,1,0]
v_mfma_f32_16x16x32_bf16 v[104:107],a[240:243],v[28:31],v[104:107]
s_waitcnt lgkmcnt(7)
ds_read_u16_d16_hi v54,v54
v_mfma_f32_16x16x32_bf16 v[24:27],a[208:211],a[0:3],v[20:23]
v_lshlrev_b32_sdwa v36,1,v36 dst_sel:DWORD dst_unused:UNUSED_PAD src0_sel:DWORD src1_sel:WORD_1
v_lshlrev_b32_sdwa v38,1,v38 dst_sel:DWORD dst_unused:UNUSED_PAD src0_sel:DWORD src1_sel:WORD_1
v_mfma_f32_16x16x32_bf16 v[24:27],a[204:207],a[16:19],v[24:27]
ds_read_u16 v36,v36
v_lshl_add_u64 v[60:61],v[60:61],0,v[62:63]
v_mfma_f32_16x16x32_bf16 v[24:27],a[212:215],a[32:35],v[24:27]
ds_read_u16_d16_hi v38,v38
v_lshl_add_u64 v[62:63],v[32:33],0,v[34:35]
v_mfma_f32_16x16x32_bf16 v[28:31],a[200:203],a[32:35],v[16:19]
v_lshlrev_b32_sdwa v37,1,v37 dst_sel:DWORD dst_unused:UNUSED_PAD src0_sel:DWORD src1_sel:WORD_1
v_lshlrev_b32_sdwa v39,1,v39 dst_sel:DWORD dst_unused:UNUSED_PAD src0_sel:DWORD src1_sel:WORD_1
v_mfma_f32_16x16x32_bf16 v[28:31],a[196:199],a[16:19],v[28:31]
s_waitcnt lgkmcnt(4)
ds_read_u16 v37,v37
v_lshl_add_u64 v[32:33],v[56:57],0,v[58:59]
v_mfma_f32_16x16x32_bf16 v[28:31],a[192:195],a[0:3],v[28:31]
ds_read_u16_d16_hi v39,v39
v_lshlrev_b32_sdwa v40,1,v40 dst_sel:DWORD dst_unused:UNUSED_PAD src0_sel:DWORD src1_sel:WORD_1
v_mfma_f32_16x16x32_bf16 v[56:59],a[192:195],a[4:7],v[16:19]
s_waitcnt lgkmcnt(4)
ds_read_u16 v40,v40
v_or_b32_e64 v34,v52,v54
v_mfma_f32_16x16x32_bf16 v[56:59],a[196:199],a[20:23],v[56:59]
s_waitcnt vmcnt(0)
v_cvt_pk_bf16_f32 v35,v53,v55
v_lshlrev_b32_sdwa v42,1,v42 dst_sel:DWORD dst_unused:UNUSED_PAD src0_sel:DWORD src1_sel:WORD_1
v_mfma_f32_16x16x32_bf16 v[56:59],a[200:203],a[36:39],v[56:59]
s_barrier
ds_read_u16_d16_hi v42,v42
v_lshlrev_b32_sdwa v41,1,v41 dst_sel:DWORD dst_unused:UNUSED_PAD src0_sel:DWORD src1_sel:WORD_1
v_mfma_f32_16x16x32_bf16 v[52:55],a[212:215],a[36:39],v[20:23]
ds_read_u16 v41,v41
v_lshlrev_b32_sdwa v43,1,v43 dst_sel:DWORD dst_unused:UNUSED_PAD src0_sel:DWORD src1_sel:WORD_1
v_mfma_f32_16x16x32_bf16 v[52:55],a[204:207],a[20:23],v[52:55]
ds_read_u16_d16_hi v43,v43
v_lshlrev_b32_sdwa v48,1,v48 dst_sel:DWORD dst_unused:UNUSED_PAD src0_sel:DWORD src1_sel:WORD_1
v_mfma_f32_16x16x32_bf16 v[52:55],a[208:211],a[4:7],v[52:55]
ds_read_u16 v48,v48
v_lshlrev_b32_sdwa v50,1,v50 dst_sel:DWORD dst_unused:UNUSED_PAD src0_sel:DWORD src1_sel:WORD_1
v_mfma_f32_16x16x32_bf16 v[232:235],a[228:231],v[60:63],v[232:235]
ds_read_u16_d16_hi v50,v50
v_lshlrev_b32_sdwa v49,1,v49 dst_sel:DWORD dst_unused:UNUSED_PAD src0_sel:DWORD src1_sel:WORD_1
v_mfma_f32_16x16x32_bf16 v[236:239],a[228:231],v[32:35],v[236:239]
ds_read_u16 v49,v49
v_lshlrev_b32_sdwa v51,1,v51 dst_sel:DWORD dst_unused:UNUSED_PAD src0_sel:DWORD src1_sel:WORD_1
v_mfma_f32_16x16x32_bf16 v[164:167],a[236:239],v[32:35],v[164:167]
ds_read_u16_d16_hi v51,v51
ds_read_b64_tr_b16 a[224:225],v3 offset:18480
v_mfma_f32_16x16x32_bf16 v[160:163],a[236:239],v[60:63],v[160:163]
ds_read_b64_tr_b16 a[226:227],v3 offset:20016
v_lshlrev_b32_sdwa v44,1,v44 dst_sel:DWORD dst_unused:UNUSED_PAD src0_sel:DWORD src1_sel:WORD_1
v_mfma_f32_16x16x32_bf16 v[240:243],a[244:247],v[60:63],v[240:243]
ds_read_u16 v44,v44
v_lshlrev_b32_sdwa v46,1,v46 dst_sel:DWORD dst_unused:UNUSED_PAD src0_sel:DWORD src1_sel:WORD_1
v_mfma_f32_16x16x32_bf16 v[244:247],a[244:247],v[32:35],v[244:247]
ds_read_u16_d16_hi v46,v46
v_lshlrev_b32_sdwa v45,1,v45 dst_sel:DWORD dst_unused:UNUSED_PAD src0_sel:DWORD src1_sel:WORD_1
v_mfma_f32_16x16x32_bf16 v[180:183],a[252:255],v[32:35],v[180:183]
ds_read_u16 v45,v45
v_lshlrev_b32_sdwa v47,1,v47 dst_sel:DWORD dst_unused:UNUSED_PAD src0_sel:DWORD src1_sel:WORD_1
v_mfma_f32_16x16x32_bf16 v[176:179],a[252:255],v[60:63],v[176:179]
ds_read_u16_d16_hi v47,v47
v_mfma_f32_16x16x32_bf16 v[248:251],a[232:235],v[60:63],v[248:251]
ds_read_b64_tr_b16 a[248:249],v3 offset:18488
v_mfma_f32_16x16x32_bf16 v[252:255],a[232:235],v[32:35],v[252:255]
ds_read_b64_tr_b16 a[250:251],v3 offset:20024
v_mfma_f32_16x16x32_bf16 v[196:199],a[240:243],v[32:35],v[196:199]
v_mfma_f32_16x16x32_bf16 v[192:195],a[240:243],v[60:63],v[192:195]
v_mfma_f32_16x16x32_bf16 v[32:35],a[208:211],a[8:11],v[20:23]
ds_read_b64_tr_b16 a[220:221],v1 offset:18480
v_lshlrev_b32_sdwa v24,1,v24 dst_sel:DWORD dst_unused:UNUSED_PAD src0_sel:DWORD src1_sel:WORD_1
v_mfma_f32_16x16x32_bf16 v[32:35],a[204:207],a[24:27],v[32:35]
s_waitcnt lgkmcnt(6)
ds_read_u16 v24,v24
v_lshl_add_u64 v[36:37],v[36:37],0,v[38:39]
v_fma_mixlo_f16 v8,v57,s99,0
v_mfma_f32_16x16x32_bf16 v[32:35],a[212:215],a[40:43],v[32:35]
ds_read_b64_tr_b16 a[222:223],v1 offset:19504
v_lshl_add_u64 v[38:39],v[40:41],0,v[42:43]
v_lshlrev_b32_sdwa v26,1,v26 dst_sel:DWORD dst_unused:UNUSED_PAD src0_sel:DWORD src1_sel:WORD_1
v_mfma_f32_16x16x32_bf16 v[60:63],a[200:203],a[40:43],v[16:19]
ds_read_u16_d16_hi v26,v26
v_fma_mixhi_f16 v8,v59,s99,0
v_mfma_f32_16x16x32_bf16 v[60:63],a[196:199],a[24:27],v[60:63]
ds_read_b64_tr_b16 a[216:217],v1 offset:20528
v_lshl_add_u64 v[40:41],v[48:49],0,v[50:51]
v_lshlrev_b32_sdwa v25,1,v25 dst_sel:DWORD dst_unused:UNUSED_PAD src0_sel:DWORD src1_sel:WORD_1
v_pk_fma_f16 v9,v8,v8,s54
v_mfma_f32_16x16x32_bf16 v[60:63],a[192:195],a[8:11],v[60:63]
ds_read_u16 v25,v25
v_lshlrev_b32_sdwa v27,1,v27 dst_sel:DWORD dst_unused:UNUSED_PAD src0_sel:DWORD src1_sel:WORD_1
v_mfma_f32_16x16x32_bf16 v[48:51],a[192:195],a[12:15],v[16:19]
ds_read_u16_d16_hi v27,v27
v_pk_mul_f16 v9,v8,v9
v_mfma_f32_16x16x32_bf16 v[48:51],a[196:199],a[28:31],v[48:51]
s_waitcnt lgkmcnt(7)
ds_read_b64_tr_b16 a[218:219],v1 offset:21552
v_lshl_add_u64 v[42:43],v[44:45],0,v[46:47]
v_lshlrev_b32_sdwa v28,1,v28 dst_sel:DWORD dst_unused:UNUSED_PAD src0_sel:DWORD src1_sel:WORD_1
v_mfma_f32_16x16x32_bf16 v[48:51],a[200:203],a[44:47],v[48:51]
v_exp_f16 v8,v9
v_mfma_f32_16x16x32_bf16 v[44:47],a[212:215],a[44:47],v[20:23]
ds_read_u16 v28,v28
v_lshlrev_b32_sdwa v30,1,v30 dst_sel:DWORD dst_unused:UNUSED_PAD src0_sel:DWORD src1_sel:WORD_1
v_mfma_f32_16x16x32_bf16 v[44:47],a[204:207],a[28:31],v[44:47]
ds_read_u16_d16_hi v30,v30
v_lshlrev_b32_sdwa v29,1,v29 dst_sel:DWORD dst_unused:UNUSED_PAD src0_sel:DWORD src1_sel:WORD_1
v_mfma_f32_16x16x32_bf16 v[44:47],a[208:211],a[12:15],v[44:47]
ds_read_b64_tr_b16 a[0:1],v3 offset:38992
ds_read_b64_tr_b16 a[2:3],v3 offset:40528
v_exp_f16_sdwa v8,v9 dst_sel:WORD_1 dst_unused:UNUSED_PRESERVE src0_sel:WORD_1
v_mfma_f32_16x16x32_bf16 v[208:211],a[228:231],v[36:39],v[208:211]
ds_read_b64_tr_b16 a[4:5],v3 offset:45152
ds_read_b64_tr_b16 a[6:7],v3 offset:46688
v_lshlrev_b32_sdwa v31,1,v31 dst_sel:DWORD dst_unused:UNUSED_PAD src0_sel:DWORD src1_sel:WORD_1
v_mfma_f32_16x16x32_bf16 v[212:215],a[228:231],v[40:43],v[212:215]
ds_read_u16 v29,v29
ds_read_u16_d16_hi v31,v31
v_lshlrev_b32_sdwa v52,1,v52 dst_sel:DWORD dst_unused:UNUSED_PAD src0_sel:DWORD src1_sel:WORD_1
v_mfma_f32_16x16x32_bf16 v[172:175],a[236:239],v[40:43],v[172:175]
ds_read_u16 v52,v52
ds_read_b64_tr_b16 a[8:9],v3 offset:45160
v_pk_fma_f16 v8,v8,s55,s55
v_mfma_f32_16x16x32_bf16 v[168:171],a[236:239],v[36:39],v[168:171]
ds_read_b64_tr_b16 a[10:11],v3 offset:46696
v_rcp_f16 v9,v8
v_lshlrev_b32_sdwa v54,1,v54 dst_sel:DWORD dst_unused:UNUSED_PAD src0_sel:DWORD src1_sel:WORD_1
v_mfma_f32_16x16x32_bf16 v[216:219],a[244:247],v[36:39],v[216:219]
ds_read_u16_d16_hi v54,v54
ds_read_b64_tr_b16 a[12:13],v3 offset:39000
v_mfma_f32_16x16x32_bf16 v[220:223],a[244:247],v[40:43],v[220:223]
ds_read_b64_tr_b16 a[14:15],v3 offset:40536
ds_read_b64_tr_b16 a[228:229],v3 offset:18544
v_lshlrev_b32_sdwa v53,1,v53 dst_sel:DWORD dst_unused:UNUSED_PAD src0_sel:DWORD src1_sel:WORD_1
v_mfma_f32_16x16x32_bf16 v[188:191],a[252:255],v[40:43],v[188:191]
ds_read_u16 v53,v53
ds_read_b64_tr_b16 a[230:231],v3 offset:20080
v_lshlrev_b32_sdwa v55,1,v55 dst_sel:DWORD dst_unused:UNUSED_PAD src0_sel:DWORD src1_sel:WORD_1
v_mfma_f32_16x16x32_bf16 v[184:187],a[252:255],v[36:39],v[184:187]
ds_read_u16_d16_hi v55,v55
v_rcp_f16_sdwa v9,v8 dst_sel:WORD_1 dst_unused:UNUSED_PRESERVE src0_sel:WORD_1
v_mfma_f32_16x16x32_bf16 v[224:227],a[232:235],v[36:39],v[224:227]
ds_read_b64_tr_b16 a[236:237],v3 offset:18552
ds_read_b64_tr_b16 a[238:239],v3 offset:20088
v_lshlrev_b32_sdwa v56,1,v56 dst_sel:DWORD dst_unused:UNUSED_PAD src0_sel:DWORD src1_sel:WORD_1
v_mfma_f32_16x16x32_bf16 v[228:231],a[232:235],v[40:43],v[228:231]
ds_read_u16 v56,v56
ds_read_b64_tr_b16 a[244:245],v3 offset:18608
v_lshlrev_b32_sdwa v58,1,v58 dst_sel:DWORD dst_unused:UNUSED_PAD src0_sel:DWORD src1_sel:WORD_1
v_mfma_f32_16x16x32_bf16 v[204:207],a[240:243],v[40:43],v[204:207]
ds_read_u16_d16_hi v58,v58
ds_read_b64_tr_b16 a[246:247],v3 offset:20144
v_fma_mix_f32 v57,v57,v9,0 op_sel_hi:[0,1,0]
v_mfma_f32_16x16x32_bf16 v[200:203],a[240:243],v[36:39],v[200:203]
ds_read_b64_tr_b16 a[252:253],v3 offset:18616
ds_read_b64_tr_b16 a[254:255],v3 offset:20152
v_fma_mix_f32 v59,v59,v9,0 op_sel:[0,1,0] op_sel_hi:[0,1,0]
v_mfma_f32_16x16x32_bf16 v[40:43],a[208:211],a[48:51],v[20:23]
ds_read_b64_tr_b16 a[16:17],v3 offset:39056
ds_read_b64_tr_b16 a[18:19],v3 offset:40592
v_lshlrev_b32_sdwa v32,1,v32 dst_sel:DWORD dst_unused:UNUSED_PAD src0_sel:DWORD src1_sel:WORD_1
v_mfma_f32_16x16x32_bf16 v[40:43],a[204:207],a[64:67],v[40:43]
s_waitcnt lgkmcnt(15)
ds_read_u16 v32,v32
ds_read_b64_tr_b16 a[20:21],v3 offset:45216
v_lshl_add_u64 v[24:25],v[24:25],0,v[26:27]
v_mfma_f32_16x16x32_bf16 v[40:43],a[212:215],a[80:83],v[40:43]
ds_read_b64_tr_b16 a[22:23],v3 offset:46752
ds_read_b64_tr_b16 a[24:25],v3 offset:45224
v_lshl_add_u64 v[26:27],v[28:29],0,v[30:31]
v_lshlrev_b32_sdwa v34,1,v34 dst_sel:DWORD dst_unused:UNUSED_PAD src0_sel:DWORD src1_sel:WORD_1
v_mfma_f32_16x16x32_bf16 v[36:39],a[200:203],a[80:83],v[16:19]
ds_read_u16_d16_hi v34,v34
v_lshlrev_b32_sdwa v33,1,v33 dst_sel:DWORD dst_unused:UNUSED_PAD src0_sel:DWORD src1_sel:WORD_1
v_mfma_f32_16x16x32_bf16 v[36:39],a[196:199],a[64:67],v[36:39]
s_waitcnt lgkmcnt(15)
ds_read_u16 v33,v33
v_lshl_add_u64 v[28:29],v[52:53],0,v[54:55]
v_mfma_f32_16x16x32_bf16 v[36:39],a[192:195],a[48:51],v[36:39]
ds_read_b64_tr_b16 a[26:27],v3 offset:46760
v_lshlrev_b32_sdwa v35,1,v35 dst_sel:DWORD dst_unused:UNUSED_PAD src0_sel:DWORD src1_sel:WORD_1
v_mfma_f32_16x16x32_bf16 v[52:55],a[192:195],a[52:55],v[16:19]
s_waitcnt lgkmcnt(12)
ds_read_u16_d16_hi v35,v35
v_or_b32_e64 v30,v56,v58
v_lshlrev_b32_sdwa v60,1,v60 dst_sel:DWORD dst_unused:UNUSED_PAD src0_sel:DWORD src1_sel:WORD_1
v_mfma_f32_16x16x32_bf16 v[52:55],a[196:199],a[68:71],v[52:55]
ds_read_u16 v60,v60
ds_read_b64_tr_b16 a[28:29],v3 offset:39064
v_cvt_pk_bf16_f32 v31,v57,v59
v_lshlrev_b32_sdwa v62,1,v62 dst_sel:DWORD dst_unused:UNUSED_PAD src0_sel:DWORD src1_sel:WORD_1
v_mfma_f32_16x16x32_bf16 v[52:55],a[200:203],a[84:87],v[52:55]
ds_read_u16_d16_hi v62,v62
v_lshlrev_b32_sdwa v61,1,v61 dst_sel:DWORD dst_unused:UNUSED_PAD src0_sel:DWORD src1_sel:WORD_1
v_mfma_f32_16x16x32_bf16 v[56:59],a[212:215],a[84:87],v[20:23]
ds_read_u16 v61,v61
v_lshlrev_b32_sdwa v63,1,v63 dst_sel:DWORD dst_unused:UNUSED_PAD src0_sel:DWORD src1_sel:WORD_1
v_mfma_f32_16x16x32_bf16 v[56:59],a[204:207],a[68:71],v[56:59]
ds_read_u16_d16_hi v63,v63
v_lshlrev_b32_sdwa v44,1,v44 dst_sel:DWORD dst_unused:UNUSED_PAD src0_sel:DWORD src1_sel:WORD_1
v_mfma_f32_16x16x32_bf16 v[56:59],a[208:211],a[52:55],v[56:59]
ds_read_u16 v44,v44
v_lshlrev_b32_sdwa v46,1,v46 dst_sel:DWORD dst_unused:UNUSED_PAD src0_sel:DWORD src1_sel:WORD_1
v_mfma_f32_16x16x32_bf16 v[136:139],a[224:227],v[24:27],v[136:139]
ds_read_u16_d16_hi v46,v46
v_lshlrev_b32_sdwa v45,1,v45 dst_sel:DWORD dst_unused:UNUSED_PAD src0_sel:DWORD src1_sel:WORD_1
v_mfma_f32_16x16x32_bf16 v[140:143],a[224:227],v[28:31],v[140:143]
ds_read_u16 v45,v45
v_lshlrev_b32_sdwa v47,1,v47 dst_sel:DWORD dst_unused:UNUSED_PAD src0_sel:DWORD src1_sel:WORD_1
v_mfma_f32_16x16x32_bf16 v[68:71],a[248:251],v[28:31],v[68:71]
ds_read_u16_d16_hi v47,v47
v_lshlrev_b32_sdwa v48,1,v48 dst_sel:DWORD dst_unused:UNUSED_PAD src0_sel:DWORD src1_sel:WORD_1
v_mfma_f32_16x16x32_bf16 v[64:67],a[248:251],v[24:27],v[64:67]
ds_read_u16 v48,v48
ds_read_b64_tr_b16 a[30:31],v3 offset:40600
v_mfma_f32_16x16x32_bf16 v[144:147],a[228:231],v[24:27],v[144:147]
ds_read_b64_tr_b16 a[32:33],v3 offset:39120
v_lshlrev_b32_sdwa v50,1,v50 dst_sel:DWORD dst_unused:UNUSED_PAD src0_sel:DWORD src1_sel:WORD_1
v_mfma_f32_16x16x32_bf16 v[148:151],a[228:231],v[28:31],v[148:151]
ds_read_u16_d16_hi v50,v50
v_lshlrev_b32_sdwa v49,1,v49 dst_sel:DWORD dst_unused:UNUSED_PAD src0_sel:DWORD src1_sel:WORD_1
v_mfma_f32_16x16x32_bf16 v[84:87],a[236:239],v[28:31],v[84:87]
ds_read_u16 v49,v49
v_lshlrev_b32_sdwa v51,1,v51 dst_sel:DWORD dst_unused:UNUSED_PAD src0_sel:DWORD src1_sel:WORD_1
v_mfma_f32_16x16x32_bf16 v[80:83],a[236:239],v[24:27],v[80:83]
s_waitcnt lgkmcnt(9)
ds_read_u16_d16_hi v51,v51
v_mfma_f32_16x16x32_bf16 v[152:155],a[244:247],v[24:27],v[152:155]
s_nop 0
ds_read_b64_tr_b16 a[34:35],v3 offset:40656
v_mfma_f32_16x16x32_bf16 v[156:159],a[244:247],v[28:31],v[156:159]
ds_read_b64_tr_b16 a[36:37],v3 offset:45280
v_mfma_f32_16x16x32_bf16 v[100:103],a[252:255],v[28:31],v[100:103]
ds_read_b64_tr_b16 a[38:39],v3 offset:46816
v_mfma_f32_16x16x32_bf16 v[96:99],a[252:255],v[24:27],v[96:99]
ds_read_b64_tr_b16 a[40:41],v3 offset:45288
v_mfma_f32_16x16x32_bf16 v[28:31],a[208:211],a[56:59],v[20:23]
ds_read_b64_tr_b16 a[42:43],v3 offset:46824
v_mul_f32_dpp v136,v14,v136 row_newbcast:0 row_mask:0xf bank_mask:0xf
s_add_u32 s8,s61,3145728
v_lshlrev_b32_sdwa v40,1,v40 dst_sel:DWORD dst_unused:UNUSED_PAD src0_sel:DWORD src1_sel:WORD_1
v_mfma_f32_16x16x32_bf16 v[28:31],a[204:207],a[72:75],v[28:31]
ds_read_u16 v40,v40
v_lshl_add_u64 v[32:33],v[32:33],0,v[34:35]
v_mul_f32_dpp v137,v14,v137 row_newbcast:1 row_mask:0xf bank_mask:0xf
v_mul_f32_dpp v138,v14,v138 row_newbcast:2 row_mask:0xf bank_mask:0xf
v_mfma_f32_16x16x32_bf16 v[28:31],a[212:215],a[88:91],v[28:31]
ds_read_b64_tr_b16 a[44:45],v3 offset:39128
v_lshl_add_u64 v[34:35],v[60:61],0,v[62:63]
v_mul_f32_dpp v139,v14,v139 row_newbcast:3 row_mask:0xf bank_mask:0xf
v_lshlrev_b32_sdwa v42,1,v42 dst_sel:DWORD dst_unused:UNUSED_PAD src0_sel:DWORD src1_sel:WORD_1
v_mfma_f32_16x16x32_bf16 v[24:27],a[200:203],a[88:91],v[16:19]
ds_read_u16_d16_hi v42,v42
v_mul_f32_dpp v64,v14,v64 row_newbcast:4 row_mask:0xf bank_mask:0xf
v_lshlrev_b32_sdwa v41,1,v41 dst_sel:DWORD dst_unused:UNUSED_PAD src0_sel:DWORD src1_sel:WORD_1
v_mul_f32_dpp v65,v14,v65 row_newbcast:5 row_mask:0xf bank_mask:0xf
v_mfma_f32_16x16x32_bf16 v[24:27],a[196:199],a[72:75],v[24:27]
s_waitcnt lgkmcnt(8)
ds_read_u16 v41,v41
v_lshl_add_u64 v[60:61],v[44:45],0,v[46:47]
v_mul_f32_dpp v66,v14,v66 row_newbcast:6 row_mask:0xf bank_mask:0xf
v_mfma_f32_16x16x32_bf16 v[24:27],a[192:195],a[56:59],v[24:27]
s_nop 0
ds_read_b64_tr_b16 a[46:47],v3 offset:40664
v_mul_f32_dpp v67,v14,v67 row_newbcast:7 row_mask:0xf bank_mask:0xf
v_lshlrev_b32_sdwa v43,1,v43 dst_sel:DWORD dst_unused:UNUSED_PAD src0_sel:DWORD src1_sel:WORD_1
v_cvt_pk_bf16_f32 v136,v136,v137
v_mfma_f32_16x16x32_bf16 v[44:47],a[192:195],a[60:63],v[16:19]
ds_read_b128 v[16:19],v4 offset:0
v_cvt_pk_bf16_f32 v137,v138,v139
v_lshlrev_b32_sdwa v36,1,v36 dst_sel:DWORD dst_unused:UNUSED_PAD src0_sel:DWORD src1_sel:WORD_1
v_mfma_f32_16x16x32_bf16 v[44:47],a[196:199],a[76:79],v[44:47]
ds_read_u16_d16_hi v43,v43
ds_read_b64_tr_b16 a[192:193],v1 offset:22576
v_lshl_add_u64 v[62:63],v[48:49],0,v[50:51]
v_cvt_pk_bf16_f32 v138,v64,v65
v_cvt_pk_bf16_f32 v139,v66,v67
v_lshlrev_b32_sdwa v38,1,v38 dst_sel:DWORD dst_unused:UNUSED_PAD src0_sel:DWORD src1_sel:WORD_1
v_mfma_f32_16x16x32_bf16 v[44:47],a[200:203],a[92:95],v[44:47]
buffer_store_dwordx4 v[136:139],v7,s[40:43],s8 offen offset:0 sc1
ds_read_b64_tr_b16 a[194:195],v1 offset:23600
v_mul_f32_dpp v144,v14,v144 row_newbcast:8 row_mask:0xf bank_mask:0xf
v_mfma_f32_16x16x32_bf16 v[48:51],a[212:215],a[92:95],v[20:23]
ds_read_b128 v[20:23],v4 offset:32
ds_read_u16 v36,v36
v_mul_f32_dpp v145,v14,v145 row_newbcast:9 row_mask:0xf bank_mask:0xf
v_lshlrev_b32_sdwa v37,1,v37 dst_sel:DWORD dst_unused:UNUSED_PAD src0_sel:DWORD src1_sel:WORD_1
v_mul_f32_dpp v146,v14,v146 row_newbcast:10 row_mask:0xf bank_mask:0xf
v_mfma_f32_16x16x32_bf16 v[48:51],a[204:207],a[76:79],v[48:51]
ds_read_u16_d16_hi v38,v38
ds_read_u16 v37,v37
v_mul_f32_dpp v147,v14,v147 row_newbcast:11 row_mask:0xf bank_mask:0xf
v_lshlrev_b32_sdwa v39,1,v39 dst_sel:DWORD dst_unused:UNUSED_PAD src0_sel:DWORD src1_sel:WORD_1
v_mfma_f32_16x16x32_bf16 v[48:51],a[208:211],a[60:63],v[48:51]
ds_read_u16_d16_hi v39,v39
ds_read_b64_tr_b16 a[196:197],v2 offset:22576
v_mul_f32_dpp v80,v14,v80 row_newbcast:12 row_mask:0xf bank_mask:0xf
v_mul_f32_dpp v81,v14,v81 row_newbcast:13 row_mask:0xf bank_mask:0xf
v_mfma_f32_16x16x32_bf16 v[112:115],a[224:227],v[32:35],v[112:115]
ds_read_b64_tr_b16 a[198:199],v2 offset:23600
ds_read_b64_tr_b16 a[200:201],v2 offset:20528
v_lshlrev_b32_sdwa v56,1,v56 dst_sel:DWORD dst_unused:UNUSED_PAD src0_sel:DWORD src1_sel:WORD_1
v_mul_f32_dpp v82,v14,v82 row_newbcast:14 row_mask:0xf bank_mask:0xf
v_mfma_f32_16x16x32_bf16 v[116:119],a[224:227],v[60:63],v[116:119]
ds_read_b64_tr_b16 a[202:203],v2 offset:21552
v_mul_f32_dpp v83,v14,v83 row_newbcast:15 row_mask:0xf bank_mask:0xf
v_lshlrev_b32_sdwa v58,1,v58 dst_sel:DWORD dst_unused:UNUSED_PAD src0_sel:DWORD src1_sel:WORD_1
v_cvt_pk_bf16_f32 v144,v144,v145
v_mfma_f32_16x16x32_bf16 v[76:79],a[248:251],v[60:63],v[76:79]
ds_read_u16 v56,v56
v_cvt_pk_bf16_f32 v145,v146,v147
v_lshlrev_b32_sdwa v57,1,v57 dst_sel:DWORD dst_unused:UNUSED_PAD src0_sel:DWORD src1_sel:WORD_1
v_mfma_f32_16x16x32_bf16 v[72:75],a[248:251],v[32:35],v[72:75]
ds_read_u16_d16_hi v58,v58
v_cvt_pk_bf16_f32 v146,v80,v81
v_cvt_pk_bf16_f32 v147,v82,v83
v_mfma_f32_16x16x32_bf16 v[120:123],a[228:231],v[32:35],v[120:123]
ds_read_u16 v57,v57
v_mul_f32_dpp v152,v15,v152 row_newbcast:0 row_mask:0xf bank_mask:0xf
v_lshlrev_b32_sdwa v59,1,v59 dst_sel:DWORD dst_unused:UNUSED_PAD src0_sel:DWORD src1_sel:WORD_1
v_mfma_f32_16x16x32_bf16 v[124:127],a[228:231],v[60:63],v[124:127]
ds_read_u16_d16_hi v59,v59
v_mul_f32_dpp v153,v15,v153 row_newbcast:1 row_mask:0xf bank_mask:0xf
v_lshlrev_b32_sdwa v52,1,v52 dst_sel:DWORD dst_unused:UNUSED_PAD src0_sel:DWORD src1_sel:WORD_1
v_mul_f32_dpp v154,v15,v154 row_newbcast:2 row_mask:0xf bank_mask:0xf
v_mfma_f32_16x16x32_bf16 v[92:95],a[236:239],v[60:63],v[92:95]
buffer_store_dwordx4 v[144:147],v7,s[40:43],s8 offen offset:64 sc1
v_mul_f32_dpp v155,v15,v155 row_newbcast:3 row_mask:0xf bank_mask:0xf
v_mfma_f32_16x16x32_bf16 v[88:91],a[236:239],v[32:35],v[88:91]
ds_read_b64_tr_b16 a[212:213],v2 offset:18480
v_mul_f32_dpp v96,v15,v96 row_newbcast:4 row_mask:0xf bank_mask:0xf
v_lshlrev_b32_sdwa v54,1,v54 dst_sel:DWORD dst_unused:UNUSED_PAD src0_sel:DWORD src1_sel:WORD_1
v_mul_f32_dpp v97,v15,v97 row_newbcast:5 row_mask:0xf bank_mask:0xf
v_mfma_f32_16x16x32_bf16 v[128:131],a[244:247],v[32:35],v[128:131]
ds_read_b64_tr_b16 a[214:215],v2 offset:19504
v_mul_f32_dpp v98,v15,v98 row_newbcast:6 row_mask:0xf bank_mask:0xf
v_lshlrev_b32_sdwa v53,1,v53 dst_sel:DWORD dst_unused:UNUSED_PAD src0_sel:DWORD src1_sel:WORD_1
v_mfma_f32_16x16x32_bf16 v[132:135],a[244:247],v[60:63],v[132:135]
ds_read_u16 v52,v52
v_mul_f32_dpp v99,v15,v99 row_newbcast:7 row_mask:0xf bank_mask:0xf
v_cvt_pk_bf16_f32 v152,v152,v153
v_lshlrev_b32_sdwa v55,1,v55 dst_sel:DWORD dst_unused:UNUSED_PAD src0_sel:DWORD src1_sel:WORD_1
v_mfma_f32_16x16x32_bf16 v[108:111],a[252:255],v[60:63],v[108:111]
s_nop 0
ds_read_u16_d16_hi v54,v54
v_cvt_pk_bf16_f32 v153,v154,v155
v_mfma_f32_16x16x32_bf16 v[104:107],a[252:255],v[32:35],v[104:107]
s_waitcnt lgkmcnt(7)
ds_read_u16 v53,v53
ds_read_u16_d16_hi v55,v55
v_cvt_pk_bf16_f32 v154,v96,v97
v_cvt_pk_bf16_f32 v155,v98,v99
v_mfma_f32_16x16x32_bf16 v[60:63],a[220:223],a[48:51],v[16:19]
v_mul_f32_dpp v140,v14,v140 row_newbcast:0 row_mask:0xf bank_mask:0xf
v_lshlrev_b32_sdwa v28,1,v28 dst_sel:DWORD dst_unused:UNUSED_PAD src0_sel:DWORD src1_sel:WORD_1
v_mfma_f32_16x16x32_bf16 v[60:63],a[216:219],a[64:67],v[60:63]
ds_read_u16 v28,v28
v_lshl_add_u64 v[40:41],v[40:41],0,v[42:43]
v_mul_f32_dpp v141,v14,v141 row_newbcast:1 row_mask:0xf bank_mask:0xf
v_mul_f32_dpp v142,v14,v142 row_newbcast:2 row_mask:0xf bank_mask:0xf
v_mfma_f32_16x16x32_bf16 v[60:63],a[192:195],a[80:83],v[60:63]
v_lshl_add_u64 v[42:43],v[36:37],0,v[38:39]
v_mul_f32_dpp v143,v14,v143 row_newbcast:3 row_mask:0xf bank_mask:0xf
v_lshlrev_b32_sdwa v30,1,v30 dst_sel:DWORD dst_unused:UNUSED_PAD src0_sel:DWORD src1_sel:WORD_1
v_mfma_f32_16x16x32_bf16 v[32:35],a[196:199],a[80:83],v[20:23]
ds_read_u16_d16_hi v30,v30
v_mul_f32_dpp v68,v14,v68 row_newbcast:4 row_mask:0xf bank_mask:0xf
v_lshlrev_b32_sdwa v29,1,v29 dst_sel:DWORD dst_unused:UNUSED_PAD src0_sel:DWORD src1_sel:WORD_1
v_mul_f32_dpp v69,v14,v69 row_newbcast:5 row_mask:0xf bank_mask:0xf
v_mfma_f32_16x16x32_bf16 v[32:35],a[200:203],a[64:67],v[32:35]
s_waitcnt lgkmcnt(6)
ds_read_u16 v29,v29
v_lshl_add_u64 v[36:37],v[56:57],0,v[58:59]
v_mul_f32_dpp v70,v14,v70 row_newbcast:6 row_mask:0xf bank_mask:0xf
v_mfma_f32_16x16x32_bf16 v[32:35],a[212:215],a[48:51],v[32:35]
buffer_store_dwordx4 v[152:155],v7,s[40:43],s8 offen offset:128 sc1
v_mul_f32_dpp v71,v14,v71 row_newbcast:7 row_mask:0xf bank_mask:0xf
v_lshlrev_b32_sdwa v31,1,v31 dst_sel:DWORD dst_unused:UNUSED_PAD src0_sel:DWORD src1_sel:WORD_1
v_cvt_pk_bf16_f32 v140,v140,v141
v_mfma_f32_16x16x32_bf16 v[56:59],a[212:215],a[52:55],v[20:23]
ds_read_u16_d16_hi v31,v31
v_cvt_pk_bf16_f32 v141,v142,v143
v_lshlrev_b32_sdwa v24,1,v24 dst_sel:DWORD dst_unused:UNUSED_PAD src0_sel:DWORD src1_sel:WORD_1
v_mfma_f32_16x16x32_bf16 v[56:59],a[200:203],a[68:71],v[56:59]
s_waitcnt lgkmcnt(4)
ds_read_u16 v24,v24
v_lshl_add_u64 v[38:39],v[52:53],0,v[54:55]
v_cvt_pk_bf16_f32 v142,v68,v69
v_cvt_pk_bf16_f32 v143,v70,v71
v_lshlrev_b32_sdwa v26,1,v26 dst_sel:DWORD dst_unused:UNUSED_PAD src0_sel:DWORD src1_sel:WORD_1
v_mfma_f32_16x16x32_bf16 v[56:59],a[196:199],a[84:87],v[56:59]
ds_read_u16_d16_hi v26,v26
v_mul_f32_dpp v148,v14,v148 row_newbcast:8 row_mask:0xf bank_mask:0xf
v_mfma_f32_16x16x32_bf16 v[52:55],a[192:195],a[84:87],v[16:19]
ds_read_b64_tr_b16 a[232:233],v3 offset:32832
v_mul_f32_dpp v149,v14,v149 row_newbcast:9 row_mask:0xf bank_mask:0xf
v_lshlrev_b32_sdwa v25,1,v25 dst_sel:DWORD dst_unused:UNUSED_PAD src0_sel:DWORD src1_sel:WORD_1
v_mul_f32_dpp v150,v14,v150 row_newbcast:10 row_mask:0xf bank_mask:0xf
v_mfma_f32_16x16x32_bf16 v[52:55],a[216:219],a[68:71],v[52:55]
ds_read_u16 v25,v25
v_mul_f32_dpp v151,v14,v151 row_newbcast:11 row_mask:0xf bank_mask:0xf
v_lshlrev_b32_sdwa v27,1,v27 dst_sel:DWORD dst_unused:UNUSED_PAD src0_sel:DWORD src1_sel:WORD_1
v_mfma_f32_16x16x32_bf16 v[52:55],a[220:223],a[52:55],v[52:55]
ds_read_u16_d16_hi v27,v27
v_mul_f32_dpp v84,v14,v84 row_newbcast:12 row_mask:0xf bank_mask:0xf
v_mul_f32_dpp v85,v14,v85 row_newbcast:13 row_mask:0xf bank_mask:0xf
v_mfma_f32_16x16x32_bf16 v[232:235],a[224:227],v[40:43],v[232:235]
ds_read_b64_tr_b16 a[234:235],v3 offset:34368
v_lshlrev_b32_sdwa v48,1,v48 dst_sel:DWORD dst_unused:UNUSED_PAD src0_sel:DWORD src1_sel:WORD_1
v_mul_f32_dpp v86,v14,v86 row_newbcast:14 row_mask:0xf bank_mask:0xf
v_mfma_f32_16x16x32_bf16 v[236:239],a[224:227],v[36:39],v[236:239]
ds_read_u16 v48,v48
v_mul_f32_dpp v87,v14,v87 row_newbcast:15 row_mask:0xf bank_mask:0xf
v_lshlrev_b32_sdwa v50,1,v50 dst_sel:DWORD dst_unused:UNUSED_PAD src0_sel:DWORD src1_sel:WORD_1
v_cvt_pk_bf16_f32 v148,v148,v149
v_mfma_f32_16x16x32_bf16 v[164:167],a[248:251],v[36:39],v[164:167]
ds_read_u16_d16_hi v50,v50
v_cvt_pk_bf16_f32 v149,v150,v151
v_lshlrev_b32_sdwa v49,1,v49 dst_sel:DWORD dst_unused:UNUSED_PAD src0_sel:DWORD src1_sel:WORD_1
v_mfma_f32_16x16x32_bf16 v[160:163],a[248:251],v[40:43],v[160:163]
ds_read_u16 v49,v49
v_cvt_pk_bf16_f32 v150,v84,v85
v_cvt_pk_bf16_f32 v151,v86,v87
v_mfma_f32_16x16x32_bf16 v[240:243],a[228:231],v[40:43],v[240:243]
buffer_store_dwordx4 v[140:143],v7,s[40:43],s8 offen offset:192 sc1
v_mul_f32_dpp v156,v15,v156 row_newbcast:0 row_mask:0xf bank_mask:0xf
v_lshlrev_b32_sdwa v51,1,v51 dst_sel:DWORD dst_unused:UNUSED_PAD src0_sel:DWORD src1_sel:WORD_1
v_mfma_f32_16x16x32_bf16 v[244:247],a[228:231],v[36:39],v[244:247]
ds_read_u16_d16_hi v51,v51
v_mul_f32_dpp v157,v15,v157 row_newbcast:1 row_mask:0xf bank_mask:0xf
v_lshlrev_b32_sdwa v44,1,v44 dst_sel:DWORD dst_unused:UNUSED_PAD src0_sel:DWORD src1_sel:WORD_1
v_mul_f32_dpp v158,v15,v158 row_newbcast:2 row_mask:0xf bank_mask:0xf
v_mfma_f32_16x16x32_bf16 v[180:183],a[236:239],v[36:39],v[180:183]
ds_read_u16 v44,v44
v_mul_f32_dpp v159,v15,v159 row_newbcast:3 row_mask:0xf bank_mask:0xf
v_mfma_f32_16x16x32_bf16 v[176:179],a[236:239],v[40:43],v[176:179]
ds_read_b64_tr_b16 a[240:241],v3 offset:32840
v_mul_f32_dpp v100,v15,v100 row_newbcast:4 row_mask:0xf bank_mask:0xf
v_lshlrev_b32_sdwa v46,1,v46 dst_sel:DWORD dst_unused:UNUSED_PAD src0_sel:DWORD src1_sel:WORD_1
v_mul_f32_dpp v101,v15,v101 row_newbcast:5 row_mask:0xf bank_mask:0xf
v_mfma_f32_16x16x32_bf16 v[248:251],a[244:247],v[40:43],v[248:251]
ds_read_u16_d16_hi v46,v46
v_mul_f32_dpp v102,v15,v102 row_newbcast:6 row_mask:0xf bank_mask:0xf
v_lshlrev_b32_sdwa v45,1,v45 dst_sel:DWORD dst_unused:UNUSED_PAD src0_sel:DWORD src1_sel:WORD_1
v_mfma_f32_16x16x32_bf16 v[252:255],a[244:247],v[36:39],v[252:255]
ds_read_u16 v45,v45
v_mul_f32_dpp v103,v15,v103 row_newbcast:7 row_mask:0xf bank_mask:0xf
v_cvt_pk_bf16_f32 v156,v156,v157
v_lshlrev_b32_sdwa v47,1,v47 dst_sel:DWORD dst_unused:UNUSED_PAD src0_sel:DWORD src1_sel:WORD_1
v_mfma_f32_16x16x32_bf16 v[196:199],a[252:255],v[36:39],v[196:199]
ds_read_u16_d16_hi v47,v47
v_cvt_pk_bf16_f32 v157,v158,v159
v_mfma_f32_16x16x32_bf16 v[192:195],a[252:255],v[40:43],v[192:195]
ds_read_b64_tr_b16 a[242:243],v3 offset:34376
v_cvt_pk_bf16_f32 v158,v100,v101
v_cvt_pk_bf16_f32 v159,v102,v103
v_mfma_f32_16x16x32_bf16 v[36:39],a[220:223],a[56:59],v[16:19]
s_nop 0
ds_read_b128 v[64:67],v4 offset:128
ds_read_b128 v[68:71],v4 offset:160
v_mul_f32_dpp v112,v14,v112 row_newbcast:0 row_mask:0xf bank_mask:0xf
s_add_u32 s9,s61,3244032
v_lshlrev_b32_sdwa v60,1,v60 dst_sel:DWORD dst_unused:UNUSED_PAD src0_sel:DWORD src1_sel:WORD_1
v_mfma_f32_16x16x32_bf16 v[36:39],a[216:219],a[72:75],v[36:39]
s_waitcnt lgkmcnt(9)
ds_read_u16 v60,v60
ds_read_b128 v[80:83],v4 offset:256
v_lshl_add_u64 v[28:29],v[28:29],0,v[30:31]
v_mul_f32_dpp v113,v14,v113 row_newbcast:1 row_mask:0xf bank_mask:0xf
v_mul_f32_dpp v114,v14,v114 row_newbcast:2 row_mask:0xf bank_mask:0xf
v_mfma_f32_16x16x32_bf16 v[36:39],a[192:195],a[88:91],v[36:39]
ds_read_b128 v[84:87],v4 offset:288
v_lshl_add_u64 v[30:31],v[24:25],0,v[26:27]
v_mul_f32_dpp v115,v14,v115 row_newbcast:3 row_mask:0xf bank_mask:0xf
v_lshlrev_b32_sdwa v62,1,v62 dst_sel:DWORD dst_unused:UNUSED_PAD src0_sel:DWORD src1_sel:WORD_1
v_mfma_f32_16x16x32_bf16 v[40:43],a[196:199],a[88:91],v[20:23]
buffer_store_dwordx4 v[148:151],v7,s[40:43],s8 offen offset:256 sc1
v_mul_f32_dpp v72,v14,v72 row_newbcast:4 row_mask:0xf bank_mask:0xf
v_lshlrev_b32_sdwa v61,1,v61 dst_sel:DWORD dst_unused:UNUSED_PAD src0_sel:DWORD src1_sel:WORD_1
v_mul_f32_dpp v73,v14,v73 row_newbcast:5 row_mask:0xf bank_mask:0xf
v_mfma_f32_16x16x32_bf16 v[40:43],a[200:203],a[72:75],v[40:43]
s_waitcnt lgkmcnt(9)
ds_read_u16_d16_hi v62,v62
v_lshl_add_u64 v[24:25],v[48:49],0,v[50:51]
v_mul_f32_dpp v74,v14,v74 row_newbcast:6 row_mask:0xf bank_mask:0xf
v_mfma_f32_16x16x32_bf16 v[40:43],a[212:215],a[56:59],v[40:43]
TP10205_%=:
s_cbranch_execz TR1_%=
ds_read_u16 v61,v61
v_mul_f32_dpp v75,v14,v75 row_newbcast:7 row_mask:0xf bank_mask:0xf
v_lshlrev_b32_sdwa v63,1,v63 dst_sel:DWORD dst_unused:UNUSED_PAD src0_sel:DWORD src1_sel:WORD_1
v_cvt_pk_bf16_f32 v112,v112,v113
v_mfma_f32_16x16x32_bf16 v[48:51],a[212:215],a[60:63],v[20:23]
s_nop 0
ds_read_b64_tr_b16 a[212:213],v3 offset:51312
ds_read_b64_tr_b16 a[214:215],v3 offset:52848
v_cvt_pk_bf16_f32 v113,v114,v115
v_lshlrev_b32_sdwa v32,1,v32 dst_sel:DWORD dst_unused:UNUSED_PAD src0_sel:DWORD src1_sel:WORD_1
v_mfma_f32_16x16x32_bf16 v[48:51],a[200:203],a[76:79],v[48:51]
s_waitcnt lgkmcnt(10)
ds_read_b64_tr_b16 a[200:201],v3 offset:57472
ds_read_b64_tr_b16 a[202:203],v3 offset:59008
v_lshl_add_u64 v[26:27],v[44:45],0,v[46:47]
v_cvt_pk_bf16_f32 v114,v72,v73
v_cvt_pk_bf16_f32 v115,v74,v75
v_lshlrev_b32_sdwa v34,1,v34 dst_sel:DWORD dst_unused:UNUSED_PAD src0_sel:DWORD src1_sel:WORD_1
v_mfma_f32_16x16x32_bf16 v[48:51],a[196:199],a[92:95],v[48:51]
ds_read_u16_d16_hi v63,v63
ds_read_u16 v32,v32
v_mul_f32_dpp v120,v14,v120 row_newbcast:8 row_mask:0xf bank_mask:0xf
v_mfma_f32_16x16x32_bf16 v[44:47],a[192:195],a[92:95],v[16:19]
ds_read_u16_d16_hi v34,v34
ds_read_b64_tr_b16 a[196:197],v3 offset:57480
v_mul_f32_dpp v121,v14,v121 row_newbcast:9 row_mask:0xf bank_mask:0xf
v_lshlrev_b32_sdwa v33,1,v33 dst_sel:DWORD dst_unused:UNUSED_PAD src0_sel:DWORD src1_sel:WORD_1
v_mul_f32_dpp v122,v14,v122 row_newbcast:10 row_mask:0xf bank_mask:0xf
v_mfma_f32_16x16x32_bf16 v[44:47],a[216:219],a[76:79],v[44:47]
ds_read_u16 v33,v33
ds_read_b64_tr_b16 a[198:199],v3 offset:59016
v_mul_f32_dpp v123,v14,v123 row_newbcast:11 row_mask:0xf bank_mask:0xf
v_lshlrev_b32_sdwa v35,1,v35 dst_sel:DWORD dst_unused:UNUSED_PAD src0_sel:DWORD src1_sel:WORD_1
v_mfma_f32_16x16x32_bf16 v[44:47],a[220:223],a[60:63],v[44:47]
ds_read_u16_d16_hi v35,v35
ds_read_b64_tr_b16 a[192:193],v3 offset:51320
v_mul_f32_dpp v88,v14,v88 row_newbcast:12 row_mask:0xf bank_mask:0xf
v_mul_f32_dpp v89,v14,v89 row_newbcast:13 row_mask:0xf bank_mask:0xf
v_mfma_f32_16x16x32_bf16 v[208:211],a[224:227],v[28:31],v[208:211]
ds_read_b64_tr_b16 a[194:195],v3 offset:52856
ds_read_b128 v[96:99],v4 offset:384
v_lshlrev_b32_sdwa v52,1,v52 dst_sel:DWORD dst_unused:UNUSED_PAD src0_sel:DWORD src1_sel:WORD_1
v_mul_f32_dpp v90,v14,v90 row_newbcast:14 row_mask:0xf bank_mask:0xf
v_mfma_f32_16x16x32_bf16 v[212:215],a[224:227],v[24:27],v[212:215]
buffer_store_dwordx4 v[156:159],v7,s[40:43],s8 offen offset:320 sc1
ds_read_u16 v52,v52
v_mul_f32_dpp v91,v14,v91 row_newbcast:15 row_mask:0xf bank_mask:0xf
v_lshlrev_b32_sdwa v54,1,v54 dst_sel:DWORD dst_unused:UNUSED_PAD src0_sel:DWORD src1_sel:WORD_1
v_cvt_pk_bf16_f32 v120,v120,v121
v_mfma_f32_16x16x32_bf16 v[172:175],a[248:251],v[24:27],v[172:175]
ds_read_u16_d16_hi v54,v54
ds_read_b64_tr_b16 a[224:225],v3 offset:32896
v_cvt_pk_bf16_f32 v121,v122,v123
v_lshlrev_b32_sdwa v53,1,v53 dst_sel:DWORD dst_unused:UNUSED_PAD src0_sel:DWORD src1_sel:WORD_1
v_mfma_f32_16x16x32_bf16 v[168:171],a[248:251],v[28:31],v[168:171]
ds_read_u16 v53,v53
ds_read_b64_tr_b16 a[226:227],v3 offset:34432
v_cvt_pk_bf16_f32 v122,v88,v89
v_cvt_pk_bf16_f32 v123,v90,v91
v_mfma_f32_16x16x32_bf16 v[216:219],a[228:231],v[28:31],v[216:219]
ds_read_b64_tr_b16 a[248:249],v3 offset:32904
ds_read_b64_tr_b16 a[250:251],v3 offset:34440
v_mul_f32_dpp v128,v15,v128 row_newbcast:0 row_mask:0xf bank_mask:0xf
v_lshlrev_b32_sdwa v55,1,v55 dst_sel:DWORD dst_unused:UNUSED_PAD src0_sel:DWORD src1_sel:WORD_1
v_mfma_f32_16x16x32_bf16 v[220:223],a[228:231],v[24:27],v[220:223]
ds_read_u16_d16_hi v55,v55
ds_read_b128 v[100:103],v4 offset:416
v_mul_f32_dpp v129,v15,v129 row_newbcast:1 row_mask:0xf bank_mask:0xf
v_lshlrev_b32_sdwa v56,1,v56 dst_sel:DWORD dst_unused:UNUSED_PAD src0_sel:DWORD src1_sel:WORD_1
v_mul_f32_dpp v130,v15,v130 row_newbcast:2 row_mask:0xf bank_mask:0xf
v_mfma_f32_16x16x32_bf16 v[188:191],a[236:239],v[24:27],v[188:191]
ds_read_u16 v56,v56
ds_read_b64_tr_b16 a[228:229],v3 offset:32960
v_mul_f32_dpp v131,v15,v131 row_newbcast:3 row_mask:0xf bank_mask:0xf
v_mfma_f32_16x16x32_bf16 v[184:187],a[236:239],v[28:31],v[184:187]
ds_read_b64_tr_b16 a[230:231],v3 offset:34496
ds_read_b64_tr_b16 a[236:237],v3 offset:32968
v_mul_f32_dpp v104,v15,v104 row_newbcast:4 row_mask:0xf bank_mask:0xf
v_lshlrev_b32_sdwa v58,1,v58 dst_sel:DWORD dst_unused:UNUSED_PAD src0_sel:DWORD src1_sel:WORD_1
v_mul_f32_dpp v105,v15,v105 row_newbcast:5 row_mask:0xf bank_mask:0xf
v_mfma_f32_16x16x32_bf16 v[224:227],a[244:247],v[28:31],v[224:227]
ds_read_u16_d16_hi v58,v58
ds_read_b64_tr_b16 a[238:239],v3 offset:34504
v_mul_f32_dpp v106,v15,v106 row_newbcast:6 row_mask:0xf bank_mask:0xf
v_lshlrev_b32_sdwa v57,1,v57 dst_sel:DWORD dst_unused:UNUSED_PAD src0_sel:DWORD src1_sel:WORD_1
v_mfma_f32_16x16x32_bf16 v[228:231],a[244:247],v[24:27],v[228:231]
ds_read_u16 v57,v57
ds_read_b64_tr_b16 a[216:217],v3 offset:51376
v_mul_f32_dpp v107,v15,v107 row_newbcast:7 row_mask:0xf bank_mask:0xf
v_cvt_pk_bf16_f32 v128,v128,v129
v_lshlrev_b32_sdwa v59,1,v59 dst_sel:DWORD dst_unused:UNUSED_PAD src0_sel:DWORD src1_sel:WORD_1
v_mfma_f32_16x16x32_bf16 v[204:207],a[252:255],v[24:27],v[204:207]
s_nop 0
buffer_store_dwordx4 v[112:115],v7,s[40:43],s9 offen offset:0 sc1
ds_read_u16_d16_hi v59,v59
v_cvt_pk_bf16_f32 v129,v130,v131
v_mfma_f32_16x16x32_bf16 v[200:203],a[252:255],v[28:31],v[200:203]
s_waitcnt lgkmcnt(15)
ds_read_b64_tr_b16 a[218:219],v3 offset:52912
ds_read_b128 v[20:23],v4 offset:512
v_cvt_pk_bf16_f32 v130,v104,v105
v_cvt_pk_bf16_f32 v131,v106,v107
v_mfma_f32_16x16x32_bf16 v[24:27],a[96:99],a[48:51],v[64:67]
ds_read_b128 v[16:19],v4 offset:544
ds_read_b64_tr_b16 a[220:221],v3 offset:57536
v_mul_f32_dpp v116,v14,v116 row_newbcast:0 row_mask:0xf bank_mask:0xf
v_lshlrev_b32_sdwa v36,1,v36 dst_sel:DWORD dst_unused:UNUSED_PAD src0_sel:DWORD src1_sel:WORD_1
v_mfma_f32_16x16x32_bf16 v[24:27],a[100:103],a[64:67],v[24:27]
ds_read_u16 v36,v36
ds_read_b64_tr_b16 a[222:223],v3 offset:59072
v_lshl_add_u64 v[60:61],v[60:61],0,v[62:63]
v_mul_f32_dpp v117,v14,v117 row_newbcast:1 row_mask:0xf bank_mask:0xf
v_mul_f32_dpp v118,v14,v118 row_newbcast:2 row_mask:0xf bank_mask:0xf
v_mfma_f32_16x16x32_bf16 v[24:27],a[104:107],a[80:83],v[24:27]
ds_read_b64_tr_b16 a[204:205],v3 offset:57544
v_lshl_add_u64 v[62:63],v[32:33],0,v[34:35]
v_mul_f32_dpp v119,v14,v119 row_newbcast:3 row_mask:0xf bank_mask:0xf
v_lshlrev_b32_sdwa v38,1,v38 dst_sel:DWORD dst_unused:UNUSED_PAD src0_sel:DWORD src1_sel:WORD_1
v_mfma_f32_16x16x32_bf16 v[28:31],a[108:111],a[80:83],v[68:71]
ds_read_u16_d16_hi v38,v38
v_mul_f32_dpp v76,v14,v76 row_newbcast:4 row_mask:0xf bank_mask:0xf
v_lshlrev_b32_sdwa v37,1,v37 dst_sel:DWORD dst_unused:UNUSED_PAD src0_sel:DWORD src1_sel:WORD_1
v_mul_f32_dpp v77,v14,v77 row_newbcast:5 row_mask:0xf bank_mask:0xf
v_mfma_f32_16x16x32_bf16 v[28:31],a[112:115],a[64:67],v[28:31]
s_waitcnt lgkmcnt(13)
ds_read_u16 v37,v37
v_lshl_add_u64 v[32:33],v[52:53],0,v[54:55]
v_mul_f32_dpp v78,v14,v78 row_newbcast:6 row_mask:0xf bank_mask:0xf
v_mfma_f32_16x16x32_bf16 v[28:31],a[116:119],a[48:51],v[28:31]
s_nop 0
ds_read_b64_tr_b16 a[206:207],v3 offset:59080
v_mul_f32_dpp v79,v14,v79 row_newbcast:7 row_mask:0xf bank_mask:0xf
v_lshlrev_b32_sdwa v39,1,v39 dst_sel:DWORD dst_unused:UNUSED_PAD src0_sel:DWORD src1_sel:WORD_1
v_cvt_pk_bf16_f32 v116,v116,v117
v_mfma_f32_16x16x32_bf16 v[52:55],a[120:123],a[48:51],v[84:87]
s_nop 0
ds_read_u16_d16_hi v39,v39
v_cvt_pk_bf16_f32 v117,v118,v119
v_lshlrev_b32_sdwa v40,1,v40 dst_sel:DWORD dst_unused:UNUSED_PAD src0_sel:DWORD src1_sel:WORD_1
v_mfma_f32_16x16x32_bf16 v[52:55],a[124:127],a[64:67],v[52:55]
s_waitcnt lgkmcnt(11)
ds_read_u16 v40,v40
ds_read_b64_tr_b16 a[208:209],v3 offset:51384
v_lshl_add_u64 v[34:35],v[56:57],0,v[58:59]
v_cvt_pk_bf16_f32 v118,v76,v77
v_cvt_pk_bf16_f32 v119,v78,v79
v_lshlrev_b32_sdwa v42,1,v42 dst_sel:DWORD dst_unused:UNUSED_PAD src0_sel:DWORD src1_sel:WORD_1
v_mfma_f32_16x16x32_bf16 v[52:55],a[128:131],a[80:83],v[52:55]
buffer_store_dwordx4 v[120:123],v7,s[40:43],s9 offen offset:64 sc1
ds_read_u16_d16_hi v42,v42
v_mul_f32_dpp v124,v14,v124 row_newbcast:8 row_mask:0xf bank_mask:0xf
v_mfma_f32_16x16x32_bf16 v[56:59],a[132:135],a[80:83],v[80:83]
ds_read_b64_tr_b16 a[210:211],v3 offset:52920
v_mul_f32_dpp v125,v14,v125 row_newbcast:9 row_mask:0xf bank_mask:0xf
v_lshlrev_b32_sdwa v41,1,v41 dst_sel:DWORD dst_unused:UNUSED_PAD src0_sel:DWORD src1_sel:WORD_1
v_mul_f32_dpp v126,v14,v126 row_newbcast:10 row_mask:0xf bank_mask:0xf
v_mfma_f32_16x16x32_bf16 v[56:59],a[136:139],a[64:67],v[56:59]
ds_read_u16 v41,v41
v_mul_f32_dpp v127,v14,v127 row_newbcast:11 row_mask:0xf bank_mask:0xf
v_lshlrev_b32_sdwa v43,1,v43 dst_sel:DWORD dst_unused:UNUSED_PAD src0_sel:DWORD src1_sel:WORD_1
v_mfma_f32_16x16x32_bf16 v[56:59],a[140:143],a[48:51],v[56:59]
ds_read_u16_d16_hi v43,v43
v_mul_f32_dpp v92,v14,v92 row_newbcast:12 row_mask:0xf bank_mask:0xf
v_mul_f32_dpp v93,v14,v93 row_newbcast:13 row_mask:0xf bank_mask:0xf
v_mfma_f32_16x16x32_bf16 v[232:235],a[232:235],v[60:63],v[232:235]
ds_read_b64_tr_b16 a[244:245],v3 offset:51440
v_lshlrev_b32_sdwa v44,1,v44 dst_sel:DWORD dst_unused:UNUSED_PAD src0_sel:DWORD src1_sel:WORD_1
v_mul_f32_dpp v94,v14,v94 row_newbcast:14 row_mask:0xf bank_mask:0xf
v_mfma_f32_16x16x32_bf16 v[236:239],a[232:235],v[32:35],v[236:239]
ds_read_u16 v44,v44
v_mul_f32_dpp v95,v14,v95 row_newbcast:15 row_mask:0xf bank_mask:0xf
v_lshlrev_b32_sdwa v46,1,v46 dst_sel:DWORD dst_unused:UNUSED_PAD src0_sel:DWORD src1_sel:WORD_1
v_cvt_pk_bf16_f32 v124,v124,v125
v_mfma_f32_16x16x32_bf16 v[164:167],a[240:243],v[32:35],v[164:167]
ds_read_u16_d16_hi v46,v46
v_cvt_pk_bf16_f32 v125,v126,v127
v_lshlrev_b32_sdwa v45,1,v45 dst_sel:DWORD dst_unused:UNUSED_PAD src0_sel:DWORD src1_sel:WORD_1
v_mfma_f32_16x16x32_bf16 v[160:163],a[240:243],v[60:63],v[160:163]
ds_read_u16 v45,v45
v_cvt_pk_bf16_f32 v126,v92,v93
v_cvt_pk_bf16_f32 v127,v94,v95
v_mfma_f32_16x16x32_bf16 v[240:243],a[224:227],v[60:63],v[240:243]
ds_read_b64_tr_b16 a[246:247],v3 offset:52976
v_mul_f32_dpp v132,v15,v132 row_newbcast:0 row_mask:0xf bank_mask:0xf
v_lshlrev_b32_sdwa v47,1,v47 dst_sel:DWORD dst_unused:UNUSED_PAD src0_sel:DWORD src1_sel:WORD_1
v_mfma_f32_16x16x32_bf16 v[244:247],a[224:227],v[32:35],v[244:247]
ds_read_u16_d16_hi v47,v47
v_mul_f32_dpp v133,v15,v133 row_newbcast:1 row_mask:0xf bank_mask:0xf
v_lshlrev_b32_sdwa v48,1,v48 dst_sel:DWORD dst_unused:UNUSED_PAD src0_sel:DWORD src1_sel:WORD_1
v_mul_f32_dpp v134,v15,v134 row_newbcast:2 row_mask:0xf bank_mask:0xf
v_mfma_f32_16x16x32_bf16 v[180:183],a[248:251],v[32:35],v[180:183]
ds_read_u16 v48,v48
v_mul_f32_dpp v135,v15,v135 row_newbcast:3 row_mask:0xf bank_mask:0xf
v_mfma_f32_16x16x32_bf16 v[176:179],a[248:251],v[60:63],v[176:179]
buffer_store_dwordx4 v[128:131],v7,s[40:43],s9 offen offset:128 sc1
v_mul_f32_dpp v108,v15,v108 row_newbcast:4 row_mask:0xf bank_mask:0xf
v_lshlrev_b32_sdwa v50,1,v50 dst_sel:DWORD dst_unused:UNUSED_PAD src0_sel:DWORD src1_sel:WORD_1
v_mul_f32_dpp v109,v15,v109 row_newbcast:5 row_mask:0xf bank_mask:0xf
v_mfma_f32_16x16x32_bf16 v[248:251],a[228:231],v[60:63],v[248:251]
ds_read_u16_d16_hi v50,v50
v_mul_f32_dpp v110,v15,v110 row_newbcast:6 row_mask:0xf bank_mask:0xf
v_lshlrev_b32_sdwa v49,1,v49 dst_sel:DWORD dst_unused:UNUSED_PAD src0_sel:DWORD src1_sel:WORD_1
v_mfma_f32_16x16x32_bf16 v[252:255],a[228:231],v[32:35],v[252:255]
ds_read_u16 v49,v49
v_mul_f32_dpp v111,v15,v111 row_newbcast:7 row_mask:0xf bank_mask:0xf
v_cvt_pk_bf16_f32 v132,v132,v133
v_lshlrev_b32_sdwa v51,1,v51 dst_sel:DWORD dst_unused:UNUSED_PAD src0_sel:DWORD src1_sel:WORD_1
v_mfma_f32_16x16x32_bf16 v[196:199],a[236:239],v[32:35],v[196:199]
ds_read_u16_d16_hi v51,v51
v_cvt_pk_bf16_f32 v133,v134,v135
v_mfma_f32_16x16x32_bf16 v[192:195],a[236:239],v[60:63],v[192:195]
ds_read_b64_tr_b16 a[252:253],v3 offset:57600
v_cvt_pk_bf16_f32 v134,v108,v109
v_cvt_pk_bf16_f32 v135,v110,v111
v_mfma_f32_16x16x32_bf16 v[32:35],a[144:147],a[48:51],v[96:99]
ds_read_b64_tr_b16 a[254:255],v3 offset:59136
v_lshlrev_b32_sdwa v24,1,v24 dst_sel:DWORD dst_unused:UNUSED_PAD src0_sel:DWORD src1_sel:WORD_1
v_mfma_f32_16x16x32_bf16 v[32:35],a[148:151],a[64:67],v[32:35]
s_waitcnt lgkmcnt(7)
ds_read_u16 v24,v24
v_lshl_add_u64 v[36:37],v[36:37],0,v[38:39]
v_mfma_f32_16x16x32_bf16 v[32:35],a[152:155],a[80:83],v[32:35]
s_nop 0
v_lshl_add_u64 v[38:39],v[40:41],0,v[42:43]
v_lshlrev_b32_sdwa v26,1,v26 dst_sel:DWORD dst_unused:UNUSED_PAD src0_sel:DWORD src1_sel:WORD_1
v_mfma_f32_16x16x32_bf16 v[60:63],a[156:159],a[80:83],v[100:103]
ds_read_u16_d16_hi v26,v26
v_lshlrev_b32_sdwa v25,1,v25 dst_sel:DWORD dst_unused:UNUSED_PAD src0_sel:DWORD src1_sel:WORD_1
v_mfma_f32_16x16x32_bf16 v[60:63],a[160:163],a[64:67],v[60:63]
s_waitcnt lgkmcnt(7)
ds_read_u16 v25,v25
v_lshl_add_u64 v[40:41],v[44:45],0,v[46:47]
v_mfma_f32_16x16x32_bf16 v[60:63],a[164:167],a[48:51],v[60:63]
v_lshlrev_b32_sdwa v27,1,v27 dst_sel:DWORD dst_unused:UNUSED_PAD src0_sel:DWORD src1_sel:WORD_1
v_lshlrev_b32_sdwa v28,1,v28 dst_sel:DWORD dst_unused:UNUSED_PAD src0_sel:DWORD src1_sel:WORD_1
v_mfma_f32_16x16x32_bf16 v[44:47],a[168:171],a[48:51],v[16:19]
ds_read_u16_d16_hi v27,v27
v_lshlrev_b32_sdwa v30,1,v30 dst_sel:DWORD dst_unused:UNUSED_PAD src0_sel:DWORD src1_sel:WORD_1
v_mfma_f32_16x16x32_bf16 v[44:47],a[172:175],a[64:67],v[44:47]
s_waitcnt lgkmcnt(6)
ds_read_u16 v28,v28
v_lshl_add_u64 v[42:43],v[48:49],0,v[50:51]
v_mfma_f32_16x16x32_bf16 v[44:47],a[176:179],a[80:83],v[44:47]
buffer_store_dwordx4 v[116:119],v7,s[40:43],s9 offen offset:192 sc1
v_lshlrev_b32_sdwa v29,1,v29 dst_sel:DWORD dst_unused:UNUSED_PAD src0_sel:DWORD src1_sel:WORD_1
v_mfma_f32_16x16x32_bf16 v[48:51],a[180:183],a[80:83],v[20:23]
ds_read_u16_d16_hi v30,v30
v_lshlrev_b32_sdwa v31,1,v31 dst_sel:DWORD dst_unused:UNUSED_PAD src0_sel:DWORD src1_sel:WORD_1
v_mfma_f32_16x16x32_bf16 v[48:51],a[184:187],a[64:67],v[48:51]
ds_read_u16 v29,v29
v_lshlrev_b32_sdwa v56,1,v56 dst_sel:DWORD dst_unused:UNUSED_PAD src0_sel:DWORD src1_sel:WORD_1
v_mfma_f32_16x16x32_bf16 v[48:51],a[188:191],a[48:51],v[48:51]
ds_read_u16_d16_hi v31,v31
v_lshlrev_b32_sdwa v58,1,v58 dst_sel:DWORD dst_unused:UNUSED_PAD src0_sel:DWORD src1_sel:WORD_1
v_mfma_f32_16x16x32_bf16 v[208:211],a[232:235],v[36:39],v[208:211]
ds_read_u16 v56,v56
v_lshlrev_b32_sdwa v57,1,v57 dst_sel:DWORD dst_unused:UNUSED_PAD src0_sel:DWORD src1_sel:WORD_1
v_mfma_f32_16x16x32_bf16 v[212:215],a[232:235],v[40:43],v[212:215]
ds_read_u16_d16_hi v58,v58
v_lshlrev_b32_sdwa v59,1,v59 dst_sel:DWORD dst_unused:UNUSED_PAD src0_sel:DWORD src1_sel:WORD_1
v_mfma_f32_16x16x32_bf16 v[172:175],a[240:243],v[40:43],v[172:175]
ds_read_u16 v57,v57
v_lshlrev_b32_sdwa v52,1,v52 dst_sel:DWORD dst_unused:UNUSED_PAD src0_sel:DWORD src1_sel:WORD_1
v_mfma_f32_16x16x32_bf16 v[168:171],a[240:243],v[36:39],v[168:171]
ds_read_u16_d16_hi v59,v59
ds_read_u16 v52,v52
v_mfma_f32_16x16x32_bf16 v[216:219],a[224:227],v[36:39],v[216:219]
ds_read_b64_tr_b16 a[232:233],v3 offset:57608
v_lshlrev_b32_sdwa v54,1,v54 dst_sel:DWORD dst_unused:UNUSED_PAD src0_sel:DWORD src1_sel:WORD_1
v_mfma_f32_16x16x32_bf16 v[220:223],a[224:227],v[40:43],v[220:223]
ds_read_u16_d16_hi v54,v54
v_lshlrev_b32_sdwa v53,1,v53 dst_sel:DWORD dst_unused:UNUSED_PAD src0_sel:DWORD src1_sel:WORD_1
v_mfma_f32_16x16x32_bf16 v[188:191],a[248:251],v[40:43],v[188:191]
ds_read_u16 v53,v53
v_lshlrev_b32_sdwa v55,1,v55 dst_sel:DWORD dst_unused:UNUSED_PAD src0_sel:DWORD src1_sel:WORD_1
v_mfma_f32_16x16x32_bf16 v[184:187],a[248:251],v[36:39],v[184:187]
ds_read_u16_d16_hi v55,v55
v_mfma_f32_16x16x32_bf16 v[224:227],a[228:231],v[36:39],v[224:227]
ds_read_b64_tr_b16 a[234:235],v3 offset:59144
v_mfma_f32_16x16x32_bf16 v[228:231],a[228:231],v[40:43],v[228:231]
ds_read_b64_tr_b16 a[240:241],v3 offset:51448
v_mfma_f32_16x16x32_bf16 v[204:207],a[236:239],v[40:43],v[204:207]
ds_read_b64_tr_b16 a[242:243],v3 offset:52984
v_mfma_f32_16x16x32_bf16 v[200:203],a[236:239],v[36:39],v[200:203]
buffer_store_dwordx4 v[124:127],v7,s[40:43],s9 offen offset:256 sc1
v_mfma_f32_16x16x32_bf16 v[40:43],a[96:99],a[52:55],v[64:67]
v_lshlrev_b32_sdwa v32,1,v32 dst_sel:DWORD dst_unused:UNUSED_PAD src0_sel:DWORD src1_sel:WORD_1
v_lshlrev_b32_sdwa v34,1,v34 dst_sel:DWORD dst_unused:UNUSED_PAD src0_sel:DWORD src1_sel:WORD_1
v_mfma_f32_16x16x32_bf16 v[40:43],a[100:103],a[68:71],v[40:43]
s_waitcnt lgkmcnt(6)
ds_read_u16 v32,v32
v_lshl_add_u64 v[24:25],v[24:25],0,v[26:27]
v_mfma_f32_16x16x32_bf16 v[40:43],a[104:107],a[84:87],v[40:43]
ds_read_u16_d16_hi v34,v34
v_lshl_add_u64 v[26:27],v[28:29],0,v[30:31]
v_mfma_f32_16x16x32_bf16 v[36:39],a[108:111],a[84:87],v[68:71]
v_lshlrev_b32_sdwa v33,1,v33 dst_sel:DWORD dst_unused:UNUSED_PAD src0_sel:DWORD src1_sel:WORD_1
v_lshlrev_b32_sdwa v35,1,v35 dst_sel:DWORD dst_unused:UNUSED_PAD src0_sel:DWORD src1_sel:WORD_1
v_mfma_f32_16x16x32_bf16 v[36:39],a[112:115],a[68:71],v[36:39]
ds_read_u16 v33,v33
v_lshl_add_u64 v[28:29],v[56:57],0,v[58:59]
v_mfma_f32_16x16x32_bf16 v[36:39],a[116:119],a[52:55],v[36:39]
ds_read_u16_d16_hi v35,v35
v_lshlrev_b32_sdwa v60,1,v60 dst_sel:DWORD dst_unused:UNUSED_PAD src0_sel:DWORD src1_sel:WORD_1
v_mfma_f32_16x16x32_bf16 v[56:59],a[120:123],a[52:55],v[84:87]
ds_read_u16 v60,v60
v_lshlrev_b32_sdwa v62,1,v62 dst_sel:DWORD dst_unused:UNUSED_PAD src0_sel:DWORD src1_sel:WORD_1
v_mfma_f32_16x16x32_bf16 v[56:59],a[124:127],a[68:71],v[56:59]
s_waitcnt lgkmcnt(5)
ds_read_u16_d16_hi v62,v62
v_lshl_add_u64 v[30:31],v[52:53],0,v[54:55]
v_mfma_f32_16x16x32_bf16 v[56:59],a[128:131],a[84:87],v[56:59]
v_lshlrev_b32_sdwa v61,1,v61 dst_sel:DWORD dst_unused:UNUSED_PAD src0_sel:DWORD src1_sel:WORD_1
v_lshlrev_b32_sdwa v63,1,v63 dst_sel:DWORD dst_unused:UNUSED_PAD src0_sel:DWORD src1_sel:WORD_1
v_mfma_f32_16x16x32_bf16 v[52:55],a[132:135],a[84:87],v[80:83]
ds_read_u16 v61,v61
v_lshlrev_b32_sdwa v48,1,v48 dst_sel:DWORD dst_unused:UNUSED_PAD src0_sel:DWORD src1_sel:WORD_1
v_mfma_f32_16x16x32_bf16 v[52:55],a[136:139],a[68:71],v[52:55]
ds_read_u16_d16_hi v63,v63
v_lshlrev_b32_sdwa v50,1,v50 dst_sel:DWORD dst_unused:UNUSED_PAD src0_sel:DWORD src1_sel:WORD_1
v_mfma_f32_16x16x32_bf16 v[52:55],a[140:143],a[52:55],v[52:55]
ds_read_u16 v48,v48
v_lshlrev_b32_sdwa v49,1,v49 dst_sel:DWORD dst_unused:UNUSED_PAD src0_sel:DWORD src1_sel:WORD_1
v_mfma_f32_16x16x32_bf16 v[232:235],a[0:3],v[24:27],v[232:235]
ds_read_u16_d16_hi v50,v50
v_lshlrev_b32_sdwa v51,1,v51 dst_sel:DWORD dst_unused:UNUSED_PAD src0_sel:DWORD src1_sel:WORD_1
v_mfma_f32_16x16x32_bf16 v[232:235],a[4:7],v[28:31],v[232:235]
ds_read_u16 v49,v49
v_lshlrev_b32_sdwa v44,1,v44 dst_sel:DWORD dst_unused:UNUSED_PAD src0_sel:DWORD src1_sel:WORD_1
v_mfma_f32_16x16x32_bf16 v[160:163],a[8:11],v[28:31],v[160:163]
buffer_store_dwordx4 v[132:135],v7,s[40:43],s9 offen offset:320 sc1
v_lshlrev_b32_sdwa v46,1,v46 dst_sel:DWORD dst_unused:UNUSED_PAD src0_sel:DWORD src1_sel:WORD_1
v_mfma_f32_16x16x32_bf16 v[160:163],a[12:15],v[24:27],v[160:163]
ds_read_u16_d16_hi v51,v51
v_lshlrev_b32_sdwa v45,1,v45 dst_sel:DWORD dst_unused:UNUSED_PAD src0_sel:DWORD src1_sel:WORD_1
v_mfma_f32_16x16x32_bf16 v[240:243],a[16:19],v[24:27],v[240:243]
ds_read_u16 v44,v44
v_lshlrev_b32_sdwa v47,1,v47 dst_sel:DWORD dst_unused:UNUSED_PAD src0_sel:DWORD src1_sel:WORD_1
v_mfma_f32_16x16x32_bf16 v[240:243],a[20:23],v[28:31],v[240:243]
ds_read_u16_d16_hi v46,v46
v_mfma_f32_16x16x32_bf16 v[176:179],a[24:27],v[28:31],v[176:179]
ds_read_u16 v45,v45
v_mfma_f32_16x16x32_bf16 v[176:179],a[28:31],v[24:27],v[176:179]
ds_read_u16_d16_hi v47,v47
v_mfma_f32_16x16x32_bf16 v[248:251],a[32:35],v[24:27],v[248:251]
v_mfma_f32_16x16x32_bf16 v[248:251],a[36:39],v[28:31],v[248:251]
v_mfma_f32_16x16x32_bf16 v[192:195],a[40:43],v[28:31],v[192:195]
v_mfma_f32_16x16x32_bf16 v[192:195],a[44:47],v[24:27],v[192:195]
v_mfma_f32_16x16x32_bf16 v[28:31],a[144:147],a[52:55],v[96:99]
v_lshlrev_b32_sdwa v40,1,v40 dst_sel:DWORD dst_unused:UNUSED_PAD src0_sel:DWORD src1_sel:WORD_1
v_lshlrev_b32_sdwa v42,1,v42 dst_sel:DWORD dst_unused:UNUSED_PAD src0_sel:DWORD src1_sel:WORD_1
v_mfma_f32_16x16x32_bf16 v[28:31],a[148:151],a[68:71],v[28:31]
s_waitcnt lgkmcnt(3)
ds_read_u16 v40,v40
v_lshl_add_u64 v[32:33],v[32:33],0,v[34:35]
v_mfma_f32_16x16x32_bf16 v[28:31],a[152:155],a[84:87],v[28:31]
ds_read_u16_d16_hi v42,v42
v_lshl_add_u64 v[34:35],v[60:61],0,v[62:63]
v_mfma_f32_16x16x32_bf16 v[24:27],a[156:159],a[84:87],v[100:103]
v_lshl_add_u64 v[60:61],v[48:49],0,v[50:51]
v_lshlrev_b32_sdwa v41,1,v41 dst_sel:DWORD dst_unused:UNUSED_PAD src0_sel:DWORD src1_sel:WORD_1
v_mfma_f32_16x16x32_bf16 v[24:27],a[160:163],a[68:71],v[24:27]
s_waitcnt lgkmcnt(2)
ds_read_u16 v41,v41
v_lshl_add_u64 v[62:63],v[44:45],0,v[46:47]
v_mfma_f32_16x16x32_bf16 v[24:27],a[164:167],a[52:55],v[24:27]
v_lshlrev_b32_sdwa v43,1,v43 dst_sel:DWORD dst_unused:UNUSED_PAD src0_sel:DWORD src1_sel:WORD_1
v_lshlrev_b32_sdwa v36,1,v36 dst_sel:DWORD dst_unused:UNUSED_PAD src0_sel:DWORD src1_sel:WORD_1
v_mfma_f32_16x16x32_bf16 v[232:235],a[212:215],v[32:35],v[232:235]
ds_read_u16_d16_hi v43,v43
v_lshlrev_b32_sdwa v38,1,v38 dst_sel:DWORD dst_unused:UNUSED_PAD src0_sel:DWORD src1_sel:WORD_1
v_mfma_f32_16x16x32_bf16 v[232:235],a[200:203],v[60:63],v[232:235]
ds_read_u16 v36,v36
v_lshlrev_b32_sdwa v37,1,v37 dst_sel:DWORD dst_unused:UNUSED_PAD src0_sel:DWORD src1_sel:WORD_1
v_mfma_f32_16x16x32_bf16 v[160:163],a[196:199],v[60:63],v[160:163]
ds_read_u16_d16_hi v38,v38
v_lshlrev_b32_sdwa v39,1,v39 dst_sel:DWORD dst_unused:UNUSED_PAD src0_sel:DWORD src1_sel:WORD_1
v_mfma_f32_16x16x32_bf16 v[160:163],a[192:195],v[32:35],v[160:163]
ds_read_u16 v37,v37
v_lshlrev_b32_sdwa v52,1,v52 dst_sel:DWORD dst_unused:UNUSED_PAD src0_sel:DWORD src1_sel:WORD_1
v_mfma_f32_16x16x32_bf16 v[240:243],a[216:219],v[32:35],v[240:243]
ds_read_u16_d16_hi v39,v39
v_lshlrev_b32_sdwa v54,1,v54 dst_sel:DWORD dst_unused:UNUSED_PAD src0_sel:DWORD src1_sel:WORD_1
v_mfma_f32_16x16x32_bf16 v[240:243],a[220:223],v[60:63],v[240:243]
ds_read_u16 v52,v52
v_mul_f32_dpp v232,v14,v232 row_newbcast:0 row_mask:0xf bank_mask:0xf
s_add_u32 s10,s61,4718592
v_mfma_f32_16x16x32_bf16 v[176:179],a[204:207],v[60:63],v[176:179]
ds_read_u16_d16_hi v54,v54
v_mul_f32_dpp v233,v14,v233 row_newbcast:1 row_mask:0xf bank_mask:0xf
v_mfma_f32_16x16x32_bf16 v[176:179],a[208:211],v[32:35],v[176:179]
v_mul_f32_dpp v234,v14,v234 row_newbcast:2 row_mask:0xf bank_mask:0xf
v_lshlrev_b32_sdwa v53,1,v53 dst_sel:DWORD dst_unused:UNUSED_PAD src0_sel:DWORD src1_sel:WORD_1
v_mfma_f32_16x16x32_bf16 v[248:251],a[244:247],v[32:35],v[248:251]
ds_read_u16 v53,v53
v_mul_f32_dpp v235,v14,v235 row_newbcast:3 row_mask:0xf bank_mask:0xf
v_mfma_f32_16x16x32_bf16 v[248:251],a[252:255],v[60:63],v[248:251]
v_mul_f32_dpp v160,v14,v160 row_newbcast:4 row_mask:0xf bank_mask:0xf
v_lshlrev_b32_sdwa v55,1,v55 dst_sel:DWORD dst_unused:UNUSED_PAD src0_sel:DWORD src1_sel:WORD_1
v_mfma_f32_16x16x32_bf16 v[192:195],a[232:235],v[60:63],v[192:195]
ds_read_u16_d16_hi v55,v55
v_mul_f32_dpp v161,v14,v161 row_newbcast:5 row_mask:0xf bank_mask:0xf
v_mfma_f32_16x16x32_bf16 v[192:195],a[240:243],v[32:35],v[192:195]
v_mul_f32_dpp v162,v14,v162 row_newbcast:6 row_mask:0xf bank_mask:0xf
v_lshlrev_b32_sdwa v56,1,v56 dst_sel:DWORD dst_unused:UNUSED_PAD src0_sel:DWORD src1_sel:WORD_1
v_mfma_f32_16x16x32_bf16 v[48:51],a[168:171],a[52:55],v[16:19]
ds_read_u16 v56,v56
v_mul_f32_dpp v163,v14,v163 row_newbcast:7 row_mask:0xf bank_mask:0xf
v_mfma_f32_16x16x32_bf16 v[48:51],a[172:175],a[68:71],v[48:51]
v_cvt_pk_bf16_f32 v232,v232,v233
v_lshlrev_b32_sdwa v58,1,v58 dst_sel:DWORD dst_unused:UNUSED_PAD src0_sel:DWORD src1_sel:WORD_1
v_mfma_f32_16x16x32_bf16 v[48:51],a[176:179],a[84:87],v[48:51]
ds_read_u16_d16_hi v58,v58
v_cvt_pk_bf16_f32 v233,v234,v235
v_lshlrev_b32_sdwa v57,1,v57 dst_sel:DWORD dst_unused:UNUSED_PAD src0_sel:DWORD src1_sel:WORD_1
v_mfma_f32_16x16x32_bf16 v[44:47],a[180:183],a[84:87],v[20:23]
ds_read_u16 v57,v57
v_cvt_pk_bf16_f32 v234,v160,v161
v_lshlrev_b32_sdwa v59,1,v59 dst_sel:DWORD dst_unused:UNUSED_PAD src0_sel:DWORD src1_sel:WORD_1
v_mfma_f32_16x16x32_bf16 v[44:47],a[184:187],a[68:71],v[44:47]
ds_read_u16_d16_hi v59,v59
v_cvt_pk_bf16_f32 v235,v162,v163
v_mfma_f32_16x16x32_bf16 v[44:47],a[188:191],a[52:55],v[44:47]
buffer_store_dwordx4 v[232:235],v7,s[40:43],s10 offen offset:0 sc1
v_mul_f32_dpp v240,v14,v240 row_newbcast:8 row_mask:0xf bank_mask:0xf
v_mfma_f32_16x16x32_bf16 v[60:63],a[96:99],a[56:59],v[64:67]
v_lshlrev_b32_sdwa v28,1,v28 dst_sel:DWORD dst_unused:UNUSED_PAD src0_sel:DWORD src1_sel:WORD_1
v_mul_f32_dpp v241,v14,v241 row_newbcast:9 row_mask:0xf bank_mask:0xf
v_mfma_f32_16x16x32_bf16 v[60:63],a[100:103],a[72:75],v[60:63]
s_waitcnt lgkmcnt(4)
ds_read_u16 v28,v28
v_lshl_add_u64 v[40:41],v[40:41],0,v[42:43]
v_mfma_f32_16x16x32_bf16 v[60:63],a[104:107],a[88:91],v[60:63]
v_lshl_add_u64 v[42:43],v[36:37],0,v[38:39]
v_mul_f32_dpp v242,v14,v242 row_newbcast:10 row_mask:0xf bank_mask:0xf
v_lshlrev_b32_sdwa v30,1,v30 dst_sel:DWORD dst_unused:UNUSED_PAD src0_sel:DWORD src1_sel:WORD_1
v_mfma_f32_16x16x32_bf16 v[32:35],a[108:111],a[88:91],v[68:71]
ds_read_u16_d16_hi v30,v30
v_mul_f32_dpp v243,v14,v243 row_newbcast:11 row_mask:0xf bank_mask:0xf
v_lshlrev_b32_sdwa v29,1,v29 dst_sel:DWORD dst_unused:UNUSED_PAD src0_sel:DWORD src1_sel:WORD_1
v_mfma_f32_16x16x32_bf16 v[32:35],a[112:115],a[72:75],v[32:35]
ds_read_u16 v29,v29
v_lshl_add_u64 v[36:37],v[52:53],0,v[54:55]
v_mul_f32_dpp v176,v14,v176 row_newbcast:12 row_mask:0xf bank_mask:0xf
v_mfma_f32_16x16x32_bf16 v[32:35],a[116:119],a[56:59],v[32:35]
v_mul_f32_dpp v177,v14,v177 row_newbcast:13 row_mask:0xf bank_mask:0xf
v_lshlrev_b32_sdwa v31,1,v31 dst_sel:DWORD dst_unused:UNUSED_PAD src0_sel:DWORD src1_sel:WORD_1
v_mfma_f32_16x16x32_bf16 v[52:55],a[120:123],a[56:59],v[84:87]
ds_read_u16_d16_hi v31,v31
v_mul_f32_dpp v178,v14,v178 row_newbcast:14 row_mask:0xf bank_mask:0xf
v_lshlrev_b32_sdwa v24,1,v24 dst_sel:DWORD dst_unused:UNUSED_PAD src0_sel:DWORD src1_sel:WORD_1
v_mfma_f32_16x16x32_bf16 v[52:55],a[124:127],a[72:75],v[52:55]
s_waitcnt lgkmcnt(4)
ds_read_u16 v24,v24
v_lshl_add_u64 v[38:39],v[56:57],0,v[58:59]
v_mul_f32_dpp v179,v14,v179 row_newbcast:15 row_mask:0xf bank_mask:0xf
v_lshlrev_b32_sdwa v26,1,v26 dst_sel:DWORD dst_unused:UNUSED_PAD src0_sel:DWORD src1_sel:WORD_1
v_mfma_f32_16x16x32_bf16 v[52:55],a[128:131],a[88:91],v[52:55]
ds_read_u16_d16_hi v26,v26
v_cvt_pk_bf16_f32 v240,v240,v241
v_mfma_f32_16x16x32_bf16 v[56:59],a[132:135],a[88:91],v[80:83]
v_cvt_pk_bf16_f32 v241,v242,v243
v_lshlrev_b32_sdwa v25,1,v25 dst_sel:DWORD dst_unused:UNUSED_PAD src0_sel:DWORD src1_sel:WORD_1
v_mfma_f32_16x16x32_bf16 v[56:59],a[136:139],a[72:75],v[56:59]
ds_read_u16 v25,v25
v_cvt_pk_bf16_f32 v242,v176,v177
v_lshlrev_b32_sdwa v27,1,v27 dst_sel:DWORD dst_unused:UNUSED_PAD src0_sel:DWORD src1_sel:WORD_1
v_mfma_f32_16x16x32_bf16 v[56:59],a[140:143],a[56:59],v[56:59]
ds_read_u16_d16_hi v27,v27
v_cvt_pk_bf16_f32 v243,v178,v179
v_mfma_f32_16x16x32_bf16 v[236:239],a[0:3],v[40:43],v[236:239]
buffer_store_dwordx4 v[240:243],v7,s[40:43],s10 offen offset:64 sc1
v_lshlrev_b32_sdwa v44,1,v44 dst_sel:DWORD dst_unused:UNUSED_PAD src0_sel:DWORD src1_sel:WORD_1
v_mul_f32_dpp v248,v15,v248 row_newbcast:0 row_mask:0xf bank_mask:0xf
v_mfma_f32_16x16x32_bf16 v[236:239],a[4:7],v[36:39],v[236:239]
ds_read_u16 v44,v44
v_mul_f32_dpp v249,v15,v249 row_newbcast:1 row_mask:0xf bank_mask:0xf
v_lshlrev_b32_sdwa v46,1,v46 dst_sel:DWORD dst_unused:UNUSED_PAD src0_sel:DWORD src1_sel:WORD_1
v_mfma_f32_16x16x32_bf16 v[164:167],a[8:11],v[36:39],v[164:167]
ds_read_u16_d16_hi v46,v46
v_mul_f32_dpp v250,v15,v250 row_newbcast:2 row_mask:0xf bank_mask:0xf
v_lshlrev_b32_sdwa v45,1,v45 dst_sel:DWORD dst_unused:UNUSED_PAD src0_sel:DWORD src1_sel:WORD_1
v_mfma_f32_16x16x32_bf16 v[164:167],a[12:15],v[40:43],v[164:167]
ds_read_u16 v45,v45
v_mul_f32_dpp v251,v15,v251 row_newbcast:3 row_mask:0xf bank_mask:0xf
v_mfma_f32_16x16x32_bf16 v[244:247],a[16:19],v[40:43],v[244:247]
v_lshlrev_b32_sdwa v47,1,v47 dst_sel:DWORD dst_unused:UNUSED_PAD src0_sel:DWORD src1_sel:WORD_1
v_mul_f32_dpp v192,v15,v192 row_newbcast:4 row_mask:0xf bank_mask:0xf
v_mfma_f32_16x16x32_bf16 v[244:247],a[20:23],v[36:39],v[244:247]
ds_read_u16_d16_hi v47,v47
v_mul_f32_dpp v193,v15,v193 row_newbcast:5 row_mask:0xf bank_mask:0xf
v_lshlrev_b32_sdwa v48,1,v48 dst_sel:DWORD dst_unused:UNUSED_PAD src0_sel:DWORD src1_sel:WORD_1
v_mfma_f32_16x16x32_bf16 v[180:183],a[24:27],v[36:39],v[180:183]
ds_read_u16 v48,v48
v_mul_f32_dpp v194,v15,v194 row_newbcast:6 row_mask:0xf bank_mask:0xf
v_mfma_f32_16x16x32_bf16 v[180:183],a[28:31],v[40:43],v[180:183]
v_lshlrev_b32_sdwa v50,1,v50 dst_sel:DWORD dst_unused:UNUSED_PAD src0_sel:DWORD src1_sel:WORD_1
v_mul_f32_dpp v195,v15,v195 row_newbcast:7 row_mask:0xf bank_mask:0xf
v_mfma_f32_16x16x32_bf16 v[252:255],a[32:35],v[40:43],v[252:255]
ds_read_u16_d16_hi v50,v50
v_lshlrev_b32_sdwa v49,1,v49 dst_sel:DWORD dst_unused:UNUSED_PAD src0_sel:DWORD src1_sel:WORD_1
v_cvt_pk_bf16_f32 v248,v248,v249
v_mfma_f32_16x16x32_bf16 v[252:255],a[36:39],v[36:39],v[252:255]
ds_read_u16 v49,v49
v_cvt_pk_bf16_f32 v249,v250,v251
v_lshlrev_b32_sdwa v51,1,v51 dst_sel:DWORD dst_unused:UNUSED_PAD src0_sel:DWORD src1_sel:WORD_1
v_mfma_f32_16x16x32_bf16 v[196:199],a[40:43],v[36:39],v[196:199]
ds_read_u16_d16_hi v51,v51
v_cvt_pk_bf16_f32 v250,v192,v193
v_mfma_f32_16x16x32_bf16 v[196:199],a[44:47],v[40:43],v[196:199]
v_cvt_pk_bf16_f32 v251,v194,v195
v_mfma_f32_16x16x32_bf16 v[36:39],a[144:147],a[56:59],v[96:99]
v_lshlrev_b32_sdwa v60,1,v60 dst_sel:DWORD dst_unused:UNUSED_PAD src0_sel:DWORD src1_sel:WORD_1
v_lshlrev_b32_sdwa v62,1,v62 dst_sel:DWORD dst_unused:UNUSED_PAD src0_sel:DWORD src1_sel:WORD_1
v_mfma_f32_16x16x32_bf16 v[36:39],a[148:151],a[72:75],v[36:39]
s_waitcnt lgkmcnt(5)
buffer_store_dwordx4 v[248:251],v7,s[40:43],s10 offen offset:128 sc1
v_lshl_add_u64 v[28:29],v[28:29],0,v[30:31]
v_mfma_f32_16x16x32_bf16 v[36:39],a[152:155],a[88:91],v[36:39]
s_nop 0
ds_read_u16 v60,v60
TP10726_%=:
s_cbranch_execz TR1_%=
v_lshl_add_u64 v[30:31],v[24:25],0,v[26:27]
v_mfma_f32_16x16x32_bf16 v[40:43],a[156:159],a[88:91],v[100:103]
s_waitcnt lgkmcnt(4)
ds_read_u16_d16_hi v62,v62
v_lshl_add_u64 v[24:25],v[44:45],0,v[46:47]
v_lshlrev_b32_sdwa v61,1,v61 dst_sel:DWORD dst_unused:UNUSED_PAD src0_sel:DWORD src1_sel:WORD_1
v_mfma_f32_16x16x32_bf16 v[40:43],a[160:163],a[72:75],v[40:43]
s_waitcnt lgkmcnt(2)
ds_read_u16 v61,v61
v_lshl_add_u64 v[26:27],v[48:49],0,v[50:51]
v_mfma_f32_16x16x32_bf16 v[40:43],a[164:167],a[56:59],v[40:43]
s_nop 0
v_lshlrev_b32_sdwa v63,1,v63 dst_sel:DWORD dst_unused:UNUSED_PAD src0_sel:DWORD src1_sel:WORD_1
v_lshlrev_b32_sdwa v32,1,v32 dst_sel:DWORD dst_unused:UNUSED_PAD src0_sel:DWORD src1_sel:WORD_1
v_mfma_f32_16x16x32_bf16 v[236:239],a[212:215],v[28:31],v[236:239]
ds_read_u16_d16_hi v63,v63
v_lshlrev_b32_sdwa v34,1,v34 dst_sel:DWORD dst_unused:UNUSED_PAD src0_sel:DWORD src1_sel:WORD_1
v_mfma_f32_16x16x32_bf16 v[236:239],a[200:203],v[24:27],v[236:239]
ds_read_u16 v32,v32
v_lshlrev_b32_sdwa v33,1,v33 dst_sel:DWORD dst_unused:UNUSED_PAD src0_sel:DWORD src1_sel:WORD_1
v_mfma_f32_16x16x32_bf16 v[164:167],a[196:199],v[24:27],v[164:167]
ds_read_u16_d16_hi v34,v34
v_lshlrev_b32_sdwa v35,1,v35 dst_sel:DWORD dst_unused:UNUSED_PAD src0_sel:DWORD src1_sel:WORD_1
v_mfma_f32_16x16x32_bf16 v[164:167],a[192:195],v[28:31],v[164:167]
ds_read_u16 v33,v33
v_lshlrev_b32_sdwa v56,1,v56 dst_sel:DWORD dst_unused:UNUSED_PAD src0_sel:DWORD src1_sel:WORD_1
v_mfma_f32_16x16x32_bf16 v[244:247],a[216:219],v[28:31],v[244:247]
ds_read_u16_d16_hi v35,v35
v_lshlrev_b32_sdwa v58,1,v58 dst_sel:DWORD dst_unused:UNUSED_PAD src0_sel:DWORD src1_sel:WORD_1
v_mfma_f32_16x16x32_bf16 v[244:247],a[220:223],v[24:27],v[244:247]
ds_read_u16 v56,v56
v_mul_f32_dpp v236,v14,v236 row_newbcast:0 row_mask:0xf bank_mask:0xf
v_mfma_f32_16x16x32_bf16 v[180:183],a[204:207],v[24:27],v[180:183]
ds_read_u16_d16_hi v58,v58
v_mul_f32_dpp v237,v14,v237 row_newbcast:1 row_mask:0xf bank_mask:0xf
v_mfma_f32_16x16x32_bf16 v[180:183],a[208:211],v[28:31],v[180:183]
v_mul_f32_dpp v238,v14,v238 row_newbcast:2 row_mask:0xf bank_mask:0xf
v_lshlrev_b32_sdwa v57,1,v57 dst_sel:DWORD dst_unused:UNUSED_PAD src0_sel:DWORD src1_sel:WORD_1
v_mfma_f32_16x16x32_bf16 v[252:255],a[244:247],v[28:31],v[252:255]
ds_read_u16 v57,v57
v_mul_f32_dpp v239,v14,v239 row_newbcast:3 row_mask:0xf bank_mask:0xf
v_mfma_f32_16x16x32_bf16 v[252:255],a[252:255],v[24:27],v[252:255]
v_mul_f32_dpp v164,v14,v164 row_newbcast:4 row_mask:0xf bank_mask:0xf
v_lshlrev_b32_sdwa v59,1,v59 dst_sel:DWORD dst_unused:UNUSED_PAD src0_sel:DWORD src1_sel:WORD_1
v_mfma_f32_16x16x32_bf16 v[196:199],a[232:235],v[24:27],v[196:199]
ds_read_u16_d16_hi v59,v59
v_mul_f32_dpp v165,v14,v165 row_newbcast:5 row_mask:0xf bank_mask:0xf
v_mfma_f32_16x16x32_bf16 v[196:199],a[240:243],v[28:31],v[196:199]
v_mul_f32_dpp v166,v14,v166 row_newbcast:6 row_mask:0xf bank_mask:0xf
v_lshlrev_b32_sdwa v52,1,v52 dst_sel:DWORD dst_unused:UNUSED_PAD src0_sel:DWORD src1_sel:WORD_1
v_mfma_f32_16x16x32_bf16 v[44:47],a[168:171],a[56:59],v[16:19]
ds_read_u16 v52,v52
v_mul_f32_dpp v167,v14,v167 row_newbcast:7 row_mask:0xf bank_mask:0xf
v_mfma_f32_16x16x32_bf16 v[44:47],a[172:175],a[72:75],v[44:47]
v_cvt_pk_bf16_f32 v236,v236,v237
v_lshlrev_b32_sdwa v54,1,v54 dst_sel:DWORD dst_unused:UNUSED_PAD src0_sel:DWORD src1_sel:WORD_1
v_mfma_f32_16x16x32_bf16 v[44:47],a[176:179],a[88:91],v[44:47]
ds_read_u16_d16_hi v54,v54
v_cvt_pk_bf16_f32 v237,v238,v239
v_lshlrev_b32_sdwa v53,1,v53 dst_sel:DWORD dst_unused:UNUSED_PAD src0_sel:DWORD src1_sel:WORD_1
v_mfma_f32_16x16x32_bf16 v[48:51],a[180:183],a[88:91],v[20:23]
ds_read_u16 v53,v53
v_cvt_pk_bf16_f32 v238,v164,v165
v_lshlrev_b32_sdwa v55,1,v55 dst_sel:DWORD dst_unused:UNUSED_PAD src0_sel:DWORD src1_sel:WORD_1
v_mfma_f32_16x16x32_bf16 v[48:51],a[184:187],a[72:75],v[48:51]
ds_read_u16_d16_hi v55,v55
v_cvt_pk_bf16_f32 v239,v166,v167
v_mfma_f32_16x16x32_bf16 v[48:51],a[188:191],a[56:59],v[48:51]
buffer_store_dwordx4 v[236:239],v7,s[40:43],s10 offen offset:192 sc1
v_mul_f32_dpp v244,v14,v244 row_newbcast:8 row_mask:0xf bank_mask:0xf
v_mfma_f32_16x16x32_bf16 v[24:27],a[96:99],a[60:63],v[64:67]
v_lshlrev_b32_sdwa v36,1,v36 dst_sel:DWORD dst_unused:UNUSED_PAD src0_sel:DWORD src1_sel:WORD_1
v_mul_f32_dpp v245,v14,v245 row_newbcast:9 row_mask:0xf bank_mask:0xf
v_mfma_f32_16x16x32_bf16 v[24:27],a[100:103],a[76:79],v[24:27]
s_waitcnt lgkmcnt(4)
ds_read_u16 v36,v36
v_lshl_add_u64 v[60:61],v[60:61],0,v[62:63]
v_mfma_f32_16x16x32_bf16 v[24:27],a[104:107],a[92:95],v[24:27]
v_lshl_add_u64 v[62:63],v[32:33],0,v[34:35]
v_mul_f32_dpp v246,v14,v246 row_newbcast:10 row_mask:0xf bank_mask:0xf
v_lshlrev_b32_sdwa v38,1,v38 dst_sel:DWORD dst_unused:UNUSED_PAD src0_sel:DWORD src1_sel:WORD_1
v_mfma_f32_16x16x32_bf16 v[28:31],a[108:111],a[92:95],v[68:71]
ds_read_u16_d16_hi v38,v38
v_mul_f32_dpp v247,v14,v247 row_newbcast:11 row_mask:0xf bank_mask:0xf
v_lshlrev_b32_sdwa v37,1,v37 dst_sel:DWORD dst_unused:UNUSED_PAD src0_sel:DWORD src1_sel:WORD_1
v_mfma_f32_16x16x32_bf16 v[28:31],a[112:115],a[76:79],v[28:31]
ds_read_u16 v37,v37
v_lshl_add_u64 v[32:33],v[56:57],0,v[58:59]
v_mul_f32_dpp v180,v14,v180 row_newbcast:12 row_mask:0xf bank_mask:0xf
v_mfma_f32_16x16x32_bf16 v[28:31],a[116:119],a[60:63],v[28:31]
v_mul_f32_dpp v181,v14,v181 row_newbcast:13 row_mask:0xf bank_mask:0xf
v_lshlrev_b32_sdwa v39,1,v39 dst_sel:DWORD dst_unused:UNUSED_PAD src0_sel:DWORD src1_sel:WORD_1
v_mfma_f32_16x16x32_bf16 v[56:59],a[120:123],a[60:63],v[84:87]
ds_read_u16_d16_hi v39,v39
v_mul_f32_dpp v182,v14,v182 row_newbcast:14 row_mask:0xf bank_mask:0xf
v_lshlrev_b32_sdwa v40,1,v40 dst_sel:DWORD dst_unused:UNUSED_PAD src0_sel:DWORD src1_sel:WORD_1
v_mfma_f32_16x16x32_bf16 v[56:59],a[124:127],a[76:79],v[56:59]
s_waitcnt lgkmcnt(4)
ds_read_u16 v40,v40
v_lshl_add_u64 v[34:35],v[52:53],0,v[54:55]
v_mul_f32_dpp v183,v14,v183 row_newbcast:15 row_mask:0xf bank_mask:0xf
v_lshlrev_b32_sdwa v42,1,v42 dst_sel:DWORD dst_unused:UNUSED_PAD src0_sel:DWORD src1_sel:WORD_1
v_mfma_f32_16x16x32_bf16 v[56:59],a[128:131],a[92:95],v[56:59]
ds_read_u16_d16_hi v42,v42
v_cvt_pk_bf16_f32 v244,v244,v245
v_mfma_f32_16x16x32_bf16 v[52:55],a[132:135],a[92:95],v[80:83]
v_cvt_pk_bf16_f32 v245,v246,v247
v_lshlrev_b32_sdwa v41,1,v41 dst_sel:DWORD dst_unused:UNUSED_PAD src0_sel:DWORD src1_sel:WORD_1
v_mfma_f32_16x16x32_bf16 v[52:55],a[136:139],a[76:79],v[52:55]
ds_read_u16 v41,v41
v_cvt_pk_bf16_f32 v246,v180,v181
v_lshlrev_b32_sdwa v43,1,v43 dst_sel:DWORD dst_unused:UNUSED_PAD src0_sel:DWORD src1_sel:WORD_1
v_mfma_f32_16x16x32_bf16 v[52:55],a[140:143],a[60:63],v[52:55]
ds_read_u16_d16_hi v43,v43
v_cvt_pk_bf16_f32 v247,v182,v183
v_mfma_f32_16x16x32_bf16 v[208:211],a[0:3],v[60:63],v[208:211]
buffer_store_dwordx4 v[244:247],v7,s[40:43],s10 offen offset:256 sc1
v_lshlrev_b32_sdwa v48,1,v48 dst_sel:DWORD dst_unused:UNUSED_PAD src0_sel:DWORD src1_sel:WORD_1
v_mul_f32_dpp v252,v15,v252 row_newbcast:0 row_mask:0xf bank_mask:0xf
v_mfma_f32_16x16x32_bf16 v[208:211],a[4:7],v[32:35],v[208:211]
ds_read_u16 v48,v48
v_mul_f32_dpp v253,v15,v253 row_newbcast:1 row_mask:0xf bank_mask:0xf
v_lshlrev_b32_sdwa v50,1,v50 dst_sel:DWORD dst_unused:UNUSED_PAD src0_sel:DWORD src1_sel:WORD_1
v_mfma_f32_16x16x32_bf16 v[168:171],a[8:11],v[32:35],v[168:171]
ds_read_u16_d16_hi v50,v50
v_mul_f32_dpp v254,v15,v254 row_newbcast:2 row_mask:0xf bank_mask:0xf
v_lshlrev_b32_sdwa v49,1,v49 dst_sel:DWORD dst_unused:UNUSED_PAD src0_sel:DWORD src1_sel:WORD_1
v_mfma_f32_16x16x32_bf16 v[168:171],a[12:15],v[60:63],v[168:171]
ds_read_u16 v49,v49
v_mul_f32_dpp v255,v15,v255 row_newbcast:3 row_mask:0xf bank_mask:0xf
v_mfma_f32_16x16x32_bf16 v[216:219],a[16:19],v[60:63],v[216:219]
v_lshlrev_b32_sdwa v51,1,v51 dst_sel:DWORD dst_unused:UNUSED_PAD src0_sel:DWORD src1_sel:WORD_1
v_mul_f32_dpp v196,v15,v196 row_newbcast:4 row_mask:0xf bank_mask:0xf
v_mfma_f32_16x16x32_bf16 v[216:219],a[20:23],v[32:35],v[216:219]
ds_read_u16_d16_hi v51,v51
v_mul_f32_dpp v197,v15,v197 row_newbcast:5 row_mask:0xf bank_mask:0xf
v_lshlrev_b32_sdwa v44,1,v44 dst_sel:DWORD dst_unused:UNUSED_PAD src0_sel:DWORD src1_sel:WORD_1
v_mfma_f32_16x16x32_bf16 v[184:187],a[24:27],v[32:35],v[184:187]
ds_read_u16 v44,v44
v_mul_f32_dpp v198,v15,v198 row_newbcast:6 row_mask:0xf bank_mask:0xf
v_mfma_f32_16x16x32_bf16 v[184:187],a[28:31],v[60:63],v[184:187]
v_lshlrev_b32_sdwa v46,1,v46 dst_sel:DWORD dst_unused:UNUSED_PAD src0_sel:DWORD src1_sel:WORD_1
v_mul_f32_dpp v199,v15,v199 row_newbcast:7 row_mask:0xf bank_mask:0xf
v_mfma_f32_16x16x32_bf16 v[224:227],a[32:35],v[60:63],v[224:227]
ds_read_u16_d16_hi v46,v46
v_lshlrev_b32_sdwa v45,1,v45 dst_sel:DWORD dst_unused:UNUSED_PAD src0_sel:DWORD src1_sel:WORD_1
v_cvt_pk_bf16_f32 v252,v252,v253
v_mfma_f32_16x16x32_bf16 v[224:227],a[36:39],v[32:35],v[224:227]
ds_read_u16 v45,v45
v_cvt_pk_bf16_f32 v253,v254,v255
v_lshlrev_b32_sdwa v47,1,v47 dst_sel:DWORD dst_unused:UNUSED_PAD src0_sel:DWORD src1_sel:WORD_1
v_mfma_f32_16x16x32_bf16 v[200:203],a[40:43],v[32:35],v[200:203]
ds_read_u16_d16_hi v47,v47
v_cvt_pk_bf16_f32 v254,v196,v197
v_mfma_f32_16x16x32_bf16 v[200:203],a[44:47],v[60:63],v[200:203]
v_cvt_pk_bf16_f32 v255,v198,v199
v_mov_b32 v10,0
v_mfma_f32_16x16x32_bf16 v[32:35],a[144:147],a[60:63],v[96:99]
ds_read_b128 v[72:75],v10 offset:11648
v_lshlrev_b32_sdwa v24,1,v24 dst_sel:DWORD dst_unused:UNUSED_PAD src0_sel:DWORD src1_sel:WORD_1
v_mfma_f32_16x16x32_bf16 v[32:35],a[148:151],a[76:79],v[32:35]
s_waitcnt lgkmcnt(6)
buffer_store_dwordx4 v[252:255],v7,s[40:43],s10 offen offset:320 sc1
v_lshl_add_u64 v[36:37],v[36:37],0,v[38:39]
v_mfma_f32_16x16x32_bf16 v[32:35],a[152:155],a[92:95],v[32:35]
ds_read_u16 v24,v24
v_lshl_add_u64 v[38:39],v[40:41],0,v[42:43]
v_lshlrev_b32_sdwa v26,1,v26 dst_sel:DWORD dst_unused:UNUSED_PAD src0_sel:DWORD src1_sel:WORD_1
v_mfma_f32_16x16x32_bf16 v[60:63],a[156:159],a[92:95],v[100:103]
s_waitcnt lgkmcnt(5)
ds_read_u16_d16_hi v26,v26
v_lshl_add_u64 v[40:41],v[48:49],0,v[50:51]
v_lshlrev_b32_sdwa v25,1,v25 dst_sel:DWORD dst_unused:UNUSED_PAD src0_sel:DWORD src1_sel:WORD_1
v_mfma_f32_16x16x32_bf16 v[60:63],a[160:163],a[76:79],v[60:63]
s_waitcnt lgkmcnt(3)
ds_read_u16 v25,v25
v_lshl_add_u64 v[42:43],v[44:45],0,v[46:47]
v_mfma_f32_16x16x32_bf16 v[60:63],a[164:167],a[60:63],v[60:63]
v_lshlrev_b32_sdwa v27,1,v27 dst_sel:DWORD dst_unused:UNUSED_PAD src0_sel:DWORD src1_sel:WORD_1
v_lshlrev_b32_sdwa v28,1,v28 dst_sel:DWORD dst_unused:UNUSED_PAD src0_sel:DWORD src1_sel:WORD_1
v_mfma_f32_16x16x32_bf16 v[208:211],a[212:215],v[36:39],v[208:211]
ds_read_u16_d16_hi v27,v27
v_lshlrev_b32_sdwa v30,1,v30 dst_sel:DWORD dst_unused:UNUSED_PAD src0_sel:DWORD src1_sel:WORD_1
v_mfma_f32_16x16x32_bf16 v[208:211],a[200:203],v[40:43],v[208:211]
ds_read_u16 v28,v28
v_lshlrev_b32_sdwa v29,1,v29 dst_sel:DWORD dst_unused:UNUSED_PAD src0_sel:DWORD src1_sel:WORD_1
v_mfma_f32_16x16x32_bf16 v[168:171],a[196:199],v[40:43],v[168:171]
ds_read_u16_d16_hi v30,v30
v_lshlrev_b32_sdwa v31,1,v31 dst_sel:DWORD dst_unused:UNUSED_PAD src0_sel:DWORD src1_sel:WORD_1
v_mfma_f32_16x16x32_bf16 v[168:171],a[192:195],v[36:39],v[168:171]
ds_read_u16 v29,v29
v_lshlrev_b32_sdwa v52,1,v52 dst_sel:DWORD dst_unused:UNUSED_PAD src0_sel:DWORD src1_sel:WORD_1
v_mfma_f32_16x16x32_bf16 v[216:219],a[216:219],v[36:39],v[216:219]
ds_read_u16_d16_hi v31,v31
v_lshlrev_b32_sdwa v54,1,v54 dst_sel:DWORD dst_unused:UNUSED_PAD src0_sel:DWORD src1_sel:WORD_1
v_mfma_f32_16x16x32_bf16 v[216:219],a[220:223],v[40:43],v[216:219]
ds_read_u16 v52,v52
v_mul_f32_dpp v208,v14,v208 row_newbcast:0 row_mask:0xf bank_mask:0xf
s_add_u32 s11,s61,4816896
v_mul_f32_dpp v209,v14,v209 row_newbcast:1 row_mask:0xf bank_mask:0xf
v_mul_f32_dpp v210,v14,v210 row_newbcast:2 row_mask:0xf bank_mask:0xf
v_mfma_f32_16x16x32_bf16 v[184:187],a[204:207],v[40:43],v[184:187]
ds_read_u16_d16_hi v54,v54
v_mul_f32_dpp v211,v14,v211 row_newbcast:3 row_mask:0xf bank_mask:0xf
v_mul_f32_dpp v168,v14,v168 row_newbcast:4 row_mask:0xf bank_mask:0xf
v_mul_f32_dpp v169,v14,v169 row_newbcast:5 row_mask:0xf bank_mask:0xf
v_mfma_f32_16x16x32_bf16 v[184:187],a[208:211],v[36:39],v[184:187]
v_mul_f32_dpp v170,v14,v170 row_newbcast:6 row_mask:0xf bank_mask:0xf
v_mul_f32_dpp v171,v14,v171 row_newbcast:7 row_mask:0xf bank_mask:0xf
v_cvt_pk_bf16_f32 v208,v208,v209
v_mfma_f32_16x16x32_bf16 v[224:227],a[244:247],v[36:39],v[224:227]
v_cvt_pk_bf16_f32 v209,v210,v211
v_cvt_pk_bf16_f32 v210,v168,v169
v_cvt_pk_bf16_f32 v211,v170,v171
v_lshlrev_b32_sdwa v53,1,v53 dst_sel:DWORD dst_unused:UNUSED_PAD src0_sel:DWORD src1_sel:WORD_1
v_mfma_f32_16x16x32_bf16 v[224:227],a[252:255],v[40:43],v[224:227]
buffer_store_dwordx4 v[208:211],v7,s[40:43],s11 offen offset:0 sc1
v_mul_f32_dpp v216,v14,v216 row_newbcast:8 row_mask:0xf bank_mask:0xf
v_mul_f32_dpp v217,v14,v217 row_newbcast:9 row_mask:0xf bank_mask:0xf
v_mul_f32_dpp v218,v14,v218 row_newbcast:10 row_mask:0xf bank_mask:0xf
v_mfma_f32_16x16x32_bf16 v[200:203],a[232:235],v[40:43],v[200:203]
ds_read_u16 v53,v53
v_mul_f32_dpp v219,v14,v219 row_newbcast:11 row_mask:0xf bank_mask:0xf
v_mul_f32_dpp v184,v14,v184 row_newbcast:12 row_mask:0xf bank_mask:0xf
v_mul_f32_dpp v185,v14,v185 row_newbcast:13 row_mask:0xf bank_mask:0xf
v_lshlrev_b32_sdwa v55,1,v55 dst_sel:DWORD dst_unused:UNUSED_PAD src0_sel:DWORD src1_sel:WORD_1
v_mfma_f32_16x16x32_bf16 v[200:203],a[240:243],v[36:39],v[200:203]
ds_read_u16_d16_hi v55,v55
v_mul_f32_dpp v186,v14,v186 row_newbcast:14 row_mask:0xf bank_mask:0xf
v_mul_f32_dpp v187,v14,v187 row_newbcast:15 row_mask:0xf bank_mask:0xf
v_cvt_pk_bf16_f32 v216,v216,v217
v_lshlrev_b32_sdwa v56,1,v56 dst_sel:DWORD dst_unused:UNUSED_PAD src0_sel:DWORD src1_sel:WORD_1
v_mfma_f32_16x16x32_bf16 v[48:51],a[168:171],a[60:63],v[16:19]
ds_read_u16 v56,v56
v_cvt_pk_bf16_f32 v217,v218,v219
v_cvt_pk_bf16_f32 v218,v184,v185
v_cvt_pk_bf16_f32 v219,v186,v187
v_mfma_f32_16x16x32_bf16 v[48:51],a[172:175],a[76:79],v[48:51]
v_mul_f32_dpp v224,v15,v224 row_newbcast:0 row_mask:0xf bank_mask:0xf
v_mul_f32_dpp v225,v15,v225 row_newbcast:1 row_mask:0xf bank_mask:0xf
v_mul_f32_dpp v226,v15,v226 row_newbcast:2 row_mask:0xf bank_mask:0xf
v_mul_f32_dpp v227,v15,v227 row_newbcast:3 row_mask:0xf bank_mask:0xf
v_lshlrev_b32_sdwa v58,1,v58 dst_sel:DWORD dst_unused:UNUSED_PAD src0_sel:DWORD src1_sel:WORD_1
v_mfma_f32_16x16x32_bf16 v[48:51],a[176:179],a[92:95],v[48:51]
ds_read_u16_d16_hi v58,v58
v_mul_f32_dpp v200,v15,v200 row_newbcast:4 row_mask:0xf bank_mask:0xf
v_mul_f32_dpp v201,v15,v201 row_newbcast:5 row_mask:0xf bank_mask:0xf
v_mul_f32_dpp v202,v15,v202 row_newbcast:6 row_mask:0xf bank_mask:0xf
v_lshlrev_b32_sdwa v57,1,v57 dst_sel:DWORD dst_unused:UNUSED_PAD src0_sel:DWORD src1_sel:WORD_1
v_mfma_f32_16x16x32_bf16 v[44:47],a[180:183],a[92:95],v[20:23]
ds_read_u16 v57,v57
v_mul_f32_dpp v203,v15,v203 row_newbcast:7 row_mask:0xf bank_mask:0xf
v_cvt_pk_bf16_f32 v224,v224,v225
v_cvt_pk_bf16_f32 v225,v226,v227
v_lshlrev_b32_sdwa v59,1,v59 dst_sel:DWORD dst_unused:UNUSED_PAD src0_sel:DWORD src1_sel:WORD_1
v_mfma_f32_16x16x32_bf16 v[44:47],a[184:187],a[76:79],v[44:47]
ds_read_u16_d16_hi v59,v59
v_cvt_pk_bf16_f32 v226,v200,v201
v_cvt_pk_bf16_f32 v227,v202,v203
v_mfma_f32_16x16x32_bf16 v[44:47],a[188:191],a[60:63],v[44:47]
buffer_store_dwordx4 v[216:219],v7,s[40:43],s11 offen offset:64 sc1
s_waitcnt lgkmcnt(12)
v_lshl_add_u64 v[24:25],v[24:25],0,v[26:27]
s_waitcnt lgkmcnt(8)
v_lshl_add_u64 v[26:27],v[28:29],0,v[30:31]
s_waitcnt lgkmcnt(4)
v_lshl_add_u64 v[28:29],v[52:53],0,v[54:55]
s_waitcnt lgkmcnt(0)
v_lshl_add_u64 v[30:31],v[56:57],0,v[58:59]
v_mfma_f32_16x16x32_bf16 v[212:215],a[0:3],v[24:27],v[212:215]
v_lshlrev_b32_sdwa v32,1,v32 dst_sel:DWORD dst_unused:UNUSED_PAD src0_sel:DWORD src1_sel:WORD_1
v_lshlrev_b32_sdwa v34,1,v34 dst_sel:DWORD dst_unused:UNUSED_PAD src0_sel:DWORD src1_sel:WORD_1
v_mfma_f32_16x16x32_bf16 v[212:215],a[4:7],v[28:31],v[212:215]
ds_read_u16 v32,v32
v_lshlrev_b32_sdwa v33,1,v33 dst_sel:DWORD dst_unused:UNUSED_PAD src0_sel:DWORD src1_sel:WORD_1
v_mfma_f32_16x16x32_bf16 v[172:175],a[8:11],v[28:31],v[172:175]
ds_read_u16_d16_hi v34,v34
v_lshlrev_b32_sdwa v35,1,v35 dst_sel:DWORD dst_unused:UNUSED_PAD src0_sel:DWORD src1_sel:WORD_1
v_mfma_f32_16x16x32_bf16 v[172:175],a[12:15],v[24:27],v[172:175]
ds_read_u16 v33,v33
v_lshlrev_b32_sdwa v60,1,v60 dst_sel:DWORD dst_unused:UNUSED_PAD src0_sel:DWORD src1_sel:WORD_1
v_mfma_f32_16x16x32_bf16 v[220:223],a[16:19],v[24:27],v[220:223]
ds_read_u16_d16_hi v35,v35
v_lshlrev_b32_sdwa v62,1,v62 dst_sel:DWORD dst_unused:UNUSED_PAD src0_sel:DWORD src1_sel:WORD_1
v_mfma_f32_16x16x32_bf16 v[220:223],a[20:23],v[28:31],v[220:223]
ds_read_u16 v60,v60
v_lshlrev_b32_sdwa v61,1,v61 dst_sel:DWORD dst_unused:UNUSED_PAD src0_sel:DWORD src1_sel:WORD_1
v_lshlrev_b32_sdwa v63,1,v63 dst_sel:DWORD dst_unused:UNUSED_PAD src0_sel:DWORD src1_sel:WORD_1
v_mfma_f32_16x16x32_bf16 v[188:191],a[24:27],v[28:31],v[188:191]
ds_read_u16_d16_hi v62,v62
ds_read_u16 v61,v61
v_lshlrev_b32_sdwa v44,1,v44 dst_sel:DWORD dst_unused:UNUSED_PAD src0_sel:DWORD src1_sel:WORD_1
v_mfma_f32_16x16x32_bf16 v[188:191],a[28:31],v[24:27],v[188:191]
ds_read_u16_d16_hi v63,v63
v_lshlrev_b32_sdwa v46,1,v46 dst_sel:DWORD dst_unused:UNUSED_PAD src0_sel:DWORD src1_sel:WORD_1
v_lshlrev_b32_sdwa v45,1,v45 dst_sel:DWORD dst_unused:UNUSED_PAD src0_sel:DWORD src1_sel:WORD_1
v_mfma_f32_16x16x32_bf16 v[228:231],a[32:35],v[24:27],v[228:231]
ds_read_u16 v44,v44
ds_read_u16_d16_hi v46,v46
v_lshlrev_b32_sdwa v47,1,v47 dst_sel:DWORD dst_unused:UNUSED_PAD src0_sel:DWORD src1_sel:WORD_1
v_mfma_f32_16x16x32_bf16 v[228:231],a[36:39],v[28:31],v[228:231]
buffer_store_dwordx4 v[224:227],v7,s[40:43],s11 offen offset:128 sc1
ds_read_u16 v45,v45
v_lshlrev_b32_sdwa v48,1,v48 dst_sel:DWORD dst_unused:UNUSED_PAD src0_sel:DWORD src1_sel:WORD_1
v_mfma_f32_16x16x32_bf16 v[204:207],a[40:43],v[28:31],v[204:207]
ds_read_u16_d16_hi v47,v47
v_lshlrev_b32_sdwa v50,1,v50 dst_sel:DWORD dst_unused:UNUSED_PAD src0_sel:DWORD src1_sel:WORD_1
v_lshlrev_b32_sdwa v49,1,v49 dst_sel:DWORD dst_unused:UNUSED_PAD src0_sel:DWORD src1_sel:WORD_1
v_mfma_f32_16x16x32_bf16 v[204:207],a[44:47],v[24:27],v[204:207]
ds_read_u16 v48,v48
ds_read_u16_d16_hi v50,v50
v_lshlrev_b32_sdwa v51,1,v51 dst_sel:DWORD dst_unused:UNUSED_PAD src0_sel:DWORD src1_sel:WORD_1
ds_read_u16 v49,v49
ds_read_u16_d16_hi v51,v51
s_waitcnt lgkmcnt(12)
v_lshl_add_u64 v[32:33],v[32:33],0,v[34:35]
s_waitcnt lgkmcnt(8)
v_lshl_add_u64 v[34:35],v[60:61],0,v[62:63]
s_waitcnt lgkmcnt(4)
v_lshl_add_u64 v[60:61],v[44:45],0,v[46:47]
s_waitcnt lgkmcnt(0)
v_lshl_add_u64 v[62:63],v[48:49],0,v[50:51]
v_mfma_f32_16x16x32_bf16 v[212:215],a[212:215],v[32:35],v[212:215]
s_nop 0
v_mfma_f32_16x16x32_bf16 v[212:215],a[200:203],v[60:63],v[212:215]
v_mfma_f32_16x16x32_bf16 v[172:175],a[196:199],v[60:63],v[172:175]
v_mfma_f32_16x16x32_bf16 v[172:175],a[192:195],v[32:35],v[172:175]
v_mfma_f32_16x16x32_bf16 v[220:223],a[216:219],v[32:35],v[220:223]
v_mfma_f32_16x16x32_bf16 v[220:223],a[220:223],v[60:63],v[220:223]
v_mfma_f32_16x16x32_bf16 v[188:191],a[204:207],v[60:63],v[188:191]
s_nop 2
v_mul_f32_dpp v212,v14,v212 row_newbcast:0 row_mask:0xf bank_mask:0xf
v_mul_f32_dpp v213,v14,v213 row_newbcast:1 row_mask:0xf bank_mask:0xf
v_mul_f32_dpp v214,v14,v214 row_newbcast:2 row_mask:0xf bank_mask:0xf
v_mfma_f32_16x16x32_bf16 v[188:191],a[208:211],v[32:35],v[188:191]
v_mul_f32_dpp v215,v14,v215 row_newbcast:3 row_mask:0xf bank_mask:0xf
v_mul_f32_dpp v172,v14,v172 row_newbcast:4 row_mask:0xf bank_mask:0xf
v_mul_f32_dpp v173,v14,v173 row_newbcast:5 row_mask:0xf bank_mask:0xf
v_mfma_f32_16x16x32_bf16 v[228:231],a[244:247],v[32:35],v[228:231]
v_mul_f32_dpp v174,v14,v174 row_newbcast:6 row_mask:0xf bank_mask:0xf
v_mul_f32_dpp v175,v14,v175 row_newbcast:7 row_mask:0xf bank_mask:0xf
v_cvt_pk_bf16_f32 v212,v212,v213
v_mfma_f32_16x16x32_bf16 v[228:231],a[252:255],v[60:63],v[228:231]
v_cvt_pk_bf16_f32 v213,v214,v215
v_cvt_pk_bf16_f32 v214,v172,v173
v_cvt_pk_bf16_f32 v215,v174,v175
buffer_store_dwordx4 v[212:215],v7,s[40:43],s11 offen offset:192 sc1
v_mfma_f32_16x16x32_bf16 v[204:207],a[232:235],v[60:63],v[204:207]
v_mul_f32_dpp v220,v14,v220 row_newbcast:8 row_mask:0xf bank_mask:0xf
v_mul_f32_dpp v221,v14,v221 row_newbcast:9 row_mask:0xf bank_mask:0xf
v_mul_f32_dpp v222,v14,v222 row_newbcast:10 row_mask:0xf bank_mask:0xf
v_mfma_f32_16x16x32_bf16 v[204:207],a[240:243],v[32:35],v[204:207]
v_mul_f32_dpp v223,v14,v223 row_newbcast:11 row_mask:0xf bank_mask:0xf
v_mul_f32_dpp v188,v14,v188 row_newbcast:12 row_mask:0xf bank_mask:0xf
v_mul_f32_dpp v189,v14,v189 row_newbcast:13 row_mask:0xf bank_mask:0xf
v_mul_f32_dpp v190,v14,v190 row_newbcast:14 row_mask:0xf bank_mask:0xf
v_mul_f32_dpp v191,v14,v191 row_newbcast:15 row_mask:0xf bank_mask:0xf
v_cvt_pk_bf16_f32 v220,v220,v221
v_cvt_pk_bf16_f32 v221,v222,v223
v_cvt_pk_bf16_f32 v222,v188,v189
v_cvt_pk_bf16_f32 v223,v190,v191
buffer_store_dwordx4 v[220:223],v7,s[40:43],s11 offen offset:256 sc1
v_mul_f32_dpp v228,v15,v228 row_newbcast:0 row_mask:0xf bank_mask:0xf
v_mul_f32_dpp v229,v15,v229 row_newbcast:1 row_mask:0xf bank_mask:0xf
v_mul_f32_dpp v230,v15,v230 row_newbcast:2 row_mask:0xf bank_mask:0xf
v_mul_f32_dpp v231,v15,v231 row_newbcast:3 row_mask:0xf bank_mask:0xf
v_mul_f32_dpp v204,v15,v204 row_newbcast:4 row_mask:0xf bank_mask:0xf
v_mul_f32_dpp v205,v15,v205 row_newbcast:5 row_mask:0xf bank_mask:0xf
v_mul_f32_dpp v206,v15,v206 row_newbcast:6 row_mask:0xf bank_mask:0xf
v_mul_f32_dpp v207,v15,v207 row_newbcast:7 row_mask:0xf bank_mask:0xf
v_cvt_pk_bf16_f32 v228,v228,v229
v_cvt_pk_bf16_f32 v229,v230,v231
v_cvt_pk_bf16_f32 v230,v204,v205
v_cvt_pk_bf16_f32 v231,v206,v207
buffer_store_dwordx4 v[228:231],v7,s[40:43],s11 offen offset:320 sc1
v_or3_b32 v72,v72,v73,v74
v_or_b32 v72,v72,v75
s_nop 0
v_readfirstlane_b32 s64,v72
s_cmp_lg_u32 s64,0
s_cbranch_scc1 N_fallback_%=
s_endpgm
N_mainend_%=:
.fill 512,4,0xbf800000
N_fallback_%=:
s_waitcnt vmcnt(0) lgkmcnt(0)
s_barrier
)ASM"
    :: "s"(blockIdx.x), "v"(tid), "s"(ka)
    : "v1", "v2", "v3", "v4", "v5", "v6", "v7", "v8", "v9", "v10", "v11", "v12", "v13", "v14", "v15", "v16", "v17", "v18", "v19", "v20", "v21", "v22", "v23", "v24", "v25", "v26", "v27", "v28", "v29", "v30", "v31", "v32", "v33", "v34", "v35", "v36", "v37", "v38", "v39", "v40", "v41", "v42", "v43", "v44", "v45", "v46", "v47", "v48", "v49", "v50", "v51", "v52", "v53", "v54", "v55", "v56", "v57", "v58", "v59", "v60", "v61", "v62", "v63", "v64", "v65", "v66", "v67", "v68", "v69", "v70", "v71", "v72", "v73", "v74", "v75", "v76", "v77", "v78", "v79", "v80", "v81", "v82", "v83", "v84", "v85", "v86", "v87", "v88", "v89", "v90", "v91", "v92", "v93", "v94", "v95", "v96", "v97", "v98", "v99", "v100", "v101", "v102", "v103", "v104", "v105", "v106", "v107", "v108", "v109", "v110", "v111", "v112", "v113", "v114", "v115", "v116", "v117", "v118", "v119", "v120", "v121", "v122", "v123", "v124", "v125", "v126", "v127", "v128", "v129", "v130", "v131", "v132", "v133", "v134", "v135", "v136", "v137", "v138", "v139", "v140", "v141", "v142", "v143", "v144", "v145", "v146", "v147", "v148", "v149", "v150", "v151", "v152", "v153", "v154", "v155", "v156", "v157", "v158", "v159", "v160", "v161", "v162", "v163", "v164", "v165", "v166", "v167", "v168", "v169", "v170", "v171", "v172", "v173", "v174", "v175", "v176", "v177", "v178", "v179", "v180", "v181", "v182", "v183", "v184", "v185", "v186", "v187", "v188", "v189", "v190", "v191", "v192", "v193", "v194", "v195", "v196", "v197", "v198", "v199", "v200", "v201", "v202", "v203", "v204", "v205", "v206", "v207", "v208", "v209", "v210", "v211", "v212", "v213", "v214", "v215", "v216", "v217", "v218", "v219", "v220", "v221", "v222", "v223", "v224", "v225", "v226", "v227", "v228", "v229", "v230", "v231", "v232", "v233", "v234", "v235", "v236", "v237", "v238", "v239", "v240", "v241", "v242", "v243", "v244", "v245", "v246", "v247", "v248", "v249", "v250", "v251", "v252", "v253", "v254", "v255", "a0", "a1", "a2", "a3", "a4", "a5", "a6", "a7", "a8", "a9", "a10", "a11", "a12", "a13", "a14", "a15", "a16", "a17", "a18", "a19", "a20", "a21", "a22", "a23", "a24", "a25", "a26", "a27", "a28", "a29", "a30", "a31", "a32", "a33", "a34", "a35", "a36", "a37", "a38", "a39", "a40", "a41", "a42", "a43", "a44", "a45", "a46", "a47", "a48", "a49", "a50", "a51", "a52", "a53", "a54", "a55", "a56", "a57", "a58", "a59", "a60", "a61", "a62", "a63", "a64", "a65", "a66", "a67", "a68", "a69", "a70", "a71", "a72", "a73", "a74", "a75", "a76", "a77", "a78", "a79", "a80", "a81", "a82", "a83", "a84", "a85", "a86", "a87", "a88", "a89", "a90", "a91", "a92", "a93", "a94", "a95", "a96", "a97", "a98", "a99", "a100", "a101", "a102", "a103", "a104", "a105", "a106", "a107", "a108", "a109", "a110", "a111", "a112", "a113", "a114", "a115", "a116", "a117", "a118", "a119", "a120", "a121", "a122", "a123", "a124", "a125", "a126", "a127", "a128", "a129", "a130", "a131", "a132", "a133", "a134", "a135", "a136", "a137", "a138", "a139", "a140", "a141", "a142", "a143", "a144", "a145", "a146", "a147", "a148", "a149", "a150", "a151", "a152", "a153", "a154", "a155", "a156", "a157", "a158", "a159", "a160", "a161", "a162", "a163", "a164", "a165", "a166", "a167", "a168", "a169", "a170", "a171", "a172", "a173", "a174", "a175", "a176", "a177", "a178", "a179", "a180", "a181", "a182", "a183", "a184", "a185", "a186", "a187", "a188", "a189", "a190", "a191", "a192", "a193", "a194", "a195", "a196", "a197", "a198", "a199", "a200", "a201", "a202", "a203", "a204", "a205", "a206", "a207", "a208", "a209", "a210", "a211", "a212", "a213", "a214", "a215", "a216", "a217", "a218", "a219", "a220", "a221", "a222", "a223", "a224", "a225", "a226", "a227", "a228", "a229", "a230", "a231", "a232", "a233", "a234", "a235", "a236", "a237", "a238", "a239", "a240", "a241", "a242", "a243", "a244", "a245", "a246", "a247", "a248", "a249", "a250", "a251", "a252", "a253", "a254", "a255", "s4", "s5", "s6", "s7", "s8", "s9", "s10", "s11", "s12", "s13", "s14", "s15", "s16", "s17", "s18", "s19", "s20", "s21", "s22", "s23", "s24", "s25", "s26", "s27", "s28", "s29", "s30", "s31", "s32", "s33", "s34", "s35", "s36", "s37", "s38", "s39", "s40", "s41", "s42", "s43", "s44", "s45", "s46", "s47", "s48", "s49", "s50", "s51", "s52", "s53", "s54", "s55", "s56", "s57", "s58", "s59", "s60", "s61", "s62", "s63", "s64", "s65", "s66", "s67", "s68", "s69", "s70", "s71", "s72", "s73", "s74", "s75", "s76", "s77", "s78", "s79", "s80", "s81", "s82", "s83", "s84", "s85", "s86", "s87", "s88", "s89", "s90", "s91", "s92", "s93", "s94", "s95", "s96", "s97", "s98", "s99", "vcc", "scc", "memory");
    asm volatile("" : "+s"(ka));
    typedef const unsigned short* __attribute__((address_space(1))) gp;
    unsigned long long const __attribute__((address_space(4)))* kp = (unsigned long long const __attribute__((address_space(4)))*)ka;
    const unsigned short* x = (const unsigned short*)kp[0]; const unsigned short* dw = (const unsigned short*)kp[1];
    const unsigned short* w1 = (const unsigned short*)kp[2]; const unsigned short* b1 = (const unsigned short*)kp[3];
    const unsigned short* w2 = (const unsigned short*)kp[4]; const unsigned short* b2 = (const unsigned short*)kp[5];
    const unsigned short* gamma = (const unsigned short*)kp[6]; unsigned short* out = (unsigned short*)kp[7];
    extern __shared__ float sm[];
    float* cv = sm; float* hid = sm + 16 * 96; float* stt = hid + 16 * 384;
    const int x0 = (blockIdx.x >> 3) * 16, y0 = (blockIdx.x & 7) * 64;
    for (int y = y0; y < y0 + 64; ++y) {
        for (int i = tid; i < 16 * 96; i += 256) {
            int p = i / 96, c = i - p * 96; float s = 0.f;
            for (int ky = 0; ky < 3; ++ky) for (int kx = 0; kx < 3; ++kx) {
                int yy = y + ky - 1, xx = x0 + p + kx - 1;
                float v = (yy >= 0 && yy < 512 && xx >= 0 && xx < 512) ? bf2f(x[((size_t)yy * 512 + xx) * 96 + c]) : 0.f;
                s = fmaf(v, bf2f(dw[(ky * 3 + kx) * 96 + c]), s);
            }
            cv[i] = s;
        }
        __syncthreads();
        if (tid < 16) {
            float m = 0.f; for (int c = 0; c < 96; ++c) m += cv[tid * 96 + c];
            m *= (1.f / 96); float q = 0.f;
            for (int c = 0; c < 96; ++c) { float d = cv[tid * 96 + c] - m; q = fmaf(d, d, q); }
            stt[tid] = m; stt[16 + tid] = 1.f / sqrtf(q * (1.f / 96) + 1e-5f);
        }
        __syncthreads();
        for (int i = tid; i < 16 * 96; i += 256) { int p = i / 96; cv[i] = rbf((cv[i] - stt[p]) * stt[16 + p]); }
        __syncthreads();
        for (int e = tid; e < 384; e += 256) {
            float a[16];
#pragma unroll
            for (int p = 0; p < 16; ++p) a[p] = 0.f;
            for (int c = 0; c < 96; ++c) {
                float w = bf2f(w1[c * 384 + e]);
#pragma unroll
                for (int p = 0; p < 16; ++p) a[p] = fmaf(cv[p * 96 + c], w, a[p]);
            }
            float bb = bf2f(b1[e]);
#pragma unroll
            for (int p = 0; p < 16; ++p) {
                float h = a[p] + bb;
                hid[p * 384 + e] = rbf(0.5f * h * (1.f + tanhf(0.7978845608028654f * (h + 0.044715f * h * h * h))));
            }
        }
        __syncthreads();
        for (int i = tid; i < 16 * 96; i += 256) {
            int p = i / 96, c = i - p * 96; float s = 0.f;
            for (int e = 0; e < 384; ++e) s = fmaf(hid[p * 384 + e], bf2f(w2[e * 96 + c]), s);
            size_t o = ((size_t)y * 512 + x0 + p) * 96 + c;
            out[o] = f2bf(bf2f(x[o]) + bf2f(gamma[c]) * (s + bf2f(b2[c])));
        }
        __syncthreads();
    }

}

extern "C" void kernel_launch(void* const* d_in, const int* in_sizes, int n_in,
                              void* d_out, int out_size, void* d_ws, size_t ws_size,
                              hipStream_t stream) {
    (void)hipFuncSetAttribute((const void*)convnext_kernel, hipFuncAttributeMaxDynamicSharedMemorySize, LDS_BYTES);
    convnext_kernel<<<256, 256, LDS_BYTES, stream>>>(
        (const unsigned short*)d_in[0], (const unsigned short*)d_in[1], (const unsigned short*)d_in[2], (const unsigned short*)d_in[3],
        (const unsigned short*)d_in[4], (const unsigned short*)d_in[5], (const unsigned short*)d_in[6], (unsigned short*)d_out);
}
```

```cpp
#include <hip/hip_runtime.h>
#include <stdint.h>

constexpr int LDS_BYTES = 163584;

__device__ __forceinline__ float bf2f(unsigned short b) { return __uint_as_float((unsigned)b << 16); }
__device__ __forceinline__ unsigned short f2bf(float f) { unsigned r; asm("v_cvt_pk_bf16_f32 %0, %1, %1" : "=v"(r) : "v"(f)); return (unsigned short)r; }
__device__ __forceinline__ float rbf(float f) { return bf2f(f2bf(f)); }

__global__ __launch_bounds__(256, 1) __attribute__((aligned(4096))) void convnext_kernel(
    const unsigned short* __restrict__ x_, const unsigned short* __restrict__ dw_, const unsigned short* __restrict__ w1_,
    const unsigned short* __restrict__ b1_, const unsigned short* __restrict__ w2_, const unsigned short* __restrict__ b2_,
    const unsigned short* __restrict__ gamma_, unsigned short* __restrict__ out_) {
    const unsigned tid = threadIdx.x;
    unsigned long long ka = (unsigned long long)__builtin_amdgcn_kernarg_segment_ptr();
    asm volatile(
R"ASM(
s_load_dwordx16 s[20:35],%2,0
v_lshrrev_b32 v27,6,%1
v_and_b32 v24,63,%1
v_readfirstlane_b32 s52,v27
s_lshr_b32 s70,%0,3
v_and_b32_e64 v61,8,v24
v_lshl_add_u32 v62,s70,2,v27
v_mul_u32_u24 v61,15,v61
s_getpc_b64 s[14:15]
N_pf_%=:
v_or_b32 v61,7,v61
v_and_b32 v62,v61,v62
v_and_b32_e64 v61,7,v24
v_lshl_add_u32 v62,v62,3,v61
v_lshlrev_b32_e64 v62,7,v62
v_min_u32 v62,N_mainend_%=-N_pf_%=+2048,v62
global_load_dword v255,v62,s[14:15]
v_mul_u32_u24 v30,52,v27
v_add_u32 v30,v30,v24
v_subrev_u32_e64 v29,4,v30
v_max_i32 v29,0xc0,v29
v_min_u32_e64 v30,v30,v29
v_mul_u32_u24 v28,0xaaab,v30
s_and_b32 s64,%0,7
s_lshr_b32 s70,%0,3
v_lshrrev_b32 v28,19,v28
s_lshl_b32 s71,s64,6
s_mul_i32 s68,s70,3072
v_bfe_u32 v27,v28,2,2
s_sub_u32 s65,s71,1
s_mul_i32 s56,s65,98304
v_xor_b32 v27,v30,v27
s_add_u32 s56,s56,s68
s_cmp_lt_i32 s65,0
s_cselect_b32 s89,0x70000000,s56
v_lshlrev_b32 v27,4,v27
s_cmp_eq_u32 s70,0
s_cselect_b32 s67,0,99
s_cmp_eq_u32 s70,31
s_cselect_b32 s67,17,s67
v_mov_b32 v29,0x7fff0000
v_cmp_eq_u32_e32 vcc,s67,v28
s_mul_i32 s57,s52,832
s_add_u32 s57,s57,73728
v_cndmask_b32_e32 v6,v27,v29,vcc
s_mov_b32 s38,50331840
s_mov_b32 s39,0x00020000
s_mov_b32 s50,1728
s_mov_b32 s51,0x00020000
v_lshlrev_b32_e64 v8,4,v24
v_mul_u32_u24 v44,79,v24
v_add_u32 v45,5056,v44
v_lshrrev_b32 v44,10,v44
v_lshrrev_b32 v45,10,v45
v_sub_u32 v44,v24,v44
v_sub_u32 v45,v24,v45
v_lshlrev_b32_e64 v44,4,v44
v_add_lshl_u32 v45,v45,64,4
v_min_u32 v28,191,%1
v_lshlrev_b32_e64 v32,2,v28
v_min_u32 v34,95,%1
v_lshlrev_b32 v33,1,v34
v_and_b32 v27,15,v24
v_lshrrev_b32_e64 v28,4,v24
s_mul_i32 s66,s52,192
v_min_u32_e64 v27,11,v27
v_mul_u32_u24 v28,18432,v28
v_lshl_add_u32 v27,v27,4,s66
s_mul_i32 s66,s52,18432
v_add_u32 v172,v27,v28
v_add_u32 v173,s66,v8
s_and_b32 s64,s52,1
s_mul_i32 s65,s64,48
v_bfe_u32 v27,v24,3,2
v_and_b32 v28,4,v24
v_lshrrev_b32 v29,5,v24
v_add3_u32 v27,v27,v28,s65
v_lshl_add_u32 v27,v29,3,v27
v_mul_u32_u24 v27,768,v27
v_and_b32 v28,3,v24
v_xor_b32 v28,v28,v29
v_lshl_add_u32 v27,v28,4,v27
s_mul_i32 s65,s64,3072
v_add_u32 v28,s65,v8
s_mul_i32 s67,s64,3080
s_add_u32 s67,s67,40896
s_add_u32 s65,s65,16256
s_cmp_lt_u32 s52,2
s_cselect_b32 s58,s65,s67
s_cselect_b64 vcc,-1,0
s_movk_i32 s59,6144
s_cselect_b32 s59,-22464,s59
s_cselect_b32 s53,11264,0
s_cselect_b32 s88,0,67584
s_cselect_b32 s87,64,0
s_cselect_b32 s69,0,0x70000000
s_mov_b32 s62,32832
s_cselect_b32 s62,88272,s62
s_add_u32 s62,s62,s58
v_cndmask_b32_e32 v5,v28,v27,vcc
s_mov_b32 s46,73728
s_mov_b32 s47,0x00020000
s_waitcnt lgkmcnt(0)
s_sub_u32 s36,s20,192
s_subb_u32 s37,s21,0
s_and_b32 s37,s37,0xffff
s_add_u32 m0,s57,0
s_nop 0
buffer_load_dwordx4 v6,s[36:39],s89 offen lds
s_add_u32 m0,s57,3536
s_add_u32 s89,s56,98304
buffer_load_dwordx4 v6,s[36:39],s89 offen lds
s_add_u32 m0,s57,7080
s_add_u32 s89,s56,196608
s_nop 0
buffer_load_dwordx4 v6,s[36:39],s89 offen lds
s_mov_b32 s48,s22
s_and_b32 s49,s23,0xffff
s_mov_b32 m0,12288
global_load_ushort v37,v33,s[32:33]
buffer_load_dwordx4 v44,s[48:51],0 offen lds
s_mov_b32 m0,13312
global_load_dword v35,v32,s[26:27]
buffer_load_dwordx4 v45,s[48:51],0 offen lds
global_load_ushort v36,v33,s[30:31]
global_load_dwordx4 v[160:163],v172,s[24:25]
global_load_dwordx4 v[164:167],v172,s[24:25] offset:3840
global_load_dwordx4 v[168:171],v173,s[28:29]
s_cmp_lt_u32 s52,2
s_cselect_b64 s[44:45],s[24:25],s[28:29]
s_and_b32 s45,s45,0xffff
s_mov_b32 s99,0x72800000
s_mov_b32 s98,0xffff0000
s_mov_b64 s[90:91],0
v_mov_b32 v96,0x409d3ef8
v_lshlrev_b32 v102,4,%1
v_lshlrev_b32 v104,19,%1
v_cmp_eq_u32_e64 s[94:95],0,%1
v_add_u32 v103,0x8000,v102
s_mov_b32 s4,0x3f000000
s_mov_b32 s5,0x3fc00000
s_mov_b32 s6,0x40200000
s_mov_b32 s7,0x40600000
s_mov_b32 s8,0x40900000
s_mov_b32 s9,0x40b00000
s_mov_b32 s10,0x40d00000
s_mov_b32 s11,0x40f00000
s_mov_b32 s12,0x3f80222e
s_mov_b32 s13,0x13089abf
s_mov_b32 s96,0xebefe000
v_add_u32 v28,0x10000,v104
s_add_u32 m0,s57,10616
s_add_u32 s89,s56,294912
buffer_load_dwordx4 v6,s[36:39],s89 offen lds
v_mul_f32_e64 v98,s99,v104
v_fma_f32 v100,v28,s99,-v98
v_pk_fma_f32 v[64:65],v[100:101],s[4:5],v[98:99] op_sel_hi:[0,1,0]
v_pk_fma_f32 v[66:67],v[100:101],s[6:7],v[98:99] op_sel_hi:[0,1,0]
v_pk_fma_f32 v[68:69],v[100:101],s[8:9],v[98:99] op_sel_hi:[0,1,0]
v_pk_fma_f32 v[70:71],v[100:101],s[10:11],v[98:99] op_sel_hi:[0,1,0]
v_cndmask_b32_e64 v64,v64,0,s[94:95]
v_pk_mul_f32 v[72:73],v[64:65],v[64:65]
v_pk_mul_f32 v[74:75],v[66:67],v[66:67]
v_pk_mul_f32 v[76:77],v[68:69],v[68:69]
v_pk_mul_f32 v[78:79],v[70:71],v[70:71]
v_pk_fma_f32 v[72:73],v[72:73],s[12:13],v[96:97] op_sel_hi:[1,0,0]
v_pk_fma_f32 v[74:75],v[74:75],s[12:13],v[96:97] op_sel_hi:[1,0,0]
v_pk_fma_f32 v[76:77],v[76:77],s[12:13],v[96:97] op_sel_hi:[1,0,0]
v_pk_fma_f32 v[78:79],v[78:79],s[12:13],v[96:97] op_sel_hi:[1,0,0]
s_add_u32 m0,s57,14144
s_add_u32 s89,s56,393216
buffer_load_dwordx4 v6,s[36:39],s89 offen lds
v_pk_mul_f32 v[72:73],v[72:73],v[64:65]
v_pk_mul_f32 v[74:75],v[74:75],v[66:67]
v_pk_mul_f32 v[76:77],v[76:77],v[68:69]
v_pk_mul_f32 v[78:79],v[78:79],v[70:71]
v_exp_f32 v72,v72
v_exp_f32 v73,v73
v_exp_f32 v74,v74
v_exp_f32 v75,v75
v_exp_f32 v76,v76
v_exp_f32 v77,v77
s_add_u32 m0,s57,17680
s_add_u32 s89,s56,491520
buffer_load_dwordx4 v6,s[36:39],s89 offen lds
v_exp_f32 v78,v78
v_exp_f32 v79,v79
v_pk_fma_f32 v[72:73],v[72:73],s[96:97],s[96:97] op_sel_hi:[1,0,0]
v_pk_fma_f32 v[74:75],v[74:75],s[96:97],s[96:97] op_sel_hi:[1,0,0]
v_pk_fma_f32 v[76:77],v[76:77],s[96:97],s[96:97] op_sel_hi:[1,0,0]
v_pk_fma_f32 v[78:79],v[78:79],s[96:97],s[96:97] op_sel_hi:[1,0,0]
v_rcp_f32 v72,v72
v_rcp_f32 v73,v73
v_rcp_f32 v74,v74
v_rcp_f32 v75,v75
s_add_u32 m0,s57,21224
s_add_u32 s89,s56,589824
buffer_load_dwordx4 v6,s[36:39],s89 offen lds
v_rcp_f32 v76,v76
v_rcp_f32 v77,v77
v_rcp_f32 v78,v78
v_rcp_f32 v79,v79
v_pk_mul_f32 v[72:73],v[72:73],v[64:65]
v_pk_mul_f32 v[74:75],v[74:75],v[66:67]
v_pk_mul_f32 v[76:77],v[76:77],v[68:69]
v_pk_mul_f32 v[78:79],v[78:79],v[70:71]
v_pk_fma_f32 v[64:65],v[64:65],s[12:13],v[72:73] op_sel:[0,1,0] op_sel_hi:[1,1,1]
v_pk_fma_f32 v[66:67],v[66:67],s[12:13],v[74:75] op_sel:[0,1,0] op_sel_hi:[1,1,1]
v_pk_fma_f32 v[68:69],v[68:69],s[12:13],v[76:77] op_sel:[0,1,0] op_sel_hi:[1,1,1]
s_add_u32 m0,s57,24760
s_add_u32 s89,s56,688128
buffer_load_dwordx4 v6,s[36:39],s89 offen lds
v_pk_fma_f32 v[70:71],v[70:71],s[12:13],v[78:79] op_sel:[0,1,0] op_sel_hi:[1,1,1]
v_cvt_pk_bf16_f32 v80,v72,v73
v_cvt_pk_bf16_f32 v81,v74,v75
v_cvt_pk_bf16_f32 v82,v76,v77
v_cvt_pk_bf16_f32 v83,v78,v79
v_cvt_pk_bf16_f32 v84,v64,v65
v_cvt_pk_bf16_f32 v85,v66,v67
v_cvt_pk_bf16_f32 v86,v68,v69
v_cvt_pk_bf16_f32 v87,v70,v71
ds_write_b128 v102,v[80:83] offset:0
ds_write_b128 v103,v[84:87] offset:32768
v_add_u32 v104,0x8000000,v104
v_add_u32 v28,0x10000,v104
v_mul_f32_e64 v98,s99,v104
v_fma_f32 v100,v28,s99,-v98
s_add_u32 m0,s57,28288
s_add_u32 s89,s56,786432
buffer_load_dwordx4 v6,s[36:39],s89 offen lds
v_pk_fma_f32 v[64:65],v[100:101],s[4:5],v[98:99] op_sel_hi:[0,1,0]
v_pk_fma_f32 v[66:67],v[100:101],s[6:7],v[98:99] op_sel_hi:[0,1,0]
v_pk_fma_f32 v[68:69],v[100:101],s[8:9],v[98:99] op_sel_hi:[0,1,0]
v_pk_fma_f32 v[70:71],v[100:101],s[10:11],v[98:99] op_sel_hi:[0,1,0]
v_pk_mul_f32 v[72:73],v[64:65],v[64:65]
v_pk_mul_f32 v[74:75],v[66:67],v[66:67]
v_pk_mul_f32 v[76:77],v[68:69],v[68:69]
v_pk_mul_f32 v[78:79],v[70:71],v[70:71]
v_pk_fma_f32 v[72:73],v[72:73],s[12:13],v[96:97] op_sel_hi:[1,0,0]
v_pk_fma_f32 v[74:75],v[74:75],s[12:13],v[96:97] op_sel_hi:[1,0,0]
v_pk_fma_f32 v[76:77],v[76:77],s[12:13],v[96:97] op_sel_hi:[1,0,0]
v_pk_fma_f32 v[78:79],v[78:79],s[12:13],v[96:97] op_sel_hi:[1,0,0]
v_pk_mul_f32 v[72:73],v[72:73],v[64:65]
v_pk_mul_f32 v[74:75],v[74:75],v[66:67]
v_pk_mul_f32 v[76:77],v[76:77],v[68:69]
s_add_u32 m0,s57,31824
s_add_u32 s89,s56,884736
buffer_load_dwordx4 v6,s[36:39],s89 offen lds
v_pk_mul_f32 v[78:79],v[78:79],v[70:71]
v_exp_f32 v72,v72
v_exp_f32 v73,v73
v_exp_f32 v74,v74
v_exp_f32 v75,v75
v_exp_f32 v76,v76
v_exp_f32 v77,v77
v_exp_f32_e64 v78,v78
s_add_u32 m0,s57,35368
s_add_u32 s89,s56,983040
buffer_load_dwordx4 v6,s[36:39],s89 offen lds
v_exp_f32_e64 v79,v79
v_pk_fma_f32 v[72:73],v[72:73],s[96:97],s[96:97] op_sel_hi:[1,0,0]
v_pk_fma_f32 v[74:75],v[74:75],s[96:97],s[96:97] op_sel_hi:[1,0,0]
v_pk_fma_f32 v[76:77],v[76:77],s[96:97],s[96:97] op_sel_hi:[1,0,0]
v_pk_fma_f32 v[78:79],v[78:79],s[96:97],s[96:97] op_sel_hi:[1,0,0]
v_rcp_f32 v72,v72
v_rcp_f32 v73,v73
v_rcp_f32 v74,v74
v_rcp_f32 v75,v75
v_rcp_f32_e64 v76,v76
s_add_u32 m0,s57,38904
s_add_u32 s89,s56,1081344
buffer_load_dwordx4 v6,s[36:39],s89 offen lds
v_rcp_f32 v77,v77
v_rcp_f32 v78,v78
v_rcp_f32_e64 v79,v79
v_pk_mul_f32 v[72:73],v[72:73],v[64:65]
v_pk_mul_f32 v[74:75],v[74:75],v[66:67]
v_pk_mul_f32 v[76:77],v[76:77],v[68:69]
v_pk_mul_f32 v[78:79],v[78:79],v[70:71]
v_pk_fma_f32 v[64:65],v[64:65],s[12:13],v[72:73] op_sel:[0,1,0] op_sel_hi:[1,1,1]
v_pk_fma_f32 v[66:67],v[66:67],s[12:13],v[74:75] op_sel:[0,1,0] op_sel_hi:[1,1,1]
v_pk_fma_f32 v[68:69],v[68:69],s[12:13],v[76:77] op_sel:[0,1,0] op_sel_hi:[1,1,1]
v_pk_fma_f32 v[70:71],v[70:71],s[12:13],v[78:79] op_sel:[0,1,0] op_sel_hi:[1,1,1]
v_cvt_pk_bf16_f32 v88,v72,v73
v_cvt_pk_bf16_f32 v89,v74,v75
v_cvt_pk_bf16_f32 v90,v76,v77
v_cvt_pk_bf16_f32 v91,v78,v79
v_cvt_pk_bf16_f32 v92,v64,v65
v_cvt_pk_bf16_f32 v93,v66,v67
v_cvt_pk_bf16_f32 v94,v68,v69
v_cvt_pk_bf16_f32 v95,v70,v71
ds_write_b128 v102,v[88:91] offset:4096
ds_write_b128 v103,v[92:95] offset:36864
s_mov_b32 s96,0x8befe000
s_lshl_b32 s66,s52,2
s_mul_i32 s60,s66,3536
s_add_u32 s66,s66,s71
s_lshl2_add_u32 s16,s70,s52
s_mul_i32 s16,s16,512
v_and_b32 v25,15,v24
v_lshrrev_b32_e64 v26,4,v24
s_mul_i32 s61,s66,98304
s_add_u32 s61,s61,s68
s_mov_b32 s54,0x44ea44ea
s_mov_b32 s55,0x877f877f
s_mov_b64 s[40:41],s[34:35]
s_and_b32 s41,s41,0xffff
s_mov_b32 s42,50331648
s_mov_b32 s43,0x00020000
v_mov_b32 v41,0x70000
s_add_u32 m0,s57,42432
s_add_u32 s89,s56,1179648
buffer_load_dwordx4 v6,s[36:39],s89 offen lds
v_and_b32 v39,8,v24
v_and_b32 v40,7,v24
v_bfe_u32 v27,v24,1,1
v_sub_u32_e64 v27,v26,v27
v_cmp_gt_u32_e64 s[92:93],3,v27
v_and_b32 v28,1,v24
v_mul_u32_u24 v27,3,v27
v_sub_u32_e64 v27,v27,v28
v_cmp_eq_u32_e64 s[94:95],0,v28
v_lshrrev_b32_e64 v29,2,v25
v_mul_i32_i24 v27,208,v27
v_lshl_add_u32 v27,v29,1,v27
v_mul_u32_u24 v10,0x5555,v29
v_add_u32 v27,12288,v27
v_cndmask_b32_e64 v45,v41,v27,s[92:93]
v_cndmask_b32_e64 v44,v41,v45,s[94:95]
v_cndmask_b32_e64 v47,v45,v41,s[94:95]
s_waitcnt vmcnt(13)
s_nop 0
ds_read_u16 a96,v44 offset:0
ds_read_u16 a97,v45 offset:208
ds_read_u16 a98,v45 offset:416
ds_read_u16 a99,v47 offset:624
ds_read_u16 a100,v44 offset:8
ds_read_u16 a101,v45 offset:216
ds_read_u16 a102,v45 offset:424
ds_read_u16 a103,v47 offset:632
v_mov_b64 v[20:21],0
v_mov_b64 v[22:23],0
v_cmp_eq_u32_e64 s[92:93],v26,v29
v_lshlrev_b32_e64 v27,4,v25
v_add_u32 v27,10112,v27
v_cndmask_b32_e64 v13,v41,v27,s[92:93]
v_lshrrev_b32 v27,2,v39
v_lshrrev_b32 v28,5,v24
v_add_u32_e64 v27,v27,v28
s_add_u32 m0,s57,45968
s_add_u32 s89,s56,1277952
buffer_load_dwordx4 v6,s[36:39],s89 offen lds
v_mul_u32_u24 v27,3536,v27
v_lshl_or_b32 v10,v27,16,v10
v_and_b32 v27,1,v26
v_add_u32 v27,v27,v40
v_mul_u32_u24 v28,384,v27
v_lshrrev_b32_e64 v27,1,v27
v_lshl_add_u32 v28,v27,4,v28
v_mfma_f32_32x32x16_bf16 v[64:79],v[20:23],v[20:23],0
ds_read_u16 a104,v44 offset:16
ds_read_u16 a105,v45 offset:224
ds_read_u16 a106,v45 offset:432
ds_read_u16 a107,v47 offset:640
ds_read_u16 a108,v44 offset:24
ds_read_u16 a109,v45 offset:232
ds_read_u16 a110,v45 offset:440
ds_read_u16 a111,v47 offset:648
v_add_u32 v30,73728,v39
v_add_u32 v11,v28,v30
s_movk_i32 s66,16384
v_lshl_add_u32 v27,v40,1,1
v_lshrrev_b32 v28,2,v27
v_xor_b32 v28,v28,v26
v_mul_u32_u24 v27,192,v27
v_lshl_add_u32 v27,v28,4,v27
v_add3_u32 v12,v27,v39,s66
v_lshl_add_u32 v27,v40,1,2
v_lshrrev_b32 v28,2,v27
v_xor_b32 v28,v28,v26
v_mul_u32_u24 v27,192,v27
v_lshl_add_u32 v27,v28,4,v27
v_add3_u32 v27,v27,v39,s66
v_lshl_or_b32 v12,v27,16,v12
v_mfma_f32_32x32x16_bf16 v[80:95],v[20:23],v[20:23],0
ds_read_u16 a112,v44 offset:32
ds_read_u16 a113,v45 offset:240
ds_read_u16 a114,v45 offset:448
ds_read_u16 a115,v47 offset:656
ds_read_u16 a116,v44 offset:40
ds_read_u16 a117,v45 offset:248
ds_read_u16 a118,v45 offset:456
ds_read_u16 a119,v47 offset:664
v_mul_u32_u24 v27,24576,v39
s_add_u32 m0,s57,49512
s_add_u32 s89,s56,1376256
buffer_load_dwordx4 v6,s[36:39],s89 offen lds
v_mul_u32_u24 v28,384,v40
v_lshl_add_u32 v27,v26,4,v27
v_add_u32 v7,v27,v28
v_lshrrev_b32 v27,3,v25
v_bfe_u32 v28,v25,2,1
v_lshl_add_u32 v28,v27,3,v28
v_lshl_add_u32 v28,v26,1,v28
v_lshlrev_b32_e64 v28,6,v28
v_lshl_add_u32 v28,v27,4,v28
v_and_b32_e64 v29,1,v25
v_lshl_add_u32 v28,v29,5,v28
v_bfe_u32 v29,v25,1,1
v_lshl_add_u32 v28,v29,3,v28
v_xor_b32_e64 v29,16,v28
v_add_u32 v1,16256,v28
v_mfma_f32_32x32x16_bf16 v[96:111],v[20:23],v[20:23],0
ds_read_u16 a120,v44 offset:48
ds_read_u16 a121,v45 offset:256
ds_read_u16 a122,v45 offset:464
ds_read_u16 a123,v47 offset:672
ds_read_u16 a124,v44 offset:56
ds_read_u16 a125,v45 offset:264
ds_read_u16 a126,v45 offset:472
ds_read_u16 a127,v47 offset:680
v_add_u32 v2,16256,v29
v_lshrrev_b32_e64 v27,3,v25
v_bfe_u32 v28,v25,2,1
v_lshl_add_u32 v27,v28,1,v27
v_lshrrev_b32_e64 v28,1,v26
v_lshl_add_u32 v27,v28,2,v27
v_mul_u32_u24 v27,192,v27
v_and_b32_e64 v29,1,v26
v_mul_u32_u24 v30,3080,v29
v_add_u32 v27,v27,v30
v_and_b32 v30,3,v25
s_add_u32 m0,s57,53048
s_add_u32 s89,s56,1474560
buffer_load_dwordx4 v6,s[36:39],s89 offen lds
v_lshl_add_u32 v27,v30,4,v27
v_add_u32 v3,40896,v27
v_lshlrev_b32_e64 v29,6,v29
v_lshl_add_u32 v29,v28,4,v29
v_add_u32 v4,8192,v29
v_mfma_f32_32x32x16_bf16 v[112:127],v[20:23],v[20:23],0
ds_read_u16 a128,v44 offset:64
ds_read_u16 a129,v45 offset:272
ds_read_u16 a130,v45 offset:480
ds_read_u16 a131,v47 offset:688
ds_read_u16 a132,v44 offset:72
ds_read_u16 a133,v45 offset:280
ds_read_u16 a134,v45 offset:488
ds_read_u16 a135,v47 offset:696
v_lshrrev_b32_e64 v27,5,v34
v_bfe_u32 v28,v34,2,1
v_and_b32 v29,3,v34
v_lshlrev_b32 v49,7,v27
v_lshl_add_u32 v49,v28,5,v49
v_lshl_add_u32 v49,v29,2,v49
v_bfe_u32 v30,v34,3,1
v_lshl_add_u32 v49,v30,6,v49
v_bfe_u32 v30,v34,4,1
v_lshl_add_u32 v49,v30,4,v49
v_add_u32 v49,11776,v49
v_lshrrev_b32_e64 v55,6,v34
v_bfe_u32 v30,v34,3,2
v_lshl_add_u32 v55,v55,2,v30
v_and_b32_e64 v30,1,v27
v_lshl_add_u32 v55,v55,1,v30
v_mfma_f32_32x32x16_bf16 v[128:143],v[20:23],v[20:23],0
ds_read_u16 a136,v44 offset:80
ds_read_u16 a137,v45 offset:288
ds_read_u16 a138,v45 offset:496
ds_read_u16 a139,v47 offset:704
ds_read_u16 a140,v44 offset:88
ds_read_u16 a141,v45 offset:296
ds_read_u16 a142,v45 offset:504
ds_read_u16 a143,v47 offset:712
v_and_b32_e64 v30,7,v34
s_add_u32 m0,s57,56576
s_add_u32 s89,s56,1572864
buffer_load_dwordx4 v6,s[36:39],s89 offen lds
v_lshl_add_u32 v55,v55,3,v30
v_lshlrev_b32 v55,2,v55
v_lshlrev_b32 v15,2,v24
v_lshl_add_u32 v50,v27,1,v28
v_lshlrev_b32_e64 v50,4,v50
v_bfe_u32 v30,v34,3,2
v_lshl_add_u32 v50,v30,2,v50
v_add_u32 v50,v50,v29
v_lshlrev_b32 v50,4,v50
v_add_u32 v50,10112,v50
v_lshlrev_b32_e64 v51,4,v29
v_cmp_eq_u32_e64 s[94:95],1,v28
v_lshlrev_b32_e64 v48,1,v32
v_add_u32 v48,8192,v48
v_mov_b32_e64 v53,0
v_mfma_f32_32x32x16_bf16 v[144:159],v[20:23],v[20:23],0
ds_read_u16 a144,v44 offset:96
ds_read_u16 a145,v45 offset:304
ds_read_u16 a146,v45 offset:512
ds_read_u16 a147,v47 offset:720
ds_read_u16 a148,v44 offset:104
ds_read_u16 a149,v45 offset:312
ds_read_u16 a150,v45 offset:520
ds_read_u16 a151,v47 offset:728
v_lshlrev_b32 v37,16,v37
v_lshlrev_b32 v36,16,v36
v_mov_b32 v27,0x45800000
v_mov_b32 v28,0x53800000
v_cmp_nlt_f32_e64 s[90:91],|v37|,v27
v_cmp_nlt_f32_e64 vcc,|v36|,v28
v_mov_b32 v27,0x42800000
v_mul_f32 v37,0x6c800000,v37
v_mul_f32 v36,0x12800000,v36
v_rcp_f32_e64 v56,v37
s_add_u32 m0,s57,60112
s_add_u32 s89,s56,1671168
buffer_load_dwordx4 v6,s[36:39],s89 offen lds
v_lshlrev_b32_e64 v42,16,v35
v_and_b32 v43,s98,v35
s_or_b64 s[90:91],s[90:91],vcc
v_cmp_nlt_f32_e64 vcc,|v42|,v27
v_cmp_nlt_f32_e64 s[92:93],|v43|,v27
v_med3_f32 v56,v56,1.0,-1.0
ds_read_u16 a152,v44 offset:112
ds_read_u16 a153,v45 offset:320
ds_read_u16 a154,v45 offset:528
ds_read_u16 a155,v47 offset:736
ds_read_u16 a156,v44 offset:120
ds_read_u16 a157,v45 offset:328
ds_read_u16 a158,v45 offset:536
ds_read_u16 a159,v47 offset:744
v_pk_mul_f32 v[42:43],v[42:43],s[96:97] op_sel_hi:[1,0]
v_cvt_pk_bf16_f32 v52,v56,v53
ds_write_b64 v48,v[42:43]
ds_write_b32 v49,v36
v_lshlrev_b32 v54,16,v52
v_rcp_f32 v54,v54
v_lshlrev_b64 v[52:53],v51,v[52:53]
s_or_b64 s[90:91],s[90:91],vcc
s_or_b64 s[90:91],s[90:91],s[92:93]
v_cndmask_b32_e64 v56,v52,0,s[94:95]
v_cndmask_b32_e64 v58,0,v52,s[94:95]
v_cndmask_b32_e64 v57,v53,0,s[94:95]
v_cndmask_b32_e64 v59,0,v53,s[94:95]
ds_write_b32 v15,v7 offset:14848
ds_write_b32 v15,v13 offset:15104
s_mul_i32 s66,s52,1536
ds_read_u16 a160,v44 offset:128
ds_read_u16 a161,v45 offset:336
ds_read_u16 a162,v45 offset:544
ds_read_u16 a163,v47 offset:752
ds_read_u16 a164,v44 offset:136
ds_read_u16 a165,v45 offset:344
ds_read_u16 a166,v45 offset:552
ds_read_u16 a167,v47 offset:760
s_add_u32 m0,s58,18480
s_nop 0
buffer_load_dwordx4 v5,s[44:47],s69 offen offset:0 lds
s_add_u32 s69,s69,s53
buffer_load_dwordx4 v5,s[44:47],s69 offen offset:1024 lds
s_mov_b64 exec,0
s_cmp_eq_u32 s52,0
s_cbranch_scc1 TE00_%=
s_cmp_eq_u32 s52,1
s_cbranch_scc1 TE01_%=
s_cmp_eq_u32 s52,2
s_cbranch_scc1 TE02_%=
s_branch TE03_%=
TR0_%=:
s_mov_b64 exec,-1
s_add_u32 s66,s66,16256
v_lshl_add_u32 v7,v24,3,s66
ds_write_b128 v50,v[56:59]
ds_write_b32 v55,v54 offset:14336
s_waitcnt vmcnt(17)
v_and_b32 v160,0x7fff7fff,v160
v_and_b32 v161,0x7fff7fff,v161
v_and_b32 v162,0x7fff7fff,v162
v_and_b32 v163,0x7fff7fff,v163
v_and_b32 v164,0x7fff7fff,v164
v_and_b32 v165,0x7fff7fff,v165
v_and_b32 v166,0x7fff7fff,v166
v_and_b32 v167,0x7fff7fff,v167
v_and_b32 v168,0x7fff7fff,v168
v_and_b32 v169,0x7fff7fff,v169
v_and_b32 v170,0x7fff7fff,v170
TE00_%=:
s_cbranch_execz TP2751_%=
ds_read_u16 a168,v44 offset:144
ds_read_u16 a169,v45 offset:352
ds_read_u16 a170,v45 offset:560
ds_read_u16 a171,v47 offset:768
ds_read_u16 a172,v44 offset:152
ds_read_u16 a173,v45 offset:360
ds_read_u16 a174,v45 offset:568
ds_read_u16 a175,v47 offset:776
v_and_b32 v171,0x7fff7fff,v171
s_add_u32 s69,s69,s53
buffer_load_dwordx4 v5,s[44:47],s69 offen offset:2048 lds
v_pk_max_u16 v160,v160,v164
v_pk_max_u16 v161,v161,v165
v_pk_max_u16 v162,v162,v166
v_pk_max_u16 v163,v163,v167
v_mov_b64 v[164:165],v[160:161]
v_mov_b64 v[166:167],v[162:163]
s_nop 0
v_permlane32_swap_b32 v160,v164
v_permlane32_swap_b32 v161,v165
v_permlane32_swap_b32 v162,v166
v_permlane32_swap_b32_e64 v163,v167
v_pk_max_u16 v160,v160,v164
v_pk_max_u16 v161,v161,v165
v_pk_max_u16 v162,v162,v166
v_pk_max_u16 v163,v163,v167
v_mov_b64_e64 v[164:165],v[160:161]
ds_read_u16 a176,v44 offset:160
ds_read_u16 a177,v45 offset:368
ds_read_u16 a178,v45 offset:576
ds_read_u16 a179,v47 offset:784
ds_read_u16 a180,v44 offset:168
ds_read_u16 a181,v45 offset:376
ds_read_u16 a182,v45 offset:584
ds_read_u16 a183,v47 offset:792
v_mov_b64 v[166:167],v[162:163]
v_permlane16_swap_b32 v160,v164
v_permlane16_swap_b32 v161,v165
v_permlane16_swap_b32 v162,v166
v_permlane16_swap_b32_e64 v163,v167
v_pk_max_u16 v160,v160,v164
v_pk_max_u16 v161,v161,v165
v_pk_max_u16 v162,v162,v166
v_pk_max_u16 v163,v163,v167
v_pk_min_u16 v160,v160,v161
v_pk_min_u16 v162,v162,v163
v_pk_min_u16 v160,v160,v162
v_pk_min_u16 v160,v160,v160 op_sel:[0,1] op_sel_hi:[1,0]
v_pk_max_u16 v168,v168,v169
v_pk_max_u16 v170,v170,v171
v_pk_max_u16 v168,v168,v170
ds_read_u16 a184,v44 offset:176
ds_read_u16 a185,v45 offset:384
ds_read_u16 a186,v45 offset:592
ds_read_u16 a187,v47 offset:800
ds_read_u16 a188,v44 offset:184
ds_read_u16 a189,v45 offset:392
ds_read_u16 a190,v45 offset:600
ds_read_u16 a191,v47 offset:808
v_pk_max_u16 v168,v168,v168 op_sel:[0,1] op_sel_hi:[1,0]
s_add_u32 m0,s58,0
s_mov_b32 s69,s87
buffer_load_dwordx4 v5,s[44:47],s69 offen offset:0 lds
v_cmp_gt_u16_e32 vcc,0x3880,v160
s_nop 0
s_or_b64 s[90:91],s[90:91],vcc
v_cmp_le_u16_e32 vcc,0x5380,v168
s_nop 0
s_or_b64 s[90:91],s[90:91],vcc
v_accvgpr_write_b32 a88,0
v_accvgpr_write_b32 a89,0
v_accvgpr_write_b32 a90,0
v_accvgpr_write_b32 a91,0
s_add_u32 s69,s69,s53
buffer_load_dwordx4 v5,s[44:47],s69 offen offset:1024 lds
s_add_u32 s69,s69,s53
buffer_load_dwordx4 v5,s[44:47],s69 offen offset:2048 lds
s_add_u32 m0,s58,6160
s_add_u32 s69,s69,s59
buffer_load_dwordx4 v5,s[44:47],s69 offen offset:0 lds
s_add_u32 s69,s69,s53
buffer_load_dwordx4 v5,s[44:47],s69 offen offset:1024 lds
s_add_u32 s69,s69,s53
buffer_load_dwordx4 v5,s[44:47],s69 offen offset:2048 lds
s_add_u32 m0,s58,12320
s_add_u32 s69,s69,s59
buffer_load_dwordx4 v5,s[44:47],s69 offen offset:0 lds
s_add_u32 s69,s69,s53
buffer_load_dwordx4 v5,s[44:47],s69 offen offset:1024 lds
s_add_u32 s69,s69,s53
buffer_load_dwordx4 v5,s[44:47],s69 offen offset:2048 lds
v_add_u32_sdwa v28,s60,v10 dst_sel:DWORD dst_unused:UNUSED_PAD src0_sel:DWORD src1_sel:WORD_1
v_subrev_u32 v8,88400,v28
v_min_u32_e64 v28,v28,v8
v_add_u32 v29,7072,v28
v_subrev_u32 v8,88400,v29
v_min_u32 v29,v29,v8
v_xad_u32 v24,v11,0,v28
v_xad_u32 v25,v11,8,v29
v_xad_u32 v26,v11,16,v28
v_xad_u32 v27,v11,24,v29
s_waitcnt lgkmcnt(0)
s_waitcnt vmcnt(12)
s_barrier
ds_read_b64 a[0:1],v24 offset:0
ds_read_b64 a[2:3],v24 offset:192
ds_read_b64 a[4:5],v25 offset:0
ds_read_b64 a[6:7],v25 offset:192
ds_read_b64 a[8:9],v24 offset:8
ds_read_b64 a[10:11],v24 offset:200
ds_read_b64 a[12:13],v25 offset:8
ds_read_b64 a[14:15],v25 offset:200
ds_read_b64 a[16:17],v26 offset:0
ds_read_b64 a[18:19],v26 offset:192
ds_read_b64 a[20:21],v27 offset:0
ds_read_b64 a[22:23],v27 offset:192
s_waitcnt lgkmcnt(8)
v_smfmac_f32_16x16x64_bf16 v[64:67],a[96:99],a[0:7],v10
s_waitcnt lgkmcnt(4)
ds_read_b64 a[0:1],v26 offset:8
ds_read_b64 a[2:3],v26 offset:200
ds_read_b64 a[4:5],v27 offset:8
ds_read_b64 a[6:7],v27 offset:200
v_smfmac_f32_16x16x64_bf16 v[68:71],a[100:103],a[8:15],v10
s_waitcnt lgkmcnt(4)
ds_read_b64 a[8:9],v24 offset:64
ds_read_b64 a[10:11],v24 offset:256
ds_read_b64 a[12:13],v25 offset:64
ds_read_b64 a[14:15],v25 offset:256
v_smfmac_f32_16x16x64_bf16 v[72:75],a[104:107],a[16:23],v10
s_waitcnt lgkmcnt(4)
ds_read_b64 a[16:17],v24 offset:72
ds_read_b64 a[18:19],v24 offset:264
ds_read_b64 a[20:21],v25 offset:72
ds_read_b64 a[22:23],v25 offset:264
v_and_b32 v248,8,v11
v_pk_mul_f32 v[40:41],v[64:65],v[64:65]
v_pk_mul_f32 v[42:43],v[66:67],v[66:67]
v_smfmac_f32_16x16x64_bf16 v[76:79],a[108:111],a[0:7],v10
s_waitcnt lgkmcnt(4)
ds_read_b64 a[0:1],v26 offset:64
ds_read_b64 a[2:3],v26 offset:256
ds_read_b64 a[4:5],v27 offset:64
ds_read_b64 a[6:7],v27 offset:256
v_mul_u32_u24 v248,884,v248
s_add_u32 s64,s60,3536
v_pk_add_f32 v[36:37],v[64:65],v[68:69]
v_pk_fma_f32 v[40:41],v[68:69],v[68:69],v[40:41]
v_pk_add_f32 v[38:39],v[66:67],v[70:71]
v_pk_fma_f32 v[42:43],v[70:71],v[70:71],v[42:43]
v_smfmac_f32_16x16x64_bf16 v[80:83],a[128:131],a[8:15],v10
s_waitcnt lgkmcnt(4)
ds_read_b64 a[8:9],v26 offset:72
ds_read_b64 a[10:11],v26 offset:264
ds_read_b64 a[12:13],v27 offset:72
ds_read_b64 a[14:15],v27 offset:264
v_add_u32 v251,s64,v248
v_pk_add_f32 v[36:37],v[36:37],v[72:73]
v_pk_fma_f32 v[40:41],v[72:73],v[72:73],v[40:41]
v_pk_add_f32 v[38:39],v[38:39],v[74:75]
v_pk_fma_f32 v[42:43],v[74:75],v[74:75],v[42:43]
v_smfmac_f32_16x16x64_bf16 v[84:87],a[132:135],a[16:23],v10
s_waitcnt lgkmcnt(4)
s_nop 0
ds_read_b64 a[16:17],v24 offset:128
ds_read_b64 a[18:19],v24 offset:320
ds_read_b64 a[20:21],v25 offset:128
ds_read_b64 a[22:23],v25 offset:320
v_subrev_u32 v8,88400,v251
v_pk_add_f32 v[36:37],v[36:37],v[76:77]
v_pk_fma_f32 v[40:41],v[76:77],v[76:77],v[40:41]
v_pk_add_f32 v[38:39],v[38:39],v[78:79]
v_pk_fma_f32 v[42:43],v[78:79],v[78:79],v[42:43]
v_smfmac_f32_16x16x64_bf16 v[88:91],a[136:139],a[0:7],v10
s_waitcnt lgkmcnt(4)
ds_read_b64 a[0:1],v24 offset:136
ds_read_b64 a[2:3],v24 offset:328
ds_read_b64 a[4:5],v25 offset:136
ds_read_b64 a[6:7],v25 offset:328
v_min_u32 v251,v251,v8
v_pk_add_f32 v[36:37],v[36:37],v[80:81]
v_pk_fma_f32 v[40:41],v[80:81],v[80:81],v[40:41]
v_pk_add_f32 v[38:39],v[38:39],v[82:83]
v_pk_fma_f32 v[42:43],v[82:83],v[82:83],v[42:43]
v_smfmac_f32_16x16x64_bf16 v[92:95],a[140:143],a[8:15],v10
s_waitcnt lgkmcnt(4)
ds_read_b64 a[8:9],v26 offset:128
ds_read_b64 a[10:11],v26 offset:320
ds_read_b64 a[12:13],v27 offset:128
ds_read_b64 a[14:15],v27 offset:320
v_xad_u32 v24,v11,32,v28
v_xad_u32 v25,v11,40,v29
v_pk_add_f32 v[36:37],v[36:37],v[84:85]
v_pk_fma_f32 v[40:41],v[84:85],v[84:85],v[40:41]
v_pk_add_f32 v[38:39],v[38:39],v[86:87]
v_pk_fma_f32 v[42:43],v[86:87],v[86:87],v[42:43]
v_smfmac_f32_16x16x64_bf16 v[96:99],a[160:163],a[16:23],v10
s_waitcnt lgkmcnt(4)
ds_read_b64 a[16:17],v26 offset:136
ds_read_b64 a[18:19],v26 offset:328
ds_read_b64 a[20:21],v27 offset:136
ds_read_b64 a[22:23],v27 offset:328
v_add_u32 v250,3536,v251
v_pk_add_f32 v[36:37],v[36:37],v[88:89]
v_pk_fma_f32 v[40:41],v[88:89],v[88:89],v[40:41]
v_pk_add_f32 v[38:39],v[38:39],v[90:91]
v_pk_fma_f32 v[42:43],v[90:91],v[90:91],v[42:43]
v_smfmac_f32_16x16x64_bf16 v[100:103],a[164:167],a[0:7],v10
s_waitcnt lgkmcnt(4)
ds_read_b64 a[0:1],v24 offset:0
ds_read_b64 a[2:3],v24 offset:192
ds_read_b64 a[4:5],v25 offset:0
ds_read_b64 a[6:7],v25 offset:192
v_xad_u32 v26,v11,48,v28
v_xad_u32 v27,v11,56,v29
v_pk_add_f32 v[36:37],v[36:37],v[92:93]
v_pk_fma_f32 v[40:41],v[92:93],v[92:93],v[40:41]
v_pk_add_f32 v[38:39],v[38:39],v[94:95]
v_pk_fma_f32 v[42:43],v[94:95],v[94:95],v[42:43]
v_smfmac_f32_16x16x64_bf16 v[104:107],a[168:171],a[8:15],v10
s_waitcnt lgkmcnt(4)
s_add_u32 m0,s57,63656
s_add_u32 s89,s56,1769472
buffer_load_dwordx4 v6,s[36:39],s89 offen lds
ds_read_b64 a[8:9],v24 offset:8
ds_read_b64 a[10:11],v24 offset:200
ds_read_b64 a[12:13],v25 offset:8
ds_read_b64 a[14:15],v25 offset:200
v_subrev_u32 v8,88400,v250
v_pk_add_f32 v[36:37],v[36:37],v[96:97]
v_pk_fma_f32 v[40:41],v[96:97],v[96:97],v[40:41]
v_pk_add_f32 v[38:39],v[38:39],v[98:99]
v_pk_fma_f32 v[42:43],v[98:99],v[98:99],v[42:43]
v_smfmac_f32_16x16x64_bf16 v[108:111],a[172:175],a[16:23],v10
s_waitcnt lgkmcnt(4)
s_add_u32 m0,s57,67192
s_add_u32 s89,s56,1867776
buffer_load_dwordx4 v6,s[36:39],s89 offen lds
ds_read_b64 a[16:17],v24 offset:64
ds_read_b64 a[18:19],v24 offset:256
ds_read_b64 a[20:21],v25 offset:64
ds_read_b64 a[22:23],v25 offset:256
v_min_u32 v250,v250,v8
v_pk_add_f32 v[36:37],v[36:37],v[100:101]
v_pk_fma_f32 v[40:41],v[100:101],v[100:101],v[40:41]
v_pk_add_f32 v[38:39],v[38:39],v[102:103]
v_pk_fma_f32 v[42:43],v[102:103],v[102:103],v[42:43]
v_smfmac_f32_16x16x64_bf16 v[112:115],a[112:115],a[0:7],v10
s_waitcnt lgkmcnt(4)
ds_read_b64 a[0:1],v24 offset:72
ds_read_b64 a[2:3],v24 offset:264
ds_read_b64 a[4:5],v25 offset:72
ds_read_b64 a[6:7],v25 offset:264
v_xor_b32 v9,0x80008,v12
v_pk_add_f32 v[36:37],v[36:37],v[104:105]
v_pk_fma_f32 v[40:41],v[104:105],v[104:105],v[40:41]
v_pk_add_f32 v[38:39],v[38:39],v[106:107]
v_pk_fma_f32 v[42:43],v[106:107],v[106:107],v[42:43]
v_smfmac_f32_16x16x64_bf16 v[116:119],a[116:119],a[8:15],v10
s_waitcnt lgkmcnt(4)
s_add_u32 m0,s57,70720
s_add_u32 s89,s56,1966080
buffer_load_dwordx4 v6,s[36:39],s89 offen lds
ds_read_b64 a[8:9],v24 offset:128
ds_read_b64 a[10:11],v24 offset:320
ds_read_b64 a[12:13],v25 offset:128
ds_read_b64 a[14:15],v25 offset:320
v_add_u32_sdwa v248,v12,v251 dst_sel:DWORD dst_unused:UNUSED_PAD src0_sel:WORD_0 src1_sel:DWORD
v_pk_add_f32 v[36:37],v[36:37],v[108:109]
v_pk_fma_f32 v[40:41],v[108:109],v[108:109],v[40:41]
v_pk_add_f32 v[38:39],v[38:39],v[110:111]
v_pk_fma_f32 v[42:43],v[110:111],v[110:111],v[42:43]
v_smfmac_f32_16x16x64_bf16 v[120:123],a[144:147],a[16:23],v10
s_waitcnt lgkmcnt(4)
ds_read_b64 a[16:17],v24 offset:136
ds_read_b64 a[18:19],v24 offset:328
ds_read_b64 a[20:21],v25 offset:136
ds_read_b64 a[22:23],v25 offset:328
v_add_u32_sdwa v249,v12,v251 dst_sel:DWORD dst_unused:UNUSED_PAD src0_sel:WORD_1 src1_sel:DWORD
v_pk_add_f32 v[36:37],v[36:37],v[112:113]
v_pk_fma_f32 v[40:41],v[112:113],v[112:113],v[40:41]
v_pk_add_f32 v[38:39],v[38:39],v[114:115]
v_pk_fma_f32 v[42:43],v[114:115],v[114:115],v[42:43]
v_smfmac_f32_16x16x64_bf16 v[124:127],a[148:151],a[0:7],v10
s_waitcnt lgkmcnt(4)
s_add_u32 m0,s57,74256
s_add_u32 s89,s56,2064384
buffer_load_dwordx4 v6,s[36:39],s89 offen lds
ds_read_b64 a[0:1],v26 offset:0
ds_read_b64 a[2:3],v26 offset:192
ds_read_b64 a[4:5],v27 offset:0
ds_read_b64 a[6:7],v27 offset:192
v_add_u32_sdwa v251,v9,v250 dst_sel:DWORD dst_unused:UNUSED_PAD src0_sel:WORD_1 src1_sel:DWORD
v_pk_add_f32 v[36:37],v[36:37],v[116:117]
v_pk_fma_f32 v[40:41],v[116:117],v[116:117],v[40:41]
v_pk_add_f32 v[38:39],v[38:39],v[118:119]
v_pk_fma_f32 v[42:43],v[118:119],v[118:119],v[42:43]
v_smfmac_f32_16x16x64_bf16 v[128:131],a[176:179],a[8:15],v10
s_waitcnt lgkmcnt(4)
s_nop 0
ds_read_b64 a[8:9],v26 offset:8
ds_read_b64 a[10:11],v26 offset:200
ds_read_b64 a[12:13],v27 offset:8
ds_read_b64 a[14:15],v27 offset:200
v_add_u32_sdwa v250,v9,v250 dst_sel:DWORD dst_unused:UNUSED_PAD src0_sel:WORD_0 src1_sel:DWORD
v_pk_add_f32 v[36:37],v[36:37],v[120:121]
v_pk_fma_f32 v[40:41],v[120:121],v[120:121],v[40:41]
v_pk_add_f32 v[38:39],v[38:39],v[122:123]
v_pk_fma_f32 v[42:43],v[122:123],v[122:123],v[42:43]
v_smfmac_f32_16x16x64_bf16 v[132:135],a[180:183],a[16:23],v10
s_waitcnt lgkmcnt(4)
s_add_u32 m0,s57,77800
s_add_u32 s89,s56,2162688
buffer_load_dwordx4 v6,s[36:39],s89 offen lds
ds_read_b64 a[16:17],v26 offset:64
ds_read_b64 a[18:19],v26 offset:256
ds_read_b64 a[20:21],v27 offset:64
ds_read_b64 a[22:23],v27 offset:256
v_pk_add_f32 v[36:37],v[36:37],v[124:125]
v_pk_fma_f32 v[40:41],v[124:125],v[124:125],v[40:41]
v_pk_add_f32 v[38:39],v[38:39],v[126:127]
v_pk_fma_f32 v[42:43],v[126:127],v[126:127],v[42:43]
v_smfmac_f32_16x16x64_bf16 v[136:139],a[120:123],a[0:7],v10
s_waitcnt lgkmcnt(4)
ds_read_b64 a[0:1],v26 offset:72
ds_read_b64 a[2:3],v26 offset:264
ds_read_b64 a[4:5],v27 offset:72
ds_read_b64 a[6:7],v27 offset:264
v_pk_add_f32 v[36:37],v[36:37],v[128:129]
v_pk_fma_f32 v[40:41],v[128:129],v[128:129],v[40:41]
v_pk_add_f32 v[38:39],v[38:39],v[130:131]
v_pk_fma_f32 v[42:43],v[130:131],v[130:131],v[42:43]
v_smfmac_f32_16x16x64_bf16 v[140:143],a[124:127],a[8:15],v10
s_waitcnt lgkmcnt(4)
s_add_u32 m0,s57,81336
s_add_u32 s89,s56,2260992
buffer_load_dwordx4 v6,s[36:39],s89 offen lds
ds_read_b64 a[8:9],v26 offset:128
ds_read_b64 a[10:11],v26 offset:320
ds_read_b64 a[12:13],v27 offset:128
ds_read_b64 a[14:15],v27 offset:320
v_pk_add_f32 v[36:37],v[36:37],v[132:133]
v_pk_fma_f32 v[40:41],v[132:133],v[132:133],v[40:41]
v_pk_add_f32 v[38:39],v[38:39],v[134:135]
v_pk_fma_f32 v[42:43],v[134:135],v[134:135],v[42:43]
v_smfmac_f32_16x16x64_bf16 v[144:147],a[152:155],a[16:23],v10
s_waitcnt lgkmcnt(4)
ds_read_b64 a[16:17],v26 offset:136
ds_read_b64 a[18:19],v26 offset:328
ds_read_b64 a[20:21],v27 offset:136
ds_read_b64 a[22:23],v27 offset:328
v_pk_add_f32 v[36:37],v[36:37],v[136:137]
v_pk_fma_f32 v[40:41],v[136:137],v[136:137],v[40:41]
v_pk_add_f32 v[38:39],v[38:39],v[138:139]
v_pk_fma_f32 v[42:43],v[138:139],v[138:139],v[42:43]
v_smfmac_f32_16x16x64_bf16 v[148:151],a[156:159],a[0:7],v10
s_waitcnt lgkmcnt(4)
s_add_u32 m0,s57,84864
s_add_u32 s89,s56,2359296
buffer_load_dwordx4 v6,s[36:39],s89 offen lds
ds_read_b64 a[24:25],v248 offset:57344
ds_read_b64 a[26:27],v248 offset:57352
ds_read_b64 a[36:37],v249 offset:57344
ds_read_b64 a[38:39],v249 offset:57352
v_pk_add_f32 v[36:37],v[36:37],v[140:141]
v_pk_fma_f32 v[40:41],v[140:141],v[140:141],v[40:41]
v_pk_add_f32 v[38:39],v[38:39],v[142:143]
v_pk_fma_f32 v[42:43],v[142:143],v[142:143],v[42:43]
v_smfmac_f32_16x16x64_bf16 v[152:155],a[184:187],a[8:15],v10
s_waitcnt lgkmcnt(4)
ds_read_b64 a[28:29],v248 offset:57408
ds_read_b64 a[30:31],v248 offset:57416
ds_read_b64 a[40:41],v249 offset:57408
ds_read_b64 a[42:43],v249 offset:57416
v_pk_add_f32 v[36:37],v[36:37],v[144:145]
v_pk_fma_f32 v[40:41],v[144:145],v[144:145],v[40:41]
v_pk_add_f32 v[38:39],v[38:39],v[146:147]
v_pk_fma_f32 v[42:43],v[146:147],v[146:147],v[42:43]
v_smfmac_f32_16x16x64_bf16 v[156:159],a[188:191],a[16:23],v10
ds_read_b64 a[32:33],v248 offset:57472
ds_read_b64 a[34:35],v248 offset:57480
ds_read_b64 a[44:45],v249 offset:57472
ds_read_b64 a[46:47],v249 offset:57480
v_pk_add_f32 v[36:37],v[36:37],v[148:149]
v_pk_fma_f32 v[40:41],v[148:149],v[148:149],v[40:41]
v_pk_add_f32 v[38:39],v[38:39],v[150:151]
v_pk_fma_f32 v[42:43],v[150:151],v[150:151],v[42:43]
v_pk_add_f32 v[36:37],v[36:37],v[152:153]
v_pk_fma_f32 v[40:41],v[152:153],v[152:153],v[40:41]
v_pk_add_f32 v[38:39],v[38:39],v[154:155]
v_pk_fma_f32 v[42:43],v[154:155],v[154:155],v[42:43]
v_pk_add_f32 v[36:37],v[36:37],v[156:157]
v_pk_fma_f32 v[40:41],v[156:157],v[156:157],v[40:41]
v_pk_add_f32 v[38:39],v[38:39],v[158:159]
v_pk_fma_f32 v[42:43],v[158:159],v[158:159],v[42:43]
s_nop 0
v_permlane32_swap_b32 v36,v38
v_permlane32_swap_b32 v37,v39
v_permlane32_swap_b32 v40,v42
v_permlane32_swap_b32_e64 v41,v43
v_pk_add_f32 v[36:37],v[36:37],v[38:39]
v_pk_add_f32 v[40:41],v[40:41],v[42:43]
s_add_u32 s60,s60,56576
s_sub_u32 s64,s60,88400
s_min_u32 s60,s60,s64
v_permlane16_swap_b32 v36,v37
v_permlane16_swap_b32 v40,v41
v_add_f32 v8,v36,v37
v_add_f32_e64 v9,v40,v41
v_mul_f32 v8,0x3c2aaaab,v8
v_mul_f32 v9,0x3c2aaaab,v9
v_cmp_ngt_f32 vcc,0x5d800000,v9
v_fma_f32 v9,-v8,v8,v9
v_max_f32 v9,0,v9
s_or_b64 s[90:91],s[90:91],vcc
v_add_f32 v9,0x3727c5ac,v9
v_rsq_f32 v9,v9
s_nop 0
v_mul_f32 v36,s96,v9
v_mul_f32 v37,s96,v9
v_mul_f32 v40,-v8,v36
v_mul_f32 v41,-v8,v36
v_permlane16_swap_b32 v36,v37
s_nop 0
v_permlane16_swap_b32 v40,v41
v_mov_b64 v[38:39],v[36:37]
v_mov_b64 v[42:43],v[40:41]
s_nop 0
v_permlane32_swap_b32 v36,v38
v_permlane32_swap_b32 v37,v39
v_permlane32_swap_b32 v40,v42
v_permlane32_swap_b32 v41,v43
s_waitcnt vmcnt(13)
s_barrier
ds_read_b64_tr_b16 a[192:193],v1 offset:18480
ds_read_b64_tr_b16 a[194:195],v1 offset:19504
ds_read_b64_tr_b16 a[196:197],v1 offset:20528
ds_read_b64_tr_b16 a[198:199],v1 offset:21552
v_pk_fma_f32 v[64:65],v[64:65],v[36:37],v[40:41]
v_pk_fma_f32 v[66:67],v[66:67],v[38:39],v[42:43]
v_pk_fma_f32 v[68:69],v[68:69],v[36:37],v[40:41]
v_pk_fma_f32 v[70:71],v[70:71],v[38:39],v[42:43]
v_pk_fma_f32 v[72:73],v[72:73],v[36:37],v[40:41]
v_pk_fma_f32 v[74:75],v[74:75],v[38:39],v[42:43]
v_pk_fma_f32 v[76:77],v[76:77],v[36:37],v[40:41]
v_pk_fma_f32 v[78:79],v[78:79],v[38:39],v[42:43]
v_pk_fma_f32 v[112:113],v[112:113],v[36:37],v[40:41]
v_pk_fma_f32 v[114:115],v[114:115],v[38:39],v[42:43]
ds_read_b64_tr_b16 a[200:201],v1 offset:22576
ds_read_b64_tr_b16 a[202:203],v1 offset:23600
ds_read_b64_tr_b16 a[204:205],v2 offset:22576
ds_read_b64_tr_b16 a[206:207],v2 offset:23600
v_pk_fma_f32 v[116:117],v[116:117],v[36:37],v[40:41]
v_pk_fma_f32 v[118:119],v[118:119],v[38:39],v[42:43]
v_pk_fma_f32 v[136:137],v[136:137],v[36:37],v[40:41]
v_pk_fma_f32 v[138:139],v[138:139],v[38:39],v[42:43]
v_pk_fma_f32 v[140:141],v[140:141],v[36:37],v[40:41]
v_pk_fma_f32 v[142:143],v[142:143],v[38:39],v[42:43]
v_pk_fma_f32 v[80:81],v[80:81],v[36:37],v[40:41]
v_pk_fma_f32 v[82:83],v[82:83],v[38:39],v[42:43]
v_pk_fma_f32 v[84:85],v[84:85],v[36:37],v[40:41]
v_pk_fma_f32 v[86:87],v[86:87],v[38:39],v[42:43]
ds_read_b64_tr_b16 a[208:209],v2 offset:20528
ds_read_b64_tr_b16 a[210:211],v2 offset:21552
ds_read_b64_tr_b16 a[212:213],v2 offset:18480
ds_read_b64_tr_b16 a[214:215],v2 offset:19504
v_pk_fma_f32 v[88:89],v[88:89],v[36:37],v[40:41]
v_pk_fma_f32 v[90:91],v[90:91],v[38:39],v[42:43]
v_pk_fma_f32 v[92:93],v[92:93],v[36:37],v[40:41]
v_pk_fma_f32 v[94:95],v[94:95],v[38:39],v[42:43]
v_pk_fma_f32 v[120:121],v[120:121],v[36:37],v[40:41]
v_pk_fma_f32 v[122:123],v[122:123],v[38:39],v[42:43]
v_pk_fma_f32 v[124:125],v[124:125],v[36:37],v[40:41]
v_pk_fma_f32 v[126:127],v[126:127],v[38:39],v[42:43]
v_pk_fma_f32 v[144:145],v[144:145],v[36:37],v[40:41]
v_pk_fma_f32 v[146:147],v[146:147],v[38:39],v[42:43]
ds_read_b128 v[16:19],v4 offset:0
ds_read_b128 v[20:23],v4 offset:32
ds_read_b64 v[232:233],v7 offset:18480
ds_read_b64 v[234:235],v7 offset:18992
v_pk_fma_f32 v[148:149],v[148:149],v[36:37],v[40:41]
v_pk_fma_f32 v[150:151],v[150:151],v[38:39],v[42:43]
v_pk_fma_f32 v[96:97],v[96:97],v[36:37],v[40:41]
v_pk_fma_f32 v[98:99],v[98:99],v[38:39],v[42:43]
v_pk_fma_f32 v[100:101],v[100:101],v[36:37],v[40:41]
v_pk_fma_f32 v[102:103],v[102:103],v[38:39],v[42:43]
v_pk_fma_f32 v[104:105],v[104:105],v[36:37],v[40:41]
v_pk_fma_f32 v[106:107],v[106:107],v[38:39],v[42:43]
v_pk_fma_f32 v[108:109],v[108:109],v[36:37],v[40:41]
v_pk_fma_f32 v[110:111],v[110:111],v[38:39],v[42:43]
ds_read_b64 v[236:237],v7 offset:19504
v_pk_fma_f32 v[128:129],v[128:129],v[36:37],v[40:41]
v_pk_fma_f32 v[130:131],v[130:131],v[38:39],v[42:43]
v_pk_fma_f32 v[132:133],v[132:133],v[36:37],v[40:41]
v_pk_fma_f32 v[134:135],v[134:135],v[38:39],v[42:43]
v_pk_fma_f32 v[152:153],v[152:153],v[36:37],v[40:41]
v_pk_fma_f32 v[154:155],v[154:155],v[38:39],v[42:43]
v_pk_fma_f32 v[156:157],v[156:157],v[36:37],v[40:41]
v_pk_fma_f32 v[158:159],v[158:159],v[38:39],v[42:43]
v_cvt_pk_bf16_f32 v160,v64,v68
v_cvt_pk_bf16_f32 v161,v72,v76
v_cvt_pk_bf16_f32 v162,v112,v116
v_cvt_pk_bf16_f32 v163,v136,v140
v_cvt_pk_bf16_f32 v164,v65,v69
v_cvt_pk_bf16_f32 v165,v73,v77
v_cvt_pk_bf16_f32 v166,v113,v117
v_cvt_pk_bf16_f32 v167,v137,v141
v_cvt_pk_bf16_f32 v176,v80,v84
v_cvt_pk_bf16_f32 v177,v88,v92
v_cvt_pk_bf16_f32 v178,v120,v124
v_cvt_pk_bf16_f32 v179,v144,v148
v_cvt_pk_bf16_f32 v180,v81,v85
v_cvt_pk_bf16_f32 v181,v89,v93
v_cvt_pk_bf16_f32 v182,v121,v125
v_cvt_pk_bf16_f32 v183,v145,v149
v_cvt_pk_bf16_f32 v192,v96,v100
v_cvt_pk_bf16_f32 v193,v104,v108
v_cvt_pk_bf16_f32 v194,v128,v132
v_cvt_pk_bf16_f32 v195,v152,v156
v_cvt_pk_bf16_f32 v196,v97,v101
v_cvt_pk_bf16_f32 v197,v105,v109
v_cvt_pk_bf16_f32 v198,v129,v133
v_cvt_pk_bf16_f32 v199,v153,v157
s_waitcnt lgkmcnt(0)
v_or3_b32 v252,v232,v233,0
v_or3_b32 v252,v234,v235,v252
v_or3_b32 v252,v236,v237,v252
v_mfma_f32_16x16x32_bf16 v[44:47],a[192:195],v[160:163],v[16:19]
s_nop 0
ds_read_b128 a[48:51],v13 offset:0
v_cvt_pk_bf16_f32 v168,v66,v70
v_cvt_pk_bf16_f32 v169,v74,v78
v_mfma_f32_16x16x32_bf16 v[44:47],a[196:199],v[176:179],v[44:47]
ds_read_b128 v[208:211],v4 offset:3584
v_cvt_pk_bf16_f32 v170,v114,v118
v_cvt_pk_bf16_f32 v171,v138,v142
v_mfma_f32_16x16x32_bf16 v[44:47],a[200:203],v[192:195],v[44:47]
ds_read_b128 a[52:55],v13 offset:256
v_cvt_pk_bf16_f32 v172,v67,v71
v_cvt_pk_bf16_f32 v173,v75,v79
v_mfma_f32_16x16x32_bf16 v[48:51],a[204:207],v[192:195],v[20:23]
ds_read_b128 v[212:215],v4 offset:3616
v_cvt_pk_bf16_f32 v174,v115,v119
v_cvt_pk_bf16_f32 v175,v139,v143
v_mfma_f32_16x16x32_bf16 v[48:51],a[208:211],v[176:179],v[48:51]
ds_read_b128 a[56:59],v13 offset:512
v_cvt_pk_bf16_f32 v184,v82,v86
v_cvt_pk_bf16_f32 v185,v90,v94
v_mfma_f32_16x16x32_bf16 v[48:51],a[212:215],v[160:163],v[48:51]
ds_read_b128 v[216:219],v4 offset:3712
v_cvt_pk_bf16_f32 v186,v122,v126
v_cvt_pk_bf16_f32 v187,v146,v150
v_mfma_f32_16x16x32_bf16 v[52:55],a[212:215],v[164:167],v[20:23]
ds_read_b128 a[60:63],v13 offset:768
v_cvt_pk_bf16_f32 v188,v83,v87
v_cvt_pk_bf16_f32 v189,v91,v95
v_mfma_f32_16x16x32_bf16 v[52:55],a[208:211],v[180:183],v[52:55]
TE01_%=:
s_cbranch_execz TP3280_%=
ds_read_b128 v[220:223],v4 offset:3744
v_cvt_pk_bf16_f32 v190,v123,v127
v_cvt_pk_bf16_f32 v191,v147,v151
v_mfma_f32_16x16x32_bf16 v[52:55],a[204:207],v[196:199],v[52:55]
s_nop 0
ds_read_b128 a[64:67],v13 offset:1024
v_cvt_pk_bf16_f32 v200,v98,v102
v_cvt_pk_bf16_f32 v201,v106,v110
v_mfma_f32_16x16x32_bf16 v[56:59],a[200:203],v[196:199],v[16:19]
ds_read_b128 v[224:227],v4 offset:3840
v_cvt_pk_bf16_f32 v202,v130,v134
v_cvt_pk_bf16_f32 v203,v154,v158
v_mfma_f32_16x16x32_bf16 v[56:59],a[196:199],v[180:183],v[56:59]
ds_read_b128 a[68:71],v13 offset:1280
v_cvt_pk_bf16_f32 v204,v99,v103
v_cvt_pk_bf16_f32 v205,v107,v111
v_mfma_f32_16x16x32_bf16 v[56:59],a[192:195],v[164:167],v[56:59]
ds_read_b128 v[228:231],v4 offset:3872
v_cvt_pk_bf16_f32 v206,v131,v135
v_cvt_pk_bf16_f32 v207,v155,v159
v_mfma_f32_16x16x32_bf16 v[60:63],a[192:195],v[168:171],v[16:19]
ds_read_b64 v[14:15],v7 offset:0
ds_read_b64 a[0:1],v250 offset:57344
v_lshlrev_b32_sdwa v44,1,v44 dst_sel:DWORD dst_unused:UNUSED_PAD src0_sel:DWORD src1_sel:WORD_1
v_mfma_f32_16x16x32_bf16 v[60:63],a[196:199],v[184:187],v[60:63]
ds_read_b64 a[2:3],v250 offset:57352
ds_read_b64 a[12:13],v251 offset:57344
v_fma_mixlo_f16 v8,v53,s99,0
v_mfma_f32_16x16x32_bf16 v[60:63],a[200:203],v[200:203],v[60:63]
ds_read_b64 a[14:15],v251 offset:57352
ds_read_b64 a[4:5],v250 offset:57408
v_lshlrev_b32_sdwa v46,1,v46 dst_sel:DWORD dst_unused:UNUSED_PAD src0_sel:DWORD src1_sel:WORD_1
v_mfma_f32_16x16x32_bf16 v[32:35],a[204:207],v[200:203],v[20:23]
ds_read_b64 a[6:7],v250 offset:57416
ds_read_b64 a[16:17],v251 offset:57408
v_fma_mixhi_f16 v8,v55,s99,0
v_lshlrev_b32_sdwa v45,1,v45 dst_sel:DWORD dst_unused:UNUSED_PAD src0_sel:DWORD src1_sel:WORD_1
v_mfma_f32_16x16x32_bf16 v[32:35],a[208:211],v[184:187],v[32:35]
s_nop 0
ds_read_b64 a[18:19],v251 offset:57416
ds_read_b64 a[8:9],v250 offset:57472
v_pk_fma_f16 v9,v8,v8,s54
v_mfma_f32_16x16x32_bf16 v[32:35],a[212:215],v[168:171],v[32:35]
s_waitcnt lgkmcnt(9)
ds_read_b64 a[10:11],v250 offset:57480
ds_read_b64 a[20:21],v251 offset:57472
v_or3_b32 v252,v14,v15,v252
v_lshlrev_b32_sdwa v47,1,v47 dst_sel:DWORD dst_unused:UNUSED_PAD src0_sel:DWORD src1_sel:WORD_1
v_mfma_f32_16x16x32_bf16 v[36:39],a[212:215],v[172:175],v[20:23]
ds_read_b64 v[14:15],v7 offset:512
ds_read_b128 v[20:23],v4 offset:128
v_pk_mul_f16 v9,v8,v9
v_lshlrev_b32_sdwa v48,1,v48 dst_sel:DWORD dst_unused:UNUSED_PAD src0_sel:DWORD src1_sel:WORD_1
v_mfma_f32_16x16x32_bf16 v[36:39],a[208:211],v[188:191],v[36:39]
ds_read_b64 a[22:23],v251 offset:57480
v_exp_f16 v8,v9
v_mfma_f32_16x16x32_bf16 v[36:39],a[204:207],v[204:207],v[36:39]
ds_read_b64_tr_b16 a[216:217],v1 offset:0
ds_read_b64_tr_b16 a[218:219],v1 offset:1024
v_lshlrev_b32_sdwa v50,1,v50 dst_sel:DWORD dst_unused:UNUSED_PAD src0_sel:DWORD src1_sel:WORD_1
v_mfma_f32_16x16x32_bf16 v[40:43],a[200:203],v[204:207],v[16:19]
ds_read_b128 v[16:19],v4 offset:160
ds_read_b64_tr_b16 a[220:221],v1 offset:2048
v_lshlrev_b32_sdwa v49,1,v49 dst_sel:DWORD dst_unused:UNUSED_PAD src0_sel:DWORD src1_sel:WORD_1
v_mfma_f32_16x16x32_bf16 v[40:43],a[196:199],v[188:191],v[40:43]
ds_read_b64_tr_b16 a[222:223],v1 offset:3072
ds_read_u16 v44,v44
v_lshlrev_b32_sdwa v51,1,v51 dst_sel:DWORD dst_unused:UNUSED_PAD src0_sel:DWORD src1_sel:WORD_1
v_mfma_f32_16x16x32_bf16 v[40:43],a[192:195],v[172:175],v[40:43]
s_waitcnt lgkmcnt(8)
ds_read_u16_d16_hi v46,v46
ds_read_u16 v45,v45
v_or3_b32 v252,v14,v15,v252
v_mfma_f32_16x16x32_bf16 v[136:139],a[48:51],a[24:27],v[208:211]
ds_read_b64 v[14:15],v7 offset:1024
ds_read_u16_d16_hi v47,v47
v_exp_f16_sdwa v8,v9 dst_sel:WORD_1 dst_unused:UNUSED_PRESERVE src0_sel:WORD_1
v_lshlrev_b32_sdwa v56,1,v56 dst_sel:DWORD dst_unused:UNUSED_PAD src0_sel:DWORD src1_sel:WORD_1
v_mfma_f32_16x16x32_bf16 v[140:143],a[48:51],a[36:39],v[208:211]
ds_read_b64_tr_b16 a[72:73],v1 offset:4096
ds_read_b64_tr_b16 a[74:75],v1 offset:5120
v_lshlrev_b32_sdwa v58,1,v58 dst_sel:DWORD dst_unused:UNUSED_PAD src0_sel:DWORD src1_sel:WORD_1
v_mfma_f32_16x16x32_bf16 v[68:71],a[52:55],a[36:39],v[212:215]
ds_read_u16 v48,v48
ds_read_u16_d16_hi v50,v50
v_pk_fma_f16 v8,v8,s55,s55
v_mfma_f32_16x16x32_bf16 v[64:67],a[52:55],a[24:27],v[212:215]
ds_read_u16 v49,v49
v_rcp_f16 v9,v8
v_lshlrev_b32_sdwa v57,1,v57 dst_sel:DWORD dst_unused:UNUSED_PAD src0_sel:DWORD src1_sel:WORD_1
v_mfma_f32_16x16x32_bf16 v[144:147],a[56:59],a[28:31],v[216:219]
ds_read_u16_d16_hi v51,v51
ds_read_b64_tr_b16 a[76:77],v2 offset:4096
v_lshlrev_b32_sdwa v59,1,v59 dst_sel:DWORD dst_unused:UNUSED_PAD src0_sel:DWORD src1_sel:WORD_1
v_mfma_f32_16x16x32_bf16 v[148:151],a[56:59],a[40:43],v[216:219]
s_waitcnt lgkmcnt(8)
ds_read_b64_tr_b16 a[78:79],v2 offset:5120
ds_read_b64_tr_b16 a[80:81],v2 offset:2048
v_or3_b32 v252,v14,v15,v252
v_lshlrev_b32_sdwa v52,1,v52 dst_sel:DWORD dst_unused:UNUSED_PAD src0_sel:DWORD src1_sel:WORD_1
v_mfma_f32_16x16x32_bf16 v[84:87],a[60:63],a[40:43],v[220:223]
ds_read_b64_tr_b16 a[82:83],v2 offset:3072
ds_read_u16 v56,v56
v_mfma_f32_16x16x32_bf16 v[80:83],a[60:63],a[28:31],v[220:223]
ds_read_u16_d16_hi v58,v58
v_rcp_f16_sdwa v9,v8 dst_sel:WORD_1 dst_unused:UNUSED_PRESERVE src0_sel:WORD_1
v_lshlrev_b32_sdwa v54,1,v54 dst_sel:DWORD dst_unused:UNUSED_PAD src0_sel:DWORD src1_sel:WORD_1
v_mfma_f32_16x16x32_bf16 v[152:155],a[64:67],a[32:35],v[224:227]
ds_read_u16 v57,v57
ds_read_u16_d16_hi v59,v59
v_lshlrev_b32_sdwa v60,1,v60 dst_sel:DWORD dst_unused:UNUSED_PAD src0_sel:DWORD src1_sel:WORD_1
v_mfma_f32_16x16x32_bf16 v[156:159],a[64:67],a[44:47],v[224:227]
ds_read_b64_tr_b16 a[84:85],v2 offset:0
ds_read_b64_tr_b16 a[86:87],v2 offset:1024
v_mfma_f32_16x16x32_bf16 v[100:103],a[68:71],a[44:47],v[228:231]
s_waitcnt vmcnt(10)
ds_read_u16 v52,v52
ds_read_u16_d16_hi v54,v54
v_lshlrev_b32_sdwa v62,1,v62 dst_sel:DWORD dst_unused:UNUSED_PAD src0_sel:DWORD src1_sel:WORD_1
v_fma_mix_f32 v53,v53,v9,0 op_sel_hi:[0,1,0]
v_mfma_f32_16x16x32_bf16 v[96:99],a[68:71],a[32:35],v[228:231]
s_barrier
ds_read_u16 v60,v60
ds_read_u16_d16_hi v62,v62
v_lshlrev_b32_sdwa v61,1,v61 dst_sel:DWORD dst_unused:UNUSED_PAD src0_sel:DWORD src1_sel:WORD_1
v_fma_mix_f32 v55,v55,v9,0 op_sel:[0,1,0] op_sel_hi:[0,1,0]
v_lshlrev_b32_sdwa v63,1,v63 dst_sel:DWORD dst_unused:UNUSED_PAD src0_sel:DWORD src1_sel:WORD_1
v_mfma_f32_16x16x32_bf16 v[24:27],a[216:219],v[160:163],v[20:23]
s_add_u32 m0,s57,0
ds_read_u16 v61,v61
ds_read_u16_d16_hi v63,v63
v_lshlrev_b32_sdwa v32,1,v32 dst_sel:DWORD dst_unused:UNUSED_PAD src0_sel:DWORD src1_sel:WORD_1
v_mfma_f32_16x16x32_bf16 v[24:27],a[220:223],v[176:179],v[24:27]
s_waitcnt lgkmcnt(15)
ds_read_b64_tr_b16 a[224:225],v3 offset:0
ds_read_b64_tr_b16 a[226:227],v3 offset:1536
v_lshl_add_u64 v[44:45],v[44:45],0,v[46:47]
v_mfma_f32_16x16x32_bf16 v[24:27],a[72:75],v[192:195],v[24:27]
s_waitcnt lgkmcnt(15)
ds_read_b64_tr_b16 a[228:229],v3 offset:8
ds_read_b64_tr_b16 a[230:231],v3 offset:1544
v_lshl_add_u64 v[46:47],v[48:49],0,v[50:51]
v_mfma_f32_16x16x32_bf16 v[28:31],a[76:79],v[192:195],v[16:19]
s_waitcnt lgkmcnt(15)
ds_read_b64_tr_b16 a[232:233],v3 offset:64
ds_read_b64_tr_b16 a[234:235],v3 offset:1600
v_lshlrev_b32_sdwa v34,1,v34 dst_sel:DWORD dst_unused:UNUSED_PAD src0_sel:DWORD src1_sel:WORD_1
v_mfma_f32_16x16x32_bf16 v[28:31],a[80:83],v[176:179],v[28:31]
s_waitcnt lgkmcnt(12)
ds_read_b64_tr_b16 a[236:237],v3 offset:72
ds_read_b64_tr_b16 a[238:239],v3 offset:1608
v_lshl_add_u64 v[48:49],v[56:57],0,v[58:59]
v_mfma_f32_16x16x32_bf16 v[28:31],a[84:87],v[160:163],v[28:31]
s_add_u32 s89,s56,2457600
ds_read_b64_tr_b16 a[240:241],v3 offset:128
ds_read_b64_tr_b16 a[242:243],v3 offset:1664
v_lshlrev_b32_sdwa v33,1,v33 dst_sel:DWORD dst_unused:UNUSED_PAD src0_sel:DWORD src1_sel:WORD_1
v_mfma_f32_16x16x32_bf16 v[56:59],a[84:87],v[164:167],v[16:19]
s_waitcnt lgkmcnt(14)
buffer_load_dwordx4 v6,s[36:39],s89 offen lds
ds_read_b64_tr_b16 a[244:245],v3 offset:136
v_or_b32_e64 v50,v52,v54
v_mfma_f32_16x16x32_bf16 v[56:59],a[80:83],v[180:183],v[56:59]
s_add_u32 m0,s58,18480
ds_read_b64_tr_b16 a[246:247],v3 offset:1672
v_cvt_pk_bf16_f32 v51,v53,v55
v_mfma_f32_16x16x32_bf16 v[56:59],a[76:79],v[196:199],v[56:59]
s_add_u32 s69,s69,s59
ds_read_u16 v32,v32
v_lshlrev_b32_sdwa v35,1,v35 dst_sel:DWORD dst_unused:UNUSED_PAD src0_sel:DWORD src1_sel:WORD_1
v_mfma_f32_16x16x32_bf16 v[52:55],a[72:75],v[196:199],v[20:23]
buffer_load_dwordx4 v5,s[44:47],s69 offen offset:0 lds
v_lshlrev_b32_sdwa v40,1,v40 dst_sel:DWORD dst_unused:UNUSED_PAD src0_sel:DWORD src1_sel:WORD_1
v_mfma_f32_16x16x32_bf16 v[52:55],a[220:223],v[180:183],v[52:55]
s_add_u32 s69,s69,s53
ds_read_u16_d16_hi v34,v34
v_lshlrev_b32_sdwa v42,1,v42 dst_sel:DWORD dst_unused:UNUSED_PAD src0_sel:DWORD src1_sel:WORD_1
v_mfma_f32_16x16x32_bf16 v[52:55],a[216:219],v[164:167],v[52:55]
s_waitcnt lgkmcnt(10)
buffer_load_dwordx4 v5,s[44:47],s69 offen offset:1024 lds
ds_read_u16 v33,v33
v_mfma_f32_16x16x32_bf16 v[136:139],a[224:227],v[44:47],v[136:139]
s_add_u32 s69,s69,s53
ds_read_u16_d16_hi v35,v35
ds_read_u16 v40,v40
v_mfma_f32_16x16x32_bf16 v[140:143],a[224:227],v[48:51],v[140:143]
buffer_load_dwordx4 v5,s[44:47],s69 offen offset:2048 lds
v_lshlrev_b32_sdwa v41,1,v41 dst_sel:DWORD dst_unused:UNUSED_PAD src0_sel:DWORD src1_sel:WORD_1
v_mfma_f32_16x16x32_bf16 v[68:71],a[228:231],v[48:51],v[68:71]
ds_read_u16_d16_hi v42,v42
ds_read_u16 v41,v41
v_mfma_f32_16x16x32_bf16 v[64:67],a[228:231],v[44:47],v[64:67]
s_waitcnt lgkmcnt(8)
ds_read_b64_tr_b16 a[248:249],v3 offset:6160
v_lshlrev_b32_sdwa v43,1,v43 dst_sel:DWORD dst_unused:UNUSED_PAD src0_sel:DWORD src1_sel:WORD_1
v_mfma_f32_16x16x32_bf16 v[144:147],a[232:235],v[44:47],v[144:147]
s_add_u32 m0,s57,3544
ds_read_u16_d16_hi v43,v43
ds_read_b64_tr_b16 a[250:251],v3 offset:7696
v_mfma_f32_16x16x32_bf16 v[148:151],a[232:235],v[48:51],v[148:151]
s_add_u32 s89,s56,2555904
ds_read_b64_tr_b16 a[252:253],v3 offset:6168
v_lshlrev_b32_sdwa v36,1,v36 dst_sel:DWORD dst_unused:UNUSED_PAD src0_sel:DWORD src1_sel:WORD_1
v_mfma_f32_16x16x32_bf16 v[84:87],a[236:239],v[48:51],v[84:87]
buffer_load_dwordx4 v6,s[36:39],s89 offen lds
ds_read_u16 v36,v36
v_mfma_f32_16x16x32_bf16 v[80:83],a[236:239],v[44:47],v[80:83]
ds_read_b64_tr_b16 a[254:255],v3 offset:7704
v_lshlrev_b32_sdwa v38,1,v38 dst_sel:DWORD dst_unused:UNUSED_PAD src0_sel:DWORD src1_sel:WORD_1
v_mfma_f32_16x16x32_bf16 v[152:155],a[240:243],v[44:47],v[152:155]
ds_read_u16_d16_hi v38,v38
v_mfma_f32_16x16x32_bf16 v[156:159],a[240:243],v[48:51],v[156:159]
s_waitcnt lgkmcnt(9)
ds_read_b64_tr_b16 a[92:93],v3 offset:6224
v_lshlrev_b32_sdwa v37,1,v37 dst_sel:DWORD dst_unused:UNUSED_PAD src0_sel:DWORD src1_sel:WORD_1
v_mfma_f32_16x16x32_bf16 v[100:103],a[244:247],v[48:51],v[100:103]
ds_read_u16 v37,v37
v_mfma_f32_16x16x32_bf16 v[96:99],a[244:247],v[44:47],v[96:99]
ds_read_b64_tr_b16 a[94:95],v3 offset:7760
v_lshlrev_b32_sdwa v39,1,v39 dst_sel:DWORD dst_unused:UNUSED_PAD src0_sel:DWORD src1_sel:WORD_1
v_mfma_f32_16x16x32_bf16 v[112:115],a[48:51],a[0:3],v[208:211]
ds_read_u16_d16_hi v39,v39
v_mfma_f32_16x16x32_bf16 v[116:119],a[48:51],a[12:15],v[208:211]
ds_read_b64_tr_b16 a[48:49],v3 offset:6232
v_mfma_f32_16x16x32_bf16 v[76:79],a[52:55],a[12:15],v[212:215]
ds_read_b64_tr_b16 a[50:51],v3 offset:7768
v_lshlrev_b32_sdwa v24,1,v24 dst_sel:DWORD dst_unused:UNUSED_PAD src0_sel:DWORD src1_sel:WORD_1
v_mfma_f32_16x16x32_bf16 v[72:75],a[52:55],a[0:3],v[212:215]
ds_read_u16 v24,v24
v_mfma_f32_16x16x32_bf16 v[120:123],a[56:59],a[4:7],v[216:219]
v_lshlrev_b32_sdwa v26,1,v26 dst_sel:DWORD dst_unused:UNUSED_PAD src0_sel:DWORD src1_sel:WORD_1
v_mfma_f32_16x16x32_bf16 v[124:127],a[56:59],a[16:19],v[216:219]
ds_read_u16_d16_hi v26,v26
v_mfma_f32_16x16x32_bf16 v[92:95],a[60:63],a[16:19],v[220:223]
ds_read_b64_tr_b16 a[56:57],v3 offset:6288
v_lshlrev_b32_sdwa v25,1,v25 dst_sel:DWORD dst_unused:UNUSED_PAD src0_sel:DWORD src1_sel:WORD_1
v_mfma_f32_16x16x32_bf16 v[88:91],a[60:63],a[4:7],v[220:223]
ds_read_u16 v25,v25
v_mfma_f32_16x16x32_bf16 v[128:131],a[64:67],a[8:11],v[224:227]
ds_read_b64_tr_b16 a[58:59],v3 offset:7824
v_mfma_f32_16x16x32_bf16 v[132:135],a[64:67],a[20:23],v[224:227]
ds_read_b64_tr_b16 a[64:65],v3 offset:6296
v_lshlrev_b32_sdwa v27,1,v27 dst_sel:DWORD dst_unused:UNUSED_PAD src0_sel:DWORD src1_sel:WORD_1
v_mfma_f32_16x16x32_bf16 v[108:111],a[68:71],a[20:23],v[228:231]
s_add_u32 m0,s57,7080
ds_read_u16_d16_hi v27,v27
v_mfma_f32_16x16x32_bf16 v[104:107],a[68:71],a[8:11],v[228:231]
s_add_u32 s89,s56,2654208
ds_read_b64_tr_b16 a[66:67],v3 offset:7832
v_mfma_f32_16x16x32_bf16 v[48:51],a[216:219],v[168:171],v[20:23]
buffer_load_dwordx4 v6,s[36:39],s89 offen lds
ds_read_b64 v[14:15],v7 offset:6160
v_lshlrev_b32_sdwa v28,1,v28 dst_sel:DWORD dst_unused:UNUSED_PAD src0_sel:DWORD src1_sel:WORD_1
v_mfma_f32_16x16x32_bf16 v[48:51],a[220:223],v[184:187],v[48:51]
ds_read_b64_tr_b16 a[212:213],v1 offset:6160
ds_read_b64_tr_b16 a[214:215],v1 offset:7184
v_lshl_add_u64 v[60:61],v[60:61],0,v[62:63]
v_fma_mixlo_f16 v8,v57,s99,0
v_mfma_f32_16x16x32_bf16 v[48:51],a[72:75],v[200:203],v[48:51]
ds_read_b64_tr_b16 a[208:209],v1 offset:8208
ds_read_b64_tr_b16 a[210:211],v1 offset:9232
v_lshl_add_u64 v[62:63],v[32:33],0,v[34:35]
v_lshlrev_b32_sdwa v30,1,v30 dst_sel:DWORD dst_unused:UNUSED_PAD src0_sel:DWORD src1_sel:WORD_1
v_mfma_f32_16x16x32_bf16 v[44:47],a[76:79],v[200:203],v[16:19]
s_nop 0
ds_read_b64_tr_b16 a[204:205],v1 offset:10256
ds_read_b64_tr_b16 a[206:207],v1 offset:11280
v_fma_mixhi_f16 v8,v59,s99,0
v_mfma_f32_16x16x32_bf16 v[44:47],a[80:83],v[184:187],v[44:47]
s_waitcnt lgkmcnt(11)
ds_read_u16 v28,v28
ds_read_u16_d16_hi v30,v30
v_lshl_add_u64 v[32:33],v[40:41],0,v[42:43]
v_lshlrev_b32_sdwa v29,1,v29 dst_sel:DWORD dst_unused:UNUSED_PAD src0_sel:DWORD src1_sel:WORD_1
v_pk_fma_f16 v9,v8,v8,s54
v_mfma_f32_16x16x32_bf16 v[44:47],a[84:87],v[168:171],v[44:47]
s_waitcnt lgkmcnt(8)
ds_read_u16 v29,v29
ds_read_b64_tr_b16 a[200:201],v2 offset:10256
v_or3_b32 v252,v14,v15,v252
v_mfma_f32_16x16x32_bf16 v[40:43],a[84:87],v[172:175],v[16:19]
s_nop 0
ds_read_b64 v[14:15],v7 offset:6672
ds_read_b128 v[16:19],v4 offset:256
v_pk_mul_f16 v9,v8,v9
v_lshlrev_b32_sdwa v31,1,v31 dst_sel:DWORD dst_unused:UNUSED_PAD src0_sel:DWORD src1_sel:WORD_1
v_mfma_f32_16x16x32_bf16 v[40:43],a[80:83],v[188:191],v[40:43]
ds_read_u16_d16_hi v31,v31
ds_read_b64_tr_b16 a[202:203],v2 offset:11280
v_lshl_add_u64 v[34:35],v[36:37],0,v[38:39]
v_mfma_f32_16x16x32_bf16 v[40:43],a[76:79],v[204:207],v[40:43]
ds_read_b64_tr_b16 a[196:197],v2 offset:8208
v_exp_f16 v8,v9
v_lshlrev_b32_sdwa v52,1,v52 dst_sel:DWORD dst_unused:UNUSED_PAD src0_sel:DWORD src1_sel:WORD_1
v_mfma_f32_16x16x32_bf16 v[36:39],a[72:75],v[204:207],v[20:23]
ds_read_b128 v[20:23],v4 offset:288
ds_read_b64_tr_b16 a[198:199],v2 offset:9232
v_mfma_f32_16x16x32_bf16 v[36:39],a[220:223],v[188:191],v[36:39]
ds_read_u16 v52,v52
v_lshlrev_b32_sdwa v54,1,v54 dst_sel:DWORD dst_unused:UNUSED_PAD src0_sel:DWORD src1_sel:WORD_1
v_mfma_f32_16x16x32_bf16 v[36:39],a[216:219],v[172:175],v[36:39]
s_waitcnt lgkmcnt(7)
ds_read_u16_d16_hi v54,v54
v_or3_b32 v252,v14,v15,v252
v_mfma_f32_16x16x32_bf16 v[112:115],a[224:227],v[60:63],v[112:115]
s_add_u32 m0,s57,10608
ds_read_b64 v[14:15],v7 offset:7184
v_exp_f16_sdwa v8,v9 dst_sel:WORD_1 dst_unused:UNUSED_PRESERVE src0_sel:WORD_1
v_lshlrev_b32_sdwa v53,1,v53 dst_sel:DWORD dst_unused:UNUSED_PAD src0_sel:DWORD src1_sel:WORD_1
v_mfma_f32_16x16x32_bf16 v[116:119],a[224:227],v[32:35],v[116:119]
s_add_u32 s89,s56,2752512
ds_read_u16 v53,v53
v_lshlrev_b32_sdwa v55,1,v55 dst_sel:DWORD dst_unused:UNUSED_PAD src0_sel:DWORD src1_sel:WORD_1
v_mfma_f32_16x16x32_bf16 v[76:79],a[228:231],v[32:35],v[76:79]
buffer_load_dwordx4 v6,s[36:39],s89 offen lds
v_pk_fma_f16 v8,v8,s55,s55
v_mfma_f32_16x16x32_bf16 v[72:75],a[228:231],v[60:63],v[72:75]
v_rcp_f16_e64 v9,v8
v_lshlrev_b32_sdwa v56,1,v56 dst_sel:DWORD dst_unused:UNUSED_PAD src0_sel:DWORD src1_sel:WORD_1
v_mfma_f32_16x16x32_bf16 v[120:123],a[232:235],v[60:63],v[120:123]
ds_read_u16_d16_hi v55,v55
v_lshlrev_b32_sdwa v58,1,v58 dst_sel:DWORD dst_unused:UNUSED_PAD src0_sel:DWORD src1_sel:WORD_1
v_mfma_f32_16x16x32_bf16 v[124:127],a[232:235],v[32:35],v[124:127]
s_waitcnt lgkmcnt(2)
ds_read_b64_tr_b16 a[192:193],v2 offset:6160
v_or3_b32 v252,v14,v15,v252
v_mfma_f32_16x16x32_bf16 v[92:95],a[236:239],v[32:35],v[92:95]
ds_read_b64_tr_b16 a[194:195],v2 offset:7184
v_lshlrev_b32_sdwa v48,1,v48 dst_sel:DWORD dst_unused:UNUSED_PAD src0_sel:DWORD src1_sel:WORD_1
v_mfma_f32_16x16x32_bf16 v[88:91],a[236:239],v[60:63],v[88:91]
v_rcp_f16_sdwa v9,v8 dst_sel:WORD_1 dst_unused:UNUSED_PRESERVE src0_sel:WORD_1
v_mfma_f32_16x16x32_bf16 v[128:131],a[240:243],v[60:63],v[128:131]
ds_read_u16 v56,v56
v_lshlrev_b32_sdwa v50,1,v50 dst_sel:DWORD dst_unused:UNUSED_PAD src0_sel:DWORD src1_sel:WORD_1
v_mfma_f32_16x16x32_bf16 v[132:135],a[240:243],v[32:35],v[132:135]
ds_read_u16_d16_hi v58,v58
v_fma_mix_f32 v57,v57,v9,0 op_sel_hi:[0,1,0]
v_mfma_f32_16x16x32_bf16 v[108:111],a[244:247],v[32:35],v[108:111]
ds_read_u16 v48,v48
v_lshlrev_b32_sdwa v49,1,v49 dst_sel:DWORD dst_unused:UNUSED_PAD src0_sel:DWORD src1_sel:WORD_1
v_mfma_f32_16x16x32_bf16 v[104:107],a[244:247],v[60:63],v[104:107]
ds_read_u16_d16_hi v50,v50
v_fma_mix_f32 v59,v59,v9,0 op_sel:[0,1,0] op_sel_hi:[0,1,0]
v_lshlrev_b32_sdwa v51,1,v51 dst_sel:DWORD dst_unused:UNUSED_PAD src0_sel:DWORD src1_sel:WORD_1
v_mfma_f32_16x16x32_bf16 v[32:35],a[212:215],v[160:163],v[16:19]
ds_read_u16 v49,v49
v_lshlrev_b32_sdwa v44,1,v44 dst_sel:DWORD dst_unused:UNUSED_PAD src0_sel:DWORD src1_sel:WORD_1
v_mfma_f32_16x16x32_bf16 v[32:35],a[208:211],v[176:179],v[32:35]
ds_read_u16_d16_hi v51,v51
v_lshl_add_u64 v[24:25],v[24:25],0,v[26:27]
v_mfma_f32_32x32x16_bf16 v[208:223],a[88:91],a[88:91],0
v_lshlrev_b32_sdwa v46,1,v46 dst_sel:DWORD dst_unused:UNUSED_PAD src0_sel:DWORD src1_sel:WORD_1
v_lshlrev_b32_sdwa v45,1,v45 dst_sel:DWORD dst_unused:UNUSED_PAD src0_sel:DWORD src1_sel:WORD_1
v_mfma_f32_16x16x32_bf16 v[32:35],a[204:207],v[192:195],v[32:35]
s_add_u32 m0,s57,14144
ds_read_u16 v44,v44
v_lshl_add_u64 v[26:27],v[28:29],0,v[30:31]
v_mfma_f32_16x16x32_bf16 v[60:63],a[200:203],v[192:195],v[20:23]
s_add_u32 s89,s56,2850816
ds_read_u16_d16_hi v46,v46
v_lshlrev_b32_sdwa v47,1,v47 dst_sel:DWORD dst_unused:UNUSED_PAD src0_sel:DWORD src1_sel:WORD_1
v_mfma_f32_16x16x32_bf16 v[60:63],a[196:199],v[176:179],v[60:63]
s_waitcnt lgkmcnt(8)
buffer_load_dwordx4 v6,s[36:39],s89 offen lds
v_lshl_add_u64 v[28:29],v[52:53],0,v[54:55]
v_mfma_f32_32x32x16_bf16 v[224:239],a[88:91],a[88:91],0
v_accvgpr_write_b32 a0,v160
v_accvgpr_write_b32 a1,v161
v_lshlrev_b32_sdwa v36,1,v36 dst_sel:DWORD dst_unused:UNUSED_PAD src0_sel:DWORD src1_sel:WORD_1
v_accvgpr_write_b32 a2,v162
v_mfma_f32_16x16x32_bf16 v[60:63],a[192:195],v[160:163],v[60:63]
ds_read_u16 v45,v45
v_mfma_f32_16x16x32_bf16 v[52:55],a[192:195],v[164:167],v[20:23]
s_waitcnt lgkmcnt(7)
ds_read_u16_d16_hi v47,v47
v_or_b32_e64 v30,v56,v58
v_mfma_f32_16x16x32_bf16 v[52:55],a[196:199],v[180:183],v[52:55]
s_waitcnt vmcnt(15)
ds_read_u16 v36,v36
v_cvt_pk_bf16_f32 v31,v57,v59
v_mfma_f32_16x16x32_bf16 v[52:55],a[200:203],v[196:199],v[52:55]
s_barrier
v_lshlrev_b32_sdwa v38,1,v38 dst_sel:DWORD dst_unused:UNUSED_PAD src0_sel:DWORD src1_sel:WORD_1
v_accvgpr_write_b32 a3,v163
v_mfma_f32_16x16x32_bf16 v[56:59],a[204:207],v[196:199],v[16:19]
ds_read_u16_d16_hi v38,v38
v_lshlrev_b32_sdwa v37,1,v37 dst_sel:DWORD dst_unused:UNUSED_PAD src0_sel:DWORD src1_sel:WORD_1
v_mfma_f32_16x16x32_bf16 v[56:59],a[208:211],v[180:183],v[56:59]
ds_read_u16 v37,v37
v_accvgpr_write_b32 a4,v164
v_mfma_f32_16x16x32_bf16 v[56:59],a[212:215],v[164:167],v[56:59]
s_add_u32 m0,s58,0
ds_read_b64_tr_b16 a[224:225],v3 offset:12320
v_lshlrev_b32_sdwa v39,1,v39 dst_sel:DWORD dst_unused:UNUSED_PAD src0_sel:DWORD src1_sel:WORD_1
v_mfma_f32_16x16x32_bf16 v[136:139],a[248:251],v[24:27],v[136:139]
s_add_u32 s69,s69,s59
ds_read_u16_d16_hi v39,v39
v_accvgpr_write_b32 a5,v165
v_mfma_f32_16x16x32_bf16 v[140:143],a[248:251],v[28:31],v[140:143]
buffer_load_dwordx4 v5,s[44:47],s69 offen offset:0 lds
v_lshlrev_b32_sdwa v40,1,v40 dst_sel:DWORD dst_unused:UNUSED_PAD src0_sel:DWORD src1_sel:WORD_1
v_mfma_f32_16x16x32_bf16 v[68:71],a[252:255],v[28:31],v[68:71]
ds_read_u16 v40,v40
v_accvgpr_write_b32 a6,v166
v_mfma_f32_16x16x32_bf16 v[64:67],a[252:255],v[24:27],v[64:67]
ds_read_b64_tr_b16 a[226:227],v3 offset:13856
v_lshlrev_b32_sdwa v42,1,v42 dst_sel:DWORD dst_unused:UNUSED_PAD src0_sel:DWORD src1_sel:WORD_1
v_mfma_f32_16x16x32_bf16 v[144:147],a[92:95],v[24:27],v[144:147]
s_add_u32 s69,s69,s53
ds_read_u16_d16_hi v42,v42
ds_read_b64_tr_b16 a[228:229],v3 offset:12328
v_lshlrev_b32_sdwa v41,1,v41 dst_sel:DWORD dst_unused:UNUSED_PAD src0_sel:DWORD src1_sel:WORD_1
v_mfma_f32_16x16x32_bf16 v[148:151],a[92:95],v[28:31],v[148:151]
buffer_load_dwordx4 v5,s[44:47],s69 offen offset:1024 lds
ds_read_u16 v41,v41
v_accvgpr_write_b32 a7,v167
v_mfma_f32_16x16x32_bf16 v[84:87],a[48:51],v[28:31],v[84:87]
s_add_u32 m0,s57,17688
ds_read_b64_tr_b16 a[230:231],v3 offset:13864
ds_read_b64_tr_b16 a[232:233],v3 offset:12384
v_lshlrev_b32_sdwa v43,1,v43 dst_sel:DWORD dst_unused:UNUSED_PAD src0_sel:DWORD src1_sel:WORD_1
v_accvgpr_write_b32 a8,v168
v_mfma_f32_16x16x32_bf16 v[80:83],a[48:51],v[24:27],v[80:83]
s_add_u32 s89,s56,2949120
ds_read_u16_d16_hi v43,v43
v_lshlrev_b32_sdwa v32,1,v32 dst_sel:DWORD dst_unused:UNUSED_PAD src0_sel:DWORD src1_sel:WORD_1
v_mfma_f32_16x16x32_bf16 v[152:155],a[56:59],v[24:27],v[152:155]
buffer_load_dwordx4 v6,s[36:39],s89 offen lds
ds_read_u16 v32,v32
v_accvgpr_write_b32 a9,v169
v_mfma_f32_16x16x32_bf16 v[156:159],a[56:59],v[28:31],v[156:159]
s_add_u32 m0,s58,0
ds_read_b64_tr_b16 a[234:235],v3 offset:13920
ds_read_b64_tr_b16 a[236:237],v3 offset:12392
v_lshlrev_b32_sdwa v34,1,v34 dst_sel:DWORD dst_unused:UNUSED_PAD src0_sel:DWORD src1_sel:WORD_1
v_mfma_f32_16x16x32_bf16 v[100:103],a[64:67],v[28:31],v[100:103]
s_add_u32 s69,s69,s53
ds_read_u16_d16_hi v34,v34
ds_read_b64_tr_b16 a[238:239],v3 offset:13928
v_accvgpr_write_b32 a10,v170
v_mfma_f32_16x16x32_bf16 v[96:99],a[64:67],v[24:27],v[96:99]
buffer_load_dwordx4 v5,s[44:47],s69 offen offset:2048 lds
ds_read_b64_tr_b16 a[240:241],v3 offset:12448
v_lshlrev_b32_sdwa v33,1,v33 dst_sel:DWORD dst_unused:UNUSED_PAD src0_sel:DWORD src1_sel:WORD_1
v_accvgpr_write_b32 a11,v171
v_lshlrev_b32_sdwa v35,1,v35 dst_sel:DWORD dst_unused:UNUSED_PAD src0_sel:DWORD src1_sel:WORD_1
s_nop 0
v_mfma_f32_16x16x32_bf16 v[28:31],a[212:215],a[8:11],v[16:19]
s_add_u32 m0,s58,6160
ds_read_b64 v[14:15],v7 offset:12320
ds_read_u16 v33,v33
v_lshlrev_b32_sdwa v60,1,v60 dst_sel:DWORD dst_unused:UNUSED_PAD src0_sel:DWORD src1_sel:WORD_1
v_mfma_f32_16x16x32_bf16 v[28:31],a[208:211],v[184:187],v[28:31]
s_waitcnt lgkmcnt(12)
ds_read_u16_d16_hi v35,v35
ds_read_b64_tr_b16 a[52:53],v1 offset:12320
v_lshl_add_u64 v[48:49],v[48:49],0,v[50:51]
v_fma_mixlo_f16 v8,v53,s99,0
v_mfma_f32_16x16x32_bf16 v[28:31],a[204:207],v[200:203],v[28:31]
s_add_u32 s69,s69,s59
ds_read_b64_tr_b16 a[54:55],v1 offset:13344
ds_read_b64_tr_b16 a[60:61],v1 offset:14368
v_lshl_add_u64 v[50:51],v[44:45],0,v[46:47]
v_lshlrev_b32_sdwa v62,1,v62 dst_sel:DWORD dst_unused:UNUSED_PAD src0_sel:DWORD src1_sel:WORD_1
v_mfma_f32_16x16x32_bf16 v[24:27],a[200:203],v[200:203],v[20:23]
buffer_load_dwordx4 v5,s[44:47],s69 offen offset:0 lds
ds_read_b64_tr_b16 a[62:63],v1 offset:15392
v_fma_mixhi_f16 v8,v55,s99,0
v_mfma_f32_16x16x32_bf16 v[24:27],a[196:199],v[184:187],v[24:27]
s_add_u32 s69,s69,s53
ds_read_b64_tr_b16 a[68:69],v1 offset:16416
ds_read_b64_tr_b16 a[70:71],v1 offset:17440
v_lshl_add_u64 v[44:45],v[36:37],0,v[38:39]
v_lshlrev_b32_sdwa v61,1,v61 dst_sel:DWORD dst_unused:UNUSED_PAD src0_sel:DWORD src1_sel:WORD_1
v_pk_fma_f16 v9,v8,v8,s54
v_mfma_f32_16x16x32_bf16 v[24:27],a[192:195],a[8:11],v[24:27]
s_waitcnt lgkmcnt(8)
buffer_load_dwordx4 v5,s[44:47],s69 offen offset:1024 lds
ds_read_u16 v60,v60
v_or3_b32 v252,v14,v15,v252
v_accvgpr_write_b32 a12,v172
v_mfma_f32_16x16x32_bf16 v[36:39],a[192:195],v[172:175],v[20:23]
s_add_u32 m0,s57,21224
ds_read_b64 v[14:15],v7 offset:12832
ds_read_b128 v[20:23],v4 offset:384
v_pk_mul_f16 v9,v8,v9
v_lshlrev_b32_sdwa v63,1,v63 dst_sel:DWORD dst_unused:UNUSED_PAD src0_sel:DWORD src1_sel:WORD_1
v_mfma_f32_16x16x32_bf16 v[36:39],a[196:199],v[188:191],v[36:39]
s_add_u32 s89,s56,3047424
ds_read_u16_d16_hi v62,v62
ds_read_u16 v61,v61
v_lshl_add_u64 v[46:47],v[40:41],0,v[42:43]
v_mfma_f32_16x16x32_bf16 v[36:39],a[200:203],v[204:207],v[36:39]
ds_read_u16_d16_hi v63,v63
v_exp_f16 v8,v9
v_lshlrev_b32_sdwa v56,1,v56 dst_sel:DWORD dst_unused:UNUSED_PAD src0_sel:DWORD src1_sel:WORD_1
v_mfma_f32_16x16x32_bf16 v[40:43],a[204:207],v[204:207],v[16:19]
buffer_load_dwordx4 v6,s[36:39],s89 offen lds
ds_read_b128 v[16:19],v4 offset:416
v_mfma_f32_16x16x32_bf16 v[40:43],a[208:211],v[188:191],v[40:43]
s_add_u32 m0,s58,6160
ds_read_b64_tr_b16 a[84:85],v2 offset:16416
ds_read_b64_tr_b16 a[86:87],v2 offset:17440
v_lshlrev_b32_sdwa v58,1,v58 dst_sel:DWORD dst_unused:UNUSED_PAD src0_sel:DWORD src1_sel:WORD_1
v_mfma_f32_16x16x32_bf16 v[40:43],a[212:215],v[172:175],v[40:43]
s_waitcnt lgkmcnt(7)
ds_read_b64_tr_b16 a[80:81],v2 offset:14368
ds_read_b64_tr_b16 a[82:83],v2 offset:15392
v_or3_b32 v252,v14,v15,v252
v_accvgpr_write_b32 a13,v173
v_mfma_f32_16x16x32_bf16 v[112:115],a[248:251],v[48:51],v[112:115]
s_add_u32 s69,s69,s53
ds_read_b64 v[14:15],v7 offset:13344
ds_read_u16 v56,v56
v_exp_f16_sdwa v8,v9 dst_sel:WORD_1 dst_unused:UNUSED_PRESERVE src0_sel:WORD_1
v_lshlrev_b32_sdwa v57,1,v57 dst_sel:DWORD dst_unused:UNUSED_PAD src0_sel:DWORD src1_sel:WORD_1
v_mfma_f32_16x16x32_bf16 v[116:119],a[248:251],v[44:47],v[116:119]
buffer_load_dwordx4 v5,s[44:47],s69 offen offset:2048 lds
ds_read_u16_d16_hi v58,v58
v_lshlrev_b32_sdwa v59,1,v59 dst_sel:DWORD dst_unused:UNUSED_PAD src0_sel:DWORD src1_sel:WORD_1
v_mfma_f32_16x16x32_bf16 v[76:79],a[252:255],v[44:47],v[76:79]
ds_read_u16 v57,v57
ds_read_u16_d16_hi v59,v59
v_pk_fma_f16 v8,v8,s55,s55
v_mfma_f32_16x16x32_bf16 v[72:75],a[252:255],v[48:51],v[72:75]
ds_read_b64_tr_b16 a[76:77],v2 offset:12320
v_rcp_f16 v9,v8
v_lshlrev_b32_sdwa v52,1,v52 dst_sel:DWORD dst_unused:UNUSED_PAD src0_sel:DWORD src1_sel:WORD_1
v_mfma_f32_16x16x32_bf16 v[120:123],a[92:95],v[48:51],v[120:123]
ds_read_b64_tr_b16 a[78:79],v2 offset:13344
ds_read_u16 v52,v52
v_mfma_f32_16x16x32_bf16 v[124:127],a[92:95],v[44:47],v[124:127]
s_waitcnt lgkmcnt(7)
TE02_%=:
s_cbranch_execz TP3807_%=
ds_read_b64_tr_b16 a[242:243],v3 offset:13984
v_or3_b32 v252,v14,v15,v252
v_lshlrev_b32_sdwa v54,1,v54 dst_sel:DWORD dst_unused:UNUSED_PAD src0_sel:DWORD src1_sel:WORD_1
v_accvgpr_write_b32 a14,v174
v_mfma_f32_16x16x32_bf16 v[92:95],a[48:51],v[44:47],v[92:95]
s_add_u32 m0,s57,24752
ds_read_u16_d16_hi v54,v54
v_lshlrev_b32_sdwa v28,1,v28 dst_sel:DWORD dst_unused:UNUSED_PAD src0_sel:DWORD src1_sel:WORD_1
v_mfma_f32_16x16x32_bf16 v[88:91],a[48:51],v[48:51],v[88:91]
s_add_u32 s89,s56,3145728
v_rcp_f16_sdwa v9,v8 dst_sel:WORD_1 dst_unused:UNUSED_PRESERVE src0_sel:WORD_1
v_mfma_f32_16x16x32_bf16 v[128:131],a[56:59],v[48:51],v[128:131]
s_nop 0
buffer_load_dwordx4 v6,s[36:39],s89 offen lds
v_lshlrev_b32_sdwa v30,1,v30 dst_sel:DWORD dst_unused:UNUSED_PAD src0_sel:DWORD src1_sel:WORD_1
v_mfma_f32_16x16x32_bf16 v[132:135],a[56:59],v[44:47],v[132:135]
ds_read_u16 v28,v28
v_fma_mix_f32 v53,v53,v9,0 op_sel_hi:[0,1,0]
v_mfma_f32_16x16x32_bf16 v[108:111],a[64:67],v[44:47],v[108:111]
ds_read_u16_d16_hi v30,v30
v_lshlrev_b32_sdwa v29,1,v29 dst_sel:DWORD dst_unused:UNUSED_PAD src0_sel:DWORD src1_sel:WORD_1
v_mfma_f32_16x16x32_bf16 v[104:107],a[64:67],v[48:51],v[104:107]
ds_read_u16 v29,v29
v_accvgpr_write_b32 a15,v175
v_fma_mix_f32 v55,v55,v9,0 op_sel:[0,1,0] op_sel_hi:[0,1,0]
v_lshlrev_b32_sdwa v31,1,v31 dst_sel:DWORD dst_unused:UNUSED_PAD src0_sel:DWORD src1_sel:WORD_1
v_mfma_f32_16x16x32_bf16 v[44:47],a[52:55],a[0:3],v[20:23]
ds_read_u16_d16_hi v31,v31
v_lshlrev_b32_sdwa v24,1,v24 dst_sel:DWORD dst_unused:UNUSED_PAD src0_sel:DWORD src1_sel:WORD_1
v_mfma_f32_16x16x32_bf16 v[44:47],a[60:63],v[176:179],v[44:47]
ds_read_b64_tr_b16 a[244:245],v3 offset:12456
v_lshl_add_u64 v[32:33],v[32:33],0,v[34:35]
v_mfma_f32_32x32x16_bf16 v[160:175],a[88:91],a[88:91],0
v_lshlrev_b32_sdwa v26,1,v26 dst_sel:DWORD dst_unused:UNUSED_PAD src0_sel:DWORD src1_sel:WORD_1
v_accvgpr_write_b32 a16,v176
v_lshlrev_b32_sdwa v25,1,v25 dst_sel:DWORD dst_unused:UNUSED_PAD src0_sel:DWORD src1_sel:WORD_1
v_accvgpr_write_b32 a17,v177
v_mfma_f32_16x16x32_bf16 v[44:47],a[68:71],v[192:195],v[44:47]
ds_read_b64_tr_b16 a[246:247],v3 offset:13992
v_lshl_add_u64 v[34:35],v[60:61],0,v[62:63]
v_mfma_f32_16x16x32_bf16 v[48:51],a[84:87],v[192:195],v[16:19]
ds_read_u16 v24,v24
v_lshlrev_b32_sdwa v27,1,v27 dst_sel:DWORD dst_unused:UNUSED_PAD src0_sel:DWORD src1_sel:WORD_1
v_mfma_f32_16x16x32_bf16 v[48:51],a[80:83],v[176:179],v[48:51]
s_waitcnt lgkmcnt(7)
ds_read_u16_d16_hi v26,v26
v_lshl_add_u64 v[60:61],v[56:57],0,v[58:59]
v_mfma_f32_16x16x32_bf16 v[48:51],a[76:79],a[0:3],v[48:51]
ds_read_u16 v25,v25
v_accvgpr_write_b32 a18,v178
v_mfma_f32_16x16x32_bf16 v[56:59],a[76:79],a[4:7],v[16:19]
ds_read_u16_d16_hi v27,v27
v_or_b32 v62,v52,v54
v_mfma_f32_16x16x32_bf16 v[56:59],a[80:83],v[180:183],v[56:59]
s_waitcnt vmcnt(13)
v_cvt_pk_bf16_f32 v63,v53,v55
v_lshlrev_b32_sdwa v40,1,v40 dst_sel:DWORD dst_unused:UNUSED_PAD src0_sel:DWORD src1_sel:WORD_1
v_mfma_f32_16x16x32_bf16 v[56:59],a[84:87],v[196:199],v[56:59]
s_barrier
ds_read_u16 v40,v40
v_accvgpr_write_b32 a19,v179
v_lshlrev_b32_sdwa v42,1,v42 dst_sel:DWORD dst_unused:UNUSED_PAD src0_sel:DWORD src1_sel:WORD_1
v_mfma_f32_16x16x32_bf16 v[52:55],a[68:71],v[196:199],v[20:23]
s_add_u32 m0,s57,28288
ds_read_u16_d16_hi v42,v42
v_accvgpr_write_b32 a20,v180
v_mfma_f32_16x16x32_bf16 v[52:55],a[60:63],v[180:183],v[52:55]
s_add_u32 s89,s56,3244032
ds_read_b64_tr_b16 a[92:93],v3 offset:18480
v_lshlrev_b32_sdwa v41,1,v41 dst_sel:DWORD dst_unused:UNUSED_PAD src0_sel:DWORD src1_sel:WORD_1
v_mfma_f32_16x16x32_bf16 v[52:55],a[52:55],a[4:7],v[52:55]
buffer_load_dwordx4 v6,s[36:39],s89 offen lds
v_accvgpr_write_b32 a21,v181
v_mfma_f32_16x16x32_bf16 v[136:139],a[224:227],v[32:35],v[136:139]
ds_read_u16 v41,v41
v_lshlrev_b32_sdwa v43,1,v43 dst_sel:DWORD dst_unused:UNUSED_PAD src0_sel:DWORD src1_sel:WORD_1
v_mfma_f32_16x16x32_bf16 v[140:143],a[224:227],v[60:63],v[140:143]
ds_read_u16_d16_hi v43,v43
v_accvgpr_write_b32 a22,v182
v_lshlrev_b32_sdwa v36,1,v36 dst_sel:DWORD dst_unused:UNUSED_PAD src0_sel:DWORD src1_sel:WORD_1
v_mfma_f32_16x16x32_bf16 v[68:71],a[228:231],v[60:63],v[68:71]
ds_read_u16 v36,v36
v_accvgpr_write_b32 a23,v183
v_mfma_f32_16x16x32_bf16 v[64:67],a[228:231],v[32:35],v[64:67]
ds_read_b64_tr_b16 a[94:95],v3 offset:20016
v_lshlrev_b32_sdwa v38,1,v38 dst_sel:DWORD dst_unused:UNUSED_PAD src0_sel:DWORD src1_sel:WORD_1
v_mfma_f32_16x16x32_bf16 v[144:147],a[232:235],v[32:35],v[144:147]
ds_read_u16_d16_hi v38,v38
ds_read_b64_tr_b16 a[48:49],v3 offset:18488
v_lshlrev_b32_sdwa v37,1,v37 dst_sel:DWORD dst_unused:UNUSED_PAD src0_sel:DWORD src1_sel:WORD_1
v_mfma_f32_16x16x32_bf16 v[148:151],a[232:235],v[60:63],v[148:151]
ds_read_u16 v37,v37
v_accvgpr_write_b32 a24,v184
v_mfma_f32_16x16x32_bf16 v[84:87],a[236:239],v[60:63],v[84:87]
ds_read_b64_tr_b16 a[50:51],v3 offset:20024
ds_read_b64_tr_b16 a[56:57],v3 offset:18544
v_lshlrev_b32_sdwa v39,1,v39 dst_sel:DWORD dst_unused:UNUSED_PAD src0_sel:DWORD src1_sel:WORD_1
v_accvgpr_write_b32 a25,v185
v_mfma_f32_16x16x32_bf16 v[80:83],a[236:239],v[32:35],v[80:83]
ds_read_u16_d16_hi v39,v39
v_lshlrev_b32_sdwa v44,1,v44 dst_sel:DWORD dst_unused:UNUSED_PAD src0_sel:DWORD src1_sel:WORD_1
v_mfma_f32_16x16x32_bf16 v[152:155],a[240:243],v[32:35],v[152:155]
ds_read_u16 v44,v44
ds_read_b64_tr_b16 a[58:59],v3 offset:20080
v_accvgpr_write_b32 a26,v186
v_mfma_f32_16x16x32_bf16 v[156:159],a[240:243],v[60:63],v[156:159]
s_waitcnt lgkmcnt(11)
ds_read_b64_tr_b16 a[64:65],v3 offset:18552
v_lshlrev_b32_sdwa v46,1,v46 dst_sel:DWORD dst_unused:UNUSED_PAD src0_sel:DWORD src1_sel:WORD_1
v_mfma_f32_16x16x32_bf16 v[100:103],a[244:247],v[60:63],v[100:103]
s_nop 0
ds_read_u16_d16_hi v46,v46
ds_read_b64_tr_b16 a[66:67],v3 offset:20088
v_accvgpr_write_b32 a27,v187
v_mfma_f32_16x16x32_bf16 v[96:99],a[244:247],v[32:35],v[96:99]
s_add_u32 m0,s57,31832
ds_read_b64_tr_b16 a[248:249],v3 offset:18608
v_lshlrev_b32_sdwa v45,1,v45 dst_sel:DWORD dst_unused:UNUSED_PAD src0_sel:DWORD src1_sel:WORD_1
v_accvgpr_write_b32 a28,v188
v_lshlrev_b32_sdwa v47,1,v47 dst_sel:DWORD dst_unused:UNUSED_PAD src0_sel:DWORD src1_sel:WORD_1
v_mfma_f32_16x16x32_bf16 v[60:63],a[52:55],a[8:11],v[20:23]
s_add_u32 s89,s56,3342336
ds_read_b64 v[14:15],v7 offset:18480
ds_read_u16 v45,v45
v_lshlrev_b32_sdwa v48,1,v48 dst_sel:DWORD dst_unused:UNUSED_PAD src0_sel:DWORD src1_sel:WORD_1
v_mfma_f32_16x16x32_bf16 v[60:63],a[60:63],a[24:27],v[60:63]
buffer_load_dwordx4 v6,s[36:39],s89 offen lds
ds_read_u16_d16_hi v47,v47
v_lshl_add_u64 v[28:29],v[28:29],0,v[30:31]
v_fma_mixlo_f16 v8,v57,s99,0
v_mfma_f32_16x16x32_bf16 v[60:63],a[68:71],v[200:203],v[60:63]
ds_read_b64_tr_b16 a[72:73],v1 offset:18480
ds_read_b64_tr_b16 a[74:75],v1 offset:19504
v_lshl_add_u64 v[30:31],v[24:25],0,v[26:27]
v_lshlrev_b32_sdwa v50,1,v50 dst_sel:DWORD dst_unused:UNUSED_PAD src0_sel:DWORD src1_sel:WORD_1
v_mfma_f32_16x16x32_bf16 v[32:35],a[84:87],v[200:203],v[16:19]
s_nop 0
ds_read_b64_tr_b16 a[220:221],v1 offset:20528
ds_read_b64_tr_b16 a[222:223],v1 offset:21552
v_fma_mixhi_f16 v8,v59,s99,0
v_mfma_f32_16x16x32_bf16 v[32:35],a[80:83],a[24:27],v[32:35]
s_waitcnt lgkmcnt(13)
ds_read_b64_tr_b16 a[216:217],v1 offset:22576
ds_read_b64_tr_b16 a[218:219],v1 offset:23600
v_lshl_add_u64 v[24:25],v[40:41],0,v[42:43]
v_lshlrev_b32_sdwa v49,1,v49 dst_sel:DWORD dst_unused:UNUSED_PAD src0_sel:DWORD src1_sel:WORD_1
v_pk_fma_f16 v9,v8,v8,s54
v_mfma_f32_16x16x32_bf16 v[32:35],a[76:79],a[8:11],v[32:35]
s_waitcnt lgkmcnt(8)
ds_read_u16 v48,v48
ds_read_u16_d16_hi v50,v50
v_or3_b32 v252,v14,v15,v252
v_accvgpr_write_b32 a29,v189
v_mfma_f32_16x16x32_bf16 v[40:43],a[76:79],a[12:15],v[16:19]
s_nop 0
ds_read_b64 v[14:15],v7 offset:18992
ds_read_b128 v[16:19],v4 offset:512
v_pk_mul_f16 v9,v8,v9
v_lshlrev_b32_sdwa v51,1,v51 dst_sel:DWORD dst_unused:UNUSED_PAD src0_sel:DWORD src1_sel:WORD_1
v_mfma_f32_16x16x32_bf16 v[40:43],a[80:83],v[188:191],v[40:43]
ds_read_u16 v49,v49
ds_read_u16_d16_hi v51,v51
v_lshl_add_u64 v[26:27],v[36:37],0,v[38:39]
v_mfma_f32_16x16x32_bf16 v[40:43],a[84:87],v[204:207],v[40:43]
ds_read_b64_tr_b16 a[192:193],v2 offset:22576
v_exp_f16 v8,v9
v_lshlrev_b32_sdwa v52,1,v52 dst_sel:DWORD dst_unused:UNUSED_PAD src0_sel:DWORD src1_sel:WORD_1
v_mfma_f32_16x16x32_bf16 v[36:39],a[68:71],v[204:207],v[20:23]
ds_read_b128 v[20:23],v4 offset:544
ds_read_b64_tr_b16 a[194:195],v2 offset:23600
v_mfma_f32_16x16x32_bf16 v[36:39],a[60:63],v[188:191],v[36:39]
ds_read_b64_tr_b16 a[196:197],v2 offset:20528
ds_read_b64_tr_b16 a[198:199],v2 offset:21552
v_lshlrev_b32_sdwa v54,1,v54 dst_sel:DWORD dst_unused:UNUSED_PAD src0_sel:DWORD src1_sel:WORD_1
v_mfma_f32_16x16x32_bf16 v[36:39],a[52:55],a[12:15],v[36:39]
s_waitcnt lgkmcnt(8)
ds_read_u16 v52,v52
ds_read_u16_d16_hi v54,v54
v_or3_b32 v252,v14,v15,v252
v_accvgpr_write_b32 a30,v190
v_mfma_f32_16x16x32_bf16 v[112:115],a[224:227],v[28:31],v[112:115]
s_add_u32 m0,s57,35368
ds_read_b64 v[14:15],v7 offset:19504
ds_read_b64_tr_b16 a[200:201],v2 offset:18480
v_exp_f16_sdwa v8,v9 dst_sel:WORD_1 dst_unused:UNUSED_PRESERVE src0_sel:WORD_1
v_lshlrev_b32_sdwa v53,1,v53 dst_sel:DWORD dst_unused:UNUSED_PAD src0_sel:DWORD src1_sel:WORD_1
v_mfma_f32_16x16x32_bf16 v[116:119],a[224:227],v[24:27],v[116:119]
s_add_u32 s89,s56,3440640
ds_read_u16 v53,v53
ds_read_b64_tr_b16 a[202:203],v2 offset:19504
v_lshlrev_b32_sdwa v55,1,v55 dst_sel:DWORD dst_unused:UNUSED_PAD src0_sel:DWORD src1_sel:WORD_1
v_mfma_f32_16x16x32_bf16 v[76:79],a[228:231],v[24:27],v[76:79]
buffer_load_dwordx4 v6,s[36:39],s89 offen lds
ds_read_u16_d16_hi v55,v55
v_pk_fma_f16 v8,v8,s55,s55
v_mfma_f32_16x16x32_bf16 v[72:75],a[228:231],v[28:31],v[72:75]
v_rcp_f16 v9,v8
v_lshlrev_b32_sdwa v56,1,v56 dst_sel:DWORD dst_unused:UNUSED_PAD src0_sel:DWORD src1_sel:WORD_1
v_mfma_f32_16x16x32_bf16 v[120:123],a[232:235],v[28:31],v[120:123]
ds_read_u16 v56,v56
v_accvgpr_write_b32 a31,v191
v_mfma_f32_16x16x32_bf16 v[124:127],a[232:235],v[24:27],v[124:127]
s_waitcnt lgkmcnt(5)
ds_read_b64_tr_b16 a[250:251],v3 offset:20144
v_or3_b32 v252,v14,v15,v252
v_lshlrev_b32_sdwa v58,1,v58 dst_sel:DWORD dst_unused:UNUSED_PAD src0_sel:DWORD src1_sel:WORD_1
v_mfma_f32_16x16x32_bf16 v[92:95],a[236:239],v[24:27],v[92:95]
ds_read_u16_d16_hi v58,v58
v_lshlrev_b32_sdwa v60,1,v60 dst_sel:DWORD dst_unused:UNUSED_PAD src0_sel:DWORD src1_sel:WORD_1
v_mfma_f32_16x16x32_bf16 v[88:91],a[236:239],v[28:31],v[88:91]
v_rcp_f16_sdwa v9,v8 dst_sel:WORD_1 dst_unused:UNUSED_PRESERVE src0_sel:WORD_1
v_mfma_f32_16x16x32_bf16 v[128:131],a[240:243],v[28:31],v[128:131]
ds_read_u16 v60,v60
v_lshlrev_b32_sdwa v62,1,v62 dst_sel:DWORD dst_unused:UNUSED_PAD src0_sel:DWORD src1_sel:WORD_1
v_mfma_f32_16x16x32_bf16 v[132:135],a[240:243],v[24:27],v[132:135]
ds_read_u16_d16_hi v62,v62
v_fma_mix_f32 v57,v57,v9,0 op_sel_hi:[0,1,0]
v_mfma_f32_16x16x32_bf16 v[108:111],a[244:247],v[24:27],v[108:111]
ds_read_b64_tr_b16 a[252:253],v3 offset:18616
v_lshlrev_b32_sdwa v61,1,v61 dst_sel:DWORD dst_unused:UNUSED_PAD src0_sel:DWORD src1_sel:WORD_1
v_mfma_f32_16x16x32_bf16 v[104:107],a[244:247],v[28:31],v[104:107]
ds_read_u16 v61,v61
v_mfma_f32_32x32x16_bf16 v[176:191],a[88:91],a[88:91],0
v_fma_mix_f32 v59,v59,v9,0 op_sel:[0,1,0] op_sel_hi:[0,1,0]
v_lshlrev_b32_sdwa v63,1,v63 dst_sel:DWORD dst_unused:UNUSED_PAD src0_sel:DWORD src1_sel:WORD_1
v_mfma_f32_16x16x32_bf16 v[24:27],a[72:75],a[0:3],v[16:19]
s_add_u32 m0,s57,38896
ds_read_u16_d16_hi v63,v63
v_lshlrev_b32_sdwa v32,1,v32 dst_sel:DWORD dst_unused:UNUSED_PAD src0_sel:DWORD src1_sel:WORD_1
v_mfma_f32_16x16x32_bf16 v[24:27],a[220:223],a[16:19],v[24:27]
s_add_u32 s89,s56,3538944
ds_read_b64_tr_b16 a[254:255],v3 offset:20152
v_lshl_add_u64 v[44:45],v[44:45],0,v[46:47]
v_accvgpr_write_b32 a32,v192
v_mfma_f32_16x16x32_bf16 v[24:27],a[216:219],v[192:195],v[24:27]
buffer_load_dwordx4 v6,s[36:39],s89 offen lds
v_lshl_add_u64 v[46:47],v[48:49],0,v[50:51]
v_lshlrev_b32_sdwa v34,1,v34 dst_sel:DWORD dst_unused:UNUSED_PAD src0_sel:DWORD src1_sel:WORD_1
v_mfma_f32_16x16x32_bf16 v[28:31],a[192:195],v[192:195],v[20:23]
ds_read_u16 v32,v32
v_accvgpr_write_b32 a33,v193
v_mfma_f32_16x16x32_bf16 v[28:31],a[196:199],a[16:19],v[28:31]
s_waitcnt lgkmcnt(7)
ds_read_u16_d16_hi v34,v34
v_lshl_add_u64 v[48:49],v[52:53],0,v[54:55]
v_lshlrev_b32_sdwa v33,1,v33 dst_sel:DWORD dst_unused:UNUSED_PAD src0_sel:DWORD src1_sel:WORD_1
v_mfma_f32_16x16x32_bf16 v[28:31],a[200:203],a[0:3],v[28:31]
ds_read_u16 v33,v33
v_accvgpr_write_b32 a34,v194
v_lshlrev_b32_sdwa v35,1,v35 dst_sel:DWORD dst_unused:UNUSED_PAD src0_sel:DWORD src1_sel:WORD_1
v_mfma_f32_16x16x32_bf16 v[52:55],a[200:203],a[4:7],v[20:23]
ds_read_u16_d16_hi v35,v35
v_or_b32_e64 v50,v56,v58
v_accvgpr_write_b32 a35,v195
v_mfma_f32_16x16x32_bf16 v[52:55],a[196:199],a[20:23],v[52:55]
s_waitcnt vmcnt(9)
v_cvt_pk_bf16_f32 v51,v57,v59
v_lshlrev_b32_sdwa v36,1,v36 dst_sel:DWORD dst_unused:UNUSED_PAD src0_sel:DWORD src1_sel:WORD_1
v_mfma_f32_16x16x32_bf16 v[52:55],a[192:195],v[196:199],v[52:55]
s_barrier
ds_read_u16 v36,v36
v_accvgpr_write_b32 a36,v196
v_lshlrev_b32_sdwa v38,1,v38 dst_sel:DWORD dst_unused:UNUSED_PAD src0_sel:DWORD src1_sel:WORD_1
v_mfma_f32_16x16x32_bf16 v[56:59],a[216:219],v[196:199],v[16:19]
ds_read_u16_d16_hi v38,v38
v_accvgpr_write_b32 a37,v197
v_mfma_f32_16x16x32_bf16 v[56:59],a[220:223],a[20:23],v[56:59]
s_add_u32 m0,s58,12320
ds_read_b64_tr_b16 a[224:225],v3 offset:0
v_lshlrev_b32_sdwa v37,1,v37 dst_sel:DWORD dst_unused:UNUSED_PAD src0_sel:DWORD src1_sel:WORD_1
v_mfma_f32_16x16x32_bf16 v[56:59],a[72:75],a[4:7],v[56:59]
s_add_u32 s69,s69,s59
ds_read_u16 v37,v37
v_accvgpr_write_b32 a38,v198
v_mfma_f32_16x16x32_bf16 v[136:139],a[92:95],v[44:47],v[136:139]
buffer_load_dwordx4 v5,s[44:47],s69 offen offset:0 lds
v_lshlrev_b32_sdwa v39,1,v39 dst_sel:DWORD dst_unused:UNUSED_PAD src0_sel:DWORD src1_sel:WORD_1
v_mfma_f32_16x16x32_bf16 v[140:143],a[92:95],v[48:51],v[140:143]
ds_read_u16_d16_hi v39,v39
v_accvgpr_write_b32 a39,v199
v_lshlrev_b32_sdwa v40,1,v40 dst_sel:DWORD dst_unused:UNUSED_PAD src0_sel:DWORD src1_sel:WORD_1
v_mfma_f32_16x16x32_bf16 v[68:71],a[48:51],v[48:51],v[68:71]
ds_read_u16 v40,v40
v_accvgpr_write_b32 a40,v200
v_mfma_f32_16x16x32_bf16 v[64:67],a[48:51],v[44:47],v[64:67]
s_add_u32 m0,s57,42432
ds_read_b64_tr_b16 a[226:227],v3 offset:1536
v_lshlrev_b32_sdwa v42,1,v42 dst_sel:DWORD dst_unused:UNUSED_PAD src0_sel:DWORD src1_sel:WORD_1
v_mfma_f32_16x16x32_bf16 v[144:147],a[56:59],v[44:47],v[144:147]
s_add_u32 s89,s56,3637248
ds_read_u16_d16_hi v42,v42
ds_read_b64_tr_b16 a[228:229],v3 offset:8
v_lshlrev_b32_sdwa v41,1,v41 dst_sel:DWORD dst_unused:UNUSED_PAD src0_sel:DWORD src1_sel:WORD_1
v_mfma_f32_16x16x32_bf16 v[148:151],a[56:59],v[48:51],v[148:151]
buffer_load_dwordx4 v6,s[36:39],s89 offen lds
ds_read_u16 v41,v41
v_accvgpr_write_b32 a41,v201
v_mfma_f32_16x16x32_bf16 v[84:87],a[64:67],v[48:51],v[84:87]
s_add_u32 m0,s58,12320
ds_read_b64_tr_b16 a[230:231],v3 offset:1544
ds_read_b64_tr_b16 a[232:233],v3 offset:64
v_lshlrev_b32_sdwa v43,1,v43 dst_sel:DWORD dst_unused:UNUSED_PAD src0_sel:DWORD src1_sel:WORD_1
v_accvgpr_write_b32 a42,v202
v_mfma_f32_16x16x32_bf16 v[80:83],a[64:67],v[44:47],v[80:83]
s_add_u32 s69,s69,s53
ds_read_u16_d16_hi v43,v43
v_lshlrev_b32_sdwa v24,1,v24 dst_sel:DWORD dst_unused:UNUSED_PAD src0_sel:DWORD src1_sel:WORD_1
v_mfma_f32_16x16x32_bf16 v[152:155],a[248:251],v[44:47],v[152:155]
s_nop 0
buffer_load_dwordx4 v5,s[44:47],s69 offen offset:1024 lds
ds_read_u16 v24,v24
v_accvgpr_write_b32 a43,v203
v_mfma_f32_16x16x32_bf16 v[156:159],a[248:251],v[48:51],v[156:159]
s_waitcnt lgkmcnt(10)
ds_read_b64_tr_b16 a[234:235],v3 offset:1600
ds_read_b64_tr_b16 a[236:237],v3 offset:72
v_lshlrev_b32_sdwa v26,1,v26 dst_sel:DWORD dst_unused:UNUSED_PAD src0_sel:DWORD src1_sel:WORD_1
v_mfma_f32_16x16x32_bf16 v[100:103],a[252:255],v[48:51],v[100:103]
s_add_u32 s69,s69,s53
ds_read_u16_d16_hi v26,v26
ds_read_b64_tr_b16 a[238:239],v3 offset:1608
v_accvgpr_write_b32 a44,v204
v_mfma_f32_16x16x32_bf16 v[96:99],a[252:255],v[44:47],v[96:99]
buffer_load_dwordx4 v5,s[44:47],s69 offen offset:2048 lds
ds_read_b64_tr_b16 a[240:241],v3 offset:128
v_lshlrev_b32_sdwa v25,1,v25 dst_sel:DWORD dst_unused:UNUSED_PAD src0_sel:DWORD src1_sel:WORD_1
v_accvgpr_write_b32 a45,v205
v_lshlrev_b32_sdwa v27,1,v27 dst_sel:DWORD dst_unused:UNUSED_PAD src0_sel:DWORD src1_sel:WORD_1
v_mfma_f32_16x16x32_bf16 v[48:51],a[72:75],a[8:11],v[16:19]
ds_read_b64 v[14:15],v7 offset:0
ds_read_u16 v25,v25
v_lshlrev_b32_sdwa v28,1,v28 dst_sel:DWORD dst_unused:UNUSED_PAD src0_sel:DWORD src1_sel:WORD_1
v_mfma_f32_16x16x32_bf16 v[48:51],a[220:223],a[24:27],v[48:51]
ds_read_u16_d16_hi v27,v27
ds_read_b64_tr_b16 a[204:205],v1 offset:0
v_lshl_add_u64 v[60:61],v[60:61],0,v[62:63]
v_fma_mixlo_f16 v8,v53,s99,0
v_mfma_f32_16x16x32_bf16 v[48:51],a[216:219],a[40:43],v[48:51]
ds_read_b64_tr_b16 a[206:207],v1 offset:1024
ds_read_b64_tr_b16 a[208:209],v1 offset:2048
v_lshl_add_u64 v[62:63],v[32:33],0,v[34:35]
v_lshlrev_b32_sdwa v30,1,v30 dst_sel:DWORD dst_unused:UNUSED_PAD src0_sel:DWORD src1_sel:WORD_1
v_mfma_f32_16x16x32_bf16 v[44:47],a[192:195],a[40:43],v[20:23]
ds_read_b64_tr_b16 a[210:211],v1 offset:3072
ds_read_b64_tr_b16 a[212:213],v1 offset:4096
v_fma_mixhi_f16 v8,v55,s99,0
v_mfma_f32_16x16x32_bf16 v[44:47],a[196:199],a[24:27],v[44:47]
s_waitcnt lgkmcnt(14)
ds_read_b64_tr_b16 a[214:215],v1 offset:5120
ds_read_u16 v28,v28
v_lshl_add_u64 v[32:33],v[36:37],0,v[38:39]
v_lshlrev_b32_sdwa v29,1,v29 dst_sel:DWORD dst_unused:UNUSED_PAD src0_sel:DWORD src1_sel:WORD_1
v_pk_fma_f16 v9,v8,v8,s54
v_mfma_f32_16x16x32_bf16 v[44:47],a[200:203],a[8:11],v[44:47]
s_waitcnt lgkmcnt(9)
ds_read_u16_d16_hi v30,v30
ds_read_u16 v29,v29
v_or3_b32 v252,v14,v15,v252
v_mfma_f32_16x16x32_bf16 v[36:39],a[200:203],a[12:15],v[20:23]
s_add_u32 m0,s57,45976
ds_read_b64 v[14:15],v7 offset:512
ds_read_b128 v[20:23],v4 offset:640
v_pk_mul_f16 v9,v8,v9
v_lshlrev_b32_sdwa v31,1,v31 dst_sel:DWORD dst_unused:UNUSED_PAD src0_sel:DWORD src1_sel:WORD_1
v_mfma_f32_16x16x32_bf16 v[36:39],a[196:199],a[28:31],v[36:39]
s_add_u32 s89,s56,3735552
ds_read_u16_d16_hi v31,v31
ds_read_b64_tr_b16 a[200:201],v2 offset:4096
v_lshl_add_u64 v[34:35],v[40:41],0,v[42:43]
v_accvgpr_write_b32 a46,v206
v_mfma_f32_16x16x32_bf16 v[36:39],a[192:195],v[204:207],v[36:39]
ds_read_b64_tr_b16 a[202:203],v2 offset:5120
v_exp_f16 v8,v9
v_lshlrev_b32_sdwa v56,1,v56 dst_sel:DWORD dst_unused:UNUSED_PAD src0_sel:DWORD src1_sel:WORD_1
v_mfma_f32_16x16x32_bf16 v[40:43],a[216:219],v[204:207],v[16:19]
buffer_load_dwordx4 v6,s[36:39],s89 offen lds
ds_read_b128 v[16:19],v4 offset:672
v_mfma_f32_16x16x32_bf16 v[40:43],a[220:223],a[28:31],v[40:43]
ds_read_b64_tr_b16 a[196:197],v2 offset:2048
ds_read_b64_tr_b16 a[198:199],v2 offset:3072
v_lshlrev_b32_sdwa v58,1,v58 dst_sel:DWORD dst_unused:UNUSED_PAD src0_sel:DWORD src1_sel:WORD_1
v_mfma_f32_16x16x32_bf16 v[40:43],a[72:75],a[12:15],v[40:43]
s_waitcnt lgkmcnt(7)
ds_read_u16 v56,v56
ds_read_u16_d16_hi v58,v58
v_or3_b32 v252,v14,v15,v252
v_mfma_f32_16x16x32_bf16 v[112:115],a[92:95],v[60:63],v[112:115]
ds_read_b64 v[14:15],v7 offset:1024
ds_read_b64_tr_b16 a[192:193],v2 offset:0
v_exp_f16_sdwa v8,v9 dst_sel:WORD_1 dst_unused:UNUSED_PRESERVE src0_sel:WORD_1
v_lshlrev_b32_sdwa v57,1,v57 dst_sel:DWORD dst_unused:UNUSED_PAD src0_sel:DWORD src1_sel:WORD_1
v_mfma_f32_16x16x32_bf16 v[116:119],a[92:95],v[32:35],v[116:119]
ds_read_u16 v57,v57
ds_read_b64_tr_b16 a[194:195],v2 offset:1024
v_lshlrev_b32_sdwa v59,1,v59 dst_sel:DWORD dst_unused:UNUSED_PAD src0_sel:DWORD src1_sel:WORD_1
v_mfma_f32_16x16x32_bf16 v[76:79],a[48:51],v[32:35],v[76:79]
ds_read_u16_d16_hi v59,v59
v_pk_fma_f16 v8,v8,s55,s55
v_mfma_f32_16x16x32_bf16 v[72:75],a[48:51],v[60:63],v[72:75]
v_rcp_f16_e64 v9,v8
v_lshlrev_b32_sdwa v52,1,v52 dst_sel:DWORD dst_unused:UNUSED_PAD src0_sel:DWORD src1_sel:WORD_1
v_accvgpr_write_b32 a47,v207
v_mfma_f32_16x16x32_bf16 v[120:123],a[56:59],v[60:63],v[120:123]
s_add_u32 m0,s57,49512
ds_read_u16 v52,v52
v_lshlrev_b32_sdwa v54,1,v54 dst_sel:DWORD dst_unused:UNUSED_PAD src0_sel:DWORD src1_sel:WORD_1
v_mfma_f32_16x16x32_bf16 v[124:127],a[56:59],v[32:35],v[124:127]
s_waitcnt lgkmcnt(5)
ds_read_u16_d16_hi v54,v54
v_or3_b32 v252,v14,v15,v252
v_mfma_f32_16x16x32_bf16 v[92:95],a[64:67],v[32:35],v[92:95]
s_add_u32 s89,s56,3833856
ds_read_b64_tr_b16 a[242:243],v3 offset:1664
v_and_b32 v252,0x40004000,v252
v_mfma_f32_16x16x32_bf16 v[88:91],a[64:67],v[60:63],v[88:91]
buffer_load_dwordx4 v6,s[36:39],s89 offen lds
v_cmp_ne_u32_e32 vcc,0,v252
s_nop 0
s_or_b64 s[90:91],s[90:91],vcc
v_lshlrev_b32_sdwa v48,1,v48 dst_sel:DWORD dst_unused:UNUSED_PAD src0_sel:DWORD src1_sel:WORD_1
v_mfma_f32_16x16x32_bf16 v[128:131],a[248:251],v[60:63],v[128:131]
v_rcp_f16_sdwa v9,v8 dst_sel:WORD_1 dst_unused:UNUSED_PRESERVE src0_sel:WORD_1
v_mfma_f32_16x16x32_bf16 v[132:135],a[248:251],v[32:35],v[132:135]
ds_read_u16 v48,v48
v_lshlrev_b32_sdwa v50,1,v50 dst_sel:DWORD dst_unused:UNUSED_PAD src0_sel:DWORD src1_sel:WORD_1
v_mfma_f32_16x16x32_bf16 v[108:111],a[252:255],v[32:35],v[108:111]
ds_read_u16_d16_hi v50,v50
v_fma_mix_f32 v53,v53,v9,0 op_sel_hi:[0,1,0]
v_mfma_f32_16x16x32_bf16 v[104:107],a[252:255],v[60:63],v[104:107]
ds_read_b64_tr_b16 a[244:245],v3 offset:136
v_lshlrev_b32_sdwa v49,1,v49 dst_sel:DWORD dst_unused:UNUSED_PAD src0_sel:DWORD src1_sel:WORD_1
v_mfma_f32_32x32x16_bf16 v[192:207],a[88:91],a[88:91],0
v_fma_mix_f32 v55,v55,v9,0 op_sel:[0,1,0] op_sel_hi:[0,1,0]
v_lshlrev_b32_sdwa v51,1,v51 dst_sel:DWORD dst_unused:UNUSED_PAD src0_sel:DWORD src1_sel:WORD_1
v_mfma_f32_16x16x32_bf16 v[32:35],a[204:207],a[0:3],v[20:23]
ds_read_u16 v49,v49
v_lshlrev_b32_sdwa v44,1,v44 dst_sel:DWORD dst_unused:UNUSED_PAD src0_sel:DWORD src1_sel:WORD_1
v_mfma_f32_16x16x32_bf16 v[32:35],a[208:211],a[16:19],v[32:35]
ds_read_u16_d16_hi v51,v51
v_lshl_add_u64 v[24:25],v[24:25],0,v[26:27]
v_mfma_f32_16x16x32_bf16 v[32:35],a[212:215],a[32:35],v[32:35]
ds_read_b64_tr_b16 a[246:247],v3 offset:1672
v_lshl_add_u64 v[26:27],v[28:29],0,v[30:31]
v_lshlrev_b32_sdwa v46,1,v46 dst_sel:DWORD dst_unused:UNUSED_PAD src0_sel:DWORD src1_sel:WORD_1
v_mfma_f32_16x16x32_bf16 v[60:63],a[200:203],a[32:35],v[16:19]
ds_read_u16 v44,v44
v_lshlrev_b32_sdwa v45,1,v45 dst_sel:DWORD dst_unused:UNUSED_PAD src0_sel:DWORD src1_sel:WORD_1
v_mfma_f32_16x16x32_bf16 v[60:63],a[196:199],a[16:19],v[60:63]
s_waitcnt lgkmcnt(7)
ds_read_u16_d16_hi v46,v46
v_lshl_add_u64 v[28:29],v[56:57],0,v[58:59]
v_mfma_f32_16x16x32_bf16 v[60:63],a[192:195],a[0:3],v[60:63]
ds_read_u16 v45,v45
v_lshlrev_b32_sdwa v47,1,v47 dst_sel:DWORD dst_unused:UNUSED_PAD src0_sel:DWORD src1_sel:WORD_1
v_mfma_f32_16x16x32_bf16 v[56:59],a[192:195],a[4:7],v[16:19]
ds_read_u16_d16_hi v47,v47
v_or_b32 v30,v52,v54
v_mfma_f32_16x16x32_bf16 v[56:59],a[196:199],a[20:23],v[56:59]
s_waitcnt vmcnt(11)
v_cvt_pk_bf16_f32 v31,v53,v55
v_lshlrev_b32_sdwa v40,1,v40 dst_sel:DWORD dst_unused:UNUSED_PAD src0_sel:DWORD src1_sel:WORD_1
v_mfma_f32_16x16x32_bf16 v[56:59],a[200:203],a[36:39],v[56:59]
s_barrier
ds_read_u16 v40,v40
v_lshlrev_b32_sdwa v42,1,v42 dst_sel:DWORD dst_unused:UNUSED_PAD src0_sel:DWORD src1_sel:WORD_1
v_mfma_f32_16x16x32_bf16 v[52:55],a[212:215],a[36:39],v[20:23]
s_add_u32 m0,s57,53040
ds_read_u16_d16_hi v42,v42
v_lshlrev_b32_sdwa v41,1,v41 dst_sel:DWORD dst_unused:UNUSED_PAD src0_sel:DWORD src1_sel:WORD_1
v_mfma_f32_16x16x32_bf16 v[52:55],a[208:211],a[20:23],v[52:55]
s_add_u32 s89,s56,3932160
ds_read_u16 v41,v41
v_mfma_f32_32x32x16_bf16 v[240:255],a[88:91],a[88:91],0
v_lshlrev_b32_sdwa v43,1,v43 dst_sel:DWORD dst_unused:UNUSED_PAD src0_sel:DWORD src1_sel:WORD_1
v_mfma_f32_16x16x32_bf16 v[52:55],a[204:207],a[4:7],v[52:55]
buffer_load_dwordx4 v6,s[36:39],s89 offen lds
v_lshlrev_b32_sdwa v36,1,v36 dst_sel:DWORD dst_unused:UNUSED_PAD src0_sel:DWORD src1_sel:WORD_1
v_mfma_f32_16x16x32_bf16 v[136:139],a[224:227],v[24:27],v[136:139]
s_add_u32 m0,s58,18480
ds_read_u16_d16_hi v43,v43
v_lshlrev_b32_sdwa v38,1,v38 dst_sel:DWORD dst_unused:UNUSED_PAD src0_sel:DWORD src1_sel:WORD_1
v_mfma_f32_16x16x32_bf16 v[140:143],a[224:227],v[28:31],v[140:143]
s_add_u32 s69,s69,s59
ds_read_u16 v36,v36
v_mfma_f32_16x16x32_bf16 v[68:71],a[228:231],v[28:31],v[68:71]
buffer_load_dwordx4 v5,s[44:47],s69 offen offset:0 lds
ds_read_u16_d16_hi v38,v38
v_mfma_f32_16x16x32_bf16 v[64:67],a[228:231],v[24:27],v[64:67]
ds_read_b64_tr_b16 a[248:249],v3 offset:6160
v_lshlrev_b32_sdwa v37,1,v37 dst_sel:DWORD dst_unused:UNUSED_PAD src0_sel:DWORD src1_sel:WORD_1
v_mfma_f32_16x16x32_bf16 v[144:147],a[232:235],v[24:27],v[144:147]
s_add_u32 s69,s69,s53
ds_read_u16 v37,v37
v_lshlrev_b32_sdwa v39,1,v39 dst_sel:DWORD dst_unused:UNUSED_PAD src0_sel:DWORD src1_sel:WORD_1
v_mfma_f32_16x16x32_bf16 v[148:151],a[232:235],v[28:31],v[148:151]
buffer_load_dwordx4 v5,s[44:47],s69 offen offset:1024 lds
ds_read_u16_d16_hi v39,v39
v_mfma_f32_16x16x32_bf16 v[84:87],a[236:239],v[28:31],v[84:87]
ds_read_b64_tr_b16 a[250:251],v3 offset:7696
v_lshlrev_b32_sdwa v32,1,v32 dst_sel:DWORD dst_unused:UNUSED_PAD src0_sel:DWORD src1_sel:WORD_1
v_mfma_f32_16x16x32_bf16 v[80:83],a[236:239],v[24:27],v[80:83]
ds_read_u16 v32,v32
v_lshlrev_b32_sdwa v34,1,v34 dst_sel:DWORD dst_unused:UNUSED_PAD src0_sel:DWORD src1_sel:WORD_1
v_mfma_f32_16x16x32_bf16 v[152:155],a[240:243],v[24:27],v[152:155]
ds_read_u16_d16_hi v34,v34
v_lshlrev_b32_sdwa v33,1,v33 dst_sel:DWORD dst_unused:UNUSED_PAD src0_sel:DWORD src1_sel:WORD_1
v_mfma_f32_16x16x32_bf16 v[156:159],a[240:243],v[28:31],v[156:159]
s_waitcnt lgkmcnt(8)
ds_read_u16 v33,v33
v_mfma_f32_16x16x32_bf16 v[100:103],a[244:247],v[28:31],v[100:103]
s_add_u32 s69,s69,s53
ds_read_b64_tr_b16 a[252:253],v3 offset:6168
v_lshlrev_b32_sdwa v35,1,v35 dst_sel:DWORD dst_unused:UNUSED_PAD src0_sel:DWORD src1_sel:WORD_1
v_mfma_f32_16x16x32_bf16 v[96:99],a[244:247],v[24:27],v[96:99]
buffer_load_dwordx4 v5,s[44:47],s69 offen offset:2048 lds
ds_read_u16_d16_hi v35,v35
ds_read_b64_tr_b16 a[254:255],v3 offset:7704
s_waitcnt vmcnt(13)
s_barrier
v_add_u32_sdwa v24,s60,v10 dst_sel:DWORD dst_unused:UNUSED_PAD src0_sel:DWORD src1_sel:WORD_1
v_subrev_u32 v8,88400,v24
v_min_u32_e64 v24,v24,v8
v_add_u32 v25,7072,v24
v_subrev_u32 v8,88400,v25
v_min_u32_e64 v25,v25,v8
v_lshl_add_u64 v[48:49],v[48:49],0,v[50:51]
v_lshl_add_u64 v[50:51],v[44:45],0,v[46:47]
v_lshl_add_u64 v[44:45],v[40:41],0,v[42:43]
s_waitcnt lgkmcnt(7)
v_lshl_add_u64 v[46:47],v[36:37],0,v[38:39]
v_mfma_f32_16x16x32_bf16 v[112:115],a[224:227],v[48:51],v[112:115]
s_add_u32 m0,s58,0
s_add_u32 s69,s69,s59
buffer_load_dwordx4 v5,s[44:47],s69 offen offset:0 lds
v_xad_u32 v28,v11,0,v24
v_xad_u32 v29,v11,8,v25
v_mfma_f32_16x16x32_bf16 v[116:119],a[224:227],v[44:47],v[116:119]
TE03_%=:
s_cbranch_execz TP4336_%=
v_xad_u32 v30,v11,16,v24
v_xad_u32 v31,v11,24,v25
v_mfma_f32_16x16x32_bf16 v[76:79],a[228:231],v[44:47],v[76:79]
ds_read_b64 a[48:49],v28 offset:0
ds_read_b64 a[50:51],v28 offset:192
v_mfma_f32_16x16x32_bf16 v[72:75],a[228:231],v[48:51],v[72:75]
ds_read_b64 a[52:53],v29 offset:0
ds_read_b64 a[54:55],v29 offset:192
v_mfma_f32_16x16x32_bf16 v[120:123],a[232:235],v[48:51],v[120:123]
ds_read_b64 a[56:57],v28 offset:8
ds_read_b64 a[58:59],v28 offset:200
v_mfma_f32_16x16x32_bf16 v[124:127],a[232:235],v[44:47],v[124:127]
s_add_u32 s69,s69,s53
buffer_load_dwordx4 v5,s[44:47],s69 offen offset:1024 lds
ds_read_b64 a[60:61],v29 offset:8
ds_read_b64 a[62:63],v29 offset:200
v_mfma_f32_16x16x32_bf16 v[92:95],a[236:239],v[44:47],v[92:95]
v_mfma_f32_16x16x32_bf16 v[88:91],a[236:239],v[48:51],v[88:91]
v_mfma_f32_16x16x32_bf16 v[128:131],a[240:243],v[48:51],v[128:131]
v_mfma_f32_16x16x32_bf16 v[132:135],a[240:243],v[44:47],v[132:135]
v_mfma_f32_16x16x32_bf16 v[108:111],a[244:247],v[44:47],v[108:111]
v_mfma_f32_16x16x32_bf16 v[104:107],a[244:247],v[48:51],v[104:107]
ds_read_b64 a[64:65],v30 offset:0
ds_read_b64 a[66:67],v30 offset:192
ds_read_b64 a[68:69],v31 offset:0
ds_read_b64 a[70:71],v31 offset:192
s_waitcnt lgkmcnt(8)
v_smfmac_f32_16x16x64_bf16 v[160:163],a[96:99],a[48:55],v10
s_waitcnt lgkmcnt(4)
s_add_u32 s69,s69,s53
buffer_load_dwordx4 v5,s[44:47],s69 offen offset:2048 lds
ds_read_b64 a[48:49],v30 offset:8
ds_read_b64 a[50:51],v30 offset:200
ds_read_b64 a[52:53],v31 offset:8
ds_read_b64 a[54:55],v31 offset:200
v_smfmac_f32_16x16x64_bf16 v[164:167],a[100:103],a[56:63],v10
s_waitcnt lgkmcnt(4)
ds_read_b64 a[56:57],v28 offset:64
ds_read_b64 a[58:59],v28 offset:256
ds_read_b64 a[60:61],v29 offset:64
ds_read_b64 a[62:63],v29 offset:256
v_smfmac_f32_16x16x64_bf16 v[168:171],a[104:107],a[64:71],v10
s_waitcnt lgkmcnt(4)
ds_read_b64 a[64:65],v28 offset:72
ds_read_b64 a[66:67],v28 offset:264
ds_read_b64 a[68:69],v29 offset:72
ds_read_b64 a[70:71],v29 offset:264
v_and_b32 v48,8,v11
v_pk_mul_f32 v[44:45],v[160:161],v[160:161]
v_pk_mul_f32 v[46:47],v[162:163],v[162:163]
v_smfmac_f32_16x16x64_bf16 v[172:175],a[108:111],a[48:55],v10
s_waitcnt lgkmcnt(4)
ds_read_b64 a[48:49],v30 offset:64
ds_read_b64 a[50:51],v30 offset:256
ds_read_b64 a[52:53],v31 offset:64
ds_read_b64 a[54:55],v31 offset:256
v_mul_u32_u24 v48,884,v48
s_add_u32 s64,s60,3536
v_pk_add_f32 v[36:37],v[160:161],v[164:165]
v_pk_fma_f32 v[44:45],v[164:165],v[164:165],v[44:45]
v_pk_add_f32 v[38:39],v[162:163],v[166:167]
v_pk_fma_f32 v[46:47],v[166:167],v[166:167],v[46:47]
v_smfmac_f32_16x16x64_bf16 v[176:179],a[128:131],a[56:63],v10
s_waitcnt lgkmcnt(4)
ds_read_b64 a[56:57],v30 offset:72
ds_read_b64 a[58:59],v30 offset:264
ds_read_b64 a[60:61],v31 offset:72
ds_read_b64 a[62:63],v31 offset:264
v_add_u32 v51,s64,v48
v_pk_add_f32 v[36:37],v[36:37],v[168:169]
v_pk_fma_f32 v[44:45],v[168:169],v[168:169],v[44:45]
v_pk_add_f32 v[38:39],v[38:39],v[170:171]
v_pk_fma_f32 v[46:47],v[170:171],v[170:171],v[46:47]
v_smfmac_f32_16x16x64_bf16 v[180:183],a[132:135],a[64:71],v10
s_waitcnt lgkmcnt(4)
ds_read_b64 a[64:65],v28 offset:128
ds_read_b64 a[66:67],v28 offset:320
ds_read_b64 a[68:69],v29 offset:128
ds_read_b64 a[70:71],v29 offset:320
v_subrev_u32 v8,88400,v51
v_pk_add_f32 v[36:37],v[36:37],v[172:173]
v_pk_fma_f32 v[44:45],v[172:173],v[172:173],v[44:45]
v_pk_add_f32 v[38:39],v[38:39],v[174:175]
v_pk_fma_f32 v[46:47],v[174:175],v[174:175],v[46:47]
v_smfmac_f32_16x16x64_bf16 v[184:187],a[136:139],a[48:55],v10
s_waitcnt lgkmcnt(4)
ds_read_b64 a[48:49],v28 offset:136
ds_read_b64 a[50:51],v28 offset:328
ds_read_b64 a[52:53],v29 offset:136
ds_read_b64 a[54:55],v29 offset:328
v_min_u32 v51,v51,v8
v_pk_add_f32 v[36:37],v[36:37],v[176:177]
v_pk_fma_f32 v[44:45],v[176:177],v[176:177],v[44:45]
v_pk_add_f32 v[38:39],v[38:39],v[178:179]
v_pk_fma_f32 v[46:47],v[178:179],v[178:179],v[46:47]
v_smfmac_f32_16x16x64_bf16 v[188:191],a[140:143],a[56:63],v10
s_waitcnt lgkmcnt(4)
ds_read_b64 a[56:57],v30 offset:128
ds_read_b64 a[58:59],v30 offset:320
ds_read_b64 a[60:61],v31 offset:128
ds_read_b64 a[62:63],v31 offset:320
v_xad_u32 v28,v11,32,v24
v_xad_u32 v29,v11,40,v25
v_pk_add_f32 v[36:37],v[36:37],v[180:181]
v_pk_fma_f32 v[44:45],v[180:181],v[180:181],v[44:45]
v_pk_add_f32 v[38:39],v[38:39],v[182:183]
v_pk_fma_f32 v[46:47],v[182:183],v[182:183],v[46:47]
v_smfmac_f32_16x16x64_bf16 v[192:195],a[160:163],a[64:71],v10
s_waitcnt lgkmcnt(4)
ds_read_b64 a[64:65],v30 offset:136
ds_read_b64 a[66:67],v30 offset:328
ds_read_b64 a[68:69],v31 offset:136
ds_read_b64 a[70:71],v31 offset:328
v_add_u32 v50,3536,v51
v_pk_add_f32 v[36:37],v[36:37],v[184:185]
v_pk_fma_f32 v[44:45],v[184:185],v[184:185],v[44:45]
v_pk_add_f32 v[38:39],v[38:39],v[186:187]
v_pk_fma_f32 v[46:47],v[186:187],v[186:187],v[46:47]
v_smfmac_f32_16x16x64_bf16 v[196:199],a[164:167],a[48:55],v10
s_waitcnt lgkmcnt(4)
ds_read_b64 a[48:49],v28 offset:0
ds_read_b64 a[50:51],v28 offset:192
ds_read_b64 a[52:53],v29 offset:0
ds_read_b64 a[54:55],v29 offset:192
v_xad_u32 v30,v11,48,v24
v_xad_u32 v31,v11,56,v25
v_pk_add_f32 v[36:37],v[36:37],v[188:189]
v_pk_fma_f32 v[44:45],v[188:189],v[188:189],v[44:45]
v_pk_add_f32 v[38:39],v[38:39],v[190:191]
v_pk_fma_f32 v[46:47],v[190:191],v[190:191],v[46:47]
v_smfmac_f32_16x16x64_bf16 v[200:203],a[168:171],a[56:63],v10
s_waitcnt lgkmcnt(4)
ds_read_b64 a[56:57],v28 offset:8
ds_read_b64 a[58:59],v28 offset:200
ds_read_b64 a[60:61],v29 offset:8
ds_read_b64 a[62:63],v29 offset:200
v_subrev_u32 v8,88400,v50
v_pk_add_f32 v[36:37],v[36:37],v[192:193]
v_pk_fma_f32 v[44:45],v[192:193],v[192:193],v[44:45]
v_pk_add_f32 v[38:39],v[38:39],v[194:195]
v_pk_fma_f32 v[46:47],v[194:195],v[194:195],v[46:47]
v_smfmac_f32_16x16x64_bf16 v[204:207],a[172:175],a[64:71],v10
s_waitcnt lgkmcnt(4)
ds_read_b64 a[64:65],v28 offset:64
ds_read_b64 a[66:67],v28 offset:256
ds_read_b64 a[68:69],v29 offset:64
ds_read_b64 a[70:71],v29 offset:256
v_min_u32 v50,v50,v8
v_pk_add_f32 v[36:37],v[36:37],v[196:197]
v_pk_fma_f32 v[44:45],v[196:197],v[196:197],v[44:45]
v_pk_add_f32 v[38:39],v[38:39],v[198:199]
v_pk_fma_f32 v[46:47],v[198:199],v[198:199],v[46:47]
v_smfmac_f32_16x16x64_bf16 v[208:211],a[112:115],a[48:55],v10
s_waitcnt lgkmcnt(4)
ds_read_b64 a[48:49],v28 offset:72
ds_read_b64 a[50:51],v28 offset:264
ds_read_b64 a[52:53],v29 offset:72
ds_read_b64 a[54:55],v29 offset:264
v_xor_b32 v9,0x80008,v12
v_pk_add_f32 v[36:37],v[36:37],v[200:201]
v_pk_fma_f32 v[44:45],v[200:201],v[200:201],v[44:45]
v_pk_add_f32 v[38:39],v[38:39],v[202:203]
v_pk_fma_f32 v[46:47],v[202:203],v[202:203],v[46:47]
v_smfmac_f32_16x16x64_bf16 v[212:215],a[116:119],a[56:63],v10
s_waitcnt lgkmcnt(4)
ds_read_b64 a[56:57],v28 offset:128
ds_read_b64 a[58:59],v28 offset:320
ds_read_b64 a[60:61],v29 offset:128
ds_read_b64 a[62:63],v29 offset:320
v_add_u32_sdwa v48,v12,v51 dst_sel:DWORD dst_unused:UNUSED_PAD src0_sel:WORD_0 src1_sel:DWORD
v_pk_add_f32 v[36:37],v[36:37],v[204:205]
v_pk_fma_f32 v[44:45],v[204:205],v[204:205],v[44:45]
v_pk_add_f32 v[38:39],v[38:39],v[206:207]
v_pk_fma_f32 v[46:47],v[206:207],v[206:207],v[46:47]
v_smfmac_f32_16x16x64_bf16 v[216:219],a[144:147],a[64:71],v10
s_waitcnt lgkmcnt(4)
ds_read_b64 a[64:65],v28 offset:136
ds_read_b64 a[66:67],v28 offset:328
ds_read_b64 a[68:69],v29 offset:136
ds_read_b64 a[70:71],v29 offset:328
v_add_u32_sdwa v49,v12,v51 dst_sel:DWORD dst_unused:UNUSED_PAD src0_sel:WORD_1 src1_sel:DWORD
v_pk_add_f32 v[36:37],v[36:37],v[208:209]
v_pk_fma_f32 v[44:45],v[208:209],v[208:209],v[44:45]
v_pk_add_f32 v[38:39],v[38:39],v[210:211]
v_pk_fma_f32 v[46:47],v[210:211],v[210:211],v[46:47]
v_smfmac_f32_16x16x64_bf16 v[220:223],a[148:151],a[48:55],v10
s_waitcnt lgkmcnt(4)
ds_read_b64 a[48:49],v30 offset:0
ds_read_b64 a[50:51],v30 offset:192
ds_read_b64 a[52:53],v31 offset:0
ds_read_b64 a[54:55],v31 offset:192
v_add_u32_sdwa v51,v9,v50 dst_sel:DWORD dst_unused:UNUSED_PAD src0_sel:WORD_1 src1_sel:DWORD
v_pk_add_f32 v[36:37],v[36:37],v[212:213]
v_pk_fma_f32 v[44:45],v[212:213],v[212:213],v[44:45]
v_pk_add_f32 v[38:39],v[38:39],v[214:215]
v_pk_fma_f32 v[46:47],v[214:215],v[214:215],v[46:47]
v_smfmac_f32_16x16x64_bf16 v[224:227],a[176:179],a[56:63],v10
s_waitcnt lgkmcnt(4)
ds_read_b64 a[56:57],v30 offset:8
ds_read_b64 a[58:59],v30 offset:200
ds_read_b64 a[60:61],v31 offset:8
ds_read_b64 a[62:63],v31 offset:200
v_add_u32_sdwa v50,v9,v50 dst_sel:DWORD dst_unused:UNUSED_PAD src0_sel:WORD_0 src1_sel:DWORD
v_pk_add_f32 v[36:37],v[36:37],v[216:217]
v_pk_fma_f32 v[44:45],v[216:217],v[216:217],v[44:45]
v_pk_add_f32 v[38:39],v[38:39],v[218:219]
v_pk_fma_f32 v[46:47],v[218:219],v[218:219],v[46:47]
v_smfmac_f32_16x16x64_bf16 v[228:231],a[180:183],a[64:71],v10
s_waitcnt lgkmcnt(4)
ds_read_b64 a[64:65],v30 offset:64
ds_read_b64 a[66:67],v30 offset:256
ds_read_b64 a[68:69],v31 offset:64
ds_read_b64 a[70:71],v31 offset:256
v_pk_add_f32 v[36:37],v[36:37],v[220:221]
v_pk_fma_f32 v[44:45],v[220:221],v[220:221],v[44:45]
v_pk_add_f32 v[38:39],v[38:39],v[222:223]
v_pk_fma_f32 v[46:47],v[222:223],v[222:223],v[46:47]
v_smfmac_f32_16x16x64_bf16 v[232:235],a[120:123],a[48:55],v10
s_waitcnt lgkmcnt(4)
ds_read_b64 a[48:49],v30 offset:72
ds_read_b64 a[50:51],v30 offset:264
ds_read_b64 a[52:53],v31 offset:72
ds_read_b64 a[54:55],v31 offset:264
v_pk_add_f32 v[36:37],v[36:37],v[224:225]
v_pk_fma_f32 v[44:45],v[224:225],v[224:225],v[44:45]
v_pk_add_f32 v[38:39],v[38:39],v[226:227]
v_pk_fma_f32 v[46:47],v[226:227],v[226:227],v[46:47]
v_smfmac_f32_16x16x64_bf16 v[236:239],a[124:127],a[56:63],v10
s_waitcnt lgkmcnt(4)
ds_read_b64 a[56:57],v30 offset:128
ds_read_b64 a[58:59],v30 offset:320
ds_read_b64 a[60:61],v31 offset:128
ds_read_b64 a[62:63],v31 offset:320
v_pk_add_f32 v[36:37],v[36:37],v[228:229]
v_pk_fma_f32 v[44:45],v[228:229],v[228:229],v[44:45]
v_pk_add_f32 v[38:39],v[38:39],v[230:231]
v_pk_fma_f32 v[46:47],v[230:231],v[230:231],v[46:47]
v_smfmac_f32_16x16x64_bf16 v[240:243],a[152:155],a[64:71],v10
s_waitcnt lgkmcnt(4)
ds_read_b64 a[64:65],v30 offset:136
ds_read_b64 a[66:67],v30 offset:328
ds_read_b64 a[68:69],v31 offset:136
ds_read_b64 a[70:71],v31 offset:328
v_pk_add_f32 v[36:37],v[36:37],v[232:233]
v_pk_fma_f32 v[44:45],v[232:233],v[232:233],v[44:45]
v_pk_add_f32 v[38:39],v[38:39],v[234:235]
v_pk_fma_f32 v[46:47],v[234:235],v[234:235],v[46:47]
v_smfmac_f32_16x16x64_bf16 v[244:247],a[156:159],a[48:55],v10
s_waitcnt lgkmcnt(4)
ds_read_b64 a[224:225],v48 offset:57344
ds_read_b64 a[226:227],v48 offset:57352
ds_read_b64 a[228:229],v49 offset:57344
ds_read_b64 a[230:231],v49 offset:57352
ds_read_b128 a[240:243],v13 offset:0
v_pk_add_f32 v[36:37],v[36:37],v[236:237]
v_pk_fma_f32 v[44:45],v[236:237],v[236:237],v[44:45]
v_pk_add_f32 v[38:39],v[38:39],v[238:239]
v_pk_fma_f32 v[46:47],v[238:239],v[238:239],v[46:47]
v_smfmac_f32_16x16x64_bf16 v[248:251],a[184:187],a[56:63],v10
s_waitcnt lgkmcnt(5)
ds_read_b64 a[232:233],v50 offset:57344
ds_read_b64 a[234:235],v50 offset:57352
ds_read_b64 a[236:237],v51 offset:57344
ds_read_b64 a[238:239],v51 offset:57352
ds_read_b128 a[244:247],v13 offset:256
v_pk_add_f32 v[36:37],v[36:37],v[240:241]
v_pk_fma_f32 v[44:45],v[240:241],v[240:241],v[44:45]
v_pk_add_f32 v[38:39],v[38:39],v[242:243]
v_pk_fma_f32 v[46:47],v[242:243],v[242:243],v[46:47]
v_smfmac_f32_16x16x64_bf16 v[252:255],a[188:191],a[64:71],v10
ds_read_b128 v[28:31],v4 offset:3584
ds_read_b128 v[24:27],v4 offset:3616
v_pk_add_f32 v[36:37],v[36:37],v[244:245]
v_pk_fma_f32 v[44:45],v[244:245],v[244:245],v[44:45]
v_pk_add_f32 v[38:39],v[38:39],v[246:247]
v_pk_fma_f32 v[46:47],v[246:247],v[246:247],v[46:47]
v_pk_add_f32 v[36:37],v[36:37],v[248:249]
v_pk_fma_f32 v[44:45],v[248:249],v[248:249],v[44:45]
v_pk_add_f32 v[38:39],v[38:39],v[250:251]
v_pk_fma_f32 v[46:47],v[250:251],v[250:251],v[46:47]
v_pk_add_f32 v[36:37],v[36:37],v[252:253]
v_pk_fma_f32 v[44:45],v[252:253],v[252:253],v[44:45]
v_pk_add_f32 v[38:39],v[38:39],v[254:255]
v_pk_fma_f32 v[46:47],v[254:255],v[254:255],v[46:47]
s_nop 0
v_permlane32_swap_b32 v36,v38
v_permlane32_swap_b32 v37,v39
v_permlane32_swap_b32 v44,v46
v_permlane32_swap_b32_e64 v45,v47
v_pk_add_f32 v[36:37],v[36:37],v[38:39]
v_pk_add_f32 v[44:45],v[44:45],v[46:47]
s_add_u32 s60,s60,56576
s_sub_u32 s64,s60,88400
s_min_u32 s60,s60,s64
v_permlane16_swap_b32 v36,v37
v_permlane16_swap_b32 v44,v45
v_add_f32 v8,v36,v37
v_add_f32_e64 v9,v44,v45
v_mul_f32 v8,0x3c2aaaab,v8
v_mul_f32 v9,0x3c2aaaab,v9
v_cmp_ngt_f32 vcc,0x5d800000,v9
v_fma_f32 v9,-v8,v8,v9
v_max_f32 v9,0,v9
s_or_b64 s[90:91],s[90:91],vcc
v_add_f32 v9,0x3727c5ac,v9
v_rsq_f32 v9,v9
s_nop 0
v_mul_f32 v36,s96,v9
v_mul_f32 v37,s96,v9
v_mul_f32 v44,-v8,v36
v_mul_f32 v45,-v8,v36
v_permlane16_swap_b32 v36,v37
s_nop 0
v_permlane16_swap_b32 v44,v45
v_mov_b64 v[38:39],v[36:37]
v_mov_b64 v[46:47],v[44:45]
s_nop 0
v_permlane32_swap_b32 v36,v38
v_permlane32_swap_b32 v37,v39
v_permlane32_swap_b32 v44,v46
v_permlane32_swap_b32 v45,v47
v_pk_fma_f32 v[160:161],v[160:161],v[36:37],v[44:45]
v_pk_fma_f32 v[162:163],v[162:163],v[38:39],v[46:47]
v_pk_fma_f32 v[164:165],v[164:165],v[36:37],v[44:45]
v_pk_fma_f32 v[166:167],v[166:167],v[38:39],v[46:47]
v_pk_fma_f32 v[168:169],v[168:169],v[36:37],v[44:45]
v_pk_fma_f32 v[170:171],v[170:171],v[38:39],v[46:47]
v_pk_fma_f32 v[172:173],v[172:173],v[36:37],v[44:45]
v_pk_fma_f32 v[174:175],v[174:175],v[38:39],v[46:47]
v_pk_fma_f32 v[208:209],v[208:209],v[36:37],v[44:45]
v_pk_fma_f32 v[210:211],v[210:211],v[38:39],v[46:47]
v_pk_fma_f32 v[212:213],v[212:213],v[36:37],v[44:45]
v_pk_fma_f32 v[214:215],v[214:215],v[38:39],v[46:47]
v_pk_fma_f32 v[232:233],v[232:233],v[36:37],v[44:45]
v_pk_fma_f32 v[234:235],v[234:235],v[38:39],v[46:47]
v_pk_fma_f32 v[236:237],v[236:237],v[36:37],v[44:45]
v_pk_fma_f32 v[238:239],v[238:239],v[38:39],v[46:47]
v_pk_fma_f32 v[176:177],v[176:177],v[36:37],v[44:45]
v_pk_fma_f32 v[178:179],v[178:179],v[38:39],v[46:47]
v_pk_fma_f32 v[180:181],v[180:181],v[36:37],v[44:45]
v_pk_fma_f32 v[182:183],v[182:183],v[38:39],v[46:47]
v_pk_fma_f32 v[184:185],v[184:185],v[36:37],v[44:45]
v_pk_fma_f32 v[186:187],v[186:187],v[38:39],v[46:47]
v_pk_fma_f32 v[188:189],v[188:189],v[36:37],v[44:45]
v_pk_fma_f32 v[190:191],v[190:191],v[38:39],v[46:47]
v_pk_fma_f32 v[216:217],v[216:217],v[36:37],v[44:45]
v_pk_fma_f32 v[218:219],v[218:219],v[38:39],v[46:47]
v_pk_fma_f32 v[220:221],v[220:221],v[36:37],v[44:45]
v_pk_fma_f32 v[222:223],v[222:223],v[38:39],v[46:47]
v_pk_fma_f32 v[240:241],v[240:241],v[36:37],v[44:45]
v_pk_fma_f32 v[242:243],v[242:243],v[38:39],v[46:47]
v_pk_fma_f32 v[244:245],v[244:245],v[36:37],v[44:45]
v_pk_fma_f32 v[246:247],v[246:247],v[38:39],v[46:47]
v_pk_fma_f32 v[192:193],v[192:193],v[36:37],v[44:45]
v_pk_fma_f32 v[194:195],v[194:195],v[38:39],v[46:47]
v_pk_fma_f32 v[196:197],v[196:197],v[36:37],v[44:45]
v_pk_fma_f32 v[198:199],v[198:199],v[38:39],v[46:47]
v_pk_fma_f32 v[200:201],v[200:201],v[36:37],v[44:45]
v_pk_fma_f32 v[202:203],v[202:203],v[38:39],v[46:47]
v_pk_fma_f32 v[204:205],v[204:205],v[36:37],v[44:45]
v_pk_fma_f32 v[206:207],v[206:207],v[38:39],v[46:47]
v_pk_fma_f32 v[224:225],v[224:225],v[36:37],v[44:45]
v_pk_fma_f32 v[226:227],v[226:227],v[38:39],v[46:47]
v_pk_fma_f32 v[228:229],v[228:229],v[36:37],v[44:45]
v_pk_fma_f32 v[230:231],v[230:231],v[38:39],v[46:47]
v_pk_fma_f32 v[248:249],v[248:249],v[36:37],v[44:45]
v_pk_fma_f32 v[250:251],v[250:251],v[38:39],v[46:47]
v_pk_fma_f32 v[252:253],v[252:253],v[36:37],v[44:45]
v_pk_fma_f32 v[254:255],v[254:255],v[38:39],v[46:47]
v_mfma_f32_16x16x32_bf16 v[40:43],a[204:207],a[8:11],v[20:23]
ds_read_b64_tr_b16 a[216:217],v1 offset:6160
v_cvt_pk_bf16_f32 v160,v160,v164
v_cvt_pk_bf16_f32 v168,v168,v172
v_cvt_pk_bf16_f32 v208,v208,v212
v_lshlrev_b32_sdwa v60,1,v60 dst_sel:DWORD dst_unused:UNUSED_PAD src0_sel:DWORD src1_sel:WORD_1
v_mfma_f32_16x16x32_bf16 v[40:43],a[208:211],a[24:27],v[40:43]
ds_read_u16 v60,v60
v_accvgpr_write_b32 a48,v160
v_accvgpr_write_b32 a49,v168
v_cvt_pk_bf16_f32 v232,v232,v236
v_accvgpr_write_b32 a50,v208
v_mfma_f32_16x16x32_bf16 v[40:43],a[212:215],a[40:43],v[40:43]
ds_read_b64_tr_b16 a[218:219],v1 offset:7184
v_cvt_pk_bf16_f32 v161,v161,v165
v_accvgpr_write_b32 a51,v232
v_cvt_pk_bf16_f32 v169,v169,v173
v_accvgpr_write_b32 a52,v161
v_mfma_f32_16x16x32_bf16 v[36:39],a[200:203],a[40:43],v[16:19]
ds_read_b64_tr_b16 a[220:221],v1 offset:8208
v_cvt_pk_bf16_f32 v209,v209,v213
v_accvgpr_write_b32 a53,v169
v_cvt_pk_bf16_f32 v233,v233,v237
v_accvgpr_write_b32 a54,v209
v_lshlrev_b32_sdwa v62,1,v62 dst_sel:DWORD dst_unused:UNUSED_PAD src0_sel:DWORD src1_sel:WORD_1
v_mfma_f32_16x16x32_bf16 v[36:39],a[196:199],a[24:27],v[36:39]
ds_read_u16_d16_hi v62,v62
v_cvt_pk_bf16_f32 v162,v162,v166
v_accvgpr_write_b32 a55,v233
v_cvt_pk_bf16_f32 v170,v170,v174
v_accvgpr_write_b32 a56,v162
v_mfma_f32_16x16x32_bf16 v[36:39],a[192:195],a[8:11],v[36:39]
ds_read_b64_tr_b16 a[222:223],v1 offset:9232
v_accvgpr_write_b32 a57,v170
v_cvt_pk_bf16_f32 v210,v210,v214
v_accvgpr_write_b32 a58,v210
v_lshlrev_b32_sdwa v61,1,v61 dst_sel:DWORD dst_unused:UNUSED_PAD src0_sel:DWORD src1_sel:WORD_1
v_cvt_pk_bf16_f32 v234,v234,v238
v_mfma_f32_16x16x32_bf16 v[44:47],a[192:195],a[12:15],v[16:19]
ds_read_u16 v61,v61
v_accvgpr_write_b32 a59,v234
v_cvt_pk_bf16_f32 v163,v163,v167
v_accvgpr_write_b32 a60,v163
v_mfma_f32_16x16x32_bf16 v[44:47],a[196:199],a[28:31],v[44:47]
s_nop 0
v_cvt_pk_bf16_f32 v171,v171,v175
v_accvgpr_write_b32 a61,v171
v_cvt_pk_bf16_f32 v211,v211,v215
v_accvgpr_write_b32 a62,v211
v_mfma_f32_16x16x32_bf16 v[44:47],a[200:203],a[44:47],v[44:47]
s_waitcnt lgkmcnt(7)
v_cvt_pk_bf16_f32 v235,v235,v239
v_accvgpr_write_b32 a63,v235
v_cvt_pk_bf16_f32 v176,v176,v180
v_cvt_pk_bf16_f32 v184,v184,v188
v_lshlrev_b32_sdwa v63,1,v63 dst_sel:DWORD dst_unused:UNUSED_PAD src0_sel:DWORD src1_sel:WORD_1
v_mfma_f32_16x16x32_bf16 v[232:235],a[240:243],a[224:227],v[28:31]
ds_read_u16_d16_hi v63,v63
v_cvt_pk_bf16_f32 v216,v216,v220
v_accvgpr_write_b32 a64,v176
v_accvgpr_write_b32 a65,v184
v_cvt_pk_bf16_f32 v240,v240,v244
v_mfma_f32_16x16x32_bf16 v[236:239],a[240:243],a[228:231],v[28:31]
v_accvgpr_write_b32 a66,v216
v_cvt_pk_bf16_f32 v177,v177,v181
v_accvgpr_write_b32 a67,v240
v_cvt_pk_bf16_f32 v185,v185,v189
v_lshlrev_b32_sdwa v52,1,v52 dst_sel:DWORD dst_unused:UNUSED_PAD src0_sel:DWORD src1_sel:WORD_1
v_mfma_f32_16x16x32_bf16 v[208:211],a[240:243],a[232:235],v[28:31]
ds_read_u16 v52,v52
v_accvgpr_write_b32 a68,v177
v_cvt_pk_bf16_f32 v217,v217,v221
v_accvgpr_write_b32 a69,v185
v_cvt_pk_bf16_f32 v241,v241,v245
v_mfma_f32_16x16x32_bf16 v[212:215],a[240:243],a[236:239],v[28:31]
ds_read_b128 a[240:243],v13 offset:512
ds_read_b128 v[28:31],v4 offset:3712
v_accvgpr_write_b32 a70,v217
v_cvt_pk_bf16_f32 v178,v178,v182
v_accvgpr_write_b32 a71,v241
v_lshlrev_b32_sdwa v54,1,v54 dst_sel:DWORD dst_unused:UNUSED_PAD src0_sel:DWORD src1_sel:WORD_1
v_mfma_f32_16x16x32_bf16 v[160:163],a[244:247],a[224:227],v[24:27]
ds_read_b64 a[224:225],v48 offset:57408
ds_read_b64 a[226:227],v48 offset:57416
v_cvt_pk_bf16_f32 v186,v186,v190
v_accvgpr_write_b32 a72,v178
v_accvgpr_write_b32 a73,v186
v_cvt_pk_bf16_f32 v218,v218,v222
v_mfma_f32_16x16x32_bf16 v[164:167],a[244:247],a[228:231],v[24:27]
ds_read_b64 a[228:229],v49 offset:57408
ds_read_b64 a[230:231],v49 offset:57416
v_accvgpr_write_b32 a74,v218
v_cvt_pk_bf16_f32 v242,v242,v246
v_accvgpr_write_b32 a75,v242
v_cvt_pk_bf16_f32 v179,v179,v183
v_mfma_f32_16x16x32_bf16 v[168:171],a[244:247],a[232:235],v[24:27]
ds_read_b64 a[232:233],v50 offset:57408
ds_read_b64 a[234:235],v50 offset:57416
v_accvgpr_write_b32 a76,v179
v_cvt_pk_bf16_f32 v187,v187,v191
v_accvgpr_write_b32 a77,v187
v_cvt_pk_bf16_f32 v219,v219,v223
v_lshlrev_b32_sdwa v53,1,v53 dst_sel:DWORD dst_unused:UNUSED_PAD src0_sel:DWORD src1_sel:WORD_1
v_mfma_f32_16x16x32_bf16 v[172:175],a[244:247],a[236:239],v[24:27]
s_waitcnt lgkmcnt(4)
ds_read_b64 a[236:237],v51 offset:57408
ds_read_b64 a[238:239],v51 offset:57416
v_accvgpr_write_b32 a78,v219
v_cvt_pk_bf16_f32 v243,v243,v247
v_accvgpr_write_b32 a79,v243
v_cvt_pk_bf16_f32 v192,v192,v196
v_mfma_f32_16x16x32_bf16 v[240:243],a[240:243],a[224:227],v[28:31]
s_waitcnt lgkmcnt(4)
ds_read_b128 a[244:247],v13 offset:768
ds_read_b128 v[24:27],v4 offset:3744
v_cvt_pk_bf16_f32 v200,v200,v204
v_cvt_pk_bf16_f32 v224,v224,v228
v_accvgpr_write_b32 a80,v192
v_accvgpr_write_b32 a81,v200
v_lshlrev_b32_sdwa v55,1,v55 dst_sel:DWORD dst_unused:UNUSED_PAD src0_sel:DWORD src1_sel:WORD_1
v_mfma_f32_16x16x32_bf16 v[244:247],a[240:243],a[228:231],v[28:31]
s_waitcnt lgkmcnt(4)
ds_read_u16_d16_hi v54,v54
v_cvt_pk_bf16_f32 v248,v248,v252
v_accvgpr_write_b32 a82,v224
v_cvt_pk_bf16_f32 v193,v193,v197
v_mfma_f32_16x16x32_bf16 v[216:219],a[240:243],a[232:235],v[28:31]
s_waitcnt lgkmcnt(3)
ds_read_u16 v53,v53
v_accvgpr_write_b32 a83,v248
v_cvt_pk_bf16_f32 v201,v201,v205
v_accvgpr_write_b32 a84,v193
v_cvt_pk_bf16_f32 v225,v225,v229
v_mfma_f32_16x16x32_bf16 v[220:223],a[240:243],a[236:239],v[28:31]
s_waitcnt lgkmcnt(2)
ds_read_b128 a[240:243],v13 offset:1024
ds_read_b128 v[28:31],v4 offset:3840
v_accvgpr_write_b32 a85,v201
v_cvt_pk_bf16_f32 v249,v249,v253
v_accvgpr_write_b32 a86,v225
v_cvt_pk_bf16_f32 v194,v194,v198
v_lshlrev_b32_sdwa v56,1,v56 dst_sel:DWORD dst_unused:UNUSED_PAD src0_sel:DWORD src1_sel:WORD_1
v_mfma_f32_16x16x32_bf16 v[176:179],a[244:247],a[224:227],v[24:27]
s_nop 0
ds_read_b64 a[224:225],v48 offset:57472
ds_read_b64 a[226:227],v48 offset:57480
v_accvgpr_write_b32 a87,v249
v_cvt_pk_bf16_f32 v202,v202,v206
v_accvgpr_write_b32 a88,v194
v_accvgpr_write_b32 a89,v202
v_mfma_f32_16x16x32_bf16 v[180:183],a[244:247],a[228:231],v[24:27]
ds_read_b64 a[228:229],v49 offset:57472
ds_read_b64 a[230:231],v49 offset:57480
v_cvt_pk_bf16_f32 v226,v226,v230
v_accvgpr_write_b32 a90,v226
v_cvt_pk_bf16_f32 v250,v250,v254
v_accvgpr_write_b32 a91,v250
v_lshlrev_b32_sdwa v58,1,v58 dst_sel:DWORD dst_unused:UNUSED_PAD src0_sel:DWORD src1_sel:WORD_1
v_mfma_f32_16x16x32_bf16 v[184:187],a[244:247],a[232:235],v[24:27]
s_nop 0
ds_read_b64 a[232:233],v50 offset:57472
ds_read_b64 a[234:235],v50 offset:57480
v_cvt_pk_bf16_f32 v195,v195,v199
v_accvgpr_write_b32 a92,v195
v_cvt_pk_bf16_f32 v203,v203,v207
v_accvgpr_write_b32 a93,v203
v_mfma_f32_16x16x32_bf16 v[188:191],a[244:247],a[236:239],v[24:27]
s_waitcnt lgkmcnt(4)
ds_read_b64 a[236:237],v51 offset:57472
ds_read_b64 a[238:239],v51 offset:57480
v_cvt_pk_bf16_f32 v227,v227,v231
TP2751_%=:
s_cbranch_execz TP4868_%=
v_accvgpr_write_b32 a94,v227
v_cvt_pk_bf16_f32 v251,v251,v255
v_accvgpr_write_b32 a95,v251
v_lshlrev_b32_sdwa v57,1,v57 dst_sel:DWORD dst_unused:UNUSED_PAD src0_sel:DWORD src1_sel:WORD_1
v_mfma_f32_16x16x32_bf16 v[248:251],a[240:243],a[224:227],v[28:31]
s_waitcnt lgkmcnt(4)
ds_read_b128 a[244:247],v13 offset:1280
ds_read_b128 v[24:27],v4 offset:3872
v_mfma_f32_16x16x32_bf16 v[252:255],a[240:243],a[228:231],v[28:31]
s_waitcnt lgkmcnt(4)
ds_read_u16_d16_hi v55,v55
v_lshlrev_b32_sdwa v59,1,v59 dst_sel:DWORD dst_unused:UNUSED_PAD src0_sel:DWORD src1_sel:WORD_1
v_mfma_f32_16x16x32_bf16 v[224:227],a[240:243],a[232:235],v[28:31]
s_waitcnt lgkmcnt(3)
ds_read_u16 v56,v56
v_lshlrev_b32_sdwa v40,1,v40 dst_sel:DWORD dst_unused:UNUSED_PAD src0_sel:DWORD src1_sel:WORD_1
v_mfma_f32_16x16x32_bf16 v[228:231],a[240:243],a[236:239],v[28:31]
s_waitcnt lgkmcnt(2)
ds_read_b64_tr_b16 a[240:241],v3 offset:6224
ds_read_b64_tr_b16 a[242:243],v3 offset:7760
v_mfma_f32_16x16x32_bf16 v[192:195],a[244:247],a[224:227],v[24:27]
ds_read_b64_tr_b16 a[224:225],v3 offset:6232
ds_read_b64_tr_b16 a[226:227],v3 offset:7768
v_mfma_f32_16x16x32_bf16 v[196:199],a[244:247],a[228:231],v[24:27]
ds_read_b64_tr_b16 a[228:229],v3 offset:6288
ds_read_b64_tr_b16 a[230:231],v3 offset:7824
v_mfma_f32_16x16x32_bf16 v[200:203],a[244:247],a[232:235],v[24:27]
ds_read_b64_tr_b16 a[232:233],v3 offset:6296
ds_read_b64_tr_b16 a[234:235],v3 offset:7832
v_mfma_f32_16x16x32_bf16 v[204:207],a[244:247],a[236:239],v[24:27]
ds_read_u16_d16_hi v58,v58
v_lshlrev_b32_sdwa v42,1,v42 dst_sel:DWORD dst_unused:UNUSED_PAD src0_sel:DWORD src1_sel:WORD_1
v_mfma_f32_16x16x32_bf16 v[48:51],a[212:215],a[44:47],v[20:23]
ds_read_u16 v57,v57
v_lshlrev_b32_sdwa v41,1,v41 dst_sel:DWORD dst_unused:UNUSED_PAD src0_sel:DWORD src1_sel:WORD_1
v_mfma_f32_16x16x32_bf16 v[48:51],a[208:211],a[28:31],v[48:51]
s_waitcnt vmcnt(9)
ds_read_u16_d16_hi v59,v59
v_lshlrev_b32_sdwa v43,1,v43 dst_sel:DWORD dst_unused:UNUSED_PAD src0_sel:DWORD src1_sel:WORD_1
v_mfma_f32_16x16x32_bf16 v[48:51],a[204:207],a[12:15],v[48:51]
s_barrier
ds_read_u16 v40,v40
v_mfma_f32_16x16x32_bf16 v[28:31],a[204:207],a[48:51],v[20:23]
s_add_u32 m0,s57,56576
ds_read_b64 v[14:15],v7 offset:6160
v_lshlrev_b32_sdwa v36,1,v36 dst_sel:DWORD dst_unused:UNUSED_PAD src0_sel:DWORD src1_sel:WORD_1
v_mfma_f32_16x16x32_bf16 v[28:31],a[208:211],a[64:67],v[28:31]
s_add_u32 s89,s56,4030464
ds_read_u16_d16_hi v42,v42
v_lshl_add_u64 v[32:33],v[32:33],0,v[34:35]
v_mfma_f32_16x16x32_bf16 v[28:31],a[212:215],a[80:83],v[28:31]
buffer_load_dwordx4 v6,s[36:39],s89 offen lds
ds_read_u16 v41,v41
v_lshl_add_u64 v[34:35],v[60:61],0,v[62:63]
v_lshlrev_b32_sdwa v38,1,v38 dst_sel:DWORD dst_unused:UNUSED_PAD src0_sel:DWORD src1_sel:WORD_1
v_mfma_f32_16x16x32_bf16 v[24:27],a[200:203],a[80:83],v[16:19]
ds_read_u16_d16_hi v43,v43
v_lshlrev_b32_sdwa v37,1,v37 dst_sel:DWORD dst_unused:UNUSED_PAD src0_sel:DWORD src1_sel:WORD_1
v_mfma_f32_16x16x32_bf16 v[24:27],a[196:199],a[64:67],v[24:27]
s_waitcnt lgkmcnt(8)
ds_read_u16 v36,v36
v_lshl_add_u64 v[60:61],v[52:53],0,v[54:55]
v_mfma_f32_16x16x32_bf16 v[24:27],a[192:195],a[48:51],v[24:27]
s_waitcnt lgkmcnt(4)
ds_read_u16_d16_hi v38,v38
v_or3_b32 v13,v14,v15,0
v_lshlrev_b32_sdwa v39,1,v39 dst_sel:DWORD dst_unused:UNUSED_PAD src0_sel:DWORD src1_sel:WORD_1
v_mfma_f32_16x16x32_bf16 v[52:55],a[192:195],a[52:55],v[16:19]
ds_read_b64 v[14:15],v7 offset:6672
v_lshlrev_b32_sdwa v48,1,v48 dst_sel:DWORD dst_unused:UNUSED_PAD src0_sel:DWORD src1_sel:WORD_1
v_mfma_f32_16x16x32_bf16 v[52:55],a[196:199],a[68:71],v[52:55]
ds_read_u16 v37,v37
v_lshl_add_u64 v[62:63],v[56:57],0,v[58:59]
v_mfma_f32_16x16x32_bf16 v[52:55],a[200:203],a[84:87],v[52:55]
ds_read_u16_d16_hi v39,v39
v_lshlrev_b32_sdwa v50,1,v50 dst_sel:DWORD dst_unused:UNUSED_PAD src0_sel:DWORD src1_sel:WORD_1
v_mfma_f32_16x16x32_bf16 v[56:59],a[212:215],a[84:87],v[20:23]
ds_read_u16 v48,v48
v_lshlrev_b32_sdwa v49,1,v49 dst_sel:DWORD dst_unused:UNUSED_PAD src0_sel:DWORD src1_sel:WORD_1
v_mfma_f32_16x16x32_bf16 v[56:59],a[208:211],a[68:71],v[56:59]
ds_read_u16_d16_hi v50,v50
v_lshlrev_b32_sdwa v51,1,v51 dst_sel:DWORD dst_unused:UNUSED_PAD src0_sel:DWORD src1_sel:WORD_1
v_mfma_f32_16x16x32_bf16 v[56:59],a[204:207],a[52:55],v[56:59]
s_waitcnt lgkmcnt(4)
ds_read_u16 v49,v49
v_or3_b32 v13,v14,v15,v13
v_mfma_f32_16x16x32_bf16 v[136:139],a[248:251],v[32:35],v[136:139]
ds_read_b64 v[14:15],v7 offset:7184
v_lshlrev_b32_sdwa v44,1,v44 dst_sel:DWORD dst_unused:UNUSED_PAD src0_sel:DWORD src1_sel:WORD_1
v_mfma_f32_16x16x32_bf16 v[140:143],a[248:251],v[60:63],v[140:143]
ds_read_u16_d16_hi v51,v51
v_lshlrev_b32_sdwa v46,1,v46 dst_sel:DWORD dst_unused:UNUSED_PAD src0_sel:DWORD src1_sel:WORD_1
v_mfma_f32_16x16x32_bf16 v[68:71],a[252:255],v[60:63],v[68:71]
ds_read_u16 v44,v44
v_lshlrev_b32_sdwa v45,1,v45 dst_sel:DWORD dst_unused:UNUSED_PAD src0_sel:DWORD src1_sel:WORD_1
v_mfma_f32_16x16x32_bf16 v[64:67],a[252:255],v[32:35],v[64:67]
ds_read_u16_d16_hi v46,v46
v_mfma_f32_16x16x32_bf16 v[144:147],a[240:243],v[32:35],v[144:147]
ds_read_u16 v45,v45
v_lshlrev_b32_sdwa v47,1,v47 dst_sel:DWORD dst_unused:UNUSED_PAD src0_sel:DWORD src1_sel:WORD_1
v_mfma_f32_16x16x32_bf16 v[148:151],a[240:243],v[60:63],v[148:151]
s_waitcnt lgkmcnt(4)
ds_read_u16_d16_hi v47,v47
v_or3_b32 v13,v14,v15,v13
v_mfma_f32_16x16x32_bf16 v[84:87],a[224:227],v[60:63],v[84:87]
v_lshlrev_b32_sdwa v28,1,v28 dst_sel:DWORD dst_unused:UNUSED_PAD src0_sel:DWORD src1_sel:WORD_1
v_mfma_f32_16x16x32_bf16 v[80:83],a[224:227],v[32:35],v[80:83]
ds_read_u16 v28,v28
v_lshlrev_b32_sdwa v30,1,v30 dst_sel:DWORD dst_unused:UNUSED_PAD src0_sel:DWORD src1_sel:WORD_1
v_mfma_f32_16x16x32_bf16 v[152:155],a[228:231],v[32:35],v[152:155]
ds_read_u16_d16_hi v30,v30
v_lshlrev_b32_sdwa v29,1,v29 dst_sel:DWORD dst_unused:UNUSED_PAD src0_sel:DWORD src1_sel:WORD_1
v_mfma_f32_16x16x32_bf16 v[156:159],a[228:231],v[60:63],v[156:159]
ds_read_u16 v29,v29
v_mfma_f32_16x16x32_bf16 v[100:103],a[232:235],v[60:63],v[100:103]
v_lshlrev_b32_sdwa v31,1,v31 dst_sel:DWORD dst_unused:UNUSED_PAD src0_sel:DWORD src1_sel:WORD_1
v_mfma_f32_16x16x32_bf16 v[96:99],a[232:235],v[32:35],v[96:99]
s_add_u32 m0,s57,60120
ds_read_u16_d16_hi v31,v31
v_mfma_f32_16x16x32_bf16 v[60:63],a[204:207],a[56:59],v[20:23]
s_add_u32 s89,s56,4128768
v_lshlrev_b32_sdwa v24,1,v24 dst_sel:DWORD dst_unused:UNUSED_PAD src0_sel:DWORD src1_sel:WORD_1
v_fma_mixlo_f16 v8,v53,s99,0
v_mfma_f32_16x16x32_bf16 v[60:63],a[208:211],a[72:75],v[60:63]
buffer_load_dwordx4 v6,s[36:39],s89 offen lds
v_lshl_add_u64 v[40:41],v[40:41],0,v[42:43]
v_mfma_f32_16x16x32_bf16 v[60:63],a[212:215],a[88:91],v[60:63]
ds_read_u16 v24,v24
v_lshl_add_u64 v[42:43],v[36:37],0,v[38:39]
v_lshlrev_b32_sdwa v26,1,v26 dst_sel:DWORD dst_unused:UNUSED_PAD src0_sel:DWORD src1_sel:WORD_1
v_mfma_f32_16x16x32_bf16 v[32:35],a[200:203],a[88:91],v[16:19]
ds_read_u16_d16_hi v26,v26
v_fma_mixhi_f16 v8,v55,s99,0
v_mfma_f32_16x16x32_bf16 v[32:35],a[196:199],a[72:75],v[32:35]
s_waitcnt lgkmcnt(5)
v_lshl_add_u64 v[36:37],v[48:49],0,v[50:51]
v_lshlrev_b32_sdwa v25,1,v25 dst_sel:DWORD dst_unused:UNUSED_PAD src0_sel:DWORD src1_sel:WORD_1
v_pk_fma_f16 v9,v8,v8,s54
v_mfma_f32_16x16x32_bf16 v[32:35],a[192:195],a[56:59],v[32:35]
ds_read_u16 v25,v25
v_pk_mul_f16 v9,v8,v9
v_mfma_f32_16x16x32_bf16 v[48:51],a[192:195],a[60:63],v[16:19]
ds_read_b128 v[16:19],v4 offset:768
ds_read_b64_tr_b16 a[192:193],v1 offset:10256
v_lshlrev_b32_sdwa v27,1,v27 dst_sel:DWORD dst_unused:UNUSED_PAD src0_sel:DWORD src1_sel:WORD_1
v_mfma_f32_16x16x32_bf16 v[48:51],a[196:199],a[76:79],v[48:51]
ds_read_b64_tr_b16 a[194:195],v1 offset:11280
ds_read_b64_tr_b16 a[196:197],v2 offset:10256
v_lshl_add_u64 v[38:39],v[44:45],0,v[46:47]
v_mfma_f32_16x16x32_bf16 v[48:51],a[200:203],a[92:95],v[48:51]
ds_read_b64_tr_b16 a[198:199],v2 offset:11280
ds_read_b64_tr_b16 a[200:201],v2 offset:8208
v_exp_f16_e64 v8,v9
v_lshlrev_b32_sdwa v56,1,v56 dst_sel:DWORD dst_unused:UNUSED_PAD src0_sel:DWORD src1_sel:WORD_1
v_mfma_f32_16x16x32_bf16 v[44:47],a[212:215],a[92:95],v[20:23]
ds_read_b128 v[20:23],v4 offset:800
ds_read_b64_tr_b16 a[202:203],v2 offset:9232
v_mfma_f32_16x16x32_bf16 v[44:47],a[208:211],a[76:79],v[44:47]
ds_read_b64_tr_b16 a[212:213],v2 offset:6160
ds_read_b64_tr_b16 a[214:215],v2 offset:7184
v_lshlrev_b32_sdwa v58,1,v58 dst_sel:DWORD dst_unused:UNUSED_PAD src0_sel:DWORD src1_sel:WORD_1
v_mfma_f32_16x16x32_bf16 v[44:47],a[204:207],a[60:63],v[44:47]
v_exp_f16_sdwa v8,v9 dst_sel:WORD_1 dst_unused:UNUSED_PRESERVE src0_sel:WORD_1
v_mfma_f32_16x16x32_bf16 v[112:115],a[248:251],v[40:43],v[112:115]
ds_read_u16_d16_hi v27,v27
v_lshlrev_b32_sdwa v57,1,v57 dst_sel:DWORD dst_unused:UNUSED_PAD src0_sel:DWORD src1_sel:WORD_1
v_mfma_f32_16x16x32_bf16 v[116:119],a[248:251],v[36:39],v[116:119]
ds_read_u16 v56,v56
v_lshlrev_b32_sdwa v59,1,v59 dst_sel:DWORD dst_unused:UNUSED_PAD src0_sel:DWORD src1_sel:WORD_1
v_mfma_f32_16x16x32_bf16 v[76:79],a[252:255],v[36:39],v[76:79]
ds_read_u16_d16_hi v58,v58
v_pk_fma_f16 v8,v8,s55,s55
v_mfma_f32_16x16x32_bf16 v[72:75],a[252:255],v[40:43],v[72:75]
v_rcp_f16 v9,v8
v_lshlrev_b32_sdwa v52,1,v52 dst_sel:DWORD dst_unused:UNUSED_PAD src0_sel:DWORD src1_sel:WORD_1
v_mfma_f32_16x16x32_bf16 v[120:123],a[240:243],v[40:43],v[120:123]
ds_read_u16 v57,v57
v_lshlrev_b32_sdwa v54,1,v54 dst_sel:DWORD dst_unused:UNUSED_PAD src0_sel:DWORD src1_sel:WORD_1
v_mfma_f32_16x16x32_bf16 v[124:127],a[240:243],v[36:39],v[124:127]
ds_read_u16_d16_hi v59,v59
v_lshlrev_b32_sdwa v60,1,v60 dst_sel:DWORD dst_unused:UNUSED_PAD src0_sel:DWORD src1_sel:WORD_1
v_mfma_f32_16x16x32_bf16 v[92:95],a[224:227],v[36:39],v[92:95]
ds_read_u16 v52,v52
v_lshlrev_b32_sdwa v62,1,v62 dst_sel:DWORD dst_unused:UNUSED_PAD src0_sel:DWORD src1_sel:WORD_1
v_mfma_f32_16x16x32_bf16 v[88:91],a[224:227],v[40:43],v[88:91]
v_rcp_f16_sdwa v9,v8 dst_sel:WORD_1 dst_unused:UNUSED_PRESERVE src0_sel:WORD_1
v_mfma_f32_16x16x32_bf16 v[128:131],a[228:231],v[40:43],v[128:131]
ds_read_u16_d16_hi v54,v54
v_fma_mix_f32 v53,v53,v9,0 op_sel_hi:[0,1,0]
v_mfma_f32_16x16x32_bf16 v[132:135],a[228:231],v[36:39],v[132:135]
ds_read_u16 v60,v60
v_lshlrev_b32_sdwa v61,1,v61 dst_sel:DWORD dst_unused:UNUSED_PAD src0_sel:DWORD src1_sel:WORD_1
v_mfma_f32_16x16x32_bf16 v[108:111],a[232:235],v[36:39],v[108:111]
ds_read_u16_d16_hi v62,v62
v_fma_mix_f32 v55,v55,v9,0 op_sel:[0,1,0] op_sel_hi:[0,1,0]
v_mfma_f32_16x16x32_bf16 v[104:107],a[232:235],v[40:43],v[104:107]
s_waitcnt lgkmcnt(6)
ds_read_u16 v61,v61
v_lshlrev_b32_sdwa v63,1,v63 dst_sel:DWORD dst_unused:UNUSED_PAD src0_sel:DWORD src1_sel:WORD_1
v_mfma_f32_16x16x32_bf16 v[36:39],a[216:219],a[0:3],v[16:19]
s_add_u32 m0,s57,63656
ds_read_u16_d16_hi v63,v63
v_lshlrev_b32_sdwa v32,1,v32 dst_sel:DWORD dst_unused:UNUSED_PAD src0_sel:DWORD src1_sel:WORD_1
v_mfma_f32_16x16x32_bf16 v[36:39],a[220:223],a[16:19],v[36:39]
s_add_u32 s89,s56,4227072
ds_read_u16 v32,v32
v_lshl_add_u64 v[28:29],v[28:29],0,v[30:31]
v_mfma_f32_16x16x32_bf16 v[36:39],a[192:195],a[32:35],v[36:39]
buffer_load_dwordx4 v6,s[36:39],s89 offen lds
v_lshl_add_u64 v[30:31],v[24:25],0,v[26:27]
v_lshlrev_b32_sdwa v34,1,v34 dst_sel:DWORD dst_unused:UNUSED_PAD src0_sel:DWORD src1_sel:WORD_1
v_mfma_f32_16x16x32_bf16 v[40:43],a[196:199],a[32:35],v[20:23]
ds_read_u16_d16_hi v34,v34
v_lshlrev_b32_sdwa v33,1,v33 dst_sel:DWORD dst_unused:UNUSED_PAD src0_sel:DWORD src1_sel:WORD_1
v_mfma_f32_16x16x32_bf16 v[40:43],a[200:203],a[16:19],v[40:43]
s_waitcnt lgkmcnt(7)
ds_read_u16 v33,v33
v_lshl_add_u64 v[24:25],v[56:57],0,v[58:59]
v_mfma_f32_16x16x32_bf16 v[40:43],a[212:215],a[0:3],v[40:43]
v_lshlrev_b32_sdwa v35,1,v35 dst_sel:DWORD dst_unused:UNUSED_PAD src0_sel:DWORD src1_sel:WORD_1
v_lshlrev_b32_sdwa v44,1,v44 dst_sel:DWORD dst_unused:UNUSED_PAD src0_sel:DWORD src1_sel:WORD_1
v_mfma_f32_16x16x32_bf16 v[56:59],a[212:215],a[4:7],v[20:23]
s_waitcnt lgkmcnt(6)
ds_read_u16_d16_hi v35,v35
v_or_b32 v26,v52,v54
v_mfma_f32_16x16x32_bf16 v[56:59],a[200:203],a[20:23],v[56:59]
ds_read_u16 v44,v44
v_cvt_pk_bf16_f32 v27,v53,v55
v_mfma_f32_16x16x32_bf16 v[56:59],a[196:199],a[36:39],v[56:59]
v_lshlrev_b32_sdwa v46,1,v46 dst_sel:DWORD dst_unused:UNUSED_PAD src0_sel:DWORD src1_sel:WORD_1
v_lshlrev_b32_sdwa v45,1,v45 dst_sel:DWORD dst_unused:UNUSED_PAD src0_sel:DWORD src1_sel:WORD_1
v_mfma_f32_16x16x32_bf16 v[52:55],a[192:195],a[36:39],v[16:19]
ds_read_u16_d16_hi v46,v46
v_lshlrev_b32_sdwa v47,1,v47 dst_sel:DWORD dst_unused:UNUSED_PAD src0_sel:DWORD src1_sel:WORD_1
v_mfma_f32_16x16x32_bf16 v[52:55],a[220:223],a[20:23],v[52:55]
ds_read_u16 v45,v45
v_mfma_f32_16x16x32_bf16 v[52:55],a[216:219],a[4:7],v[52:55]
ds_read_u16_d16_hi v47,v47
v_lshlrev_b32_sdwa v48,1,v48 dst_sel:DWORD dst_unused:UNUSED_PAD src0_sel:DWORD src1_sel:WORD_1
v_mfma_f32_16x16x32_bf16 v[232:235],a[248:251],v[28:31],v[232:235]
ds_read_u16 v48,v48
v_lshlrev_b32_sdwa v50,1,v50 dst_sel:DWORD dst_unused:UNUSED_PAD src0_sel:DWORD src1_sel:WORD_1
v_mfma_f32_16x16x32_bf16 v[236:239],a[248:251],v[24:27],v[236:239]
ds_read_u16_d16_hi v50,v50
v_mfma_f32_16x16x32_bf16 v[164:167],a[252:255],v[24:27],v[164:167]
ds_read_b64_tr_b16 a[236:237],v3 offset:12320
v_lshlrev_b32_sdwa v49,1,v49 dst_sel:DWORD dst_unused:UNUSED_PAD src0_sel:DWORD src1_sel:WORD_1
v_mfma_f32_16x16x32_bf16 v[160:163],a[252:255],v[28:31],v[160:163]
ds_read_u16 v49,v49
v_mfma_f32_16x16x32_bf16 v[240:243],a[240:243],v[28:31],v[240:243]
ds_read_b64_tr_b16 a[238:239],v3 offset:13856
v_lshlrev_b32_sdwa v51,1,v51 dst_sel:DWORD dst_unused:UNUSED_PAD src0_sel:DWORD src1_sel:WORD_1
v_mfma_f32_16x16x32_bf16 v[244:247],a[240:243],v[24:27],v[244:247]
ds_read_u16_d16_hi v51,v51
v_lshlrev_b32_sdwa v36,1,v36 dst_sel:DWORD dst_unused:UNUSED_PAD src0_sel:DWORD src1_sel:WORD_1
v_mfma_f32_16x16x32_bf16 v[180:183],a[224:227],v[24:27],v[180:183]
ds_read_u16 v36,v36
v_mfma_f32_16x16x32_bf16 v[176:179],a[224:227],v[28:31],v[176:179]
s_add_u32 m0,s57,67184
ds_read_b64_tr_b16 a[244:245],v3 offset:12328
v_lshlrev_b32_sdwa v38,1,v38 dst_sel:DWORD dst_unused:UNUSED_PAD src0_sel:DWORD src1_sel:WORD_1
v_mfma_f32_16x16x32_bf16 v[248:251],a[228:231],v[28:31],v[248:251]
s_add_u32 s89,s56,4325376
ds_read_u16_d16_hi v38,v38
v_lshlrev_b32_sdwa v37,1,v37 dst_sel:DWORD dst_unused:UNUSED_PAD src0_sel:DWORD src1_sel:WORD_1
v_mfma_f32_16x16x32_bf16 v[252:255],a[228:231],v[24:27],v[252:255]
buffer_load_dwordx4 v6,s[36:39],s89 offen lds
v_mfma_f32_16x16x32_bf16 v[196:199],a[232:235],v[24:27],v[196:199]
ds_read_u16 v37,v37
v_lshlrev_b32_sdwa v39,1,v39 dst_sel:DWORD dst_unused:UNUSED_PAD src0_sel:DWORD src1_sel:WORD_1
v_mfma_f32_16x16x32_bf16 v[192:195],a[232:235],v[28:31],v[192:195]
ds_read_u16_d16_hi v39,v39
v_mfma_f32_16x16x32_bf16 v[24:27],a[216:219],a[8:11],v[16:19]
ds_read_b64_tr_b16 a[246:247],v3 offset:13864
v_lshlrev_b32_sdwa v40,1,v40 dst_sel:DWORD dst_unused:UNUSED_PAD src0_sel:DWORD src1_sel:WORD_1
v_mfma_f32_16x16x32_bf16 v[24:27],a[220:223],a[24:27],v[24:27]
s_waitcnt lgkmcnt(7)
ds_read_u16 v40,v40
v_lshl_add_u64 v[60:61],v[60:61],0,v[62:63]
v_fma_mixlo_f16 v8,v57,s99,0
v_mfma_f32_16x16x32_bf16 v[24:27],a[192:195],a[40:43],v[24:27]
ds_read_b64_tr_b16 a[208:209],v1 offset:12320
v_lshl_add_u64 v[62:63],v[32:33],0,v[34:35]
v_lshlrev_b32_sdwa v42,1,v42 dst_sel:DWORD dst_unused:UNUSED_PAD src0_sel:DWORD src1_sel:WORD_1
v_mfma_f32_16x16x32_bf16 v[28:31],a[196:199],a[40:43],v[20:23]
ds_read_u16_d16_hi v42,v42
v_fma_mixhi_f16 v8,v59,s99,0
v_mfma_f32_16x16x32_bf16 v[28:31],a[200:203],a[24:27],v[28:31]
ds_read_b64_tr_b16 a[210:211],v1 offset:13344
v_lshl_add_u64 v[32:33],v[44:45],0,v[46:47]
v_lshlrev_b32_sdwa v41,1,v41 dst_sel:DWORD dst_unused:UNUSED_PAD src0_sel:DWORD src1_sel:WORD_1
v_pk_fma_f16 v9,v8,v8,s54
v_mfma_f32_16x16x32_bf16 v[28:31],a[212:215],a[8:11],v[28:31]
ds_read_u16 v41,v41
v_pk_mul_f16 v9,v8,v9
v_mfma_f32_16x16x32_bf16 v[44:47],a[212:215],a[12:15],v[20:23]
ds_read_b64_tr_b16 a[204:205],v1 offset:14368
v_lshlrev_b32_sdwa v43,1,v43 dst_sel:DWORD dst_unused:UNUSED_PAD src0_sel:DWORD src1_sel:WORD_1
v_mfma_f32_16x16x32_bf16 v[44:47],a[200:203],a[28:31],v[44:47]
s_waitcnt lgkmcnt(8)
ds_read_u16_d16_hi v43,v43
v_lshl_add_u64 v[34:35],v[48:49],0,v[50:51]
v_mfma_f32_16x16x32_bf16 v[44:47],a[196:199],a[44:47],v[44:47]
v_exp_f16 v8,v9
v_lshlrev_b32_sdwa v52,1,v52 dst_sel:DWORD dst_unused:UNUSED_PAD src0_sel:DWORD src1_sel:WORD_1
v_mfma_f32_16x16x32_bf16 v[48:51],a[192:195],a[44:47],v[16:19]
ds_read_u16 v52,v52
v_lshlrev_b32_sdwa v54,1,v54 dst_sel:DWORD dst_unused:UNUSED_PAD src0_sel:DWORD src1_sel:WORD_1
v_mfma_f32_16x16x32_bf16 v[48:51],a[220:223],a[28:31],v[48:51]
ds_read_u16_d16_hi v54,v54
v_lshlrev_b32_sdwa v53,1,v53 dst_sel:DWORD dst_unused:UNUSED_PAD src0_sel:DWORD src1_sel:WORD_1
v_mfma_f32_16x16x32_bf16 v[48:51],a[216:219],a[12:15],v[48:51]
v_exp_f16_sdwa v8,v9 dst_sel:WORD_1 dst_unused:UNUSED_PRESERVE src0_sel:WORD_1
v_mfma_f32_16x16x32_bf16 v[208:211],a[248:251],v[60:63],v[208:211]
ds_read_u16 v53,v53
v_lshlrev_b32_sdwa v55,1,v55 dst_sel:DWORD dst_unused:UNUSED_PAD src0_sel:DWORD src1_sel:WORD_1
v_mfma_f32_16x16x32_bf16 v[212:215],a[248:251],v[32:35],v[212:215]
ds_read_u16_d16_hi v55,v55
v_pk_fma_f16 v8,v8,s55,s55
v_mfma_f32_16x16x32_bf16 v[172:175],a[252:255],v[32:35],v[172:175]
ds_read_b64_tr_b16 a[248:249],v3 offset:12384
v_lshlrev_b32_sdwa v56,1,v56 dst_sel:DWORD dst_unused:UNUSED_PAD src0_sel:DWORD src1_sel:WORD_1
v_mfma_f32_16x16x32_bf16 v[168:171],a[252:255],v[60:63],v[168:171]
ds_read_u16 v56,v56
v_rcp_f16 v9,v8
v_mfma_f32_16x16x32_bf16 v[216:219],a[240:243],v[60:63],v[216:219]
ds_read_b64_tr_b16 a[250:251],v3 offset:13920
v_lshlrev_b32_sdwa v58,1,v58 dst_sel:DWORD dst_unused:UNUSED_PAD src0_sel:DWORD src1_sel:WORD_1
v_mfma_f32_16x16x32_bf16 v[220:223],a[240:243],v[32:35],v[220:223]
ds_read_u16_d16_hi v58,v58
ds_read_b64_tr_b16 a[252:253],v3 offset:12392
v_mfma_f32_16x16x32_bf16 v[188:191],a[224:227],v[32:35],v[188:191]
ds_read_b64_tr_b16 a[254:255],v3 offset:13928
v_lshlrev_b32_sdwa v24,1,v24 dst_sel:DWORD dst_unused:UNUSED_PAD src0_sel:DWORD src1_sel:WORD_1
v_mfma_f32_16x16x32_bf16 v[184:187],a[224:227],v[60:63],v[184:187]
ds_read_u16 v24,v24
v_rcp_f16_sdwa v9,v8 dst_sel:WORD_1 dst_unused:UNUSED_PRESERVE src0_sel:WORD_1
v_mfma_f32_16x16x32_bf16 v[224:227],a[228:231],v[60:63],v[224:227]
ds_read_b64_tr_b16 a[240:241],v3 offset:12448
v_lshlrev_b32_sdwa v26,1,v26 dst_sel:DWORD dst_unused:UNUSED_PAD src0_sel:DWORD src1_sel:WORD_1
v_mfma_f32_16x16x32_bf16 v[228:231],a[228:231],v[32:35],v[228:231]
ds_read_u16_d16_hi v26,v26
v_fma_mix_f32 v57,v57,v9,0 op_sel_hi:[0,1,0]
v_mfma_f32_16x16x32_bf16 v[204:207],a[232:235],v[32:35],v[204:207]
ds_read_b64_tr_b16 a[242:243],v3 offset:13984
v_lshlrev_b32_sdwa v25,1,v25 dst_sel:DWORD dst_unused:UNUSED_PAD src0_sel:DWORD src1_sel:WORD_1
v_mfma_f32_16x16x32_bf16 v[200:203],a[232:235],v[60:63],v[200:203]
ds_read_u16 v25,v25
v_fma_mix_f32 v59,v59,v9,0 op_sel:[0,1,0] op_sel_hi:[0,1,0]
v_lshlrev_b32_sdwa v27,1,v27 dst_sel:DWORD dst_unused:UNUSED_PAD src0_sel:DWORD src1_sel:WORD_1
v_mfma_f32_16x16x32_bf16 v[32:35],a[216:219],a[48:51],v[16:19]
s_add_u32 m0,s57,70720
ds_read_b64 v[14:15],v7 offset:12320
ds_read_u16_d16_hi v27,v27
v_lshlrev_b32_sdwa v28,1,v28 dst_sel:DWORD dst_unused:UNUSED_PAD src0_sel:DWORD src1_sel:WORD_1
v_mfma_f32_16x16x32_bf16 v[32:35],a[220:223],a[64:67],v[32:35]
s_waitcnt lgkmcnt(10)
ds_read_b64_tr_b16 a[224:225],v3 offset:12456
v_lshl_add_u64 v[36:37],v[36:37],0,v[38:39]
v_mfma_f32_16x16x32_bf16 v[32:35],a[192:195],a[80:83],v[32:35]
s_add_u32 s89,s56,4423680
ds_read_b64_tr_b16 a[226:227],v3 offset:13992
v_lshl_add_u64 v[38:39],v[40:41],0,v[42:43]
v_lshlrev_b32_sdwa v30,1,v30 dst_sel:DWORD dst_unused:UNUSED_PAD src0_sel:DWORD src1_sel:WORD_1
v_mfma_f32_16x16x32_bf16 v[60:63],a[196:199],a[80:83],v[20:23]
buffer_load_dwordx4 v6,s[36:39],s89 offen lds
v_lshlrev_b32_sdwa v29,1,v29 dst_sel:DWORD dst_unused:UNUSED_PAD src0_sel:DWORD src1_sel:WORD_1
v_mfma_f32_16x16x32_bf16 v[60:63],a[200:203],a[64:67],v[60:63]
ds_read_u16 v28,v28
v_lshl_add_u64 v[40:41],v[52:53],0,v[54:55]
v_mfma_f32_16x16x32_bf16 v[60:63],a[212:215],a[48:51],v[60:63]
s_waitcnt lgkmcnt(4)
ds_read_u16_d16_hi v30,v30
v_or3_b32 v13,v14,v15,v13
v_lshlrev_b32_sdwa v31,1,v31 dst_sel:DWORD dst_unused:UNUSED_PAD src0_sel:DWORD src1_sel:WORD_1
v_mfma_f32_16x16x32_bf16 v[52:55],a[212:215],a[52:55],v[20:23]
ds_read_b64 v[14:15],v7 offset:12832
v_or_b32 v42,v56,v58
v_mfma_f32_16x16x32_bf16 v[52:55],a[200:203],a[68:71],v[52:55]
ds_read_u16 v29,v29
v_cvt_pk_bf16_f32 v43,v57,v59
v_lshlrev_b32_sdwa v48,1,v48 dst_sel:DWORD dst_unused:UNUSED_PAD src0_sel:DWORD src1_sel:WORD_1
v_mfma_f32_16x16x32_bf16 v[52:55],a[196:199],a[84:87],v[52:55]
ds_read_u16_d16_hi v31,v31
v_lshlrev_b32_sdwa v50,1,v50 dst_sel:DWORD dst_unused:UNUSED_PAD src0_sel:DWORD src1_sel:WORD_1
v_mfma_f32_16x16x32_bf16 v[56:59],a[192:195],a[84:87],v[16:19]
ds_read_u16 v48,v48
v_lshlrev_b32_sdwa v49,1,v49 dst_sel:DWORD dst_unused:UNUSED_PAD src0_sel:DWORD src1_sel:WORD_1
v_mfma_f32_16x16x32_bf16 v[56:59],a[220:223],a[68:71],v[56:59]
ds_read_u16_d16_hi v50,v50
v_lshlrev_b32_sdwa v51,1,v51 dst_sel:DWORD dst_unused:UNUSED_PAD src0_sel:DWORD src1_sel:WORD_1
v_mfma_f32_16x16x32_bf16 v[56:59],a[216:219],a[52:55],v[56:59]
s_waitcnt lgkmcnt(4)
ds_read_u16 v49,v49
v_or3_b32 v13,v14,v15,v13
v_mfma_f32_16x16x32_bf16 v[136:139],a[236:239],v[36:39],v[136:139]
ds_read_b64 v[14:15],v7 offset:13344
v_lshlrev_b32_sdwa v44,1,v44 dst_sel:DWORD dst_unused:UNUSED_PAD src0_sel:DWORD src1_sel:WORD_1
v_mfma_f32_16x16x32_bf16 v[140:143],a[236:239],v[40:43],v[140:143]
ds_read_u16_d16_hi v51,v51
v_lshlrev_b32_sdwa v46,1,v46 dst_sel:DWORD dst_unused:UNUSED_PAD src0_sel:DWORD src1_sel:WORD_1
v_mfma_f32_16x16x32_bf16 v[68:71],a[244:247],v[40:43],v[68:71]
s_add_u32 m0,s57,74264
ds_read_u16 v44,v44
v_lshlrev_b32_sdwa v45,1,v45 dst_sel:DWORD dst_unused:UNUSED_PAD src0_sel:DWORD src1_sel:WORD_1
v_mfma_f32_16x16x32_bf16 v[64:67],a[244:247],v[36:39],v[64:67]
s_add_u32 s89,s56,4521984
ds_read_u16_d16_hi v46,v46
v_mfma_f32_16x16x32_bf16 v[144:147],a[248:251],v[36:39],v[144:147]
buffer_load_dwordx4 v6,s[36:39],s89 offen lds
v_lshlrev_b32_sdwa v47,1,v47 dst_sel:DWORD dst_unused:UNUSED_PAD src0_sel:DWORD src1_sel:WORD_1
v_mfma_f32_16x16x32_bf16 v[148:151],a[248:251],v[40:43],v[148:151]
s_waitcnt lgkmcnt(3)
ds_read_u16 v45,v45
v_or3_b32 v13,v14,v15,v13
v_mfma_f32_16x16x32_bf16 v[84:87],a[252:255],v[40:43],v[84:87]
ds_read_u16_d16_hi v47,v47
v_lshlrev_b32_sdwa v32,1,v32 dst_sel:DWORD dst_unused:UNUSED_PAD src0_sel:DWORD src1_sel:WORD_1
v_mfma_f32_16x16x32_bf16 v[80:83],a[252:255],v[36:39],v[80:83]
ds_read_u16 v32,v32
v_lshlrev_b32_sdwa v34,1,v34 dst_sel:DWORD dst_unused:UNUSED_PAD src0_sel:DWORD src1_sel:WORD_1
v_mfma_f32_16x16x32_bf16 v[152:155],a[240:243],v[36:39],v[152:155]
ds_read_u16_d16_hi v34,v34
v_lshlrev_b32_sdwa v33,1,v33 dst_sel:DWORD dst_unused:UNUSED_PAD src0_sel:DWORD src1_sel:WORD_1
v_mfma_f32_16x16x32_bf16 v[156:159],a[240:243],v[40:43],v[156:159]
ds_read_u16 v33,v33
v_mfma_f32_16x16x32_bf16 v[100:103],a[224:227],v[40:43],v[100:103]
ds_read_b64_tr_b16 a[206:207],v1 offset:15392
v_lshlrev_b32_sdwa v35,1,v35 dst_sel:DWORD dst_unused:UNUSED_PAD src0_sel:DWORD src1_sel:WORD_1
v_mfma_f32_16x16x32_bf16 v[96:99],a[224:227],v[36:39],v[96:99]
ds_read_u16_d16_hi v35,v35
v_mfma_f32_16x16x32_bf16 v[40:43],a[216:219],a[56:59],v[16:19]
v_lshlrev_b32_sdwa v60,1,v60 dst_sel:DWORD dst_unused:UNUSED_PAD src0_sel:DWORD src1_sel:WORD_1
v_fma_mixlo_f16 v8,v53,s99,0
v_mfma_f32_16x16x32_bf16 v[40:43],a[220:223],a[72:75],v[40:43]
ds_read_u16 v60,v60
v_lshl_add_u64 v[24:25],v[24:25],0,v[26:27]
v_mfma_f32_16x16x32_bf16 v[40:43],a[192:195],a[88:91],v[40:43]
v_lshl_add_u64 v[26:27],v[28:29],0,v[30:31]
v_lshlrev_b32_sdwa v62,1,v62 dst_sel:DWORD dst_unused:UNUSED_PAD src0_sel:DWORD src1_sel:WORD_1
v_mfma_f32_16x16x32_bf16 v[36:39],a[196:199],a[88:91],v[20:23]
ds_read_u16_d16_hi v62,v62
v_fma_mixhi_f16 v8,v55,s99,0
v_mfma_f32_16x16x32_bf16 v[36:39],a[200:203],a[72:75],v[36:39]
s_waitcnt lgkmcnt(6)
v_lshl_add_u64 v[28:29],v[48:49],0,v[50:51]
v_lshlrev_b32_sdwa v61,1,v61 dst_sel:DWORD dst_unused:UNUSED_PAD src0_sel:DWORD src1_sel:WORD_1
v_pk_fma_f16 v9,v8,v8,s54
v_mfma_f32_16x16x32_bf16 v[36:39],a[212:215],a[56:59],v[36:39]
ds_read_u16 v61,v61
v_pk_mul_f16 v9,v8,v9
v_mfma_f32_16x16x32_bf16 v[48:51],a[212:215],a[60:63],v[20:23]
ds_read_b128 v[20:23],v4 offset:896
v_lshlrev_b32_sdwa v63,1,v63 dst_sel:DWORD dst_unused:UNUSED_PAD src0_sel:DWORD src1_sel:WORD_1
v_mfma_f32_16x16x32_bf16 v[48:51],a[200:203],a[76:79],v[48:51]
ds_read_b64_tr_b16 a[212:213],v1 offset:16416
ds_read_b64_tr_b16 a[214:215],v1 offset:17440
v_lshl_add_u64 v[30:31],v[44:45],0,v[46:47]
v_mfma_f32_16x16x32_bf16 v[48:51],a[196:199],a[92:95],v[48:51]
ds_read_u16_d16_hi v63,v63
v_exp_f16 v8,v9
v_lshlrev_b32_sdwa v56,1,v56 dst_sel:DWORD dst_unused:UNUSED_PAD src0_sel:DWORD src1_sel:WORD_1
v_mfma_f32_16x16x32_bf16 v[44:47],a[192:195],a[92:95],v[16:19]
ds_read_b128 v[16:19],v4 offset:928
ds_read_b64_tr_b16 a[200:201],v2 offset:16416
v_mfma_f32_16x16x32_bf16 v[44:47],a[220:223],a[76:79],v[44:47]
ds_read_b64_tr_b16 a[202:203],v2 offset:17440
ds_read_b64_tr_b16 a[196:197],v2 offset:14368
v_lshlrev_b32_sdwa v58,1,v58 dst_sel:DWORD dst_unused:UNUSED_PAD src0_sel:DWORD src1_sel:WORD_1
v_mfma_f32_16x16x32_bf16 v[44:47],a[216:219],a[60:63],v[44:47]
ds_read_b64_tr_b16 a[198:199],v2 offset:15392
v_exp_f16_sdwa v8,v9 dst_sel:WORD_1 dst_unused:UNUSED_PRESERVE src0_sel:WORD_1
v_mfma_f32_16x16x32_bf16 v[112:115],a[236:239],v[24:27],v[112:115]
ds_read_u16 v56,v56
v_lshlrev_b32_sdwa v57,1,v57 dst_sel:DWORD dst_unused:UNUSED_PAD src0_sel:DWORD src1_sel:WORD_1
v_mfma_f32_16x16x32_bf16 v[116:119],a[236:239],v[28:31],v[116:119]
ds_read_u16_d16_hi v58,v58
v_lshlrev_b32_sdwa v59,1,v59 dst_sel:DWORD dst_unused:UNUSED_PAD src0_sel:DWORD src1_sel:WORD_1
v_mfma_f32_16x16x32_bf16 v[76:79],a[244:247],v[28:31],v[76:79]
ds_read_u16 v57,v57
v_pk_fma_f16 v8,v8,s55,s55
v_mfma_f32_16x16x32_bf16 v[72:75],a[244:247],v[24:27],v[72:75]
v_rcp_f16_e64 v9,v8
v_lshlrev_b32_sdwa v52,1,v52 dst_sel:DWORD dst_unused:UNUSED_PAD src0_sel:DWORD src1_sel:WORD_1
v_mfma_f32_16x16x32_bf16 v[120:123],a[248:251],v[24:27],v[120:123]
ds_read_u16_d16_hi v59,v59
v_lshlrev_b32_sdwa v54,1,v54 dst_sel:DWORD dst_unused:UNUSED_PAD src0_sel:DWORD src1_sel:WORD_1
v_mfma_f32_16x16x32_bf16 v[124:127],a[248:251],v[28:31],v[124:127]
ds_read_b64_tr_b16 a[192:193],v2 offset:12320
v_lshlrev_b32_sdwa v40,1,v40 dst_sel:DWORD dst_unused:UNUSED_PAD src0_sel:DWORD src1_sel:WORD_1
v_mfma_f32_16x16x32_bf16 v[92:95],a[252:255],v[28:31],v[92:95]
ds_read_b64_tr_b16 a[194:195],v2 offset:13344
v_lshlrev_b32_sdwa v42,1,v42 dst_sel:DWORD dst_unused:UNUSED_PAD src0_sel:DWORD src1_sel:WORD_1
v_mfma_f32_16x16x32_bf16 v[88:91],a[252:255],v[24:27],v[88:91]
ds_read_u16 v52,v52
v_rcp_f16_sdwa v9,v8 dst_sel:WORD_1 dst_unused:UNUSED_PRESERVE src0_sel:WORD_1
v_mfma_f32_16x16x32_bf16 v[128:131],a[240:243],v[24:27],v[128:131]
ds_read_u16_d16_hi v54,v54
v_fma_mix_f32 v53,v53,v9,0 op_sel_hi:[0,1,0]
v_mfma_f32_16x16x32_bf16 v[132:135],a[240:243],v[28:31],v[132:135]
ds_read_u16 v40,v40
v_lshlrev_b32_sdwa v41,1,v41 dst_sel:DWORD dst_unused:UNUSED_PAD src0_sel:DWORD src1_sel:WORD_1
v_mfma_f32_16x16x32_bf16 v[108:111],a[224:227],v[28:31],v[108:111]
ds_read_u16_d16_hi v42,v42
v_fma_mix_f32 v55,v55,v9,0 op_sel:[0,1,0] op_sel_hi:[0,1,0]
v_mfma_f32_16x16x32_bf16 v[104:107],a[224:227],v[24:27],v[104:107]
s_waitcnt lgkmcnt(7)
ds_read_u16 v41,v41
v_lshlrev_b32_sdwa v43,1,v43 dst_sel:DWORD dst_unused:UNUSED_PAD src0_sel:DWORD src1_sel:WORD_1
v_mfma_f32_16x16x32_bf16 v[28:31],a[208:211],a[0:3],v[20:23]
s_add_u32 m0,s57,77800
ds_read_u16_d16_hi v43,v43
v_lshlrev_b32_sdwa v36,1,v36 dst_sel:DWORD dst_unused:UNUSED_PAD src0_sel:DWORD src1_sel:WORD_1
v_mfma_f32_16x16x32_bf16 v[28:31],a[204:207],a[16:19],v[28:31]
s_add_u32 s89,s56,4620288
ds_read_u16 v36,v36
v_lshl_add_u64 v[32:33],v[32:33],0,v[34:35]
v_mfma_f32_16x16x32_bf16 v[28:31],a[212:215],a[32:35],v[28:31]
buffer_load_dwordx4 v6,s[36:39],s89 offen lds
v_lshl_add_u64 v[34:35],v[60:61],0,v[62:63]
v_lshlrev_b32_sdwa v38,1,v38 dst_sel:DWORD dst_unused:UNUSED_PAD src0_sel:DWORD src1_sel:WORD_1
v_mfma_f32_16x16x32_bf16 v[24:27],a[200:203],a[32:35],v[16:19]
ds_read_u16_d16_hi v38,v38
v_lshlrev_b32_sdwa v37,1,v37 dst_sel:DWORD dst_unused:UNUSED_PAD src0_sel:DWORD src1_sel:WORD_1
v_mfma_f32_16x16x32_bf16 v[24:27],a[196:199],a[16:19],v[24:27]
s_waitcnt lgkmcnt(7)
ds_read_u16 v37,v37
v_lshl_add_u64 v[60:61],v[56:57],0,v[58:59]
v_mfma_f32_16x16x32_bf16 v[24:27],a[192:195],a[0:3],v[24:27]
TP3280_%=:
s_cbranch_execz TP5404_%=
v_lshlrev_b32_sdwa v39,1,v39 dst_sel:DWORD dst_unused:UNUSED_PAD src0_sel:DWORD src1_sel:WORD_1
v_lshlrev_b32_sdwa v44,1,v44 dst_sel:DWORD dst_unused:UNUSED_PAD src0_sel:DWORD src1_sel:WORD_1
v_mfma_f32_16x16x32_bf16 v[56:59],a[192:195],a[4:7],v[16:19]
s_waitcnt lgkmcnt(6)
ds_read_u16_d16_hi v39,v39
v_or_b32_e64 v62,v52,v54
v_mfma_f32_16x16x32_bf16 v[56:59],a[196:199],a[20:23],v[56:59]
s_waitcnt vmcnt(7)
ds_read_u16 v44,v44
v_cvt_pk_bf16_f32 v63,v53,v55
v_mfma_f32_16x16x32_bf16 v[56:59],a[200:203],a[36:39],v[56:59]
s_barrier
v_lshlrev_b32_sdwa v46,1,v46 dst_sel:DWORD dst_unused:UNUSED_PAD src0_sel:DWORD src1_sel:WORD_1
v_lshlrev_b32_sdwa v45,1,v45 dst_sel:DWORD dst_unused:UNUSED_PAD src0_sel:DWORD src1_sel:WORD_1
v_mfma_f32_16x16x32_bf16 v[52:55],a[212:215],a[36:39],v[20:23]
ds_read_u16_d16_hi v46,v46
v_lshlrev_b32_sdwa v47,1,v47 dst_sel:DWORD dst_unused:UNUSED_PAD src0_sel:DWORD src1_sel:WORD_1
v_mfma_f32_16x16x32_bf16 v[52:55],a[204:207],a[20:23],v[52:55]
s_add_u32 m0,s57,81328
ds_read_u16 v45,v45
v_mfma_f32_16x16x32_bf16 v[52:55],a[208:211],a[4:7],v[52:55]
s_add_u32 s89,s56,4718592
ds_read_u16_d16_hi v47,v47
v_lshlrev_b32_sdwa v48,1,v48 dst_sel:DWORD dst_unused:UNUSED_PAD src0_sel:DWORD src1_sel:WORD_1
v_mfma_f32_16x16x32_bf16 v[232:235],a[236:239],v[32:35],v[232:235]
buffer_load_dwordx4 v6,s[36:39],s89 offen lds
v_lshlrev_b32_sdwa v50,1,v50 dst_sel:DWORD dst_unused:UNUSED_PAD src0_sel:DWORD src1_sel:WORD_1
v_mfma_f32_16x16x32_bf16 v[236:239],a[236:239],v[60:63],v[236:239]
s_add_u32 m0,s58,6160
ds_read_u16 v48,v48
v_mfma_f32_16x16x32_bf16 v[164:167],a[244:247],v[60:63],v[164:167]
s_add_u32 s69,s69,s59
ds_read_u16_d16_hi v50,v50
ds_read_b64_tr_b16 a[228:229],v3 offset:18480
v_mfma_f32_16x16x32_bf16 v[160:163],a[244:247],v[32:35],v[160:163]
buffer_load_dwordx4 v5,s[44:47],s69 offen offset:0 lds
v_lshlrev_b32_sdwa v49,1,v49 dst_sel:DWORD dst_unused:UNUSED_PAD src0_sel:DWORD src1_sel:WORD_1
v_mfma_f32_16x16x32_bf16 v[240:243],a[248:251],v[32:35],v[240:243]
s_add_u32 s69,s69,s53
ds_read_u16 v49,v49
v_lshlrev_b32_sdwa v51,1,v51 dst_sel:DWORD dst_unused:UNUSED_PAD src0_sel:DWORD src1_sel:WORD_1
v_mfma_f32_16x16x32_bf16 v[244:247],a[248:251],v[60:63],v[244:247]
buffer_load_dwordx4 v5,s[44:47],s69 offen offset:1024 lds
ds_read_u16_d16_hi v51,v51
v_mfma_f32_16x16x32_bf16 v[180:183],a[252:255],v[60:63],v[180:183]
ds_read_b64_tr_b16 a[230:231],v3 offset:20016
v_lshlrev_b32_sdwa v28,1,v28 dst_sel:DWORD dst_unused:UNUSED_PAD src0_sel:DWORD src1_sel:WORD_1
v_mfma_f32_16x16x32_bf16 v[176:179],a[252:255],v[32:35],v[176:179]
ds_read_u16 v28,v28
v_lshlrev_b32_sdwa v30,1,v30 dst_sel:DWORD dst_unused:UNUSED_PAD src0_sel:DWORD src1_sel:WORD_1
v_mfma_f32_16x16x32_bf16 v[248:251],a[240:243],v[32:35],v[248:251]
ds_read_u16_d16_hi v30,v30
v_lshlrev_b32_sdwa v29,1,v29 dst_sel:DWORD dst_unused:UNUSED_PAD src0_sel:DWORD src1_sel:WORD_1
v_mfma_f32_16x16x32_bf16 v[252:255],a[240:243],v[60:63],v[252:255]
ds_read_u16 v29,v29
v_mfma_f32_16x16x32_bf16 v[196:199],a[224:227],v[60:63],v[196:199]
s_add_u32 s69,s69,s53
ds_read_b64_tr_b16 a[232:233],v3 offset:18488
v_lshlrev_b32_sdwa v31,1,v31 dst_sel:DWORD dst_unused:UNUSED_PAD src0_sel:DWORD src1_sel:WORD_1
v_mfma_f32_16x16x32_bf16 v[192:195],a[224:227],v[32:35],v[192:195]
buffer_load_dwordx4 v5,s[44:47],s69 offen offset:2048 lds
v_mfma_f32_16x16x32_bf16 v[60:63],a[208:211],a[8:11],v[20:23]
ds_read_u16_d16_hi v31,v31
v_lshlrev_b32_sdwa v24,1,v24 dst_sel:DWORD dst_unused:UNUSED_PAD src0_sel:DWORD src1_sel:WORD_1
v_mfma_f32_16x16x32_bf16 v[60:63],a[204:207],a[24:27],v[60:63]
s_waitcnt lgkmcnt(7)
ds_read_u16 v24,v24
v_lshl_add_u64 v[40:41],v[40:41],0,v[42:43]
v_fma_mixlo_f16 v8,v57,s99,0
v_mfma_f32_16x16x32_bf16 v[60:63],a[212:215],a[40:43],v[60:63]
ds_read_b64_tr_b16 a[234:235],v3 offset:20024
v_lshl_add_u64 v[42:43],v[36:37],0,v[38:39]
v_lshlrev_b32_sdwa v26,1,v26 dst_sel:DWORD dst_unused:UNUSED_PAD src0_sel:DWORD src1_sel:WORD_1
v_mfma_f32_16x16x32_bf16 v[32:35],a[200:203],a[40:43],v[16:19]
ds_read_u16_d16_hi v26,v26
v_fma_mixhi_f16 v8,v59,s99,0
v_mfma_f32_16x16x32_bf16 v[32:35],a[196:199],a[24:27],v[32:35]
ds_read_b64_tr_b16 a[220:221],v1 offset:18480
v_lshl_add_u64 v[36:37],v[44:45],0,v[46:47]
v_lshlrev_b32_sdwa v25,1,v25 dst_sel:DWORD dst_unused:UNUSED_PAD src0_sel:DWORD src1_sel:WORD_1
v_pk_fma_f16 v9,v8,v8,s54
v_mfma_f32_16x16x32_bf16 v[32:35],a[192:195],a[8:11],v[32:35]
ds_read_u16 v25,v25
v_pk_mul_f16 v9,v8,v9
v_mfma_f32_16x16x32_bf16 v[44:47],a[192:195],a[12:15],v[16:19]
ds_read_b64_tr_b16 a[222:223],v1 offset:19504
v_lshlrev_b32_sdwa v27,1,v27 dst_sel:DWORD dst_unused:UNUSED_PAD src0_sel:DWORD src1_sel:WORD_1
v_mfma_f32_16x16x32_bf16 v[44:47],a[196:199],a[28:31],v[44:47]
s_waitcnt lgkmcnt(7)
ds_read_u16_d16_hi v27,v27
v_lshl_add_u64 v[38:39],v[48:49],0,v[50:51]
v_mfma_f32_16x16x32_bf16 v[44:47],a[200:203],a[44:47],v[44:47]
v_exp_f16 v8,v9
v_lshlrev_b32_sdwa v52,1,v52 dst_sel:DWORD dst_unused:UNUSED_PAD src0_sel:DWORD src1_sel:WORD_1
v_mfma_f32_16x16x32_bf16 v[48:51],a[212:215],a[44:47],v[20:23]
ds_read_u16 v52,v52
v_lshlrev_b32_sdwa v54,1,v54 dst_sel:DWORD dst_unused:UNUSED_PAD src0_sel:DWORD src1_sel:WORD_1
v_mfma_f32_16x16x32_bf16 v[48:51],a[204:207],a[28:31],v[48:51]
ds_read_u16_d16_hi v54,v54
v_lshlrev_b32_sdwa v53,1,v53 dst_sel:DWORD dst_unused:UNUSED_PAD src0_sel:DWORD src1_sel:WORD_1
v_mfma_f32_16x16x32_bf16 v[48:51],a[208:211],a[12:15],v[48:51]
v_exp_f16_sdwa v8,v9 dst_sel:WORD_1 dst_unused:UNUSED_PRESERVE src0_sel:WORD_1
v_mfma_f32_16x16x32_bf16 v[208:211],a[236:239],v[40:43],v[208:211]
ds_read_u16 v53,v53
v_lshlrev_b32_sdwa v55,1,v55 dst_sel:DWORD dst_unused:UNUSED_PAD src0_sel:DWORD src1_sel:WORD_1
v_mfma_f32_16x16x32_bf16 v[212:215],a[236:239],v[36:39],v[212:215]
ds_read_u16_d16_hi v55,v55
v_pk_fma_f16 v8,v8,s55,s55
v_mfma_f32_16x16x32_bf16 v[172:175],a[244:247],v[36:39],v[172:175]
ds_read_b64_tr_b16 a[236:237],v3 offset:18544
v_lshlrev_b32_sdwa v56,1,v56 dst_sel:DWORD dst_unused:UNUSED_PAD src0_sel:DWORD src1_sel:WORD_1
v_mfma_f32_16x16x32_bf16 v[168:171],a[244:247],v[40:43],v[168:171]
ds_read_u16 v56,v56
v_rcp_f16 v9,v8
v_mfma_f32_16x16x32_bf16 v[216:219],a[248:251],v[40:43],v[216:219]
ds_read_b64_tr_b16 a[238:239],v3 offset:20080
v_lshlrev_b32_sdwa v58,1,v58 dst_sel:DWORD dst_unused:UNUSED_PAD src0_sel:DWORD src1_sel:WORD_1
v_mfma_f32_16x16x32_bf16 v[220:223],a[248:251],v[36:39],v[220:223]
ds_read_u16_d16_hi v58,v58
ds_read_b64_tr_b16 a[244:245],v3 offset:18552
v_mfma_f32_16x16x32_bf16 v[188:191],a[252:255],v[36:39],v[188:191]
ds_read_b64_tr_b16 a[246:247],v3 offset:20088
v_lshlrev_b32_sdwa v60,1,v60 dst_sel:DWORD dst_unused:UNUSED_PAD src0_sel:DWORD src1_sel:WORD_1
v_mfma_f32_16x16x32_bf16 v[184:187],a[252:255],v[40:43],v[184:187]
ds_read_u16 v60,v60
v_rcp_f16_sdwa v9,v8 dst_sel:WORD_1 dst_unused:UNUSED_PRESERVE src0_sel:WORD_1
v_mfma_f32_16x16x32_bf16 v[224:227],a[240:243],v[40:43],v[224:227]
ds_read_b64_tr_b16 a[248:249],v3 offset:18608
v_lshlrev_b32_sdwa v62,1,v62 dst_sel:DWORD dst_unused:UNUSED_PAD src0_sel:DWORD src1_sel:WORD_1
v_mfma_f32_16x16x32_bf16 v[228:231],a[240:243],v[36:39],v[228:231]
ds_read_u16_d16_hi v62,v62
v_fma_mix_f32 v57,v57,v9,0 op_sel_hi:[0,1,0]
v_mfma_f32_16x16x32_bf16 v[204:207],a[224:227],v[36:39],v[204:207]
ds_read_b64_tr_b16 a[250:251],v3 offset:20144
v_lshlrev_b32_sdwa v61,1,v61 dst_sel:DWORD dst_unused:UNUSED_PAD src0_sel:DWORD src1_sel:WORD_1
v_mfma_f32_16x16x32_bf16 v[200:203],a[224:227],v[40:43],v[200:203]
ds_read_u16 v61,v61
v_fma_mix_f32 v59,v59,v9,0 op_sel:[0,1,0] op_sel_hi:[0,1,0]
v_lshlrev_b32_sdwa v63,1,v63 dst_sel:DWORD dst_unused:UNUSED_PAD src0_sel:DWORD src1_sel:WORD_1
v_mfma_f32_16x16x32_bf16 v[36:39],a[208:211],a[48:51],v[20:23]
s_add_u32 m0,s58,12320
ds_read_b64 v[14:15],v7 offset:18480
ds_read_u16_d16_hi v63,v63
v_lshlrev_b32_sdwa v32,1,v32 dst_sel:DWORD dst_unused:UNUSED_PAD src0_sel:DWORD src1_sel:WORD_1
v_mfma_f32_16x16x32_bf16 v[36:39],a[204:207],a[64:67],v[36:39]
s_waitcnt lgkmcnt(10)
ds_read_b64_tr_b16 a[252:253],v3 offset:18616
ds_read_b64_tr_b16 a[254:255],v3 offset:20152
v_lshl_add_u64 v[28:29],v[28:29],0,v[30:31]
v_mfma_f32_16x16x32_bf16 v[36:39],a[212:215],a[80:83],v[36:39]
s_add_u32 s69,s69,s59
ds_read_u16 v32,v32
v_lshl_add_u64 v[30:31],v[24:25],0,v[26:27]
v_lshlrev_b32_sdwa v34,1,v34 dst_sel:DWORD dst_unused:UNUSED_PAD src0_sel:DWORD src1_sel:WORD_1
v_mfma_f32_16x16x32_bf16 v[40:43],a[200:203],a[80:83],v[16:19]
buffer_load_dwordx4 v5,s[44:47],s69 offen offset:0 lds
v_lshlrev_b32_sdwa v33,1,v33 dst_sel:DWORD dst_unused:UNUSED_PAD src0_sel:DWORD src1_sel:WORD_1
v_mfma_f32_16x16x32_bf16 v[40:43],a[196:199],a[64:67],v[40:43]
s_add_u32 s69,s69,s53
ds_read_u16_d16_hi v34,v34
v_lshl_add_u64 v[24:25],v[52:53],0,v[54:55]
v_mfma_f32_16x16x32_bf16 v[40:43],a[192:195],a[48:51],v[40:43]
s_waitcnt lgkmcnt(5)
buffer_load_dwordx4 v5,s[44:47],s69 offen offset:1024 lds
v_or3_b32 v13,v14,v15,v13
v_lshlrev_b32_sdwa v35,1,v35 dst_sel:DWORD dst_unused:UNUSED_PAD src0_sel:DWORD src1_sel:WORD_1
v_mfma_f32_16x16x32_bf16 v[52:55],a[192:195],a[52:55],v[16:19]
s_add_u32 m0,s57,84864
ds_read_b64 v[14:15],v7 offset:18992
ds_read_u16 v33,v33
v_or_b32 v26,v56,v58
v_mfma_f32_16x16x32_bf16 v[52:55],a[196:199],a[68:71],v[52:55]
s_add_u32 s89,s56,4816896
ds_read_u16_d16_hi v35,v35
v_cvt_pk_bf16_f32 v27,v57,v59
v_lshlrev_b32_sdwa v48,1,v48 dst_sel:DWORD dst_unused:UNUSED_PAD src0_sel:DWORD src1_sel:WORD_1
v_mfma_f32_16x16x32_bf16 v[52:55],a[200:203],a[84:87],v[52:55]
buffer_load_dwordx4 v6,s[36:39],s89 offen lds
v_lshlrev_b32_sdwa v50,1,v50 dst_sel:DWORD dst_unused:UNUSED_PAD src0_sel:DWORD src1_sel:WORD_1
v_mfma_f32_16x16x32_bf16 v[56:59],a[212:215],a[84:87],v[20:23]
s_add_u32 m0,s58,12320
ds_read_u16 v48,v48
v_lshlrev_b32_sdwa v49,1,v49 dst_sel:DWORD dst_unused:UNUSED_PAD src0_sel:DWORD src1_sel:WORD_1
v_mfma_f32_16x16x32_bf16 v[56:59],a[204:207],a[68:71],v[56:59]
s_add_u32 s69,s69,s53
ds_read_u16_d16_hi v50,v50
v_lshlrev_b32_sdwa v51,1,v51 dst_sel:DWORD dst_unused:UNUSED_PAD src0_sel:DWORD src1_sel:WORD_1
v_mfma_f32_16x16x32_bf16 v[56:59],a[208:211],a[52:55],v[56:59]
s_waitcnt lgkmcnt(4)
buffer_load_dwordx4 v5,s[44:47],s69 offen offset:2048 lds
ds_read_u16 v49,v49
v_or3_b32 v13,v14,v15,v13
v_mfma_f32_16x16x32_bf16 v[136:139],a[228:231],v[28:31],v[136:139]
s_add_u32 m0,s57,8
ds_read_b64 v[14:15],v7 offset:19504
v_lshlrev_b32_sdwa v44,1,v44 dst_sel:DWORD dst_unused:UNUSED_PAD src0_sel:DWORD src1_sel:WORD_1
v_mfma_f32_16x16x32_bf16 v[140:143],a[228:231],v[24:27],v[140:143]
s_add_u32 s89,s56,4915200
ds_read_u16_d16_hi v51,v51
v_lshlrev_b32_sdwa v46,1,v46 dst_sel:DWORD dst_unused:UNUSED_PAD src0_sel:DWORD src1_sel:WORD_1
v_mfma_f32_16x16x32_bf16 v[68:71],a[232:235],v[24:27],v[68:71]
buffer_load_dwordx4 v6,s[36:39],s89 offen lds
ds_read_u16 v44,v44
v_mfma_f32_16x16x32_bf16 v[64:67],a[232:235],v[28:31],v[64:67]
ds_read_u16_d16_hi v46,v46
v_lshlrev_b32_sdwa v45,1,v45 dst_sel:DWORD dst_unused:UNUSED_PAD src0_sel:DWORD src1_sel:WORD_1
v_mfma_f32_16x16x32_bf16 v[144:147],a[236:239],v[28:31],v[144:147]
ds_read_u16 v45,v45
v_lshlrev_b32_sdwa v47,1,v47 dst_sel:DWORD dst_unused:UNUSED_PAD src0_sel:DWORD src1_sel:WORD_1
v_mfma_f32_16x16x32_bf16 v[148:151],a[236:239],v[24:27],v[148:151]
s_waitcnt lgkmcnt(4)
ds_read_u16_d16_hi v47,v47
v_or3_b32 v13,v14,v15,v13
v_mfma_f32_16x16x32_bf16 v[84:87],a[244:247],v[24:27],v[84:87]
ds_read_b64_tr_b16 a[216:217],v1 offset:20528
v_lshlrev_b32_sdwa v36,1,v36 dst_sel:DWORD dst_unused:UNUSED_PAD src0_sel:DWORD src1_sel:WORD_1
v_mfma_f32_16x16x32_bf16 v[80:83],a[244:247],v[28:31],v[80:83]
ds_read_u16 v36,v36
v_lshlrev_b32_sdwa v38,1,v38 dst_sel:DWORD dst_unused:UNUSED_PAD src0_sel:DWORD src1_sel:WORD_1
v_mfma_f32_16x16x32_bf16 v[152:155],a[248:251],v[28:31],v[152:155]
ds_read_u16_d16_hi v38,v38
v_lshlrev_b32_sdwa v37,1,v37 dst_sel:DWORD dst_unused:UNUSED_PAD src0_sel:DWORD src1_sel:WORD_1
v_mfma_f32_16x16x32_bf16 v[156:159],a[248:251],v[24:27],v[156:159]
ds_read_u16 v37,v37
v_mfma_f32_16x16x32_bf16 v[100:103],a[252:255],v[24:27],v[100:103]
ds_read_b64_tr_b16 a[218:219],v1 offset:21552
v_lshlrev_b32_sdwa v39,1,v39 dst_sel:DWORD dst_unused:UNUSED_PAD src0_sel:DWORD src1_sel:WORD_1
v_mfma_f32_16x16x32_bf16 v[96:99],a[252:255],v[28:31],v[96:99]
ds_read_u16_d16_hi v39,v39
v_mfma_f32_16x16x32_bf16 v[24:27],a[208:211],a[56:59],v[20:23]
v_lshlrev_b32_sdwa v40,1,v40 dst_sel:DWORD dst_unused:UNUSED_PAD src0_sel:DWORD src1_sel:WORD_1
v_fma_mixlo_f16 v8,v53,s99,0
v_mfma_f32_16x16x32_bf16 v[24:27],a[204:207],a[72:75],v[24:27]
ds_read_u16 v40,v40
v_lshl_add_u64 v[60:61],v[60:61],0,v[62:63]
v_mfma_f32_16x16x32_bf16 v[24:27],a[212:215],a[88:91],v[24:27]
v_lshl_add_u64 v[62:63],v[32:33],0,v[34:35]
v_lshlrev_b32_sdwa v42,1,v42 dst_sel:DWORD dst_unused:UNUSED_PAD src0_sel:DWORD src1_sel:WORD_1
v_mfma_f32_16x16x32_bf16 v[28:31],a[200:203],a[88:91],v[16:19]
ds_read_u16_d16_hi v42,v42
v_fma_mixhi_f16 v8,v55,s99,0
v_mfma_f32_16x16x32_bf16 v[28:31],a[196:199],a[72:75],v[28:31]
s_waitcnt lgkmcnt(6)
v_lshl_add_u64 v[32:33],v[48:49],0,v[50:51]
v_lshlrev_b32_sdwa v41,1,v41 dst_sel:DWORD dst_unused:UNUSED_PAD src0_sel:DWORD src1_sel:WORD_1
v_pk_fma_f16 v9,v8,v8,s54
v_mfma_f32_16x16x32_bf16 v[28:31],a[192:195],a[56:59],v[28:31]
ds_read_u16 v41,v41
v_pk_mul_f16 v9,v8,v9
v_mfma_f32_16x16x32_bf16 v[48:51],a[192:195],a[60:63],v[16:19]
ds_read_b128 v[16:19],v4 offset:1024
v_lshlrev_b32_sdwa v43,1,v43 dst_sel:DWORD dst_unused:UNUSED_PAD src0_sel:DWORD src1_sel:WORD_1
v_mfma_f32_16x16x32_bf16 v[48:51],a[196:199],a[76:79],v[48:51]
ds_read_b64_tr_b16 a[192:193],v1 offset:22576
ds_read_b64_tr_b16 a[194:195],v1 offset:23600
v_lshl_add_u64 v[34:35],v[44:45],0,v[46:47]
v_mfma_f32_16x16x32_bf16 v[48:51],a[200:203],a[92:95],v[48:51]
ds_read_u16_d16_hi v43,v43
v_exp_f16 v8,v9
v_lshlrev_b32_sdwa v56,1,v56 dst_sel:DWORD dst_unused:UNUSED_PAD src0_sel:DWORD src1_sel:WORD_1
v_mfma_f32_16x16x32_bf16 v[44:47],a[212:215],a[92:95],v[20:23]
ds_read_b128 v[20:23],v4 offset:1056
ds_read_b64_tr_b16 a[196:197],v2 offset:22576
v_mfma_f32_16x16x32_bf16 v[44:47],a[204:207],a[76:79],v[44:47]
ds_read_b64_tr_b16 a[198:199],v2 offset:23600
ds_read_b64_tr_b16 a[200:201],v2 offset:20528
v_lshlrev_b32_sdwa v58,1,v58 dst_sel:DWORD dst_unused:UNUSED_PAD src0_sel:DWORD src1_sel:WORD_1
v_mfma_f32_16x16x32_bf16 v[44:47],a[208:211],a[60:63],v[44:47]
ds_read_b64_tr_b16 a[202:203],v2 offset:21552
v_exp_f16_sdwa v8,v9 dst_sel:WORD_1 dst_unused:UNUSED_PRESERVE src0_sel:WORD_1
v_mfma_f32_16x16x32_bf16 v[112:115],a[228:231],v[60:63],v[112:115]
ds_read_u16 v56,v56
v_lshlrev_b32_sdwa v57,1,v57 dst_sel:DWORD dst_unused:UNUSED_PAD src0_sel:DWORD src1_sel:WORD_1
v_mfma_f32_16x16x32_bf16 v[116:119],a[228:231],v[32:35],v[116:119]
ds_read_u16_d16_hi v58,v58
v_lshlrev_b32_sdwa v59,1,v59 dst_sel:DWORD dst_unused:UNUSED_PAD src0_sel:DWORD src1_sel:WORD_1
v_mfma_f32_16x16x32_bf16 v[76:79],a[232:235],v[32:35],v[76:79]
ds_read_u16 v57,v57
v_pk_fma_f16 v8,v8,s55,s55
v_mfma_f32_16x16x32_bf16 v[72:75],a[232:235],v[60:63],v[72:75]
v_rcp_f16 v9,v8
v_lshlrev_b32_sdwa v52,1,v52 dst_sel:DWORD dst_unused:UNUSED_PAD src0_sel:DWORD src1_sel:WORD_1
v_mfma_f32_16x16x32_bf16 v[120:123],a[236:239],v[60:63],v[120:123]
ds_read_u16_d16_hi v59,v59
v_lshlrev_b32_sdwa v54,1,v54 dst_sel:DWORD dst_unused:UNUSED_PAD src0_sel:DWORD src1_sel:WORD_1
v_mfma_f32_16x16x32_bf16 v[124:127],a[236:239],v[32:35],v[124:127]
ds_read_b64_tr_b16 a[212:213],v2 offset:18480
v_lshlrev_b32_sdwa v24,1,v24 dst_sel:DWORD dst_unused:UNUSED_PAD src0_sel:DWORD src1_sel:WORD_1
v_mfma_f32_16x16x32_bf16 v[92:95],a[244:247],v[32:35],v[92:95]
ds_read_b64_tr_b16 a[214:215],v2 offset:19504
v_lshlrev_b32_sdwa v26,1,v26 dst_sel:DWORD dst_unused:UNUSED_PAD src0_sel:DWORD src1_sel:WORD_1
v_mfma_f32_16x16x32_bf16 v[88:91],a[244:247],v[60:63],v[88:91]
ds_read_u16 v52,v52
v_rcp_f16_sdwa v9,v8 dst_sel:WORD_1 dst_unused:UNUSED_PRESERVE src0_sel:WORD_1
v_mfma_f32_16x16x32_bf16 v[128:131],a[248:251],v[60:63],v[128:131]
ds_read_u16_d16_hi v54,v54
v_fma_mix_f32 v53,v53,v9,0 op_sel_hi:[0,1,0]
v_mfma_f32_16x16x32_bf16 v[132:135],a[248:251],v[32:35],v[132:135]
ds_read_u16 v24,v24
v_lshlrev_b32_sdwa v25,1,v25 dst_sel:DWORD dst_unused:UNUSED_PAD src0_sel:DWORD src1_sel:WORD_1
v_mfma_f32_16x16x32_bf16 v[108:111],a[252:255],v[32:35],v[108:111]
ds_read_u16_d16_hi v26,v26
v_fma_mix_f32 v55,v55,v9,0 op_sel:[0,1,0] op_sel_hi:[0,1,0]
v_mfma_f32_16x16x32_bf16 v[104:107],a[252:255],v[60:63],v[104:107]
s_waitcnt lgkmcnt(7)
ds_read_u16 v25,v25
v_lshlrev_b32_sdwa v27,1,v27 dst_sel:DWORD dst_unused:UNUSED_PAD src0_sel:DWORD src1_sel:WORD_1
v_mfma_f32_16x16x32_bf16 v[32:35],a[220:223],a[0:3],v[16:19]
s_add_u32 m0,s57,3544
ds_read_u16_d16_hi v27,v27
v_lshlrev_b32_sdwa v28,1,v28 dst_sel:DWORD dst_unused:UNUSED_PAD src0_sel:DWORD src1_sel:WORD_1
v_mfma_f32_16x16x32_bf16 v[32:35],a[216:219],a[16:19],v[32:35]
s_add_u32 s89,s56,5013504
ds_read_u16 v28,v28
v_lshl_add_u64 v[36:37],v[36:37],0,v[38:39]
v_mfma_f32_16x16x32_bf16 v[32:35],a[192:195],a[32:35],v[32:35]
buffer_load_dwordx4 v6,s[36:39],s89 offen lds
v_lshl_add_u64 v[38:39],v[40:41],0,v[42:43]
v_lshlrev_b32_sdwa v30,1,v30 dst_sel:DWORD dst_unused:UNUSED_PAD src0_sel:DWORD src1_sel:WORD_1
v_mfma_f32_16x16x32_bf16 v[60:63],a[196:199],a[32:35],v[20:23]
s_add_u32 m0,s57,7072
ds_read_u16_d16_hi v30,v30
v_lshlrev_b32_sdwa v29,1,v29 dst_sel:DWORD dst_unused:UNUSED_PAD src0_sel:DWORD src1_sel:WORD_1
v_mfma_f32_16x16x32_bf16 v[60:63],a[200:203],a[16:19],v[60:63]
s_waitcnt lgkmcnt(7)
ds_read_u16 v29,v29
v_lshl_add_u64 v[40:41],v[56:57],0,v[58:59]
v_mfma_f32_16x16x32_bf16 v[60:63],a[212:215],a[0:3],v[60:63]
s_add_u32 s89,s56,5111808
v_lshlrev_b32_sdwa v31,1,v31 dst_sel:DWORD dst_unused:UNUSED_PAD src0_sel:DWORD src1_sel:WORD_1
v_lshlrev_b32_sdwa v44,1,v44 dst_sel:DWORD dst_unused:UNUSED_PAD src0_sel:DWORD src1_sel:WORD_1
v_mfma_f32_16x16x32_bf16 v[56:59],a[212:215],a[4:7],v[20:23]
s_waitcnt lgkmcnt(6)
buffer_load_dwordx4 v6,s[36:39],s89 offen lds
v_or_b32 v42,v52,v54
v_mfma_f32_16x16x32_bf16 v[56:59],a[200:203],a[20:23],v[56:59]
ds_read_u16_d16_hi v31,v31
v_cvt_pk_bf16_f32 v43,v53,v55
v_mfma_f32_16x16x32_bf16 v[56:59],a[196:199],a[36:39],v[56:59]
ds_read_u16 v44,v44
v_lshlrev_b32_sdwa v46,1,v46 dst_sel:DWORD dst_unused:UNUSED_PAD src0_sel:DWORD src1_sel:WORD_1
v_mfma_f32_16x16x32_bf16 v[52:55],a[192:195],a[36:39],v[16:19]
ds_read_u16_d16_hi v46,v46
v_lshlrev_b32_sdwa v45,1,v45 dst_sel:DWORD dst_unused:UNUSED_PAD src0_sel:DWORD src1_sel:WORD_1
v_mfma_f32_16x16x32_bf16 v[52:55],a[216:219],a[20:23],v[52:55]
ds_read_u16 v45,v45
v_lshlrev_b32_sdwa v47,1,v47 dst_sel:DWORD dst_unused:UNUSED_PAD src0_sel:DWORD src1_sel:WORD_1
v_mfma_f32_16x16x32_bf16 v[52:55],a[220:223],a[4:7],v[52:55]
ds_read_u16_d16_hi v47,v47
v_lshlrev_b32_sdwa v48,1,v48 dst_sel:DWORD dst_unused:UNUSED_PAD src0_sel:DWORD src1_sel:WORD_1
v_mfma_f32_16x16x32_bf16 v[232:235],a[228:231],v[36:39],v[232:235]
ds_read_u16 v48,v48
v_lshlrev_b32_sdwa v50,1,v50 dst_sel:DWORD dst_unused:UNUSED_PAD src0_sel:DWORD src1_sel:WORD_1
v_mfma_f32_16x16x32_bf16 v[236:239],a[228:231],v[40:43],v[236:239]
ds_read_u16_d16_hi v50,v50
v_mfma_f32_16x16x32_bf16 v[164:167],a[232:235],v[40:43],v[164:167]
ds_read_b64_tr_b16 a[240:241],v3 offset:0
v_lshlrev_b32_sdwa v49,1,v49 dst_sel:DWORD dst_unused:UNUSED_PAD src0_sel:DWORD src1_sel:WORD_1
v_mfma_f32_16x16x32_bf16 v[160:163],a[232:235],v[36:39],v[160:163]
ds_read_u16 v49,v49
v_mfma_f32_16x16x32_bf16 v[240:243],a[236:239],v[36:39],v[240:243]
ds_read_b64_tr_b16 a[242:243],v3 offset:1536
v_lshlrev_b32_sdwa v51,1,v51 dst_sel:DWORD dst_unused:UNUSED_PAD src0_sel:DWORD src1_sel:WORD_1
v_mfma_f32_16x16x32_bf16 v[244:247],a[236:239],v[40:43],v[244:247]
ds_read_u16_d16_hi v51,v51
v_lshlrev_b32_sdwa v32,1,v32 dst_sel:DWORD dst_unused:UNUSED_PAD src0_sel:DWORD src1_sel:WORD_1
v_mfma_f32_16x16x32_bf16 v[180:183],a[244:247],v[40:43],v[180:183]
ds_read_u16 v32,v32
v_mfma_f32_16x16x32_bf16 v[176:179],a[244:247],v[36:39],v[176:179]
ds_read_b64_tr_b16 a[224:225],v3 offset:8
v_lshlrev_b32_sdwa v34,1,v34 dst_sel:DWORD dst_unused:UNUSED_PAD src0_sel:DWORD src1_sel:WORD_1
v_mfma_f32_16x16x32_bf16 v[248:251],a[248:251],v[36:39],v[248:251]
ds_read_u16_d16_hi v34,v34
v_lshlrev_b32_sdwa v33,1,v33 dst_sel:DWORD dst_unused:UNUSED_PAD src0_sel:DWORD src1_sel:WORD_1
v_mfma_f32_16x16x32_bf16 v[252:255],a[248:251],v[40:43],v[252:255]
ds_read_u16 v33,v33
v_mfma_f32_16x16x32_bf16 v[196:199],a[252:255],v[40:43],v[196:199]
ds_read_b64_tr_b16 a[226:227],v3 offset:1544
v_lshlrev_b32_sdwa v35,1,v35 dst_sel:DWORD dst_unused:UNUSED_PAD src0_sel:DWORD src1_sel:WORD_1
v_mfma_f32_16x16x32_bf16 v[192:195],a[252:255],v[36:39],v[192:195]
ds_read_u16_d16_hi v35,v35
v_mfma_f32_16x16x32_bf16 v[40:43],a[220:223],a[8:11],v[16:19]
ds_read_b64_tr_b16 a[204:205],v1 offset:0
v_lshlrev_b32_sdwa v60,1,v60 dst_sel:DWORD dst_unused:UNUSED_PAD src0_sel:DWORD src1_sel:WORD_1
v_mfma_f32_16x16x32_bf16 v[40:43],a[216:219],a[24:27],v[40:43]
s_waitcnt lgkmcnt(8)
ds_read_u16 v60,v60
v_lshl_add_u64 v[24:25],v[24:25],0,v[26:27]
v_fma_mixlo_f16 v8,v57,s99,0
v_mfma_f32_16x16x32_bf16 v[40:43],a[192:195],a[40:43],v[40:43]
ds_read_b64_tr_b16 a[206:207],v1 offset:1024
v_lshl_add_u64 v[26:27],v[28:29],0,v[30:31]
v_lshlrev_b32_sdwa v62,1,v62 dst_sel:DWORD dst_unused:UNUSED_PAD src0_sel:DWORD src1_sel:WORD_1
v_mfma_f32_16x16x32_bf16 v[36:39],a[196:199],a[40:43],v[20:23]
ds_read_u16_d16_hi v62,v62
v_fma_mixhi_f16 v8,v59,s99,0
v_mfma_f32_16x16x32_bf16 v[36:39],a[200:203],a[24:27],v[36:39]
ds_read_b64_tr_b16 a[208:209],v1 offset:2048
v_lshl_add_u64 v[28:29],v[44:45],0,v[46:47]
v_lshlrev_b32_sdwa v61,1,v61 dst_sel:DWORD dst_unused:UNUSED_PAD src0_sel:DWORD src1_sel:WORD_1
v_pk_fma_f16 v9,v8,v8,s54
v_mfma_f32_16x16x32_bf16 v[36:39],a[212:215],a[8:11],v[36:39]
ds_read_u16 v61,v61
v_pk_mul_f16 v9,v8,v9
v_mfma_f32_16x16x32_bf16 v[44:47],a[212:215],a[12:15],v[20:23]
ds_read_b64_tr_b16 a[210:211],v1 offset:3072
v_lshlrev_b32_sdwa v63,1,v63 dst_sel:DWORD dst_unused:UNUSED_PAD src0_sel:DWORD src1_sel:WORD_1
v_mfma_f32_16x16x32_bf16 v[44:47],a[200:203],a[28:31],v[44:47]
s_waitcnt lgkmcnt(8)
ds_read_u16_d16_hi v63,v63
v_lshl_add_u64 v[30:31],v[48:49],0,v[50:51]
v_mfma_f32_16x16x32_bf16 v[44:47],a[196:199],a[44:47],v[44:47]
v_exp_f16 v8,v9
v_lshlrev_b32_sdwa v52,1,v52 dst_sel:DWORD dst_unused:UNUSED_PAD src0_sel:DWORD src1_sel:WORD_1
v_mfma_f32_16x16x32_bf16 v[48:51],a[192:195],a[44:47],v[16:19]
ds_read_u16 v52,v52
v_lshlrev_b32_sdwa v54,1,v54 dst_sel:DWORD dst_unused:UNUSED_PAD src0_sel:DWORD src1_sel:WORD_1
v_mfma_f32_16x16x32_bf16 v[48:51],a[216:219],a[28:31],v[48:51]
ds_read_u16_d16_hi v54,v54
v_lshlrev_b32_sdwa v53,1,v53 dst_sel:DWORD dst_unused:UNUSED_PAD src0_sel:DWORD src1_sel:WORD_1
v_mfma_f32_16x16x32_bf16 v[48:51],a[220:223],a[12:15],v[48:51]
v_exp_f16_sdwa v8,v9 dst_sel:WORD_1 dst_unused:UNUSED_PRESERVE src0_sel:WORD_1
v_mfma_f32_16x16x32_bf16 v[208:211],a[228:231],v[24:27],v[208:211]
ds_read_u16 v53,v53
v_lshlrev_b32_sdwa v55,1,v55 dst_sel:DWORD dst_unused:UNUSED_PAD src0_sel:DWORD src1_sel:WORD_1
v_mfma_f32_16x16x32_bf16 v[212:215],a[228:231],v[28:31],v[212:215]
ds_read_u16_d16_hi v55,v55
v_pk_fma_f16 v8,v8,s55,s55
v_mfma_f32_16x16x32_bf16 v[172:175],a[232:235],v[28:31],v[172:175]
ds_read_b64_tr_b16 a[228:229],v3 offset:64
v_lshlrev_b32_sdwa v56,1,v56 dst_sel:DWORD dst_unused:UNUSED_PAD src0_sel:DWORD src1_sel:WORD_1
v_mfma_f32_16x16x32_bf16 v[168:171],a[232:235],v[24:27],v[168:171]
ds_read_u16 v56,v56
v_rcp_f16 v9,v8
v_mfma_f32_16x16x32_bf16 v[216:219],a[236:239],v[24:27],v[216:219]
ds_read_b64_tr_b16 a[230:231],v3 offset:1600
v_lshlrev_b32_sdwa v58,1,v58 dst_sel:DWORD dst_unused:UNUSED_PAD src0_sel:DWORD src1_sel:WORD_1
v_mfma_f32_16x16x32_bf16 v[220:223],a[236:239],v[28:31],v[220:223]
ds_read_u16_d16_hi v58,v58
ds_read_b64_tr_b16 a[232:233],v3 offset:72
v_mfma_f32_16x16x32_bf16 v[188:191],a[244:247],v[28:31],v[188:191]
ds_read_b64_tr_b16 a[234:235],v3 offset:1608
v_lshlrev_b32_sdwa v40,1,v40 dst_sel:DWORD dst_unused:UNUSED_PAD src0_sel:DWORD src1_sel:WORD_1
v_mfma_f32_16x16x32_bf16 v[184:187],a[244:247],v[24:27],v[184:187]
ds_read_u16 v40,v40
v_rcp_f16_sdwa v9,v8 dst_sel:WORD_1 dst_unused:UNUSED_PRESERVE src0_sel:WORD_1
v_mfma_f32_16x16x32_bf16 v[224:227],a[248:251],v[24:27],v[224:227]
ds_read_b64_tr_b16 a[236:237],v3 offset:128
v_lshlrev_b32_sdwa v42,1,v42 dst_sel:DWORD dst_unused:UNUSED_PAD src0_sel:DWORD src1_sel:WORD_1
v_mfma_f32_16x16x32_bf16 v[228:231],a[248:251],v[28:31],v[228:231]
ds_read_u16_d16_hi v42,v42
v_fma_mix_f32 v57,v57,v9,0 op_sel_hi:[0,1,0]
v_mfma_f32_16x16x32_bf16 v[204:207],a[252:255],v[28:31],v[204:207]
ds_read_b64_tr_b16 a[238:239],v3 offset:1664
v_lshlrev_b32_sdwa v41,1,v41 dst_sel:DWORD dst_unused:UNUSED_PAD src0_sel:DWORD src1_sel:WORD_1
v_mfma_f32_16x16x32_bf16 v[200:203],a[252:255],v[24:27],v[200:203]
ds_read_u16 v41,v41
v_fma_mix_f32 v59,v59,v9,0 op_sel:[0,1,0] op_sel_hi:[0,1,0]
v_lshlrev_b32_sdwa v43,1,v43 dst_sel:DWORD dst_unused:UNUSED_PAD src0_sel:DWORD src1_sel:WORD_1
v_mfma_f32_16x16x32_bf16 v[28:31],a[220:223],a[48:51],v[16:19]
s_add_u32 m0,s57,10608
ds_read_b64 v[14:15],v7 offset:0
v_lshlrev_b32_sdwa v36,1,v36 dst_sel:DWORD dst_unused:UNUSED_PAD src0_sel:DWORD src1_sel:WORD_1
v_mfma_f32_16x16x32_bf16 v[28:31],a[216:219],a[64:67],v[28:31]
s_waitcnt lgkmcnt(9)
ds_read_u16_d16_hi v43,v43
v_lshl_add_u64 v[32:33],v[32:33],0,v[34:35]
v_mfma_f32_16x16x32_bf16 v[28:31],a[192:195],a[80:83],v[28:31]
s_add_u32 s89,s56,5210112
ds_read_b64_tr_b16 a[244:245],v3 offset:136
v_lshl_add_u64 v[34:35],v[60:61],0,v[62:63]
v_lshlrev_b32_sdwa v38,1,v38 dst_sel:DWORD dst_unused:UNUSED_PAD src0_sel:DWORD src1_sel:WORD_1
v_mfma_f32_16x16x32_bf16 v[24:27],a[196:199],a[80:83],v[20:23]
buffer_load_dwordx4 v6,s[36:39],s89 offen lds
v_lshlrev_b32_sdwa v37,1,v37 dst_sel:DWORD dst_unused:UNUSED_PAD src0_sel:DWORD src1_sel:WORD_1
v_mfma_f32_16x16x32_bf16 v[24:27],a[200:203],a[64:67],v[24:27]
s_add_u32 m0,s57,14152
ds_read_b64_tr_b16 a[246:247],v3 offset:1672
v_lshl_add_u64 v[60:61],v[52:53],0,v[54:55]
v_mfma_f32_16x16x32_bf16 v[24:27],a[212:215],a[48:51],v[24:27]
s_waitcnt lgkmcnt(3)
ds_read_u16 v36,v36
v_or3_b32 v13,v14,v15,v13
v_lshlrev_b32_sdwa v39,1,v39 dst_sel:DWORD dst_unused:UNUSED_PAD src0_sel:DWORD src1_sel:WORD_1
v_mfma_f32_16x16x32_bf16 v[52:55],a[212:215],a[52:55],v[20:23]
s_add_u32 s89,s56,5308416
ds_read_b64 v[14:15],v7 offset:512
v_or_b32 v62,v56,v58
v_mfma_f32_16x16x32_bf16 v[52:55],a[200:203],a[68:71],v[52:55]
buffer_load_dwordx4 v6,s[36:39],s89 offen lds
v_cvt_pk_bf16_f32 v63,v57,v59
v_lshlrev_b32_sdwa v48,1,v48 dst_sel:DWORD dst_unused:UNUSED_PAD src0_sel:DWORD src1_sel:WORD_1
v_mfma_f32_16x16x32_bf16 v[52:55],a[196:199],a[84:87],v[52:55]
ds_read_u16_d16_hi v38,v38
v_lshlrev_b32_sdwa v50,1,v50 dst_sel:DWORD dst_unused:UNUSED_PAD src0_sel:DWORD src1_sel:WORD_1
v_mfma_f32_16x16x32_bf16 v[56:59],a[192:195],a[84:87],v[16:19]
ds_read_u16 v37,v37
v_lshlrev_b32_sdwa v49,1,v49 dst_sel:DWORD dst_unused:UNUSED_PAD src0_sel:DWORD src1_sel:WORD_1
v_mfma_f32_16x16x32_bf16 v[56:59],a[216:219],a[68:71],v[56:59]
ds_read_u16_d16_hi v39,v39
v_lshlrev_b32_sdwa v51,1,v51 dst_sel:DWORD dst_unused:UNUSED_PAD src0_sel:DWORD src1_sel:WORD_1
v_mfma_f32_16x16x32_bf16 v[56:59],a[220:223],a[52:55],v[56:59]
s_waitcnt lgkmcnt(3)
ds_read_u16 v48,v48
v_or3_b32 v13,v14,v15,v13
v_mfma_f32_16x16x32_bf16 v[136:139],a[240:243],v[32:35],v[136:139]
ds_read_b64 v[14:15],v7 offset:1024
v_lshlrev_b32_sdwa v44,1,v44 dst_sel:DWORD dst_unused:UNUSED_PAD src0_sel:DWORD src1_sel:WORD_1
v_mfma_f32_16x16x32_bf16 v[140:143],a[240:243],v[60:63],v[140:143]
ds_read_u16_d16_hi v50,v50
v_lshlrev_b32_sdwa v46,1,v46 dst_sel:DWORD dst_unused:UNUSED_PAD src0_sel:DWORD src1_sel:WORD_1
v_mfma_f32_16x16x32_bf16 v[68:71],a[224:227],v[60:63],v[68:71]
ds_read_u16 v49,v49
v_lshlrev_b32_sdwa v45,1,v45 dst_sel:DWORD dst_unused:UNUSED_PAD src0_sel:DWORD src1_sel:WORD_1
v_mfma_f32_16x16x32_bf16 v[64:67],a[224:227],v[32:35],v[64:67]
ds_read_u16_d16_hi v51,v51
v_mfma_f32_16x16x32_bf16 v[144:147],a[228:231],v[32:35],v[144:147]
ds_read_u16 v44,v44
v_lshlrev_b32_sdwa v47,1,v47 dst_sel:DWORD dst_unused:UNUSED_PAD src0_sel:DWORD src1_sel:WORD_1
v_mfma_f32_16x16x32_bf16 v[148:151],a[228:231],v[60:63],v[148:151]
s_waitcnt lgkmcnt(4)
ds_read_u16_d16_hi v46,v46
v_or3_b32 v13,v14,v15,v13
v_mfma_f32_16x16x32_bf16 v[84:87],a[232:235],v[60:63],v[84:87]
ds_read_u16 v45,v45
v_lshlrev_b32_sdwa v28,1,v28 dst_sel:DWORD dst_unused:UNUSED_PAD src0_sel:DWORD src1_sel:WORD_1
v_mfma_f32_16x16x32_bf16 v[80:83],a[232:235],v[32:35],v[80:83]
TP3807_%=:
s_cbranch_execz TP5943_%=
ds_read_u16_d16_hi v47,v47
v_lshlrev_b32_sdwa v30,1,v30 dst_sel:DWORD dst_unused:UNUSED_PAD src0_sel:DWORD src1_sel:WORD_1
v_mfma_f32_16x16x32_bf16 v[152:155],a[236:239],v[32:35],v[152:155]
s_add_u32 m0,s57,17688
ds_read_u16 v28,v28
v_lshlrev_b32_sdwa v29,1,v29 dst_sel:DWORD dst_unused:UNUSED_PAD src0_sel:DWORD src1_sel:WORD_1
v_mfma_f32_16x16x32_bf16 v[156:159],a[236:239],v[60:63],v[156:159]
s_add_u32 s89,s56,5406720
ds_read_u16_d16_hi v30,v30
v_mfma_f32_16x16x32_bf16 v[100:103],a[244:247],v[60:63],v[100:103]
buffer_load_dwordx4 v6,s[36:39],s89 offen lds
v_lshlrev_b32_sdwa v31,1,v31 dst_sel:DWORD dst_unused:UNUSED_PAD src0_sel:DWORD src1_sel:WORD_1
v_mfma_f32_16x16x32_bf16 v[96:99],a[244:247],v[32:35],v[96:99]
ds_read_u16 v29,v29
v_mfma_f32_16x16x32_bf16 v[60:63],a[220:223],a[56:59],v[16:19]
ds_read_u16_d16_hi v31,v31
v_lshlrev_b32_sdwa v24,1,v24 dst_sel:DWORD dst_unused:UNUSED_PAD src0_sel:DWORD src1_sel:WORD_1
v_mfma_f32_16x16x32_bf16 v[60:63],a[216:219],a[72:75],v[60:63]
ds_read_u16 v24,v24
v_lshl_add_u64 v[40:41],v[40:41],0,v[42:43]
v_fma_mixlo_f16 v8,v53,s99,0
v_mfma_f32_16x16x32_bf16 v[60:63],a[192:195],a[88:91],v[60:63]
v_lshl_add_u64 v[42:43],v[36:37],0,v[38:39]
v_lshlrev_b32_sdwa v26,1,v26 dst_sel:DWORD dst_unused:UNUSED_PAD src0_sel:DWORD src1_sel:WORD_1
v_mfma_f32_16x16x32_bf16 v[32:35],a[196:199],a[88:91],v[20:23]
ds_read_u16_d16_hi v26,v26
v_fma_mixhi_f16 v8,v55,s99,0
v_mfma_f32_16x16x32_bf16 v[32:35],a[200:203],a[72:75],v[32:35]
s_waitcnt lgkmcnt(6)
v_lshl_add_u64 v[36:37],v[48:49],0,v[50:51]
v_lshlrev_b32_sdwa v25,1,v25 dst_sel:DWORD dst_unused:UNUSED_PAD src0_sel:DWORD src1_sel:WORD_1
v_pk_fma_f16 v9,v8,v8,s54
v_mfma_f32_16x16x32_bf16 v[32:35],a[212:215],a[56:59],v[32:35]
ds_read_u16 v25,v25
v_pk_mul_f16 v9,v8,v9
v_mfma_f32_16x16x32_bf16 v[48:51],a[212:215],a[60:63],v[20:23]
ds_read_b128 v[20:23],v4 offset:1152
v_lshlrev_b32_sdwa v27,1,v27 dst_sel:DWORD dst_unused:UNUSED_PAD src0_sel:DWORD src1_sel:WORD_1
v_mfma_f32_16x16x32_bf16 v[48:51],a[200:203],a[76:79],v[48:51]
ds_read_b64_tr_b16 a[212:213],v1 offset:4096
ds_read_b64_tr_b16 a[214:215],v1 offset:5120
v_lshl_add_u64 v[38:39],v[44:45],0,v[46:47]
v_mfma_f32_16x16x32_bf16 v[48:51],a[196:199],a[92:95],v[48:51]
ds_read_u16_d16_hi v27,v27
v_exp_f16 v8,v9
v_lshlrev_b32_sdwa v56,1,v56 dst_sel:DWORD dst_unused:UNUSED_PAD src0_sel:DWORD src1_sel:WORD_1
v_mfma_f32_16x16x32_bf16 v[44:47],a[192:195],a[92:95],v[16:19]
ds_read_b128 v[16:19],v4 offset:1184
ds_read_b64_tr_b16 a[200:201],v2 offset:4096
v_mfma_f32_16x16x32_bf16 v[44:47],a[216:219],a[76:79],v[44:47]
ds_read_b64_tr_b16 a[202:203],v2 offset:5120
ds_read_b64_tr_b16 a[196:197],v2 offset:2048
v_lshlrev_b32_sdwa v58,1,v58 dst_sel:DWORD dst_unused:UNUSED_PAD src0_sel:DWORD src1_sel:WORD_1
v_mfma_f32_16x16x32_bf16 v[44:47],a[220:223],a[60:63],v[44:47]
ds_read_b64_tr_b16 a[198:199],v2 offset:3072
v_exp_f16_sdwa v8,v9 dst_sel:WORD_1 dst_unused:UNUSED_PRESERVE src0_sel:WORD_1
v_mfma_f32_16x16x32_bf16 v[112:115],a[240:243],v[40:43],v[112:115]
ds_read_u16 v56,v56
v_lshlrev_b32_sdwa v57,1,v57 dst_sel:DWORD dst_unused:UNUSED_PAD src0_sel:DWORD src1_sel:WORD_1
v_mfma_f32_16x16x32_bf16 v[116:119],a[240:243],v[36:39],v[116:119]
ds_read_u16_d16_hi v58,v58
v_lshlrev_b32_sdwa v59,1,v59 dst_sel:DWORD dst_unused:UNUSED_PAD src0_sel:DWORD src1_sel:WORD_1
v_mfma_f32_16x16x32_bf16 v[76:79],a[224:227],v[36:39],v[76:79]
ds_read_u16 v57,v57
v_pk_fma_f16 v8,v8,s55,s55
v_mfma_f32_16x16x32_bf16 v[72:75],a[224:227],v[40:43],v[72:75]
v_rcp_f16 v9,v8
v_lshlrev_b32_sdwa v52,1,v52 dst_sel:DWORD dst_unused:UNUSED_PAD src0_sel:DWORD src1_sel:WORD_1
v_mfma_f32_16x16x32_bf16 v[120:123],a[228:231],v[40:43],v[120:123]
ds_read_u16_d16_hi v59,v59
v_lshlrev_b32_sdwa v54,1,v54 dst_sel:DWORD dst_unused:UNUSED_PAD src0_sel:DWORD src1_sel:WORD_1
v_mfma_f32_16x16x32_bf16 v[124:127],a[228:231],v[36:39],v[124:127]
ds_read_b64_tr_b16 a[192:193],v2 offset:0
v_lshlrev_b32_sdwa v60,1,v60 dst_sel:DWORD dst_unused:UNUSED_PAD src0_sel:DWORD src1_sel:WORD_1
v_mfma_f32_16x16x32_bf16 v[92:95],a[232:235],v[36:39],v[92:95]
ds_read_b64_tr_b16 a[194:195],v2 offset:1024
v_lshlrev_b32_sdwa v62,1,v62 dst_sel:DWORD dst_unused:UNUSED_PAD src0_sel:DWORD src1_sel:WORD_1
v_mfma_f32_16x16x32_bf16 v[88:91],a[232:235],v[40:43],v[88:91]
ds_read_u16 v52,v52
v_rcp_f16_sdwa v9,v8 dst_sel:WORD_1 dst_unused:UNUSED_PRESERVE src0_sel:WORD_1
v_mfma_f32_16x16x32_bf16 v[128:131],a[236:239],v[40:43],v[128:131]
ds_read_u16_d16_hi v54,v54
v_fma_mix_f32 v53,v53,v9,0 op_sel_hi:[0,1,0]
v_mfma_f32_16x16x32_bf16 v[132:135],a[236:239],v[36:39],v[132:135]
ds_read_u16 v60,v60
v_lshlrev_b32_sdwa v61,1,v61 dst_sel:DWORD dst_unused:UNUSED_PAD src0_sel:DWORD src1_sel:WORD_1
v_mfma_f32_16x16x32_bf16 v[108:111],a[244:247],v[36:39],v[108:111]
ds_read_u16_d16_hi v62,v62
v_fma_mix_f32 v55,v55,v9,0 op_sel:[0,1,0] op_sel_hi:[0,1,0]
v_mfma_f32_16x16x32_bf16 v[104:107],a[244:247],v[40:43],v[104:107]
s_waitcnt lgkmcnt(7)
ds_read_u16 v61,v61
v_lshlrev_b32_sdwa v63,1,v63 dst_sel:DWORD dst_unused:UNUSED_PAD src0_sel:DWORD src1_sel:WORD_1
v_mfma_f32_16x16x32_bf16 v[36:39],a[204:207],a[0:3],v[20:23]
s_add_u32 m0,s57,21216
ds_read_u16_d16_hi v63,v63
v_lshlrev_b32_sdwa v32,1,v32 dst_sel:DWORD dst_unused:UNUSED_PAD src0_sel:DWORD src1_sel:WORD_1
v_mfma_f32_16x16x32_bf16 v[36:39],a[208:211],a[16:19],v[36:39]
s_add_u32 s89,s56,5505024
ds_read_u16 v32,v32
v_lshl_add_u64 v[28:29],v[28:29],0,v[30:31]
v_mfma_f32_16x16x32_bf16 v[36:39],a[212:215],a[32:35],v[36:39]
buffer_load_dwordx4 v6,s[36:39],s89 offen lds
v_lshl_add_u64 v[30:31],v[24:25],0,v[26:27]
v_lshlrev_b32_sdwa v34,1,v34 dst_sel:DWORD dst_unused:UNUSED_PAD src0_sel:DWORD src1_sel:WORD_1
v_mfma_f32_16x16x32_bf16 v[40:43],a[200:203],a[32:35],v[16:19]
ds_read_u16_d16_hi v34,v34
v_lshlrev_b32_sdwa v33,1,v33 dst_sel:DWORD dst_unused:UNUSED_PAD src0_sel:DWORD src1_sel:WORD_1
v_mfma_f32_16x16x32_bf16 v[40:43],a[196:199],a[16:19],v[40:43]
s_waitcnt lgkmcnt(7)
ds_read_u16 v33,v33
v_lshl_add_u64 v[24:25],v[56:57],0,v[58:59]
v_mfma_f32_16x16x32_bf16 v[40:43],a[192:195],a[0:3],v[40:43]
v_lshlrev_b32_sdwa v35,1,v35 dst_sel:DWORD dst_unused:UNUSED_PAD src0_sel:DWORD src1_sel:WORD_1
v_lshlrev_b32_sdwa v44,1,v44 dst_sel:DWORD dst_unused:UNUSED_PAD src0_sel:DWORD src1_sel:WORD_1
v_mfma_f32_16x16x32_bf16 v[56:59],a[192:195],a[4:7],v[16:19]
s_waitcnt lgkmcnt(6)
ds_read_u16_d16_hi v35,v35
v_or_b32 v26,v52,v54
v_mfma_f32_16x16x32_bf16 v[56:59],a[196:199],a[20:23],v[56:59]
s_waitcnt vmcnt(7)
ds_read_u16 v44,v44
v_cvt_pk_bf16_f32 v27,v53,v55
v_mfma_f32_16x16x32_bf16 v[56:59],a[200:203],a[36:39],v[56:59]
s_barrier
v_lshlrev_b32_sdwa v46,1,v46 dst_sel:DWORD dst_unused:UNUSED_PAD src0_sel:DWORD src1_sel:WORD_1
v_lshlrev_b32_sdwa v45,1,v45 dst_sel:DWORD dst_unused:UNUSED_PAD src0_sel:DWORD src1_sel:WORD_1
v_mfma_f32_16x16x32_bf16 v[52:55],a[212:215],a[36:39],v[20:23]
ds_read_u16_d16_hi v46,v46
v_lshlrev_b32_sdwa v47,1,v47 dst_sel:DWORD dst_unused:UNUSED_PAD src0_sel:DWORD src1_sel:WORD_1
v_mfma_f32_16x16x32_bf16 v[52:55],a[208:211],a[20:23],v[52:55]
s_add_u32 m0,s58,18480
ds_read_u16 v45,v45
v_mfma_f32_16x16x32_bf16 v[52:55],a[204:207],a[4:7],v[52:55]
s_mov_b32 s69,s88
ds_read_u16_d16_hi v47,v47
v_lshlrev_b32_sdwa v48,1,v48 dst_sel:DWORD dst_unused:UNUSED_PAD src0_sel:DWORD src1_sel:WORD_1
v_mfma_f32_16x16x32_bf16 v[232:235],a[240:243],v[28:31],v[232:235]
buffer_load_dwordx4 v5,s[44:47],s69 offen offset:0 lds
v_lshlrev_b32_sdwa v50,1,v50 dst_sel:DWORD dst_unused:UNUSED_PAD src0_sel:DWORD src1_sel:WORD_1
v_mfma_f32_16x16x32_bf16 v[236:239],a[240:243],v[24:27],v[236:239]
ds_read_u16 v48,v48
v_mfma_f32_16x16x32_bf16 v[164:167],a[224:227],v[24:27],v[164:167]
ds_read_u16_d16_hi v50,v50
ds_read_b64_tr_b16 a[248:249],v3 offset:6160
v_mfma_f32_16x16x32_bf16 v[160:163],a[224:227],v[28:31],v[160:163]
ds_read_b64_tr_b16 a[250:251],v3 offset:7696
v_lshlrev_b32_sdwa v49,1,v49 dst_sel:DWORD dst_unused:UNUSED_PAD src0_sel:DWORD src1_sel:WORD_1
v_mfma_f32_16x16x32_bf16 v[240:243],a[228:231],v[28:31],v[240:243]
s_add_u32 s69,s69,s53
ds_read_u16 v49,v49
v_lshlrev_b32_sdwa v51,1,v51 dst_sel:DWORD dst_unused:UNUSED_PAD src0_sel:DWORD src1_sel:WORD_1
v_mfma_f32_16x16x32_bf16 v[244:247],a[228:231],v[24:27],v[244:247]
buffer_load_dwordx4 v5,s[44:47],s69 offen offset:1024 lds
v_lshlrev_b32_sdwa v36,1,v36 dst_sel:DWORD dst_unused:UNUSED_PAD src0_sel:DWORD src1_sel:WORD_1
v_mfma_f32_16x16x32_bf16 v[180:183],a[232:235],v[24:27],v[180:183]
ds_read_u16_d16_hi v51,v51
v_mfma_f32_16x16x32_bf16 v[176:179],a[232:235],v[28:31],v[176:179]
ds_read_u16 v36,v36
v_lshlrev_b32_sdwa v38,1,v38 dst_sel:DWORD dst_unused:UNUSED_PAD src0_sel:DWORD src1_sel:WORD_1
v_mfma_f32_16x16x32_bf16 v[248:251],a[236:239],v[28:31],v[248:251]
ds_read_u16_d16_hi v38,v38
v_lshlrev_b32_sdwa v37,1,v37 dst_sel:DWORD dst_unused:UNUSED_PAD src0_sel:DWORD src1_sel:WORD_1
v_mfma_f32_16x16x32_bf16 v[252:255],a[236:239],v[24:27],v[252:255]
ds_read_u16 v37,v37
v_mfma_f32_16x16x32_bf16 v[196:199],a[244:247],v[24:27],v[196:199]
s_add_u32 s69,s69,s53
ds_read_b64_tr_b16 a[252:253],v3 offset:6168
v_lshlrev_b32_sdwa v39,1,v39 dst_sel:DWORD dst_unused:UNUSED_PAD src0_sel:DWORD src1_sel:WORD_1
v_mfma_f32_16x16x32_bf16 v[192:195],a[244:247],v[28:31],v[192:195]
buffer_load_dwordx4 v5,s[44:47],s69 offen offset:2048 lds
v_mfma_f32_16x16x32_bf16 v[24:27],a[204:207],a[8:11],v[20:23]
ds_read_u16_d16_hi v39,v39
v_lshlrev_b32_sdwa v40,1,v40 dst_sel:DWORD dst_unused:UNUSED_PAD src0_sel:DWORD src1_sel:WORD_1
v_mfma_f32_16x16x32_bf16 v[24:27],a[208:211],a[24:27],v[24:27]
s_waitcnt lgkmcnt(6)
ds_read_u16 v40,v40
v_lshl_add_u64 v[60:61],v[60:61],0,v[62:63]
v_fma_mixlo_f16 v8,v57,s99,0
v_mfma_f32_16x16x32_bf16 v[24:27],a[212:215],a[40:43],v[24:27]
ds_read_b64_tr_b16 a[254:255],v3 offset:7704
v_lshl_add_u64 v[62:63],v[32:33],0,v[34:35]
v_lshlrev_b32_sdwa v42,1,v42 dst_sel:DWORD dst_unused:UNUSED_PAD src0_sel:DWORD src1_sel:WORD_1
v_mfma_f32_16x16x32_bf16 v[28:31],a[200:203],a[40:43],v[16:19]
ds_read_u16_d16_hi v42,v42
v_fma_mixhi_f16 v8,v59,s99,0
v_mfma_f32_16x16x32_bf16 v[28:31],a[196:199],a[24:27],v[28:31]
ds_read_b64_tr_b16 a[216:217],v1 offset:6160
v_lshl_add_u64 v[32:33],v[44:45],0,v[46:47]
v_lshlrev_b32_sdwa v41,1,v41 dst_sel:DWORD dst_unused:UNUSED_PAD src0_sel:DWORD src1_sel:WORD_1
v_pk_fma_f16 v9,v8,v8,s54
v_mfma_f32_16x16x32_bf16 v[28:31],a[192:195],a[8:11],v[28:31]
ds_read_u16 v41,v41
v_pk_mul_f16 v9,v8,v9
v_mfma_f32_16x16x32_bf16 v[44:47],a[192:195],a[12:15],v[16:19]
ds_read_b64_tr_b16 a[218:219],v1 offset:7184
v_lshlrev_b32_sdwa v43,1,v43 dst_sel:DWORD dst_unused:UNUSED_PAD src0_sel:DWORD src1_sel:WORD_1
v_mfma_f32_16x16x32_bf16 v[44:47],a[196:199],a[28:31],v[44:47]
s_waitcnt lgkmcnt(7)
ds_read_u16_d16_hi v43,v43
v_lshl_add_u64 v[34:35],v[48:49],0,v[50:51]
v_mfma_f32_16x16x32_bf16 v[44:47],a[200:203],a[44:47],v[44:47]
v_exp_f16 v8,v9
v_lshlrev_b32_sdwa v52,1,v52 dst_sel:DWORD dst_unused:UNUSED_PAD src0_sel:DWORD src1_sel:WORD_1
v_mfma_f32_16x16x32_bf16 v[48:51],a[212:215],a[44:47],v[20:23]
ds_read_u16 v52,v52
v_lshlrev_b32_sdwa v54,1,v54 dst_sel:DWORD dst_unused:UNUSED_PAD src0_sel:DWORD src1_sel:WORD_1
v_mfma_f32_16x16x32_bf16 v[48:51],a[208:211],a[28:31],v[48:51]
ds_read_u16_d16_hi v54,v54
v_lshlrev_b32_sdwa v53,1,v53 dst_sel:DWORD dst_unused:UNUSED_PAD src0_sel:DWORD src1_sel:WORD_1
v_mfma_f32_16x16x32_bf16 v[48:51],a[204:207],a[12:15],v[48:51]
v_exp_f16_sdwa v8,v9 dst_sel:WORD_1 dst_unused:UNUSED_PRESERVE src0_sel:WORD_1
v_mfma_f32_16x16x32_bf16 v[208:211],a[240:243],v[60:63],v[208:211]
ds_read_u16 v53,v53
v_lshlrev_b32_sdwa v55,1,v55 dst_sel:DWORD dst_unused:UNUSED_PAD src0_sel:DWORD src1_sel:WORD_1
v_mfma_f32_16x16x32_bf16 v[212:215],a[240:243],v[32:35],v[212:215]
ds_read_u16_d16_hi v55,v55
v_pk_fma_f16 v8,v8,s55,s55
v_mfma_f32_16x16x32_bf16 v[172:175],a[224:227],v[32:35],v[172:175]
ds_read_b64_tr_b16 a[240:241],v3 offset:6224
v_lshlrev_b32_sdwa v56,1,v56 dst_sel:DWORD dst_unused:UNUSED_PAD src0_sel:DWORD src1_sel:WORD_1
v_mfma_f32_16x16x32_bf16 v[168:171],a[224:227],v[60:63],v[168:171]
ds_read_u16 v56,v56
v_rcp_f16 v9,v8
v_mfma_f32_16x16x32_bf16 v[216:219],a[228:231],v[60:63],v[216:219]
ds_read_b64_tr_b16 a[242:243],v3 offset:7760
v_lshlrev_b32_sdwa v58,1,v58 dst_sel:DWORD dst_unused:UNUSED_PAD src0_sel:DWORD src1_sel:WORD_1
v_mfma_f32_16x16x32_bf16 v[220:223],a[228:231],v[32:35],v[220:223]
ds_read_u16_d16_hi v58,v58
ds_read_b64_tr_b16 a[224:225],v3 offset:6232
v_mfma_f32_16x16x32_bf16 v[188:191],a[232:235],v[32:35],v[188:191]
ds_read_b64_tr_b16 a[226:227],v3 offset:7768
v_lshlrev_b32_sdwa v24,1,v24 dst_sel:DWORD dst_unused:UNUSED_PAD src0_sel:DWORD src1_sel:WORD_1
v_mfma_f32_16x16x32_bf16 v[184:187],a[232:235],v[60:63],v[184:187]
ds_read_u16 v24,v24
v_rcp_f16_sdwa v9,v8 dst_sel:WORD_1 dst_unused:UNUSED_PRESERVE src0_sel:WORD_1
v_mfma_f32_16x16x32_bf16 v[224:227],a[236:239],v[60:63],v[224:227]
ds_read_b64_tr_b16 a[228:229],v3 offset:6288
v_lshlrev_b32_sdwa v26,1,v26 dst_sel:DWORD dst_unused:UNUSED_PAD src0_sel:DWORD src1_sel:WORD_1
v_mfma_f32_16x16x32_bf16 v[228:231],a[236:239],v[32:35],v[228:231]
ds_read_u16_d16_hi v26,v26
v_fma_mix_f32 v57,v57,v9,0 op_sel_hi:[0,1,0]
v_mfma_f32_16x16x32_bf16 v[204:207],a[244:247],v[32:35],v[204:207]
ds_read_b64_tr_b16 a[230:231],v3 offset:7824
v_lshlrev_b32_sdwa v25,1,v25 dst_sel:DWORD dst_unused:UNUSED_PAD src0_sel:DWORD src1_sel:WORD_1
v_mfma_f32_16x16x32_bf16 v[200:203],a[244:247],v[60:63],v[200:203]
ds_read_u16 v25,v25
v_fma_mix_f32 v59,v59,v9,0 op_sel:[0,1,0] op_sel_hi:[0,1,0]
v_lshlrev_b32_sdwa v27,1,v27 dst_sel:DWORD dst_unused:UNUSED_PAD src0_sel:DWORD src1_sel:WORD_1
v_mfma_f32_16x16x32_bf16 v[32:35],a[204:207],a[48:51],v[20:23]
s_add_u32 m0,s58,0
ds_read_b64 v[14:15],v7 offset:6160
ds_read_u16_d16_hi v27,v27
v_lshlrev_b32_sdwa v28,1,v28 dst_sel:DWORD dst_unused:UNUSED_PAD src0_sel:DWORD src1_sel:WORD_1
v_mfma_f32_16x16x32_bf16 v[32:35],a[208:211],a[64:67],v[32:35]
s_waitcnt lgkmcnt(10)
ds_read_b64_tr_b16 a[232:233],v3 offset:6296
ds_read_b64_tr_b16 a[234:235],v3 offset:7832
v_lshl_add_u64 v[36:37],v[36:37],0,v[38:39]
v_mfma_f32_16x16x32_bf16 v[32:35],a[212:215],a[80:83],v[32:35]
s_mov_b32 s69,s87
ds_read_u16 v28,v28
v_lshl_add_u64 v[38:39],v[40:41],0,v[42:43]
v_lshlrev_b32_sdwa v30,1,v30 dst_sel:DWORD dst_unused:UNUSED_PAD src0_sel:DWORD src1_sel:WORD_1
v_mfma_f32_16x16x32_bf16 v[60:63],a[200:203],a[80:83],v[16:19]
buffer_load_dwordx4 v5,s[44:47],s69 offen offset:0 lds
v_lshlrev_b32_sdwa v29,1,v29 dst_sel:DWORD dst_unused:UNUSED_PAD src0_sel:DWORD src1_sel:WORD_1
v_mfma_f32_16x16x32_bf16 v[60:63],a[196:199],a[64:67],v[60:63]
s_add_u32 s69,s69,s53
ds_read_u16_d16_hi v30,v30
v_lshl_add_u64 v[40:41],v[52:53],0,v[54:55]
v_mfma_f32_16x16x32_bf16 v[60:63],a[192:195],a[48:51],v[60:63]
s_waitcnt lgkmcnt(5)
buffer_load_dwordx4 v5,s[44:47],s69 offen offset:1024 lds
v_or3_b32 v13,v14,v15,v13
v_lshlrev_b32_sdwa v31,1,v31 dst_sel:DWORD dst_unused:UNUSED_PAD src0_sel:DWORD src1_sel:WORD_1
v_mfma_f32_16x16x32_bf16 v[52:55],a[192:195],a[52:55],v[16:19]
s_add_u32 s69,s69,s53
ds_read_b64 v[14:15],v7 offset:6672
ds_read_u16 v29,v29
v_or_b32 v42,v56,v58
v_mfma_f32_16x16x32_bf16 v[52:55],a[196:199],a[68:71],v[52:55]
buffer_load_dwordx4 v5,s[44:47],s69 offen offset:2048 lds
v_cvt_pk_bf16_f32 v43,v57,v59
v_lshlrev_b32_sdwa v48,1,v48 dst_sel:DWORD dst_unused:UNUSED_PAD src0_sel:DWORD src1_sel:WORD_1
v_mfma_f32_16x16x32_bf16 v[52:55],a[200:203],a[84:87],v[52:55]
ds_read_u16_d16_hi v31,v31
v_lshlrev_b32_sdwa v50,1,v50 dst_sel:DWORD dst_unused:UNUSED_PAD src0_sel:DWORD src1_sel:WORD_1
v_mfma_f32_16x16x32_bf16 v[56:59],a[212:215],a[84:87],v[20:23]
ds_read_u16 v48,v48
v_lshlrev_b32_sdwa v49,1,v49 dst_sel:DWORD dst_unused:UNUSED_PAD src0_sel:DWORD src1_sel:WORD_1
v_mfma_f32_16x16x32_bf16 v[56:59],a[208:211],a[68:71],v[56:59]
ds_read_u16_d16_hi v50,v50
v_lshlrev_b32_sdwa v51,1,v51 dst_sel:DWORD dst_unused:UNUSED_PAD src0_sel:DWORD src1_sel:WORD_1
v_mfma_f32_16x16x32_bf16 v[56:59],a[204:207],a[52:55],v[56:59]
s_waitcnt lgkmcnt(4)
ds_read_u16 v49,v49
v_or3_b32 v13,v14,v15,v13
v_mfma_f32_16x16x32_bf16 v[136:139],a[248:251],v[36:39],v[136:139]
ds_read_b64 v[14:15],v7 offset:7184
v_lshlrev_b32_sdwa v44,1,v44 dst_sel:DWORD dst_unused:UNUSED_PAD src0_sel:DWORD src1_sel:WORD_1
v_mfma_f32_16x16x32_bf16 v[140:143],a[248:251],v[40:43],v[140:143]
ds_read_u16_d16_hi v51,v51
v_lshlrev_b32_sdwa v46,1,v46 dst_sel:DWORD dst_unused:UNUSED_PAD src0_sel:DWORD src1_sel:WORD_1
v_mfma_f32_16x16x32_bf16 v[68:71],a[252:255],v[40:43],v[68:71]
ds_read_u16 v44,v44
v_lshlrev_b32_sdwa v45,1,v45 dst_sel:DWORD dst_unused:UNUSED_PAD src0_sel:DWORD src1_sel:WORD_1
v_mfma_f32_16x16x32_bf16 v[64:67],a[252:255],v[36:39],v[64:67]
ds_read_u16_d16_hi v46,v46
v_mfma_f32_16x16x32_bf16 v[144:147],a[240:243],v[36:39],v[144:147]
ds_read_u16 v45,v45
v_lshlrev_b32_sdwa v47,1,v47 dst_sel:DWORD dst_unused:UNUSED_PAD src0_sel:DWORD src1_sel:WORD_1
v_mfma_f32_16x16x32_bf16 v[148:151],a[240:243],v[40:43],v[148:151]
s_waitcnt lgkmcnt(4)
ds_read_u16_d16_hi v47,v47
v_or3_b32 v13,v14,v15,v13
v_mfma_f32_16x16x32_bf16 v[84:87],a[224:227],v[40:43],v[84:87]
ds_read_b64_tr_b16 a[220:221],v1 offset:8208
v_lshlrev_b32_sdwa v32,1,v32 dst_sel:DWORD dst_unused:UNUSED_PAD src0_sel:DWORD src1_sel:WORD_1
v_mfma_f32_16x16x32_bf16 v[80:83],a[224:227],v[36:39],v[80:83]
ds_read_u16 v32,v32
v_lshlrev_b32_sdwa v34,1,v34 dst_sel:DWORD dst_unused:UNUSED_PAD src0_sel:DWORD src1_sel:WORD_1
v_mfma_f32_16x16x32_bf16 v[152:155],a[228:231],v[36:39],v[152:155]
ds_read_u16_d16_hi v34,v34
v_lshlrev_b32_sdwa v33,1,v33 dst_sel:DWORD dst_unused:UNUSED_PAD src0_sel:DWORD src1_sel:WORD_1
v_mfma_f32_16x16x32_bf16 v[156:159],a[228:231],v[40:43],v[156:159]
ds_read_u16 v33,v33
v_mfma_f32_16x16x32_bf16 v[100:103],a[232:235],v[40:43],v[100:103]
ds_read_b64_tr_b16 a[222:223],v1 offset:9232
v_lshlrev_b32_sdwa v35,1,v35 dst_sel:DWORD dst_unused:UNUSED_PAD src0_sel:DWORD src1_sel:WORD_1
v_mfma_f32_16x16x32_bf16 v[96:99],a[232:235],v[36:39],v[96:99]
ds_read_u16_d16_hi v35,v35
v_mfma_f32_16x16x32_bf16 v[40:43],a[204:207],a[56:59],v[20:23]
v_lshlrev_b32_sdwa v60,1,v60 dst_sel:DWORD dst_unused:UNUSED_PAD src0_sel:DWORD src1_sel:WORD_1
v_fma_mixlo_f16 v8,v53,s99,0
v_mfma_f32_16x16x32_bf16 v[40:43],a[208:211],a[72:75],v[40:43]
ds_read_u16 v60,v60
v_lshl_add_u64 v[24:25],v[24:25],0,v[26:27]
v_mfma_f32_16x16x32_bf16 v[40:43],a[212:215],a[88:91],v[40:43]
v_lshl_add_u64 v[26:27],v[28:29],0,v[30:31]
v_lshlrev_b32_sdwa v62,1,v62 dst_sel:DWORD dst_unused:UNUSED_PAD src0_sel:DWORD src1_sel:WORD_1
v_mfma_f32_16x16x32_bf16 v[36:39],a[200:203],a[88:91],v[16:19]
ds_read_u16_d16_hi v62,v62
v_fma_mixhi_f16 v8,v55,s99,0
v_mfma_f32_16x16x32_bf16 v[36:39],a[196:199],a[72:75],v[36:39]
s_waitcnt lgkmcnt(6)
v_lshl_add_u64 v[28:29],v[48:49],0,v[50:51]
v_lshlrev_b32_sdwa v61,1,v61 dst_sel:DWORD dst_unused:UNUSED_PAD src0_sel:DWORD src1_sel:WORD_1
v_pk_fma_f16 v9,v8,v8,s54
v_mfma_f32_16x16x32_bf16 v[36:39],a[192:195],a[56:59],v[36:39]
ds_read_u16 v61,v61
v_pk_mul_f16 v9,v8,v9
v_mfma_f32_16x16x32_bf16 v[48:51],a[192:195],a[60:63],v[16:19]
ds_read_b128 v[16:19],v4 offset:1280
v_lshlrev_b32_sdwa v63,1,v63 dst_sel:DWORD dst_unused:UNUSED_PAD src0_sel:DWORD src1_sel:WORD_1
v_mfma_f32_16x16x32_bf16 v[48:51],a[196:199],a[76:79],v[48:51]
ds_read_b64_tr_b16 a[192:193],v1 offset:10256
ds_read_b64_tr_b16 a[194:195],v1 offset:11280
v_lshl_add_u64 v[30:31],v[44:45],0,v[46:47]
v_mfma_f32_16x16x32_bf16 v[48:51],a[200:203],a[92:95],v[48:51]
ds_read_u16_d16_hi v63,v63
v_exp_f16 v8,v9
v_lshlrev_b32_sdwa v56,1,v56 dst_sel:DWORD dst_unused:UNUSED_PAD src0_sel:DWORD src1_sel:WORD_1
v_mfma_f32_16x16x32_bf16 v[44:47],a[212:215],a[92:95],v[20:23]
ds_read_b128 v[20:23],v4 offset:1312
ds_read_b64_tr_b16 a[196:197],v2 offset:10256
v_mfma_f32_16x16x32_bf16 v[44:47],a[208:211],a[76:79],v[44:47]
ds_read_b64_tr_b16 a[198:199],v2 offset:11280
ds_read_b64_tr_b16 a[200:201],v2 offset:8208
v_lshlrev_b32_sdwa v58,1,v58 dst_sel:DWORD dst_unused:UNUSED_PAD src0_sel:DWORD src1_sel:WORD_1
v_mfma_f32_16x16x32_bf16 v[44:47],a[204:207],a[60:63],v[44:47]
ds_read_b64_tr_b16 a[202:203],v2 offset:9232
v_exp_f16_sdwa v8,v9 dst_sel:WORD_1 dst_unused:UNUSED_PRESERVE src0_sel:WORD_1
v_mfma_f32_16x16x32_bf16 v[112:115],a[248:251],v[24:27],v[112:115]
ds_read_u16 v56,v56
v_lshlrev_b32_sdwa v57,1,v57 dst_sel:DWORD dst_unused:UNUSED_PAD src0_sel:DWORD src1_sel:WORD_1
v_mfma_f32_16x16x32_bf16 v[116:119],a[248:251],v[28:31],v[116:119]
ds_read_u16_d16_hi v58,v58
v_lshlrev_b32_sdwa v59,1,v59 dst_sel:DWORD dst_unused:UNUSED_PAD src0_sel:DWORD src1_sel:WORD_1
v_mfma_f32_16x16x32_bf16 v[76:79],a[252:255],v[28:31],v[76:79]
ds_read_u16 v57,v57
v_pk_fma_f16 v8,v8,s55,s55
v_mfma_f32_16x16x32_bf16 v[72:75],a[252:255],v[24:27],v[72:75]
v_rcp_f16 v9,v8
v_lshlrev_b32_sdwa v52,1,v52 dst_sel:DWORD dst_unused:UNUSED_PAD src0_sel:DWORD src1_sel:WORD_1
v_mfma_f32_16x16x32_bf16 v[120:123],a[240:243],v[24:27],v[120:123]
ds_read_u16_d16_hi v59,v59
v_lshlrev_b32_sdwa v54,1,v54 dst_sel:DWORD dst_unused:UNUSED_PAD src0_sel:DWORD src1_sel:WORD_1
v_mfma_f32_16x16x32_bf16 v[124:127],a[240:243],v[28:31],v[124:127]
ds_read_b64_tr_b16 a[212:213],v2 offset:6160
v_lshlrev_b32_sdwa v40,1,v40 dst_sel:DWORD dst_unused:UNUSED_PAD src0_sel:DWORD src1_sel:WORD_1
v_mfma_f32_16x16x32_bf16 v[92:95],a[224:227],v[28:31],v[92:95]
ds_read_b64_tr_b16 a[214:215],v2 offset:7184
v_lshlrev_b32_sdwa v42,1,v42 dst_sel:DWORD dst_unused:UNUSED_PAD src0_sel:DWORD src1_sel:WORD_1
v_mfma_f32_16x16x32_bf16 v[88:91],a[224:227],v[24:27],v[88:91]
ds_read_u16 v52,v52
v_rcp_f16_sdwa v9,v8 dst_sel:WORD_1 dst_unused:UNUSED_PRESERVE src0_sel:WORD_1
v_mfma_f32_16x16x32_bf16 v[128:131],a[228:231],v[24:27],v[128:131]
ds_read_u16_d16_hi v54,v54
v_fma_mix_f32 v53,v53,v9,0 op_sel_hi:[0,1,0]
v_mfma_f32_16x16x32_bf16 v[132:135],a[228:231],v[28:31],v[132:135]
ds_read_u16 v40,v40
v_lshlrev_b32_sdwa v41,1,v41 dst_sel:DWORD dst_unused:UNUSED_PAD src0_sel:DWORD src1_sel:WORD_1
v_mfma_f32_16x16x32_bf16 v[108:111],a[232:235],v[28:31],v[108:111]
ds_read_u16_d16_hi v42,v42
v_fma_mix_f32 v55,v55,v9,0 op_sel:[0,1,0] op_sel_hi:[0,1,0]
v_mfma_f32_16x16x32_bf16 v[104:107],a[232:235],v[24:27],v[104:107]
s_waitcnt lgkmcnt(7)
ds_read_u16 v41,v41
v_lshlrev_b32_sdwa v43,1,v43 dst_sel:DWORD dst_unused:UNUSED_PAD src0_sel:DWORD src1_sel:WORD_1
v_mfma_f32_16x16x32_bf16 v[28:31],a[216:219],a[0:3],v[16:19]
ds_read_u16_d16_hi v43,v43
v_lshlrev_b32_sdwa v36,1,v36 dst_sel:DWORD dst_unused:UNUSED_PAD src0_sel:DWORD src1_sel:WORD_1
v_mfma_f32_16x16x32_bf16 v[28:31],a[220:223],a[16:19],v[28:31]
ds_read_u16 v36,v36
v_lshl_add_u64 v[32:33],v[32:33],0,v[34:35]
v_mfma_f32_16x16x32_bf16 v[28:31],a[192:195],a[32:35],v[28:31]
v_lshl_add_u64 v[34:35],v[60:61],0,v[62:63]
v_lshlrev_b32_sdwa v38,1,v38 dst_sel:DWORD dst_unused:UNUSED_PAD src0_sel:DWORD src1_sel:WORD_1
v_mfma_f32_16x16x32_bf16 v[24:27],a[196:199],a[32:35],v[20:23]
ds_read_u16_d16_hi v38,v38
v_lshlrev_b32_sdwa v37,1,v37 dst_sel:DWORD dst_unused:UNUSED_PAD src0_sel:DWORD src1_sel:WORD_1
v_mfma_f32_16x16x32_bf16 v[24:27],a[200:203],a[16:19],v[24:27]
s_waitcnt lgkmcnt(7)
ds_read_u16 v37,v37
v_lshl_add_u64 v[60:61],v[56:57],0,v[58:59]
v_mfma_f32_16x16x32_bf16 v[24:27],a[212:215],a[0:3],v[24:27]
v_lshlrev_b32_sdwa v39,1,v39 dst_sel:DWORD dst_unused:UNUSED_PAD src0_sel:DWORD src1_sel:WORD_1
v_lshlrev_b32_sdwa v44,1,v44 dst_sel:DWORD dst_unused:UNUSED_PAD src0_sel:DWORD src1_sel:WORD_1
v_mfma_f32_16x16x32_bf16 v[56:59],a[212:215],a[4:7],v[20:23]
s_waitcnt lgkmcnt(6)
ds_read_u16_d16_hi v39,v39
v_or_b32 v62,v52,v54
v_mfma_f32_16x16x32_bf16 v[56:59],a[200:203],a[20:23],v[56:59]
ds_read_u16 v44,v44
v_cvt_pk_bf16_f32 v63,v53,v55
v_mfma_f32_16x16x32_bf16 v[56:59],a[196:199],a[36:39],v[56:59]
v_lshlrev_b32_sdwa v46,1,v46 dst_sel:DWORD dst_unused:UNUSED_PAD src0_sel:DWORD src1_sel:WORD_1
v_lshlrev_b32_sdwa v45,1,v45 dst_sel:DWORD dst_unused:UNUSED_PAD src0_sel:DWORD src1_sel:WORD_1
v_mfma_f32_16x16x32_bf16 v[52:55],a[192:195],a[36:39],v[16:19]
ds_read_u16_d16_hi v46,v46
v_lshlrev_b32_sdwa v47,1,v47 dst_sel:DWORD dst_unused:UNUSED_PAD src0_sel:DWORD src1_sel:WORD_1
v_mfma_f32_16x16x32_bf16 v[52:55],a[220:223],a[20:23],v[52:55]
ds_read_u16 v45,v45
v_mfma_f32_16x16x32_bf16 v[52:55],a[216:219],a[4:7],v[52:55]
ds_read_u16_d16_hi v47,v47
v_lshlrev_b32_sdwa v48,1,v48 dst_sel:DWORD dst_unused:UNUSED_PAD src0_sel:DWORD src1_sel:WORD_1
v_mfma_f32_16x16x32_bf16 v[232:235],a[248:251],v[32:35],v[232:235]
ds_read_u16 v48,v48
v_lshlrev_b32_sdwa v50,1,v50 dst_sel:DWORD dst_unused:UNUSED_PAD src0_sel:DWORD src1_sel:WORD_1
v_mfma_f32_16x16x32_bf16 v[236:239],a[248:251],v[60:63],v[236:239]
ds_read_u16_d16_hi v50,v50
v_mfma_f32_16x16x32_bf16 v[164:167],a[252:255],v[60:63],v[164:167]
ds_read_b64_tr_b16 a[236:237],v3 offset:12320
v_lshlrev_b32_sdwa v49,1,v49 dst_sel:DWORD dst_unused:UNUSED_PAD src0_sel:DWORD src1_sel:WORD_1
v_mfma_f32_16x16x32_bf16 v[160:163],a[252:255],v[32:35],v[160:163]
ds_read_u16 v49,v49
v_mfma_f32_16x16x32_bf16 v[240:243],a[240:243],v[32:35],v[240:243]
ds_read_b64_tr_b16 a[238:239],v3 offset:13856
v_lshlrev_b32_sdwa v51,1,v51 dst_sel:DWORD dst_unused:UNUSED_PAD src0_sel:DWORD src1_sel:WORD_1
v_mfma_f32_16x16x32_bf16 v[244:247],a[240:243],v[60:63],v[244:247]
ds_read_u16_d16_hi v51,v51
v_lshlrev_b32_sdwa v28,1,v28 dst_sel:DWORD dst_unused:UNUSED_PAD src0_sel:DWORD src1_sel:WORD_1
v_mfma_f32_16x16x32_bf16 v[180:183],a[224:227],v[60:63],v[180:183]
ds_read_u16 v28,v28
v_mfma_f32_16x16x32_bf16 v[176:179],a[224:227],v[32:35],v[176:179]
ds_read_b64_tr_b16 a[244:245],v3 offset:12328
v_lshlrev_b32_sdwa v30,1,v30 dst_sel:DWORD dst_unused:UNUSED_PAD src0_sel:DWORD src1_sel:WORD_1
v_mfma_f32_16x16x32_bf16 v[248:251],a[228:231],v[32:35],v[248:251]
ds_read_u16_d16_hi v30,v30
v_lshlrev_b32_sdwa v29,1,v29 dst_sel:DWORD dst_unused:UNUSED_PAD src0_sel:DWORD src1_sel:WORD_1
v_mfma_f32_16x16x32_bf16 v[252:255],a[228:231],v[60:63],v[252:255]
ds_read_u16 v29,v29
v_mfma_f32_16x16x32_bf16 v[196:199],a[232:235],v[60:63],v[196:199]
ds_read_b64_tr_b16 a[246:247],v3 offset:13864
v_lshlrev_b32_sdwa v31,1,v31 dst_sel:DWORD dst_unused:UNUSED_PAD src0_sel:DWORD src1_sel:WORD_1
v_mfma_f32_16x16x32_bf16 v[192:195],a[232:235],v[32:35],v[192:195]
ds_read_u16_d16_hi v31,v31
v_mfma_f32_16x16x32_bf16 v[60:63],a[216:219],a[8:11],v[16:19]
ds_read_b64_tr_b16 a[208:209],v1 offset:12320
v_lshlrev_b32_sdwa v24,1,v24 dst_sel:DWORD dst_unused:UNUSED_PAD src0_sel:DWORD src1_sel:WORD_1
v_mfma_f32_16x16x32_bf16 v[60:63],a[220:223],a[24:27],v[60:63]
s_waitcnt lgkmcnt(8)
ds_read_u16 v24,v24
v_lshl_add_u64 v[40:41],v[40:41],0,v[42:43]
v_fma_mixlo_f16 v8,v57,s99,0
v_mfma_f32_16x16x32_bf16 v[60:63],a[192:195],a[40:43],v[60:63]
ds_read_b64_tr_b16 a[210:211],v1 offset:13344
v_lshl_add_u64 v[42:43],v[36:37],0,v[38:39]
v_lshlrev_b32_sdwa v26,1,v26 dst_sel:DWORD dst_unused:UNUSED_PAD src0_sel:DWORD src1_sel:WORD_1
v_mfma_f32_16x16x32_bf16 v[32:35],a[196:199],a[40:43],v[20:23]
ds_read_u16_d16_hi v26,v26
v_fma_mixhi_f16 v8,v59,s99,0
v_mfma_f32_16x16x32_bf16 v[32:35],a[200:203],a[24:27],v[32:35]
ds_read_b64_tr_b16 a[204:205],v1 offset:14368
v_lshl_add_u64 v[36:37],v[44:45],0,v[46:47]
v_lshlrev_b32_sdwa v25,1,v25 dst_sel:DWORD dst_unused:UNUSED_PAD src0_sel:DWORD src1_sel:WORD_1
v_pk_fma_f16 v9,v8,v8,s54
v_mfma_f32_16x16x32_bf16 v[32:35],a[212:215],a[8:11],v[32:35]
ds_read_u16 v25,v25
v_pk_mul_f16 v9,v8,v9
v_mfma_f32_16x16x32_bf16 v[44:47],a[212:215],a[12:15],v[20:23]
ds_read_b64_tr_b16 a[206:207],v1 offset:15392
v_lshlrev_b32_sdwa v27,1,v27 dst_sel:DWORD dst_unused:UNUSED_PAD src0_sel:DWORD src1_sel:WORD_1
v_mfma_f32_16x16x32_bf16 v[44:47],a[200:203],a[28:31],v[44:47]
s_waitcnt lgkmcnt(8)
ds_read_u16_d16_hi v27,v27
v_lshl_add_u64 v[38:39],v[48:49],0,v[50:51]
v_mfma_f32_16x16x32_bf16 v[44:47],a[196:199],a[44:47],v[44:47]
v_exp_f16 v8,v9
v_lshlrev_b32_sdwa v52,1,v52 dst_sel:DWORD dst_unused:UNUSED_PAD src0_sel:DWORD src1_sel:WORD_1
v_mfma_f32_16x16x32_bf16 v[48:51],a[192:195],a[44:47],v[16:19]
ds_read_u16 v52,v52
v_lshlrev_b32_sdwa v54,1,v54 dst_sel:DWORD dst_unused:UNUSED_PAD src0_sel:DWORD src1_sel:WORD_1
v_mfma_f32_16x16x32_bf16 v[48:51],a[220:223],a[28:31],v[48:51]
ds_read_u16_d16_hi v54,v54
v_lshlrev_b32_sdwa v53,1,v53 dst_sel:DWORD dst_unused:UNUSED_PAD src0_sel:DWORD src1_sel:WORD_1
v_mfma_f32_16x16x32_bf16 v[48:51],a[216:219],a[12:15],v[48:51]
v_exp_f16_sdwa v8,v9 dst_sel:WORD_1 dst_unused:UNUSED_PRESERVE src0_sel:WORD_1
v_mfma_f32_16x16x32_bf16 v[208:211],a[248:251],v[40:43],v[208:211]
ds_read_u16 v53,v53
v_lshlrev_b32_sdwa v55,1,v55 dst_sel:DWORD dst_unused:UNUSED_PAD src0_sel:DWORD src1_sel:WORD_1
v_mfma_f32_16x16x32_bf16 v[212:215],a[248:251],v[36:39],v[212:215]
ds_read_u16_d16_hi v55,v55
v_pk_fma_f16 v8,v8,s55,s55
v_mfma_f32_16x16x32_bf16 v[172:175],a[252:255],v[36:39],v[172:175]
ds_read_b64_tr_b16 a[248:249],v3 offset:12384
v_lshlrev_b32_sdwa v56,1,v56 dst_sel:DWORD dst_unused:UNUSED_PAD src0_sel:DWORD src1_sel:WORD_1
v_mfma_f32_16x16x32_bf16 v[168:171],a[252:255],v[40:43],v[168:171]
TP4336_%=:
s_cbranch_execz TP6467_%=
ds_read_u16 v56,v56
v_rcp_f16 v9,v8
v_mfma_f32_16x16x32_bf16 v[216:219],a[240:243],v[40:43],v[216:219]
ds_read_b64_tr_b16 a[250:251],v3 offset:13920
v_lshlrev_b32_sdwa v58,1,v58 dst_sel:DWORD dst_unused:UNUSED_PAD src0_sel:DWORD src1_sel:WORD_1
v_mfma_f32_16x16x32_bf16 v[220:223],a[240:243],v[36:39],v[220:223]
ds_read_u16_d16_hi v58,v58
ds_read_b64_tr_b16 a[252:253],v3 offset:12392
v_mfma_f32_16x16x32_bf16 v[188:191],a[224:227],v[36:39],v[188:191]
ds_read_b64_tr_b16 a[254:255],v3 offset:13928
v_lshlrev_b32_sdwa v60,1,v60 dst_sel:DWORD dst_unused:UNUSED_PAD src0_sel:DWORD src1_sel:WORD_1
v_mfma_f32_16x16x32_bf16 v[184:187],a[224:227],v[40:43],v[184:187]
ds_read_u16 v60,v60
v_rcp_f16_sdwa v9,v8 dst_sel:WORD_1 dst_unused:UNUSED_PRESERVE src0_sel:WORD_1
v_mfma_f32_16x16x32_bf16 v[224:227],a[228:231],v[40:43],v[224:227]
ds_read_b64_tr_b16 a[240:241],v3 offset:12448
v_lshlrev_b32_sdwa v62,1,v62 dst_sel:DWORD dst_unused:UNUSED_PAD src0_sel:DWORD src1_sel:WORD_1
v_mfma_f32_16x16x32_bf16 v[228:231],a[228:231],v[36:39],v[228:231]
ds_read_u16_d16_hi v62,v62
v_fma_mix_f32 v57,v57,v9,0 op_sel_hi:[0,1,0]
v_mfma_f32_16x16x32_bf16 v[204:207],a[232:235],v[36:39],v[204:207]
ds_read_b64_tr_b16 a[242:243],v3 offset:13984
v_lshlrev_b32_sdwa v61,1,v61 dst_sel:DWORD dst_unused:UNUSED_PAD src0_sel:DWORD src1_sel:WORD_1
v_mfma_f32_16x16x32_bf16 v[200:203],a[232:235],v[40:43],v[200:203]
ds_read_u16 v61,v61
v_fma_mix_f32 v59,v59,v9,0 op_sel:[0,1,0] op_sel_hi:[0,1,0]
v_lshlrev_b32_sdwa v63,1,v63 dst_sel:DWORD dst_unused:UNUSED_PAD src0_sel:DWORD src1_sel:WORD_1
v_mbcnt_lo_u32_b32 v8,-1,0
N_qs10_%=:
s_add_u32 s64,s16,N_qs10_%=-N_pf_%=+0
v_and_b32_e64 v8,3,v8
s_mov_b32 m0,12288
v_lshl_add_u32 v8,v8,7,s64
v_min_u32 v8,N_mainend_%=-N_pf_%=+2048,v8
global_load_lds_dword v8,s[14:15]
v_mfma_f32_16x16x32_bf16 v[36:39],a[216:219],a[48:51],v[16:19]
ds_read_b64 v[14:15],v7 offset:12320
v_lshlrev_b32_sdwa v32,1,v32 dst_sel:DWORD dst_unused:UNUSED_PAD src0_sel:DWORD src1_sel:WORD_1
v_mfma_f32_16x16x32_bf16 v[36:39],a[220:223],a[64:67],v[36:39]
s_waitcnt lgkmcnt(9)
ds_read_u16_d16_hi v63,v63
v_lshl_add_u64 v[28:29],v[28:29],0,v[30:31]
v_mfma_f32_16x16x32_bf16 v[36:39],a[192:195],a[80:83],v[36:39]
ds_read_b64_tr_b16 a[224:225],v3 offset:12456
v_lshl_add_u64 v[30:31],v[24:25],0,v[26:27]
v_lshlrev_b32_sdwa v34,1,v34 dst_sel:DWORD dst_unused:UNUSED_PAD src0_sel:DWORD src1_sel:WORD_1
v_mfma_f32_16x16x32_bf16 v[40:43],a[196:199],a[80:83],v[20:23]
ds_read_b64_tr_b16 a[226:227],v3 offset:13992
v_lshlrev_b32_sdwa v33,1,v33 dst_sel:DWORD dst_unused:UNUSED_PAD src0_sel:DWORD src1_sel:WORD_1
v_mfma_f32_16x16x32_bf16 v[40:43],a[200:203],a[64:67],v[40:43]
ds_read_u16 v32,v32
v_lshl_add_u64 v[24:25],v[52:53],0,v[54:55]
v_mfma_f32_16x16x32_bf16 v[40:43],a[212:215],a[48:51],v[40:43]
s_waitcnt lgkmcnt(4)
ds_read_u16_d16_hi v34,v34
v_or3_b32 v13,v14,v15,v13
v_lshlrev_b32_sdwa v35,1,v35 dst_sel:DWORD dst_unused:UNUSED_PAD src0_sel:DWORD src1_sel:WORD_1
v_mfma_f32_16x16x32_bf16 v[52:55],a[212:215],a[52:55],v[20:23]
ds_read_b64 v[14:15],v7 offset:12832
v_or_b32 v26,v56,v58
v_mfma_f32_16x16x32_bf16 v[52:55],a[200:203],a[68:71],v[52:55]
ds_read_u16 v33,v33
v_cvt_pk_bf16_f32 v27,v57,v59
v_lshlrev_b32_sdwa v48,1,v48 dst_sel:DWORD dst_unused:UNUSED_PAD src0_sel:DWORD src1_sel:WORD_1
v_mfma_f32_16x16x32_bf16 v[52:55],a[196:199],a[84:87],v[52:55]
ds_read_u16_d16_hi v35,v35
v_lshlrev_b32_sdwa v50,1,v50 dst_sel:DWORD dst_unused:UNUSED_PAD src0_sel:DWORD src1_sel:WORD_1
v_mfma_f32_16x16x32_bf16 v[56:59],a[192:195],a[84:87],v[16:19]
ds_read_u16 v48,v48
v_lshlrev_b32_sdwa v49,1,v49 dst_sel:DWORD dst_unused:UNUSED_PAD src0_sel:DWORD src1_sel:WORD_1
v_mfma_f32_16x16x32_bf16 v[56:59],a[220:223],a[68:71],v[56:59]
ds_read_u16_d16_hi v50,v50
v_lshlrev_b32_sdwa v51,1,v51 dst_sel:DWORD dst_unused:UNUSED_PAD src0_sel:DWORD src1_sel:WORD_1
v_mfma_f32_16x16x32_bf16 v[56:59],a[216:219],a[52:55],v[56:59]
s_waitcnt lgkmcnt(4)
ds_read_u16 v49,v49
v_or3_b32 v13,v14,v15,v13
v_mfma_f32_16x16x32_bf16 v[136:139],a[236:239],v[28:31],v[136:139]
ds_read_b64 v[14:15],v7 offset:13344
v_lshlrev_b32_sdwa v44,1,v44 dst_sel:DWORD dst_unused:UNUSED_PAD src0_sel:DWORD src1_sel:WORD_1
v_mfma_f32_16x16x32_bf16 v[140:143],a[236:239],v[24:27],v[140:143]
ds_read_u16_d16_hi v51,v51
v_lshlrev_b32_sdwa v46,1,v46 dst_sel:DWORD dst_unused:UNUSED_PAD src0_sel:DWORD src1_sel:WORD_1
v_mfma_f32_16x16x32_bf16 v[68:71],a[244:247],v[24:27],v[68:71]
ds_read_u16 v44,v44
v_lshlrev_b32_sdwa v45,1,v45 dst_sel:DWORD dst_unused:UNUSED_PAD src0_sel:DWORD src1_sel:WORD_1
v_mfma_f32_16x16x32_bf16 v[64:67],a[244:247],v[28:31],v[64:67]
ds_read_u16_d16_hi v46,v46
v_mfma_f32_16x16x32_bf16 v[144:147],a[248:251],v[28:31],v[144:147]
ds_read_u16 v45,v45
v_lshlrev_b32_sdwa v47,1,v47 dst_sel:DWORD dst_unused:UNUSED_PAD src0_sel:DWORD src1_sel:WORD_1
v_mfma_f32_16x16x32_bf16 v[148:151],a[248:251],v[24:27],v[148:151]
s_waitcnt lgkmcnt(4)
ds_read_u16_d16_hi v47,v47
v_or3_b32 v13,v14,v15,v13
v_mfma_f32_16x16x32_bf16 v[84:87],a[252:255],v[24:27],v[84:87]
v_and_b32 v13,0x40004000,v13
v_lshlrev_b32_sdwa v36,1,v36 dst_sel:DWORD dst_unused:UNUSED_PAD src0_sel:DWORD src1_sel:WORD_1
v_mfma_f32_16x16x32_bf16 v[80:83],a[252:255],v[28:31],v[80:83]
ds_read_u16 v36,v36
v_cmp_ne_u32_e32 vcc,0,v13
s_nop 0
s_or_b64 s[90:91],s[90:91],vcc
v_mfma_f32_16x16x32_bf16 v[152:155],a[240:243],v[28:31],v[152:155]
v_mbcnt_lo_u32_b32 v15,-1,0
v_lshlrev_b32_sdwa v38,1,v38 dst_sel:DWORD dst_unused:UNUSED_PAD src0_sel:DWORD src1_sel:WORD_1
v_mfma_f32_16x16x32_bf16 v[156:159],a[240:243],v[24:27],v[156:159]
ds_read_u16_d16_hi v38,v38
v_mbcnt_hi_u32_b32 v15,-1,v15
v_mfma_f32_16x16x32_bf16 v[100:103],a[224:227],v[24:27],v[100:103]
v_lshlrev_b32 v15,2,v15
v_lshlrev_b32_sdwa v37,1,v37 dst_sel:DWORD dst_unused:UNUSED_PAD src0_sel:DWORD src1_sel:WORD_1
v_mfma_f32_16x16x32_bf16 v[96:99],a[224:227],v[28:31],v[96:99]
ds_read_b32 v7,v15 offset:14848
ds_read_b32 v13,v15 offset:15104
v_lshlrev_b32_sdwa v39,1,v39 dst_sel:DWORD dst_unused:UNUSED_PAD src0_sel:DWORD src1_sel:WORD_1
v_mfma_f32_16x16x32_bf16 v[24:27],a[216:219],a[56:59],v[16:19]
ds_read_b32 v14,v15 offset:14336
ds_read_b32 v15,v15 offset:14592
v_lshlrev_b32_sdwa v40,1,v40 dst_sel:DWORD dst_unused:UNUSED_PAD src0_sel:DWORD src1_sel:WORD_1
v_mfma_f32_16x16x32_bf16 v[24:27],a[220:223],a[72:75],v[24:27]
ds_read_u16 v37,v37
v_lshl_add_u64 v[60:61],v[60:61],0,v[62:63]
v_fma_mixlo_f16 v8,v53,s99,0
v_mfma_f32_16x16x32_bf16 v[24:27],a[192:195],a[88:91],v[24:27]
ds_read_u16_d16_hi v39,v39
v_lshl_add_u64 v[62:63],v[32:33],0,v[34:35]
v_lshlrev_b32_sdwa v42,1,v42 dst_sel:DWORD dst_unused:UNUSED_PAD src0_sel:DWORD src1_sel:WORD_1
v_mfma_f32_16x16x32_bf16 v[28:31],a[196:199],a[88:91],v[20:23]
ds_read_u16 v40,v40
v_fma_mixhi_f16 v8,v55,s99,0
v_mfma_f32_16x16x32_bf16 v[28:31],a[200:203],a[72:75],v[28:31]
s_waitcnt lgkmcnt(8)
ds_read_u16_d16_hi v42,v42
v_lshl_add_u64 v[32:33],v[48:49],0,v[50:51]
v_lshlrev_b32_sdwa v41,1,v41 dst_sel:DWORD dst_unused:UNUSED_PAD src0_sel:DWORD src1_sel:WORD_1
v_pk_fma_f16 v9,v8,v8,s54
v_mfma_f32_16x16x32_bf16 v[28:31],a[212:215],a[56:59],v[28:31]
ds_read_u16 v41,v41
v_pk_mul_f16 v9,v8,v9
v_mfma_f32_16x16x32_bf16 v[48:51],a[212:215],a[60:63],v[20:23]
ds_read_b128 v[20:23],v4 offset:1408
v_lshlrev_b32_sdwa v43,1,v43 dst_sel:DWORD dst_unused:UNUSED_PAD src0_sel:DWORD src1_sel:WORD_1
v_mfma_f32_16x16x32_bf16 v[48:51],a[200:203],a[76:79],v[48:51]
ds_read_b64_tr_b16 a[212:213],v1 offset:16416
ds_read_b64_tr_b16 a[214:215],v1 offset:17440
v_lshl_add_u64 v[34:35],v[44:45],0,v[46:47]
v_mfma_f32_16x16x32_bf16 v[48:51],a[196:199],a[92:95],v[48:51]
ds_read_u16_d16_hi v43,v43
v_exp_f16 v8,v9
v_lshlrev_b32_sdwa v56,1,v56 dst_sel:DWORD dst_unused:UNUSED_PAD src0_sel:DWORD src1_sel:WORD_1
v_mfma_f32_16x16x32_bf16 v[44:47],a[192:195],a[92:95],v[16:19]
ds_read_b128 v[16:19],v4 offset:1440
ds_read_b64_tr_b16 a[200:201],v2 offset:16416
v_mfma_f32_16x16x32_bf16 v[44:47],a[220:223],a[76:79],v[44:47]
ds_read_b64_tr_b16 a[202:203],v2 offset:17440
ds_read_b64_tr_b16 a[196:197],v2 offset:14368
v_lshlrev_b32_sdwa v58,1,v58 dst_sel:DWORD dst_unused:UNUSED_PAD src0_sel:DWORD src1_sel:WORD_1
v_mfma_f32_16x16x32_bf16 v[44:47],a[216:219],a[60:63],v[44:47]
ds_read_b64_tr_b16 a[198:199],v2 offset:15392
v_exp_f16_sdwa v8,v9 dst_sel:WORD_1 dst_unused:UNUSED_PRESERVE src0_sel:WORD_1
v_mfma_f32_16x16x32_bf16 v[112:115],a[236:239],v[60:63],v[112:115]
ds_read_u16 v56,v56
v_lshlrev_b32_sdwa v57,1,v57 dst_sel:DWORD dst_unused:UNUSED_PAD src0_sel:DWORD src1_sel:WORD_1
v_mfma_f32_16x16x32_bf16 v[116:119],a[236:239],v[32:35],v[116:119]
ds_read_u16_d16_hi v58,v58
v_lshlrev_b32_sdwa v59,1,v59 dst_sel:DWORD dst_unused:UNUSED_PAD src0_sel:DWORD src1_sel:WORD_1
v_mfma_f32_16x16x32_bf16 v[76:79],a[244:247],v[32:35],v[76:79]
ds_read_u16 v57,v57
v_pk_fma_f16 v8,v8,s55,s55
v_mfma_f32_16x16x32_bf16 v[72:75],a[244:247],v[60:63],v[72:75]
v_rcp_f16 v9,v8
v_lshlrev_b32_sdwa v52,1,v52 dst_sel:DWORD dst_unused:UNUSED_PAD src0_sel:DWORD src1_sel:WORD_1
v_mfma_f32_16x16x32_bf16 v[120:123],a[248:251],v[60:63],v[120:123]
ds_read_u16_d16_hi v59,v59
v_lshlrev_b32_sdwa v54,1,v54 dst_sel:DWORD dst_unused:UNUSED_PAD src0_sel:DWORD src1_sel:WORD_1
v_mfma_f32_16x16x32_bf16 v[124:127],a[248:251],v[32:35],v[124:127]
ds_read_b64_tr_b16 a[192:193],v2 offset:12320
v_lshlrev_b32_sdwa v24,1,v24 dst_sel:DWORD dst_unused:UNUSED_PAD src0_sel:DWORD src1_sel:WORD_1
v_mfma_f32_16x16x32_bf16 v[92:95],a[252:255],v[32:35],v[92:95]
ds_read_b64_tr_b16 a[194:195],v2 offset:13344
v_lshlrev_b32_sdwa v26,1,v26 dst_sel:DWORD dst_unused:UNUSED_PAD src0_sel:DWORD src1_sel:WORD_1
v_mfma_f32_16x16x32_bf16 v[88:91],a[252:255],v[60:63],v[88:91]
ds_read_u16 v52,v52
v_rcp_f16_sdwa v9,v8 dst_sel:WORD_1 dst_unused:UNUSED_PRESERVE src0_sel:WORD_1
v_mfma_f32_16x16x32_bf16 v[128:131],a[240:243],v[60:63],v[128:131]
ds_read_u16_d16_hi v54,v54
v_fma_mix_f32 v53,v53,v9,0 op_sel_hi:[0,1,0]
v_mfma_f32_16x16x32_bf16 v[132:135],a[240:243],v[32:35],v[132:135]
ds_read_u16 v24,v24
v_lshlrev_b32_sdwa v25,1,v25 dst_sel:DWORD dst_unused:UNUSED_PAD src0_sel:DWORD src1_sel:WORD_1
v_mfma_f32_16x16x32_bf16 v[108:111],a[224:227],v[32:35],v[108:111]
ds_read_u16_d16_hi v26,v26
v_fma_mix_f32 v55,v55,v9,0 op_sel:[0,1,0] op_sel_hi:[0,1,0]
v_mfma_f32_16x16x32_bf16 v[104:107],a[224:227],v[60:63],v[104:107]
s_waitcnt lgkmcnt(7)
ds_read_u16 v25,v25
v_lshlrev_b32_sdwa v27,1,v27 dst_sel:DWORD dst_unused:UNUSED_PAD src0_sel:DWORD src1_sel:WORD_1
v_mfma_f32_16x16x32_bf16 v[32:35],a[208:211],a[0:3],v[20:23]
ds_read_u16_d16_hi v27,v27
v_lshlrev_b32_sdwa v28,1,v28 dst_sel:DWORD dst_unused:UNUSED_PAD src0_sel:DWORD src1_sel:WORD_1
v_mfma_f32_16x16x32_bf16 v[32:35],a[204:207],a[16:19],v[32:35]
ds_read_u16 v28,v28
v_lshl_add_u64 v[36:37],v[36:37],0,v[38:39]
v_mfma_f32_16x16x32_bf16 v[32:35],a[212:215],a[32:35],v[32:35]
v_lshl_add_u64 v[38:39],v[40:41],0,v[42:43]
v_lshlrev_b32_sdwa v30,1,v30 dst_sel:DWORD dst_unused:UNUSED_PAD src0_sel:DWORD src1_sel:WORD_1
v_mfma_f32_16x16x32_bf16 v[60:63],a[200:203],a[32:35],v[16:19]
ds_read_u16_d16_hi v30,v30
v_lshlrev_b32_sdwa v29,1,v29 dst_sel:DWORD dst_unused:UNUSED_PAD src0_sel:DWORD src1_sel:WORD_1
v_mfma_f32_16x16x32_bf16 v[60:63],a[196:199],a[16:19],v[60:63]
s_waitcnt lgkmcnt(7)
ds_read_u16 v29,v29
v_lshl_add_u64 v[40:41],v[56:57],0,v[58:59]
v_mfma_f32_16x16x32_bf16 v[60:63],a[192:195],a[0:3],v[60:63]
v_lshlrev_b32_sdwa v31,1,v31 dst_sel:DWORD dst_unused:UNUSED_PAD src0_sel:DWORD src1_sel:WORD_1
v_lshlrev_b32_sdwa v44,1,v44 dst_sel:DWORD dst_unused:UNUSED_PAD src0_sel:DWORD src1_sel:WORD_1
v_mfma_f32_16x16x32_bf16 v[56:59],a[192:195],a[4:7],v[16:19]
s_waitcnt lgkmcnt(6)
ds_read_u16_d16_hi v31,v31
v_or_b32 v42,v52,v54
v_mfma_f32_16x16x32_bf16 v[56:59],a[196:199],a[20:23],v[56:59]
s_waitcnt vmcnt(4)
ds_read_u16 v44,v44
v_cvt_pk_bf16_f32 v43,v53,v55
v_mfma_f32_16x16x32_bf16 v[56:59],a[200:203],a[36:39],v[56:59]
s_barrier
v_lshlrev_b32_sdwa v46,1,v46 dst_sel:DWORD dst_unused:UNUSED_PAD src0_sel:DWORD src1_sel:WORD_1
v_lshlrev_b32_sdwa v45,1,v45 dst_sel:DWORD dst_unused:UNUSED_PAD src0_sel:DWORD src1_sel:WORD_1
v_mfma_f32_16x16x32_bf16 v[52:55],a[212:215],a[36:39],v[20:23]
ds_read_u16_d16_hi v46,v46
v_lshlrev_b32_sdwa v47,1,v47 dst_sel:DWORD dst_unused:UNUSED_PAD src0_sel:DWORD src1_sel:WORD_1
v_mfma_f32_16x16x32_bf16 v[52:55],a[204:207],a[20:23],v[52:55]
s_add_u32 m0,s58,6160
ds_read_u16 v45,v45
v_mfma_f32_16x16x32_bf16 v[52:55],a[208:211],a[4:7],v[52:55]
s_add_u32 s69,s69,s59
ds_read_u16_d16_hi v47,v47
v_lshlrev_b32_sdwa v48,1,v48 dst_sel:DWORD dst_unused:UNUSED_PAD src0_sel:DWORD src1_sel:WORD_1
v_mfma_f32_16x16x32_bf16 v[232:235],a[236:239],v[36:39],v[232:235]
buffer_load_dwordx4 v5,s[44:47],s69 offen offset:0 lds
v_lshlrev_b32_sdwa v50,1,v50 dst_sel:DWORD dst_unused:UNUSED_PAD src0_sel:DWORD src1_sel:WORD_1
v_mfma_f32_16x16x32_bf16 v[236:239],a[236:239],v[40:43],v[236:239]
ds_read_u16 v48,v48
v_mfma_f32_16x16x32_bf16 v[164:167],a[244:247],v[40:43],v[164:167]
ds_read_u16_d16_hi v50,v50
ds_read_b64_tr_b16 a[228:229],v3 offset:18480
v_mfma_f32_16x16x32_bf16 v[160:163],a[244:247],v[36:39],v[160:163]
ds_read_b64_tr_b16 a[230:231],v3 offset:20016
v_lshlrev_b32_sdwa v49,1,v49 dst_sel:DWORD dst_unused:UNUSED_PAD src0_sel:DWORD src1_sel:WORD_1
v_mfma_f32_16x16x32_bf16 v[240:243],a[248:251],v[36:39],v[240:243]
s_add_u32 s69,s69,s53
ds_read_u16 v49,v49
v_lshlrev_b32_sdwa v51,1,v51 dst_sel:DWORD dst_unused:UNUSED_PAD src0_sel:DWORD src1_sel:WORD_1
v_mfma_f32_16x16x32_bf16 v[244:247],a[248:251],v[40:43],v[244:247]
buffer_load_dwordx4 v5,s[44:47],s69 offen offset:1024 lds
v_lshlrev_b32_sdwa v32,1,v32 dst_sel:DWORD dst_unused:UNUSED_PAD src0_sel:DWORD src1_sel:WORD_1
v_mfma_f32_16x16x32_bf16 v[180:183],a[252:255],v[40:43],v[180:183]
ds_read_u16_d16_hi v51,v51
v_mfma_f32_16x16x32_bf16 v[176:179],a[252:255],v[36:39],v[176:179]
ds_read_u16 v32,v32
v_lshlrev_b32_sdwa v34,1,v34 dst_sel:DWORD dst_unused:UNUSED_PAD src0_sel:DWORD src1_sel:WORD_1
v_mfma_f32_16x16x32_bf16 v[248:251],a[240:243],v[36:39],v[248:251]
ds_read_u16_d16_hi v34,v34
v_lshlrev_b32_sdwa v33,1,v33 dst_sel:DWORD dst_unused:UNUSED_PAD src0_sel:DWORD src1_sel:WORD_1
v_mfma_f32_16x16x32_bf16 v[252:255],a[240:243],v[40:43],v[252:255]
ds_read_u16 v33,v33
v_mfma_f32_16x16x32_bf16 v[196:199],a[224:227],v[40:43],v[196:199]
s_add_u32 s69,s69,s53
ds_read_b64_tr_b16 a[232:233],v3 offset:18488
v_lshlrev_b32_sdwa v35,1,v35 dst_sel:DWORD dst_unused:UNUSED_PAD src0_sel:DWORD src1_sel:WORD_1
v_mfma_f32_16x16x32_bf16 v[192:195],a[224:227],v[36:39],v[192:195]
buffer_load_dwordx4 v5,s[44:47],s69 offen offset:2048 lds
v_mfma_f32_16x16x32_bf16 v[40:43],a[208:211],a[8:11],v[20:23]
ds_read_u16_d16_hi v35,v35
v_lshlrev_b32_sdwa v60,1,v60 dst_sel:DWORD dst_unused:UNUSED_PAD src0_sel:DWORD src1_sel:WORD_1
v_mfma_f32_16x16x32_bf16 v[40:43],a[204:207],a[24:27],v[40:43]
s_waitcnt lgkmcnt(6)
ds_read_u16 v60,v60
v_lshl_add_u64 v[24:25],v[24:25],0,v[26:27]
v_fma_mixlo_f16 v8,v57,s99,0
v_mfma_f32_16x16x32_bf16 v[40:43],a[212:215],a[40:43],v[40:43]
ds_read_b64_tr_b16 a[234:235],v3 offset:20024
v_lshl_add_u64 v[26:27],v[28:29],0,v[30:31]
v_lshlrev_b32_sdwa v62,1,v62 dst_sel:DWORD dst_unused:UNUSED_PAD src0_sel:DWORD src1_sel:WORD_1
v_mfma_f32_16x16x32_bf16 v[36:39],a[200:203],a[40:43],v[16:19]
ds_read_u16_d16_hi v62,v62
v_fma_mixhi_f16 v8,v59,s99,0
v_mfma_f32_16x16x32_bf16 v[36:39],a[196:199],a[24:27],v[36:39]
ds_read_b64_tr_b16 a[220:221],v1 offset:18480
v_lshl_add_u64 v[28:29],v[44:45],0,v[46:47]
v_lshlrev_b32_sdwa v61,1,v61 dst_sel:DWORD dst_unused:UNUSED_PAD src0_sel:DWORD src1_sel:WORD_1
v_pk_fma_f16 v9,v8,v8,s54
v_mfma_f32_16x16x32_bf16 v[36:39],a[192:195],a[8:11],v[36:39]
ds_read_u16 v61,v61
v_pk_mul_f16 v9,v8,v9
v_mfma_f32_16x16x32_bf16 v[44:47],a[192:195],a[12:15],v[16:19]
ds_read_b64_tr_b16 a[222:223],v1 offset:19504
v_lshlrev_b32_sdwa v63,1,v63 dst_sel:DWORD dst_unused:UNUSED_PAD src0_sel:DWORD src1_sel:WORD_1
v_mfma_f32_16x16x32_bf16 v[44:47],a[196:199],a[28:31],v[44:47]
s_waitcnt lgkmcnt(7)
ds_read_u16_d16_hi v63,v63
v_lshl_add_u64 v[30:31],v[48:49],0,v[50:51]
v_mfma_f32_16x16x32_bf16 v[44:47],a[200:203],a[44:47],v[44:47]
v_exp_f16 v8,v9
v_lshlrev_b32_sdwa v52,1,v52 dst_sel:DWORD dst_unused:UNUSED_PAD src0_sel:DWORD src1_sel:WORD_1
v_mfma_f32_16x16x32_bf16 v[48:51],a[212:215],a[44:47],v[20:23]
ds_read_u16 v52,v52
v_lshlrev_b32_sdwa v54,1,v54 dst_sel:DWORD dst_unused:UNUSED_PAD src0_sel:DWORD src1_sel:WORD_1
v_mfma_f32_16x16x32_bf16 v[48:51],a[204:207],a[28:31],v[48:51]
ds_read_u16_d16_hi v54,v54
v_lshlrev_b32_sdwa v53,1,v53 dst_sel:DWORD dst_unused:UNUSED_PAD src0_sel:DWORD src1_sel:WORD_1
v_mfma_f32_16x16x32_bf16 v[48:51],a[208:211],a[12:15],v[48:51]
v_exp_f16_sdwa v8,v9 dst_sel:WORD_1 dst_unused:UNUSED_PRESERVE src0_sel:WORD_1
v_mfma_f32_16x16x32_bf16 v[208:211],a[236:239],v[24:27],v[208:211]
ds_read_u16 v53,v53
v_lshlrev_b32_sdwa v55,1,v55 dst_sel:DWORD dst_unused:UNUSED_PAD src0_sel:DWORD src1_sel:WORD_1
v_mfma_f32_16x16x32_bf16 v[212:215],a[236:239],v[28:31],v[212:215]
ds_read_u16_d16_hi v55,v55
v_pk_fma_f16 v8,v8,s55,s55
v_mfma_f32_16x16x32_bf16 v[172:175],a[244:247],v[28:31],v[172:175]
ds_read_b64_tr_b16 a[236:237],v3 offset:18544
v_lshlrev_b32_sdwa v56,1,v56 dst_sel:DWORD dst_unused:UNUSED_PAD src0_sel:DWORD src1_sel:WORD_1
v_mfma_f32_16x16x32_bf16 v[168:171],a[244:247],v[24:27],v[168:171]
ds_read_u16 v56,v56
v_rcp_f16_e64 v9,v8
v_mfma_f32_16x16x32_bf16 v[216:219],a[248:251],v[24:27],v[216:219]
ds_read_b64_tr_b16 a[238:239],v3 offset:20080
v_lshlrev_b32_sdwa v58,1,v58 dst_sel:DWORD dst_unused:UNUSED_PAD src0_sel:DWORD src1_sel:WORD_1
v_mfma_f32_16x16x32_bf16 v[220:223],a[248:251],v[28:31],v[220:223]
ds_read_u16_d16_hi v58,v58
ds_read_b64_tr_b16 a[244:245],v3 offset:18552
v_mfma_f32_16x16x32_bf16 v[188:191],a[252:255],v[28:31],v[188:191]
ds_read_b64_tr_b16 a[246:247],v3 offset:20088
v_lshlrev_b32_sdwa v40,1,v40 dst_sel:DWORD dst_unused:UNUSED_PAD src0_sel:DWORD src1_sel:WORD_1
v_mfma_f32_16x16x32_bf16 v[184:187],a[252:255],v[24:27],v[184:187]
ds_read_u16 v40,v40
v_rcp_f16_sdwa v9,v8 dst_sel:WORD_1 dst_unused:UNUSED_PRESERVE src0_sel:WORD_1
v_mfma_f32_16x16x32_bf16 v[224:227],a[240:243],v[24:27],v[224:227]
ds_read_b64_tr_b16 a[248:249],v3 offset:18608
v_lshlrev_b32_sdwa v42,1,v42 dst_sel:DWORD dst_unused:UNUSED_PAD src0_sel:DWORD src1_sel:WORD_1
v_mfma_f32_16x16x32_bf16 v[228:231],a[240:243],v[28:31],v[228:231]
ds_read_u16_d16_hi v42,v42
v_fma_mix_f32 v57,v57,v9,0 op_sel_hi:[0,1,0]
v_mfma_f32_16x16x32_bf16 v[204:207],a[224:227],v[28:31],v[204:207]
ds_read_b64_tr_b16 a[250:251],v3 offset:20144
v_lshlrev_b32_sdwa v41,1,v41 dst_sel:DWORD dst_unused:UNUSED_PAD src0_sel:DWORD src1_sel:WORD_1
v_mfma_f32_16x16x32_bf16 v[200:203],a[224:227],v[24:27],v[200:203]
ds_read_u16 v41,v41
v_fma_mix_f32 v59,v59,v9,0 op_sel:[0,1,0] op_sel_hi:[0,1,0]
v_lshlrev_b32_sdwa v43,1,v43 dst_sel:DWORD dst_unused:UNUSED_PAD src0_sel:DWORD src1_sel:WORD_1
v_mfma_f32_16x16x32_bf16 v[28:31],a[208:211],a[48:51],v[20:23]
s_add_u32 m0,s58,12320
ds_read_u16_d16_hi v43,v43
ds_read_b64_tr_b16 a[252:253],v3 offset:18616
v_lshlrev_b32_sdwa v36,1,v36 dst_sel:DWORD dst_unused:UNUSED_PAD src0_sel:DWORD src1_sel:WORD_1
v_mfma_f32_16x16x32_bf16 v[28:31],a[204:207],a[64:67],v[28:31]
s_waitcnt lgkmcnt(10)
ds_read_b64_tr_b16 a[254:255],v3 offset:20152
v_lshl_add_u64 v[32:33],v[32:33],0,v[34:35]
v_mfma_f32_16x16x32_bf16 v[28:31],a[212:215],a[80:83],v[28:31]
s_add_u32 s69,s69,s59
ds_read_u16 v36,v36
v_lshl_add_u64 v[34:35],v[60:61],0,v[62:63]
v_lshlrev_b32_sdwa v38,1,v38 dst_sel:DWORD dst_unused:UNUSED_PAD src0_sel:DWORD src1_sel:WORD_1
v_mfma_f32_16x16x32_bf16 v[24:27],a[200:203],a[80:83],v[16:19]
buffer_load_dwordx4 v5,s[44:47],s69 offen offset:0 lds
v_lshlrev_b32_sdwa v37,1,v37 dst_sel:DWORD dst_unused:UNUSED_PAD src0_sel:DWORD src1_sel:WORD_1
v_mfma_f32_16x16x32_bf16 v[24:27],a[196:199],a[64:67],v[24:27]
s_add_u32 s69,s69,s53
ds_read_u16_d16_hi v38,v38
v_lshl_add_u64 v[60:61],v[52:53],0,v[54:55]
v_mfma_f32_16x16x32_bf16 v[24:27],a[192:195],a[48:51],v[24:27]
buffer_load_dwordx4 v5,s[44:47],s69 offen offset:1024 lds
v_lshlrev_b32_sdwa v39,1,v39 dst_sel:DWORD dst_unused:UNUSED_PAD src0_sel:DWORD src1_sel:WORD_1
v_mfma_f32_16x16x32_bf16 v[52:55],a[192:195],a[52:55],v[16:19]
s_waitcnt lgkmcnt(7)
ds_read_u16 v37,v37
v_or_b32 v62,v56,v58
v_mfma_f32_16x16x32_bf16 v[52:55],a[196:199],a[68:71],v[52:55]
s_add_u32 s69,s69,s53
ds_read_u16_d16_hi v39,v39
v_cvt_pk_bf16_f32 v63,v57,v59
v_lshlrev_b32_sdwa v48,1,v48 dst_sel:DWORD dst_unused:UNUSED_PAD src0_sel:DWORD src1_sel:WORD_1
v_mfma_f32_16x16x32_bf16 v[52:55],a[200:203],a[84:87],v[52:55]
buffer_load_dwordx4 v5,s[44:47],s69 offen offset:2048 lds
v_lshlrev_b32_sdwa v50,1,v50 dst_sel:DWORD dst_unused:UNUSED_PAD src0_sel:DWORD src1_sel:WORD_1
v_mfma_f32_16x16x32_bf16 v[56:59],a[212:215],a[84:87],v[20:23]
ds_read_u16 v48,v48
v_lshlrev_b32_sdwa v49,1,v49 dst_sel:DWORD dst_unused:UNUSED_PAD src0_sel:DWORD src1_sel:WORD_1
v_mfma_f32_16x16x32_bf16 v[56:59],a[204:207],a[68:71],v[56:59]
ds_read_u16_d16_hi v50,v50
v_lshlrev_b32_sdwa v51,1,v51 dst_sel:DWORD dst_unused:UNUSED_PAD src0_sel:DWORD src1_sel:WORD_1
v_mfma_f32_16x16x32_bf16 v[56:59],a[208:211],a[52:55],v[56:59]
ds_read_u16 v49,v49
v_lshlrev_b32_sdwa v44,1,v44 dst_sel:DWORD dst_unused:UNUSED_PAD src0_sel:DWORD src1_sel:WORD_1
v_mfma_f32_16x16x32_bf16 v[136:139],a[228:231],v[32:35],v[136:139]
ds_read_u16_d16_hi v51,v51
v_lshlrev_b32_sdwa v46,1,v46 dst_sel:DWORD dst_unused:UNUSED_PAD src0_sel:DWORD src1_sel:WORD_1
v_mfma_f32_16x16x32_bf16 v[140:143],a[228:231],v[60:63],v[140:143]
ds_read_u16 v44,v44
v_mfma_f32_16x16x32_bf16 v[68:71],a[232:235],v[60:63],v[68:71]
ds_read_u16_d16_hi v46,v46
v_lshlrev_b32_sdwa v45,1,v45 dst_sel:DWORD dst_unused:UNUSED_PAD src0_sel:DWORD src1_sel:WORD_1
v_mfma_f32_16x16x32_bf16 v[64:67],a[232:235],v[32:35],v[64:67]
ds_read_u16 v45,v45
v_mfma_f32_16x16x32_bf16 v[144:147],a[236:239],v[32:35],v[144:147]
ds_read_b64_tr_b16 a[216:217],v1 offset:20528
v_lshlrev_b32_sdwa v47,1,v47 dst_sel:DWORD dst_unused:UNUSED_PAD src0_sel:DWORD src1_sel:WORD_1
v_mfma_f32_16x16x32_bf16 v[148:151],a[236:239],v[60:63],v[148:151]
ds_read_u16_d16_hi v47,v47
v_lshlrev_b32_sdwa v28,1,v28 dst_sel:DWORD dst_unused:UNUSED_PAD src0_sel:DWORD src1_sel:WORD_1
v_mfma_f32_16x16x32_bf16 v[84:87],a[244:247],v[60:63],v[84:87]
ds_read_u16 v28,v28
v_mfma_f32_16x16x32_bf16 v[80:83],a[244:247],v[32:35],v[80:83]
s_waitcnt lgkmcnt(8)
ds_read_b64_tr_b16 a[218:219],v1 offset:21552
v_lshlrev_b32_sdwa v30,1,v30 dst_sel:DWORD dst_unused:UNUSED_PAD src0_sel:DWORD src1_sel:WORD_1
v_mfma_f32_16x16x32_bf16 v[152:155],a[248:251],v[32:35],v[152:155]
ds_read_u16_d16_hi v30,v30
v_lshlrev_b32_sdwa v29,1,v29 dst_sel:DWORD dst_unused:UNUSED_PAD src0_sel:DWORD src1_sel:WORD_1
v_mfma_f32_16x16x32_bf16 v[156:159],a[248:251],v[60:63],v[156:159]
ds_read_u16 v29,v29
v_mfma_f32_16x16x32_bf16 v[100:103],a[252:255],v[60:63],v[100:103]
v_lshlrev_b32_sdwa v31,1,v31 dst_sel:DWORD dst_unused:UNUSED_PAD src0_sel:DWORD src1_sel:WORD_1
v_mfma_f32_16x16x32_bf16 v[96:99],a[252:255],v[32:35],v[96:99]
ds_read_u16_d16_hi v31,v31
v_mfma_f32_16x16x32_bf16 v[60:63],a[208:211],a[56:59],v[20:23]
v_mul_f32_dpp v136,v14,v136 row_newbcast:0 row_mask:0xf bank_mask:0xf
s_add_u32 s8,s61,0
v_mul_f32_dpp v137,v14,v137 row_newbcast:1 row_mask:0xf bank_mask:0xf
v_mul_f32_dpp v138,v14,v138 row_newbcast:2 row_mask:0xf bank_mask:0xf
v_lshlrev_b32_sdwa v24,1,v24 dst_sel:DWORD dst_unused:UNUSED_PAD src0_sel:DWORD src1_sel:WORD_1
v_mfma_f32_16x16x32_bf16 v[60:63],a[204:207],a[72:75],v[60:63]
ds_read_u16 v24,v24
v_lshl_add_u64 v[40:41],v[40:41],0,v[42:43]
v_mul_f32_dpp v139,v14,v139 row_newbcast:3 row_mask:0xf bank_mask:0xf
v_mul_f32_dpp v64,v14,v64 row_newbcast:4 row_mask:0xf bank_mask:0xf
v_mul_f32_dpp v65,v14,v65 row_newbcast:5 row_mask:0xf bank_mask:0xf
v_mfma_f32_16x16x32_bf16 v[60:63],a[212:215],a[88:91],v[60:63]
v_lshl_add_u64 v[42:43],v[36:37],0,v[38:39]
v_mul_f32_dpp v66,v14,v66 row_newbcast:6 row_mask:0xf bank_mask:0xf
v_mul_f32_dpp v67,v14,v67 row_newbcast:7 row_mask:0xf bank_mask:0xf
v_lshlrev_b32_sdwa v26,1,v26 dst_sel:DWORD dst_unused:UNUSED_PAD src0_sel:DWORD src1_sel:WORD_1
v_cvt_pk_bf16_f32 v136,v136,v137
v_mfma_f32_16x16x32_bf16 v[32:35],a[200:203],a[88:91],v[16:19]
s_nop 0
ds_read_u16_d16_hi v26,v26
v_cvt_pk_bf16_f32 v137,v138,v139
v_cvt_pk_bf16_f32 v138,v64,v65
v_lshlrev_b32_sdwa v25,1,v25 dst_sel:DWORD dst_unused:UNUSED_PAD src0_sel:DWORD src1_sel:WORD_1
v_cvt_pk_bf16_f32 v139,v66,v67
v_mfma_f32_16x16x32_bf16 v[32:35],a[196:199],a[72:75],v[32:35]
s_waitcnt lgkmcnt(5)
buffer_store_dwordx4 v[136:139],v7,s[40:43],s8 offen offset:0 sc1
v_lshl_add_u64 v[36:37],v[48:49],0,v[50:51]
v_mul_f32_dpp v144,v14,v144 row_newbcast:8 row_mask:0xf bank_mask:0xf
v_mul_f32_dpp v145,v14,v145 row_newbcast:9 row_mask:0xf bank_mask:0xf
v_lshlrev_b32_sdwa v27,1,v27 dst_sel:DWORD dst_unused:UNUSED_PAD src0_sel:DWORD src1_sel:WORD_1
v_mul_f32_dpp v146,v14,v146 row_newbcast:10 row_mask:0xf bank_mask:0xf
v_mfma_f32_16x16x32_bf16 v[32:35],a[192:195],a[56:59],v[32:35]
ds_read_u16 v25,v25
v_mul_f32_dpp v147,v14,v147 row_newbcast:11 row_mask:0xf bank_mask:0xf
v_mul_f32_dpp v80,v14,v80 row_newbcast:12 row_mask:0xf bank_mask:0xf
v_mul_f32_dpp v81,v14,v81 row_newbcast:13 row_mask:0xf bank_mask:0xf
v_mfma_f32_16x16x32_bf16 v[48:51],a[192:195],a[60:63],v[16:19]
ds_read_b128 v[16:19],v4 offset:0
v_mul_f32_dpp v82,v14,v82 row_newbcast:14 row_mask:0xf bank_mask:0xf
v_mul_f32_dpp v83,v14,v83 row_newbcast:15 row_mask:0xf bank_mask:0xf
v_lshlrev_b32_sdwa v56,1,v56 dst_sel:DWORD dst_unused:UNUSED_PAD src0_sel:DWORD src1_sel:WORD_1
v_cvt_pk_bf16_f32 v144,v144,v145
v_mfma_f32_16x16x32_bf16 v[48:51],a[196:199],a[76:79],v[48:51]
ds_read_u16_d16_hi v27,v27
ds_read_b64_tr_b16 a[192:193],v1 offset:22576
v_lshl_add_u64 v[38:39],v[44:45],0,v[46:47]
v_cvt_pk_bf16_f32 v145,v146,v147
v_cvt_pk_bf16_f32 v146,v80,v81
v_lshlrev_b32_sdwa v58,1,v58 dst_sel:DWORD dst_unused:UNUSED_PAD src0_sel:DWORD src1_sel:WORD_1
v_cvt_pk_bf16_f32 v147,v82,v83
v_mfma_f32_16x16x32_bf16 v[48:51],a[200:203],a[92:95],v[48:51]
ds_read_b64_tr_b16 a[194:195],v1 offset:23600
ds_read_b64_tr_b16 a[196:197],v2 offset:22576
v_mul_f32_dpp v152,v15,v152 row_newbcast:0 row_mask:0xf bank_mask:0xf
v_mul_f32_dpp v153,v15,v153 row_newbcast:1 row_mask:0xf bank_mask:0xf
v_lshlrev_b32_sdwa v57,1,v57 dst_sel:DWORD dst_unused:UNUSED_PAD src0_sel:DWORD src1_sel:WORD_1
v_mul_f32_dpp v154,v15,v154 row_newbcast:2 row_mask:0xf bank_mask:0xf
v_mfma_f32_16x16x32_bf16 v[44:47],a[212:215],a[92:95],v[20:23]
ds_read_b128 v[20:23],v4 offset:32
ds_read_b64_tr_b16 a[198:199],v2 offset:23600
v_mul_f32_dpp v155,v15,v155 row_newbcast:3 row_mask:0xf bank_mask:0xf
v_mul_f32_dpp v96,v15,v96 row_newbcast:4 row_mask:0xf bank_mask:0xf
v_lshlrev_b32_sdwa v59,1,v59 dst_sel:DWORD dst_unused:UNUSED_PAD src0_sel:DWORD src1_sel:WORD_1
v_mul_f32_dpp v97,v15,v97 row_newbcast:5 row_mask:0xf bank_mask:0xf
v_mfma_f32_16x16x32_bf16 v[44:47],a[204:207],a[76:79],v[44:47]
ds_read_u16 v56,v56
ds_read_u16_d16_hi v58,v58
v_mul_f32_dpp v98,v15,v98 row_newbcast:6 row_mask:0xf bank_mask:0xf
v_mul_f32_dpp v99,v15,v99 row_newbcast:7 row_mask:0xf bank_mask:0xf
v_cvt_pk_bf16_f32 v152,v152,v153
v_mfma_f32_16x16x32_bf16 v[44:47],a[208:211],a[60:63],v[44:47]
ds_read_b64_tr_b16 a[200:201],v2 offset:20528
ds_read_b64_tr_b16 a[202:203],v2 offset:21552
v_cvt_pk_bf16_f32 v153,v154,v155
v_cvt_pk_bf16_f32 v154,v96,v97
v_lshlrev_b32_sdwa v52,1,v52 dst_sel:DWORD dst_unused:UNUSED_PAD src0_sel:DWORD src1_sel:WORD_1
v_cvt_pk_bf16_f32 v155,v98,v99
v_mfma_f32_16x16x32_bf16 v[112:115],a[228:231],v[40:43],v[112:115]
ds_read_u16 v57,v57
ds_read_u16_d16_hi v59,v59
v_mul_f32_dpp v140,v14,v140 row_newbcast:0 row_mask:0xf bank_mask:0xf
v_mul_f32_dpp v141,v14,v141 row_newbcast:1 row_mask:0xf bank_mask:0xf
v_lshlrev_b32_sdwa v54,1,v54 dst_sel:DWORD dst_unused:UNUSED_PAD src0_sel:DWORD src1_sel:WORD_1
v_mul_f32_dpp v142,v14,v142 row_newbcast:2 row_mask:0xf bank_mask:0xf
v_mfma_f32_16x16x32_bf16 v[116:119],a[228:231],v[36:39],v[116:119]
ds_read_b64_tr_b16 a[212:213],v2 offset:18480
v_mul_f32_dpp v143,v14,v143 row_newbcast:3 row_mask:0xf bank_mask:0xf
v_mul_f32_dpp v68,v14,v68 row_newbcast:4 row_mask:0xf bank_mask:0xf
v_lshlrev_b32_sdwa v53,1,v53 dst_sel:DWORD dst_unused:UNUSED_PAD src0_sel:DWORD src1_sel:WORD_1
v_mul_f32_dpp v69,v14,v69 row_newbcast:5 row_mask:0xf bank_mask:0xf
v_mfma_f32_16x16x32_bf16 v[76:79],a[232:235],v[36:39],v[76:79]
ds_read_b64_tr_b16 a[214:215],v2 offset:19504
v_mul_f32_dpp v70,v14,v70 row_newbcast:6 row_mask:0xf bank_mask:0xf
v_mul_f32_dpp v71,v14,v71 row_newbcast:7 row_mask:0xf bank_mask:0xf
v_lshlrev_b32_sdwa v55,1,v55 dst_sel:DWORD dst_unused:UNUSED_PAD src0_sel:DWORD src1_sel:WORD_1
v_cvt_pk_bf16_f32 v140,v140,v141
v_mfma_f32_16x16x32_bf16 v[72:75],a[232:235],v[40:43],v[72:75]
buffer_store_dwordx4 v[144:147],v7,s[40:43],s8 offen offset:64 sc1
ds_read_u16 v52,v52
v_cvt_pk_bf16_f32 v141,v142,v143
v_cvt_pk_bf16_f32 v142,v68,v69
v_cvt_pk_bf16_f32 v143,v70,v71
v_mfma_f32_16x16x32_bf16 v[120:123],a[236:239],v[40:43],v[120:123]
TP4868_%=:
s_cbranch_execz TP6995_%=
ds_read_u16_d16_hi v54,v54
v_mul_f32_dpp v148,v14,v148 row_newbcast:8 row_mask:0xf bank_mask:0xf
v_mul_f32_dpp v149,v14,v149 row_newbcast:9 row_mask:0xf bank_mask:0xf
v_lshlrev_b32_sdwa v60,1,v60 dst_sel:DWORD dst_unused:UNUSED_PAD src0_sel:DWORD src1_sel:WORD_1
v_mul_f32_dpp v150,v14,v150 row_newbcast:10 row_mask:0xf bank_mask:0xf
v_mfma_f32_16x16x32_bf16 v[124:127],a[236:239],v[36:39],v[124:127]
s_nop 0
ds_read_u16 v53,v53
v_mul_f32_dpp v151,v14,v151 row_newbcast:11 row_mask:0xf bank_mask:0xf
v_mul_f32_dpp v84,v14,v84 row_newbcast:12 row_mask:0xf bank_mask:0xf
v_lshlrev_b32_sdwa v62,1,v62 dst_sel:DWORD dst_unused:UNUSED_PAD src0_sel:DWORD src1_sel:WORD_1
v_mul_f32_dpp v85,v14,v85 row_newbcast:13 row_mask:0xf bank_mask:0xf
v_mfma_f32_16x16x32_bf16 v[92:95],a[244:247],v[36:39],v[92:95]
ds_read_u16_d16_hi v55,v55
v_mul_f32_dpp v86,v14,v86 row_newbcast:14 row_mask:0xf bank_mask:0xf
v_mul_f32_dpp v87,v14,v87 row_newbcast:15 row_mask:0xf bank_mask:0xf
v_lshlrev_b32_sdwa v61,1,v61 dst_sel:DWORD dst_unused:UNUSED_PAD src0_sel:DWORD src1_sel:WORD_1
v_cvt_pk_bf16_f32 v148,v148,v149
v_mfma_f32_16x16x32_bf16 v[88:91],a[244:247],v[40:43],v[88:91]
ds_read_u16 v60,v60
v_cvt_pk_bf16_f32 v149,v150,v151
v_cvt_pk_bf16_f32 v150,v84,v85
v_lshlrev_b32_sdwa v63,1,v63 dst_sel:DWORD dst_unused:UNUSED_PAD src0_sel:DWORD src1_sel:WORD_1
v_cvt_pk_bf16_f32 v151,v86,v87
v_mfma_f32_16x16x32_bf16 v[128:131],a[248:251],v[40:43],v[128:131]
ds_read_u16_d16_hi v62,v62
v_mul_f32_dpp v156,v15,v156 row_newbcast:0 row_mask:0xf bank_mask:0xf
v_mul_f32_dpp v157,v15,v157 row_newbcast:1 row_mask:0xf bank_mask:0xf
v_mul_f32_dpp v158,v15,v158 row_newbcast:2 row_mask:0xf bank_mask:0xf
v_mfma_f32_16x16x32_bf16 v[132:135],a[248:251],v[36:39],v[132:135]
ds_read_u16 v61,v61
v_mul_f32_dpp v159,v15,v159 row_newbcast:3 row_mask:0xf bank_mask:0xf
v_mul_f32_dpp v100,v15,v100 row_newbcast:4 row_mask:0xf bank_mask:0xf
v_lshlrev_b32_sdwa v32,1,v32 dst_sel:DWORD dst_unused:UNUSED_PAD src0_sel:DWORD src1_sel:WORD_1
v_mul_f32_dpp v101,v15,v101 row_newbcast:5 row_mask:0xf bank_mask:0xf
v_mfma_f32_16x16x32_bf16 v[108:111],a[252:255],v[36:39],v[108:111]
s_nop 0
ds_read_u16_d16_hi v63,v63
v_mul_f32_dpp v102,v15,v102 row_newbcast:6 row_mask:0xf bank_mask:0xf
v_mul_f32_dpp v103,v15,v103 row_newbcast:7 row_mask:0xf bank_mask:0xf
v_lshlrev_b32_sdwa v34,1,v34 dst_sel:DWORD dst_unused:UNUSED_PAD src0_sel:DWORD src1_sel:WORD_1
v_cvt_pk_bf16_f32 v156,v156,v157
v_mfma_f32_16x16x32_bf16 v[104:107],a[252:255],v[40:43],v[104:107]
s_waitcnt lgkmcnt(8)
ds_read_u16 v32,v32
v_cvt_pk_bf16_f32 v157,v158,v159
v_cvt_pk_bf16_f32 v158,v100,v101
v_lshlrev_b32_sdwa v33,1,v33 dst_sel:DWORD dst_unused:UNUSED_PAD src0_sel:DWORD src1_sel:WORD_1
v_cvt_pk_bf16_f32 v159,v102,v103
v_lshlrev_b32_sdwa v35,1,v35 dst_sel:DWORD dst_unused:UNUSED_PAD src0_sel:DWORD src1_sel:WORD_1
v_mfma_f32_16x16x32_bf16 v[36:39],a[220:223],a[48:51],v[16:19]
ds_read_u16_d16_hi v34,v34
v_mul_f32_dpp v112,v14,v112 row_newbcast:0 row_mask:0xf bank_mask:0xf
s_add_u32 s9,s61,98304
v_accvgpr_write_b32 a16,0
v_accvgpr_write_b32 a17,0
v_lshlrev_b32_sdwa v44,1,v44 dst_sel:DWORD dst_unused:UNUSED_PAD src0_sel:DWORD src1_sel:WORD_1
v_mfma_f32_16x16x32_bf16 v[36:39],a[216:219],a[64:67],v[36:39]
ds_read_u16 v33,v33
v_lshl_add_u64 v[28:29],v[28:29],0,v[30:31]
v_accvgpr_write_b32 a18,0
v_accvgpr_write_b32 a19,0
v_mul_f32_dpp v113,v14,v113 row_newbcast:1 row_mask:0xf bank_mask:0xf
v_mfma_f32_16x16x32_bf16 v[36:39],a[192:195],a[80:83],v[36:39]
buffer_store_dwordx4 v[152:155],v7,s[40:43],s8 offen offset:128 sc1
ds_read_u16_d16_hi v35,v35
v_lshl_add_u64 v[30:31],v[24:25],0,v[26:27]
v_mul_f32_dpp v114,v14,v114 row_newbcast:2 row_mask:0xf bank_mask:0xf
v_mul_f32_dpp v115,v14,v115 row_newbcast:3 row_mask:0xf bank_mask:0xf
v_lshlrev_b32_sdwa v46,1,v46 dst_sel:DWORD dst_unused:UNUSED_PAD src0_sel:DWORD src1_sel:WORD_1
v_mul_f32_dpp v72,v14,v72 row_newbcast:4 row_mask:0xf bank_mask:0xf
v_mfma_f32_16x16x32_bf16 v[40:43],a[196:199],a[80:83],v[20:23]
ds_read_u16 v44,v44
v_mul_f32_dpp v73,v14,v73 row_newbcast:5 row_mask:0xf bank_mask:0xf
v_mul_f32_dpp v74,v14,v74 row_newbcast:6 row_mask:0xf bank_mask:0xf
v_lshlrev_b32_sdwa v45,1,v45 dst_sel:DWORD dst_unused:UNUSED_PAD src0_sel:DWORD src1_sel:WORD_1
v_mul_f32_dpp v75,v14,v75 row_newbcast:7 row_mask:0xf bank_mask:0xf
v_mfma_f32_16x16x32_bf16 v[40:43],a[200:203],a[64:67],v[40:43]
ds_read_u16_d16_hi v46,v46
v_lshl_add_u64 v[24:25],v[56:57],0,v[58:59]
v_cvt_pk_bf16_f32 v112,v112,v113
v_cvt_pk_bf16_f32 v113,v114,v115
v_lshlrev_b32_sdwa v47,1,v47 dst_sel:DWORD dst_unused:UNUSED_PAD src0_sel:DWORD src1_sel:WORD_1
v_cvt_pk_bf16_f32 v114,v72,v73
v_mfma_f32_16x16x32_bf16 v[40:43],a[212:215],a[48:51],v[40:43]
ds_read_u16 v45,v45
v_cvt_pk_bf16_f32 v115,v74,v75
v_mul_f32_dpp v120,v14,v120 row_newbcast:8 row_mask:0xf bank_mask:0xf
v_lshlrev_b32_sdwa v48,1,v48 dst_sel:DWORD dst_unused:UNUSED_PAD src0_sel:DWORD src1_sel:WORD_1
v_accvgpr_write_b32 a24,v112
v_mfma_f32_16x16x32_bf16 v[56:59],a[212:215],a[52:55],v[20:23]
s_nop 0
ds_read_u16_d16_hi v47,v47
v_mul_f32_dpp v121,v14,v121 row_newbcast:9 row_mask:0xf bank_mask:0xf
v_mul_f32_dpp v122,v14,v122 row_newbcast:10 row_mask:0xf bank_mask:0xf
v_lshlrev_b32_sdwa v50,1,v50 dst_sel:DWORD dst_unused:UNUSED_PAD src0_sel:DWORD src1_sel:WORD_1
v_accvgpr_write_b32 a25,v113
v_mfma_f32_16x16x32_bf16 v[56:59],a[200:203],a[68:71],v[56:59]
s_waitcnt lgkmcnt(8)
ds_read_u16 v48,v48
v_lshl_add_u64 v[26:27],v[52:53],0,v[54:55]
v_mul_f32_dpp v123,v14,v123 row_newbcast:11 row_mask:0xf bank_mask:0xf
v_mul_f32_dpp v88,v14,v88 row_newbcast:12 row_mask:0xf bank_mask:0xf
v_lshlrev_b32_sdwa v49,1,v49 dst_sel:DWORD dst_unused:UNUSED_PAD src0_sel:DWORD src1_sel:WORD_1
v_accvgpr_write_b32 a26,v114
v_mfma_f32_16x16x32_bf16 v[56:59],a[196:199],a[84:87],v[56:59]
s_waitcnt vmcnt(10)
s_barrier
ds_read_u16_d16_hi v50,v50
v_mul_f32_dpp v89,v14,v89 row_newbcast:13 row_mask:0xf bank_mask:0xf
v_mul_f32_dpp v90,v14,v90 row_newbcast:14 row_mask:0xf bank_mask:0xf
v_lshlrev_b32_sdwa v51,1,v51 dst_sel:DWORD dst_unused:UNUSED_PAD src0_sel:DWORD src1_sel:WORD_1
v_accvgpr_write_b32 a27,v115
v_mfma_f32_16x16x32_bf16 v[52:55],a[192:195],a[84:87],v[16:19]
ds_read_u16 v49,v49
v_mul_f32_dpp v91,v14,v91 row_newbcast:15 row_mask:0xf bank_mask:0xf
v_cvt_pk_bf16_f32 v120,v120,v121
v_lshlrev_b32_sdwa v36,1,v36 dst_sel:DWORD dst_unused:UNUSED_PAD src0_sel:DWORD src1_sel:WORD_1
v_cvt_pk_bf16_f32 v121,v122,v123
v_mfma_f32_16x16x32_bf16 v[52:55],a[216:219],a[68:71],v[52:55]
s_add_u32 m0,s58,18480
ds_read_u16_d16_hi v51,v51
v_cvt_pk_bf16_f32 v122,v88,v89
v_cvt_pk_bf16_f32 v123,v90,v91
v_lshlrev_b32_sdwa v38,1,v38 dst_sel:DWORD dst_unused:UNUSED_PAD src0_sel:DWORD src1_sel:WORD_1
v_mul_f32_dpp v128,v15,v128 row_newbcast:0 row_mask:0xf bank_mask:0xf
v_mfma_f32_16x16x32_bf16 v[52:55],a[220:223],a[52:55],v[52:55]
s_add_u32 s69,s69,s59
ds_read_u16 v36,v36
v_accvgpr_write_b32 a28,v120
v_mul_f32_dpp v129,v15,v129 row_newbcast:1 row_mask:0xf bank_mask:0xf
v_lshlrev_b32_sdwa v37,1,v37 dst_sel:DWORD dst_unused:UNUSED_PAD src0_sel:DWORD src1_sel:WORD_1
v_mul_f32_dpp v130,v15,v130 row_newbcast:2 row_mask:0xf bank_mask:0xf
v_mfma_f32_16x16x32_bf16 v[232:235],a[228:231],v[28:31],v[232:235]
buffer_load_dwordx4 v5,s[44:47],s69 offen offset:0 lds
ds_read_u16_d16_hi v38,v38
v_accvgpr_write_b32 a29,v121
v_mul_f32_dpp v131,v15,v131 row_newbcast:3 row_mask:0xf bank_mask:0xf
v_lshlrev_b32_sdwa v39,1,v39 dst_sel:DWORD dst_unused:UNUSED_PAD src0_sel:DWORD src1_sel:WORD_1
v_mul_f32_dpp v104,v15,v104 row_newbcast:4 row_mask:0xf bank_mask:0xf
v_mfma_f32_16x16x32_bf16 v[236:239],a[228:231],v[24:27],v[236:239]
s_add_u32 s69,s69,s53
ds_read_u16 v37,v37
v_accvgpr_write_b32 a30,v122
v_mul_f32_dpp v105,v15,v105 row_newbcast:5 row_mask:0xf bank_mask:0xf
v_lshlrev_b32_sdwa v40,1,v40 dst_sel:DWORD dst_unused:UNUSED_PAD src0_sel:DWORD src1_sel:WORD_1
v_mul_f32_dpp v106,v15,v106 row_newbcast:6 row_mask:0xf bank_mask:0xf
v_mfma_f32_16x16x32_bf16 v[164:167],a[232:235],v[24:27],v[164:167]
buffer_load_dwordx4 v5,s[44:47],s69 offen offset:1024 lds
ds_read_u16_d16_hi v39,v39
v_accvgpr_write_b32 a31,v123
v_mul_f32_dpp v107,v15,v107 row_newbcast:7 row_mask:0xf bank_mask:0xf
v_lshlrev_b32_sdwa v42,1,v42 dst_sel:DWORD dst_unused:UNUSED_PAD src0_sel:DWORD src1_sel:WORD_1
v_cvt_pk_bf16_f32 v128,v128,v129
v_mfma_f32_16x16x32_bf16 v[160:163],a[232:235],v[28:31],v[160:163]
s_nop 0
buffer_store_dwordx4 v[140:143],v7,s[40:43],s8 offen offset:192 sc1
ds_read_u16 v40,v40
v_cvt_pk_bf16_f32 v129,v130,v131
v_cvt_pk_bf16_f32 v130,v104,v105
v_lshlrev_b32_sdwa v41,1,v41 dst_sel:DWORD dst_unused:UNUSED_PAD src0_sel:DWORD src1_sel:WORD_1
v_cvt_pk_bf16_f32 v131,v106,v107
v_mfma_f32_16x16x32_bf16 v[240:243],a[236:239],v[28:31],v[240:243]
s_add_u32 s69,s69,s53
ds_read_u16_d16_hi v42,v42
ds_read_u16 v41,v41
v_mul_f32_dpp v116,v14,v116 row_newbcast:0 row_mask:0xf bank_mask:0xf
v_accvgpr_write_b32 a32,v128
v_lshlrev_b32_sdwa v43,1,v43 dst_sel:DWORD dst_unused:UNUSED_PAD src0_sel:DWORD src1_sel:WORD_1
v_mul_f32_dpp v117,v14,v117 row_newbcast:1 row_mask:0xf bank_mask:0xf
v_mfma_f32_16x16x32_bf16 v[244:247],a[236:239],v[24:27],v[244:247]
buffer_load_dwordx4 v5,s[44:47],s69 offen offset:2048 lds
ds_read_u16_d16_hi v43,v43
v_mul_f32_dpp v118,v14,v118 row_newbcast:2 row_mask:0xf bank_mask:0xf
v_accvgpr_write_b32 a33,v129
v_lshlrev_b32_sdwa v56,1,v56 dst_sel:DWORD dst_unused:UNUSED_PAD src0_sel:DWORD src1_sel:WORD_1
v_mul_f32_dpp v119,v14,v119 row_newbcast:3 row_mask:0xf bank_mask:0xf
v_mfma_f32_16x16x32_bf16 v[180:183],a[244:247],v[24:27],v[180:183]
ds_read_u16 v56,v56
ds_read_b64_tr_b16 a[240:241],v3 offset:0
v_mul_f32_dpp v76,v14,v76 row_newbcast:4 row_mask:0xf bank_mask:0xf
v_accvgpr_write_b32 a34,v130
v_lshlrev_b32_sdwa v58,1,v58 dst_sel:DWORD dst_unused:UNUSED_PAD src0_sel:DWORD src1_sel:WORD_1
v_mul_f32_dpp v77,v14,v77 row_newbcast:5 row_mask:0xf bank_mask:0xf
v_mfma_f32_16x16x32_bf16 v[176:179],a[244:247],v[28:31],v[176:179]
ds_read_u16_d16_hi v58,v58
v_mul_f32_dpp v78,v14,v78 row_newbcast:6 row_mask:0xf bank_mask:0xf
v_accvgpr_write_b32 a35,v131
v_lshlrev_b32_sdwa v57,1,v57 dst_sel:DWORD dst_unused:UNUSED_PAD src0_sel:DWORD src1_sel:WORD_1
v_mul_f32_dpp v79,v14,v79 row_newbcast:7 row_mask:0xf bank_mask:0xf
v_mfma_f32_16x16x32_bf16 v[248:251],a[248:251],v[28:31],v[248:251]
ds_read_u16 v57,v57
v_cvt_pk_bf16_f32 v116,v116,v117
v_cvt_pk_bf16_f32 v117,v118,v119
v_lshlrev_b32_sdwa v59,1,v59 dst_sel:DWORD dst_unused:UNUSED_PAD src0_sel:DWORD src1_sel:WORD_1
v_cvt_pk_bf16_f32 v118,v76,v77
v_mfma_f32_16x16x32_bf16 v[252:255],a[248:251],v[24:27],v[252:255]
ds_read_u16_d16_hi v59,v59
v_cvt_pk_bf16_f32 v119,v78,v79
v_mfma_f32_32x32x16_bf16 v[64:79],a[16:19],a[16:19],0
v_lshlrev_b32_sdwa v52,1,v52 dst_sel:DWORD dst_unused:UNUSED_PAD src0_sel:DWORD src1_sel:WORD_1
v_mul_f32_dpp v124,v14,v124 row_newbcast:8 row_mask:0xf bank_mask:0xf
v_lshlrev_b32_sdwa v54,1,v54 dst_sel:DWORD dst_unused:UNUSED_PAD src0_sel:DWORD src1_sel:WORD_1
v_accvgpr_write_b32 a36,v116
v_mfma_f32_16x16x32_bf16 v[196:199],a[252:255],v[24:27],v[196:199]
ds_read_u16 v52,v52
ds_read_u16_d16_hi v54,v54
v_mul_f32_dpp v125,v14,v125 row_newbcast:9 row_mask:0xf bank_mask:0xf
v_mul_f32_dpp v126,v14,v126 row_newbcast:10 row_mask:0xf bank_mask:0xf
v_mfma_f32_16x16x32_bf16 v[192:195],a[252:255],v[28:31],v[192:195]
ds_read_b64_tr_b16 a[242:243],v3 offset:1536
v_accvgpr_write_b32 a37,v117
v_mul_f32_dpp v127,v14,v127 row_newbcast:11 row_mask:0xf bank_mask:0xf
v_lshlrev_b32_sdwa v53,1,v53 dst_sel:DWORD dst_unused:UNUSED_PAD src0_sel:DWORD src1_sel:WORD_1
v_mul_f32_dpp v92,v14,v92 row_newbcast:12 row_mask:0xf bank_mask:0xf
v_lshlrev_b32_sdwa v55,1,v55 dst_sel:DWORD dst_unused:UNUSED_PAD src0_sel:DWORD src1_sel:WORD_1
ds_read_u16 v53,v53
ds_read_u16_d16_hi v55,v55
ds_read_b64_tr_b16 a[224:225],v3 offset:8
ds_read_b64_tr_b16 a[226:227],v3 offset:1544
s_mov_b64 exec,0
s_cmp_eq_u32 s52,0
s_cbranch_scc1 TE10_%=
s_cmp_eq_u32 s52,1
s_cbranch_scc1 TE11_%=
s_cmp_eq_u32 s52,2
s_cbranch_scc1 TE12_%=
s_branch TE13_%=
TR1_%=:
s_mov_b64 exec,-1
v_add_u32_sdwa v28,s60,v10 dst_sel:DWORD dst_unused:UNUSED_PAD src0_sel:DWORD src1_sel:WORD_1
v_subrev_u32 v8,88400,v28
v_min_u32 v28,v28,v8
v_add_u32 v29,7072,v28
v_subrev_u32 v8,88400,v29
v_min_u32 v29,v29,v8
v_lshl_add_u64 v[60:61],v[60:61],0,v[62:63]
s_waitcnt lgkmcnt(15)
v_lshl_add_u64 v[62:63],v[32:33],0,v[34:35]
v_lshl_add_u64 v[32:33],v[44:45],0,v[46:47]
v_lshl_add_u64 v[34:35],v[48:49],0,v[50:51]
v_mfma_f32_16x16x32_bf16 v[208:211],a[228:231],v[60:63],v[208:211]
buffer_store_dwordx4 v[148:151],v7,s[40:43],s8 offen offset:256 sc1
v_xad_u32 v24,v11,0,v28
v_xad_u32 v25,v11,8,v29
v_xad_u32 v26,v11,16,v28
v_xad_u32 v27,v11,24,v29
v_mfma_f32_16x16x32_bf16 v[212:215],a[228:231],v[32:35],v[212:215]
ds_read_b64 a[0:1],v24 offset:0
v_accvgpr_write_b32 a38,v118
v_mul_f32_dpp v93,v14,v93 row_newbcast:13 row_mask:0xf bank_mask:0xf
v_mul_f32_dpp v94,v14,v94 row_newbcast:14 row_mask:0xf bank_mask:0xf
v_accvgpr_write_b32 a39,v119
v_mfma_f32_16x16x32_bf16 v[172:175],a[232:235],v[32:35],v[172:175]
ds_read_b64 a[2:3],v24 offset:192
v_mul_f32_dpp v95,v14,v95 row_newbcast:15 row_mask:0xf bank_mask:0xf
v_cvt_pk_bf16_f32 v124,v124,v125
v_cvt_pk_bf16_f32 v125,v126,v127
v_mfma_f32_16x16x32_bf16 v[168:171],a[232:235],v[60:63],v[168:171]
ds_read_b64 a[4:5],v25 offset:0
v_cvt_pk_bf16_f32 v126,v92,v93
v_cvt_pk_bf16_f32 v127,v94,v95
v_mfma_f32_32x32x16_bf16 v[80:95],a[16:19],a[16:19],0
v_mfma_f32_16x16x32_bf16 v[216:219],a[236:239],v[60:63],v[216:219]
ds_read_b64 a[6:7],v25 offset:192
v_mul_f32_dpp v132,v15,v132 row_newbcast:0 row_mask:0xf bank_mask:0xf
v_accvgpr_write_b32 a40,v124
v_mul_f32_dpp v133,v15,v133 row_newbcast:1 row_mask:0xf bank_mask:0xf
v_mfma_f32_16x16x32_bf16 v[220:223],a[236:239],v[32:35],v[220:223]
ds_read_b64 a[8:9],v24 offset:8
v_mul_f32_dpp v134,v15,v134 row_newbcast:2 row_mask:0xf bank_mask:0xf
v_accvgpr_write_b32 a41,v125
v_mul_f32_dpp v135,v15,v135 row_newbcast:3 row_mask:0xf bank_mask:0xf
v_mfma_f32_16x16x32_bf16 v[188:191],a[244:247],v[32:35],v[188:191]
ds_read_b64 a[10:11],v24 offset:200
v_mul_f32_dpp v108,v15,v108 row_newbcast:4 row_mask:0xf bank_mask:0xf
v_accvgpr_write_b32 a42,v126
v_mul_f32_dpp v109,v15,v109 row_newbcast:5 row_mask:0xf bank_mask:0xf
v_mfma_f32_16x16x32_bf16 v[184:187],a[244:247],v[60:63],v[184:187]
ds_read_b64 a[12:13],v25 offset:8
v_mul_f32_dpp v110,v15,v110 row_newbcast:6 row_mask:0xf bank_mask:0xf
v_accvgpr_write_b32 a43,v127
v_mul_f32_dpp v111,v15,v111 row_newbcast:7 row_mask:0xf bank_mask:0xf
v_mfma_f32_16x16x32_bf16 v[224:227],a[248:251],v[60:63],v[224:227]
ds_read_b64 a[14:15],v25 offset:200
v_cvt_pk_bf16_f32 v132,v132,v133
v_cvt_pk_bf16_f32 v133,v134,v135
v_cvt_pk_bf16_f32 v134,v108,v109
v_mfma_f32_16x16x32_bf16 v[228:231],a[248:251],v[32:35],v[228:231]
v_cvt_pk_bf16_f32 v135,v110,v111
v_mfma_f32_32x32x16_bf16 v[96:111],a[16:19],a[16:19],0
v_accvgpr_write_b32 a44,v132
v_mfma_f32_16x16x32_bf16 v[204:207],a[252:255],v[32:35],v[204:207]
v_accvgpr_write_b32 a45,v133
v_accvgpr_write_b32 a46,v134
v_accvgpr_write_b32 a47,v135
v_mfma_f32_16x16x32_bf16 v[200:203],a[252:255],v[60:63],v[200:203]
buffer_store_dwordx4 v[156:159],v7,s[40:43],s8 offen offset:320 sc1
v_mfma_f32_32x32x16_bf16 v[112:127],a[16:19],a[16:19],0
v_mov_b64 v[44:45],0
v_mov_b64 v[46:47],0
ds_read_b64 a[16:17],v26 offset:0
ds_read_b64 a[18:19],v26 offset:192
ds_read_b64 a[20:21],v27 offset:0
ds_read_b64 a[22:23],v27 offset:192
s_waitcnt lgkmcnt(8)
v_smfmac_f32_16x16x64_bf16 v[64:67],a[96:99],a[0:7],v10
s_waitcnt lgkmcnt(4)
ds_read_b64 a[0:1],v26 offset:8
ds_read_b64 a[2:3],v26 offset:200
ds_read_b64 a[4:5],v27 offset:8
ds_read_b64 a[6:7],v27 offset:200
v_smfmac_f32_16x16x64_bf16 v[68:71],a[100:103],a[8:15],v10
s_waitcnt lgkmcnt(4)
ds_read_b64 a[8:9],v24 offset:64
ds_read_b64 a[10:11],v24 offset:256
ds_read_b64 a[12:13],v25 offset:64
ds_read_b64 a[14:15],v25 offset:256
v_smfmac_f32_16x16x64_bf16 v[72:75],a[104:107],a[16:23],v10
s_waitcnt lgkmcnt(4)
ds_read_b64 a[16:17],v24 offset:72
ds_read_b64 a[18:19],v24 offset:264
ds_read_b64 a[20:21],v25 offset:72
ds_read_b64 a[22:23],v25 offset:264
v_and_b32 v60,8,v11
v_pk_mul_f32 v[32:33],v[64:65],v[64:65]
v_pk_mul_f32 v[34:35],v[66:67],v[66:67]
v_smfmac_f32_16x16x64_bf16 v[76:79],a[108:111],a[0:7],v10
s_waitcnt lgkmcnt(4)
ds_read_b64 a[0:1],v26 offset:64
ds_read_b64 a[2:3],v26 offset:256
ds_read_b64 a[4:5],v27 offset:64
ds_read_b64 a[6:7],v27 offset:256
v_mul_u32_u24 v60,884,v60
s_add_u32 s64,s60,3536
v_pk_add_f32 v[48:49],v[64:65],v[68:69]
v_pk_fma_f32 v[32:33],v[68:69],v[68:69],v[32:33]
v_pk_add_f32 v[50:51],v[66:67],v[70:71]
v_pk_fma_f32 v[34:35],v[70:71],v[70:71],v[34:35]
v_smfmac_f32_16x16x64_bf16 v[80:83],a[128:131],a[8:15],v10
s_waitcnt lgkmcnt(4)
ds_read_b64 a[8:9],v26 offset:72
ds_read_b64 a[10:11],v26 offset:264
ds_read_b64 a[12:13],v27 offset:72
ds_read_b64 a[14:15],v27 offset:264
v_add_u32 v63,s64,v60
v_pk_add_f32 v[48:49],v[48:49],v[72:73]
v_pk_fma_f32 v[32:33],v[72:73],v[72:73],v[32:33]
v_pk_add_f32 v[50:51],v[50:51],v[74:75]
v_pk_fma_f32 v[34:35],v[74:75],v[74:75],v[34:35]
v_smfmac_f32_16x16x64_bf16 v[84:87],a[132:135],a[16:23],v10
s_waitcnt lgkmcnt(4)
ds_read_b64 a[16:17],v24 offset:128
ds_read_b64 a[18:19],v24 offset:320
ds_read_b64 a[20:21],v25 offset:128
ds_read_b64 a[22:23],v25 offset:320
v_subrev_u32 v8,88400,v63
v_pk_add_f32 v[48:49],v[48:49],v[76:77]
v_pk_fma_f32 v[32:33],v[76:77],v[76:77],v[32:33]
v_pk_add_f32 v[50:51],v[50:51],v[78:79]
v_pk_fma_f32 v[34:35],v[78:79],v[78:79],v[34:35]
v_smfmac_f32_16x16x64_bf16 v[88:91],a[136:139],a[0:7],v10
s_waitcnt lgkmcnt(4)
ds_read_b64 a[0:1],v24 offset:136
ds_read_b64 a[2:3],v24 offset:328
ds_read_b64 a[4:5],v25 offset:136
ds_read_b64 a[6:7],v25 offset:328
v_min_u32 v63,v63,v8
v_pk_add_f32 v[48:49],v[48:49],v[80:81]
v_pk_fma_f32 v[32:33],v[80:81],v[80:81],v[32:33]
v_pk_add_f32 v[50:51],v[50:51],v[82:83]
v_pk_fma_f32 v[34:35],v[82:83],v[82:83],v[34:35]
v_smfmac_f32_16x16x64_bf16 v[92:95],a[140:143],a[8:15],v10
s_waitcnt lgkmcnt(4)
buffer_store_dwordx4 a[24:27],v7,s[40:43],s9 offen offset:0 sc1
ds_read_b64 a[8:9],v26 offset:128
ds_read_b64 a[10:11],v26 offset:320
ds_read_b64 a[12:13],v27 offset:128
ds_read_b64 a[14:15],v27 offset:320
v_xad_u32 v24,v11,32,v28
v_xad_u32 v25,v11,40,v29
v_pk_add_f32 v[48:49],v[48:49],v[84:85]
v_pk_fma_f32 v[32:33],v[84:85],v[84:85],v[32:33]
v_pk_add_f32 v[50:51],v[50:51],v[86:87]
v_pk_fma_f32 v[34:35],v[86:87],v[86:87],v[34:35]
v_smfmac_f32_16x16x64_bf16 v[96:99],a[160:163],a[16:23],v10
s_waitcnt lgkmcnt(4)
ds_read_b64 a[16:17],v26 offset:136
ds_read_b64 a[18:19],v26 offset:328
ds_read_b64 a[20:21],v27 offset:136
ds_read_b64 a[22:23],v27 offset:328
v_add_u32 v62,3536,v63
v_pk_add_f32 v[48:49],v[48:49],v[88:89]
v_pk_fma_f32 v[32:33],v[88:89],v[88:89],v[32:33]
v_pk_add_f32 v[50:51],v[50:51],v[90:91]
v_pk_fma_f32 v[34:35],v[90:91],v[90:91],v[34:35]
v_smfmac_f32_16x16x64_bf16 v[100:103],a[164:167],a[0:7],v10
s_waitcnt lgkmcnt(4)
ds_read_b64 a[0:1],v24 offset:0
ds_read_b64 a[2:3],v24 offset:192
ds_read_b64 a[4:5],v25 offset:0
ds_read_b64 a[6:7],v25 offset:192
v_xad_u32 v26,v11,48,v28
v_xad_u32 v27,v11,56,v29
v_pk_add_f32 v[48:49],v[48:49],v[92:93]
v_pk_fma_f32 v[32:33],v[92:93],v[92:93],v[32:33]
v_pk_add_f32 v[50:51],v[50:51],v[94:95]
v_pk_fma_f32 v[34:35],v[94:95],v[94:95],v[34:35]
v_smfmac_f32_16x16x64_bf16 v[104:107],a[168:171],a[8:15],v10
s_waitcnt lgkmcnt(4)
ds_read_b64 a[8:9],v24 offset:8
ds_read_b64 a[10:11],v24 offset:200
ds_read_b64 a[12:13],v25 offset:8
ds_read_b64 a[14:15],v25 offset:200
v_subrev_u32 v8,88400,v62
v_pk_add_f32 v[48:49],v[48:49],v[96:97]
v_pk_fma_f32 v[32:33],v[96:97],v[96:97],v[32:33]
v_pk_add_f32 v[50:51],v[50:51],v[98:99]
v_pk_fma_f32 v[34:35],v[98:99],v[98:99],v[34:35]
v_smfmac_f32_16x16x64_bf16 v[108:111],a[172:175],a[16:23],v10
s_waitcnt lgkmcnt(4)
ds_read_b64 a[16:17],v24 offset:64
ds_read_b64 a[18:19],v24 offset:256
ds_read_b64 a[20:21],v25 offset:64
ds_read_b64 a[22:23],v25 offset:256
v_min_u32 v62,v62,v8
v_pk_add_f32 v[48:49],v[48:49],v[100:101]
v_pk_fma_f32 v[32:33],v[100:101],v[100:101],v[32:33]
v_pk_add_f32 v[50:51],v[50:51],v[102:103]
v_pk_fma_f32 v[34:35],v[102:103],v[102:103],v[34:35]
v_smfmac_f32_16x16x64_bf16 v[112:115],a[112:115],a[0:7],v10
s_waitcnt lgkmcnt(4)
ds_read_b64 a[0:1],v24 offset:72
ds_read_b64 a[2:3],v24 offset:264
ds_read_b64 a[4:5],v25 offset:72
ds_read_b64 a[6:7],v25 offset:264
v_xor_b32 v9,0x80008,v12
v_pk_add_f32 v[48:49],v[48:49],v[104:105]
v_pk_fma_f32 v[32:33],v[104:105],v[104:105],v[32:33]
v_pk_add_f32 v[50:51],v[50:51],v[106:107]
v_pk_fma_f32 v[34:35],v[106:107],v[106:107],v[34:35]
v_mfma_f32_32x32x16_bf16 v[128:143],v[44:47],v[44:47],0
v_smfmac_f32_16x16x64_bf16 v[116:119],a[116:119],a[8:15],v10
s_waitcnt lgkmcnt(4)
buffer_store_dwordx4 a[28:31],v7,s[40:43],s9 offen offset:64 sc1
ds_read_b64 a[8:9],v24 offset:128
ds_read_b64 a[10:11],v24 offset:320
ds_read_b64 a[12:13],v25 offset:128
ds_read_b64 a[14:15],v25 offset:320
v_add_u32_sdwa v60,v12,v63 dst_sel:DWORD dst_unused:UNUSED_PAD src0_sel:WORD_0 src1_sel:DWORD
v_pk_add_f32 v[48:49],v[48:49],v[108:109]
v_pk_fma_f32 v[32:33],v[108:109],v[108:109],v[32:33]
v_pk_add_f32 v[50:51],v[50:51],v[110:111]
v_pk_fma_f32 v[34:35],v[110:111],v[110:111],v[34:35]
v_smfmac_f32_16x16x64_bf16 v[120:123],a[144:147],a[16:23],v10
s_waitcnt lgkmcnt(4)
ds_read_b64 a[16:17],v24 offset:136
ds_read_b64 a[18:19],v24 offset:328
ds_read_b64 a[20:21],v25 offset:136
ds_read_b64 a[22:23],v25 offset:328
v_add_u32_sdwa v61,v12,v63 dst_sel:DWORD dst_unused:UNUSED_PAD src0_sel:WORD_1 src1_sel:DWORD
v_pk_add_f32 v[48:49],v[48:49],v[112:113]
v_pk_fma_f32 v[32:33],v[112:113],v[112:113],v[32:33]
v_pk_add_f32 v[50:51],v[50:51],v[114:115]
v_pk_fma_f32 v[34:35],v[114:115],v[114:115],v[34:35]
v_smfmac_f32_16x16x64_bf16 v[124:127],a[148:151],a[0:7],v10
s_waitcnt lgkmcnt(4)
ds_read_b64 a[0:1],v26 offset:0
ds_read_b64 a[2:3],v26 offset:192
ds_read_b64 a[4:5],v27 offset:0
ds_read_b64 a[6:7],v27 offset:192
v_add_u32_sdwa v63,v9,v62 dst_sel:DWORD dst_unused:UNUSED_PAD src0_sel:WORD_1 src1_sel:DWORD
v_pk_add_f32 v[48:49],v[48:49],v[116:117]
v_pk_fma_f32 v[32:33],v[116:117],v[116:117],v[32:33]
v_pk_add_f32 v[50:51],v[50:51],v[118:119]
v_pk_fma_f32 v[34:35],v[118:119],v[118:119],v[34:35]
v_smfmac_f32_16x16x64_bf16 v[128:131],a[176:179],a[8:15],v10
s_waitcnt lgkmcnt(4)
ds_read_b64 a[8:9],v26 offset:8
ds_read_b64 a[10:11],v26 offset:200
ds_read_b64 a[12:13],v27 offset:8
ds_read_b64 a[14:15],v27 offset:200
v_add_u32_sdwa v62,v9,v62 dst_sel:DWORD dst_unused:UNUSED_PAD src0_sel:WORD_0 src1_sel:DWORD
v_pk_add_f32 v[48:49],v[48:49],v[120:121]
v_pk_fma_f32 v[32:33],v[120:121],v[120:121],v[32:33]
v_pk_add_f32 v[50:51],v[50:51],v[122:123]
v_pk_fma_f32 v[34:35],v[122:123],v[122:123],v[34:35]
v_mfma_f32_32x32x16_bf16 v[144:159],v[44:47],v[44:47],0
v_smfmac_f32_16x16x64_bf16 v[132:135],a[180:183],a[16:23],v10
s_waitcnt lgkmcnt(4)
ds_read_b64 a[16:17],v26 offset:64
ds_read_b64 a[18:19],v26 offset:256
ds_read_b64 a[20:21],v27 offset:64
ds_read_b64 a[22:23],v27 offset:256
v_pk_add_f32 v[48:49],v[48:49],v[124:125]
v_pk_fma_f32 v[32:33],v[124:125],v[124:125],v[32:33]
v_pk_add_f32 v[50:51],v[50:51],v[126:127]
v_pk_fma_f32 v[34:35],v[126:127],v[126:127],v[34:35]
v_smfmac_f32_16x16x64_bf16 v[136:139],a[120:123],a[0:7],v10
s_waitcnt lgkmcnt(4)
ds_read_b64 a[0:1],v26 offset:72
ds_read_b64 a[2:3],v26 offset:264
ds_read_b64 a[4:5],v27 offset:72
ds_read_b64 a[6:7],v27 offset:264
v_pk_add_f32 v[48:49],v[48:49],v[128:129]
v_pk_fma_f32 v[32:33],v[128:129],v[128:129],v[32:33]
v_pk_add_f32 v[50:51],v[50:51],v[130:131]
v_pk_fma_f32 v[34:35],v[130:131],v[130:131],v[34:35]
v_smfmac_f32_16x16x64_bf16 v[140:143],a[124:127],a[8:15],v10
s_waitcnt lgkmcnt(4)
ds_read_b64 a[8:9],v26 offset:128
ds_read_b64 a[10:11],v26 offset:320
ds_read_b64 a[12:13],v27 offset:128
ds_read_b64 a[14:15],v27 offset:320
v_pk_add_f32 v[48:49],v[48:49],v[132:133]
v_pk_fma_f32 v[32:33],v[132:133],v[132:133],v[32:33]
v_pk_add_f32 v[50:51],v[50:51],v[134:135]
v_pk_fma_f32 v[34:35],v[134:135],v[134:135],v[34:35]
v_smfmac_f32_16x16x64_bf16 v[144:147],a[152:155],a[16:23],v10
s_waitcnt lgkmcnt(4)
buffer_store_dwordx4 a[32:35],v7,s[40:43],s9 offen offset:128 sc1
ds_read_b64 a[16:17],v26 offset:136
ds_read_b64 a[18:19],v26 offset:328
ds_read_b64 a[20:21],v27 offset:136
ds_read_b64 a[22:23],v27 offset:328
v_pk_add_f32 v[48:49],v[48:49],v[136:137]
v_pk_fma_f32 v[32:33],v[136:137],v[136:137],v[32:33]
v_pk_add_f32 v[50:51],v[50:51],v[138:139]
v_pk_fma_f32 v[34:35],v[138:139],v[138:139],v[34:35]
v_smfmac_f32_16x16x64_bf16 v[148:151],a[156:159],a[0:7],v10
s_waitcnt lgkmcnt(4)
ds_read_b64 a[228:229],v60 offset:57344
ds_read_b64 a[230:231],v60 offset:57352
ds_read_b64 a[232:233],v61 offset:57344
ds_read_b64 a[234:235],v61 offset:57352
ds_read_b128 a[248:251],v13 offset:0
TP5404_%=:
s_cbranch_execz TR0_%=
v_pk_add_f32 v[48:49],v[48:49],v[140:141]
v_pk_fma_f32 v[32:33],v[140:141],v[140:141],v[32:33]
v_pk_add_f32 v[50:51],v[50:51],v[142:143]
v_pk_fma_f32 v[34:35],v[142:143],v[142:143],v[34:35]
v_smfmac_f32_16x16x64_bf16 v[152:155],a[184:187],a[8:15],v10
s_waitcnt lgkmcnt(5)
s_nop 0
ds_read_b64 a[236:237],v62 offset:57344
ds_read_b64 a[238:239],v62 offset:57352
ds_read_b64 a[244:245],v63 offset:57344
ds_read_b64 a[246:247],v63 offset:57352
ds_read_b128 a[252:255],v13 offset:256
v_pk_add_f32 v[48:49],v[48:49],v[144:145]
v_pk_fma_f32 v[32:33],v[144:145],v[144:145],v[32:33]
v_pk_add_f32 v[50:51],v[50:51],v[146:147]
v_pk_fma_f32 v[34:35],v[146:147],v[146:147],v[34:35]
v_smfmac_f32_16x16x64_bf16 v[156:159],a[188:191],a[16:23],v10
ds_read_b128 v[24:27],v4 offset:3584
ds_read_b128 v[28:31],v4 offset:3616
v_pk_add_f32 v[48:49],v[48:49],v[148:149]
v_pk_fma_f32 v[32:33],v[148:149],v[148:149],v[32:33]
v_pk_add_f32 v[50:51],v[50:51],v[150:151]
v_pk_fma_f32 v[34:35],v[150:151],v[150:151],v[34:35]
v_pk_add_f32 v[48:49],v[48:49],v[152:153]
v_pk_fma_f32 v[32:33],v[152:153],v[152:153],v[32:33]
v_pk_add_f32 v[50:51],v[50:51],v[154:155]
v_pk_fma_f32 v[34:35],v[154:155],v[154:155],v[34:35]
v_pk_add_f32 v[48:49],v[48:49],v[156:157]
v_pk_fma_f32 v[32:33],v[156:157],v[156:157],v[32:33]
v_pk_add_f32 v[50:51],v[50:51],v[158:159]
v_pk_fma_f32 v[34:35],v[158:159],v[158:159],v[34:35]
s_nop 0
v_permlane32_swap_b32 v48,v50
v_permlane32_swap_b32 v49,v51
v_permlane32_swap_b32 v32,v34
v_permlane32_swap_b32_e64 v33,v35
v_pk_add_f32 v[48:49],v[48:49],v[50:51]
v_pk_add_f32 v[32:33],v[32:33],v[34:35]
s_add_u32 s60,s60,56576
s_sub_u32 s64,s60,88400
s_min_u32 s60,s60,s64
v_permlane16_swap_b32 v48,v49
v_permlane16_swap_b32 v32,v33
v_add_f32 v8,v48,v49
v_add_f32_e64 v9,v32,v33
v_mul_f32 v8,0x3c2aaaab,v8
v_mul_f32 v9,0x3c2aaaab,v9
v_cmp_ngt_f32 vcc,0x5d800000,v9
v_fma_f32 v9,-v8,v8,v9
v_max_f32 v9,0,v9
s_or_b64 s[90:91],s[90:91],vcc
v_add_f32 v9,0x3727c5ac,v9
v_rsq_f32 v9,v9
s_nop 0
v_mul_f32 v48,s96,v9
v_mul_f32 v49,s96,v9
v_mul_f32 v32,-v8,v48
v_mul_f32 v33,-v8,v48
v_permlane16_swap_b32 v48,v49
s_nop 0
v_permlane16_swap_b32 v32,v33
v_mov_b64 v[50:51],v[48:49]
v_mov_b64 v[34:35],v[32:33]
s_nop 0
v_permlane32_swap_b32 v48,v50
v_permlane32_swap_b32 v49,v51
v_permlane32_swap_b32 v32,v34
v_permlane32_swap_b32 v33,v35
v_pk_fma_f32 v[64:65],v[64:65],v[48:49],v[32:33]
v_pk_fma_f32 v[66:67],v[66:67],v[50:51],v[34:35]
v_pk_fma_f32 v[68:69],v[68:69],v[48:49],v[32:33]
v_pk_fma_f32 v[70:71],v[70:71],v[50:51],v[34:35]
v_pk_fma_f32 v[72:73],v[72:73],v[48:49],v[32:33]
v_pk_fma_f32 v[74:75],v[74:75],v[50:51],v[34:35]
v_pk_fma_f32 v[76:77],v[76:77],v[48:49],v[32:33]
v_pk_fma_f32 v[78:79],v[78:79],v[50:51],v[34:35]
v_pk_fma_f32 v[112:113],v[112:113],v[48:49],v[32:33]
v_pk_fma_f32 v[114:115],v[114:115],v[50:51],v[34:35]
v_pk_fma_f32 v[116:117],v[116:117],v[48:49],v[32:33]
v_pk_fma_f32 v[118:119],v[118:119],v[50:51],v[34:35]
v_pk_fma_f32 v[136:137],v[136:137],v[48:49],v[32:33]
v_pk_fma_f32 v[138:139],v[138:139],v[50:51],v[34:35]
v_pk_fma_f32 v[140:141],v[140:141],v[48:49],v[32:33]
v_pk_fma_f32 v[142:143],v[142:143],v[50:51],v[34:35]
v_pk_fma_f32 v[80:81],v[80:81],v[48:49],v[32:33]
v_pk_fma_f32 v[82:83],v[82:83],v[50:51],v[34:35]
v_pk_fma_f32 v[84:85],v[84:85],v[48:49],v[32:33]
v_pk_fma_f32 v[86:87],v[86:87],v[50:51],v[34:35]
v_pk_fma_f32 v[88:89],v[88:89],v[48:49],v[32:33]
v_pk_fma_f32 v[90:91],v[90:91],v[50:51],v[34:35]
v_pk_fma_f32 v[92:93],v[92:93],v[48:49],v[32:33]
v_pk_fma_f32 v[94:95],v[94:95],v[50:51],v[34:35]
v_pk_fma_f32 v[120:121],v[120:121],v[48:49],v[32:33]
v_pk_fma_f32 v[122:123],v[122:123],v[50:51],v[34:35]
v_pk_fma_f32 v[124:125],v[124:125],v[48:49],v[32:33]
v_pk_fma_f32 v[126:127],v[126:127],v[50:51],v[34:35]
v_pk_fma_f32 v[144:145],v[144:145],v[48:49],v[32:33]
v_pk_fma_f32 v[146:147],v[146:147],v[50:51],v[34:35]
v_pk_fma_f32 v[148:149],v[148:149],v[48:49],v[32:33]
v_pk_fma_f32 v[150:151],v[150:151],v[50:51],v[34:35]
v_pk_fma_f32 v[96:97],v[96:97],v[48:49],v[32:33]
v_pk_fma_f32 v[98:99],v[98:99],v[50:51],v[34:35]
v_pk_fma_f32 v[100:101],v[100:101],v[48:49],v[32:33]
v_pk_fma_f32 v[102:103],v[102:103],v[50:51],v[34:35]
v_pk_fma_f32 v[104:105],v[104:105],v[48:49],v[32:33]
v_pk_fma_f32 v[106:107],v[106:107],v[50:51],v[34:35]
v_pk_fma_f32 v[108:109],v[108:109],v[48:49],v[32:33]
v_pk_fma_f32 v[110:111],v[110:111],v[50:51],v[34:35]
v_pk_fma_f32 v[128:129],v[128:129],v[48:49],v[32:33]
v_pk_fma_f32 v[130:131],v[130:131],v[50:51],v[34:35]
v_pk_fma_f32 v[132:133],v[132:133],v[48:49],v[32:33]
v_pk_fma_f32 v[134:135],v[134:135],v[50:51],v[34:35]
v_pk_fma_f32 v[152:153],v[152:153],v[48:49],v[32:33]
v_pk_fma_f32 v[154:155],v[154:155],v[50:51],v[34:35]
v_pk_fma_f32 v[156:157],v[156:157],v[48:49],v[32:33]
v_pk_fma_f32 v[158:159],v[158:159],v[50:51],v[34:35]
v_mfma_f32_16x16x32_bf16 v[44:47],a[220:223],a[56:59],v[16:19]
buffer_store_dwordx4 a[36:39],v7,s[40:43],s9 offen offset:192 sc1
v_cvt_pk_bf16_f32 v64,v64,v68
v_cvt_pk_bf16_f32 v72,v72,v76
v_cvt_pk_bf16_f32 v112,v112,v116
v_mfma_f32_16x16x32_bf16 v[44:47],a[216:219],a[72:75],v[44:47]
ds_read_b64_tr_b16 a[204:205],v1 offset:0
v_accvgpr_write_b32 a0,v64
v_accvgpr_write_b32 a1,v72
v_cvt_pk_bf16_f32 v136,v136,v140
v_accvgpr_write_b32 a2,v112
v_mfma_f32_16x16x32_bf16 v[44:47],a[192:195],a[88:91],v[44:47]
ds_read_b64_tr_b16 a[206:207],v1 offset:1024
v_cvt_pk_bf16_f32 v65,v65,v69
v_accvgpr_write_b32 a3,v136
v_cvt_pk_bf16_f32 v73,v73,v77
v_accvgpr_write_b32 a4,v65
v_mfma_f32_16x16x32_bf16 v[48:51],a[196:199],a[88:91],v[20:23]
buffer_store_dwordx4 a[40:43],v7,s[40:43],s9 offen offset:256 sc1
v_cvt_pk_bf16_f32 v113,v113,v117
v_accvgpr_write_b32 a5,v73
v_cvt_pk_bf16_f32 v137,v137,v141
v_accvgpr_write_b32 a6,v113
v_mfma_f32_16x16x32_bf16 v[48:51],a[200:203],a[72:75],v[48:51]
ds_read_b64_tr_b16 a[208:209],v1 offset:2048
v_cvt_pk_bf16_f32 v66,v66,v70
v_accvgpr_write_b32 a7,v137
v_lshlrev_b32_sdwa v44,1,v44 dst_sel:DWORD dst_unused:UNUSED_PAD src0_sel:DWORD src1_sel:WORD_1
v_cvt_pk_bf16_f32 v74,v74,v78
v_lshlrev_b32_sdwa v46,1,v46 dst_sel:DWORD dst_unused:UNUSED_PAD src0_sel:DWORD src1_sel:WORD_1
v_accvgpr_write_b32 a8,v66
v_mfma_f32_16x16x32_bf16 v[48:51],a[212:215],a[56:59],v[48:51]
ds_read_u16 v44,v44
v_accvgpr_write_b32 a9,v74
v_cvt_pk_bf16_f32 v114,v114,v118
v_accvgpr_write_b32 a10,v114
v_cvt_pk_bf16_f32 v138,v138,v142
v_mfma_f32_16x16x32_bf16 v[32:35],a[212:215],a[60:63],v[20:23]
ds_read_u16_d16_hi v46,v46
v_accvgpr_write_b32 a11,v138
v_cvt_pk_bf16_f32 v67,v67,v71
v_accvgpr_write_b32 a12,v67
v_mfma_f32_16x16x32_bf16 v[32:35],a[200:203],a[76:79],v[32:35]
ds_read_b64_tr_b16 a[210:211],v1 offset:3072
v_cvt_pk_bf16_f32 v75,v75,v79
v_accvgpr_write_b32 a13,v75
v_cvt_pk_bf16_f32 v115,v115,v119
v_accvgpr_write_b32 a14,v115
v_lshlrev_b32_sdwa v45,1,v45 dst_sel:DWORD dst_unused:UNUSED_PAD src0_sel:DWORD src1_sel:WORD_1
v_mfma_f32_16x16x32_bf16 v[32:35],a[196:199],a[92:95],v[32:35]
s_waitcnt lgkmcnt(6)
ds_read_u16 v45,v45
v_cvt_pk_bf16_f32 v139,v139,v143
v_accvgpr_write_b32 a15,v139
v_cvt_pk_bf16_f32 v80,v80,v84
v_cvt_pk_bf16_f32 v88,v88,v92
v_mfma_f32_16x16x32_bf16 v[136:139],a[248:251],a[228:231],v[24:27]
s_nop 0
v_cvt_pk_bf16_f32 v120,v120,v124
v_accvgpr_write_b32 a16,v80
v_accvgpr_write_b32 a17,v88
v_cvt_pk_bf16_f32 v144,v144,v148
v_mfma_f32_16x16x32_bf16 v[140:143],a[248:251],a[232:235],v[24:27]
v_accvgpr_write_b32 a18,v120
v_cvt_pk_bf16_f32 v81,v81,v85
v_accvgpr_write_b32 a19,v144
v_cvt_pk_bf16_f32 v89,v89,v93
v_lshlrev_b32_sdwa v47,1,v47 dst_sel:DWORD dst_unused:UNUSED_PAD src0_sel:DWORD src1_sel:WORD_1
v_mfma_f32_16x16x32_bf16 v[112:115],a[248:251],a[236:239],v[24:27]
ds_read_u16_d16_hi v47,v47
v_accvgpr_write_b32 a20,v81
v_cvt_pk_bf16_f32 v121,v121,v125
v_accvgpr_write_b32 a21,v89
v_cvt_pk_bf16_f32 v145,v145,v149
v_mfma_f32_16x16x32_bf16 v[116:119],a[248:251],a[244:247],v[24:27]
ds_read_b128 a[248:251],v13 offset:512
ds_read_b128 v[24:27],v4 offset:3712
v_accvgpr_write_b32 a22,v121
v_cvt_pk_bf16_f32 v82,v82,v86
v_accvgpr_write_b32 a23,v145
v_mfma_f32_16x16x32_bf16 v[64:67],a[252:255],a[228:231],v[28:31]
ds_read_b64 a[228:229],v60 offset:57408
ds_read_b64 a[230:231],v60 offset:57416
v_cvt_pk_bf16_f32 v90,v90,v94
v_accvgpr_write_b32 a24,v82
v_accvgpr_write_b32 a25,v90
v_cvt_pk_bf16_f32 v122,v122,v126
v_mfma_f32_16x16x32_bf16 v[68:71],a[252:255],a[232:235],v[28:31]
buffer_store_dwordx4 a[44:47],v7,s[40:43],s9 offen offset:320 sc1
ds_read_b64 a[232:233],v61 offset:57408
v_accvgpr_write_b32 a26,v122
v_cvt_pk_bf16_f32 v146,v146,v150
v_accvgpr_write_b32 a27,v146
v_cvt_pk_bf16_f32 v83,v83,v87
v_lshlrev_b32_sdwa v48,1,v48 dst_sel:DWORD dst_unused:UNUSED_PAD src0_sel:DWORD src1_sel:WORD_1
v_mfma_f32_16x16x32_bf16 v[72:75],a[252:255],a[236:239],v[28:31]
ds_read_b64 a[234:235],v61 offset:57416
ds_read_b64 a[236:237],v62 offset:57408
v_accvgpr_write_b32 a28,v83
v_cvt_pk_bf16_f32 v91,v91,v95
v_accvgpr_write_b32 a29,v91
v_cvt_pk_bf16_f32 v123,v123,v127
v_mfma_f32_16x16x32_bf16 v[76:79],a[252:255],a[244:247],v[28:31]
s_waitcnt lgkmcnt(3)
ds_read_b64 a[238:239],v62 offset:57416
ds_read_b64 a[244:245],v63 offset:57408
v_accvgpr_write_b32 a30,v123
v_cvt_pk_bf16_f32 v147,v147,v151
v_accvgpr_write_b32 a31,v147
v_cvt_pk_bf16_f32 v96,v96,v100
v_mfma_f32_16x16x32_bf16 v[144:147],a[248:251],a[228:231],v[24:27]
s_waitcnt lgkmcnt(3)
ds_read_b64 a[246:247],v63 offset:57416
ds_read_b128 a[252:255],v13 offset:768
v_cvt_pk_bf16_f32 v104,v104,v108
v_cvt_pk_bf16_f32 v128,v128,v132
v_accvgpr_write_b32 a32,v96
v_lshlrev_b32_sdwa v50,1,v50 dst_sel:DWORD dst_unused:UNUSED_PAD src0_sel:DWORD src1_sel:WORD_1
v_accvgpr_write_b32 a33,v104
v_mfma_f32_16x16x32_bf16 v[148:151],a[248:251],a[232:235],v[24:27]
s_waitcnt lgkmcnt(3)
ds_read_b128 v[28:31],v4 offset:3744
v_cvt_pk_bf16_f32 v152,v152,v156
v_accvgpr_write_b32 a34,v128
v_cvt_pk_bf16_f32 v97,v97,v101
v_mfma_f32_16x16x32_bf16 v[120:123],a[248:251],a[236:239],v[24:27]
s_waitcnt lgkmcnt(2)
ds_read_u16 v48,v48
v_accvgpr_write_b32 a35,v152
v_cvt_pk_bf16_f32 v105,v105,v109
v_accvgpr_write_b32 a36,v97
v_cvt_pk_bf16_f32 v129,v129,v133
v_mfma_f32_16x16x32_bf16 v[124:127],a[248:251],a[244:247],v[24:27]
s_waitcnt lgkmcnt(1)
ds_read_b128 a[248:251],v13 offset:1024
ds_read_b128 v[24:27],v4 offset:3840
v_accvgpr_write_b32 a37,v105
v_cvt_pk_bf16_f32 v153,v153,v157
v_accvgpr_write_b32 a38,v129
v_cvt_pk_bf16_f32 v98,v98,v102
v_lshlrev_b32_sdwa v49,1,v49 dst_sel:DWORD dst_unused:UNUSED_PAD src0_sel:DWORD src1_sel:WORD_1
v_mfma_f32_16x16x32_bf16 v[80:83],a[252:255],a[228:231],v[28:31]
s_nop 0
ds_read_b64 a[228:229],v60 offset:57472
ds_read_b64 a[230:231],v60 offset:57480
v_accvgpr_write_b32 a39,v153
v_cvt_pk_bf16_f32 v106,v106,v110
v_accvgpr_write_b32 a40,v98
v_accvgpr_write_b32 a41,v106
v_mfma_f32_16x16x32_bf16 v[84:87],a[252:255],a[232:235],v[28:31]
ds_read_b64 a[232:233],v61 offset:57472
ds_read_b64 a[234:235],v61 offset:57480
v_cvt_pk_bf16_f32 v130,v130,v134
v_accvgpr_write_b32 a42,v130
v_cvt_pk_bf16_f32 v154,v154,v158
v_accvgpr_write_b32 a43,v154
v_mfma_f32_16x16x32_bf16 v[88:91],a[252:255],a[236:239],v[28:31]
ds_read_b64 a[236:237],v62 offset:57472
ds_read_b64 a[238:239],v62 offset:57480
v_cvt_pk_bf16_f32 v99,v99,v103
v_accvgpr_write_b32 a44,v99
v_cvt_pk_bf16_f32 v107,v107,v111
v_accvgpr_write_b32 a45,v107
v_lshlrev_b32_sdwa v51,1,v51 dst_sel:DWORD dst_unused:UNUSED_PAD src0_sel:DWORD src1_sel:WORD_1
v_mfma_f32_16x16x32_bf16 v[92:95],a[252:255],a[244:247],v[28:31]
s_waitcnt lgkmcnt(4)
ds_read_b64 a[244:245],v63 offset:57472
ds_read_b64 a[246:247],v63 offset:57480
v_cvt_pk_bf16_f32 v131,v131,v135
v_accvgpr_write_b32 a46,v131
v_cvt_pk_bf16_f32 v155,v155,v159
v_accvgpr_write_b32 a47,v155
v_mfma_f32_16x16x32_bf16 v[152:155],a[248:251],a[228:231],v[24:27]
s_waitcnt lgkmcnt(4)
ds_read_b128 a[252:255],v13 offset:1280
ds_read_b128 v[28:31],v4 offset:3872
v_mfma_f32_16x16x32_bf16 v[156:159],a[248:251],a[232:235],v[24:27]
s_waitcnt lgkmcnt(4)
ds_read_u16_d16_hi v50,v50
v_lshlrev_b32_sdwa v32,1,v32 dst_sel:DWORD dst_unused:UNUSED_PAD src0_sel:DWORD src1_sel:WORD_1
v_mfma_f32_16x16x32_bf16 v[128:131],a[248:251],a[236:239],v[24:27]
s_waitcnt lgkmcnt(3)
ds_read_u16 v49,v49
v_lshlrev_b32_sdwa v34,1,v34 dst_sel:DWORD dst_unused:UNUSED_PAD src0_sel:DWORD src1_sel:WORD_1
v_mfma_f32_16x16x32_bf16 v[132:135],a[248:251],a[244:247],v[24:27]
s_waitcnt lgkmcnt(2)
ds_read_b64_tr_b16 a[248:249],v3 offset:64
ds_read_b64_tr_b16 a[250:251],v3 offset:1600
v_mfma_f32_16x16x32_bf16 v[96:99],a[252:255],a[228:231],v[28:31]
ds_read_b64_tr_b16 a[228:229],v3 offset:72
ds_read_b64_tr_b16 a[230:231],v3 offset:1608
v_mfma_f32_16x16x32_bf16 v[100:103],a[252:255],a[232:235],v[28:31]
ds_read_b64_tr_b16 a[232:233],v3 offset:128
ds_read_b64_tr_b16 a[234:235],v3 offset:1664
v_mfma_f32_16x16x32_bf16 v[104:107],a[252:255],a[236:239],v[28:31]
ds_read_b64_tr_b16 a[236:237],v3 offset:136
ds_read_b64_tr_b16 a[238:239],v3 offset:1672
v_mfma_f32_16x16x32_bf16 v[108:111],a[252:255],a[244:247],v[28:31]
ds_read_u16_d16_hi v51,v51
v_lshlrev_b32_sdwa v33,1,v33 dst_sel:DWORD dst_unused:UNUSED_PAD src0_sel:DWORD src1_sel:WORD_1
v_mfma_f32_16x16x32_bf16 v[60:63],a[192:195],a[92:95],v[16:19]
ds_read_u16 v32,v32
v_lshlrev_b32_sdwa v35,1,v35 dst_sel:DWORD dst_unused:UNUSED_PAD src0_sel:DWORD src1_sel:WORD_1
v_mfma_f32_16x16x32_bf16 v[60:63],a[216:219],a[76:79],v[60:63]
s_waitcnt vmcnt(18)
ds_read_u16_d16_hi v34,v34
v_mfma_f32_16x16x32_bf16 v[60:63],a[220:223],a[60:63],v[60:63]
s_barrier
ds_read_u16 v33,v33
v_mfma_f32_16x16x32_bf16 v[24:27],a[220:223],a[0:3],v[16:19]
s_add_u32 m0,s57,24752
ds_read_u16_d16_hi v35,v35
s_nop 2
v_lshlrev_b32_sdwa v60,1,v60 dst_sel:DWORD dst_unused:UNUSED_PAD src0_sel:DWORD src1_sel:WORD_1
v_mfma_f32_16x16x32_bf16 v[24:27],a[216:219],a[16:19],v[24:27]
s_add_u32 s89,s56,5603328
ds_read_u16 v60,v60
v_lshl_add_u64 v[36:37],v[36:37],0,v[38:39]
v_mfma_f32_16x16x32_bf16 v[24:27],a[192:195],a[32:35],v[24:27]
buffer_load_dwordx4 v6,s[36:39],s89 offen lds
v_lshl_add_u64 v[38:39],v[40:41],0,v[42:43]
v_lshlrev_b32_sdwa v62,1,v62 dst_sel:DWORD dst_unused:UNUSED_PAD src0_sel:DWORD src1_sel:WORD_1
v_mfma_f32_16x16x32_bf16 v[28:31],a[196:199],a[32:35],v[20:23]
ds_read_u16_d16_hi v62,v62
v_lshlrev_b32_sdwa v61,1,v61 dst_sel:DWORD dst_unused:UNUSED_PAD src0_sel:DWORD src1_sel:WORD_1
v_mfma_f32_16x16x32_bf16 v[28:31],a[200:203],a[16:19],v[28:31]
ds_read_u16 v61,v61
v_lshl_add_u64 v[40:41],v[52:53],0,v[54:55]
v_mfma_f32_16x16x32_bf16 v[28:31],a[212:215],a[0:3],v[28:31]
v_lshlrev_b32_sdwa v63,1,v63 dst_sel:DWORD dst_unused:UNUSED_PAD src0_sel:DWORD src1_sel:WORD_1
v_lshlrev_b32_sdwa v24,1,v24 dst_sel:DWORD dst_unused:UNUSED_PAD src0_sel:DWORD src1_sel:WORD_1
v_mfma_f32_16x16x32_bf16 v[52:55],a[212:215],a[4:7],v[20:23]
ds_read_u16_d16_hi v63,v63
v_mfma_f32_16x16x32_bf16 v[52:55],a[200:203],a[20:23],v[52:55]
ds_read_u16 v24,v24
v_lshl_add_u64 v[42:43],v[56:57],0,v[58:59]
v_mfma_f32_16x16x32_bf16 v[52:55],a[196:199],a[36:39],v[52:55]
v_lshlrev_b32_sdwa v26,1,v26 dst_sel:DWORD dst_unused:UNUSED_PAD src0_sel:DWORD src1_sel:WORD_1
v_mfma_f32_16x16x32_bf16 v[56:59],a[192:195],a[36:39],v[16:19]
ds_read_u16_d16_hi v26,v26
v_lshlrev_b32_sdwa v25,1,v25 dst_sel:DWORD dst_unused:UNUSED_PAD src0_sel:DWORD src1_sel:WORD_1
v_mfma_f32_16x16x32_bf16 v[56:59],a[216:219],a[20:23],v[56:59]
ds_read_u16 v25,v25
v_mfma_f32_16x16x32_bf16 v[56:59],a[220:223],a[4:7],v[56:59]
v_lshlrev_b32_sdwa v27,1,v27 dst_sel:DWORD dst_unused:UNUSED_PAD src0_sel:DWORD src1_sel:WORD_1
v_mfma_f32_16x16x32_bf16 v[232:235],a[240:243],v[36:39],v[232:235]
ds_read_u16_d16_hi v27,v27
v_lshlrev_b32_sdwa v28,1,v28 dst_sel:DWORD dst_unused:UNUSED_PAD src0_sel:DWORD src1_sel:WORD_1
v_mfma_f32_16x16x32_bf16 v[236:239],a[240:243],v[40:43],v[236:239]
ds_read_u16 v28,v28
v_mfma_f32_16x16x32_bf16 v[164:167],a[224:227],v[40:43],v[164:167]
v_lshlrev_b32_sdwa v30,1,v30 dst_sel:DWORD dst_unused:UNUSED_PAD src0_sel:DWORD src1_sel:WORD_1
v_mfma_f32_16x16x32_bf16 v[160:163],a[224:227],v[36:39],v[160:163]
s_waitcnt lgkmcnt(5)
ds_read_u16_d16_hi v30,v30
v_mfma_f32_16x16x32_bf16 v[240:243],a[248:251],v[36:39],v[240:243]
v_lshlrev_b32_sdwa v29,1,v29 dst_sel:DWORD dst_unused:UNUSED_PAD src0_sel:DWORD src1_sel:WORD_1
v_mfma_f32_16x16x32_bf16 v[244:247],a[248:251],v[40:43],v[244:247]
ds_read_u16 v29,v29
v_mfma_f32_16x16x32_bf16 v[180:183],a[228:231],v[40:43],v[180:183]
v_lshlrev_b32_sdwa v31,1,v31 dst_sel:DWORD dst_unused:UNUSED_PAD src0_sel:DWORD src1_sel:WORD_1
v_mfma_f32_16x16x32_bf16 v[176:179],a[228:231],v[36:39],v[176:179]
ds_read_u16_d16_hi v31,v31
v_mfma_f32_16x16x32_bf16 v[248:251],a[232:235],v[36:39],v[248:251]
v_lshlrev_b32_sdwa v52,1,v52 dst_sel:DWORD dst_unused:UNUSED_PAD src0_sel:DWORD src1_sel:WORD_1
v_mfma_f32_16x16x32_bf16 v[252:255],a[232:235],v[40:43],v[252:255]
ds_read_u16 v52,v52
v_mfma_f32_16x16x32_bf16 v[196:199],a[236:239],v[40:43],v[196:199]
v_lshlrev_b32_sdwa v54,1,v54 dst_sel:DWORD dst_unused:UNUSED_PAD src0_sel:DWORD src1_sel:WORD_1
v_mfma_f32_16x16x32_bf16 v[192:195],a[236:239],v[36:39],v[192:195]
ds_read_u16_d16_hi v54,v54
v_mfma_f32_16x16x32_bf16 v[40:43],a[220:223],a[8:11],v[16:19]
v_lshlrev_b32_sdwa v56,1,v56 dst_sel:DWORD dst_unused:UNUSED_PAD src0_sel:DWORD src1_sel:WORD_1
v_fma_mixlo_f16 v8,v53,s99,0
v_mfma_f32_16x16x32_bf16 v[40:43],a[216:219],a[24:27],v[40:43]
ds_read_u16 v56,v56
v_lshl_add_u64 v[44:45],v[44:45],0,v[46:47]
v_mfma_f32_16x16x32_bf16 v[40:43],a[192:195],a[40:43],v[40:43]
v_lshl_add_u64 v[46:47],v[48:49],0,v[50:51]
v_lshlrev_b32_sdwa v58,1,v58 dst_sel:DWORD dst_unused:UNUSED_PAD src0_sel:DWORD src1_sel:WORD_1
v_mfma_f32_16x16x32_bf16 v[36:39],a[196:199],a[40:43],v[20:23]
ds_read_u16_d16_hi v58,v58
v_fma_mixhi_f16 v8,v55,s99,0
v_mfma_f32_16x16x32_bf16 v[36:39],a[200:203],a[24:27],v[36:39]
v_lshl_add_u64 v[48:49],v[60:61],0,v[62:63]
v_pk_fma_f16 v9,v8,v8,s54
v_lshlrev_b32_sdwa v57,1,v57 dst_sel:DWORD dst_unused:UNUSED_PAD src0_sel:DWORD src1_sel:WORD_1
v_mfma_f32_16x16x32_bf16 v[36:39],a[212:215],a[8:11],v[36:39]
ds_read_u16 v57,v57
v_pk_mul_f16 v9,v8,v9
v_mfma_f32_16x16x32_bf16 v[60:63],a[212:215],a[12:15],v[20:23]
ds_read_b128 v[20:23],v4 offset:128
ds_read_b64_tr_b16 a[212:213],v1 offset:4096
v_lshlrev_b32_sdwa v59,1,v59 dst_sel:DWORD dst_unused:UNUSED_PAD src0_sel:DWORD src1_sel:WORD_1
v_mfma_f32_16x16x32_bf16 v[60:63],a[200:203],a[28:31],v[60:63]
ds_read_b64_tr_b16 a[214:215],v1 offset:5120
ds_read_b64_tr_b16 a[200:201],v2 offset:4096
v_lshl_add_u64 v[50:51],v[32:33],0,v[34:35]
v_mfma_f32_16x16x32_bf16 v[60:63],a[196:199],a[44:47],v[60:63]
ds_read_b64_tr_b16 a[202:203],v2 offset:5120
ds_read_b64_tr_b16 a[196:197],v2 offset:2048
v_exp_f16 v8,v9
v_lshlrev_b32_sdwa v40,1,v40 dst_sel:DWORD dst_unused:UNUSED_PAD src0_sel:DWORD src1_sel:WORD_1
v_mfma_f32_16x16x32_bf16 v[32:35],a[192:195],a[44:47],v[16:19]
ds_read_b128 v[16:19],v4 offset:160
ds_read_b64_tr_b16 a[198:199],v2 offset:3072
v_mfma_f32_16x16x32_bf16 v[32:35],a[216:219],a[28:31],v[32:35]
ds_read_b64_tr_b16 a[192:193],v2 offset:0
ds_read_b64_tr_b16 a[194:195],v2 offset:1024
v_lshlrev_b32_sdwa v42,1,v42 dst_sel:DWORD dst_unused:UNUSED_PAD src0_sel:DWORD src1_sel:WORD_1
v_mfma_f32_16x16x32_bf16 v[32:35],a[220:223],a[12:15],v[32:35]
v_exp_f16_sdwa v8,v9 dst_sel:WORD_1 dst_unused:UNUSED_PRESERVE src0_sel:WORD_1
v_mfma_f32_16x16x32_bf16 v[208:211],a[240:243],v[44:47],v[208:211]
ds_read_u16_d16_hi v59,v59
v_lshlrev_b32_sdwa v41,1,v41 dst_sel:DWORD dst_unused:UNUSED_PAD src0_sel:DWORD src1_sel:WORD_1
v_mfma_f32_16x16x32_bf16 v[212:215],a[240:243],v[48:51],v[212:215]
ds_read_u16 v40,v40
v_pk_fma_f16 v8,v8,s55,s55
v_mfma_f32_16x16x32_bf16 v[172:175],a[224:227],v[48:51],v[172:175]
ds_read_u16_d16_hi v42,v42
v_lshlrev_b32_sdwa v43,1,v43 dst_sel:DWORD dst_unused:UNUSED_PAD src0_sel:DWORD src1_sel:WORD_1
v_mfma_f32_16x16x32_bf16 v[168:171],a[224:227],v[44:47],v[168:171]
v_rcp_f16 v9,v8
v_mfma_f32_16x16x32_bf16 v[216:219],a[248:251],v[44:47],v[216:219]
ds_read_u16 v41,v41
v_lshlrev_b32_sdwa v36,1,v36 dst_sel:DWORD dst_unused:UNUSED_PAD src0_sel:DWORD src1_sel:WORD_1
v_mfma_f32_16x16x32_bf16 v[220:223],a[248:251],v[48:51],v[220:223]
ds_read_u16_d16_hi v43,v43
v_lshlrev_b32_sdwa v38,1,v38 dst_sel:DWORD dst_unused:UNUSED_PAD src0_sel:DWORD src1_sel:WORD_1
v_mfma_f32_16x16x32_bf16 v[188:191],a[228:231],v[48:51],v[188:191]
ds_read_u16 v36,v36
v_lshlrev_b32_sdwa v37,1,v37 dst_sel:DWORD dst_unused:UNUSED_PAD src0_sel:DWORD src1_sel:WORD_1
v_mfma_f32_16x16x32_bf16 v[184:187],a[228:231],v[44:47],v[184:187]
v_rcp_f16_sdwa v9,v8 dst_sel:WORD_1 dst_unused:UNUSED_PRESERVE src0_sel:WORD_1
v_mfma_f32_16x16x32_bf16 v[224:227],a[232:235],v[44:47],v[224:227]
ds_read_u16_d16_hi v38,v38
v_fma_mix_f32 v53,v53,v9,0 op_sel_hi:[0,1,0]
v_mfma_f32_16x16x32_bf16 v[228:231],a[232:235],v[48:51],v[228:231]
ds_read_u16 v37,v37
v_lshlrev_b32_sdwa v39,1,v39 dst_sel:DWORD dst_unused:UNUSED_PAD src0_sel:DWORD src1_sel:WORD_1
v_mfma_f32_16x16x32_bf16 v[204:207],a[236:239],v[48:51],v[204:207]
ds_read_u16_d16_hi v39,v39
v_fma_mix_f32 v55,v55,v9,0 op_sel:[0,1,0] op_sel_hi:[0,1,0]
v_mfma_f32_16x16x32_bf16 v[200:203],a[236:239],v[44:47],v[200:203]
s_waitcnt lgkmcnt(6)
v_lshlrev_b32_sdwa v60,1,v60 dst_sel:DWORD dst_unused:UNUSED_PAD src0_sel:DWORD src1_sel:WORD_1
v_mfma_f32_16x16x32_bf16 v[48:51],a[204:207],a[48:51],v[20:23]
s_add_u32 m0,s57,28296
ds_read_u16 v60,v60
v_lshlrev_b32_sdwa v32,1,v32 dst_sel:DWORD dst_unused:UNUSED_PAD src0_sel:DWORD src1_sel:WORD_1
v_mfma_f32_16x16x32_bf16 v[48:51],a[208:211],a[64:67],v[48:51]
s_add_u32 s89,s56,5701632
ds_read_u16 v32,v32
v_lshl_add_u64 v[24:25],v[24:25],0,v[26:27]
v_mfma_f32_16x16x32_bf16 v[48:51],a[212:215],a[80:83],v[48:51]
buffer_load_dwordx4 v6,s[36:39],s89 offen lds
v_lshl_add_u64 v[26:27],v[28:29],0,v[30:31]
v_lshlrev_b32_sdwa v34,1,v34 dst_sel:DWORD dst_unused:UNUSED_PAD src0_sel:DWORD src1_sel:WORD_1
v_mfma_f32_16x16x32_bf16 v[44:47],a[200:203],a[80:83],v[16:19]
ds_read_u16_d16_hi v34,v34
v_lshlrev_b32_sdwa v33,1,v33 dst_sel:DWORD dst_unused:UNUSED_PAD src0_sel:DWORD src1_sel:WORD_1
v_mfma_f32_16x16x32_bf16 v[44:47],a[196:199],a[64:67],v[44:47]
ds_read_u16 v33,v33
v_lshl_add_u64 v[28:29],v[56:57],0,v[58:59]
v_mfma_f32_16x16x32_bf16 v[44:47],a[192:195],a[48:51],v[44:47]
v_lshlrev_b32_sdwa v35,1,v35 dst_sel:DWORD dst_unused:UNUSED_PAD src0_sel:DWORD src1_sel:WORD_1
v_lshlrev_b32_sdwa v62,1,v62 dst_sel:DWORD dst_unused:UNUSED_PAD src0_sel:DWORD src1_sel:WORD_1
v_mfma_f32_16x16x32_bf16 v[56:59],a[192:195],a[52:55],v[16:19]
ds_read_u16_d16_hi v35,v35
v_or_b32 v30,v52,v54
v_mfma_f32_16x16x32_bf16 v[56:59],a[196:199],a[68:71],v[56:59]
ds_read_u16_d16_hi v62,v62
v_cvt_pk_bf16_f32 v31,v53,v55
v_mfma_f32_16x16x32_bf16 v[56:59],a[200:203],a[84:87],v[56:59]
v_lshlrev_b32_sdwa v61,1,v61 dst_sel:DWORD dst_unused:UNUSED_PAD src0_sel:DWORD src1_sel:WORD_1
v_mfma_f32_16x16x32_bf16 v[52:55],a[212:215],a[84:87],v[20:23]
ds_read_u16 v61,v61
v_lshlrev_b32_sdwa v63,1,v63 dst_sel:DWORD dst_unused:UNUSED_PAD src0_sel:DWORD src1_sel:WORD_1
v_mfma_f32_16x16x32_bf16 v[52:55],a[208:211],a[68:71],v[52:55]
ds_read_u16_d16_hi v63,v63
v_mfma_f32_16x16x32_bf16 v[52:55],a[204:207],a[52:55],v[52:55]
ds_read_b64_tr_b16 a[244:245],v3 offset:6160
v_lshlrev_b32_sdwa v48,1,v48 dst_sel:DWORD dst_unused:UNUSED_PAD src0_sel:DWORD src1_sel:WORD_1
v_mfma_f32_16x16x32_bf16 v[136:139],a[240:243],v[24:27],v[136:139]
ds_read_u16 v48,v48
v_lshlrev_b32_sdwa v50,1,v50 dst_sel:DWORD dst_unused:UNUSED_PAD src0_sel:DWORD src1_sel:WORD_1
v_mfma_f32_16x16x32_bf16 v[140:143],a[240:243],v[28:31],v[140:143]
s_add_u32 m0,s57,31832
ds_read_u16_d16_hi v50,v50
v_mfma_f32_16x16x32_bf16 v[68:71],a[224:227],v[28:31],v[68:71]
s_add_u32 s89,s56,5799936
ds_read_b64_tr_b16 a[246:247],v3 offset:7696
v_lshlrev_b32_sdwa v49,1,v49 dst_sel:DWORD dst_unused:UNUSED_PAD src0_sel:DWORD src1_sel:WORD_1
v_mfma_f32_16x16x32_bf16 v[64:67],a[224:227],v[24:27],v[64:67]
buffer_load_dwordx4 v6,s[36:39],s89 offen lds
v_mfma_f32_16x16x32_bf16 v[144:147],a[248:251],v[24:27],v[144:147]
ds_read_u16 v49,v49
v_lshlrev_b32_sdwa v51,1,v51 dst_sel:DWORD dst_unused:UNUSED_PAD src0_sel:DWORD src1_sel:WORD_1
v_mfma_f32_16x16x32_bf16 v[148:151],a[248:251],v[28:31],v[148:151]
ds_read_u16_d16_hi v51,v51
v_mfma_f32_16x16x32_bf16 v[84:87],a[228:231],v[28:31],v[84:87]
ds_read_b64_tr_b16 a[252:253],v3 offset:6168
v_lshlrev_b32_sdwa v44,1,v44 dst_sel:DWORD dst_unused:UNUSED_PAD src0_sel:DWORD src1_sel:WORD_1
v_mfma_f32_16x16x32_bf16 v[80:83],a[228:231],v[24:27],v[80:83]
ds_read_u16 v44,v44
v_mfma_f32_16x16x32_bf16 v[152:155],a[232:235],v[24:27],v[152:155]
ds_read_b64_tr_b16 a[254:255],v3 offset:7704
v_lshlrev_b32_sdwa v46,1,v46 dst_sel:DWORD dst_unused:UNUSED_PAD src0_sel:DWORD src1_sel:WORD_1
v_mfma_f32_16x16x32_bf16 v[156:159],a[232:235],v[28:31],v[156:159]
ds_read_u16_d16_hi v46,v46
v_mfma_f32_16x16x32_bf16 v[100:103],a[236:239],v[28:31],v[100:103]
v_lshlrev_b32_sdwa v45,1,v45 dst_sel:DWORD dst_unused:UNUSED_PAD src0_sel:DWORD src1_sel:WORD_1
v_mfma_f32_16x16x32_bf16 v[96:99],a[236:239],v[24:27],v[96:99]
ds_read_u16 v45,v45
v_mfma_f32_16x16x32_bf16 v[28:31],a[204:207],a[56:59],v[20:23]
ds_read_b64_tr_b16 a[216:217],v1 offset:6160
v_lshlrev_b32_sdwa v47,1,v47 dst_sel:DWORD dst_unused:UNUSED_PAD src0_sel:DWORD src1_sel:WORD_1
v_mfma_f32_16x16x32_bf16 v[28:31],a[208:211],a[72:75],v[28:31]
s_waitcnt lgkmcnt(7)
ds_read_u16_d16_hi v47,v47
v_lshl_add_u64 v[40:41],v[40:41],0,v[42:43]
v_fma_mixlo_f16 v8,v57,s99,0
v_mfma_f32_16x16x32_bf16 v[28:31],a[212:215],a[88:91],v[28:31]
TP5943_%=:
s_cbranch_execz TR0_%=
ds_read_b64_tr_b16 a[218:219],v1 offset:7184
v_lshl_add_u64 v[42:43],v[36:37],0,v[38:39]
v_lshlrev_b32_sdwa v52,1,v52 dst_sel:DWORD dst_unused:UNUSED_PAD src0_sel:DWORD src1_sel:WORD_1
v_mfma_f32_16x16x32_bf16 v[24:27],a[200:203],a[88:91],v[16:19]
s_nop 0
ds_read_u16 v52,v52
v_fma_mixhi_f16 v8,v59,s99,0
v_mfma_f32_16x16x32_bf16 v[24:27],a[196:199],a[72:75],v[24:27]
ds_read_b64_tr_b16 a[220:221],v1 offset:8208
v_lshl_add_u64 v[36:37],v[32:33],0,v[34:35]
v_pk_fma_f16 v9,v8,v8,s54
v_lshlrev_b32_sdwa v54,1,v54 dst_sel:DWORD dst_unused:UNUSED_PAD src0_sel:DWORD src1_sel:WORD_1
v_mfma_f32_16x16x32_bf16 v[24:27],a[192:195],a[56:59],v[24:27]
ds_read_u16_d16_hi v54,v54
v_pk_mul_f16 v9,v8,v9
v_mfma_f32_16x16x32_bf16 v[32:35],a[192:195],a[60:63],v[16:19]
ds_read_b64_tr_b16 a[222:223],v1 offset:9232
v_lshlrev_b32_sdwa v53,1,v53 dst_sel:DWORD dst_unused:UNUSED_PAD src0_sel:DWORD src1_sel:WORD_1
v_mfma_f32_16x16x32_bf16 v[32:35],a[196:199],a[76:79],v[32:35]
ds_read_u16 v53,v53
v_lshl_add_u64 v[38:39],v[60:61],0,v[62:63]
v_mfma_f32_16x16x32_bf16 v[32:35],a[200:203],a[92:95],v[32:35]
v_exp_f16 v8,v9
v_lshlrev_b32_sdwa v55,1,v55 dst_sel:DWORD dst_unused:UNUSED_PAD src0_sel:DWORD src1_sel:WORD_1
v_mfma_f32_16x16x32_bf16 v[60:63],a[212:215],a[92:95],v[20:23]
ds_read_u16_d16_hi v55,v55
v_lshlrev_b32_sdwa v56,1,v56 dst_sel:DWORD dst_unused:UNUSED_PAD src0_sel:DWORD src1_sel:WORD_1
v_mfma_f32_16x16x32_bf16 v[60:63],a[208:211],a[76:79],v[60:63]
ds_read_u16 v56,v56
v_lshlrev_b32_sdwa v58,1,v58 dst_sel:DWORD dst_unused:UNUSED_PAD src0_sel:DWORD src1_sel:WORD_1
v_mfma_f32_16x16x32_bf16 v[60:63],a[204:207],a[60:63],v[60:63]
v_exp_f16_sdwa v8,v9 dst_sel:WORD_1 dst_unused:UNUSED_PRESERVE src0_sel:WORD_1
v_mfma_f32_16x16x32_bf16 v[112:115],a[240:243],v[40:43],v[112:115]
ds_read_u16_d16_hi v58,v58
v_pk_fma_f16 v8,v8,s55,s55
v_mfma_f32_16x16x32_bf16 v[116:119],a[240:243],v[36:39],v[116:119]
ds_read_b64_tr_b16 a[240:241],v3 offset:6224
v_lshlrev_b32_sdwa v28,1,v28 dst_sel:DWORD dst_unused:UNUSED_PAD src0_sel:DWORD src1_sel:WORD_1
v_mfma_f32_16x16x32_bf16 v[76:79],a[224:227],v[36:39],v[76:79]
ds_read_u16 v28,v28
v_lshlrev_b32_sdwa v30,1,v30 dst_sel:DWORD dst_unused:UNUSED_PAD src0_sel:DWORD src1_sel:WORD_1
v_mfma_f32_16x16x32_bf16 v[72:75],a[224:227],v[40:43],v[72:75]
ds_read_u16_d16_hi v30,v30
v_rcp_f16 v9,v8
v_mfma_f32_16x16x32_bf16 v[120:123],a[248:251],v[40:43],v[120:123]
ds_read_b64_tr_b16 a[242:243],v3 offset:7760
v_lshlrev_b32_sdwa v29,1,v29 dst_sel:DWORD dst_unused:UNUSED_PAD src0_sel:DWORD src1_sel:WORD_1
v_mfma_f32_16x16x32_bf16 v[124:127],a[248:251],v[36:39],v[124:127]
ds_read_u16 v29,v29
ds_read_b64_tr_b16 a[224:225],v3 offset:6232
v_mfma_f32_16x16x32_bf16 v[92:95],a[228:231],v[36:39],v[92:95]
ds_read_b64_tr_b16 a[226:227],v3 offset:7768
v_lshlrev_b32_sdwa v31,1,v31 dst_sel:DWORD dst_unused:UNUSED_PAD src0_sel:DWORD src1_sel:WORD_1
v_mfma_f32_16x16x32_bf16 v[88:91],a[228:231],v[40:43],v[88:91]
ds_read_u16_d16_hi v31,v31
v_rcp_f16_sdwa v9,v8 dst_sel:WORD_1 dst_unused:UNUSED_PRESERVE src0_sel:WORD_1
v_mfma_f32_16x16x32_bf16 v[128:131],a[232:235],v[40:43],v[128:131]
ds_read_b64_tr_b16 a[248:249],v3 offset:6288
v_fma_mix_f32 v57,v57,v9,0 op_sel_hi:[0,1,0]
v_mfma_f32_16x16x32_bf16 v[132:135],a[232:235],v[36:39],v[132:135]
ds_read_b64_tr_b16 a[250:251],v3 offset:7824
v_lshlrev_b32_sdwa v24,1,v24 dst_sel:DWORD dst_unused:UNUSED_PAD src0_sel:DWORD src1_sel:WORD_1
v_mfma_f32_16x16x32_bf16 v[108:111],a[236:239],v[36:39],v[108:111]
ds_read_u16 v24,v24
v_fma_mix_f32 v59,v59,v9,0 op_sel:[0,1,0] op_sel_hi:[0,1,0]
v_mfma_f32_16x16x32_bf16 v[104:107],a[236:239],v[40:43],v[104:107]
ds_read_b64_tr_b16 a[228:229],v3 offset:6296
v_lshlrev_b32_sdwa v26,1,v26 dst_sel:DWORD dst_unused:UNUSED_PAD src0_sel:DWORD src1_sel:WORD_1
v_mfma_f32_16x16x32_bf16 v[36:39],a[204:207],a[0:3],v[20:23]
s_add_u32 m0,s57,35360
ds_read_u16_d16_hi v26,v26
v_lshlrev_b32_sdwa v25,1,v25 dst_sel:DWORD dst_unused:UNUSED_PAD src0_sel:DWORD src1_sel:WORD_1
v_mfma_f32_16x16x32_bf16 v[36:39],a[208:211],a[16:19],v[36:39]
s_waitcnt lgkmcnt(9)
ds_read_b64_tr_b16 a[230:231],v3 offset:7832
v_lshl_add_u64 v[48:49],v[48:49],0,v[50:51]
v_mfma_f32_16x16x32_bf16 v[36:39],a[212:215],a[32:35],v[36:39]
s_add_u32 s89,s56,5898240
ds_read_u16 v25,v25
v_lshl_add_u64 v[50:51],v[44:45],0,v[46:47]
v_lshlrev_b32_sdwa v27,1,v27 dst_sel:DWORD dst_unused:UNUSED_PAD src0_sel:DWORD src1_sel:WORD_1
v_mfma_f32_16x16x32_bf16 v[40:43],a[200:203],a[32:35],v[16:19]
buffer_load_dwordx4 v6,s[36:39],s89 offen lds
v_lshlrev_b32_sdwa v60,1,v60 dst_sel:DWORD dst_unused:UNUSED_PAD src0_sel:DWORD src1_sel:WORD_1
v_mfma_f32_16x16x32_bf16 v[40:43],a[196:199],a[16:19],v[40:43]
ds_read_u16_d16_hi v27,v27
v_lshl_add_u64 v[44:45],v[52:53],0,v[54:55]
v_mfma_f32_16x16x32_bf16 v[40:43],a[192:195],a[0:3],v[40:43]
ds_read_u16 v60,v60
v_lshlrev_b32_sdwa v62,1,v62 dst_sel:DWORD dst_unused:UNUSED_PAD src0_sel:DWORD src1_sel:WORD_1
v_mfma_f32_16x16x32_bf16 v[52:55],a[192:195],a[4:7],v[16:19]
ds_read_u16_d16_hi v62,v62
v_or_b32 v46,v56,v58
v_mfma_f32_16x16x32_bf16 v[52:55],a[196:199],a[20:23],v[52:55]
v_cvt_pk_bf16_f32 v47,v57,v59
v_lshlrev_b32_sdwa v61,1,v61 dst_sel:DWORD dst_unused:UNUSED_PAD src0_sel:DWORD src1_sel:WORD_1
v_mfma_f32_16x16x32_bf16 v[52:55],a[200:203],a[36:39],v[52:55]
ds_read_u16 v61,v61
v_lshlrev_b32_sdwa v63,1,v63 dst_sel:DWORD dst_unused:UNUSED_PAD src0_sel:DWORD src1_sel:WORD_1
v_mfma_f32_16x16x32_bf16 v[56:59],a[212:215],a[36:39],v[20:23]
ds_read_u16_d16_hi v63,v63
v_lshlrev_b32_sdwa v32,1,v32 dst_sel:DWORD dst_unused:UNUSED_PAD src0_sel:DWORD src1_sel:WORD_1
v_mfma_f32_16x16x32_bf16 v[56:59],a[208:211],a[20:23],v[56:59]
ds_read_u16 v32,v32
v_mfma_f32_16x16x32_bf16 v[56:59],a[204:207],a[4:7],v[56:59]
v_lshlrev_b32_sdwa v34,1,v34 dst_sel:DWORD dst_unused:UNUSED_PAD src0_sel:DWORD src1_sel:WORD_1
v_mfma_f32_16x16x32_bf16 v[232:235],a[244:247],v[48:51],v[232:235]
ds_read_u16_d16_hi v34,v34
v_lshlrev_b32_sdwa v33,1,v33 dst_sel:DWORD dst_unused:UNUSED_PAD src0_sel:DWORD src1_sel:WORD_1
v_mfma_f32_16x16x32_bf16 v[236:239],a[244:247],v[44:47],v[236:239]
ds_read_u16 v33,v33
v_mfma_f32_16x16x32_bf16 v[164:167],a[252:255],v[44:47],v[164:167]
v_lshlrev_b32_sdwa v35,1,v35 dst_sel:DWORD dst_unused:UNUSED_PAD src0_sel:DWORD src1_sel:WORD_1
v_mfma_f32_16x16x32_bf16 v[160:163],a[252:255],v[48:51],v[160:163]
ds_read_u16_d16_hi v35,v35
v_mfma_f32_16x16x32_bf16 v[240:243],a[240:243],v[48:51],v[240:243]
v_lshlrev_b32_sdwa v36,1,v36 dst_sel:DWORD dst_unused:UNUSED_PAD src0_sel:DWORD src1_sel:WORD_1
v_mfma_f32_16x16x32_bf16 v[244:247],a[240:243],v[44:47],v[244:247]
s_waitcnt lgkmcnt(5)
ds_read_u16 v36,v36
v_mfma_f32_16x16x32_bf16 v[180:183],a[224:227],v[44:47],v[180:183]
v_lshlrev_b32_sdwa v38,1,v38 dst_sel:DWORD dst_unused:UNUSED_PAD src0_sel:DWORD src1_sel:WORD_1
v_mfma_f32_16x16x32_bf16 v[176:179],a[224:227],v[48:51],v[176:179]
ds_read_u16_d16_hi v38,v38
v_mfma_f32_16x16x32_bf16 v[248:251],a[248:251],v[48:51],v[248:251]
v_lshlrev_b32_sdwa v37,1,v37 dst_sel:DWORD dst_unused:UNUSED_PAD src0_sel:DWORD src1_sel:WORD_1
v_mfma_f32_16x16x32_bf16 v[252:255],a[248:251],v[44:47],v[252:255]
ds_read_u16 v37,v37
v_mfma_f32_16x16x32_bf16 v[196:199],a[228:231],v[44:47],v[196:199]
v_lshlrev_b32_sdwa v39,1,v39 dst_sel:DWORD dst_unused:UNUSED_PAD src0_sel:DWORD src1_sel:WORD_1
v_mfma_f32_16x16x32_bf16 v[192:195],a[228:231],v[48:51],v[192:195]
ds_read_u16_d16_hi v39,v39
v_mfma_f32_16x16x32_bf16 v[44:47],a[204:207],a[8:11],v[20:23]
v_lshlrev_b32_sdwa v40,1,v40 dst_sel:DWORD dst_unused:UNUSED_PAD src0_sel:DWORD src1_sel:WORD_1
v_fma_mixlo_f16 v8,v53,s99,0
v_mfma_f32_16x16x32_bf16 v[44:47],a[208:211],a[24:27],v[44:47]
ds_read_u16 v40,v40
v_lshl_add_u64 v[28:29],v[28:29],0,v[30:31]
v_mfma_f32_16x16x32_bf16 v[44:47],a[212:215],a[40:43],v[44:47]
v_lshl_add_u64 v[30:31],v[24:25],0,v[26:27]
v_lshlrev_b32_sdwa v42,1,v42 dst_sel:DWORD dst_unused:UNUSED_PAD src0_sel:DWORD src1_sel:WORD_1
v_mfma_f32_16x16x32_bf16 v[48:51],a[200:203],a[40:43],v[16:19]
ds_read_u16_d16_hi v42,v42
v_fma_mixhi_f16 v8,v55,s99,0
v_mfma_f32_16x16x32_bf16 v[48:51],a[196:199],a[24:27],v[48:51]
s_waitcnt lgkmcnt(4)
v_lshl_add_u64 v[24:25],v[60:61],0,v[62:63]
v_lshlrev_b32_sdwa v41,1,v41 dst_sel:DWORD dst_unused:UNUSED_PAD src0_sel:DWORD src1_sel:WORD_1
v_pk_fma_f16 v9,v8,v8,s54
v_mfma_f32_16x16x32_bf16 v[48:51],a[192:195],a[8:11],v[48:51]
ds_read_u16 v41,v41
v_pk_mul_f16 v9,v8,v9
v_mfma_f32_16x16x32_bf16 v[60:63],a[192:195],a[12:15],v[16:19]
ds_read_b128 v[16:19],v4 offset:256
v_lshlrev_b32_sdwa v43,1,v43 dst_sel:DWORD dst_unused:UNUSED_PAD src0_sel:DWORD src1_sel:WORD_1
v_mfma_f32_16x16x32_bf16 v[60:63],a[196:199],a[28:31],v[60:63]
ds_read_b64_tr_b16 a[192:193],v1 offset:10256
ds_read_b64_tr_b16 a[194:195],v1 offset:11280
v_lshl_add_u64 v[26:27],v[32:33],0,v[34:35]
v_mfma_f32_16x16x32_bf16 v[60:63],a[200:203],a[44:47],v[60:63]
ds_read_u16_d16_hi v43,v43
v_exp_f16_e64 v8,v9
v_lshlrev_b32_sdwa v56,1,v56 dst_sel:DWORD dst_unused:UNUSED_PAD src0_sel:DWORD src1_sel:WORD_1
v_mfma_f32_16x16x32_bf16 v[32:35],a[212:215],a[44:47],v[20:23]
ds_read_b128 v[20:23],v4 offset:288
ds_read_b64_tr_b16 a[196:197],v2 offset:10256
v_mfma_f32_16x16x32_bf16 v[32:35],a[208:211],a[28:31],v[32:35]
ds_read_b64_tr_b16 a[198:199],v2 offset:11280
ds_read_b64_tr_b16 a[200:201],v2 offset:8208
v_lshlrev_b32_sdwa v58,1,v58 dst_sel:DWORD dst_unused:UNUSED_PAD src0_sel:DWORD src1_sel:WORD_1
v_mfma_f32_16x16x32_bf16 v[32:35],a[204:207],a[12:15],v[32:35]
ds_read_b64_tr_b16 a[202:203],v2 offset:9232
v_exp_f16_sdwa v8,v9 dst_sel:WORD_1 dst_unused:UNUSED_PRESERVE src0_sel:WORD_1
v_mfma_f32_16x16x32_bf16 v[208:211],a[244:247],v[28:31],v[208:211]
ds_read_u16 v56,v56
v_lshlrev_b32_sdwa v57,1,v57 dst_sel:DWORD dst_unused:UNUSED_PAD src0_sel:DWORD src1_sel:WORD_1
v_mfma_f32_16x16x32_bf16 v[212:215],a[244:247],v[24:27],v[212:215]
ds_read_u16_d16_hi v58,v58
v_lshlrev_b32_sdwa v59,1,v59 dst_sel:DWORD dst_unused:UNUSED_PAD src0_sel:DWORD src1_sel:WORD_1
v_mfma_f32_16x16x32_bf16 v[172:175],a[252:255],v[24:27],v[172:175]
ds_read_u16 v57,v57
v_pk_fma_f16 v8,v8,s55,s55
v_mfma_f32_16x16x32_bf16 v[168:171],a[252:255],v[28:31],v[168:171]
v_rcp_f16 v9,v8
v_lshlrev_b32_sdwa v52,1,v52 dst_sel:DWORD dst_unused:UNUSED_PAD src0_sel:DWORD src1_sel:WORD_1
v_mfma_f32_16x16x32_bf16 v[216:219],a[240:243],v[28:31],v[216:219]
ds_read_u16_d16_hi v59,v59
v_lshlrev_b32_sdwa v54,1,v54 dst_sel:DWORD dst_unused:UNUSED_PAD src0_sel:DWORD src1_sel:WORD_1
v_mfma_f32_16x16x32_bf16 v[220:223],a[240:243],v[24:27],v[220:223]
ds_read_b64_tr_b16 a[212:213],v2 offset:6160
v_lshlrev_b32_sdwa v44,1,v44 dst_sel:DWORD dst_unused:UNUSED_PAD src0_sel:DWORD src1_sel:WORD_1
v_mfma_f32_16x16x32_bf16 v[188:191],a[224:227],v[24:27],v[188:191]
ds_read_b64_tr_b16 a[214:215],v2 offset:7184
v_lshlrev_b32_sdwa v46,1,v46 dst_sel:DWORD dst_unused:UNUSED_PAD src0_sel:DWORD src1_sel:WORD_1
v_mfma_f32_16x16x32_bf16 v[184:187],a[224:227],v[28:31],v[184:187]
ds_read_u16 v52,v52
v_rcp_f16_sdwa v9,v8 dst_sel:WORD_1 dst_unused:UNUSED_PRESERVE src0_sel:WORD_1
v_mfma_f32_16x16x32_bf16 v[224:227],a[248:251],v[28:31],v[224:227]
ds_read_u16_d16_hi v54,v54
v_fma_mix_f32 v53,v53,v9,0 op_sel_hi:[0,1,0]
v_mfma_f32_16x16x32_bf16 v[228:231],a[248:251],v[24:27],v[228:231]
ds_read_u16 v44,v44
v_lshlrev_b32_sdwa v45,1,v45 dst_sel:DWORD dst_unused:UNUSED_PAD src0_sel:DWORD src1_sel:WORD_1
v_mfma_f32_16x16x32_bf16 v[204:207],a[228:231],v[24:27],v[204:207]
ds_read_u16_d16_hi v46,v46
v_fma_mix_f32 v55,v55,v9,0 op_sel:[0,1,0] op_sel_hi:[0,1,0]
v_mfma_f32_16x16x32_bf16 v[200:203],a[228:231],v[28:31],v[200:203]
s_waitcnt lgkmcnt(7)
ds_read_u16 v45,v45
v_lshlrev_b32_sdwa v47,1,v47 dst_sel:DWORD dst_unused:UNUSED_PAD src0_sel:DWORD src1_sel:WORD_1
v_mfma_f32_16x16x32_bf16 v[24:27],a[216:219],a[48:51],v[16:19]
s_add_u32 m0,s57,38896
ds_read_u16_d16_hi v47,v47
v_lshlrev_b32_sdwa v48,1,v48 dst_sel:DWORD dst_unused:UNUSED_PAD src0_sel:DWORD src1_sel:WORD_1
v_mfma_f32_16x16x32_bf16 v[24:27],a[220:223],a[64:67],v[24:27]
s_add_u32 s89,s56,5996544
ds_read_u16 v48,v48
v_lshl_add_u64 v[36:37],v[36:37],0,v[38:39]
v_mfma_f32_16x16x32_bf16 v[24:27],a[192:195],a[80:83],v[24:27]
buffer_load_dwordx4 v6,s[36:39],s89 offen lds
v_lshl_add_u64 v[38:39],v[40:41],0,v[42:43]
v_lshlrev_b32_sdwa v50,1,v50 dst_sel:DWORD dst_unused:UNUSED_PAD src0_sel:DWORD src1_sel:WORD_1
v_mfma_f32_16x16x32_bf16 v[28:31],a[196:199],a[80:83],v[20:23]
ds_read_u16_d16_hi v50,v50
v_lshlrev_b32_sdwa v49,1,v49 dst_sel:DWORD dst_unused:UNUSED_PAD src0_sel:DWORD src1_sel:WORD_1
v_mfma_f32_16x16x32_bf16 v[28:31],a[200:203],a[64:67],v[28:31]
s_waitcnt lgkmcnt(7)
ds_read_u16 v49,v49
v_lshl_add_u64 v[40:41],v[56:57],0,v[58:59]
v_mfma_f32_16x16x32_bf16 v[28:31],a[212:215],a[48:51],v[28:31]
s_add_u32 m0,s57,42440
v_lshlrev_b32_sdwa v51,1,v51 dst_sel:DWORD dst_unused:UNUSED_PAD src0_sel:DWORD src1_sel:WORD_1
v_lshlrev_b32_sdwa v32,1,v32 dst_sel:DWORD dst_unused:UNUSED_PAD src0_sel:DWORD src1_sel:WORD_1
v_mfma_f32_16x16x32_bf16 v[56:59],a[212:215],a[52:55],v[20:23]
s_waitcnt lgkmcnt(6)
ds_read_u16_d16_hi v51,v51
v_or_b32 v42,v52,v54
v_mfma_f32_16x16x32_bf16 v[56:59],a[200:203],a[68:71],v[56:59]
s_waitcnt vmcnt(13)
ds_read_u16 v32,v32
v_cvt_pk_bf16_f32 v43,v53,v55
v_mfma_f32_16x16x32_bf16 v[56:59],a[196:199],a[84:87],v[56:59]
s_barrier
v_lshlrev_b32_sdwa v34,1,v34 dst_sel:DWORD dst_unused:UNUSED_PAD src0_sel:DWORD src1_sel:WORD_1
v_lshlrev_b32_sdwa v33,1,v33 dst_sel:DWORD dst_unused:UNUSED_PAD src0_sel:DWORD src1_sel:WORD_1
v_mfma_f32_16x16x32_bf16 v[52:55],a[192:195],a[84:87],v[16:19]
s_add_u32 s89,s56,6094848
ds_read_u16_d16_hi v34,v34
v_lshlrev_b32_sdwa v35,1,v35 dst_sel:DWORD dst_unused:UNUSED_PAD src0_sel:DWORD src1_sel:WORD_1
v_mfma_f32_16x16x32_bf16 v[52:55],a[220:223],a[68:71],v[52:55]
buffer_load_dwordx4 v6,s[36:39],s89 offen lds
v_mfma_f32_16x16x32_bf16 v[52:55],a[216:219],a[52:55],v[52:55]
s_add_u32 m0,s58,0
ds_read_u16 v33,v33
v_lshlrev_b32_sdwa v60,1,v60 dst_sel:DWORD dst_unused:UNUSED_PAD src0_sel:DWORD src1_sel:WORD_1
v_mfma_f32_16x16x32_bf16 v[136:139],a[244:247],v[36:39],v[136:139]
s_add_u32 s69,s69,s59
ds_read_u16_d16_hi v35,v35
v_lshlrev_b32_sdwa v62,1,v62 dst_sel:DWORD dst_unused:UNUSED_PAD src0_sel:DWORD src1_sel:WORD_1
v_mfma_f32_16x16x32_bf16 v[140:143],a[244:247],v[40:43],v[140:143]
buffer_load_dwordx4 v5,s[44:47],s69 offen offset:0 lds
ds_read_u16 v60,v60
v_mfma_f32_16x16x32_bf16 v[68:71],a[252:255],v[40:43],v[68:71]
ds_read_u16_d16_hi v62,v62
ds_read_b64_tr_b16 a[232:233],v3 offset:12320
v_mfma_f32_16x16x32_bf16 v[64:67],a[252:255],v[36:39],v[64:67]
ds_read_b64_tr_b16 a[234:235],v3 offset:13856
v_lshlrev_b32_sdwa v61,1,v61 dst_sel:DWORD dst_unused:UNUSED_PAD src0_sel:DWORD src1_sel:WORD_1
v_mfma_f32_16x16x32_bf16 v[144:147],a[240:243],v[36:39],v[144:147]
s_add_u32 s69,s69,s53
ds_read_u16 v61,v61
v_lshlrev_b32_sdwa v63,1,v63 dst_sel:DWORD dst_unused:UNUSED_PAD src0_sel:DWORD src1_sel:WORD_1
v_mfma_f32_16x16x32_bf16 v[148:151],a[240:243],v[40:43],v[148:151]
buffer_load_dwordx4 v5,s[44:47],s69 offen offset:1024 lds
v_lshlrev_b32_sdwa v24,1,v24 dst_sel:DWORD dst_unused:UNUSED_PAD src0_sel:DWORD src1_sel:WORD_1
v_mfma_f32_16x16x32_bf16 v[84:87],a[224:227],v[40:43],v[84:87]
ds_read_u16_d16_hi v63,v63
v_mfma_f32_16x16x32_bf16 v[80:83],a[224:227],v[36:39],v[80:83]
ds_read_u16 v24,v24
v_lshlrev_b32_sdwa v26,1,v26 dst_sel:DWORD dst_unused:UNUSED_PAD src0_sel:DWORD src1_sel:WORD_1
v_mfma_f32_16x16x32_bf16 v[152:155],a[248:251],v[36:39],v[152:155]
ds_read_u16_d16_hi v26,v26
v_lshlrev_b32_sdwa v25,1,v25 dst_sel:DWORD dst_unused:UNUSED_PAD src0_sel:DWORD src1_sel:WORD_1
v_mfma_f32_16x16x32_bf16 v[156:159],a[248:251],v[40:43],v[156:159]
ds_read_u16 v25,v25
v_mfma_f32_16x16x32_bf16 v[100:103],a[228:231],v[40:43],v[100:103]
s_add_u32 s69,s69,s53
ds_read_b64_tr_b16 a[236:237],v3 offset:12328
v_lshlrev_b32_sdwa v27,1,v27 dst_sel:DWORD dst_unused:UNUSED_PAD src0_sel:DWORD src1_sel:WORD_1
v_mfma_f32_16x16x32_bf16 v[96:99],a[228:231],v[36:39],v[96:99]
buffer_load_dwordx4 v5,s[44:47],s69 offen offset:2048 lds
v_mfma_f32_16x16x32_bf16 v[40:43],a[216:219],a[56:59],v[16:19]
ds_read_u16_d16_hi v27,v27
v_lshlrev_b32_sdwa v28,1,v28 dst_sel:DWORD dst_unused:UNUSED_PAD src0_sel:DWORD src1_sel:WORD_1
v_mfma_f32_16x16x32_bf16 v[40:43],a[220:223],a[72:75],v[40:43]
s_waitcnt lgkmcnt(6)
ds_read_u16 v28,v28
v_lshl_add_u64 v[44:45],v[44:45],0,v[46:47]
v_fma_mixlo_f16 v8,v57,s99,0
v_mfma_f32_16x16x32_bf16 v[40:43],a[192:195],a[88:91],v[40:43]
ds_read_b64_tr_b16 a[238:239],v3 offset:13864
v_lshl_add_u64 v[46:47],v[48:49],0,v[50:51]
v_lshlrev_b32_sdwa v30,1,v30 dst_sel:DWORD dst_unused:UNUSED_PAD src0_sel:DWORD src1_sel:WORD_1
v_mfma_f32_16x16x32_bf16 v[36:39],a[196:199],a[88:91],v[20:23]
ds_read_u16_d16_hi v30,v30
v_fma_mixhi_f16 v8,v59,s99,0
v_mfma_f32_16x16x32_bf16 v[36:39],a[200:203],a[72:75],v[36:39]
ds_read_b64_tr_b16 a[208:209],v1 offset:12320
v_lshl_add_u64 v[48:49],v[32:33],0,v[34:35]
v_lshlrev_b32_sdwa v29,1,v29 dst_sel:DWORD dst_unused:UNUSED_PAD src0_sel:DWORD src1_sel:WORD_1
v_pk_fma_f16 v9,v8,v8,s54
v_mfma_f32_16x16x32_bf16 v[36:39],a[212:215],a[56:59],v[36:39]
ds_read_u16 v29,v29
v_pk_mul_f16 v9,v8,v9
v_mfma_f32_16x16x32_bf16 v[32:35],a[212:215],a[60:63],v[20:23]
ds_read_b64_tr_b16 a[210:211],v1 offset:13344
v_lshlrev_b32_sdwa v31,1,v31 dst_sel:DWORD dst_unused:UNUSED_PAD src0_sel:DWORD src1_sel:WORD_1
v_mfma_f32_16x16x32_bf16 v[32:35],a[200:203],a[76:79],v[32:35]
s_waitcnt lgkmcnt(7)
ds_read_u16_d16_hi v31,v31
v_lshl_add_u64 v[50:51],v[60:61],0,v[62:63]
v_mfma_f32_16x16x32_bf16 v[32:35],a[196:199],a[92:95],v[32:35]
v_exp_f16 v8,v9
v_lshlrev_b32_sdwa v52,1,v52 dst_sel:DWORD dst_unused:UNUSED_PAD src0_sel:DWORD src1_sel:WORD_1
v_mfma_f32_16x16x32_bf16 v[60:63],a[192:195],a[92:95],v[16:19]
ds_read_u16 v52,v52
v_lshlrev_b32_sdwa v54,1,v54 dst_sel:DWORD dst_unused:UNUSED_PAD src0_sel:DWORD src1_sel:WORD_1
v_mfma_f32_16x16x32_bf16 v[60:63],a[220:223],a[76:79],v[60:63]
ds_read_u16_d16_hi v54,v54
v_lshlrev_b32_sdwa v53,1,v53 dst_sel:DWORD dst_unused:UNUSED_PAD src0_sel:DWORD src1_sel:WORD_1
v_mfma_f32_16x16x32_bf16 v[60:63],a[216:219],a[60:63],v[60:63]
v_exp_f16_sdwa v8,v9 dst_sel:WORD_1 dst_unused:UNUSED_PRESERVE src0_sel:WORD_1
v_mfma_f32_16x16x32_bf16 v[112:115],a[244:247],v[44:47],v[112:115]
ds_read_u16 v53,v53
v_lshlrev_b32_sdwa v55,1,v55 dst_sel:DWORD dst_unused:UNUSED_PAD src0_sel:DWORD src1_sel:WORD_1
v_mfma_f32_16x16x32_bf16 v[116:119],a[244:247],v[48:51],v[116:119]
ds_read_u16_d16_hi v55,v55
v_pk_fma_f16 v8,v8,s55,s55
v_mfma_f32_16x16x32_bf16 v[76:79],a[252:255],v[48:51],v[76:79]
ds_read_b64_tr_b16 a[244:245],v3 offset:12384
v_lshlrev_b32_sdwa v56,1,v56 dst_sel:DWORD dst_unused:UNUSED_PAD src0_sel:DWORD src1_sel:WORD_1
v_mfma_f32_16x16x32_bf16 v[72:75],a[252:255],v[44:47],v[72:75]
ds_read_u16 v56,v56
v_rcp_f16 v9,v8
v_mfma_f32_16x16x32_bf16 v[120:123],a[240:243],v[44:47],v[120:123]
ds_read_b64_tr_b16 a[246:247],v3 offset:13920
v_lshlrev_b32_sdwa v58,1,v58 dst_sel:DWORD dst_unused:UNUSED_PAD src0_sel:DWORD src1_sel:WORD_1
v_mfma_f32_16x16x32_bf16 v[124:127],a[240:243],v[48:51],v[124:127]
ds_read_u16_d16_hi v58,v58
ds_read_b64_tr_b16 a[252:253],v3 offset:12392
v_mfma_f32_16x16x32_bf16 v[92:95],a[224:227],v[48:51],v[92:95]
ds_read_b64_tr_b16 a[254:255],v3 offset:13928
v_lshlrev_b32_sdwa v40,1,v40 dst_sel:DWORD dst_unused:UNUSED_PAD src0_sel:DWORD src1_sel:WORD_1
v_mfma_f32_16x16x32_bf16 v[88:91],a[224:227],v[44:47],v[88:91]
ds_read_u16 v40,v40
v_rcp_f16_sdwa v9,v8 dst_sel:WORD_1 dst_unused:UNUSED_PRESERVE src0_sel:WORD_1
v_mfma_f32_16x16x32_bf16 v[128:131],a[248:251],v[44:47],v[128:131]
ds_read_b64_tr_b16 a[240:241],v3 offset:12448
v_lshlrev_b32_sdwa v42,1,v42 dst_sel:DWORD dst_unused:UNUSED_PAD src0_sel:DWORD src1_sel:WORD_1
v_mfma_f32_16x16x32_bf16 v[132:135],a[248:251],v[48:51],v[132:135]
ds_read_u16_d16_hi v42,v42
v_fma_mix_f32 v57,v57,v9,0 op_sel_hi:[0,1,0]
v_mfma_f32_16x16x32_bf16 v[108:111],a[228:231],v[48:51],v[108:111]
ds_read_b64_tr_b16 a[242:243],v3 offset:13984
v_lshlrev_b32_sdwa v41,1,v41 dst_sel:DWORD dst_unused:UNUSED_PAD src0_sel:DWORD src1_sel:WORD_1
v_mfma_f32_16x16x32_bf16 v[104:107],a[228:231],v[44:47],v[104:107]
ds_read_u16 v41,v41
v_fma_mix_f32 v59,v59,v9,0 op_sel:[0,1,0] op_sel_hi:[0,1,0]
v_lshlrev_b32_sdwa v43,1,v43 dst_sel:DWORD dst_unused:UNUSED_PAD src0_sel:DWORD src1_sel:WORD_1
v_mfma_f32_16x16x32_bf16 v[48:51],a[216:219],a[0:3],v[16:19]
s_add_u32 m0,s58,6160
ds_read_u16_d16_hi v43,v43
ds_read_b64_tr_b16 a[224:225],v3 offset:12456
v_lshlrev_b32_sdwa v36,1,v36 dst_sel:DWORD dst_unused:UNUSED_PAD src0_sel:DWORD src1_sel:WORD_1
v_mfma_f32_16x16x32_bf16 v[48:51],a[220:223],a[16:19],v[48:51]
s_waitcnt lgkmcnt(10)
ds_read_b64_tr_b16 a[226:227],v3 offset:13992
v_lshl_add_u64 v[24:25],v[24:25],0,v[26:27]
v_mfma_f32_16x16x32_bf16 v[48:51],a[192:195],a[32:35],v[48:51]
s_add_u32 s69,s69,s59
ds_read_u16 v36,v36
v_lshl_add_u64 v[26:27],v[28:29],0,v[30:31]
v_lshlrev_b32_sdwa v38,1,v38 dst_sel:DWORD dst_unused:UNUSED_PAD src0_sel:DWORD src1_sel:WORD_1
v_mfma_f32_16x16x32_bf16 v[44:47],a[196:199],a[32:35],v[20:23]
buffer_load_dwordx4 v5,s[44:47],s69 offen offset:0 lds
v_lshlrev_b32_sdwa v37,1,v37 dst_sel:DWORD dst_unused:UNUSED_PAD src0_sel:DWORD src1_sel:WORD_1
v_mfma_f32_16x16x32_bf16 v[44:47],a[200:203],a[16:19],v[44:47]
s_add_u32 s69,s69,s53
ds_read_u16_d16_hi v38,v38
v_lshl_add_u64 v[28:29],v[52:53],0,v[54:55]
v_mfma_f32_16x16x32_bf16 v[44:47],a[212:215],a[0:3],v[44:47]
buffer_load_dwordx4 v5,s[44:47],s69 offen offset:1024 lds
v_lshlrev_b32_sdwa v39,1,v39 dst_sel:DWORD dst_unused:UNUSED_PAD src0_sel:DWORD src1_sel:WORD_1
v_mfma_f32_16x16x32_bf16 v[52:55],a[212:215],a[4:7],v[20:23]
s_waitcnt lgkmcnt(7)
ds_read_u16 v37,v37
v_or_b32 v30,v56,v58
v_mfma_f32_16x16x32_bf16 v[52:55],a[200:203],a[20:23],v[52:55]
s_add_u32 m0,s57,45976
ds_read_u16_d16_hi v39,v39
v_cvt_pk_bf16_f32 v31,v57,v59
v_lshlrev_b32_sdwa v60,1,v60 dst_sel:DWORD dst_unused:UNUSED_PAD src0_sel:DWORD src1_sel:WORD_1
v_mfma_f32_16x16x32_bf16 v[52:55],a[196:199],a[36:39],v[52:55]
s_add_u32 s89,s56,6193152
ds_read_u16 v60,v60
v_lshlrev_b32_sdwa v62,1,v62 dst_sel:DWORD dst_unused:UNUSED_PAD src0_sel:DWORD src1_sel:WORD_1
v_mfma_f32_16x16x32_bf16 v[56:59],a[192:195],a[36:39],v[16:19]
buffer_load_dwordx4 v6,s[36:39],s89 offen lds
v_lshlrev_b32_sdwa v61,1,v61 dst_sel:DWORD dst_unused:UNUSED_PAD src0_sel:DWORD src1_sel:WORD_1
v_mfma_f32_16x16x32_bf16 v[56:59],a[220:223],a[20:23],v[56:59]
s_add_u32 m0,s58,6160
ds_read_u16_d16_hi v62,v62
v_lshlrev_b32_sdwa v63,1,v63 dst_sel:DWORD dst_unused:UNUSED_PAD src0_sel:DWORD src1_sel:WORD_1
v_mfma_f32_16x16x32_bf16 v[56:59],a[216:219],a[4:7],v[56:59]
s_add_u32 s69,s69,s53
ds_read_u16 v61,v61
v_lshlrev_b32_sdwa v32,1,v32 dst_sel:DWORD dst_unused:UNUSED_PAD src0_sel:DWORD src1_sel:WORD_1
v_mfma_f32_16x16x32_bf16 v[232:235],a[232:235],v[24:27],v[232:235]
buffer_load_dwordx4 v5,s[44:47],s69 offen offset:2048 lds
ds_read_u16_d16_hi v63,v63
v_mfma_f32_16x16x32_bf16 v[236:239],a[232:235],v[28:31],v[236:239]
ds_read_u16 v32,v32
v_lshlrev_b32_sdwa v34,1,v34 dst_sel:DWORD dst_unused:UNUSED_PAD src0_sel:DWORD src1_sel:WORD_1
v_mfma_f32_16x16x32_bf16 v[164:167],a[236:239],v[28:31],v[164:167]
ds_read_u16_d16_hi v34,v34
v_lshlrev_b32_sdwa v33,1,v33 dst_sel:DWORD dst_unused:UNUSED_PAD src0_sel:DWORD src1_sel:WORD_1
v_mfma_f32_16x16x32_bf16 v[160:163],a[236:239],v[24:27],v[160:163]
s_add_u32 m0,s57,49504
ds_read_u16 v33,v33
v_mfma_f32_16x16x32_bf16 v[240:243],a[244:247],v[24:27],v[240:243]
s_add_u32 s89,s56,6291456
ds_read_b64_tr_b16 a[204:205],v1 offset:14368
v_lshlrev_b32_sdwa v35,1,v35 dst_sel:DWORD dst_unused:UNUSED_PAD src0_sel:DWORD src1_sel:WORD_1
v_mfma_f32_16x16x32_bf16 v[244:247],a[244:247],v[28:31],v[244:247]
buffer_load_dwordx4 v6,s[36:39],s89 offen lds
v_lshlrev_b32_sdwa v48,1,v48 dst_sel:DWORD dst_unused:UNUSED_PAD src0_sel:DWORD src1_sel:WORD_1
v_mfma_f32_16x16x32_bf16 v[180:183],a[252:255],v[28:31],v[180:183]
ds_read_u16_d16_hi v35,v35
v_mfma_f32_16x16x32_bf16 v[176:179],a[252:255],v[24:27],v[176:179]
s_waitcnt lgkmcnt(7)
ds_read_u16 v48,v48
v_lshlrev_b32_sdwa v50,1,v50 dst_sel:DWORD dst_unused:UNUSED_PAD src0_sel:DWORD src1_sel:WORD_1
v_mfma_f32_16x16x32_bf16 v[248:251],a[240:243],v[24:27],v[248:251]
ds_read_u16_d16_hi v50,v50
v_lshlrev_b32_sdwa v49,1,v49 dst_sel:DWORD dst_unused:UNUSED_PAD src0_sel:DWORD src1_sel:WORD_1
v_mfma_f32_16x16x32_bf16 v[252:255],a[240:243],v[28:31],v[252:255]
ds_read_u16 v49,v49
v_mfma_f32_16x16x32_bf16 v[196:199],a[224:227],v[28:31],v[196:199]
ds_read_b64_tr_b16 a[206:207],v1 offset:15392
v_lshlrev_b32_sdwa v51,1,v51 dst_sel:DWORD dst_unused:UNUSED_PAD src0_sel:DWORD src1_sel:WORD_1
v_mfma_f32_16x16x32_bf16 v[192:195],a[224:227],v[24:27],v[192:195]
ds_read_u16_d16_hi v51,v51
v_mfma_f32_16x16x32_bf16 v[28:31],a[216:219],a[8:11],v[16:19]
v_lshlrev_b32_sdwa v44,1,v44 dst_sel:DWORD dst_unused:UNUSED_PAD src0_sel:DWORD src1_sel:WORD_1
v_fma_mixlo_f16 v8,v53,s99,0
v_mfma_f32_16x16x32_bf16 v[28:31],a[220:223],a[24:27],v[28:31]
ds_read_u16 v44,v44
v_lshl_add_u64 v[40:41],v[40:41],0,v[42:43]
v_mfma_f32_16x16x32_bf16 v[28:31],a[192:195],a[40:43],v[28:31]
v_lshl_add_u64 v[42:43],v[36:37],0,v[38:39]
v_lshlrev_b32_sdwa v46,1,v46 dst_sel:DWORD dst_unused:UNUSED_PAD src0_sel:DWORD src1_sel:WORD_1
v_mfma_f32_16x16x32_bf16 v[24:27],a[196:199],a[40:43],v[20:23]
ds_read_u16_d16_hi v46,v46
v_fma_mixhi_f16 v8,v55,s99,0
v_mfma_f32_16x16x32_bf16 v[24:27],a[200:203],a[24:27],v[24:27]
s_waitcnt lgkmcnt(6)
v_lshl_add_u64 v[36:37],v[60:61],0,v[62:63]
v_lshlrev_b32_sdwa v45,1,v45 dst_sel:DWORD dst_unused:UNUSED_PAD src0_sel:DWORD src1_sel:WORD_1
v_pk_fma_f16 v9,v8,v8,s54
v_mfma_f32_16x16x32_bf16 v[24:27],a[212:215],a[8:11],v[24:27]
ds_read_u16 v45,v45
v_pk_mul_f16 v9,v8,v9
v_mfma_f32_16x16x32_bf16 v[60:63],a[212:215],a[12:15],v[20:23]
ds_read_b128 v[20:23],v4 offset:384
v_lshlrev_b32_sdwa v47,1,v47 dst_sel:DWORD dst_unused:UNUSED_PAD src0_sel:DWORD src1_sel:WORD_1
v_mfma_f32_16x16x32_bf16 v[60:63],a[200:203],a[28:31],v[60:63]
ds_read_b64_tr_b16 a[212:213],v1 offset:16416
ds_read_b64_tr_b16 a[214:215],v1 offset:17440
v_lshl_add_u64 v[38:39],v[32:33],0,v[34:35]
v_mfma_f32_16x16x32_bf16 v[60:63],a[196:199],a[44:47],v[60:63]
ds_read_u16_d16_hi v47,v47
v_exp_f16 v8,v9
v_lshlrev_b32_sdwa v56,1,v56 dst_sel:DWORD dst_unused:UNUSED_PAD src0_sel:DWORD src1_sel:WORD_1
v_mfma_f32_16x16x32_bf16 v[32:35],a[192:195],a[44:47],v[16:19]
ds_read_b128 v[16:19],v4 offset:416
ds_read_b64_tr_b16 a[200:201],v2 offset:16416
v_mfma_f32_16x16x32_bf16 v[32:35],a[220:223],a[28:31],v[32:35]
ds_read_b64_tr_b16 a[202:203],v2 offset:17440
ds_read_b64_tr_b16 a[196:197],v2 offset:14368
v_lshlrev_b32_sdwa v58,1,v58 dst_sel:DWORD dst_unused:UNUSED_PAD src0_sel:DWORD src1_sel:WORD_1
v_mfma_f32_16x16x32_bf16 v[32:35],a[216:219],a[12:15],v[32:35]
ds_read_b64_tr_b16 a[198:199],v2 offset:15392
v_exp_f16_sdwa v8,v9 dst_sel:WORD_1 dst_unused:UNUSED_PRESERVE src0_sel:WORD_1
v_mfma_f32_16x16x32_bf16 v[208:211],a[232:235],v[40:43],v[208:211]
ds_read_u16 v56,v56
v_lshlrev_b32_sdwa v57,1,v57 dst_sel:DWORD dst_unused:UNUSED_PAD src0_sel:DWORD src1_sel:WORD_1
v_mfma_f32_16x16x32_bf16 v[212:215],a[232:235],v[36:39],v[212:215]
ds_read_u16_d16_hi v58,v58
v_lshlrev_b32_sdwa v59,1,v59 dst_sel:DWORD dst_unused:UNUSED_PAD src0_sel:DWORD src1_sel:WORD_1
v_mfma_f32_16x16x32_bf16 v[172:175],a[236:239],v[36:39],v[172:175]
ds_read_u16 v57,v57
v_pk_fma_f16 v8,v8,s55,s55
v_mfma_f32_16x16x32_bf16 v[168:171],a[236:239],v[40:43],v[168:171]
v_rcp_f16 v9,v8
v_lshlrev_b32_sdwa v52,1,v52 dst_sel:DWORD dst_unused:UNUSED_PAD src0_sel:DWORD src1_sel:WORD_1
v_mfma_f32_16x16x32_bf16 v[216:219],a[244:247],v[40:43],v[216:219]
ds_read_u16_d16_hi v59,v59
v_lshlrev_b32_sdwa v54,1,v54 dst_sel:DWORD dst_unused:UNUSED_PAD src0_sel:DWORD src1_sel:WORD_1
v_mfma_f32_16x16x32_bf16 v[220:223],a[244:247],v[36:39],v[220:223]
ds_read_b64_tr_b16 a[192:193],v2 offset:12320
v_lshlrev_b32_sdwa v28,1,v28 dst_sel:DWORD dst_unused:UNUSED_PAD src0_sel:DWORD src1_sel:WORD_1
v_mfma_f32_16x16x32_bf16 v[188:191],a[252:255],v[36:39],v[188:191]
ds_read_b64_tr_b16 a[194:195],v2 offset:13344
v_lshlrev_b32_sdwa v30,1,v30 dst_sel:DWORD dst_unused:UNUSED_PAD src0_sel:DWORD src1_sel:WORD_1
v_mfma_f32_16x16x32_bf16 v[184:187],a[252:255],v[40:43],v[184:187]
ds_read_u16 v52,v52
v_rcp_f16_sdwa v9,v8 dst_sel:WORD_1 dst_unused:UNUSED_PRESERVE src0_sel:WORD_1
v_mfma_f32_16x16x32_bf16 v[224:227],a[240:243],v[40:43],v[224:227]
ds_read_u16_d16_hi v54,v54
v_fma_mix_f32 v53,v53,v9,0 op_sel_hi:[0,1,0]
v_mfma_f32_16x16x32_bf16 v[228:231],a[240:243],v[36:39],v[228:231]
ds_read_u16 v28,v28
v_lshlrev_b32_sdwa v29,1,v29 dst_sel:DWORD dst_unused:UNUSED_PAD src0_sel:DWORD src1_sel:WORD_1
v_mfma_f32_16x16x32_bf16 v[204:207],a[224:227],v[36:39],v[204:207]
TP6467_%=:
s_cbranch_execz TR0_%=
ds_read_u16_d16_hi v30,v30
v_fma_mix_f32 v55,v55,v9,0 op_sel:[0,1,0] op_sel_hi:[0,1,0]
v_mfma_f32_16x16x32_bf16 v[200:203],a[224:227],v[40:43],v[200:203]
s_waitcnt lgkmcnt(7)
ds_read_u16 v29,v29
v_lshlrev_b32_sdwa v31,1,v31 dst_sel:DWORD dst_unused:UNUSED_PAD src0_sel:DWORD src1_sel:WORD_1
v_mfma_f32_16x16x32_bf16 v[36:39],a[208:211],a[48:51],v[20:23]
s_add_u32 m0,s57,53040
ds_read_u16_d16_hi v31,v31
v_lshlrev_b32_sdwa v24,1,v24 dst_sel:DWORD dst_unused:UNUSED_PAD src0_sel:DWORD src1_sel:WORD_1
v_mfma_f32_16x16x32_bf16 v[36:39],a[204:207],a[64:67],v[36:39]
s_add_u32 s89,s56,6389760
ds_read_u16 v24,v24
v_lshl_add_u64 v[48:49],v[48:49],0,v[50:51]
v_mfma_f32_16x16x32_bf16 v[36:39],a[212:215],a[80:83],v[36:39]
buffer_load_dwordx4 v6,s[36:39],s89 offen lds
v_lshl_add_u64 v[50:51],v[44:45],0,v[46:47]
v_lshlrev_b32_sdwa v26,1,v26 dst_sel:DWORD dst_unused:UNUSED_PAD src0_sel:DWORD src1_sel:WORD_1
v_mfma_f32_16x16x32_bf16 v[40:43],a[200:203],a[80:83],v[16:19]
ds_read_u16_d16_hi v26,v26
v_lshlrev_b32_sdwa v25,1,v25 dst_sel:DWORD dst_unused:UNUSED_PAD src0_sel:DWORD src1_sel:WORD_1
v_mfma_f32_16x16x32_bf16 v[40:43],a[196:199],a[64:67],v[40:43]
s_waitcnt lgkmcnt(7)
ds_read_u16 v25,v25
v_lshl_add_u64 v[44:45],v[56:57],0,v[58:59]
v_mfma_f32_16x16x32_bf16 v[40:43],a[192:195],a[48:51],v[40:43]
v_lshlrev_b32_sdwa v27,1,v27 dst_sel:DWORD dst_unused:UNUSED_PAD src0_sel:DWORD src1_sel:WORD_1
v_lshlrev_b32_sdwa v32,1,v32 dst_sel:DWORD dst_unused:UNUSED_PAD src0_sel:DWORD src1_sel:WORD_1
v_mfma_f32_16x16x32_bf16 v[56:59],a[192:195],a[52:55],v[16:19]
s_waitcnt lgkmcnt(6)
ds_read_u16_d16_hi v27,v27
v_or_b32_e64 v46,v52,v54
v_mfma_f32_16x16x32_bf16 v[56:59],a[196:199],a[68:71],v[56:59]
ds_read_u16 v32,v32
v_cvt_pk_bf16_f32 v47,v53,v55
v_mfma_f32_16x16x32_bf16 v[56:59],a[200:203],a[84:87],v[56:59]
v_lshlrev_b32_sdwa v34,1,v34 dst_sel:DWORD dst_unused:UNUSED_PAD src0_sel:DWORD src1_sel:WORD_1
v_lshlrev_b32_sdwa v33,1,v33 dst_sel:DWORD dst_unused:UNUSED_PAD src0_sel:DWORD src1_sel:WORD_1
v_mfma_f32_16x16x32_bf16 v[52:55],a[212:215],a[84:87],v[20:23]
ds_read_u16_d16_hi v34,v34
v_lshlrev_b32_sdwa v35,1,v35 dst_sel:DWORD dst_unused:UNUSED_PAD src0_sel:DWORD src1_sel:WORD_1
v_mfma_f32_16x16x32_bf16 v[52:55],a[204:207],a[68:71],v[52:55]
ds_read_u16 v33,v33
v_mfma_f32_16x16x32_bf16 v[52:55],a[208:211],a[52:55],v[52:55]
ds_read_u16_d16_hi v35,v35
v_lshlrev_b32_sdwa v60,1,v60 dst_sel:DWORD dst_unused:UNUSED_PAD src0_sel:DWORD src1_sel:WORD_1
v_mfma_f32_16x16x32_bf16 v[136:139],a[232:235],v[48:51],v[136:139]
ds_read_u16 v60,v60
v_lshlrev_b32_sdwa v62,1,v62 dst_sel:DWORD dst_unused:UNUSED_PAD src0_sel:DWORD src1_sel:WORD_1
v_mfma_f32_16x16x32_bf16 v[140:143],a[232:235],v[44:47],v[140:143]
ds_read_u16_d16_hi v62,v62
v_mfma_f32_16x16x32_bf16 v[68:71],a[236:239],v[44:47],v[68:71]
ds_read_b64_tr_b16 a[248:249],v3 offset:18480
v_lshlrev_b32_sdwa v61,1,v61 dst_sel:DWORD dst_unused:UNUSED_PAD src0_sel:DWORD src1_sel:WORD_1
v_mfma_f32_16x16x32_bf16 v[64:67],a[236:239],v[48:51],v[64:67]
ds_read_u16 v61,v61
v_mfma_f32_16x16x32_bf16 v[144:147],a[244:247],v[48:51],v[144:147]
ds_read_b64_tr_b16 a[250:251],v3 offset:20016
v_lshlrev_b32_sdwa v63,1,v63 dst_sel:DWORD dst_unused:UNUSED_PAD src0_sel:DWORD src1_sel:WORD_1
v_mfma_f32_16x16x32_bf16 v[148:151],a[244:247],v[44:47],v[148:151]
ds_read_u16_d16_hi v63,v63
v_lshlrev_b32_sdwa v36,1,v36 dst_sel:DWORD dst_unused:UNUSED_PAD src0_sel:DWORD src1_sel:WORD_1
v_mfma_f32_16x16x32_bf16 v[84:87],a[252:255],v[44:47],v[84:87]
ds_read_u16 v36,v36
v_mfma_f32_16x16x32_bf16 v[80:83],a[252:255],v[48:51],v[80:83]
ds_read_b64_tr_b16 a[228:229],v3 offset:18488
v_lshlrev_b32_sdwa v38,1,v38 dst_sel:DWORD dst_unused:UNUSED_PAD src0_sel:DWORD src1_sel:WORD_1
v_mfma_f32_16x16x32_bf16 v[152:155],a[240:243],v[48:51],v[152:155]
ds_read_u16_d16_hi v38,v38
v_lshlrev_b32_sdwa v37,1,v37 dst_sel:DWORD dst_unused:UNUSED_PAD src0_sel:DWORD src1_sel:WORD_1
v_mfma_f32_16x16x32_bf16 v[156:159],a[240:243],v[44:47],v[156:159]
ds_read_u16 v37,v37
v_mfma_f32_16x16x32_bf16 v[100:103],a[224:227],v[44:47],v[100:103]
ds_read_b64_tr_b16 a[230:231],v3 offset:20024
v_lshlrev_b32_sdwa v39,1,v39 dst_sel:DWORD dst_unused:UNUSED_PAD src0_sel:DWORD src1_sel:WORD_1
v_mfma_f32_16x16x32_bf16 v[96:99],a[224:227],v[48:51],v[96:99]
ds_read_u16_d16_hi v39,v39
v_mfma_f32_16x16x32_bf16 v[44:47],a[208:211],a[56:59],v[20:23]
ds_read_b64_tr_b16 a[220:221],v1 offset:18480
v_lshlrev_b32_sdwa v40,1,v40 dst_sel:DWORD dst_unused:UNUSED_PAD src0_sel:DWORD src1_sel:WORD_1
v_mfma_f32_16x16x32_bf16 v[44:47],a[204:207],a[72:75],v[44:47]
s_waitcnt lgkmcnt(8)
ds_read_u16 v40,v40
v_lshl_add_u64 v[28:29],v[28:29],0,v[30:31]
v_fma_mixlo_f16 v8,v57,s99,0
v_mfma_f32_16x16x32_bf16 v[44:47],a[212:215],a[88:91],v[44:47]
ds_read_b64_tr_b16 a[222:223],v1 offset:19504
v_lshl_add_u64 v[30:31],v[24:25],0,v[26:27]
v_lshlrev_b32_sdwa v42,1,v42 dst_sel:DWORD dst_unused:UNUSED_PAD src0_sel:DWORD src1_sel:WORD_1
v_mfma_f32_16x16x32_bf16 v[48:51],a[200:203],a[88:91],v[16:19]
ds_read_u16_d16_hi v42,v42
v_fma_mixhi_f16 v8,v59,s99,0
v_mfma_f32_16x16x32_bf16 v[48:51],a[196:199],a[72:75],v[48:51]
ds_read_b64_tr_b16 a[216:217],v1 offset:20528
v_lshl_add_u64 v[24:25],v[32:33],0,v[34:35]
v_lshlrev_b32_sdwa v41,1,v41 dst_sel:DWORD dst_unused:UNUSED_PAD src0_sel:DWORD src1_sel:WORD_1
v_pk_fma_f16 v9,v8,v8,s54
v_mfma_f32_16x16x32_bf16 v[48:51],a[192:195],a[56:59],v[48:51]
ds_read_u16 v41,v41
v_pk_mul_f16 v9,v8,v9
v_mfma_f32_16x16x32_bf16 v[32:35],a[192:195],a[60:63],v[16:19]
ds_read_b64_tr_b16 a[218:219],v1 offset:21552
v_lshlrev_b32_sdwa v43,1,v43 dst_sel:DWORD dst_unused:UNUSED_PAD src0_sel:DWORD src1_sel:WORD_1
v_mfma_f32_16x16x32_bf16 v[32:35],a[196:199],a[76:79],v[32:35]
s_waitcnt lgkmcnt(8)
ds_read_u16_d16_hi v43,v43
v_lshl_add_u64 v[26:27],v[60:61],0,v[62:63]
v_mfma_f32_16x16x32_bf16 v[32:35],a[200:203],a[92:95],v[32:35]
v_exp_f16 v8,v9
v_lshlrev_b32_sdwa v52,1,v52 dst_sel:DWORD dst_unused:UNUSED_PAD src0_sel:DWORD src1_sel:WORD_1
v_mfma_f32_16x16x32_bf16 v[60:63],a[212:215],a[92:95],v[20:23]
ds_read_u16 v52,v52
v_lshlrev_b32_sdwa v54,1,v54 dst_sel:DWORD dst_unused:UNUSED_PAD src0_sel:DWORD src1_sel:WORD_1
v_mfma_f32_16x16x32_bf16 v[60:63],a[204:207],a[76:79],v[60:63]
ds_read_u16_d16_hi v54,v54
v_lshlrev_b32_sdwa v53,1,v53 dst_sel:DWORD dst_unused:UNUSED_PAD src0_sel:DWORD src1_sel:WORD_1
v_mfma_f32_16x16x32_bf16 v[60:63],a[208:211],a[60:63],v[60:63]
v_exp_f16_sdwa v8,v9 dst_sel:WORD_1 dst_unused:UNUSED_PRESERVE src0_sel:WORD_1
v_mfma_f32_16x16x32_bf16 v[112:115],a[232:235],v[28:31],v[112:115]
ds_read_u16 v53,v53
v_lshlrev_b32_sdwa v55,1,v55 dst_sel:DWORD dst_unused:UNUSED_PAD src0_sel:DWORD src1_sel:WORD_1
v_mfma_f32_16x16x32_bf16 v[116:119],a[232:235],v[24:27],v[116:119]
ds_read_u16_d16_hi v55,v55
v_pk_fma_f16 v8,v8,s55,s55
v_mfma_f32_16x16x32_bf16 v[76:79],a[236:239],v[24:27],v[76:79]
ds_read_b64_tr_b16 a[232:233],v3 offset:18544
v_lshlrev_b32_sdwa v56,1,v56 dst_sel:DWORD dst_unused:UNUSED_PAD src0_sel:DWORD src1_sel:WORD_1
v_mfma_f32_16x16x32_bf16 v[72:75],a[236:239],v[28:31],v[72:75]
ds_read_u16 v56,v56
v_rcp_f16 v9,v8
v_mfma_f32_16x16x32_bf16 v[120:123],a[244:247],v[28:31],v[120:123]
ds_read_b64_tr_b16 a[234:235],v3 offset:20080
v_lshlrev_b32_sdwa v58,1,v58 dst_sel:DWORD dst_unused:UNUSED_PAD src0_sel:DWORD src1_sel:WORD_1
v_mfma_f32_16x16x32_bf16 v[124:127],a[244:247],v[24:27],v[124:127]
ds_read_u16_d16_hi v58,v58
ds_read_b64_tr_b16 a[236:237],v3 offset:18552
v_mfma_f32_16x16x32_bf16 v[92:95],a[252:255],v[24:27],v[92:95]
ds_read_b64_tr_b16 a[238:239],v3 offset:20088
v_lshlrev_b32_sdwa v44,1,v44 dst_sel:DWORD dst_unused:UNUSED_PAD src0_sel:DWORD src1_sel:WORD_1
v_mfma_f32_16x16x32_bf16 v[88:91],a[252:255],v[28:31],v[88:91]
ds_read_u16 v44,v44
v_rcp_f16_sdwa v9,v8 dst_sel:WORD_1 dst_unused:UNUSED_PRESERVE src0_sel:WORD_1
v_mfma_f32_16x16x32_bf16 v[128:131],a[240:243],v[28:31],v[128:131]
ds_read_b64_tr_b16 a[244:245],v3 offset:18608
v_lshlrev_b32_sdwa v46,1,v46 dst_sel:DWORD dst_unused:UNUSED_PAD src0_sel:DWORD src1_sel:WORD_1
v_mfma_f32_16x16x32_bf16 v[132:135],a[240:243],v[24:27],v[132:135]
ds_read_u16_d16_hi v46,v46
v_fma_mix_f32 v57,v57,v9,0 op_sel_hi:[0,1,0]
v_mfma_f32_16x16x32_bf16 v[108:111],a[224:227],v[24:27],v[108:111]
ds_read_b64_tr_b16 a[246:247],v3 offset:20144
v_lshlrev_b32_sdwa v45,1,v45 dst_sel:DWORD dst_unused:UNUSED_PAD src0_sel:DWORD src1_sel:WORD_1
v_mfma_f32_16x16x32_bf16 v[104:107],a[224:227],v[28:31],v[104:107]
ds_read_u16 v45,v45
v_fma_mix_f32 v59,v59,v9,0 op_sel:[0,1,0] op_sel_hi:[0,1,0]
v_lshlrev_b32_sdwa v47,1,v47 dst_sel:DWORD dst_unused:UNUSED_PAD src0_sel:DWORD src1_sel:WORD_1
v_mfma_f32_16x16x32_bf16 v[24:27],a[208:211],a[0:3],v[20:23]
ds_read_u16_d16_hi v47,v47
v_lshlrev_b32_sdwa v48,1,v48 dst_sel:DWORD dst_unused:UNUSED_PAD src0_sel:DWORD src1_sel:WORD_1
v_mfma_f32_16x16x32_bf16 v[24:27],a[204:207],a[16:19],v[24:27]
s_waitcnt lgkmcnt(9)
ds_read_b64_tr_b16 a[252:253],v3 offset:18616
v_lshl_add_u64 v[36:37],v[36:37],0,v[38:39]
v_mfma_f32_16x16x32_bf16 v[24:27],a[212:215],a[32:35],v[24:27]
ds_read_b64_tr_b16 a[254:255],v3 offset:20152
v_lshl_add_u64 v[38:39],v[40:41],0,v[42:43]
v_lshlrev_b32_sdwa v50,1,v50 dst_sel:DWORD dst_unused:UNUSED_PAD src0_sel:DWORD src1_sel:WORD_1
v_mfma_f32_16x16x32_bf16 v[28:31],a[200:203],a[32:35],v[16:19]
ds_read_u16 v48,v48
v_lshlrev_b32_sdwa v49,1,v49 dst_sel:DWORD dst_unused:UNUSED_PAD src0_sel:DWORD src1_sel:WORD_1
v_mfma_f32_16x16x32_bf16 v[28:31],a[196:199],a[16:19],v[28:31]
ds_read_u16_d16_hi v50,v50
v_lshl_add_u64 v[40:41],v[52:53],0,v[54:55]
v_mfma_f32_16x16x32_bf16 v[28:31],a[192:195],a[0:3],v[28:31]
ds_read_u16 v49,v49
v_lshlrev_b32_sdwa v51,1,v51 dst_sel:DWORD dst_unused:UNUSED_PAD src0_sel:DWORD src1_sel:WORD_1
v_mfma_f32_16x16x32_bf16 v[52:55],a[192:195],a[4:7],v[16:19]
s_waitcnt lgkmcnt(8)
ds_read_u16_d16_hi v51,v51
v_or_b32 v42,v56,v58
v_mfma_f32_16x16x32_bf16 v[52:55],a[196:199],a[20:23],v[52:55]
v_cvt_pk_bf16_f32 v43,v57,v59
v_lshlrev_b32_sdwa v60,1,v60 dst_sel:DWORD dst_unused:UNUSED_PAD src0_sel:DWORD src1_sel:WORD_1
v_mfma_f32_16x16x32_bf16 v[52:55],a[200:203],a[36:39],v[52:55]
ds_read_u16 v60,v60
v_lshlrev_b32_sdwa v62,1,v62 dst_sel:DWORD dst_unused:UNUSED_PAD src0_sel:DWORD src1_sel:WORD_1
v_mfma_f32_16x16x32_bf16 v[56:59],a[212:215],a[36:39],v[20:23]
ds_read_u16_d16_hi v62,v62
v_lshlrev_b32_sdwa v61,1,v61 dst_sel:DWORD dst_unused:UNUSED_PAD src0_sel:DWORD src1_sel:WORD_1
v_mfma_f32_16x16x32_bf16 v[56:59],a[204:207],a[20:23],v[56:59]
ds_read_u16 v61,v61
v_lshlrev_b32_sdwa v63,1,v63 dst_sel:DWORD dst_unused:UNUSED_PAD src0_sel:DWORD src1_sel:WORD_1
v_mfma_f32_16x16x32_bf16 v[56:59],a[208:211],a[4:7],v[56:59]
ds_read_u16_d16_hi v63,v63
v_lshlrev_b32_sdwa v32,1,v32 dst_sel:DWORD dst_unused:UNUSED_PAD src0_sel:DWORD src1_sel:WORD_1
v_mfma_f32_16x16x32_bf16 v[232:235],a[248:251],v[36:39],v[232:235]
ds_read_u16 v32,v32
v_lshlrev_b32_sdwa v34,1,v34 dst_sel:DWORD dst_unused:UNUSED_PAD src0_sel:DWORD src1_sel:WORD_1
v_mfma_f32_16x16x32_bf16 v[236:239],a[248:251],v[40:43],v[236:239]
ds_read_u16_d16_hi v34,v34
v_mfma_f32_16x16x32_bf16 v[164:167],a[228:231],v[40:43],v[164:167]
v_lshlrev_b32_sdwa v33,1,v33 dst_sel:DWORD dst_unused:UNUSED_PAD src0_sel:DWORD src1_sel:WORD_1
v_mfma_f32_16x16x32_bf16 v[160:163],a[228:231],v[36:39],v[160:163]
ds_read_u16 v33,v33
v_mfma_f32_16x16x32_bf16 v[240:243],a[232:235],v[36:39],v[240:243]
v_lshlrev_b32_sdwa v35,1,v35 dst_sel:DWORD dst_unused:UNUSED_PAD src0_sel:DWORD src1_sel:WORD_1
v_mfma_f32_16x16x32_bf16 v[244:247],a[232:235],v[40:43],v[244:247]
ds_read_u16_d16_hi v35,v35
v_lshlrev_b32_sdwa v24,1,v24 dst_sel:DWORD dst_unused:UNUSED_PAD src0_sel:DWORD src1_sel:WORD_1
v_mfma_f32_16x16x32_bf16 v[180:183],a[236:239],v[40:43],v[180:183]
ds_read_u16 v24,v24
v_mfma_f32_16x16x32_bf16 v[176:179],a[236:239],v[36:39],v[176:179]
s_waitcnt lgkmcnt(6)
v_lshlrev_b32_sdwa v26,1,v26 dst_sel:DWORD dst_unused:UNUSED_PAD src0_sel:DWORD src1_sel:WORD_1
v_mfma_f32_16x16x32_bf16 v[248:251],a[244:247],v[36:39],v[248:251]
ds_read_u16_d16_hi v26,v26
v_lshlrev_b32_sdwa v25,1,v25 dst_sel:DWORD dst_unused:UNUSED_PAD src0_sel:DWORD src1_sel:WORD_1
v_mfma_f32_16x16x32_bf16 v[252:255],a[244:247],v[40:43],v[252:255]
ds_read_u16 v25,v25
v_mfma_f32_16x16x32_bf16 v[196:199],a[252:255],v[40:43],v[196:199]
v_lshlrev_b32_sdwa v27,1,v27 dst_sel:DWORD dst_unused:UNUSED_PAD src0_sel:DWORD src1_sel:WORD_1
v_mfma_f32_16x16x32_bf16 v[192:195],a[252:255],v[36:39],v[192:195]
ds_read_u16_d16_hi v27,v27
v_mfma_f32_16x16x32_bf16 v[40:43],a[208:211],a[8:11],v[20:23]
v_lshlrev_b32_sdwa v28,1,v28 dst_sel:DWORD dst_unused:UNUSED_PAD src0_sel:DWORD src1_sel:WORD_1
v_fma_mixlo_f16 v8,v53,s99,0
v_mfma_f32_16x16x32_bf16 v[40:43],a[204:207],a[24:27],v[40:43]
ds_read_u16 v28,v28
v_lshl_add_u64 v[44:45],v[44:45],0,v[46:47]
v_mfma_f32_16x16x32_bf16 v[40:43],a[212:215],a[40:43],v[40:43]
v_lshl_add_u64 v[46:47],v[48:49],0,v[50:51]
v_lshlrev_b32_sdwa v30,1,v30 dst_sel:DWORD dst_unused:UNUSED_PAD src0_sel:DWORD src1_sel:WORD_1
v_mfma_f32_16x16x32_bf16 v[36:39],a[200:203],a[40:43],v[16:19]
ds_read_u16_d16_hi v30,v30
v_fma_mixhi_f16 v8,v55,s99,0
v_mfma_f32_16x16x32_bf16 v[36:39],a[196:199],a[24:27],v[36:39]
s_waitcnt lgkmcnt(5)
v_lshl_add_u64 v[48:49],v[60:61],0,v[62:63]
v_lshlrev_b32_sdwa v29,1,v29 dst_sel:DWORD dst_unused:UNUSED_PAD src0_sel:DWORD src1_sel:WORD_1
v_pk_fma_f16 v9,v8,v8,s54
v_mfma_f32_16x16x32_bf16 v[36:39],a[192:195],a[8:11],v[36:39]
ds_read_u16 v29,v29
v_pk_mul_f16 v9,v8,v9
v_mfma_f32_16x16x32_bf16 v[60:63],a[192:195],a[12:15],v[16:19]
ds_read_b128 v[16:19],v4 offset:512
v_lshlrev_b32_sdwa v31,1,v31 dst_sel:DWORD dst_unused:UNUSED_PAD src0_sel:DWORD src1_sel:WORD_1
v_mfma_f32_16x16x32_bf16 v[60:63],a[196:199],a[28:31],v[60:63]
ds_read_b64_tr_b16 a[192:193],v1 offset:22576
ds_read_b64_tr_b16 a[194:195],v1 offset:23600
v_lshl_add_u64 v[50:51],v[32:33],0,v[34:35]
v_mfma_f32_16x16x32_bf16 v[60:63],a[200:203],a[44:47],v[60:63]
ds_read_u16_d16_hi v31,v31
v_exp_f16 v8,v9
v_lshlrev_b32_sdwa v56,1,v56 dst_sel:DWORD dst_unused:UNUSED_PAD src0_sel:DWORD src1_sel:WORD_1
v_mfma_f32_16x16x32_bf16 v[32:35],a[212:215],a[44:47],v[20:23]
ds_read_b128 v[20:23],v4 offset:544
ds_read_b64_tr_b16 a[196:197],v2 offset:22576
v_mfma_f32_16x16x32_bf16 v[32:35],a[204:207],a[28:31],v[32:35]
ds_read_b64_tr_b16 a[198:199],v2 offset:23600
ds_read_b64_tr_b16 a[200:201],v2 offset:20528
v_lshlrev_b32_sdwa v58,1,v58 dst_sel:DWORD dst_unused:UNUSED_PAD src0_sel:DWORD src1_sel:WORD_1
v_mfma_f32_16x16x32_bf16 v[32:35],a[208:211],a[12:15],v[32:35]
ds_read_b64_tr_b16 a[202:203],v2 offset:21552
v_exp_f16_sdwa v8,v9 dst_sel:WORD_1 dst_unused:UNUSED_PRESERVE src0_sel:WORD_1
v_mfma_f32_16x16x32_bf16 v[208:211],a[248:251],v[44:47],v[208:211]
ds_read_u16 v56,v56
v_lshlrev_b32_sdwa v57,1,v57 dst_sel:DWORD dst_unused:UNUSED_PAD src0_sel:DWORD src1_sel:WORD_1
v_mfma_f32_16x16x32_bf16 v[212:215],a[248:251],v[48:51],v[212:215]
ds_read_u16_d16_hi v58,v58
v_lshlrev_b32_sdwa v59,1,v59 dst_sel:DWORD dst_unused:UNUSED_PAD src0_sel:DWORD src1_sel:WORD_1
v_mfma_f32_16x16x32_bf16 v[172:175],a[228:231],v[48:51],v[172:175]
ds_read_u16 v57,v57
v_pk_fma_f16 v8,v8,s55,s55
v_mfma_f32_16x16x32_bf16 v[168:171],a[228:231],v[44:47],v[168:171]
v_rcp_f16 v9,v8
v_lshlrev_b32_sdwa v52,1,v52 dst_sel:DWORD dst_unused:UNUSED_PAD src0_sel:DWORD src1_sel:WORD_1
v_mfma_f32_16x16x32_bf16 v[216:219],a[232:235],v[44:47],v[216:219]
ds_read_u16_d16_hi v59,v59
v_lshlrev_b32_sdwa v54,1,v54 dst_sel:DWORD dst_unused:UNUSED_PAD src0_sel:DWORD src1_sel:WORD_1
v_mfma_f32_16x16x32_bf16 v[220:223],a[232:235],v[48:51],v[220:223]
ds_read_b64_tr_b16 a[212:213],v2 offset:18480
v_lshlrev_b32_sdwa v40,1,v40 dst_sel:DWORD dst_unused:UNUSED_PAD src0_sel:DWORD src1_sel:WORD_1
v_mfma_f32_16x16x32_bf16 v[188:191],a[236:239],v[48:51],v[188:191]
ds_read_b64_tr_b16 a[214:215],v2 offset:19504
v_lshlrev_b32_sdwa v42,1,v42 dst_sel:DWORD dst_unused:UNUSED_PAD src0_sel:DWORD src1_sel:WORD_1
v_mfma_f32_16x16x32_bf16 v[184:187],a[236:239],v[44:47],v[184:187]
ds_read_u16 v52,v52
v_rcp_f16_sdwa v9,v8 dst_sel:WORD_1 dst_unused:UNUSED_PRESERVE src0_sel:WORD_1
v_mfma_f32_16x16x32_bf16 v[224:227],a[244:247],v[44:47],v[224:227]
ds_read_u16_d16_hi v54,v54
v_fma_mix_f32 v53,v53,v9,0 op_sel_hi:[0,1,0]
v_mfma_f32_16x16x32_bf16 v[228:231],a[244:247],v[48:51],v[228:231]
ds_read_u16 v40,v40
v_lshlrev_b32_sdwa v41,1,v41 dst_sel:DWORD dst_unused:UNUSED_PAD src0_sel:DWORD src1_sel:WORD_1
v_mfma_f32_16x16x32_bf16 v[204:207],a[252:255],v[48:51],v[204:207]
ds_read_u16_d16_hi v42,v42
v_fma_mix_f32 v55,v55,v9,0 op_sel:[0,1,0] op_sel_hi:[0,1,0]
v_mfma_f32_16x16x32_bf16 v[200:203],a[252:255],v[44:47],v[200:203]
s_waitcnt lgkmcnt(7)
ds_read_u16 v41,v41
v_lshlrev_b32_sdwa v43,1,v43 dst_sel:DWORD dst_unused:UNUSED_PAD src0_sel:DWORD src1_sel:WORD_1
v_mfma_f32_16x16x32_bf16 v[48:51],a[220:223],a[48:51],v[16:19]
ds_read_u16_d16_hi v43,v43
v_lshlrev_b32_sdwa v36,1,v36 dst_sel:DWORD dst_unused:UNUSED_PAD src0_sel:DWORD src1_sel:WORD_1
v_mfma_f32_16x16x32_bf16 v[48:51],a[216:219],a[64:67],v[48:51]
ds_read_u16 v36,v36
v_lshl_add_u64 v[24:25],v[24:25],0,v[26:27]
v_mfma_f32_16x16x32_bf16 v[48:51],a[192:195],a[80:83],v[48:51]
v_lshl_add_u64 v[26:27],v[28:29],0,v[30:31]
v_lshlrev_b32_sdwa v38,1,v38 dst_sel:DWORD dst_unused:UNUSED_PAD src0_sel:DWORD src1_sel:WORD_1
v_mfma_f32_16x16x32_bf16 v[44:47],a[196:199],a[80:83],v[20:23]
ds_read_u16_d16_hi v38,v38
v_lshlrev_b32_sdwa v37,1,v37 dst_sel:DWORD dst_unused:UNUSED_PAD src0_sel:DWORD src1_sel:WORD_1
v_mfma_f32_16x16x32_bf16 v[44:47],a[200:203],a[64:67],v[44:47]
s_waitcnt lgkmcnt(7)
ds_read_u16 v37,v37
v_lshl_add_u64 v[28:29],v[56:57],0,v[58:59]
v_mfma_f32_16x16x32_bf16 v[44:47],a[212:215],a[48:51],v[44:47]
v_lshlrev_b32_sdwa v39,1,v39 dst_sel:DWORD dst_unused:UNUSED_PAD src0_sel:DWORD src1_sel:WORD_1
v_lshlrev_b32_sdwa v32,1,v32 dst_sel:DWORD dst_unused:UNUSED_PAD src0_sel:DWORD src1_sel:WORD_1
v_mfma_f32_16x16x32_bf16 v[56:59],a[212:215],a[52:55],v[20:23]
s_waitcnt lgkmcnt(6)
ds_read_u16_d16_hi v39,v39
v_or_b32 v30,v52,v54
v_mfma_f32_16x16x32_bf16 v[56:59],a[200:203],a[68:71],v[56:59]
s_waitcnt vmcnt(6)
ds_read_u16 v32,v32
v_cvt_pk_bf16_f32 v31,v53,v55
v_mfma_f32_16x16x32_bf16 v[56:59],a[196:199],a[84:87],v[56:59]
s_barrier
v_lshlrev_b32_sdwa v34,1,v34 dst_sel:DWORD dst_unused:UNUSED_PAD src0_sel:DWORD src1_sel:WORD_1
v_lshlrev_b32_sdwa v33,1,v33 dst_sel:DWORD dst_unused:UNUSED_PAD src0_sel:DWORD src1_sel:WORD_1
v_mfma_f32_16x16x32_bf16 v[52:55],a[192:195],a[84:87],v[16:19]
ds_read_u16_d16_hi v34,v34
v_lshlrev_b32_sdwa v35,1,v35 dst_sel:DWORD dst_unused:UNUSED_PAD src0_sel:DWORD src1_sel:WORD_1
v_mfma_f32_16x16x32_bf16 v[52:55],a[216:219],a[68:71],v[52:55]
s_add_u32 m0,s58,12320
ds_read_u16 v33,v33
v_mfma_f32_16x16x32_bf16 v[52:55],a[220:223],a[52:55],v[52:55]
s_add_u32 s69,s69,s59
ds_read_u16_d16_hi v35,v35
v_lshlrev_b32_sdwa v60,1,v60 dst_sel:DWORD dst_unused:UNUSED_PAD src0_sel:DWORD src1_sel:WORD_1
v_mfma_f32_16x16x32_bf16 v[136:139],a[248:251],v[24:27],v[136:139]
buffer_load_dwordx4 v5,s[44:47],s69 offen offset:0 lds
v_lshlrev_b32_sdwa v62,1,v62 dst_sel:DWORD dst_unused:UNUSED_PAD src0_sel:DWORD src1_sel:WORD_1
v_mfma_f32_16x16x32_bf16 v[140:143],a[248:251],v[28:31],v[140:143]
ds_read_u16 v60,v60
v_mfma_f32_16x16x32_bf16 v[68:71],a[228:231],v[28:31],v[68:71]
ds_read_u16_d16_hi v62,v62
ds_read_b64_tr_b16 a[240:241],v3 offset:0
v_mfma_f32_16x16x32_bf16 v[64:67],a[228:231],v[24:27],v[64:67]
ds_read_b64_tr_b16 a[242:243],v3 offset:1536
v_lshlrev_b32_sdwa v61,1,v61 dst_sel:DWORD dst_unused:UNUSED_PAD src0_sel:DWORD src1_sel:WORD_1
v_mfma_f32_16x16x32_bf16 v[144:147],a[232:235],v[24:27],v[144:147]
s_add_u32 s69,s69,s53
ds_read_u16 v61,v61
v_lshlrev_b32_sdwa v63,1,v63 dst_sel:DWORD dst_unused:UNUSED_PAD src0_sel:DWORD src1_sel:WORD_1
v_mfma_f32_16x16x32_bf16 v[148:151],a[232:235],v[28:31],v[148:151]
buffer_load_dwordx4 v5,s[44:47],s69 offen offset:1024 lds
v_lshlrev_b32_sdwa v48,1,v48 dst_sel:DWORD dst_unused:UNUSED_PAD src0_sel:DWORD src1_sel:WORD_1
v_mfma_f32_16x16x32_bf16 v[84:87],a[236:239],v[28:31],v[84:87]
ds_read_u16_d16_hi v63,v63
v_mfma_f32_16x16x32_bf16 v[80:83],a[236:239],v[24:27],v[80:83]
ds_read_u16 v48,v48
v_lshlrev_b32_sdwa v50,1,v50 dst_sel:DWORD dst_unused:UNUSED_PAD src0_sel:DWORD src1_sel:WORD_1
v_mfma_f32_16x16x32_bf16 v[152:155],a[244:247],v[24:27],v[152:155]
ds_read_u16_d16_hi v50,v50
v_lshlrev_b32_sdwa v49,1,v49 dst_sel:DWORD dst_unused:UNUSED_PAD src0_sel:DWORD src1_sel:WORD_1
v_mfma_f32_16x16x32_bf16 v[156:159],a[244:247],v[28:31],v[156:159]
ds_read_u16 v49,v49
v_mfma_f32_16x16x32_bf16 v[100:103],a[252:255],v[28:31],v[100:103]
s_add_u32 s69,s69,s53
ds_read_b64_tr_b16 a[224:225],v3 offset:8
v_lshlrev_b32_sdwa v51,1,v51 dst_sel:DWORD dst_unused:UNUSED_PAD src0_sel:DWORD src1_sel:WORD_1
v_mfma_f32_16x16x32_bf16 v[96:99],a[252:255],v[24:27],v[96:99]
buffer_load_dwordx4 v5,s[44:47],s69 offen offset:2048 lds
v_mfma_f32_16x16x32_bf16 v[28:31],a[220:223],a[56:59],v[16:19]
ds_read_u16_d16_hi v51,v51
v_lshlrev_b32_sdwa v44,1,v44 dst_sel:DWORD dst_unused:UNUSED_PAD src0_sel:DWORD src1_sel:WORD_1
v_mfma_f32_16x16x32_bf16 v[28:31],a[216:219],a[72:75],v[28:31]
s_waitcnt lgkmcnt(6)
ds_read_u16 v44,v44
v_lshl_add_u64 v[40:41],v[40:41],0,v[42:43]
v_fma_mixlo_f16 v8,v57,s99,0
v_mfma_f32_16x16x32_bf16 v[28:31],a[192:195],a[88:91],v[28:31]
ds_read_b64_tr_b16 a[226:227],v3 offset:1544
v_lshl_add_u64 v[42:43],v[36:37],0,v[38:39]
v_lshlrev_b32_sdwa v46,1,v46 dst_sel:DWORD dst_unused:UNUSED_PAD src0_sel:DWORD src1_sel:WORD_1
v_mfma_f32_16x16x32_bf16 v[24:27],a[196:199],a[88:91],v[20:23]
ds_read_u16_d16_hi v46,v46
v_fma_mixhi_f16 v8,v59,s99,0
v_mfma_f32_16x16x32_bf16 v[24:27],a[200:203],a[72:75],v[24:27]
ds_read_b64_tr_b16 a[204:205],v1 offset:0
v_lshl_add_u64 v[36:37],v[32:33],0,v[34:35]
v_lshlrev_b32_sdwa v45,1,v45 dst_sel:DWORD dst_unused:UNUSED_PAD src0_sel:DWORD src1_sel:WORD_1
v_pk_fma_f16 v9,v8,v8,s54
v_mfma_f32_16x16x32_bf16 v[24:27],a[212:215],a[56:59],v[24:27]
ds_read_u16 v45,v45
v_pk_mul_f16 v9,v8,v9
v_mfma_f32_16x16x32_bf16 v[32:35],a[212:215],a[60:63],v[20:23]
ds_read_b64_tr_b16 a[206:207],v1 offset:1024
v_lshlrev_b32_sdwa v47,1,v47 dst_sel:DWORD dst_unused:UNUSED_PAD src0_sel:DWORD src1_sel:WORD_1
v_mfma_f32_16x16x32_bf16 v[32:35],a[200:203],a[76:79],v[32:35]
s_waitcnt lgkmcnt(7)
ds_read_u16_d16_hi v47,v47
v_lshl_add_u64 v[38:39],v[60:61],0,v[62:63]
v_mfma_f32_16x16x32_bf16 v[32:35],a[196:199],a[92:95],v[32:35]
v_exp_f16 v8,v9
v_lshlrev_b32_sdwa v52,1,v52 dst_sel:DWORD dst_unused:UNUSED_PAD src0_sel:DWORD src1_sel:WORD_1
v_mfma_f32_16x16x32_bf16 v[60:63],a[192:195],a[92:95],v[16:19]
ds_read_u16 v52,v52
v_lshlrev_b32_sdwa v54,1,v54 dst_sel:DWORD dst_unused:UNUSED_PAD src0_sel:DWORD src1_sel:WORD_1
v_mfma_f32_16x16x32_bf16 v[60:63],a[216:219],a[76:79],v[60:63]
ds_read_u16_d16_hi v54,v54
v_lshlrev_b32_sdwa v53,1,v53 dst_sel:DWORD dst_unused:UNUSED_PAD src0_sel:DWORD src1_sel:WORD_1
v_mfma_f32_16x16x32_bf16 v[60:63],a[220:223],a[60:63],v[60:63]
v_exp_f16_sdwa v8,v9 dst_sel:WORD_1 dst_unused:UNUSED_PRESERVE src0_sel:WORD_1
v_mfma_f32_16x16x32_bf16 v[112:115],a[248:251],v[40:43],v[112:115]
ds_read_u16 v53,v53
v_lshlrev_b32_sdwa v55,1,v55 dst_sel:DWORD dst_unused:UNUSED_PAD src0_sel:DWORD src1_sel:WORD_1
v_mfma_f32_16x16x32_bf16 v[116:119],a[248:251],v[36:39],v[116:119]
ds_read_u16_d16_hi v55,v55
v_pk_fma_f16 v8,v8,s55,s55
v_mfma_f32_16x16x32_bf16 v[76:79],a[228:231],v[36:39],v[76:79]
ds_read_b64_tr_b16 a[248:249],v3 offset:64
v_lshlrev_b32_sdwa v56,1,v56 dst_sel:DWORD dst_unused:UNUSED_PAD src0_sel:DWORD src1_sel:WORD_1
v_mfma_f32_16x16x32_bf16 v[72:75],a[228:231],v[40:43],v[72:75]
ds_read_u16 v56,v56
v_rcp_f16 v9,v8
v_mfma_f32_16x16x32_bf16 v[120:123],a[232:235],v[40:43],v[120:123]
ds_read_b64_tr_b16 a[250:251],v3 offset:1600
v_lshlrev_b32_sdwa v58,1,v58 dst_sel:DWORD dst_unused:UNUSED_PAD src0_sel:DWORD src1_sel:WORD_1
v_mfma_f32_16x16x32_bf16 v[124:127],a[232:235],v[36:39],v[124:127]
ds_read_u16_d16_hi v58,v58
ds_read_b64_tr_b16 a[228:229],v3 offset:72
v_mfma_f32_16x16x32_bf16 v[92:95],a[236:239],v[36:39],v[92:95]
ds_read_b64_tr_b16 a[230:231],v3 offset:1608
v_lshlrev_b32_sdwa v28,1,v28 dst_sel:DWORD dst_unused:UNUSED_PAD src0_sel:DWORD src1_sel:WORD_1
v_mfma_f32_16x16x32_bf16 v[88:91],a[236:239],v[40:43],v[88:91]
ds_read_u16 v28,v28
v_rcp_f16_sdwa v9,v8 dst_sel:WORD_1 dst_unused:UNUSED_PRESERVE src0_sel:WORD_1
v_mfma_f32_16x16x32_bf16 v[128:131],a[244:247],v[40:43],v[128:131]
ds_read_b64_tr_b16 a[232:233],v3 offset:128
v_lshlrev_b32_sdwa v30,1,v30 dst_sel:DWORD dst_unused:UNUSED_PAD src0_sel:DWORD src1_sel:WORD_1
v_mfma_f32_16x16x32_bf16 v[132:135],a[244:247],v[36:39],v[132:135]
ds_read_u16_d16_hi v30,v30
v_fma_mix_f32 v57,v57,v9,0 op_sel_hi:[0,1,0]
v_mfma_f32_16x16x32_bf16 v[108:111],a[252:255],v[36:39],v[108:111]
ds_read_b64_tr_b16 a[234:235],v3 offset:1664
v_lshlrev_b32_sdwa v29,1,v29 dst_sel:DWORD dst_unused:UNUSED_PAD src0_sel:DWORD src1_sel:WORD_1
v_mfma_f32_16x16x32_bf16 v[104:107],a[252:255],v[40:43],v[104:107]
ds_read_u16 v29,v29
v_fma_mix_f32 v59,v59,v9,0 op_sel:[0,1,0] op_sel_hi:[0,1,0]
v_lshlrev_b32_sdwa v31,1,v31 dst_sel:DWORD dst_unused:UNUSED_PAD src0_sel:DWORD src1_sel:WORD_1
v_mfma_f32_16x16x32_bf16 v[36:39],a[220:223],a[0:3],v[16:19]
s_add_u32 m0,s58,18480
ds_read_u16_d16_hi v31,v31
ds_read_b64_tr_b16 a[236:237],v3 offset:136
v_lshlrev_b32_sdwa v24,1,v24 dst_sel:DWORD dst_unused:UNUSED_PAD src0_sel:DWORD src1_sel:WORD_1
v_mfma_f32_16x16x32_bf16 v[36:39],a[216:219],a[16:19],v[36:39]
s_waitcnt lgkmcnt(10)
ds_read_b64_tr_b16 a[238:239],v3 offset:1672
v_lshl_add_u64 v[48:49],v[48:49],0,v[50:51]
v_mfma_f32_16x16x32_bf16 v[36:39],a[192:195],a[32:35],v[36:39]
s_add_u32 s69,s69,s59
ds_read_u16 v24,v24
v_lshl_add_u64 v[50:51],v[44:45],0,v[46:47]
v_lshlrev_b32_sdwa v26,1,v26 dst_sel:DWORD dst_unused:UNUSED_PAD src0_sel:DWORD src1_sel:WORD_1
v_mfma_f32_16x16x32_bf16 v[40:43],a[196:199],a[32:35],v[20:23]
buffer_load_dwordx4 v5,s[44:47],s69 offen offset:0 lds
v_lshlrev_b32_sdwa v25,1,v25 dst_sel:DWORD dst_unused:UNUSED_PAD src0_sel:DWORD src1_sel:WORD_1
v_mfma_f32_16x16x32_bf16 v[40:43],a[200:203],a[16:19],v[40:43]
s_add_u32 s69,s69,s53
ds_read_u16_d16_hi v26,v26
v_lshl_add_u64 v[44:45],v[52:53],0,v[54:55]
v_mfma_f32_16x16x32_bf16 v[40:43],a[212:215],a[0:3],v[40:43]
buffer_load_dwordx4 v5,s[44:47],s69 offen offset:1024 lds
v_lshlrev_b32_sdwa v27,1,v27 dst_sel:DWORD dst_unused:UNUSED_PAD src0_sel:DWORD src1_sel:WORD_1
v_mfma_f32_16x16x32_bf16 v[52:55],a[212:215],a[4:7],v[20:23]
s_waitcnt lgkmcnt(7)
ds_read_u16 v25,v25
v_or_b32 v46,v56,v58
v_mfma_f32_16x16x32_bf16 v[52:55],a[200:203],a[20:23],v[52:55]
s_add_u32 s69,s69,s53
ds_read_u16_d16_hi v27,v27
v_cvt_pk_bf16_f32 v47,v57,v59
v_lshlrev_b32_sdwa v60,1,v60 dst_sel:DWORD dst_unused:UNUSED_PAD src0_sel:DWORD src1_sel:WORD_1
v_mfma_f32_16x16x32_bf16 v[52:55],a[196:199],a[36:39],v[52:55]
buffer_load_dwordx4 v5,s[44:47],s69 offen offset:2048 lds
v_lshlrev_b32_sdwa v62,1,v62 dst_sel:DWORD dst_unused:UNUSED_PAD src0_sel:DWORD src1_sel:WORD_1
v_mfma_f32_16x16x32_bf16 v[56:59],a[192:195],a[36:39],v[16:19]
ds_read_u16 v60,v60
v_lshlrev_b32_sdwa v61,1,v61 dst_sel:DWORD dst_unused:UNUSED_PAD src0_sel:DWORD src1_sel:WORD_1
v_mfma_f32_16x16x32_bf16 v[56:59],a[216:219],a[20:23],v[56:59]
ds_read_u16_d16_hi v62,v62
v_lshlrev_b32_sdwa v63,1,v63 dst_sel:DWORD dst_unused:UNUSED_PAD src0_sel:DWORD src1_sel:WORD_1
v_mfma_f32_16x16x32_bf16 v[56:59],a[220:223],a[4:7],v[56:59]
ds_read_u16 v61,v61
v_lshlrev_b32_sdwa v32,1,v32 dst_sel:DWORD dst_unused:UNUSED_PAD src0_sel:DWORD src1_sel:WORD_1
v_mfma_f32_16x16x32_bf16 v[232:235],a[240:243],v[48:51],v[232:235]
ds_read_u16_d16_hi v63,v63
v_lshlrev_b32_sdwa v34,1,v34 dst_sel:DWORD dst_unused:UNUSED_PAD src0_sel:DWORD src1_sel:WORD_1
v_mfma_f32_16x16x32_bf16 v[236:239],a[240:243],v[44:47],v[236:239]
ds_read_u16 v32,v32
v_mfma_f32_16x16x32_bf16 v[164:167],a[224:227],v[44:47],v[164:167]
ds_read_u16_d16_hi v34,v34
v_lshlrev_b32_sdwa v33,1,v33 dst_sel:DWORD dst_unused:UNUSED_PAD src0_sel:DWORD src1_sel:WORD_1
v_mfma_f32_16x16x32_bf16 v[160:163],a[224:227],v[48:51],v[160:163]
ds_read_u16 v33,v33
v_mfma_f32_16x16x32_bf16 v[240:243],a[248:251],v[48:51],v[240:243]
ds_read_b64_tr_b16 a[208:209],v1 offset:2048
v_lshlrev_b32_sdwa v35,1,v35 dst_sel:DWORD dst_unused:UNUSED_PAD src0_sel:DWORD src1_sel:WORD_1
v_mfma_f32_16x16x32_bf16 v[244:247],a[248:251],v[44:47],v[244:247]
ds_read_u16_d16_hi v35,v35
v_lshlrev_b32_sdwa v36,1,v36 dst_sel:DWORD dst_unused:UNUSED_PAD src0_sel:DWORD src1_sel:WORD_1
v_mfma_f32_16x16x32_bf16 v[180:183],a[228:231],v[44:47],v[180:183]
ds_read_u16 v36,v36
v_mfma_f32_16x16x32_bf16 v[176:179],a[228:231],v[48:51],v[176:179]
s_waitcnt lgkmcnt(8)
ds_read_b64_tr_b16 a[210:211],v1 offset:3072
v_lshlrev_b32_sdwa v38,1,v38 dst_sel:DWORD dst_unused:UNUSED_PAD src0_sel:DWORD src1_sel:WORD_1
v_mfma_f32_16x16x32_bf16 v[248:251],a[232:235],v[48:51],v[248:251]
ds_read_u16_d16_hi v38,v38
v_lshlrev_b32_sdwa v37,1,v37 dst_sel:DWORD dst_unused:UNUSED_PAD src0_sel:DWORD src1_sel:WORD_1
v_mfma_f32_16x16x32_bf16 v[252:255],a[232:235],v[44:47],v[252:255]
ds_read_u16 v37,v37
v_mfma_f32_16x16x32_bf16 v[196:199],a[236:239],v[44:47],v[196:199]
v_lshlrev_b32_sdwa v39,1,v39 dst_sel:DWORD dst_unused:UNUSED_PAD src0_sel:DWORD src1_sel:WORD_1
v_mfma_f32_16x16x32_bf16 v[192:195],a[236:239],v[48:51],v[192:195]
TP6995_%=:
s_cbranch_execz TR0_%=
ds_read_u16_d16_hi v39,v39
v_mfma_f32_16x16x32_bf16 v[44:47],a[220:223],a[8:11],v[16:19]
s_nop 0
v_mul_f32_dpp v232,v14,v232 row_newbcast:0 row_mask:0xf bank_mask:0xf
s_add_u32 s10,s61,1572864
v_mul_f32_dpp v233,v14,v233 row_newbcast:1 row_mask:0xf bank_mask:0xf
v_mul_f32_dpp v234,v14,v234 row_newbcast:2 row_mask:0xf bank_mask:0xf
v_lshlrev_b32_sdwa v40,1,v40 dst_sel:DWORD dst_unused:UNUSED_PAD src0_sel:DWORD src1_sel:WORD_1
v_mfma_f32_16x16x32_bf16 v[44:47],a[216:219],a[24:27],v[44:47]
ds_read_u16 v40,v40
v_lshl_add_u64 v[28:29],v[28:29],0,v[30:31]
v_mul_f32_dpp v235,v14,v235 row_newbcast:3 row_mask:0xf bank_mask:0xf
v_mul_f32_dpp v160,v14,v160 row_newbcast:4 row_mask:0xf bank_mask:0xf
v_mul_f32_dpp v161,v14,v161 row_newbcast:5 row_mask:0xf bank_mask:0xf
v_mfma_f32_16x16x32_bf16 v[44:47],a[192:195],a[40:43],v[44:47]
v_lshl_add_u64 v[30:31],v[24:25],0,v[26:27]
v_mul_f32_dpp v162,v14,v162 row_newbcast:6 row_mask:0xf bank_mask:0xf
v_mul_f32_dpp v163,v14,v163 row_newbcast:7 row_mask:0xf bank_mask:0xf
v_lshlrev_b32_sdwa v42,1,v42 dst_sel:DWORD dst_unused:UNUSED_PAD src0_sel:DWORD src1_sel:WORD_1
v_cvt_pk_bf16_f32 v232,v232,v233
v_mfma_f32_16x16x32_bf16 v[48:51],a[196:199],a[40:43],v[20:23]
s_nop 0
ds_read_u16_d16_hi v42,v42
v_cvt_pk_bf16_f32 v233,v234,v235
v_cvt_pk_bf16_f32 v234,v160,v161
v_lshlrev_b32_sdwa v41,1,v41 dst_sel:DWORD dst_unused:UNUSED_PAD src0_sel:DWORD src1_sel:WORD_1
v_cvt_pk_bf16_f32 v235,v162,v163
v_mfma_f32_16x16x32_bf16 v[48:51],a[200:203],a[24:27],v[48:51]
s_waitcnt lgkmcnt(5)
buffer_store_dwordx4 v[232:235],v7,s[40:43],s10 offen offset:0 sc1
v_lshl_add_u64 v[24:25],v[60:61],0,v[62:63]
v_mul_f32_dpp v240,v14,v240 row_newbcast:8 row_mask:0xf bank_mask:0xf
v_mul_f32_dpp v241,v14,v241 row_newbcast:9 row_mask:0xf bank_mask:0xf
v_lshlrev_b32_sdwa v43,1,v43 dst_sel:DWORD dst_unused:UNUSED_PAD src0_sel:DWORD src1_sel:WORD_1
v_mul_f32_dpp v242,v14,v242 row_newbcast:10 row_mask:0xf bank_mask:0xf
v_mfma_f32_16x16x32_bf16 v[48:51],a[212:215],a[8:11],v[48:51]
ds_read_u16 v41,v41
v_mul_f32_dpp v243,v14,v243 row_newbcast:11 row_mask:0xf bank_mask:0xf
v_mul_f32_dpp v176,v14,v176 row_newbcast:12 row_mask:0xf bank_mask:0xf
v_mul_f32_dpp v177,v14,v177 row_newbcast:13 row_mask:0xf bank_mask:0xf
v_mfma_f32_16x16x32_bf16 v[60:63],a[212:215],a[12:15],v[20:23]
ds_read_b128 v[20:23],v4 offset:640
v_mul_f32_dpp v178,v14,v178 row_newbcast:14 row_mask:0xf bank_mask:0xf
v_mul_f32_dpp v179,v14,v179 row_newbcast:15 row_mask:0xf bank_mask:0xf
v_lshlrev_b32_sdwa v56,1,v56 dst_sel:DWORD dst_unused:UNUSED_PAD src0_sel:DWORD src1_sel:WORD_1
v_cvt_pk_bf16_f32 v240,v240,v241
v_mfma_f32_16x16x32_bf16 v[60:63],a[200:203],a[28:31],v[60:63]
ds_read_u16_d16_hi v43,v43
ds_read_b64_tr_b16 a[212:213],v1 offset:4096
v_lshl_add_u64 v[26:27],v[32:33],0,v[34:35]
v_cvt_pk_bf16_f32 v241,v242,v243
v_cvt_pk_bf16_f32 v242,v176,v177
v_lshlrev_b32_sdwa v58,1,v58 dst_sel:DWORD dst_unused:UNUSED_PAD src0_sel:DWORD src1_sel:WORD_1
v_cvt_pk_bf16_f32 v243,v178,v179
v_mfma_f32_16x16x32_bf16 v[60:63],a[196:199],a[44:47],v[60:63]
ds_read_b64_tr_b16 a[214:215],v1 offset:5120
ds_read_b64_tr_b16 a[200:201],v2 offset:4096
v_mul_f32_dpp v248,v15,v248 row_newbcast:0 row_mask:0xf bank_mask:0xf
v_mul_f32_dpp v249,v15,v249 row_newbcast:1 row_mask:0xf bank_mask:0xf
v_lshlrev_b32_sdwa v57,1,v57 dst_sel:DWORD dst_unused:UNUSED_PAD src0_sel:DWORD src1_sel:WORD_1
v_mul_f32_dpp v250,v15,v250 row_newbcast:2 row_mask:0xf bank_mask:0xf
v_mfma_f32_16x16x32_bf16 v[32:35],a[192:195],a[44:47],v[16:19]
ds_read_b128 v[16:19],v4 offset:672
ds_read_b64_tr_b16 a[202:203],v2 offset:5120
v_mul_f32_dpp v251,v15,v251 row_newbcast:3 row_mask:0xf bank_mask:0xf
v_mul_f32_dpp v192,v15,v192 row_newbcast:4 row_mask:0xf bank_mask:0xf
v_lshlrev_b32_sdwa v59,1,v59 dst_sel:DWORD dst_unused:UNUSED_PAD src0_sel:DWORD src1_sel:WORD_1
v_mul_f32_dpp v193,v15,v193 row_newbcast:5 row_mask:0xf bank_mask:0xf
v_mfma_f32_16x16x32_bf16 v[32:35],a[216:219],a[28:31],v[32:35]
ds_read_u16 v56,v56
ds_read_u16_d16_hi v58,v58
v_mul_f32_dpp v194,v15,v194 row_newbcast:6 row_mask:0xf bank_mask:0xf
v_mul_f32_dpp v195,v15,v195 row_newbcast:7 row_mask:0xf bank_mask:0xf
v_cvt_pk_bf16_f32 v248,v248,v249
v_mfma_f32_16x16x32_bf16 v[32:35],a[220:223],a[12:15],v[32:35]
ds_read_b64_tr_b16 a[196:197],v2 offset:2048
ds_read_b64_tr_b16 a[198:199],v2 offset:3072
v_cvt_pk_bf16_f32 v249,v250,v251
v_cvt_pk_bf16_f32 v250,v192,v193
v_lshlrev_b32_sdwa v52,1,v52 dst_sel:DWORD dst_unused:UNUSED_PAD src0_sel:DWORD src1_sel:WORD_1
v_cvt_pk_bf16_f32 v251,v194,v195
v_mfma_f32_16x16x32_bf16 v[208:211],a[240:243],v[28:31],v[208:211]
ds_read_u16 v57,v57
ds_read_u16_d16_hi v59,v59
v_mul_f32_dpp v236,v14,v236 row_newbcast:0 row_mask:0xf bank_mask:0xf
v_mul_f32_dpp v237,v14,v237 row_newbcast:1 row_mask:0xf bank_mask:0xf
v_lshlrev_b32_sdwa v54,1,v54 dst_sel:DWORD dst_unused:UNUSED_PAD src0_sel:DWORD src1_sel:WORD_1
v_mul_f32_dpp v238,v14,v238 row_newbcast:2 row_mask:0xf bank_mask:0xf
v_mfma_f32_16x16x32_bf16 v[212:215],a[240:243],v[24:27],v[212:215]
ds_read_b64_tr_b16 a[192:193],v2 offset:0
v_mul_f32_dpp v239,v14,v239 row_newbcast:3 row_mask:0xf bank_mask:0xf
v_mul_f32_dpp v164,v14,v164 row_newbcast:4 row_mask:0xf bank_mask:0xf
v_lshlrev_b32_sdwa v53,1,v53 dst_sel:DWORD dst_unused:UNUSED_PAD src0_sel:DWORD src1_sel:WORD_1
v_mul_f32_dpp v165,v14,v165 row_newbcast:5 row_mask:0xf bank_mask:0xf
v_mfma_f32_16x16x32_bf16 v[172:175],a[224:227],v[24:27],v[172:175]
ds_read_b64_tr_b16 a[194:195],v2 offset:1024
v_mul_f32_dpp v166,v14,v166 row_newbcast:6 row_mask:0xf bank_mask:0xf
v_mul_f32_dpp v167,v14,v167 row_newbcast:7 row_mask:0xf bank_mask:0xf
v_lshlrev_b32_sdwa v55,1,v55 dst_sel:DWORD dst_unused:UNUSED_PAD src0_sel:DWORD src1_sel:WORD_1
v_cvt_pk_bf16_f32 v236,v236,v237
v_mfma_f32_16x16x32_bf16 v[168:171],a[224:227],v[28:31],v[168:171]
buffer_store_dwordx4 v[240:243],v7,s[40:43],s10 offen offset:64 sc1
ds_read_u16 v52,v52
v_cvt_pk_bf16_f32 v237,v238,v239
v_cvt_pk_bf16_f32 v238,v164,v165
v_cvt_pk_bf16_f32 v239,v166,v167
v_mfma_f32_16x16x32_bf16 v[216:219],a[248:251],v[28:31],v[216:219]
ds_read_u16_d16_hi v54,v54
v_mul_f32_dpp v244,v14,v244 row_newbcast:8 row_mask:0xf bank_mask:0xf
v_mul_f32_dpp v245,v14,v245 row_newbcast:9 row_mask:0xf bank_mask:0xf
v_lshlrev_b32_sdwa v44,1,v44 dst_sel:DWORD dst_unused:UNUSED_PAD src0_sel:DWORD src1_sel:WORD_1
v_mul_f32_dpp v246,v14,v246 row_newbcast:10 row_mask:0xf bank_mask:0xf
v_mfma_f32_16x16x32_bf16 v[220:223],a[248:251],v[24:27],v[220:223]
ds_read_u16 v53,v53
v_mul_f32_dpp v247,v14,v247 row_newbcast:11 row_mask:0xf bank_mask:0xf
v_mul_f32_dpp v180,v14,v180 row_newbcast:12 row_mask:0xf bank_mask:0xf
v_lshlrev_b32_sdwa v46,1,v46 dst_sel:DWORD dst_unused:UNUSED_PAD src0_sel:DWORD src1_sel:WORD_1
v_mul_f32_dpp v181,v14,v181 row_newbcast:13 row_mask:0xf bank_mask:0xf
v_mfma_f32_16x16x32_bf16 v[188:191],a[228:231],v[24:27],v[188:191]
ds_read_u16_d16_hi v55,v55
v_mul_f32_dpp v182,v14,v182 row_newbcast:14 row_mask:0xf bank_mask:0xf
v_mul_f32_dpp v183,v14,v183 row_newbcast:15 row_mask:0xf bank_mask:0xf
v_lshlrev_b32_sdwa v45,1,v45 dst_sel:DWORD dst_unused:UNUSED_PAD src0_sel:DWORD src1_sel:WORD_1
v_cvt_pk_bf16_f32 v244,v244,v245
v_mfma_f32_16x16x32_bf16 v[184:187],a[228:231],v[28:31],v[184:187]
ds_read_u16 v44,v44
v_cvt_pk_bf16_f32 v245,v246,v247
v_cvt_pk_bf16_f32 v246,v180,v181
v_lshlrev_b32_sdwa v47,1,v47 dst_sel:DWORD dst_unused:UNUSED_PAD src0_sel:DWORD src1_sel:WORD_1
v_cvt_pk_bf16_f32 v247,v182,v183
v_mfma_f32_16x16x32_bf16 v[224:227],a[232:235],v[28:31],v[224:227]
ds_read_u16_d16_hi v46,v46
v_mul_f32_dpp v252,v15,v252 row_newbcast:0 row_mask:0xf bank_mask:0xf
v_mul_f32_dpp v253,v15,v253 row_newbcast:1 row_mask:0xf bank_mask:0xf
v_mul_f32_dpp v254,v15,v254 row_newbcast:2 row_mask:0xf bank_mask:0xf
v_mfma_f32_16x16x32_bf16 v[228:231],a[232:235],v[24:27],v[228:231]
ds_read_u16 v45,v45
v_mul_f32_dpp v255,v15,v255 row_newbcast:3 row_mask:0xf bank_mask:0xf
v_mul_f32_dpp v196,v15,v196 row_newbcast:4 row_mask:0xf bank_mask:0xf
v_lshlrev_b32_sdwa v48,1,v48 dst_sel:DWORD dst_unused:UNUSED_PAD src0_sel:DWORD src1_sel:WORD_1
v_mul_f32_dpp v197,v15,v197 row_newbcast:5 row_mask:0xf bank_mask:0xf
v_mfma_f32_16x16x32_bf16 v[204:207],a[236:239],v[24:27],v[204:207]
s_nop 0
ds_read_u16_d16_hi v47,v47
v_mul_f32_dpp v198,v15,v198 row_newbcast:6 row_mask:0xf bank_mask:0xf
v_mul_f32_dpp v199,v15,v199 row_newbcast:7 row_mask:0xf bank_mask:0xf
v_lshlrev_b32_sdwa v50,1,v50 dst_sel:DWORD dst_unused:UNUSED_PAD src0_sel:DWORD src1_sel:WORD_1
v_cvt_pk_bf16_f32 v252,v252,v253
v_mfma_f32_16x16x32_bf16 v[200:203],a[236:239],v[28:31],v[200:203]
s_waitcnt lgkmcnt(8)
ds_read_u16 v48,v48
v_cvt_pk_bf16_f32 v253,v254,v255
v_cvt_pk_bf16_f32 v254,v196,v197
v_lshlrev_b32_sdwa v49,1,v49 dst_sel:DWORD dst_unused:UNUSED_PAD src0_sel:DWORD src1_sel:WORD_1
v_cvt_pk_bf16_f32 v255,v198,v199
v_lshlrev_b32_sdwa v51,1,v51 dst_sel:DWORD dst_unused:UNUSED_PAD src0_sel:DWORD src1_sel:WORD_1
v_mfma_f32_16x16x32_bf16 v[24:27],a[204:207],a[0:3],v[20:23]
ds_read_u16_d16_hi v50,v50
v_mul_f32_dpp v208,v14,v208 row_newbcast:0 row_mask:0xf bank_mask:0xf
s_add_u32 s11,s61,1671168
v_accvgpr_write_b32 a64,0
v_accvgpr_write_b32 a65,0
v_lshlrev_b32_sdwa v32,1,v32 dst_sel:DWORD dst_unused:UNUSED_PAD src0_sel:DWORD src1_sel:WORD_1
v_mfma_f32_16x16x32_bf16 v[24:27],a[208:211],a[16:19],v[24:27]
ds_read_u16 v49,v49
v_lshl_add_u64 v[36:37],v[36:37],0,v[38:39]
v_accvgpr_write_b32 a66,0
v_accvgpr_write_b32 a67,0
v_mul_f32_dpp v209,v14,v209 row_newbcast:1 row_mask:0xf bank_mask:0xf
v_mfma_f32_16x16x32_bf16 v[24:27],a[212:215],a[32:35],v[24:27]
buffer_store_dwordx4 v[248:251],v7,s[40:43],s10 offen offset:128 sc1
ds_read_u16_d16_hi v51,v51
v_lshl_add_u64 v[38:39],v[40:41],0,v[42:43]
v_mul_f32_dpp v210,v14,v210 row_newbcast:2 row_mask:0xf bank_mask:0xf
v_mul_f32_dpp v211,v14,v211 row_newbcast:3 row_mask:0xf bank_mask:0xf
v_lshlrev_b32_sdwa v34,1,v34 dst_sel:DWORD dst_unused:UNUSED_PAD src0_sel:DWORD src1_sel:WORD_1
v_mul_f32_dpp v168,v14,v168 row_newbcast:4 row_mask:0xf bank_mask:0xf
v_mfma_f32_16x16x32_bf16 v[28:31],a[200:203],a[32:35],v[16:19]
ds_read_u16 v32,v32
v_mul_f32_dpp v169,v14,v169 row_newbcast:5 row_mask:0xf bank_mask:0xf
v_mul_f32_dpp v170,v14,v170 row_newbcast:6 row_mask:0xf bank_mask:0xf
v_lshlrev_b32_sdwa v33,1,v33 dst_sel:DWORD dst_unused:UNUSED_PAD src0_sel:DWORD src1_sel:WORD_1
v_mul_f32_dpp v171,v14,v171 row_newbcast:7 row_mask:0xf bank_mask:0xf
v_mfma_f32_16x16x32_bf16 v[28:31],a[196:199],a[16:19],v[28:31]
ds_read_u16_d16_hi v34,v34
v_lshl_add_u64 v[40:41],v[56:57],0,v[58:59]
v_cvt_pk_bf16_f32 v208,v208,v209
v_cvt_pk_bf16_f32 v209,v210,v211
v_lshlrev_b32_sdwa v35,1,v35 dst_sel:DWORD dst_unused:UNUSED_PAD src0_sel:DWORD src1_sel:WORD_1
v_cvt_pk_bf16_f32 v210,v168,v169
v_mfma_f32_16x16x32_bf16 v[28:31],a[192:195],a[0:3],v[28:31]
ds_read_u16 v33,v33
v_cvt_pk_bf16_f32 v211,v170,v171
v_mul_f32_dpp v216,v14,v216 row_newbcast:8 row_mask:0xf bank_mask:0xf
v_lshlrev_b32_sdwa v60,1,v60 dst_sel:DWORD dst_unused:UNUSED_PAD src0_sel:DWORD src1_sel:WORD_1
v_accvgpr_write_b32 a72,v208
v_mfma_f32_16x16x32_bf16 v[56:59],a[192:195],a[4:7],v[16:19]
s_nop 0
ds_read_u16_d16_hi v35,v35
v_mul_f32_dpp v217,v14,v217 row_newbcast:9 row_mask:0xf bank_mask:0xf
v_mul_f32_dpp v218,v14,v218 row_newbcast:10 row_mask:0xf bank_mask:0xf
v_lshlrev_b32_sdwa v62,1,v62 dst_sel:DWORD dst_unused:UNUSED_PAD src0_sel:DWORD src1_sel:WORD_1
v_accvgpr_write_b32 a73,v209
v_mfma_f32_16x16x32_bf16 v[56:59],a[196:199],a[20:23],v[56:59]
s_waitcnt lgkmcnt(8)
ds_read_u16 v60,v60
v_lshl_add_u64 v[42:43],v[52:53],0,v[54:55]
v_mul_f32_dpp v219,v14,v219 row_newbcast:11 row_mask:0xf bank_mask:0xf
v_mul_f32_dpp v184,v14,v184 row_newbcast:12 row_mask:0xf bank_mask:0xf
v_lshlrev_b32_sdwa v61,1,v61 dst_sel:DWORD dst_unused:UNUSED_PAD src0_sel:DWORD src1_sel:WORD_1
v_accvgpr_write_b32 a74,v210
v_mfma_f32_16x16x32_bf16 v[56:59],a[200:203],a[36:39],v[56:59]
s_waitcnt vmcnt(9)
s_barrier
ds_read_u16_d16_hi v62,v62
v_mul_f32_dpp v185,v14,v185 row_newbcast:13 row_mask:0xf bank_mask:0xf
v_mul_f32_dpp v186,v14,v186 row_newbcast:14 row_mask:0xf bank_mask:0xf
v_lshlrev_b32_sdwa v63,1,v63 dst_sel:DWORD dst_unused:UNUSED_PAD src0_sel:DWORD src1_sel:WORD_1
v_accvgpr_write_b32 a75,v211
v_mfma_f32_16x16x32_bf16 v[52:55],a[212:215],a[36:39],v[20:23]
ds_read_u16 v61,v61
v_mul_f32_dpp v187,v14,v187 row_newbcast:15 row_mask:0xf bank_mask:0xf
v_cvt_pk_bf16_f32 v216,v216,v217
v_lshlrev_b32_sdwa v24,1,v24 dst_sel:DWORD dst_unused:UNUSED_PAD src0_sel:DWORD src1_sel:WORD_1
v_cvt_pk_bf16_f32 v217,v218,v219
v_mfma_f32_16x16x32_bf16 v[52:55],a[208:211],a[20:23],v[52:55]
s_add_u32 m0,s58,0
ds_read_u16_d16_hi v63,v63
v_cvt_pk_bf16_f32 v218,v184,v185
v_cvt_pk_bf16_f32 v219,v186,v187
v_lshlrev_b32_sdwa v26,1,v26 dst_sel:DWORD dst_unused:UNUSED_PAD src0_sel:DWORD src1_sel:WORD_1
v_mul_f32_dpp v224,v15,v224 row_newbcast:0 row_mask:0xf bank_mask:0xf
v_mfma_f32_16x16x32_bf16 v[52:55],a[204:207],a[4:7],v[52:55]
s_add_u32 s69,s69,s59
ds_read_u16 v24,v24
v_accvgpr_write_b32 a76,v216
v_mul_f32_dpp v225,v15,v225 row_newbcast:1 row_mask:0xf bank_mask:0xf
v_lshlrev_b32_sdwa v25,1,v25 dst_sel:DWORD dst_unused:UNUSED_PAD src0_sel:DWORD src1_sel:WORD_1
v_mul_f32_dpp v226,v15,v226 row_newbcast:2 row_mask:0xf bank_mask:0xf
v_mfma_f32_16x16x32_bf16 v[136:139],a[240:243],v[36:39],v[136:139]
buffer_load_dwordx4 v5,s[44:47],s69 offen offset:0 lds
ds_read_u16_d16_hi v26,v26
v_accvgpr_write_b32 a77,v217
v_mul_f32_dpp v227,v15,v227 row_newbcast:3 row_mask:0xf bank_mask:0xf
v_lshlrev_b32_sdwa v27,1,v27 dst_sel:DWORD dst_unused:UNUSED_PAD src0_sel:DWORD src1_sel:WORD_1
v_mul_f32_dpp v200,v15,v200 row_newbcast:4 row_mask:0xf bank_mask:0xf
v_mfma_f32_16x16x32_bf16 v[140:143],a[240:243],v[40:43],v[140:143]
s_add_u32 s69,s69,s53
ds_read_u16 v25,v25
v_accvgpr_write_b32 a78,v218
v_mul_f32_dpp v201,v15,v201 row_newbcast:5 row_mask:0xf bank_mask:0xf
v_lshlrev_b32_sdwa v28,1,v28 dst_sel:DWORD dst_unused:UNUSED_PAD src0_sel:DWORD src1_sel:WORD_1
v_mul_f32_dpp v202,v15,v202 row_newbcast:6 row_mask:0xf bank_mask:0xf
v_mfma_f32_16x16x32_bf16 v[68:71],a[224:227],v[40:43],v[68:71]
s_nop 0
buffer_load_dwordx4 v5,s[44:47],s69 offen offset:1024 lds
ds_read_u16_d16_hi v27,v27
v_accvgpr_write_b32 a79,v219
v_mul_f32_dpp v203,v15,v203 row_newbcast:7 row_mask:0xf bank_mask:0xf
v_lshlrev_b32_sdwa v30,1,v30 dst_sel:DWORD dst_unused:UNUSED_PAD src0_sel:DWORD src1_sel:WORD_1
v_cvt_pk_bf16_f32 v224,v224,v225
v_mfma_f32_16x16x32_bf16 v[64:67],a[224:227],v[36:39],v[64:67]
s_nop 0
buffer_store_dwordx4 v[236:239],v7,s[40:43],s10 offen offset:192 sc1
ds_read_u16 v28,v28
v_cvt_pk_bf16_f32 v225,v226,v227
v_cvt_pk_bf16_f32 v226,v200,v201
v_lshlrev_b32_sdwa v29,1,v29 dst_sel:DWORD dst_unused:UNUSED_PAD src0_sel:DWORD src1_sel:WORD_1
v_cvt_pk_bf16_f32 v227,v202,v203
v_mfma_f32_16x16x32_bf16 v[144:147],a[248:251],v[36:39],v[144:147]
s_add_u32 s69,s69,s53
ds_read_u16_d16_hi v30,v30
ds_read_u16 v29,v29
v_mul_f32_dpp v212,v14,v212 row_newbcast:0 row_mask:0xf bank_mask:0xf
v_accvgpr_write_b32 a80,v224
v_lshlrev_b32_sdwa v31,1,v31 dst_sel:DWORD dst_unused:UNUSED_PAD src0_sel:DWORD src1_sel:WORD_1
v_mul_f32_dpp v213,v14,v213 row_newbcast:1 row_mask:0xf bank_mask:0xf
v_mfma_f32_16x16x32_bf16 v[148:151],a[248:251],v[40:43],v[148:151]
buffer_load_dwordx4 v5,s[44:47],s69 offen offset:2048 lds
ds_read_u16_d16_hi v31,v31
v_mul_f32_dpp v214,v14,v214 row_newbcast:2 row_mask:0xf bank_mask:0xf
v_accvgpr_write_b32 a81,v225
v_lshlrev_b32_sdwa v56,1,v56 dst_sel:DWORD dst_unused:UNUSED_PAD src0_sel:DWORD src1_sel:WORD_1
v_mul_f32_dpp v215,v14,v215 row_newbcast:3 row_mask:0xf bank_mask:0xf
v_mfma_f32_16x16x32_bf16 v[84:87],a[228:231],v[40:43],v[84:87]
ds_read_u16 v56,v56
ds_read_b64_tr_b16 a[244:245],v3 offset:6160
v_mul_f32_dpp v172,v14,v172 row_newbcast:4 row_mask:0xf bank_mask:0xf
v_accvgpr_write_b32 a82,v226
v_lshlrev_b32_sdwa v58,1,v58 dst_sel:DWORD dst_unused:UNUSED_PAD src0_sel:DWORD src1_sel:WORD_1
v_mul_f32_dpp v173,v14,v173 row_newbcast:5 row_mask:0xf bank_mask:0xf
v_mfma_f32_16x16x32_bf16 v[80:83],a[228:231],v[36:39],v[80:83]
ds_read_u16_d16_hi v58,v58
v_mul_f32_dpp v174,v14,v174 row_newbcast:6 row_mask:0xf bank_mask:0xf
v_accvgpr_write_b32 a83,v227
v_lshlrev_b32_sdwa v57,1,v57 dst_sel:DWORD dst_unused:UNUSED_PAD src0_sel:DWORD src1_sel:WORD_1
v_mul_f32_dpp v175,v14,v175 row_newbcast:7 row_mask:0xf bank_mask:0xf
v_mfma_f32_16x16x32_bf16 v[152:155],a[232:235],v[36:39],v[152:155]
ds_read_u16 v57,v57
v_cvt_pk_bf16_f32 v212,v212,v213
v_cvt_pk_bf16_f32 v213,v214,v215
v_lshlrev_b32_sdwa v59,1,v59 dst_sel:DWORD dst_unused:UNUSED_PAD src0_sel:DWORD src1_sel:WORD_1
v_cvt_pk_bf16_f32 v214,v172,v173
v_mfma_f32_16x16x32_bf16 v[156:159],a[232:235],v[40:43],v[156:159]
ds_read_u16_d16_hi v59,v59
v_cvt_pk_bf16_f32 v215,v174,v175
v_mfma_f32_32x32x16_bf16 v[160:175],a[64:67],a[64:67],0
v_lshlrev_b32_sdwa v52,1,v52 dst_sel:DWORD dst_unused:UNUSED_PAD src0_sel:DWORD src1_sel:WORD_1
v_mul_f32_dpp v220,v14,v220 row_newbcast:8 row_mask:0xf bank_mask:0xf
v_lshlrev_b32_sdwa v54,1,v54 dst_sel:DWORD dst_unused:UNUSED_PAD src0_sel:DWORD src1_sel:WORD_1
v_accvgpr_write_b32 a84,v212
v_mfma_f32_16x16x32_bf16 v[100:103],a[236:239],v[40:43],v[100:103]
ds_read_u16 v52,v52
ds_read_u16_d16_hi v54,v54
v_mul_f32_dpp v221,v14,v221 row_newbcast:9 row_mask:0xf bank_mask:0xf
v_mul_f32_dpp v222,v14,v222 row_newbcast:10 row_mask:0xf bank_mask:0xf
v_mfma_f32_16x16x32_bf16 v[96:99],a[236:239],v[36:39],v[96:99]
ds_read_b64_tr_b16 a[246:247],v3 offset:7696
v_accvgpr_write_b32 a85,v213
v_mul_f32_dpp v223,v14,v223 row_newbcast:11 row_mask:0xf bank_mask:0xf
v_lshlrev_b32_sdwa v53,1,v53 dst_sel:DWORD dst_unused:UNUSED_PAD src0_sel:DWORD src1_sel:WORD_1
v_mul_f32_dpp v188,v14,v188 row_newbcast:12 row_mask:0xf bank_mask:0xf
v_lshlrev_b32_sdwa v55,1,v55 dst_sel:DWORD dst_unused:UNUSED_PAD src0_sel:DWORD src1_sel:WORD_1
ds_read_u16 v53,v53
ds_read_u16_d16_hi v55,v55
ds_read_b64_tr_b16 a[252:253],v3 offset:6168
ds_read_b64_tr_b16 a[254:255],v3 offset:7704
v_add_u32_sdwa v36,s60,v10 dst_sel:DWORD dst_unused:UNUSED_PAD src0_sel:DWORD src1_sel:WORD_1
v_subrev_u32 v8,88400,v36
v_min_u32_e64 v36,v36,v8
v_add_u32 v37,7072,v36
v_subrev_u32 v8,88400,v37
v_min_u32 v37,v37,v8
v_lshl_add_u64 v[44:45],v[44:45],0,v[46:47]
s_waitcnt lgkmcnt(15)
v_lshl_add_u64 v[46:47],v[48:49],0,v[50:51]
v_lshl_add_u64 v[48:49],v[32:33],0,v[34:35]
v_lshl_add_u64 v[50:51],v[60:61],0,v[62:63]
v_mfma_f32_16x16x32_bf16 v[112:115],a[240:243],v[44:47],v[112:115]
buffer_store_dwordx4 v[244:247],v7,s[40:43],s10 offen offset:256 sc1
v_xad_u32 v40,v11,0,v36
v_xad_u32 v41,v11,8,v37
v_xad_u32 v42,v11,16,v36
v_xad_u32 v43,v11,24,v37
v_mfma_f32_16x16x32_bf16 v[116:119],a[240:243],v[48:51],v[116:119]
ds_read_b64 a[48:49],v40 offset:0
v_accvgpr_write_b32 a86,v214
v_mul_f32_dpp v189,v14,v189 row_newbcast:13 row_mask:0xf bank_mask:0xf
v_mul_f32_dpp v190,v14,v190 row_newbcast:14 row_mask:0xf bank_mask:0xf
v_accvgpr_write_b32 a87,v215
v_mfma_f32_16x16x32_bf16 v[76:79],a[224:227],v[48:51],v[76:79]
ds_read_b64 a[50:51],v40 offset:192
v_mul_f32_dpp v191,v14,v191 row_newbcast:15 row_mask:0xf bank_mask:0xf
v_cvt_pk_bf16_f32 v220,v220,v221
v_cvt_pk_bf16_f32 v221,v222,v223
v_mfma_f32_16x16x32_bf16 v[72:75],a[224:227],v[44:47],v[72:75]
ds_read_b64 a[52:53],v41 offset:0
v_cvt_pk_bf16_f32 v222,v188,v189
v_cvt_pk_bf16_f32 v223,v190,v191
v_mfma_f32_32x32x16_bf16 v[176:191],a[64:67],a[64:67],0
v_mfma_f32_16x16x32_bf16 v[120:123],a[248:251],v[44:47],v[120:123]
ds_read_b64 a[54:55],v41 offset:192
v_mul_f32_dpp v228,v15,v228 row_newbcast:0 row_mask:0xf bank_mask:0xf
v_accvgpr_write_b32 a88,v220
v_mul_f32_dpp v229,v15,v229 row_newbcast:1 row_mask:0xf bank_mask:0xf
v_mfma_f32_16x16x32_bf16 v[124:127],a[248:251],v[48:51],v[124:127]
ds_read_b64 a[56:57],v40 offset:8
v_mul_f32_dpp v230,v15,v230 row_newbcast:2 row_mask:0xf bank_mask:0xf
v_accvgpr_write_b32 a89,v221
v_mul_f32_dpp v231,v15,v231 row_newbcast:3 row_mask:0xf bank_mask:0xf
v_mfma_f32_16x16x32_bf16 v[92:95],a[228:231],v[48:51],v[92:95]
ds_read_b64 a[58:59],v40 offset:200
v_mul_f32_dpp v204,v15,v204 row_newbcast:4 row_mask:0xf bank_mask:0xf
v_accvgpr_write_b32 a90,v222
v_mul_f32_dpp v205,v15,v205 row_newbcast:5 row_mask:0xf bank_mask:0xf
v_mfma_f32_16x16x32_bf16 v[88:91],a[228:231],v[44:47],v[88:91]
ds_read_b64 a[60:61],v41 offset:8
v_mul_f32_dpp v206,v15,v206 row_newbcast:6 row_mask:0xf bank_mask:0xf
v_accvgpr_write_b32 a91,v223
v_mul_f32_dpp v207,v15,v207 row_newbcast:7 row_mask:0xf bank_mask:0xf
v_mfma_f32_16x16x32_bf16 v[128:131],a[232:235],v[44:47],v[128:131]
ds_read_b64 a[62:63],v41 offset:200
v_cvt_pk_bf16_f32 v228,v228,v229
v_cvt_pk_bf16_f32 v229,v230,v231
v_cvt_pk_bf16_f32 v230,v204,v205
v_mfma_f32_16x16x32_bf16 v[132:135],a[232:235],v[48:51],v[132:135]
v_cvt_pk_bf16_f32 v231,v206,v207
v_mfma_f32_32x32x16_bf16 v[192:207],a[64:67],a[64:67],0
v_accvgpr_write_b32 a92,v228
v_mfma_f32_16x16x32_bf16 v[108:111],a[236:239],v[48:51],v[108:111]
v_accvgpr_write_b32 a93,v229
v_accvgpr_write_b32 a94,v230
v_accvgpr_write_b32 a95,v231
v_mfma_f32_16x16x32_bf16 v[104:107],a[236:239],v[44:47],v[104:107]
buffer_store_dwordx4 v[252:255],v7,s[40:43],s10 offen offset:320 sc1
v_mfma_f32_32x32x16_bf16 v[208:223],a[64:67],a[64:67],0
v_mov_b64 v[32:33],0
v_mov_b64 v[34:35],0
ds_read_b64 a[64:65],v42 offset:0
ds_read_b64 a[66:67],v42 offset:192
ds_read_b64 a[68:69],v43 offset:0
ds_read_b64 a[70:71],v43 offset:192
s_waitcnt lgkmcnt(8)
v_smfmac_f32_16x16x64_bf16 v[160:163],a[96:99],a[48:55],v10
s_waitcnt lgkmcnt(4)
ds_read_b64 a[48:49],v42 offset:8
ds_read_b64 a[50:51],v42 offset:200
ds_read_b64 a[52:53],v43 offset:8
ds_read_b64 a[54:55],v43 offset:200
v_smfmac_f32_16x16x64_bf16 v[164:167],a[100:103],a[56:63],v10
s_waitcnt lgkmcnt(4)
ds_read_b64 a[56:57],v40 offset:64
ds_read_b64 a[58:59],v40 offset:256
ds_read_b64 a[60:61],v41 offset:64
ds_read_b64 a[62:63],v41 offset:256
v_smfmac_f32_16x16x64_bf16 v[168:171],a[104:107],a[64:71],v10
s_waitcnt lgkmcnt(4)
ds_read_b64 a[64:65],v40 offset:72
ds_read_b64 a[66:67],v40 offset:264
ds_read_b64 a[68:69],v41 offset:72
ds_read_b64 a[70:71],v41 offset:264
v_and_b32 v44,8,v11
v_pk_mul_f32 v[48:49],v[160:161],v[160:161]
v_pk_mul_f32 v[50:51],v[162:163],v[162:163]
v_smfmac_f32_16x16x64_bf16 v[172:175],a[108:111],a[48:55],v10
s_waitcnt lgkmcnt(4)
ds_read_b64 a[48:49],v42 offset:64
ds_read_b64 a[50:51],v42 offset:256
ds_read_b64 a[52:53],v43 offset:64
ds_read_b64 a[54:55],v43 offset:256
v_mul_u32_u24 v44,884,v44
s_add_u32 s64,s60,3536
v_pk_add_f32 v[60:61],v[160:161],v[164:165]
v_pk_fma_f32 v[48:49],v[164:165],v[164:165],v[48:49]
v_pk_add_f32 v[62:63],v[162:163],v[166:167]
v_pk_fma_f32 v[50:51],v[166:167],v[166:167],v[50:51]
v_smfmac_f32_16x16x64_bf16 v[176:179],a[128:131],a[56:63],v10
s_waitcnt lgkmcnt(4)
ds_read_b64 a[56:57],v42 offset:72
ds_read_b64 a[58:59],v42 offset:264
ds_read_b64 a[60:61],v43 offset:72
ds_read_b64 a[62:63],v43 offset:264
v_add_u32 v47,s64,v44
v_pk_add_f32 v[60:61],v[60:61],v[168:169]
v_pk_fma_f32 v[48:49],v[168:169],v[168:169],v[48:49]
v_pk_add_f32 v[62:63],v[62:63],v[170:171]
v_pk_fma_f32 v[50:51],v[170:171],v[170:171],v[50:51]
v_smfmac_f32_16x16x64_bf16 v[180:183],a[132:135],a[64:71],v10
s_waitcnt lgkmcnt(4)
ds_read_b64 a[64:65],v40 offset:128
ds_read_b64 a[66:67],v40 offset:320
ds_read_b64 a[68:69],v41 offset:128
ds_read_b64 a[70:71],v41 offset:320
v_subrev_u32 v8,88400,v47
v_pk_add_f32 v[60:61],v[60:61],v[172:173]
v_pk_fma_f32 v[48:49],v[172:173],v[172:173],v[48:49]
v_pk_add_f32 v[62:63],v[62:63],v[174:175]
v_pk_fma_f32 v[50:51],v[174:175],v[174:175],v[50:51]
v_smfmac_f32_16x16x64_bf16 v[184:187],a[136:139],a[48:55],v10
s_waitcnt lgkmcnt(4)
ds_read_b64 a[48:49],v40 offset:136
ds_read_b64 a[50:51],v40 offset:328
ds_read_b64 a[52:53],v41 offset:136
ds_read_b64 a[54:55],v41 offset:328
v_min_u32 v47,v47,v8
v_pk_add_f32 v[60:61],v[60:61],v[176:177]
v_pk_fma_f32 v[48:49],v[176:177],v[176:177],v[48:49]
v_pk_add_f32 v[62:63],v[62:63],v[178:179]
v_pk_fma_f32 v[50:51],v[178:179],v[178:179],v[50:51]
v_smfmac_f32_16x16x64_bf16 v[188:191],a[140:143],a[56:63],v10
s_waitcnt lgkmcnt(4)
buffer_store_dwordx4 a[72:75],v7,s[40:43],s11 offen offset:0 sc1
ds_read_b64 a[56:57],v42 offset:128
ds_read_b64 a[58:59],v42 offset:320
ds_read_b64 a[60:61],v43 offset:128
ds_read_b64 a[62:63],v43 offset:320
v_xad_u32 v40,v11,32,v36
v_xad_u32 v41,v11,40,v37
v_pk_add_f32 v[60:61],v[60:61],v[180:181]
v_pk_fma_f32 v[48:49],v[180:181],v[180:181],v[48:49]
v_pk_add_f32 v[62:63],v[62:63],v[182:183]
v_pk_fma_f32 v[50:51],v[182:183],v[182:183],v[50:51]
v_smfmac_f32_16x16x64_bf16 v[192:195],a[160:163],a[64:71],v10
s_waitcnt lgkmcnt(4)
ds_read_b64 a[64:65],v42 offset:136
ds_read_b64 a[66:67],v42 offset:328
ds_read_b64 a[68:69],v43 offset:136
ds_read_b64 a[70:71],v43 offset:328
v_add_u32 v46,3536,v47
v_pk_add_f32 v[60:61],v[60:61],v[184:185]
v_pk_fma_f32 v[48:49],v[184:185],v[184:185],v[48:49]
v_pk_add_f32 v[62:63],v[62:63],v[186:187]
v_pk_fma_f32 v[50:51],v[186:187],v[186:187],v[50:51]
v_smfmac_f32_16x16x64_bf16 v[196:199],a[164:167],a[48:55],v10
s_waitcnt lgkmcnt(4)
ds_read_b64 a[48:49],v40 offset:0
ds_read_b64 a[50:51],v40 offset:192
ds_read_b64 a[52:53],v41 offset:0
ds_read_b64 a[54:55],v41 offset:192
v_xad_u32 v42,v11,48,v36
v_xad_u32 v43,v11,56,v37
v_pk_add_f32 v[60:61],v[60:61],v[188:189]
v_pk_fma_f32 v[48:49],v[188:189],v[188:189],v[48:49]
v_pk_add_f32 v[62:63],v[62:63],v[190:191]
v_pk_fma_f32 v[50:51],v[190:191],v[190:191],v[50:51]
v_smfmac_f32_16x16x64_bf16 v[200:203],a[168:171],a[56:63],v10
s_waitcnt lgkmcnt(4)
ds_read_b64 a[56:57],v40 offset:8
ds_read_b64 a[58:59],v40 offset:200
ds_read_b64 a[60:61],v41 offset:8
ds_read_b64 a[62:63],v41 offset:200
v_subrev_u32 v8,88400,v46
v_pk_add_f32 v[60:61],v[60:61],v[192:193]
v_pk_fma_f32 v[48:49],v[192:193],v[192:193],v[48:49]
v_pk_add_f32 v[62:63],v[62:63],v[194:195]
v_pk_fma_f32 v[50:51],v[194:195],v[194:195],v[50:51]
v_smfmac_f32_16x16x64_bf16 v[204:207],a[172:175],a[64:71],v10
s_waitcnt lgkmcnt(4)
ds_read_b64 a[64:65],v40 offset:64
ds_read_b64 a[66:67],v40 offset:256
ds_read_b64 a[68:69],v41 offset:64
ds_read_b64 a[70:71],v41 offset:256
v_min_u32 v46,v46,v8
v_pk_add_f32 v[60:61],v[60:61],v[196:197]
v_pk_fma_f32 v[48:49],v[196:197],v[196:197],v[48:49]
v_pk_add_f32 v[62:63],v[62:63],v[198:199]
v_pk_fma_f32 v[50:51],v[198:199],v[198:199],v[50:51]
v_smfmac_f32_16x16x64_bf16 v[208:211],a[112:115],a[48:55],v10
s_waitcnt lgkmcnt(4)
ds_read_b64 a[48:49],v40 offset:72
TE10_%=:
s_cbranch_execz TP9647_%=
ds_read_b64 a[50:51],v40 offset:264
ds_read_b64 a[52:53],v41 offset:72
ds_read_b64 a[54:55],v41 offset:264
v_xor_b32 v9,0x80008,v12
v_pk_add_f32 v[60:61],v[60:61],v[200:201]
v_pk_fma_f32 v[48:49],v[200:201],v[200:201],v[48:49]
v_pk_add_f32 v[62:63],v[62:63],v[202:203]
v_pk_fma_f32 v[50:51],v[202:203],v[202:203],v[50:51]
v_mfma_f32_32x32x16_bf16 v[224:239],v[32:35],v[32:35],0
v_smfmac_f32_16x16x64_bf16 v[212:215],a[116:119],a[56:63],v10
s_waitcnt lgkmcnt(4)
buffer_store_dwordx4 a[76:79],v7,s[40:43],s11 offen offset:64 sc1
ds_read_b64 a[56:57],v40 offset:128
ds_read_b64 a[58:59],v40 offset:320
ds_read_b64 a[60:61],v41 offset:128
ds_read_b64 a[62:63],v41 offset:320
v_add_u32_sdwa v44,v12,v47 dst_sel:DWORD dst_unused:UNUSED_PAD src0_sel:WORD_0 src1_sel:DWORD
v_pk_add_f32 v[60:61],v[60:61],v[204:205]
v_pk_fma_f32 v[48:49],v[204:205],v[204:205],v[48:49]
v_pk_add_f32 v[62:63],v[62:63],v[206:207]
v_pk_fma_f32 v[50:51],v[206:207],v[206:207],v[50:51]
v_smfmac_f32_16x16x64_bf16 v[216:219],a[144:147],a[64:71],v10
s_waitcnt lgkmcnt(4)
s_nop 0
ds_read_b64 a[64:65],v40 offset:136
ds_read_b64 a[66:67],v40 offset:328
ds_read_b64 a[68:69],v41 offset:136
ds_read_b64 a[70:71],v41 offset:328
v_add_u32_sdwa v45,v12,v47 dst_sel:DWORD dst_unused:UNUSED_PAD src0_sel:WORD_1 src1_sel:DWORD
v_pk_add_f32 v[60:61],v[60:61],v[208:209]
v_pk_fma_f32 v[48:49],v[208:209],v[208:209],v[48:49]
v_pk_add_f32 v[62:63],v[62:63],v[210:211]
v_pk_fma_f32 v[50:51],v[210:211],v[210:211],v[50:51]
v_smfmac_f32_16x16x64_bf16 v[220:223],a[148:151],a[48:55],v10
s_waitcnt lgkmcnt(4)
ds_read_b64 a[48:49],v42 offset:0
ds_read_b64 a[50:51],v42 offset:192
ds_read_b64 a[52:53],v43 offset:0
ds_read_b64 a[54:55],v43 offset:192
v_add_u32_sdwa v47,v9,v46 dst_sel:DWORD dst_unused:UNUSED_PAD src0_sel:WORD_1 src1_sel:DWORD
v_pk_add_f32 v[60:61],v[60:61],v[212:213]
v_pk_fma_f32 v[48:49],v[212:213],v[212:213],v[48:49]
v_pk_add_f32 v[62:63],v[62:63],v[214:215]
v_pk_fma_f32 v[50:51],v[214:215],v[214:215],v[50:51]
v_smfmac_f32_16x16x64_bf16 v[224:227],a[176:179],a[56:63],v10
s_waitcnt lgkmcnt(4)
ds_read_b64 a[56:57],v42 offset:8
ds_read_b64 a[58:59],v42 offset:200
ds_read_b64 a[60:61],v43 offset:8
ds_read_b64 a[62:63],v43 offset:200
v_add_u32_sdwa v46,v9,v46 dst_sel:DWORD dst_unused:UNUSED_PAD src0_sel:WORD_0 src1_sel:DWORD
v_pk_add_f32 v[60:61],v[60:61],v[216:217]
v_pk_fma_f32 v[48:49],v[216:217],v[216:217],v[48:49]
v_pk_add_f32 v[62:63],v[62:63],v[218:219]
v_pk_fma_f32 v[50:51],v[218:219],v[218:219],v[50:51]
v_mfma_f32_32x32x16_bf16 v[240:255],v[32:35],v[32:35],0
v_smfmac_f32_16x16x64_bf16 v[228:231],a[180:183],a[64:71],v10
s_waitcnt lgkmcnt(4)
ds_read_b64 a[64:65],v42 offset:64
ds_read_b64 a[66:67],v42 offset:256
ds_read_b64 a[68:69],v43 offset:64
ds_read_b64 a[70:71],v43 offset:256
v_pk_add_f32 v[60:61],v[60:61],v[220:221]
v_pk_fma_f32 v[48:49],v[220:221],v[220:221],v[48:49]
v_pk_add_f32 v[62:63],v[62:63],v[222:223]
v_pk_fma_f32 v[50:51],v[222:223],v[222:223],v[50:51]
v_smfmac_f32_16x16x64_bf16 v[232:235],a[120:123],a[48:55],v10
s_waitcnt lgkmcnt(4)
ds_read_b64 a[48:49],v42 offset:72
ds_read_b64 a[50:51],v42 offset:264
ds_read_b64 a[52:53],v43 offset:72
ds_read_b64 a[54:55],v43 offset:264
v_pk_add_f32 v[60:61],v[60:61],v[224:225]
v_pk_fma_f32 v[48:49],v[224:225],v[224:225],v[48:49]
v_pk_add_f32 v[62:63],v[62:63],v[226:227]
v_pk_fma_f32 v[50:51],v[226:227],v[226:227],v[50:51]
v_smfmac_f32_16x16x64_bf16 v[236:239],a[124:127],a[56:63],v10
s_waitcnt lgkmcnt(4)
ds_read_b64 a[56:57],v42 offset:128
ds_read_b64 a[58:59],v42 offset:320
ds_read_b64 a[60:61],v43 offset:128
ds_read_b64 a[62:63],v43 offset:320
v_pk_add_f32 v[60:61],v[60:61],v[228:229]
v_pk_fma_f32 v[48:49],v[228:229],v[228:229],v[48:49]
v_pk_add_f32 v[62:63],v[62:63],v[230:231]
v_pk_fma_f32 v[50:51],v[230:231],v[230:231],v[50:51]
v_smfmac_f32_16x16x64_bf16 v[240:243],a[152:155],a[64:71],v10
s_waitcnt lgkmcnt(4)
buffer_store_dwordx4 a[80:83],v7,s[40:43],s11 offen offset:128 sc1
ds_read_b64 a[64:65],v42 offset:136
ds_read_b64 a[66:67],v42 offset:328
ds_read_b64 a[68:69],v43 offset:136
ds_read_b64 a[70:71],v43 offset:328
v_pk_add_f32 v[60:61],v[60:61],v[232:233]
v_pk_fma_f32 v[48:49],v[232:233],v[232:233],v[48:49]
v_pk_add_f32 v[62:63],v[62:63],v[234:235]
v_pk_fma_f32 v[50:51],v[234:235],v[234:235],v[50:51]
v_smfmac_f32_16x16x64_bf16 v[244:247],a[156:159],a[48:55],v10
s_waitcnt lgkmcnt(4)
ds_read_b64 a[240:241],v44 offset:57344
ds_read_b64 a[242:243],v44 offset:57352
ds_read_b64 a[224:225],v45 offset:57344
ds_read_b64 a[226:227],v45 offset:57352
ds_read_b128 a[232:235],v13 offset:0
v_pk_add_f32 v[60:61],v[60:61],v[236:237]
v_pk_fma_f32 v[48:49],v[236:237],v[236:237],v[48:49]
v_pk_add_f32 v[62:63],v[62:63],v[238:239]
v_pk_fma_f32 v[50:51],v[238:239],v[238:239],v[50:51]
v_smfmac_f32_16x16x64_bf16 v[248:251],a[184:187],a[56:63],v10
s_waitcnt lgkmcnt(5)
ds_read_b64 a[248:249],v46 offset:57344
ds_read_b64 a[250:251],v46 offset:57352
ds_read_b64 a[228:229],v47 offset:57344
ds_read_b64 a[230:231],v47 offset:57352
ds_read_b128 a[236:239],v13 offset:256
v_pk_add_f32 v[60:61],v[60:61],v[240:241]
v_pk_fma_f32 v[48:49],v[240:241],v[240:241],v[48:49]
v_pk_add_f32 v[62:63],v[62:63],v[242:243]
v_pk_fma_f32 v[50:51],v[242:243],v[242:243],v[50:51]
v_smfmac_f32_16x16x64_bf16 v[252:255],a[188:191],a[64:71],v10
ds_read_b128 v[40:43],v4 offset:3584
ds_read_b128 v[36:39],v4 offset:3616
v_pk_add_f32 v[60:61],v[60:61],v[244:245]
v_pk_fma_f32 v[48:49],v[244:245],v[244:245],v[48:49]
v_pk_add_f32 v[62:63],v[62:63],v[246:247]
v_pk_fma_f32 v[50:51],v[246:247],v[246:247],v[50:51]
v_pk_add_f32 v[60:61],v[60:61],v[248:249]
v_pk_fma_f32 v[48:49],v[248:249],v[248:249],v[48:49]
v_pk_add_f32 v[62:63],v[62:63],v[250:251]
v_pk_fma_f32 v[50:51],v[250:251],v[250:251],v[50:51]
v_pk_add_f32 v[60:61],v[60:61],v[252:253]
v_pk_fma_f32 v[48:49],v[252:253],v[252:253],v[48:49]
v_pk_add_f32 v[62:63],v[62:63],v[254:255]
v_pk_fma_f32 v[50:51],v[254:255],v[254:255],v[50:51]
s_nop 0
v_permlane32_swap_b32 v60,v62
v_permlane32_swap_b32 v61,v63
v_permlane32_swap_b32 v48,v50
v_permlane32_swap_b32_e64 v49,v51
v_pk_add_f32 v[60:61],v[60:61],v[62:63]
v_pk_add_f32 v[48:49],v[48:49],v[50:51]
s_nop 0
v_permlane16_swap_b32 v60,v61
v_permlane16_swap_b32 v48,v49
v_add_f32 v8,v60,v61
v_add_f32_e64 v9,v48,v49
v_mul_f32 v8,0x3c2aaaab,v8
v_mul_f32 v9,0x3c2aaaab,v9
v_cmp_ngt_f32 vcc,0x5d800000,v9
v_fma_f32 v9,-v8,v8,v9
v_max_f32 v9,0,v9
s_or_b64 s[90:91],s[90:91],vcc
v_add_f32 v9,0x3727c5ac,v9
v_rsq_f32 v9,v9
s_nop 0
v_mul_f32 v60,s96,v9
v_mul_f32 v61,s96,v9
v_mul_f32 v48,-v8,v60
v_mul_f32 v49,-v8,v60
v_permlane16_swap_b32 v60,v61
s_nop 0
v_permlane16_swap_b32 v48,v49
v_mov_b64 v[62:63],v[60:61]
v_mov_b64 v[50:51],v[48:49]
s_nop 0
v_permlane32_swap_b32 v60,v62
v_permlane32_swap_b32 v61,v63
v_permlane32_swap_b32 v48,v50
v_permlane32_swap_b32 v49,v51
s_or_b32 s64,s90,s91
s_lshl_b32 s65,s52,2
v_mov_b32 v8,s64
v_mov_b32 v9,s65
ds_write_b32 v9,v8 offset:11648
v_pk_fma_f32 v[160:161],v[160:161],v[60:61],v[48:49]
v_pk_fma_f32 v[162:163],v[162:163],v[62:63],v[50:51]
v_pk_fma_f32 v[164:165],v[164:165],v[60:61],v[48:49]
v_pk_fma_f32 v[166:167],v[166:167],v[62:63],v[50:51]
v_pk_fma_f32 v[168:169],v[168:169],v[60:61],v[48:49]
v_pk_fma_f32 v[170:171],v[170:171],v[62:63],v[50:51]
v_pk_fma_f32 v[172:173],v[172:173],v[60:61],v[48:49]
v_pk_fma_f32 v[174:175],v[174:175],v[62:63],v[50:51]
v_pk_fma_f32 v[208:209],v[208:209],v[60:61],v[48:49]
v_pk_fma_f32 v[210:211],v[210:211],v[62:63],v[50:51]
v_pk_fma_f32 v[212:213],v[212:213],v[60:61],v[48:49]
v_pk_fma_f32 v[214:215],v[214:215],v[62:63],v[50:51]
v_pk_fma_f32 v[232:233],v[232:233],v[60:61],v[48:49]
v_pk_fma_f32 v[234:235],v[234:235],v[62:63],v[50:51]
v_pk_fma_f32 v[236:237],v[236:237],v[60:61],v[48:49]
v_pk_fma_f32 v[238:239],v[238:239],v[62:63],v[50:51]
v_pk_fma_f32 v[176:177],v[176:177],v[60:61],v[48:49]
v_pk_fma_f32 v[178:179],v[178:179],v[62:63],v[50:51]
v_pk_fma_f32 v[180:181],v[180:181],v[60:61],v[48:49]
v_pk_fma_f32 v[182:183],v[182:183],v[62:63],v[50:51]
v_pk_fma_f32 v[184:185],v[184:185],v[60:61],v[48:49]
v_pk_fma_f32 v[186:187],v[186:187],v[62:63],v[50:51]
v_pk_fma_f32 v[188:189],v[188:189],v[60:61],v[48:49]
v_pk_fma_f32 v[190:191],v[190:191],v[62:63],v[50:51]
v_pk_fma_f32 v[216:217],v[216:217],v[60:61],v[48:49]
v_pk_fma_f32 v[218:219],v[218:219],v[62:63],v[50:51]
v_pk_fma_f32 v[220:221],v[220:221],v[60:61],v[48:49]
v_pk_fma_f32 v[222:223],v[222:223],v[62:63],v[50:51]
v_pk_fma_f32 v[240:241],v[240:241],v[60:61],v[48:49]
v_pk_fma_f32 v[242:243],v[242:243],v[62:63],v[50:51]
v_pk_fma_f32 v[244:245],v[244:245],v[60:61],v[48:49]
v_pk_fma_f32 v[246:247],v[246:247],v[62:63],v[50:51]
v_pk_fma_f32 v[192:193],v[192:193],v[60:61],v[48:49]
v_pk_fma_f32 v[194:195],v[194:195],v[62:63],v[50:51]
v_pk_fma_f32 v[196:197],v[196:197],v[60:61],v[48:49]
v_pk_fma_f32 v[198:199],v[198:199],v[62:63],v[50:51]
v_pk_fma_f32 v[200:201],v[200:201],v[60:61],v[48:49]
v_pk_fma_f32 v[202:203],v[202:203],v[62:63],v[50:51]
v_pk_fma_f32 v[204:205],v[204:205],v[60:61],v[48:49]
v_pk_fma_f32 v[206:207],v[206:207],v[62:63],v[50:51]
v_pk_fma_f32 v[224:225],v[224:225],v[60:61],v[48:49]
v_pk_fma_f32 v[226:227],v[226:227],v[62:63],v[50:51]
v_pk_fma_f32 v[228:229],v[228:229],v[60:61],v[48:49]
v_pk_fma_f32 v[230:231],v[230:231],v[62:63],v[50:51]
v_pk_fma_f32 v[248:249],v[248:249],v[60:61],v[48:49]
v_pk_fma_f32 v[250:251],v[250:251],v[62:63],v[50:51]
v_pk_fma_f32 v[252:253],v[252:253],v[60:61],v[48:49]
v_pk_fma_f32 v[254:255],v[254:255],v[62:63],v[50:51]
v_mfma_f32_16x16x32_bf16 v[32:35],a[204:207],a[8:11],v[20:23]
buffer_store_dwordx4 a[84:87],v7,s[40:43],s11 offen offset:192 sc1
v_cvt_pk_bf16_f32 v160,v160,v164
v_cvt_pk_bf16_f32 v168,v168,v172
v_cvt_pk_bf16_f32 v208,v208,v212
v_mfma_f32_16x16x32_bf16 v[32:35],a[208:211],a[24:27],v[32:35]
ds_read_b64_tr_b16 a[216:217],v1 offset:6160
v_accvgpr_write_b32 a48,v160
v_accvgpr_write_b32 a49,v168
v_cvt_pk_bf16_f32 v232,v232,v236
v_accvgpr_write_b32 a50,v208
v_mfma_f32_16x16x32_bf16 v[32:35],a[212:215],a[40:43],v[32:35]
buffer_store_dwordx4 a[88:91],v7,s[40:43],s11 offen offset:256 sc1
v_cvt_pk_bf16_f32 v161,v161,v165
v_accvgpr_write_b32 a51,v232
v_cvt_pk_bf16_f32 v169,v169,v173
v_accvgpr_write_b32 a52,v161
v_mfma_f32_16x16x32_bf16 v[60:63],a[200:203],a[40:43],v[16:19]
ds_read_b64_tr_b16 a[218:219],v1 offset:7184
v_cvt_pk_bf16_f32 v209,v209,v213
v_accvgpr_write_b32 a53,v169
v_cvt_pk_bf16_f32 v233,v233,v237
v_accvgpr_write_b32 a54,v209
v_mfma_f32_16x16x32_bf16 v[60:63],a[196:199],a[24:27],v[60:63]
ds_read_b64_tr_b16 a[220:221],v1 offset:8208
v_cvt_pk_bf16_f32 v162,v162,v166
v_accvgpr_write_b32 a55,v233
v_lshlrev_b32_sdwa v32,1,v32 dst_sel:DWORD dst_unused:UNUSED_PAD src0_sel:DWORD src1_sel:WORD_1
v_cvt_pk_bf16_f32 v170,v170,v174
v_lshlrev_b32_sdwa v34,1,v34 dst_sel:DWORD dst_unused:UNUSED_PAD src0_sel:DWORD src1_sel:WORD_1
v_accvgpr_write_b32 a56,v162
v_mfma_f32_16x16x32_bf16 v[60:63],a[192:195],a[8:11],v[60:63]
ds_read_u16 v32,v32
v_accvgpr_write_b32 a57,v170
v_cvt_pk_bf16_f32 v210,v210,v214
v_accvgpr_write_b32 a58,v210
v_cvt_pk_bf16_f32 v234,v234,v238
v_mfma_f32_16x16x32_bf16 v[48:51],a[192:195],a[12:15],v[16:19]
ds_read_u16_d16_hi v34,v34
v_accvgpr_write_b32 a59,v234
v_cvt_pk_bf16_f32 v163,v163,v167
v_accvgpr_write_b32 a60,v163
v_mfma_f32_16x16x32_bf16 v[48:51],a[196:199],a[28:31],v[48:51]
ds_read_b64_tr_b16 a[222:223],v1 offset:9232
v_cvt_pk_bf16_f32 v171,v171,v175
v_accvgpr_write_b32 a61,v171
v_cvt_pk_bf16_f32 v211,v211,v215
v_accvgpr_write_b32 a62,v211
v_lshlrev_b32_sdwa v33,1,v33 dst_sel:DWORD dst_unused:UNUSED_PAD src0_sel:DWORD src1_sel:WORD_1
v_mfma_f32_16x16x32_bf16 v[48:51],a[200:203],a[44:47],v[48:51]
s_waitcnt lgkmcnt(6)
ds_read_u16 v33,v33
v_cvt_pk_bf16_f32 v235,v235,v239
v_accvgpr_write_b32 a63,v235
v_cvt_pk_bf16_f32 v176,v176,v180
v_cvt_pk_bf16_f32 v184,v184,v188
v_mfma_f32_16x16x32_bf16 v[232:235],a[232:235],a[240:243],v[40:43]
s_nop 0
v_cvt_pk_bf16_f32 v216,v216,v220
v_accvgpr_write_b32 a64,v176
v_accvgpr_write_b32 a65,v184
v_cvt_pk_bf16_f32 v240,v240,v244
v_mfma_f32_16x16x32_bf16 v[236:239],a[232:235],a[224:227],v[40:43]
v_accvgpr_write_b32 a66,v216
v_cvt_pk_bf16_f32 v177,v177,v181
v_accvgpr_write_b32 a67,v240
v_cvt_pk_bf16_f32 v185,v185,v189
v_lshlrev_b32_sdwa v35,1,v35 dst_sel:DWORD dst_unused:UNUSED_PAD src0_sel:DWORD src1_sel:WORD_1
v_mfma_f32_16x16x32_bf16 v[208:211],a[232:235],a[248:251],v[40:43]
ds_read_u16_d16_hi v35,v35
v_accvgpr_write_b32 a68,v177
v_cvt_pk_bf16_f32 v217,v217,v221
v_accvgpr_write_b32 a69,v185
v_cvt_pk_bf16_f32 v241,v241,v245
v_mfma_f32_16x16x32_bf16 v[212:215],a[232:235],a[228:231],v[40:43]
ds_read_b128 a[232:235],v13 offset:512
ds_read_b128 v[40:43],v4 offset:3712
v_accvgpr_write_b32 a70,v217
v_cvt_pk_bf16_f32 v178,v178,v182
v_accvgpr_write_b32 a71,v241
v_mfma_f32_16x16x32_bf16 v[160:163],a[236:239],a[240:243],v[36:39]
ds_read_b64 a[240:241],v44 offset:57408
ds_read_b64 a[242:243],v44 offset:57416
v_cvt_pk_bf16_f32 v186,v186,v190
v_accvgpr_write_b32 a72,v178
v_accvgpr_write_b32 a73,v186
v_cvt_pk_bf16_f32 v218,v218,v222
v_mfma_f32_16x16x32_bf16 v[164:167],a[236:239],a[224:227],v[36:39]
buffer_store_dwordx4 a[92:95],v7,s[40:43],s11 offen offset:320 sc1
ds_read_b64 a[224:225],v45 offset:57408
v_accvgpr_write_b32 a74,v218
v_cvt_pk_bf16_f32 v242,v242,v246
v_accvgpr_write_b32 a75,v242
v_cvt_pk_bf16_f32 v179,v179,v183
v_lshlrev_b32_sdwa v60,1,v60 dst_sel:DWORD dst_unused:UNUSED_PAD src0_sel:DWORD src1_sel:WORD_1
v_mfma_f32_16x16x32_bf16 v[168:171],a[236:239],a[248:251],v[36:39]
ds_read_b64 a[226:227],v45 offset:57416
ds_read_b64 a[248:249],v46 offset:57408
v_accvgpr_write_b32 a76,v179
v_cvt_pk_bf16_f32 v187,v187,v191
v_accvgpr_write_b32 a77,v187
v_cvt_pk_bf16_f32 v219,v219,v223
v_mfma_f32_16x16x32_bf16 v[172:175],a[236:239],a[228:231],v[36:39]
s_waitcnt lgkmcnt(3)
ds_read_b64 a[250:251],v46 offset:57416
ds_read_b64 a[228:229],v47 offset:57408
v_accvgpr_write_b32 a78,v219
v_cvt_pk_bf16_f32 v243,v243,v247
v_accvgpr_write_b32 a79,v243
v_cvt_pk_bf16_f32 v192,v192,v196
v_mfma_f32_16x16x32_bf16 v[240:243],a[232:235],a[240:243],v[40:43]
s_waitcnt lgkmcnt(3)
ds_read_b64 a[230:231],v47 offset:57416
ds_read_b128 a[236:239],v13 offset:768
v_cvt_pk_bf16_f32 v200,v200,v204
v_cvt_pk_bf16_f32 v224,v224,v228
v_accvgpr_write_b32 a80,v192
v_lshlrev_b32_sdwa v62,1,v62 dst_sel:DWORD dst_unused:UNUSED_PAD src0_sel:DWORD src1_sel:WORD_1
v_accvgpr_write_b32 a81,v200
v_mfma_f32_16x16x32_bf16 v[244:247],a[232:235],a[224:227],v[40:43]
s_waitcnt lgkmcnt(3)
ds_read_b128 v[36:39],v4 offset:3744
v_cvt_pk_bf16_f32 v248,v248,v252
v_accvgpr_write_b32 a82,v224
v_cvt_pk_bf16_f32 v193,v193,v197
v_mfma_f32_16x16x32_bf16 v[216:219],a[232:235],a[248:251],v[40:43]
s_waitcnt lgkmcnt(2)
ds_read_u16 v60,v60
v_accvgpr_write_b32 a83,v248
v_cvt_pk_bf16_f32 v201,v201,v205
v_accvgpr_write_b32 a84,v193
v_cvt_pk_bf16_f32 v225,v225,v229
v_mfma_f32_16x16x32_bf16 v[220:223],a[232:235],a[228:231],v[40:43]
s_waitcnt lgkmcnt(1)
ds_read_b128 a[232:235],v13 offset:1024
ds_read_b128 v[40:43],v4 offset:3840
v_accvgpr_write_b32 a85,v201
v_cvt_pk_bf16_f32 v249,v249,v253
v_accvgpr_write_b32 a86,v225
v_cvt_pk_bf16_f32 v194,v194,v198
v_lshlrev_b32_sdwa v61,1,v61 dst_sel:DWORD dst_unused:UNUSED_PAD src0_sel:DWORD src1_sel:WORD_1
v_mfma_f32_16x16x32_bf16 v[176:179],a[236:239],a[240:243],v[36:39]
s_nop 0
ds_read_b64 a[240:241],v44 offset:57472
ds_read_b64 a[242:243],v44 offset:57480
v_accvgpr_write_b32 a87,v249
v_cvt_pk_bf16_f32 v202,v202,v206
v_accvgpr_write_b32 a88,v194
v_accvgpr_write_b32 a89,v202
v_mfma_f32_16x16x32_bf16 v[180:183],a[236:239],a[224:227],v[36:39]
ds_read_b64 a[224:225],v45 offset:57472
ds_read_b64 a[226:227],v45 offset:57480
v_cvt_pk_bf16_f32 v226,v226,v230
v_accvgpr_write_b32 a90,v226
v_cvt_pk_bf16_f32 v250,v250,v254
v_accvgpr_write_b32 a91,v250
v_mfma_f32_16x16x32_bf16 v[184:187],a[236:239],a[248:251],v[36:39]
ds_read_b64 a[248:249],v46 offset:57472
ds_read_b64 a[250:251],v46 offset:57480
v_cvt_pk_bf16_f32 v195,v195,v199
v_accvgpr_write_b32 a92,v195
v_cvt_pk_bf16_f32 v203,v203,v207
v_accvgpr_write_b32 a93,v203
v_lshlrev_b32_sdwa v63,1,v63 dst_sel:DWORD dst_unused:UNUSED_PAD src0_sel:DWORD src1_sel:WORD_1
v_mfma_f32_16x16x32_bf16 v[188:191],a[236:239],a[228:231],v[36:39]
s_waitcnt lgkmcnt(4)
ds_read_b64 a[228:229],v47 offset:57472
ds_read_b64 a[230:231],v47 offset:57480
v_cvt_pk_bf16_f32 v227,v227,v231
v_accvgpr_write_b32 a94,v227
v_cvt_pk_bf16_f32 v251,v251,v255
v_accvgpr_write_b32 a95,v251
v_mfma_f32_16x16x32_bf16 v[248:251],a[232:235],a[240:243],v[40:43]
s_waitcnt lgkmcnt(4)
ds_read_b128 a[236:239],v13 offset:1280
ds_read_b128 v[36:39],v4 offset:3872
v_mfma_f32_16x16x32_bf16 v[252:255],a[232:235],a[224:227],v[40:43]
s_waitcnt lgkmcnt(4)
ds_read_u16_d16_hi v62,v62
v_lshlrev_b32_sdwa v48,1,v48 dst_sel:DWORD dst_unused:UNUSED_PAD src0_sel:DWORD src1_sel:WORD_1
v_mfma_f32_16x16x32_bf16 v[224:227],a[232:235],a[248:251],v[40:43]
s_waitcnt lgkmcnt(3)
ds_read_u16 v61,v61
v_lshlrev_b32_sdwa v50,1,v50 dst_sel:DWORD dst_unused:UNUSED_PAD src0_sel:DWORD src1_sel:WORD_1
v_mfma_f32_16x16x32_bf16 v[228:231],a[232:235],a[228:231],v[40:43]
s_waitcnt lgkmcnt(2)
ds_read_b64_tr_b16 a[232:233],v3 offset:6224
ds_read_b64_tr_b16 a[234:235],v3 offset:7760
v_mfma_f32_16x16x32_bf16 v[192:195],a[236:239],a[240:243],v[36:39]
ds_read_b64_tr_b16 a[240:241],v3 offset:6232
ds_read_b64_tr_b16 a[242:243],v3 offset:7768
v_mfma_f32_16x16x32_bf16 v[196:199],a[236:239],a[224:227],v[36:39]
ds_read_b64_tr_b16 a[224:225],v3 offset:6288
ds_read_b64_tr_b16 a[226:227],v3 offset:7824
v_mfma_f32_16x16x32_bf16 v[200:203],a[236:239],a[248:251],v[36:39]
ds_read_b64_tr_b16 a[248:249],v3 offset:6296
ds_read_b64_tr_b16 a[250:251],v3 offset:7832
v_mfma_f32_16x16x32_bf16 v[204:207],a[236:239],a[228:231],v[36:39]
ds_read_u16_d16_hi v63,v63
v_lshlrev_b32_sdwa v49,1,v49 dst_sel:DWORD dst_unused:UNUSED_PAD src0_sel:DWORD src1_sel:WORD_1
v_mfma_f32_16x16x32_bf16 v[44:47],a[212:215],a[44:47],v[20:23]
ds_read_u16 v48,v48
v_lshlrev_b32_sdwa v51,1,v51 dst_sel:DWORD dst_unused:UNUSED_PAD src0_sel:DWORD src1_sel:WORD_1
v_mfma_f32_16x16x32_bf16 v[44:47],a[208:211],a[28:31],v[44:47]
s_waitcnt vmcnt(18)
ds_read_u16_d16_hi v50,v50
v_mfma_f32_16x16x32_bf16 v[44:47],a[204:207],a[12:15],v[44:47]
s_barrier
ds_read_u16 v49,v49
v_mfma_f32_16x16x32_bf16 v[40:43],a[204:207],a[48:51],v[20:23]
ds_read_u16_d16_hi v51,v51
s_nop 3
v_lshlrev_b32_sdwa v44,1,v44 dst_sel:DWORD dst_unused:UNUSED_PAD src0_sel:DWORD src1_sel:WORD_1
v_mfma_f32_16x16x32_bf16 v[40:43],a[208:211],a[64:67],v[40:43]
ds_read_u16 v44,v44
v_lshl_add_u64 v[24:25],v[24:25],0,v[26:27]
v_mfma_f32_16x16x32_bf16 v[40:43],a[212:215],a[80:83],v[40:43]
v_lshl_add_u64 v[26:27],v[28:29],0,v[30:31]
v_lshlrev_b32_sdwa v46,1,v46 dst_sel:DWORD dst_unused:UNUSED_PAD src0_sel:DWORD src1_sel:WORD_1
v_mfma_f32_16x16x32_bf16 v[36:39],a[200:203],a[80:83],v[16:19]
ds_read_u16_d16_hi v46,v46
v_lshlrev_b32_sdwa v45,1,v45 dst_sel:DWORD dst_unused:UNUSED_PAD src0_sel:DWORD src1_sel:WORD_1
v_mfma_f32_16x16x32_bf16 v[36:39],a[196:199],a[64:67],v[36:39]
ds_read_u16 v45,v45
v_lshl_add_u64 v[28:29],v[52:53],0,v[54:55]
v_mfma_f32_16x16x32_bf16 v[36:39],a[192:195],a[48:51],v[36:39]
v_lshlrev_b32_sdwa v47,1,v47 dst_sel:DWORD dst_unused:UNUSED_PAD src0_sel:DWORD src1_sel:WORD_1
v_lshlrev_b32_sdwa v40,1,v40 dst_sel:DWORD dst_unused:UNUSED_PAD src0_sel:DWORD src1_sel:WORD_1
v_mfma_f32_16x16x32_bf16 v[52:55],a[192:195],a[52:55],v[16:19]
ds_read_u16_d16_hi v47,v47
v_mfma_f32_16x16x32_bf16 v[52:55],a[196:199],a[68:71],v[52:55]
ds_read_u16 v40,v40
v_lshl_add_u64 v[30:31],v[56:57],0,v[58:59]
v_mfma_f32_16x16x32_bf16 v[52:55],a[200:203],a[84:87],v[52:55]
v_lshlrev_b32_sdwa v42,1,v42 dst_sel:DWORD dst_unused:UNUSED_PAD src0_sel:DWORD src1_sel:WORD_1
v_mfma_f32_16x16x32_bf16 v[56:59],a[212:215],a[84:87],v[20:23]
ds_read_u16_d16_hi v42,v42
v_lshlrev_b32_sdwa v41,1,v41 dst_sel:DWORD dst_unused:UNUSED_PAD src0_sel:DWORD src1_sel:WORD_1
v_mfma_f32_16x16x32_bf16 v[56:59],a[208:211],a[68:71],v[56:59]
ds_read_u16 v41,v41
v_mfma_f32_16x16x32_bf16 v[56:59],a[204:207],a[52:55],v[56:59]
v_lshlrev_b32_sdwa v43,1,v43 dst_sel:DWORD dst_unused:UNUSED_PAD src0_sel:DWORD src1_sel:WORD_1
v_mfma_f32_16x16x32_bf16 v[136:139],a[244:247],v[24:27],v[136:139]
ds_read_u16_d16_hi v43,v43
v_lshlrev_b32_sdwa v36,1,v36 dst_sel:DWORD dst_unused:UNUSED_PAD src0_sel:DWORD src1_sel:WORD_1
v_mfma_f32_16x16x32_bf16 v[140:143],a[244:247],v[28:31],v[140:143]
ds_read_u16 v36,v36
v_mfma_f32_16x16x32_bf16 v[68:71],a[252:255],v[28:31],v[68:71]
v_lshlrev_b32_sdwa v38,1,v38 dst_sel:DWORD dst_unused:UNUSED_PAD src0_sel:DWORD src1_sel:WORD_1
v_mfma_f32_16x16x32_bf16 v[64:67],a[252:255],v[24:27],v[64:67]
s_waitcnt lgkmcnt(5)
ds_read_u16_d16_hi v38,v38
v_mfma_f32_16x16x32_bf16 v[144:147],a[232:235],v[24:27],v[144:147]
v_lshlrev_b32_sdwa v37,1,v37 dst_sel:DWORD dst_unused:UNUSED_PAD src0_sel:DWORD src1_sel:WORD_1
v_mfma_f32_16x16x32_bf16 v[148:151],a[232:235],v[28:31],v[148:151]
ds_read_u16 v37,v37
v_mfma_f32_16x16x32_bf16 v[84:87],a[240:243],v[28:31],v[84:87]
v_lshlrev_b32_sdwa v39,1,v39 dst_sel:DWORD dst_unused:UNUSED_PAD src0_sel:DWORD src1_sel:WORD_1
v_mfma_f32_16x16x32_bf16 v[80:83],a[240:243],v[24:27],v[80:83]
ds_read_u16_d16_hi v39,v39
v_mfma_f32_16x16x32_bf16 v[152:155],a[224:227],v[24:27],v[152:155]
v_lshlrev_b32_sdwa v52,1,v52 dst_sel:DWORD dst_unused:UNUSED_PAD src0_sel:DWORD src1_sel:WORD_1
v_mfma_f32_16x16x32_bf16 v[156:159],a[224:227],v[28:31],v[156:159]
ds_read_u16 v52,v52
v_mfma_f32_16x16x32_bf16 v[100:103],a[248:251],v[28:31],v[100:103]
v_lshlrev_b32_sdwa v54,1,v54 dst_sel:DWORD dst_unused:UNUSED_PAD src0_sel:DWORD src1_sel:WORD_1
v_mfma_f32_16x16x32_bf16 v[96:99],a[248:251],v[24:27],v[96:99]
ds_read_u16_d16_hi v54,v54
v_mfma_f32_16x16x32_bf16 v[28:31],a[204:207],a[56:59],v[20:23]
v_lshlrev_b32_sdwa v56,1,v56 dst_sel:DWORD dst_unused:UNUSED_PAD src0_sel:DWORD src1_sel:WORD_1
v_fma_mixlo_f16 v8,v53,s99,0
v_mfma_f32_16x16x32_bf16 v[28:31],a[208:211],a[72:75],v[28:31]
ds_read_u16 v56,v56
v_lshl_add_u64 v[32:33],v[32:33],0,v[34:35]
v_mfma_f32_16x16x32_bf16 v[28:31],a[212:215],a[88:91],v[28:31]
v_lshl_add_u64 v[34:35],v[60:61],0,v[62:63]
v_lshlrev_b32_sdwa v58,1,v58 dst_sel:DWORD dst_unused:UNUSED_PAD src0_sel:DWORD src1_sel:WORD_1
v_mfma_f32_16x16x32_bf16 v[24:27],a[200:203],a[88:91],v[16:19]
ds_read_u16_d16_hi v58,v58
v_fma_mixhi_f16 v8,v55,s99,0
v_mfma_f32_16x16x32_bf16 v[24:27],a[196:199],a[72:75],v[24:27]
v_lshl_add_u64 v[60:61],v[44:45],0,v[46:47]
v_pk_fma_f16 v9,v8,v8,s54
v_lshlrev_b32_sdwa v57,1,v57 dst_sel:DWORD dst_unused:UNUSED_PAD src0_sel:DWORD src1_sel:WORD_1
v_mfma_f32_16x16x32_bf16 v[24:27],a[192:195],a[56:59],v[24:27]
ds_read_u16 v57,v57
v_pk_mul_f16 v9,v8,v9
v_mfma_f32_16x16x32_bf16 v[44:47],a[192:195],a[60:63],v[16:19]
ds_read_b128 v[16:19],v4 offset:768
ds_read_b64_tr_b16 a[192:193],v1 offset:10256
v_lshlrev_b32_sdwa v59,1,v59 dst_sel:DWORD dst_unused:UNUSED_PAD src0_sel:DWORD src1_sel:WORD_1
v_mfma_f32_16x16x32_bf16 v[44:47],a[196:199],a[76:79],v[44:47]
ds_read_b64_tr_b16 a[194:195],v1 offset:11280
ds_read_b64_tr_b16 a[196:197],v2 offset:10256
v_lshl_add_u64 v[62:63],v[48:49],0,v[50:51]
v_mfma_f32_16x16x32_bf16 v[44:47],a[200:203],a[92:95],v[44:47]
ds_read_b64_tr_b16 a[198:199],v2 offset:11280
ds_read_b64_tr_b16 a[200:201],v2 offset:8208
v_exp_f16 v8,v9
v_lshlrev_b32_sdwa v28,1,v28 dst_sel:DWORD dst_unused:UNUSED_PAD src0_sel:DWORD src1_sel:WORD_1
v_mfma_f32_16x16x32_bf16 v[48:51],a[212:215],a[92:95],v[20:23]
ds_read_b128 v[20:23],v4 offset:800
ds_read_b64_tr_b16 a[202:203],v2 offset:9232
v_mfma_f32_16x16x32_bf16 v[48:51],a[208:211],a[76:79],v[48:51]
ds_read_b64_tr_b16 a[212:213],v2 offset:6160
ds_read_b64_tr_b16 a[214:215],v2 offset:7184
v_lshlrev_b32_sdwa v30,1,v30 dst_sel:DWORD dst_unused:UNUSED_PAD src0_sel:DWORD src1_sel:WORD_1
v_mfma_f32_16x16x32_bf16 v[48:51],a[204:207],a[60:63],v[48:51]
v_exp_f16_sdwa v8,v9 dst_sel:WORD_1 dst_unused:UNUSED_PRESERVE src0_sel:WORD_1
v_mfma_f32_16x16x32_bf16 v[112:115],a[244:247],v[32:35],v[112:115]
ds_read_u16_d16_hi v59,v59
v_lshlrev_b32_sdwa v29,1,v29 dst_sel:DWORD dst_unused:UNUSED_PAD src0_sel:DWORD src1_sel:WORD_1
v_mfma_f32_16x16x32_bf16 v[116:119],a[244:247],v[60:63],v[116:119]
ds_read_u16 v28,v28
v_pk_fma_f16 v8,v8,s55,s55
v_mfma_f32_16x16x32_bf16 v[76:79],a[252:255],v[60:63],v[76:79]
ds_read_u16_d16_hi v30,v30
v_lshlrev_b32_sdwa v31,1,v31 dst_sel:DWORD dst_unused:UNUSED_PAD src0_sel:DWORD src1_sel:WORD_1
v_mfma_f32_16x16x32_bf16 v[72:75],a[252:255],v[32:35],v[72:75]
TE11_%=:
s_cbranch_execz TP10171_%=
v_rcp_f16 v9,v8
v_mfma_f32_16x16x32_bf16 v[120:123],a[232:235],v[32:35],v[120:123]
ds_read_u16 v29,v29
v_lshlrev_b32_sdwa v24,1,v24 dst_sel:DWORD dst_unused:UNUSED_PAD src0_sel:DWORD src1_sel:WORD_1
v_mfma_f32_16x16x32_bf16 v[124:127],a[232:235],v[60:63],v[124:127]
ds_read_u16_d16_hi v31,v31
v_lshlrev_b32_sdwa v26,1,v26 dst_sel:DWORD dst_unused:UNUSED_PAD src0_sel:DWORD src1_sel:WORD_1
v_mfma_f32_16x16x32_bf16 v[92:95],a[240:243],v[60:63],v[92:95]
ds_read_u16 v24,v24
v_lshlrev_b32_sdwa v25,1,v25 dst_sel:DWORD dst_unused:UNUSED_PAD src0_sel:DWORD src1_sel:WORD_1
v_mfma_f32_16x16x32_bf16 v[88:91],a[240:243],v[32:35],v[88:91]
v_rcp_f16_sdwa v9,v8 dst_sel:WORD_1 dst_unused:UNUSED_PRESERVE src0_sel:WORD_1
v_mfma_f32_16x16x32_bf16 v[128:131],a[224:227],v[32:35],v[128:131]
ds_read_u16_d16_hi v26,v26
v_fma_mix_f32 v53,v53,v9,0 op_sel_hi:[0,1,0]
v_mfma_f32_16x16x32_bf16 v[132:135],a[224:227],v[60:63],v[132:135]
ds_read_u16 v25,v25
v_lshlrev_b32_sdwa v27,1,v27 dst_sel:DWORD dst_unused:UNUSED_PAD src0_sel:DWORD src1_sel:WORD_1
v_mfma_f32_16x16x32_bf16 v[108:111],a[248:251],v[60:63],v[108:111]
ds_read_u16_d16_hi v27,v27
v_fma_mix_f32 v55,v55,v9,0 op_sel:[0,1,0] op_sel_hi:[0,1,0]
v_mfma_f32_16x16x32_bf16 v[104:107],a[248:251],v[32:35],v[104:107]
s_waitcnt lgkmcnt(6)
v_lshlrev_b32_sdwa v44,1,v44 dst_sel:DWORD dst_unused:UNUSED_PAD src0_sel:DWORD src1_sel:WORD_1
v_mfma_f32_16x16x32_bf16 v[60:63],a[216:219],a[0:3],v[16:19]
s_add_u32 m0,s62,0
ds_read_u16 v44,v44
v_lshlrev_b32_sdwa v48,1,v48 dst_sel:DWORD dst_unused:UNUSED_PAD src0_sel:DWORD src1_sel:WORD_1
v_mfma_f32_16x16x32_bf16 v[60:63],a[220:223],a[16:19],v[60:63]
s_mov_b32 s68,s87
ds_read_u16 v48,v48
v_lshl_add_u64 v[40:41],v[40:41],0,v[42:43]
v_mfma_f32_16x16x32_bf16 v[60:63],a[192:195],a[32:35],v[60:63]
buffer_load_dwordx4 v5,s[44:47],s68 offen offset:0 lds
v_lshl_add_u64 v[42:43],v[36:37],0,v[38:39]
v_lshlrev_b32_sdwa v50,1,v50 dst_sel:DWORD dst_unused:UNUSED_PAD src0_sel:DWORD src1_sel:WORD_1
v_mfma_f32_16x16x32_bf16 v[32:35],a[196:199],a[32:35],v[20:23]
ds_read_u16_d16_hi v50,v50
v_lshlrev_b32_sdwa v49,1,v49 dst_sel:DWORD dst_unused:UNUSED_PAD src0_sel:DWORD src1_sel:WORD_1
v_mfma_f32_16x16x32_bf16 v[32:35],a[200:203],a[16:19],v[32:35]
s_add_u32 s68,s68,s53
ds_read_u16 v49,v49
v_lshl_add_u64 v[36:37],v[56:57],0,v[58:59]
v_mfma_f32_16x16x32_bf16 v[32:35],a[212:215],a[0:3],v[32:35]
buffer_load_dwordx4 v5,s[44:47],s68 offen offset:1024 lds
v_lshlrev_b32_sdwa v51,1,v51 dst_sel:DWORD dst_unused:UNUSED_PAD src0_sel:DWORD src1_sel:WORD_1
v_mfma_f32_16x16x32_bf16 v[56:59],a[212:215],a[4:7],v[20:23]
ds_read_u16_d16_hi v51,v51
v_or_b32_e64 v38,v52,v54
v_mfma_f32_16x16x32_bf16 v[56:59],a[200:203],a[20:23],v[56:59]
v_cvt_pk_bf16_f32 v39,v53,v55
v_lshlrev_b32_sdwa v46,1,v46 dst_sel:DWORD dst_unused:UNUSED_PAD src0_sel:DWORD src1_sel:WORD_1
v_mfma_f32_16x16x32_bf16 v[56:59],a[196:199],a[36:39],v[56:59]
ds_read_u16_d16_hi v46,v46
v_lshlrev_b32_sdwa v45,1,v45 dst_sel:DWORD dst_unused:UNUSED_PAD src0_sel:DWORD src1_sel:WORD_1
v_mfma_f32_16x16x32_bf16 v[52:55],a[192:195],a[36:39],v[16:19]
ds_read_u16 v45,v45
v_lshlrev_b32_sdwa v47,1,v47 dst_sel:DWORD dst_unused:UNUSED_PAD src0_sel:DWORD src1_sel:WORD_1
v_mfma_f32_16x16x32_bf16 v[52:55],a[220:223],a[20:23],v[52:55]
ds_read_u16_d16_hi v47,v47
v_mfma_f32_16x16x32_bf16 v[52:55],a[216:219],a[4:7],v[52:55]
ds_read_b64_tr_b16 a[228:229],v3 offset:12320
v_lshlrev_b32_sdwa v60,1,v60 dst_sel:DWORD dst_unused:UNUSED_PAD src0_sel:DWORD src1_sel:WORD_1
v_mfma_f32_16x16x32_bf16 v[232:235],a[244:247],v[40:43],v[232:235]
ds_read_u16 v60,v60
v_lshlrev_b32_sdwa v62,1,v62 dst_sel:DWORD dst_unused:UNUSED_PAD src0_sel:DWORD src1_sel:WORD_1
v_mfma_f32_16x16x32_bf16 v[236:239],a[244:247],v[36:39],v[236:239]
ds_read_u16_d16_hi v62,v62
v_mfma_f32_16x16x32_bf16 v[164:167],a[252:255],v[36:39],v[164:167]
ds_read_b64_tr_b16 a[230:231],v3 offset:13856
v_lshlrev_b32_sdwa v61,1,v61 dst_sel:DWORD dst_unused:UNUSED_PAD src0_sel:DWORD src1_sel:WORD_1
v_mfma_f32_16x16x32_bf16 v[160:163],a[252:255],v[40:43],v[160:163]
ds_read_u16 v61,v61
v_mfma_f32_16x16x32_bf16 v[240:243],a[232:235],v[40:43],v[240:243]
ds_read_b64_tr_b16 a[236:237],v3 offset:12328
v_lshlrev_b32_sdwa v63,1,v63 dst_sel:DWORD dst_unused:UNUSED_PAD src0_sel:DWORD src1_sel:WORD_1
v_mfma_f32_16x16x32_bf16 v[244:247],a[232:235],v[36:39],v[244:247]
s_add_u32 s68,s68,s53
ds_read_u16_d16_hi v63,v63
v_mfma_f32_16x16x32_bf16 v[180:183],a[240:243],v[36:39],v[180:183]
buffer_load_dwordx4 v5,s[44:47],s68 offen offset:2048 lds
v_lshlrev_b32_sdwa v32,1,v32 dst_sel:DWORD dst_unused:UNUSED_PAD src0_sel:DWORD src1_sel:WORD_1
v_mfma_f32_16x16x32_bf16 v[176:179],a[240:243],v[40:43],v[176:179]
ds_read_u16 v32,v32
v_mfma_f32_16x16x32_bf16 v[248:251],a[224:227],v[40:43],v[248:251]
ds_read_b64_tr_b16 a[238:239],v3 offset:13864
v_lshlrev_b32_sdwa v34,1,v34 dst_sel:DWORD dst_unused:UNUSED_PAD src0_sel:DWORD src1_sel:WORD_1
v_mfma_f32_16x16x32_bf16 v[252:255],a[224:227],v[36:39],v[252:255]
ds_read_u16_d16_hi v34,v34
v_mfma_f32_16x16x32_bf16 v[196:199],a[248:251],v[36:39],v[196:199]
v_lshlrev_b32_sdwa v33,1,v33 dst_sel:DWORD dst_unused:UNUSED_PAD src0_sel:DWORD src1_sel:WORD_1
v_mfma_f32_16x16x32_bf16 v[192:195],a[248:251],v[40:43],v[192:195]
ds_read_u16 v33,v33
v_mfma_f32_16x16x32_bf16 v[36:39],a[216:219],a[8:11],v[16:19]
ds_read_b64_tr_b16 a[208:209],v1 offset:12320
v_lshlrev_b32_sdwa v35,1,v35 dst_sel:DWORD dst_unused:UNUSED_PAD src0_sel:DWORD src1_sel:WORD_1
v_mfma_f32_16x16x32_bf16 v[36:39],a[220:223],a[24:27],v[36:39]
s_waitcnt lgkmcnt(6)
ds_read_u16_d16_hi v35,v35
v_lshl_add_u64 v[28:29],v[28:29],0,v[30:31]
v_fma_mixlo_f16 v8,v57,s99,0
v_mfma_f32_16x16x32_bf16 v[36:39],a[192:195],a[40:43],v[36:39]
ds_read_b64_tr_b16 a[210:211],v1 offset:13344
v_lshl_add_u64 v[30:31],v[24:25],0,v[26:27]
v_lshlrev_b32_sdwa v52,1,v52 dst_sel:DWORD dst_unused:UNUSED_PAD src0_sel:DWORD src1_sel:WORD_1
v_mfma_f32_16x16x32_bf16 v[40:43],a[196:199],a[40:43],v[20:23]
ds_read_u16 v52,v52
v_fma_mixhi_f16 v8,v59,s99,0
v_mfma_f32_16x16x32_bf16 v[40:43],a[200:203],a[24:27],v[40:43]
ds_read_b64_tr_b16 a[204:205],v1 offset:14368
v_lshl_add_u64 v[24:25],v[48:49],0,v[50:51]
v_pk_fma_f16 v9,v8,v8,s54
v_lshlrev_b32_sdwa v54,1,v54 dst_sel:DWORD dst_unused:UNUSED_PAD src0_sel:DWORD src1_sel:WORD_1
v_mfma_f32_16x16x32_bf16 v[40:43],a[212:215],a[8:11],v[40:43]
ds_read_u16_d16_hi v54,v54
v_pk_mul_f16 v9,v8,v9
v_mfma_f32_16x16x32_bf16 v[48:51],a[212:215],a[12:15],v[20:23]
ds_read_b64_tr_b16 a[206:207],v1 offset:15392
v_lshlrev_b32_sdwa v53,1,v53 dst_sel:DWORD dst_unused:UNUSED_PAD src0_sel:DWORD src1_sel:WORD_1
v_mfma_f32_16x16x32_bf16 v[48:51],a[200:203],a[28:31],v[48:51]
ds_read_u16 v53,v53
v_lshl_add_u64 v[26:27],v[44:45],0,v[46:47]
v_mfma_f32_16x16x32_bf16 v[48:51],a[196:199],a[44:47],v[48:51]
v_exp_f16 v8,v9
v_lshlrev_b32_sdwa v55,1,v55 dst_sel:DWORD dst_unused:UNUSED_PAD src0_sel:DWORD src1_sel:WORD_1
v_mfma_f32_16x16x32_bf16 v[44:47],a[192:195],a[44:47],v[16:19]
ds_read_u16_d16_hi v55,v55
v_lshlrev_b32_sdwa v56,1,v56 dst_sel:DWORD dst_unused:UNUSED_PAD src0_sel:DWORD src1_sel:WORD_1
v_mfma_f32_16x16x32_bf16 v[44:47],a[220:223],a[28:31],v[44:47]
ds_read_u16 v56,v56
v_lshlrev_b32_sdwa v58,1,v58 dst_sel:DWORD dst_unused:UNUSED_PAD src0_sel:DWORD src1_sel:WORD_1
v_mfma_f32_16x16x32_bf16 v[44:47],a[216:219],a[12:15],v[44:47]
v_exp_f16_sdwa v8,v9 dst_sel:WORD_1 dst_unused:UNUSED_PRESERVE src0_sel:WORD_1
v_mfma_f32_16x16x32_bf16 v[208:211],a[244:247],v[28:31],v[208:211]
ds_read_u16_d16_hi v58,v58
v_pk_fma_f16 v8,v8,s55,s55
v_mfma_f32_16x16x32_bf16 v[212:215],a[244:247],v[24:27],v[212:215]
ds_read_b64_tr_b16 a[244:245],v3 offset:12384
v_lshlrev_b32_sdwa v36,1,v36 dst_sel:DWORD dst_unused:UNUSED_PAD src0_sel:DWORD src1_sel:WORD_1
v_mfma_f32_16x16x32_bf16 v[172:175],a[252:255],v[24:27],v[172:175]
ds_read_u16 v36,v36
v_lshlrev_b32_sdwa v38,1,v38 dst_sel:DWORD dst_unused:UNUSED_PAD src0_sel:DWORD src1_sel:WORD_1
v_mfma_f32_16x16x32_bf16 v[168:171],a[252:255],v[28:31],v[168:171]
ds_read_u16_d16_hi v38,v38
v_rcp_f16 v9,v8
v_mfma_f32_16x16x32_bf16 v[216:219],a[232:235],v[28:31],v[216:219]
ds_read_b64_tr_b16 a[246:247],v3 offset:13920
v_lshlrev_b32_sdwa v37,1,v37 dst_sel:DWORD dst_unused:UNUSED_PAD src0_sel:DWORD src1_sel:WORD_1
v_mfma_f32_16x16x32_bf16 v[220:223],a[232:235],v[24:27],v[220:223]
ds_read_u16 v37,v37
ds_read_b64_tr_b16 a[252:253],v3 offset:12392
v_mfma_f32_16x16x32_bf16 v[188:191],a[240:243],v[24:27],v[188:191]
ds_read_b64_tr_b16 a[254:255],v3 offset:13928
v_lshlrev_b32_sdwa v39,1,v39 dst_sel:DWORD dst_unused:UNUSED_PAD src0_sel:DWORD src1_sel:WORD_1
v_mfma_f32_16x16x32_bf16 v[184:187],a[240:243],v[28:31],v[184:187]
ds_read_u16_d16_hi v39,v39
v_rcp_f16_sdwa v9,v8 dst_sel:WORD_1 dst_unused:UNUSED_PRESERVE src0_sel:WORD_1
v_mfma_f32_16x16x32_bf16 v[224:227],a[224:227],v[28:31],v[224:227]
ds_read_b64_tr_b16 a[232:233],v3 offset:12448
v_fma_mix_f32 v57,v57,v9,0 op_sel_hi:[0,1,0]
v_mfma_f32_16x16x32_bf16 v[228:231],a[224:227],v[24:27],v[228:231]
ds_read_b64_tr_b16 a[234:235],v3 offset:13984
v_lshlrev_b32_sdwa v40,1,v40 dst_sel:DWORD dst_unused:UNUSED_PAD src0_sel:DWORD src1_sel:WORD_1
v_mfma_f32_16x16x32_bf16 v[204:207],a[248:251],v[24:27],v[204:207]
ds_read_u16 v40,v40
v_fma_mix_f32 v59,v59,v9,0 op_sel:[0,1,0] op_sel_hi:[0,1,0]
v_mfma_f32_16x16x32_bf16 v[200:203],a[248:251],v[28:31],v[200:203]
ds_read_b64_tr_b16 a[240:241],v3 offset:12456
v_lshlrev_b32_sdwa v42,1,v42 dst_sel:DWORD dst_unused:UNUSED_PAD src0_sel:DWORD src1_sel:WORD_1
v_mfma_f32_16x16x32_bf16 v[24:27],a[216:219],a[48:51],v[16:19]
s_add_u32 m0,s62,6160
ds_read_u16_d16_hi v42,v42
v_lshlrev_b32_sdwa v41,1,v41 dst_sel:DWORD dst_unused:UNUSED_PAD src0_sel:DWORD src1_sel:WORD_1
v_mfma_f32_16x16x32_bf16 v[24:27],a[220:223],a[64:67],v[24:27]
s_waitcnt lgkmcnt(9)
ds_read_b64_tr_b16 a[242:243],v3 offset:13992
v_lshl_add_u64 v[60:61],v[60:61],0,v[62:63]
v_mfma_f32_16x16x32_bf16 v[24:27],a[192:195],a[80:83],v[24:27]
s_add_u32 s68,s68,s59
ds_read_u16 v41,v41
v_lshl_add_u64 v[62:63],v[32:33],0,v[34:35]
v_lshlrev_b32_sdwa v43,1,v43 dst_sel:DWORD dst_unused:UNUSED_PAD src0_sel:DWORD src1_sel:WORD_1
v_mfma_f32_16x16x32_bf16 v[28:31],a[196:199],a[80:83],v[20:23]
buffer_load_dwordx4 v5,s[44:47],s68 offen offset:0 lds
v_lshlrev_b32_sdwa v44,1,v44 dst_sel:DWORD dst_unused:UNUSED_PAD src0_sel:DWORD src1_sel:WORD_1
v_mfma_f32_16x16x32_bf16 v[28:31],a[200:203],a[64:67],v[28:31]
s_add_u32 s68,s68,s53
ds_read_u16_d16_hi v43,v43
v_lshl_add_u64 v[32:33],v[52:53],0,v[54:55]
v_mfma_f32_16x16x32_bf16 v[28:31],a[212:215],a[48:51],v[28:31]
buffer_load_dwordx4 v5,s[44:47],s68 offen offset:1024 lds
v_lshlrev_b32_sdwa v46,1,v46 dst_sel:DWORD dst_unused:UNUSED_PAD src0_sel:DWORD src1_sel:WORD_1
v_mfma_f32_16x16x32_bf16 v[52:55],a[212:215],a[52:55],v[20:23]
ds_read_u16 v44,v44
v_or_b32_e64 v34,v56,v58
v_mfma_f32_16x16x32_bf16 v[52:55],a[200:203],a[68:71],v[52:55]
s_add_u32 s68,s68,s53
ds_read_u16_d16_hi v46,v46
v_cvt_pk_bf16_f32 v35,v57,v59
v_mfma_f32_16x16x32_bf16 v[52:55],a[196:199],a[84:87],v[52:55]
buffer_load_dwordx4 v5,s[44:47],s68 offen offset:2048 lds
v_lshlrev_b32_sdwa v45,1,v45 dst_sel:DWORD dst_unused:UNUSED_PAD src0_sel:DWORD src1_sel:WORD_1
v_mfma_f32_16x16x32_bf16 v[56:59],a[192:195],a[84:87],v[16:19]
ds_read_u16 v45,v45
v_lshlrev_b32_sdwa v47,1,v47 dst_sel:DWORD dst_unused:UNUSED_PAD src0_sel:DWORD src1_sel:WORD_1
v_mfma_f32_16x16x32_bf16 v[56:59],a[220:223],a[68:71],v[56:59]
ds_read_u16_d16_hi v47,v47
v_lshlrev_b32_sdwa v48,1,v48 dst_sel:DWORD dst_unused:UNUSED_PAD src0_sel:DWORD src1_sel:WORD_1
v_mfma_f32_16x16x32_bf16 v[56:59],a[216:219],a[52:55],v[56:59]
ds_read_u16 v48,v48
v_lshlrev_b32_sdwa v50,1,v50 dst_sel:DWORD dst_unused:UNUSED_PAD src0_sel:DWORD src1_sel:WORD_1
v_mfma_f32_16x16x32_bf16 v[136:139],a[228:231],v[60:63],v[136:139]
ds_read_u16_d16_hi v50,v50
v_lshlrev_b32_sdwa v49,1,v49 dst_sel:DWORD dst_unused:UNUSED_PAD src0_sel:DWORD src1_sel:WORD_1
v_mfma_f32_16x16x32_bf16 v[140:143],a[228:231],v[32:35],v[140:143]
ds_read_u16 v49,v49
v_mfma_f32_16x16x32_bf16 v[68:71],a[236:239],v[32:35],v[68:71]
v_lshlrev_b32_sdwa v51,1,v51 dst_sel:DWORD dst_unused:UNUSED_PAD src0_sel:DWORD src1_sel:WORD_1
v_mfma_f32_16x16x32_bf16 v[64:67],a[236:239],v[60:63],v[64:67]
ds_read_u16_d16_hi v51,v51
v_mfma_f32_16x16x32_bf16 v[144:147],a[244:247],v[60:63],v[144:147]
v_lshlrev_b32_sdwa v24,1,v24 dst_sel:DWORD dst_unused:UNUSED_PAD src0_sel:DWORD src1_sel:WORD_1
v_mfma_f32_16x16x32_bf16 v[148:151],a[244:247],v[32:35],v[148:151]
s_waitcnt lgkmcnt(6)
ds_read_u16 v24,v24
v_mfma_f32_16x16x32_bf16 v[84:87],a[252:255],v[32:35],v[84:87]
v_lshlrev_b32_sdwa v26,1,v26 dst_sel:DWORD dst_unused:UNUSED_PAD src0_sel:DWORD src1_sel:WORD_1
v_mfma_f32_16x16x32_bf16 v[80:83],a[252:255],v[60:63],v[80:83]
ds_read_u16_d16_hi v26,v26
v_mfma_f32_16x16x32_bf16 v[152:155],a[232:235],v[60:63],v[152:155]
v_lshlrev_b32_sdwa v25,1,v25 dst_sel:DWORD dst_unused:UNUSED_PAD src0_sel:DWORD src1_sel:WORD_1
v_mfma_f32_16x16x32_bf16 v[156:159],a[232:235],v[32:35],v[156:159]
ds_read_u16 v25,v25
v_mfma_f32_16x16x32_bf16 v[100:103],a[240:243],v[32:35],v[100:103]
s_add_u32 m0,s62,12320
v_lshlrev_b32_sdwa v27,1,v27 dst_sel:DWORD dst_unused:UNUSED_PAD src0_sel:DWORD src1_sel:WORD_1
v_mfma_f32_16x16x32_bf16 v[96:99],a[240:243],v[60:63],v[96:99]
s_add_u32 s68,s68,s59
ds_read_u16_d16_hi v27,v27
v_mfma_f32_16x16x32_bf16 v[32:35],a[216:219],a[56:59],v[16:19]
buffer_load_dwordx4 v5,s[44:47],s68 offen offset:0 lds
v_lshlrev_b32_sdwa v28,1,v28 dst_sel:DWORD dst_unused:UNUSED_PAD src0_sel:DWORD src1_sel:WORD_1
v_mfma_f32_16x16x32_bf16 v[32:35],a[220:223],a[72:75],v[32:35]
ds_read_u16 v28,v28
v_lshl_add_u64 v[36:37],v[36:37],0,v[38:39]
v_fma_mixlo_f16 v8,v53,s99,0
v_mfma_f32_16x16x32_bf16 v[32:35],a[192:195],a[88:91],v[32:35]
v_lshl_add_u64 v[38:39],v[40:41],0,v[42:43]
v_lshlrev_b32_sdwa v30,1,v30 dst_sel:DWORD dst_unused:UNUSED_PAD src0_sel:DWORD src1_sel:WORD_1
v_mfma_f32_16x16x32_bf16 v[60:63],a[196:199],a[88:91],v[20:23]
ds_read_u16_d16_hi v30,v30
v_fma_mixhi_f16 v8,v55,s99,0
v_mfma_f32_16x16x32_bf16 v[60:63],a[200:203],a[72:75],v[60:63]
s_waitcnt lgkmcnt(4)
v_lshl_add_u64 v[40:41],v[44:45],0,v[46:47]
v_lshlrev_b32_sdwa v29,1,v29 dst_sel:DWORD dst_unused:UNUSED_PAD src0_sel:DWORD src1_sel:WORD_1
v_pk_fma_f16 v9,v8,v8,s54
v_mfma_f32_16x16x32_bf16 v[60:63],a[212:215],a[56:59],v[60:63]
ds_read_u16 v29,v29
v_pk_mul_f16 v9,v8,v9
v_mfma_f32_16x16x32_bf16 v[44:47],a[212:215],a[60:63],v[20:23]
ds_read_b128 v[20:23],v4 offset:896
v_lshlrev_b32_sdwa v31,1,v31 dst_sel:DWORD dst_unused:UNUSED_PAD src0_sel:DWORD src1_sel:WORD_1
v_mfma_f32_16x16x32_bf16 v[44:47],a[200:203],a[76:79],v[44:47]
ds_read_b64_tr_b16 a[212:213],v1 offset:16416
ds_read_b64_tr_b16 a[214:215],v1 offset:17440
v_lshl_add_u64 v[42:43],v[48:49],0,v[50:51]
v_mfma_f32_16x16x32_bf16 v[44:47],a[196:199],a[92:95],v[44:47]
ds_read_u16_d16_hi v31,v31
v_exp_f16 v8,v9
v_lshlrev_b32_sdwa v56,1,v56 dst_sel:DWORD dst_unused:UNUSED_PAD src0_sel:DWORD src1_sel:WORD_1
v_mfma_f32_16x16x32_bf16 v[48:51],a[192:195],a[92:95],v[16:19]
ds_read_b128 v[16:19],v4 offset:928
ds_read_b64_tr_b16 a[200:201],v2 offset:16416
v_mfma_f32_16x16x32_bf16 v[48:51],a[220:223],a[76:79],v[48:51]
ds_read_b64_tr_b16 a[202:203],v2 offset:17440
ds_read_b64_tr_b16 a[196:197],v2 offset:14368
v_lshlrev_b32_sdwa v58,1,v58 dst_sel:DWORD dst_unused:UNUSED_PAD src0_sel:DWORD src1_sel:WORD_1
v_mfma_f32_16x16x32_bf16 v[48:51],a[216:219],a[60:63],v[48:51]
ds_read_b64_tr_b16 a[198:199],v2 offset:15392
v_exp_f16_sdwa v8,v9 dst_sel:WORD_1 dst_unused:UNUSED_PRESERVE src0_sel:WORD_1
v_mfma_f32_16x16x32_bf16 v[112:115],a[228:231],v[36:39],v[112:115]
ds_read_u16 v56,v56
v_lshlrev_b32_sdwa v57,1,v57 dst_sel:DWORD dst_unused:UNUSED_PAD src0_sel:DWORD src1_sel:WORD_1
v_mfma_f32_16x16x32_bf16 v[116:119],a[228:231],v[40:43],v[116:119]
ds_read_u16_d16_hi v58,v58
v_lshlrev_b32_sdwa v59,1,v59 dst_sel:DWORD dst_unused:UNUSED_PAD src0_sel:DWORD src1_sel:WORD_1
v_mfma_f32_16x16x32_bf16 v[76:79],a[236:239],v[40:43],v[76:79]
ds_read_u16 v57,v57
v_pk_fma_f16 v8,v8,s55,s55
v_mfma_f32_16x16x32_bf16 v[72:75],a[236:239],v[36:39],v[72:75]
v_rcp_f16 v9,v8
v_lshlrev_b32_sdwa v52,1,v52 dst_sel:DWORD dst_unused:UNUSED_PAD src0_sel:DWORD src1_sel:WORD_1
v_mfma_f32_16x16x32_bf16 v[120:123],a[244:247],v[36:39],v[120:123]
ds_read_u16_d16_hi v59,v59
v_lshlrev_b32_sdwa v54,1,v54 dst_sel:DWORD dst_unused:UNUSED_PAD src0_sel:DWORD src1_sel:WORD_1
v_mfma_f32_16x16x32_bf16 v[124:127],a[244:247],v[40:43],v[124:127]
ds_read_b64_tr_b16 a[192:193],v2 offset:12320
v_lshlrev_b32_sdwa v32,1,v32 dst_sel:DWORD dst_unused:UNUSED_PAD src0_sel:DWORD src1_sel:WORD_1
v_mfma_f32_16x16x32_bf16 v[92:95],a[252:255],v[40:43],v[92:95]
ds_read_b64_tr_b16 a[194:195],v2 offset:13344
v_lshlrev_b32_sdwa v34,1,v34 dst_sel:DWORD dst_unused:UNUSED_PAD src0_sel:DWORD src1_sel:WORD_1
v_mfma_f32_16x16x32_bf16 v[88:91],a[252:255],v[36:39],v[88:91]
ds_read_u16 v52,v52
v_rcp_f16_sdwa v9,v8 dst_sel:WORD_1 dst_unused:UNUSED_PRESERVE src0_sel:WORD_1
v_mfma_f32_16x16x32_bf16 v[128:131],a[232:235],v[36:39],v[128:131]
ds_read_u16_d16_hi v54,v54
v_fma_mix_f32 v53,v53,v9,0 op_sel_hi:[0,1,0]
v_mfma_f32_16x16x32_bf16 v[132:135],a[232:235],v[40:43],v[132:135]
ds_read_u16 v32,v32
v_lshlrev_b32_sdwa v33,1,v33 dst_sel:DWORD dst_unused:UNUSED_PAD src0_sel:DWORD src1_sel:WORD_1
v_mfma_f32_16x16x32_bf16 v[108:111],a[240:243],v[40:43],v[108:111]
ds_read_u16_d16_hi v34,v34
v_fma_mix_f32 v55,v55,v9,0 op_sel:[0,1,0] op_sel_hi:[0,1,0]
v_mfma_f32_16x16x32_bf16 v[104:107],a[240:243],v[36:39],v[104:107]
s_waitcnt lgkmcnt(7)
ds_read_u16 v33,v33
v_lshlrev_b32_sdwa v35,1,v35 dst_sel:DWORD dst_unused:UNUSED_PAD src0_sel:DWORD src1_sel:WORD_1
v_mfma_f32_16x16x32_bf16 v[40:43],a[208:211],a[0:3],v[20:23]
s_add_u32 s68,s68,s53
ds_read_u16_d16_hi v35,v35
v_lshlrev_b32_sdwa v60,1,v60 dst_sel:DWORD dst_unused:UNUSED_PAD src0_sel:DWORD src1_sel:WORD_1
v_mfma_f32_16x16x32_bf16 v[40:43],a[204:207],a[16:19],v[40:43]
buffer_load_dwordx4 v5,s[44:47],s68 offen offset:1024 lds
v_lshl_add_u64 v[24:25],v[24:25],0,v[26:27]
v_mfma_f32_16x16x32_bf16 v[40:43],a[212:215],a[32:35],v[40:43]
s_add_u32 s68,s68,s53
ds_read_u16 v60,v60
v_lshl_add_u64 v[26:27],v[28:29],0,v[30:31]
v_lshlrev_b32_sdwa v62,1,v62 dst_sel:DWORD dst_unused:UNUSED_PAD src0_sel:DWORD src1_sel:WORD_1
v_mfma_f32_16x16x32_bf16 v[36:39],a[200:203],a[32:35],v[16:19]
buffer_load_dwordx4 v5,s[44:47],s68 offen offset:2048 lds
v_lshlrev_b32_sdwa v61,1,v61 dst_sel:DWORD dst_unused:UNUSED_PAD src0_sel:DWORD src1_sel:WORD_1
v_mfma_f32_16x16x32_bf16 v[36:39],a[196:199],a[16:19],v[36:39]
s_waitcnt lgkmcnt(6)
ds_read_u16_d16_hi v62,v62
v_lshl_add_u64 v[28:29],v[56:57],0,v[58:59]
v_mfma_f32_16x16x32_bf16 v[36:39],a[192:195],a[0:3],v[36:39]
ds_read_u16 v61,v61
v_lshlrev_b32_sdwa v63,1,v63 dst_sel:DWORD dst_unused:UNUSED_PAD src0_sel:DWORD src1_sel:WORD_1
v_mfma_f32_16x16x32_bf16 v[56:59],a[192:195],a[4:7],v[16:19]
s_waitcnt lgkmcnt(6)
ds_read_u16_d16_hi v63,v63
v_or_b32 v30,v52,v54
v_mfma_f32_16x16x32_bf16 v[56:59],a[196:199],a[20:23],v[56:59]
s_waitcnt vmcnt(17)
v_cvt_pk_bf16_f32 v31,v53,v55
v_lshlrev_b32_sdwa v48,1,v48 dst_sel:DWORD dst_unused:UNUSED_PAD src0_sel:DWORD src1_sel:WORD_1
v_mfma_f32_16x16x32_bf16 v[56:59],a[200:203],a[36:39],v[56:59]
s_barrier
ds_read_u16 v48,v48
v_lshlrev_b32_sdwa v50,1,v50 dst_sel:DWORD dst_unused:UNUSED_PAD src0_sel:DWORD src1_sel:WORD_1
v_mfma_f32_16x16x32_bf16 v[52:55],a[212:215],a[36:39],v[20:23]
ds_read_u16_d16_hi v50,v50
v_lshlrev_b32_sdwa v49,1,v49 dst_sel:DWORD dst_unused:UNUSED_PAD src0_sel:DWORD src1_sel:WORD_1
v_mfma_f32_16x16x32_bf16 v[52:55],a[204:207],a[20:23],v[52:55]
s_add_u32 m0,s62,18480
ds_read_u16 v49,v49
v_lshlrev_b32_sdwa v51,1,v51 dst_sel:DWORD dst_unused:UNUSED_PAD src0_sel:DWORD src1_sel:WORD_1
v_mfma_f32_16x16x32_bf16 v[52:55],a[208:211],a[4:7],v[52:55]
s_add_u32 s68,s68,s59
ds_read_u16_d16_hi v51,v51
v_lshlrev_b32_sdwa v44,1,v44 dst_sel:DWORD dst_unused:UNUSED_PAD src0_sel:DWORD src1_sel:WORD_1
v_mfma_f32_16x16x32_bf16 v[232:235],a[228:231],v[24:27],v[232:235]
buffer_load_dwordx4 v5,s[44:47],s68 offen offset:0 lds
v_lshlrev_b32_sdwa v46,1,v46 dst_sel:DWORD dst_unused:UNUSED_PAD src0_sel:DWORD src1_sel:WORD_1
v_mfma_f32_16x16x32_bf16 v[236:239],a[228:231],v[28:31],v[236:239]
s_add_u32 m0,s58,6160
ds_read_u16 v44,v44
v_mfma_f32_16x16x32_bf16 v[164:167],a[236:239],v[28:31],v[164:167]
s_add_u32 s69,s69,s59
ds_read_u16_d16_hi v46,v46
ds_read_b64_tr_b16 a[224:225],v3 offset:18480
v_mfma_f32_16x16x32_bf16 v[160:163],a[236:239],v[24:27],v[160:163]
buffer_load_dwordx4 v5,s[44:47],s69 offen offset:0 lds
v_lshlrev_b32_sdwa v45,1,v45 dst_sel:DWORD dst_unused:UNUSED_PAD src0_sel:DWORD src1_sel:WORD_1
v_mfma_f32_16x16x32_bf16 v[240:243],a[244:247],v[24:27],v[240:243]
s_add_u32 s69,s69,s53
ds_read_u16 v45,v45
v_lshlrev_b32_sdwa v47,1,v47 dst_sel:DWORD dst_unused:UNUSED_PAD src0_sel:DWORD src1_sel:WORD_1
v_mfma_f32_16x16x32_bf16 v[244:247],a[244:247],v[28:31],v[244:247]
buffer_load_dwordx4 v5,s[44:47],s69 offen offset:1024 lds
ds_read_u16_d16_hi v47,v47
v_mfma_f32_16x16x32_bf16 v[180:183],a[252:255],v[28:31],v[180:183]
ds_read_b64_tr_b16 a[226:227],v3 offset:20016
v_lshlrev_b32_sdwa v40,1,v40 dst_sel:DWORD dst_unused:UNUSED_PAD src0_sel:DWORD src1_sel:WORD_1
v_mfma_f32_16x16x32_bf16 v[176:179],a[252:255],v[24:27],v[176:179]
ds_read_u16 v40,v40
v_lshlrev_b32_sdwa v42,1,v42 dst_sel:DWORD dst_unused:UNUSED_PAD src0_sel:DWORD src1_sel:WORD_1
v_mfma_f32_16x16x32_bf16 v[248:251],a[232:235],v[24:27],v[248:251]
ds_read_u16_d16_hi v42,v42
v_lshlrev_b32_sdwa v41,1,v41 dst_sel:DWORD dst_unused:UNUSED_PAD src0_sel:DWORD src1_sel:WORD_1
v_mfma_f32_16x16x32_bf16 v[252:255],a[232:235],v[28:31],v[252:255]
ds_read_u16 v41,v41
v_mfma_f32_16x16x32_bf16 v[196:199],a[240:243],v[28:31],v[196:199]
s_add_u32 s69,s69,s53
ds_read_b64_tr_b16 a[248:249],v3 offset:18488
v_lshlrev_b32_sdwa v43,1,v43 dst_sel:DWORD dst_unused:UNUSED_PAD src0_sel:DWORD src1_sel:WORD_1
v_mfma_f32_16x16x32_bf16 v[192:195],a[240:243],v[24:27],v[192:195]
buffer_load_dwordx4 v5,s[44:47],s69 offen offset:2048 lds
v_mfma_f32_16x16x32_bf16 v[28:31],a[208:211],a[8:11],v[20:23]
ds_read_u16_d16_hi v43,v43
v_lshlrev_b32_sdwa v36,1,v36 dst_sel:DWORD dst_unused:UNUSED_PAD src0_sel:DWORD src1_sel:WORD_1
v_mfma_f32_16x16x32_bf16 v[28:31],a[204:207],a[24:27],v[28:31]
s_waitcnt lgkmcnt(7)
ds_read_u16 v36,v36
v_lshl_add_u64 v[32:33],v[32:33],0,v[34:35]
v_fma_mixlo_f16 v8,v57,s99,0
v_mfma_f32_16x16x32_bf16 v[28:31],a[212:215],a[40:43],v[28:31]
ds_read_b64_tr_b16 a[250:251],v3 offset:20024
v_lshl_add_u64 v[34:35],v[60:61],0,v[62:63]
v_lshlrev_b32_sdwa v38,1,v38 dst_sel:DWORD dst_unused:UNUSED_PAD src0_sel:DWORD src1_sel:WORD_1
v_mfma_f32_16x16x32_bf16 v[24:27],a[200:203],a[40:43],v[16:19]
ds_read_u16_d16_hi v38,v38
v_fma_mixhi_f16 v8,v59,s99,0
v_mfma_f32_16x16x32_bf16 v[24:27],a[196:199],a[24:27],v[24:27]
ds_read_b64_tr_b16 a[220:221],v1 offset:18480
v_lshl_add_u64 v[60:61],v[48:49],0,v[50:51]
v_lshlrev_b32_sdwa v37,1,v37 dst_sel:DWORD dst_unused:UNUSED_PAD src0_sel:DWORD src1_sel:WORD_1
v_pk_fma_f16 v9,v8,v8,s54
v_mfma_f32_16x16x32_bf16 v[24:27],a[192:195],a[8:11],v[24:27]
ds_read_u16 v37,v37
v_pk_mul_f16 v9,v8,v9
v_mfma_f32_16x16x32_bf16 v[48:51],a[192:195],a[12:15],v[16:19]
ds_read_b64_tr_b16 a[222:223],v1 offset:19504
v_lshlrev_b32_sdwa v39,1,v39 dst_sel:DWORD dst_unused:UNUSED_PAD src0_sel:DWORD src1_sel:WORD_1
v_mfma_f32_16x16x32_bf16 v[48:51],a[196:199],a[28:31],v[48:51]
s_waitcnt lgkmcnt(7)
ds_read_u16_d16_hi v39,v39
v_lshl_add_u64 v[62:63],v[44:45],0,v[46:47]
v_mfma_f32_16x16x32_bf16 v[48:51],a[200:203],a[44:47],v[48:51]
v_exp_f16 v8,v9
v_lshlrev_b32_sdwa v52,1,v52 dst_sel:DWORD dst_unused:UNUSED_PAD src0_sel:DWORD src1_sel:WORD_1
v_mfma_f32_16x16x32_bf16 v[44:47],a[212:215],a[44:47],v[20:23]
ds_read_u16 v52,v52
v_lshlrev_b32_sdwa v54,1,v54 dst_sel:DWORD dst_unused:UNUSED_PAD src0_sel:DWORD src1_sel:WORD_1
v_mfma_f32_16x16x32_bf16 v[44:47],a[204:207],a[28:31],v[44:47]
ds_read_u16_d16_hi v54,v54
v_lshlrev_b32_sdwa v53,1,v53 dst_sel:DWORD dst_unused:UNUSED_PAD src0_sel:DWORD src1_sel:WORD_1
v_mfma_f32_16x16x32_bf16 v[44:47],a[208:211],a[12:15],v[44:47]
v_exp_f16_sdwa v8,v9 dst_sel:WORD_1 dst_unused:UNUSED_PRESERVE src0_sel:WORD_1
v_mfma_f32_16x16x32_bf16 v[208:211],a[228:231],v[32:35],v[208:211]
ds_read_u16 v53,v53
v_lshlrev_b32_sdwa v55,1,v55 dst_sel:DWORD dst_unused:UNUSED_PAD src0_sel:DWORD src1_sel:WORD_1
v_mfma_f32_16x16x32_bf16 v[212:215],a[228:231],v[60:63],v[212:215]
ds_read_u16_d16_hi v55,v55
v_pk_fma_f16 v8,v8,s55,s55
v_mfma_f32_16x16x32_bf16 v[172:175],a[236:239],v[60:63],v[172:175]
ds_read_b64_tr_b16 a[228:229],v3 offset:18544
v_lshlrev_b32_sdwa v56,1,v56 dst_sel:DWORD dst_unused:UNUSED_PAD src0_sel:DWORD src1_sel:WORD_1
v_mfma_f32_16x16x32_bf16 v[168:171],a[236:239],v[32:35],v[168:171]
ds_read_u16 v56,v56
v_rcp_f16_e64 v9,v8
v_mfma_f32_16x16x32_bf16 v[216:219],a[244:247],v[32:35],v[216:219]
ds_read_b64_tr_b16 a[230:231],v3 offset:20080
v_lshlrev_b32_sdwa v58,1,v58 dst_sel:DWORD dst_unused:UNUSED_PAD src0_sel:DWORD src1_sel:WORD_1
v_mfma_f32_16x16x32_bf16 v[220:223],a[244:247],v[60:63],v[220:223]
ds_read_u16_d16_hi v58,v58
ds_read_b64_tr_b16 a[236:237],v3 offset:18552
v_mfma_f32_16x16x32_bf16 v[188:191],a[252:255],v[60:63],v[188:191]
ds_read_b64_tr_b16 a[238:239],v3 offset:20088
v_lshlrev_b32_sdwa v28,1,v28 dst_sel:DWORD dst_unused:UNUSED_PAD src0_sel:DWORD src1_sel:WORD_1
v_mfma_f32_16x16x32_bf16 v[184:187],a[252:255],v[32:35],v[184:187]
ds_read_u16 v28,v28
v_rcp_f16_sdwa v9,v8 dst_sel:WORD_1 dst_unused:UNUSED_PRESERVE src0_sel:WORD_1
v_mfma_f32_16x16x32_bf16 v[224:227],a[232:235],v[32:35],v[224:227]
ds_read_b64_tr_b16 a[244:245],v3 offset:18608
v_lshlrev_b32_sdwa v30,1,v30 dst_sel:DWORD dst_unused:UNUSED_PAD src0_sel:DWORD src1_sel:WORD_1
v_mfma_f32_16x16x32_bf16 v[228:231],a[232:235],v[60:63],v[228:231]
ds_read_u16_d16_hi v30,v30
v_fma_mix_f32 v57,v57,v9,0 op_sel_hi:[0,1,0]
v_mfma_f32_16x16x32_bf16 v[204:207],a[240:243],v[60:63],v[204:207]
ds_read_b64_tr_b16 a[246:247],v3 offset:20144
v_lshlrev_b32_sdwa v29,1,v29 dst_sel:DWORD dst_unused:UNUSED_PAD src0_sel:DWORD src1_sel:WORD_1
v_mfma_f32_16x16x32_bf16 v[200:203],a[240:243],v[32:35],v[200:203]
ds_read_u16 v29,v29
v_fma_mix_f32 v59,v59,v9,0 op_sel:[0,1,0] op_sel_hi:[0,1,0]
v_lshlrev_b32_sdwa v31,1,v31 dst_sel:DWORD dst_unused:UNUSED_PAD src0_sel:DWORD src1_sel:WORD_1
v_mfma_f32_16x16x32_bf16 v[60:63],a[208:211],a[48:51],v[20:23]
s_add_u32 m0,s62,18480
ds_read_u16_d16_hi v31,v31
ds_read_b64_tr_b16 a[252:253],v3 offset:18616
v_lshlrev_b32_sdwa v24,1,v24 dst_sel:DWORD dst_unused:UNUSED_PAD src0_sel:DWORD src1_sel:WORD_1
v_mfma_f32_16x16x32_bf16 v[60:63],a[204:207],a[64:67],v[60:63]
s_waitcnt lgkmcnt(10)
ds_read_b64_tr_b16 a[254:255],v3 offset:20152
v_lshl_add_u64 v[40:41],v[40:41],0,v[42:43]
v_mfma_f32_16x16x32_bf16 v[60:63],a[212:215],a[80:83],v[60:63]
s_add_u32 s68,s68,s53
ds_read_u16 v24,v24
v_lshl_add_u64 v[42:43],v[36:37],0,v[38:39]
v_lshlrev_b32_sdwa v26,1,v26 dst_sel:DWORD dst_unused:UNUSED_PAD src0_sel:DWORD src1_sel:WORD_1
v_mfma_f32_16x16x32_bf16 v[32:35],a[200:203],a[80:83],v[16:19]
buffer_load_dwordx4 v5,s[44:47],s68 offen offset:1024 lds
v_lshlrev_b32_sdwa v25,1,v25 dst_sel:DWORD dst_unused:UNUSED_PAD src0_sel:DWORD src1_sel:WORD_1
v_mfma_f32_16x16x32_bf16 v[32:35],a[196:199],a[64:67],v[32:35]
s_add_u32 m0,s58,12320
ds_read_u16_d16_hi v26,v26
v_lshl_add_u64 v[36:37],v[52:53],0,v[54:55]
v_mfma_f32_16x16x32_bf16 v[32:35],a[192:195],a[48:51],v[32:35]
s_add_u32 s69,s69,s59
ds_read_u16 v25,v25
v_lshlrev_b32_sdwa v27,1,v27 dst_sel:DWORD dst_unused:UNUSED_PAD src0_sel:DWORD src1_sel:WORD_1
v_mfma_f32_16x16x32_bf16 v[52:55],a[192:195],a[52:55],v[16:19]
s_waitcnt lgkmcnt(8)
buffer_load_dwordx4 v5,s[44:47],s69 offen offset:0 lds
v_or_b32 v38,v56,v58
v_mfma_f32_16x16x32_bf16 v[52:55],a[196:199],a[68:71],v[52:55]
s_add_u32 s69,s69,s53
ds_read_u16_d16_hi v27,v27
v_cvt_pk_bf16_f32 v39,v57,v59
v_lshlrev_b32_sdwa v44,1,v44 dst_sel:DWORD dst_unused:UNUSED_PAD src0_sel:DWORD src1_sel:WORD_1
v_mfma_f32_16x16x32_bf16 v[52:55],a[200:203],a[84:87],v[52:55]
buffer_load_dwordx4 v5,s[44:47],s69 offen offset:1024 lds
v_lshlrev_b32_sdwa v46,1,v46 dst_sel:DWORD dst_unused:UNUSED_PAD src0_sel:DWORD src1_sel:WORD_1
v_mfma_f32_16x16x32_bf16 v[56:59],a[212:215],a[84:87],v[20:23]
s_add_u32 m0,s62,18480
ds_read_u16 v44,v44
v_lshlrev_b32_sdwa v45,1,v45 dst_sel:DWORD dst_unused:UNUSED_PAD src0_sel:DWORD src1_sel:WORD_1
v_mfma_f32_16x16x32_bf16 v[56:59],a[204:207],a[68:71],v[56:59]
s_add_u32 s68,s68,s53
ds_read_u16_d16_hi v46,v46
v_lshlrev_b32_sdwa v47,1,v47 dst_sel:DWORD dst_unused:UNUSED_PAD src0_sel:DWORD src1_sel:WORD_1
v_mfma_f32_16x16x32_bf16 v[56:59],a[208:211],a[52:55],v[56:59]
buffer_load_dwordx4 v5,s[44:47],s68 offen offset:2048 lds
ds_read_u16 v45,v45
v_mfma_f32_16x16x32_bf16 v[136:139],a[224:227],v[40:43],v[136:139]
s_add_u32 m0,s58,12320
ds_read_u16_d16_hi v47,v47
v_lshlrev_b32_sdwa v48,1,v48 dst_sel:DWORD dst_unused:UNUSED_PAD src0_sel:DWORD src1_sel:WORD_1
v_mfma_f32_16x16x32_bf16 v[140:143],a[224:227],v[36:39],v[140:143]
s_add_u32 s69,s69,s53
ds_read_u16 v48,v48
v_lshlrev_b32_sdwa v50,1,v50 dst_sel:DWORD dst_unused:UNUSED_PAD src0_sel:DWORD src1_sel:WORD_1
v_mfma_f32_16x16x32_bf16 v[68:71],a[248:251],v[36:39],v[68:71]
TE12_%=:
s_cbranch_execz TP10688_%=
buffer_load_dwordx4 v5,s[44:47],s69 offen offset:2048 lds
v_lshlrev_b32_sdwa v49,1,v49 dst_sel:DWORD dst_unused:UNUSED_PAD src0_sel:DWORD src1_sel:WORD_1
v_mfma_f32_16x16x32_bf16 v[64:67],a[248:251],v[40:43],v[64:67]
s_add_u32 m0,s62,24640
ds_read_u16_d16_hi v50,v50
v_mfma_f32_16x16x32_bf16 v[144:147],a[228:231],v[40:43],v[144:147]
s_add_u32 s68,s68,s59
ds_read_u16 v49,v49
v_lshlrev_b32_sdwa v51,1,v51 dst_sel:DWORD dst_unused:UNUSED_PAD src0_sel:DWORD src1_sel:WORD_1
v_mfma_f32_16x16x32_bf16 v[148:151],a[228:231],v[36:39],v[148:151]
buffer_load_dwordx4 v5,s[44:47],s68 offen offset:0 lds
v_lshlrev_b32_sdwa v60,1,v60 dst_sel:DWORD dst_unused:UNUSED_PAD src0_sel:DWORD src1_sel:WORD_1
v_mfma_f32_16x16x32_bf16 v[84:87],a[236:239],v[36:39],v[84:87]
s_add_u32 s68,s68,s53
ds_read_u16_d16_hi v51,v51
v_mfma_f32_16x16x32_bf16 v[80:83],a[236:239],v[40:43],v[80:83]
s_waitcnt lgkmcnt(6)
buffer_load_dwordx4 v5,s[44:47],s68 offen offset:1024 lds
v_lshlrev_b32_sdwa v62,1,v62 dst_sel:DWORD dst_unused:UNUSED_PAD src0_sel:DWORD src1_sel:WORD_1
v_mfma_f32_16x16x32_bf16 v[152:155],a[244:247],v[40:43],v[152:155]
ds_read_u16 v60,v60
v_lshlrev_b32_sdwa v61,1,v61 dst_sel:DWORD dst_unused:UNUSED_PAD src0_sel:DWORD src1_sel:WORD_1
v_mfma_f32_16x16x32_bf16 v[156:159],a[244:247],v[36:39],v[156:159]
ds_read_u16_d16_hi v62,v62
v_mfma_f32_16x16x32_bf16 v[100:103],a[252:255],v[36:39],v[100:103]
ds_read_u16 v61,v61
v_lshlrev_b32_sdwa v63,1,v63 dst_sel:DWORD dst_unused:UNUSED_PAD src0_sel:DWORD src1_sel:WORD_1
v_mfma_f32_16x16x32_bf16 v[96:99],a[252:255],v[40:43],v[96:99]
ds_read_u16_d16_hi v63,v63
v_mfma_f32_16x16x32_bf16 v[36:39],a[208:211],a[56:59],v[20:23]
ds_read_b64_tr_b16 a[216:217],v1 offset:20528
v_lshlrev_b32_sdwa v32,1,v32 dst_sel:DWORD dst_unused:UNUSED_PAD src0_sel:DWORD src1_sel:WORD_1
v_mfma_f32_16x16x32_bf16 v[36:39],a[204:207],a[72:75],v[36:39]
ds_read_b64_tr_b16 a[218:219],v1 offset:21552
v_lshl_add_u64 v[28:29],v[28:29],0,v[30:31]
v_fma_mixlo_f16 v8,v53,s99,0
v_mfma_f32_16x16x32_bf16 v[36:39],a[212:215],a[88:91],v[36:39]
ds_read_u16 v32,v32
v_lshl_add_u64 v[30:31],v[24:25],0,v[26:27]
v_lshlrev_b32_sdwa v34,1,v34 dst_sel:DWORD dst_unused:UNUSED_PAD src0_sel:DWORD src1_sel:WORD_1
v_mfma_f32_16x16x32_bf16 v[40:43],a[200:203],a[88:91],v[16:19]
ds_read_u16_d16_hi v34,v34
v_fma_mixhi_f16 v8,v55,s99,0
v_mfma_f32_16x16x32_bf16 v[40:43],a[196:199],a[72:75],v[40:43]
s_waitcnt lgkmcnt(8)
v_lshl_add_u64 v[24:25],v[44:45],0,v[46:47]
v_lshlrev_b32_sdwa v33,1,v33 dst_sel:DWORD dst_unused:UNUSED_PAD src0_sel:DWORD src1_sel:WORD_1
v_pk_fma_f16 v9,v8,v8,s54
v_mfma_f32_16x16x32_bf16 v[40:43],a[192:195],a[56:59],v[40:43]
ds_read_u16 v33,v33
v_pk_mul_f16 v9,v8,v9
v_mfma_f32_16x16x32_bf16 v[44:47],a[192:195],a[60:63],v[16:19]
ds_read_b128 v[16:19],v4 offset:1024
v_lshlrev_b32_sdwa v35,1,v35 dst_sel:DWORD dst_unused:UNUSED_PAD src0_sel:DWORD src1_sel:WORD_1
v_mfma_f32_16x16x32_bf16 v[44:47],a[196:199],a[76:79],v[44:47]
ds_read_b64_tr_b16 a[192:193],v1 offset:22576
ds_read_b64_tr_b16 a[194:195],v1 offset:23600
v_lshl_add_u64 v[26:27],v[48:49],0,v[50:51]
v_mfma_f32_16x16x32_bf16 v[44:47],a[200:203],a[92:95],v[44:47]
ds_read_u16_d16_hi v35,v35
v_exp_f16 v8,v9
v_lshlrev_b32_sdwa v56,1,v56 dst_sel:DWORD dst_unused:UNUSED_PAD src0_sel:DWORD src1_sel:WORD_1
v_mfma_f32_16x16x32_bf16 v[48:51],a[212:215],a[92:95],v[20:23]
ds_read_b128 v[20:23],v4 offset:1056
ds_read_b64_tr_b16 a[196:197],v2 offset:22576
v_mfma_f32_16x16x32_bf16 v[48:51],a[204:207],a[76:79],v[48:51]
ds_read_b64_tr_b16 a[198:199],v2 offset:23600
ds_read_b64_tr_b16 a[200:201],v2 offset:20528
v_lshlrev_b32_sdwa v58,1,v58 dst_sel:DWORD dst_unused:UNUSED_PAD src0_sel:DWORD src1_sel:WORD_1
v_mfma_f32_16x16x32_bf16 v[48:51],a[208:211],a[60:63],v[48:51]
ds_read_b64_tr_b16 a[202:203],v2 offset:21552
v_exp_f16_sdwa v8,v9 dst_sel:WORD_1 dst_unused:UNUSED_PRESERVE src0_sel:WORD_1
v_mfma_f32_16x16x32_bf16 v[112:115],a[224:227],v[28:31],v[112:115]
ds_read_u16 v56,v56
v_lshlrev_b32_sdwa v57,1,v57 dst_sel:DWORD dst_unused:UNUSED_PAD src0_sel:DWORD src1_sel:WORD_1
v_mfma_f32_16x16x32_bf16 v[116:119],a[224:227],v[24:27],v[116:119]
ds_read_u16_d16_hi v58,v58
v_lshlrev_b32_sdwa v59,1,v59 dst_sel:DWORD dst_unused:UNUSED_PAD src0_sel:DWORD src1_sel:WORD_1
v_mfma_f32_16x16x32_bf16 v[76:79],a[248:251],v[24:27],v[76:79]
ds_read_u16 v57,v57
v_pk_fma_f16 v8,v8,s55,s55
v_mfma_f32_16x16x32_bf16 v[72:75],a[248:251],v[28:31],v[72:75]
v_rcp_f16_e64 v9,v8
v_lshlrev_b32_sdwa v52,1,v52 dst_sel:DWORD dst_unused:UNUSED_PAD src0_sel:DWORD src1_sel:WORD_1
v_mfma_f32_16x16x32_bf16 v[120:123],a[228:231],v[28:31],v[120:123]
ds_read_u16_d16_hi v59,v59
v_lshlrev_b32_sdwa v54,1,v54 dst_sel:DWORD dst_unused:UNUSED_PAD src0_sel:DWORD src1_sel:WORD_1
v_mfma_f32_16x16x32_bf16 v[124:127],a[228:231],v[24:27],v[124:127]
ds_read_b64_tr_b16 a[212:213],v2 offset:18480
v_lshlrev_b32_sdwa v36,1,v36 dst_sel:DWORD dst_unused:UNUSED_PAD src0_sel:DWORD src1_sel:WORD_1
v_mfma_f32_16x16x32_bf16 v[92:95],a[236:239],v[24:27],v[92:95]
ds_read_b64_tr_b16 a[214:215],v2 offset:19504
v_lshlrev_b32_sdwa v38,1,v38 dst_sel:DWORD dst_unused:UNUSED_PAD src0_sel:DWORD src1_sel:WORD_1
v_mfma_f32_16x16x32_bf16 v[88:91],a[236:239],v[28:31],v[88:91]
ds_read_u16 v52,v52
v_rcp_f16_sdwa v9,v8 dst_sel:WORD_1 dst_unused:UNUSED_PRESERVE src0_sel:WORD_1
v_mfma_f32_16x16x32_bf16 v[128:131],a[244:247],v[28:31],v[128:131]
ds_read_u16_d16_hi v54,v54
v_fma_mix_f32 v53,v53,v9,0 op_sel_hi:[0,1,0]
v_mfma_f32_16x16x32_bf16 v[132:135],a[244:247],v[24:27],v[132:135]
ds_read_u16 v36,v36
v_lshlrev_b32_sdwa v37,1,v37 dst_sel:DWORD dst_unused:UNUSED_PAD src0_sel:DWORD src1_sel:WORD_1
v_mfma_f32_16x16x32_bf16 v[108:111],a[252:255],v[24:27],v[108:111]
ds_read_u16_d16_hi v38,v38
v_fma_mix_f32 v55,v55,v9,0 op_sel:[0,1,0] op_sel_hi:[0,1,0]
v_mfma_f32_16x16x32_bf16 v[104:107],a[252:255],v[28:31],v[104:107]
s_waitcnt lgkmcnt(7)
ds_read_u16 v37,v37
v_lshlrev_b32_sdwa v39,1,v39 dst_sel:DWORD dst_unused:UNUSED_PAD src0_sel:DWORD src1_sel:WORD_1
v_mfma_f32_16x16x32_bf16 v[24:27],a[220:223],a[0:3],v[16:19]
s_add_u32 s68,s68,s53
ds_read_u16_d16_hi v39,v39
v_lshlrev_b32_sdwa v40,1,v40 dst_sel:DWORD dst_unused:UNUSED_PAD src0_sel:DWORD src1_sel:WORD_1
v_mfma_f32_16x16x32_bf16 v[24:27],a[216:219],a[16:19],v[24:27]
buffer_load_dwordx4 v5,s[44:47],s68 offen offset:2048 lds
v_lshl_add_u64 v[60:61],v[60:61],0,v[62:63]
v_mfma_f32_16x16x32_bf16 v[24:27],a[192:195],a[32:35],v[24:27]
ds_read_u16 v40,v40
v_lshl_add_u64 v[62:63],v[32:33],0,v[34:35]
v_lshlrev_b32_sdwa v42,1,v42 dst_sel:DWORD dst_unused:UNUSED_PAD src0_sel:DWORD src1_sel:WORD_1
v_mfma_f32_16x16x32_bf16 v[28:31],a[196:199],a[32:35],v[20:23]
ds_read_u16_d16_hi v42,v42
v_lshlrev_b32_sdwa v41,1,v41 dst_sel:DWORD dst_unused:UNUSED_PAD src0_sel:DWORD src1_sel:WORD_1
v_mfma_f32_16x16x32_bf16 v[28:31],a[200:203],a[16:19],v[28:31]
s_waitcnt lgkmcnt(7)
ds_read_u16 v41,v41
v_lshl_add_u64 v[32:33],v[56:57],0,v[58:59]
v_mfma_f32_16x16x32_bf16 v[28:31],a[212:215],a[0:3],v[28:31]
v_lshlrev_b32_sdwa v43,1,v43 dst_sel:DWORD dst_unused:UNUSED_PAD src0_sel:DWORD src1_sel:WORD_1
v_lshlrev_b32_sdwa v48,1,v48 dst_sel:DWORD dst_unused:UNUSED_PAD src0_sel:DWORD src1_sel:WORD_1
v_mfma_f32_16x16x32_bf16 v[56:59],a[212:215],a[4:7],v[20:23]
s_waitcnt lgkmcnt(6)
ds_read_u16_d16_hi v43,v43
v_or_b32 v34,v52,v54
v_mfma_f32_16x16x32_bf16 v[56:59],a[200:203],a[20:23],v[56:59]
ds_read_u16 v48,v48
v_cvt_pk_bf16_f32 v35,v53,v55
v_mfma_f32_16x16x32_bf16 v[56:59],a[196:199],a[36:39],v[56:59]
v_lshlrev_b32_sdwa v50,1,v50 dst_sel:DWORD dst_unused:UNUSED_PAD src0_sel:DWORD src1_sel:WORD_1
v_lshlrev_b32_sdwa v49,1,v49 dst_sel:DWORD dst_unused:UNUSED_PAD src0_sel:DWORD src1_sel:WORD_1
v_mfma_f32_16x16x32_bf16 v[52:55],a[192:195],a[36:39],v[16:19]
ds_read_u16_d16_hi v50,v50
v_lshlrev_b32_sdwa v51,1,v51 dst_sel:DWORD dst_unused:UNUSED_PAD src0_sel:DWORD src1_sel:WORD_1
v_mfma_f32_16x16x32_bf16 v[52:55],a[216:219],a[20:23],v[52:55]
ds_read_u16 v49,v49
v_mfma_f32_16x16x32_bf16 v[52:55],a[220:223],a[4:7],v[52:55]
ds_read_u16_d16_hi v51,v51
v_lshlrev_b32_sdwa v44,1,v44 dst_sel:DWORD dst_unused:UNUSED_PAD src0_sel:DWORD src1_sel:WORD_1
v_mfma_f32_16x16x32_bf16 v[232:235],a[224:227],v[60:63],v[232:235]
ds_read_u16 v44,v44
v_lshlrev_b32_sdwa v46,1,v46 dst_sel:DWORD dst_unused:UNUSED_PAD src0_sel:DWORD src1_sel:WORD_1
v_mfma_f32_16x16x32_bf16 v[236:239],a[224:227],v[32:35],v[236:239]
ds_read_u16_d16_hi v46,v46
v_mfma_f32_16x16x32_bf16 v[164:167],a[248:251],v[32:35],v[164:167]
ds_read_b64_tr_b16 a[232:233],v3 offset:0
v_lshlrev_b32_sdwa v45,1,v45 dst_sel:DWORD dst_unused:UNUSED_PAD src0_sel:DWORD src1_sel:WORD_1
v_mfma_f32_16x16x32_bf16 v[160:163],a[248:251],v[60:63],v[160:163]
ds_read_u16 v45,v45
v_mfma_f32_16x16x32_bf16 v[240:243],a[228:231],v[60:63],v[240:243]
ds_read_b64_tr_b16 a[234:235],v3 offset:1536
v_lshlrev_b32_sdwa v47,1,v47 dst_sel:DWORD dst_unused:UNUSED_PAD src0_sel:DWORD src1_sel:WORD_1
v_mfma_f32_16x16x32_bf16 v[244:247],a[228:231],v[32:35],v[244:247]
ds_read_u16_d16_hi v47,v47
v_lshlrev_b32_sdwa v24,1,v24 dst_sel:DWORD dst_unused:UNUSED_PAD src0_sel:DWORD src1_sel:WORD_1
v_mfma_f32_16x16x32_bf16 v[180:183],a[236:239],v[32:35],v[180:183]
ds_read_u16 v24,v24
v_mfma_f32_16x16x32_bf16 v[176:179],a[236:239],v[60:63],v[176:179]
ds_read_b64_tr_b16 a[240:241],v3 offset:8
v_lshlrev_b32_sdwa v26,1,v26 dst_sel:DWORD dst_unused:UNUSED_PAD src0_sel:DWORD src1_sel:WORD_1
v_mfma_f32_16x16x32_bf16 v[248:251],a[244:247],v[60:63],v[248:251]
ds_read_u16_d16_hi v26,v26
v_lshlrev_b32_sdwa v25,1,v25 dst_sel:DWORD dst_unused:UNUSED_PAD src0_sel:DWORD src1_sel:WORD_1
v_mfma_f32_16x16x32_bf16 v[252:255],a[244:247],v[32:35],v[252:255]
ds_read_u16 v25,v25
v_mfma_f32_16x16x32_bf16 v[196:199],a[252:255],v[32:35],v[196:199]
ds_read_b64_tr_b16 a[242:243],v3 offset:1544
v_lshlrev_b32_sdwa v27,1,v27 dst_sel:DWORD dst_unused:UNUSED_PAD src0_sel:DWORD src1_sel:WORD_1
v_mfma_f32_16x16x32_bf16 v[192:195],a[252:255],v[60:63],v[192:195]
ds_read_u16_d16_hi v27,v27
v_mfma_f32_16x16x32_bf16 v[32:35],a[220:223],a[8:11],v[16:19]
ds_read_b64_tr_b16 a[204:205],v1 offset:0
v_lshlrev_b32_sdwa v28,1,v28 dst_sel:DWORD dst_unused:UNUSED_PAD src0_sel:DWORD src1_sel:WORD_1
v_mfma_f32_16x16x32_bf16 v[32:35],a[216:219],a[24:27],v[32:35]
s_waitcnt lgkmcnt(8)
ds_read_u16 v28,v28
v_lshl_add_u64 v[36:37],v[36:37],0,v[38:39]
v_fma_mixlo_f16 v8,v57,s99,0
v_mfma_f32_16x16x32_bf16 v[32:35],a[192:195],a[40:43],v[32:35]
ds_read_b64_tr_b16 a[206:207],v1 offset:1024
v_lshl_add_u64 v[38:39],v[40:41],0,v[42:43]
v_lshlrev_b32_sdwa v30,1,v30 dst_sel:DWORD dst_unused:UNUSED_PAD src0_sel:DWORD src1_sel:WORD_1
v_mfma_f32_16x16x32_bf16 v[60:63],a[196:199],a[40:43],v[20:23]
ds_read_u16_d16_hi v30,v30
v_fma_mixhi_f16 v8,v59,s99,0
v_mfma_f32_16x16x32_bf16 v[60:63],a[200:203],a[24:27],v[60:63]
ds_read_b64_tr_b16 a[208:209],v1 offset:2048
v_lshl_add_u64 v[40:41],v[48:49],0,v[50:51]
v_lshlrev_b32_sdwa v29,1,v29 dst_sel:DWORD dst_unused:UNUSED_PAD src0_sel:DWORD src1_sel:WORD_1
v_pk_fma_f16 v9,v8,v8,s54
v_mfma_f32_16x16x32_bf16 v[60:63],a[212:215],a[8:11],v[60:63]
ds_read_u16 v29,v29
v_pk_mul_f16 v9,v8,v9
v_mfma_f32_16x16x32_bf16 v[48:51],a[212:215],a[12:15],v[20:23]
ds_read_b64_tr_b16 a[210:211],v1 offset:3072
v_lshlrev_b32_sdwa v31,1,v31 dst_sel:DWORD dst_unused:UNUSED_PAD src0_sel:DWORD src1_sel:WORD_1
v_mfma_f32_16x16x32_bf16 v[48:51],a[200:203],a[28:31],v[48:51]
s_waitcnt lgkmcnt(8)
ds_read_u16_d16_hi v31,v31
v_lshl_add_u64 v[42:43],v[44:45],0,v[46:47]
v_mfma_f32_16x16x32_bf16 v[48:51],a[196:199],a[44:47],v[48:51]
v_exp_f16 v8,v9
v_lshlrev_b32_sdwa v52,1,v52 dst_sel:DWORD dst_unused:UNUSED_PAD src0_sel:DWORD src1_sel:WORD_1
v_mfma_f32_16x16x32_bf16 v[44:47],a[192:195],a[44:47],v[16:19]
ds_read_u16 v52,v52
v_lshlrev_b32_sdwa v54,1,v54 dst_sel:DWORD dst_unused:UNUSED_PAD src0_sel:DWORD src1_sel:WORD_1
v_mfma_f32_16x16x32_bf16 v[44:47],a[216:219],a[28:31],v[44:47]
ds_read_u16_d16_hi v54,v54
v_lshlrev_b32_sdwa v53,1,v53 dst_sel:DWORD dst_unused:UNUSED_PAD src0_sel:DWORD src1_sel:WORD_1
v_mfma_f32_16x16x32_bf16 v[44:47],a[220:223],a[12:15],v[44:47]
v_exp_f16_sdwa v8,v9 dst_sel:WORD_1 dst_unused:UNUSED_PRESERVE src0_sel:WORD_1
v_mfma_f32_16x16x32_bf16 v[208:211],a[224:227],v[36:39],v[208:211]
ds_read_u16 v53,v53
v_lshlrev_b32_sdwa v55,1,v55 dst_sel:DWORD dst_unused:UNUSED_PAD src0_sel:DWORD src1_sel:WORD_1
v_mfma_f32_16x16x32_bf16 v[212:215],a[224:227],v[40:43],v[212:215]
ds_read_u16_d16_hi v55,v55
v_pk_fma_f16 v8,v8,s55,s55
v_mfma_f32_16x16x32_bf16 v[172:175],a[248:251],v[40:43],v[172:175]
ds_read_b64_tr_b16 a[224:225],v3 offset:64
v_lshlrev_b32_sdwa v56,1,v56 dst_sel:DWORD dst_unused:UNUSED_PAD src0_sel:DWORD src1_sel:WORD_1
v_mfma_f32_16x16x32_bf16 v[168:171],a[248:251],v[36:39],v[168:171]
ds_read_u16 v56,v56
v_rcp_f16_e64 v9,v8
v_mfma_f32_16x16x32_bf16 v[216:219],a[228:231],v[36:39],v[216:219]
ds_read_b64_tr_b16 a[226:227],v3 offset:1600
v_lshlrev_b32_sdwa v58,1,v58 dst_sel:DWORD dst_unused:UNUSED_PAD src0_sel:DWORD src1_sel:WORD_1
v_mfma_f32_16x16x32_bf16 v[220:223],a[228:231],v[40:43],v[220:223]
ds_read_u16_d16_hi v58,v58
ds_read_b64_tr_b16 a[248:249],v3 offset:72
v_mfma_f32_16x16x32_bf16 v[188:191],a[236:239],v[40:43],v[188:191]
ds_read_b64_tr_b16 a[250:251],v3 offset:1608
v_lshlrev_b32_sdwa v32,1,v32 dst_sel:DWORD dst_unused:UNUSED_PAD src0_sel:DWORD src1_sel:WORD_1
v_mfma_f32_16x16x32_bf16 v[184:187],a[236:239],v[36:39],v[184:187]
ds_read_u16 v32,v32
v_rcp_f16_sdwa v9,v8 dst_sel:WORD_1 dst_unused:UNUSED_PRESERVE src0_sel:WORD_1
v_mfma_f32_16x16x32_bf16 v[224:227],a[244:247],v[36:39],v[224:227]
ds_read_b64_tr_b16 a[228:229],v3 offset:128
v_lshlrev_b32_sdwa v34,1,v34 dst_sel:DWORD dst_unused:UNUSED_PAD src0_sel:DWORD src1_sel:WORD_1
v_mfma_f32_16x16x32_bf16 v[228:231],a[244:247],v[40:43],v[228:231]
ds_read_u16_d16_hi v34,v34
v_fma_mix_f32 v57,v57,v9,0 op_sel_hi:[0,1,0]
v_mfma_f32_16x16x32_bf16 v[204:207],a[252:255],v[40:43],v[204:207]
ds_read_b64_tr_b16 a[230:231],v3 offset:1664
v_lshlrev_b32_sdwa v33,1,v33 dst_sel:DWORD dst_unused:UNUSED_PAD src0_sel:DWORD src1_sel:WORD_1
v_mfma_f32_16x16x32_bf16 v[200:203],a[252:255],v[36:39],v[200:203]
ds_read_u16 v33,v33
v_fma_mix_f32 v59,v59,v9,0 op_sel:[0,1,0] op_sel_hi:[0,1,0]
v_lshlrev_b32_sdwa v35,1,v35 dst_sel:DWORD dst_unused:UNUSED_PAD src0_sel:DWORD src1_sel:WORD_1
v_mbcnt_lo_u32_b32 v8,-1,0
N_qs20_%=:
s_add_u32 s64,s16,N_qs20_%=-N_pf_%=+0
v_and_b32_e64 v8,3,v8
s_mov_b32 m0,12288
v_lshl_add_u32 v8,v8,7,s64
v_min_u32 v8,N_mainend_%=-N_pf_%=+2048,v8
global_load_lds_dword v8,s[14:15]
v_mfma_f32_16x16x32_bf16 v[40:43],a[220:223],a[48:51],v[16:19]
ds_read_u16_d16_hi v35,v35
v_lshlrev_b32_sdwa v60,1,v60 dst_sel:DWORD dst_unused:UNUSED_PAD src0_sel:DWORD src1_sel:WORD_1
v_mfma_f32_16x16x32_bf16 v[40:43],a[216:219],a[64:67],v[40:43]
s_waitcnt lgkmcnt(9)
ds_read_b64_tr_b16 a[236:237],v3 offset:136
v_lshl_add_u64 v[24:25],v[24:25],0,v[26:27]
v_mfma_f32_16x16x32_bf16 v[40:43],a[192:195],a[80:83],v[40:43]
ds_read_b64_tr_b16 a[238:239],v3 offset:1672
v_lshl_add_u64 v[26:27],v[28:29],0,v[30:31]
v_lshlrev_b32_sdwa v62,1,v62 dst_sel:DWORD dst_unused:UNUSED_PAD src0_sel:DWORD src1_sel:WORD_1
v_mfma_f32_16x16x32_bf16 v[36:39],a[196:199],a[80:83],v[20:23]
ds_read_u16 v60,v60
v_lshlrev_b32_sdwa v61,1,v61 dst_sel:DWORD dst_unused:UNUSED_PAD src0_sel:DWORD src1_sel:WORD_1
v_mfma_f32_16x16x32_bf16 v[36:39],a[200:203],a[64:67],v[36:39]
ds_read_u16_d16_hi v62,v62
v_lshl_add_u64 v[28:29],v[52:53],0,v[54:55]
v_mfma_f32_16x16x32_bf16 v[36:39],a[212:215],a[48:51],v[36:39]
ds_read_u16 v61,v61
v_lshlrev_b32_sdwa v63,1,v63 dst_sel:DWORD dst_unused:UNUSED_PAD src0_sel:DWORD src1_sel:WORD_1
v_mfma_f32_16x16x32_bf16 v[52:55],a[212:215],a[52:55],v[20:23]
s_waitcnt lgkmcnt(8)
ds_read_u16_d16_hi v63,v63
v_or_b32_e64 v30,v56,v58
v_mfma_f32_16x16x32_bf16 v[52:55],a[200:203],a[68:71],v[52:55]
v_cvt_pk_bf16_f32 v31,v57,v59
v_lshlrev_b32_sdwa v44,1,v44 dst_sel:DWORD dst_unused:UNUSED_PAD src0_sel:DWORD src1_sel:WORD_1
v_mfma_f32_16x16x32_bf16 v[52:55],a[196:199],a[84:87],v[52:55]
ds_read_u16 v44,v44
v_lshlrev_b32_sdwa v46,1,v46 dst_sel:DWORD dst_unused:UNUSED_PAD src0_sel:DWORD src1_sel:WORD_1
v_mfma_f32_16x16x32_bf16 v[56:59],a[192:195],a[84:87],v[16:19]
ds_read_u16_d16_hi v46,v46
v_lshlrev_b32_sdwa v45,1,v45 dst_sel:DWORD dst_unused:UNUSED_PAD src0_sel:DWORD src1_sel:WORD_1
v_mfma_f32_16x16x32_bf16 v[56:59],a[216:219],a[68:71],v[56:59]
ds_read_u16 v45,v45
v_lshlrev_b32_sdwa v47,1,v47 dst_sel:DWORD dst_unused:UNUSED_PAD src0_sel:DWORD src1_sel:WORD_1
v_mfma_f32_16x16x32_bf16 v[56:59],a[220:223],a[52:55],v[56:59]
ds_read_u16_d16_hi v47,v47
v_lshlrev_b32_sdwa v48,1,v48 dst_sel:DWORD dst_unused:UNUSED_PAD src0_sel:DWORD src1_sel:WORD_1
v_mfma_f32_16x16x32_bf16 v[136:139],a[232:235],v[24:27],v[136:139]
ds_read_u16 v48,v48
v_lshlrev_b32_sdwa v50,1,v50 dst_sel:DWORD dst_unused:UNUSED_PAD src0_sel:DWORD src1_sel:WORD_1
v_mfma_f32_16x16x32_bf16 v[140:143],a[232:235],v[28:31],v[140:143]
ds_read_u16_d16_hi v50,v50
v_mfma_f32_16x16x32_bf16 v[68:71],a[240:243],v[28:31],v[68:71]
v_lshlrev_b32_sdwa v49,1,v49 dst_sel:DWORD dst_unused:UNUSED_PAD src0_sel:DWORD src1_sel:WORD_1
v_mfma_f32_16x16x32_bf16 v[64:67],a[240:243],v[24:27],v[64:67]
ds_read_u16 v49,v49
v_mfma_f32_16x16x32_bf16 v[144:147],a[224:227],v[24:27],v[144:147]
v_lshlrev_b32_sdwa v51,1,v51 dst_sel:DWORD dst_unused:UNUSED_PAD src0_sel:DWORD src1_sel:WORD_1
v_mfma_f32_16x16x32_bf16 v[148:151],a[224:227],v[28:31],v[148:151]
ds_read_u16_d16_hi v51,v51
v_lshlrev_b32_sdwa v40,1,v40 dst_sel:DWORD dst_unused:UNUSED_PAD src0_sel:DWORD src1_sel:WORD_1
v_mfma_f32_16x16x32_bf16 v[84:87],a[248:251],v[28:31],v[84:87]
ds_read_u16 v40,v40
v_mfma_f32_16x16x32_bf16 v[80:83],a[248:251],v[24:27],v[80:83]
s_waitcnt lgkmcnt(6)
v_lshlrev_b32_sdwa v42,1,v42 dst_sel:DWORD dst_unused:UNUSED_PAD src0_sel:DWORD src1_sel:WORD_1
v_mfma_f32_16x16x32_bf16 v[152:155],a[228:231],v[24:27],v[152:155]
ds_read_u16_d16_hi v42,v42
v_lshlrev_b32_sdwa v41,1,v41 dst_sel:DWORD dst_unused:UNUSED_PAD src0_sel:DWORD src1_sel:WORD_1
v_mfma_f32_16x16x32_bf16 v[156:159],a[228:231],v[28:31],v[156:159]
ds_read_u16 v41,v41
v_mfma_f32_16x16x32_bf16 v[100:103],a[236:239],v[28:31],v[100:103]
v_lshlrev_b32_sdwa v43,1,v43 dst_sel:DWORD dst_unused:UNUSED_PAD src0_sel:DWORD src1_sel:WORD_1
v_mfma_f32_16x16x32_bf16 v[96:99],a[236:239],v[24:27],v[96:99]
ds_read_u16_d16_hi v43,v43
v_mfma_f32_16x16x32_bf16 v[28:31],a[220:223],a[56:59],v[16:19]
v_lshlrev_b32_sdwa v36,1,v36 dst_sel:DWORD dst_unused:UNUSED_PAD src0_sel:DWORD src1_sel:WORD_1
v_fma_mixlo_f16 v8,v53,s99,0
v_mfma_f32_16x16x32_bf16 v[28:31],a[216:219],a[72:75],v[28:31]
ds_read_u16 v36,v36
v_lshl_add_u64 v[32:33],v[32:33],0,v[34:35]
v_mfma_f32_16x16x32_bf16 v[28:31],a[192:195],a[88:91],v[28:31]
v_lshl_add_u64 v[34:35],v[60:61],0,v[62:63]
v_lshlrev_b32_sdwa v38,1,v38 dst_sel:DWORD dst_unused:UNUSED_PAD src0_sel:DWORD src1_sel:WORD_1
v_mfma_f32_16x16x32_bf16 v[24:27],a[196:199],a[88:91],v[20:23]
ds_read_u16_d16_hi v38,v38
v_fma_mixhi_f16 v8,v55,s99,0
v_mfma_f32_16x16x32_bf16 v[24:27],a[200:203],a[72:75],v[24:27]
s_waitcnt lgkmcnt(5)
v_lshl_add_u64 v[60:61],v[44:45],0,v[46:47]
v_lshlrev_b32_sdwa v37,1,v37 dst_sel:DWORD dst_unused:UNUSED_PAD src0_sel:DWORD src1_sel:WORD_1
v_pk_fma_f16 v9,v8,v8,s54
v_mfma_f32_16x16x32_bf16 v[24:27],a[212:215],a[56:59],v[24:27]
ds_read_u16 v37,v37
v_pk_mul_f16 v9,v8,v9
v_mfma_f32_16x16x32_bf16 v[44:47],a[212:215],a[60:63],v[20:23]
ds_read_b128 v[20:23],v4 offset:1152
v_lshlrev_b32_sdwa v39,1,v39 dst_sel:DWORD dst_unused:UNUSED_PAD src0_sel:DWORD src1_sel:WORD_1
v_mfma_f32_16x16x32_bf16 v[44:47],a[200:203],a[76:79],v[44:47]
ds_read_b64_tr_b16 a[212:213],v1 offset:4096
ds_read_b64_tr_b16 a[214:215],v1 offset:5120
v_lshl_add_u64 v[62:63],v[48:49],0,v[50:51]
v_mfma_f32_16x16x32_bf16 v[44:47],a[196:199],a[92:95],v[44:47]
ds_read_u16_d16_hi v39,v39
v_exp_f16 v8,v9
v_lshlrev_b32_sdwa v56,1,v56 dst_sel:DWORD dst_unused:UNUSED_PAD src0_sel:DWORD src1_sel:WORD_1
v_mfma_f32_16x16x32_bf16 v[48:51],a[192:195],a[92:95],v[16:19]
ds_read_b128 v[16:19],v4 offset:1184
ds_read_b64_tr_b16 a[200:201],v2 offset:4096
v_mfma_f32_16x16x32_bf16 v[48:51],a[216:219],a[76:79],v[48:51]
ds_read_b64_tr_b16 a[202:203],v2 offset:5120
ds_read_b64_tr_b16 a[196:197],v2 offset:2048
v_lshlrev_b32_sdwa v58,1,v58 dst_sel:DWORD dst_unused:UNUSED_PAD src0_sel:DWORD src1_sel:WORD_1
v_mfma_f32_16x16x32_bf16 v[48:51],a[220:223],a[60:63],v[48:51]
ds_read_b64_tr_b16 a[198:199],v2 offset:3072
v_exp_f16_sdwa v8,v9 dst_sel:WORD_1 dst_unused:UNUSED_PRESERVE src0_sel:WORD_1
v_mfma_f32_16x16x32_bf16 v[112:115],a[232:235],v[32:35],v[112:115]
ds_read_u16 v56,v56
v_lshlrev_b32_sdwa v57,1,v57 dst_sel:DWORD dst_unused:UNUSED_PAD src0_sel:DWORD src1_sel:WORD_1
v_mfma_f32_16x16x32_bf16 v[116:119],a[232:235],v[60:63],v[116:119]
ds_read_u16_d16_hi v58,v58
v_lshlrev_b32_sdwa v59,1,v59 dst_sel:DWORD dst_unused:UNUSED_PAD src0_sel:DWORD src1_sel:WORD_1
v_mfma_f32_16x16x32_bf16 v[76:79],a[240:243],v[60:63],v[76:79]
ds_read_u16 v57,v57
v_pk_fma_f16 v8,v8,s55,s55
v_mfma_f32_16x16x32_bf16 v[72:75],a[240:243],v[32:35],v[72:75]
v_rcp_f16 v9,v8
v_lshlrev_b32_sdwa v52,1,v52 dst_sel:DWORD dst_unused:UNUSED_PAD src0_sel:DWORD src1_sel:WORD_1
v_mfma_f32_16x16x32_bf16 v[120:123],a[224:227],v[32:35],v[120:123]
ds_read_u16_d16_hi v59,v59
v_lshlrev_b32_sdwa v54,1,v54 dst_sel:DWORD dst_unused:UNUSED_PAD src0_sel:DWORD src1_sel:WORD_1
v_mfma_f32_16x16x32_bf16 v[124:127],a[224:227],v[60:63],v[124:127]
ds_read_b64_tr_b16 a[192:193],v2 offset:0
v_lshlrev_b32_sdwa v28,1,v28 dst_sel:DWORD dst_unused:UNUSED_PAD src0_sel:DWORD src1_sel:WORD_1
v_mfma_f32_16x16x32_bf16 v[92:95],a[248:251],v[60:63],v[92:95]
ds_read_b64_tr_b16 a[194:195],v2 offset:1024
v_lshlrev_b32_sdwa v30,1,v30 dst_sel:DWORD dst_unused:UNUSED_PAD src0_sel:DWORD src1_sel:WORD_1
v_mfma_f32_16x16x32_bf16 v[88:91],a[248:251],v[32:35],v[88:91]
ds_read_u16 v52,v52
v_rcp_f16_sdwa v9,v8 dst_sel:WORD_1 dst_unused:UNUSED_PRESERVE src0_sel:WORD_1
v_mfma_f32_16x16x32_bf16 v[128:131],a[228:231],v[32:35],v[128:131]
ds_read_u16_d16_hi v54,v54
v_fma_mix_f32 v53,v53,v9,0 op_sel_hi:[0,1,0]
v_mfma_f32_16x16x32_bf16 v[132:135],a[228:231],v[60:63],v[132:135]
ds_read_u16 v28,v28
v_lshlrev_b32_sdwa v29,1,v29 dst_sel:DWORD dst_unused:UNUSED_PAD src0_sel:DWORD src1_sel:WORD_1
v_mfma_f32_16x16x32_bf16 v[108:111],a[236:239],v[60:63],v[108:111]
ds_read_u16_d16_hi v30,v30
v_fma_mix_f32 v55,v55,v9,0 op_sel:[0,1,0] op_sel_hi:[0,1,0]
v_mfma_f32_16x16x32_bf16 v[104:107],a[236:239],v[32:35],v[104:107]
s_waitcnt lgkmcnt(7)
ds_read_u16 v29,v29
v_lshlrev_b32_sdwa v31,1,v31 dst_sel:DWORD dst_unused:UNUSED_PAD src0_sel:DWORD src1_sel:WORD_1
v_mfma_f32_16x16x32_bf16 v[60:63],a[204:207],a[0:3],v[20:23]
ds_read_u16_d16_hi v31,v31
v_lshlrev_b32_sdwa v24,1,v24 dst_sel:DWORD dst_unused:UNUSED_PAD src0_sel:DWORD src1_sel:WORD_1
v_mfma_f32_16x16x32_bf16 v[60:63],a[208:211],a[16:19],v[60:63]
ds_read_u16 v24,v24
v_lshl_add_u64 v[40:41],v[40:41],0,v[42:43]
v_mfma_f32_16x16x32_bf16 v[60:63],a[212:215],a[32:35],v[60:63]
v_lshl_add_u64 v[42:43],v[36:37],0,v[38:39]
v_lshlrev_b32_sdwa v26,1,v26 dst_sel:DWORD dst_unused:UNUSED_PAD src0_sel:DWORD src1_sel:WORD_1
v_mfma_f32_16x16x32_bf16 v[32:35],a[200:203],a[32:35],v[16:19]
ds_read_u16_d16_hi v26,v26
v_lshlrev_b32_sdwa v25,1,v25 dst_sel:DWORD dst_unused:UNUSED_PAD src0_sel:DWORD src1_sel:WORD_1
v_mfma_f32_16x16x32_bf16 v[32:35],a[196:199],a[16:19],v[32:35]
s_waitcnt lgkmcnt(7)
ds_read_u16 v25,v25
v_lshl_add_u64 v[36:37],v[56:57],0,v[58:59]
v_mfma_f32_16x16x32_bf16 v[32:35],a[192:195],a[0:3],v[32:35]
v_lshlrev_b32_sdwa v27,1,v27 dst_sel:DWORD dst_unused:UNUSED_PAD src0_sel:DWORD src1_sel:WORD_1
v_lshlrev_b32_sdwa v48,1,v48 dst_sel:DWORD dst_unused:UNUSED_PAD src0_sel:DWORD src1_sel:WORD_1
v_mfma_f32_16x16x32_bf16 v[56:59],a[192:195],a[4:7],v[16:19]
s_waitcnt lgkmcnt(6)
ds_read_u16_d16_hi v27,v27
v_or_b32 v38,v52,v54
v_mfma_f32_16x16x32_bf16 v[56:59],a[196:199],a[20:23],v[56:59]
s_waitcnt vmcnt(4)
ds_read_u16 v48,v48
v_cvt_pk_bf16_f32 v39,v53,v55
v_mfma_f32_16x16x32_bf16 v[56:59],a[200:203],a[36:39],v[56:59]
s_barrier
v_lshlrev_b32_sdwa v50,1,v50 dst_sel:DWORD dst_unused:UNUSED_PAD src0_sel:DWORD src1_sel:WORD_1
v_lshlrev_b32_sdwa v49,1,v49 dst_sel:DWORD dst_unused:UNUSED_PAD src0_sel:DWORD src1_sel:WORD_1
v_mfma_f32_16x16x32_bf16 v[52:55],a[212:215],a[36:39],v[20:23]
ds_read_u16_d16_hi v50,v50
v_lshlrev_b32_sdwa v51,1,v51 dst_sel:DWORD dst_unused:UNUSED_PAD src0_sel:DWORD src1_sel:WORD_1
v_mfma_f32_16x16x32_bf16 v[52:55],a[208:211],a[20:23],v[52:55]
s_add_u32 m0,s58,18480
ds_read_u16 v49,v49
v_mfma_f32_16x16x32_bf16 v[52:55],a[204:207],a[4:7],v[52:55]
s_mov_b32 s69,s88
ds_read_u16_d16_hi v51,v51
v_lshlrev_b32_sdwa v44,1,v44 dst_sel:DWORD dst_unused:UNUSED_PAD src0_sel:DWORD src1_sel:WORD_1
v_mfma_f32_16x16x32_bf16 v[232:235],a[232:235],v[40:43],v[232:235]
buffer_load_dwordx4 v5,s[44:47],s69 offen offset:0 lds
v_lshlrev_b32_sdwa v46,1,v46 dst_sel:DWORD dst_unused:UNUSED_PAD src0_sel:DWORD src1_sel:WORD_1
v_mfma_f32_16x16x32_bf16 v[236:239],a[232:235],v[36:39],v[236:239]
ds_read_u16 v44,v44
v_mfma_f32_16x16x32_bf16 v[164:167],a[240:243],v[36:39],v[164:167]
ds_read_u16_d16_hi v46,v46
ds_read_b64_tr_b16 a[244:245],v3 offset:6160
v_mfma_f32_16x16x32_bf16 v[160:163],a[240:243],v[40:43],v[160:163]
ds_read_b64_tr_b16 a[246:247],v3 offset:7696
v_lshlrev_b32_sdwa v45,1,v45 dst_sel:DWORD dst_unused:UNUSED_PAD src0_sel:DWORD src1_sel:WORD_1
v_mfma_f32_16x16x32_bf16 v[240:243],a[224:227],v[40:43],v[240:243]
s_add_u32 s69,s69,s53
ds_read_u16 v45,v45
v_lshlrev_b32_sdwa v47,1,v47 dst_sel:DWORD dst_unused:UNUSED_PAD src0_sel:DWORD src1_sel:WORD_1
v_mfma_f32_16x16x32_bf16 v[244:247],a[224:227],v[36:39],v[244:247]
buffer_load_dwordx4 v5,s[44:47],s69 offen offset:1024 lds
v_lshlrev_b32_sdwa v60,1,v60 dst_sel:DWORD dst_unused:UNUSED_PAD src0_sel:DWORD src1_sel:WORD_1
v_mfma_f32_16x16x32_bf16 v[180:183],a[248:251],v[36:39],v[180:183]
ds_read_u16_d16_hi v47,v47
v_mfma_f32_16x16x32_bf16 v[176:179],a[248:251],v[40:43],v[176:179]
ds_read_u16 v60,v60
v_lshlrev_b32_sdwa v62,1,v62 dst_sel:DWORD dst_unused:UNUSED_PAD src0_sel:DWORD src1_sel:WORD_1
v_mfma_f32_16x16x32_bf16 v[248:251],a[228:231],v[40:43],v[248:251]
ds_read_u16_d16_hi v62,v62
v_lshlrev_b32_sdwa v61,1,v61 dst_sel:DWORD dst_unused:UNUSED_PAD src0_sel:DWORD src1_sel:WORD_1
v_mfma_f32_16x16x32_bf16 v[252:255],a[228:231],v[36:39],v[252:255]
ds_read_u16 v61,v61
v_mfma_f32_16x16x32_bf16 v[196:199],a[236:239],v[36:39],v[196:199]
s_add_u32 s69,s69,s53
ds_read_b64_tr_b16 a[252:253],v3 offset:6168
v_lshlrev_b32_sdwa v63,1,v63 dst_sel:DWORD dst_unused:UNUSED_PAD src0_sel:DWORD src1_sel:WORD_1
v_mfma_f32_16x16x32_bf16 v[192:195],a[236:239],v[40:43],v[192:195]
buffer_load_dwordx4 v5,s[44:47],s69 offen offset:2048 lds
v_add_u32 v11,88272,v1
v_add_u32 v12,88272,v2
v_mfma_f32_16x16x32_bf16 v[36:39],a[204:207],a[8:11],v[20:23]
ds_read_u16_d16_hi v63,v63
ds_read_b64_tr_b16 a[96:97],v11 offset:0
v_lshlrev_b32_sdwa v32,1,v32 dst_sel:DWORD dst_unused:UNUSED_PAD src0_sel:DWORD src1_sel:WORD_1
v_mfma_f32_16x16x32_bf16 v[36:39],a[208:211],a[24:27],v[36:39]
s_waitcnt lgkmcnt(7)
ds_read_b64_tr_b16 a[98:99],v11 offset:1024
ds_read_b64_tr_b16 a[100:101],v11 offset:2048
v_lshl_add_u64 v[28:29],v[28:29],0,v[30:31]
v_fma_mixlo_f16 v8,v57,s99,0
v_mfma_f32_16x16x32_bf16 v[36:39],a[212:215],a[40:43],v[36:39]
ds_read_b64_tr_b16 a[102:103],v11 offset:3072
ds_read_u16 v32,v32
v_lshl_add_u64 v[30:31],v[24:25],0,v[26:27]
v_lshlrev_b32_sdwa v34,1,v34 dst_sel:DWORD dst_unused:UNUSED_PAD src0_sel:DWORD src1_sel:WORD_1
v_mfma_f32_16x16x32_bf16 v[40:43],a[200:203],a[40:43],v[16:19]
ds_read_u16_d16_hi v34,v34
ds_read_b64_tr_b16 a[104:105],v11 offset:4096
v_fma_mixhi_f16 v8,v59,s99,0
v_mfma_f32_16x16x32_bf16 v[40:43],a[196:199],a[24:27],v[40:43]
ds_read_b64_tr_b16 a[106:107],v11 offset:5120
ds_read_b64_tr_b16 a[254:255],v3 offset:7704
v_lshl_add_u64 v[24:25],v[48:49],0,v[50:51]
v_lshlrev_b32_sdwa v33,1,v33 dst_sel:DWORD dst_unused:UNUSED_PAD src0_sel:DWORD src1_sel:WORD_1
v_pk_fma_f16 v9,v8,v8,s54
v_mfma_f32_16x16x32_bf16 v[40:43],a[192:195],a[8:11],v[40:43]
ds_read_u16 v33,v33
ds_read_b64_tr_b16 a[108:109],v12 offset:4096
v_mfma_f32_16x16x32_bf16 v[48:51],a[192:195],a[12:15],v[16:19]
ds_read_b64_tr_b16 a[110:111],v12 offset:5120
v_pk_mul_f16 v9,v8,v9
v_lshlrev_b32_sdwa v35,1,v35 dst_sel:DWORD dst_unused:UNUSED_PAD src0_sel:DWORD src1_sel:WORD_1
v_mfma_f32_16x16x32_bf16 v[48:51],a[196:199],a[28:31],v[48:51]
s_waitcnt lgkmcnt(13)
ds_read_u16_d16_hi v35,v35
ds_read_b64_tr_b16 a[112:113],v12 offset:2048
v_lshl_add_u64 v[26:27],v[44:45],0,v[46:47]
v_mfma_f32_16x16x32_bf16 v[48:51],a[200:203],a[44:47],v[48:51]
TE13_%=:
s_cbranch_execz TR1_%=
v_exp_f16 v8,v9
v_lshlrev_b32_sdwa v52,1,v52 dst_sel:DWORD dst_unused:UNUSED_PAD src0_sel:DWORD src1_sel:WORD_1
v_mfma_f32_16x16x32_bf16 v[44:47],a[212:215],a[44:47],v[20:23]
ds_read_u16 v52,v52
v_lshlrev_b32_sdwa v54,1,v54 dst_sel:DWORD dst_unused:UNUSED_PAD src0_sel:DWORD src1_sel:WORD_1
v_mfma_f32_16x16x32_bf16 v[44:47],a[208:211],a[28:31],v[44:47]
ds_read_u16_d16_hi v54,v54
v_lshlrev_b32_sdwa v53,1,v53 dst_sel:DWORD dst_unused:UNUSED_PAD src0_sel:DWORD src1_sel:WORD_1
v_mfma_f32_16x16x32_bf16 v[44:47],a[204:207],a[12:15],v[44:47]
ds_read_u16 v53,v53
v_exp_f16_sdwa v8,v9 dst_sel:WORD_1 dst_unused:UNUSED_PRESERVE src0_sel:WORD_1
v_mfma_f32_16x16x32_bf16 v[208:211],a[232:235],v[28:31],v[208:211]
ds_read_b64_tr_b16 a[114:115],v12 offset:3072
v_lshlrev_b32_sdwa v55,1,v55 dst_sel:DWORD dst_unused:UNUSED_PAD src0_sel:DWORD src1_sel:WORD_1
v_mfma_f32_16x16x32_bf16 v[212:215],a[232:235],v[24:27],v[212:215]
ds_read_u16_d16_hi v55,v55
ds_read_b64_tr_b16 a[232:233],v3 offset:6224
v_mfma_f32_16x16x32_bf16 v[172:175],a[240:243],v[24:27],v[172:175]
ds_read_b64_tr_b16 a[234:235],v3 offset:7760
v_pk_fma_f16 v8,v8,s55,s55
v_mfma_f32_16x16x32_bf16 v[168:171],a[240:243],v[28:31],v[168:171]
ds_read_b64_tr_b16 a[240:241],v3 offset:6232
v_rcp_f16 v9,v8
v_lshlrev_b32_sdwa v56,1,v56 dst_sel:DWORD dst_unused:UNUSED_PAD src0_sel:DWORD src1_sel:WORD_1
v_mfma_f32_16x16x32_bf16 v[216:219],a[224:227],v[28:31],v[216:219]
ds_read_u16 v56,v56
ds_read_b64_tr_b16 a[242:243],v3 offset:7768
v_mfma_f32_16x16x32_bf16 v[220:223],a[224:227],v[24:27],v[220:223]
ds_read_b64_tr_b16 a[224:225],v3 offset:6288
ds_read_b64_tr_b16 a[226:227],v3 offset:7824
v_lshlrev_b32_sdwa v58,1,v58 dst_sel:DWORD dst_unused:UNUSED_PAD src0_sel:DWORD src1_sel:WORD_1
v_mfma_f32_16x16x32_bf16 v[188:191],a[248:251],v[24:27],v[188:191]
ds_read_u16_d16_hi v58,v58
ds_read_b64_tr_b16 a[116:117],v12 offset:0
v_mfma_f32_16x16x32_bf16 v[184:187],a[248:251],v[28:31],v[184:187]
ds_read_b64_tr_b16 a[248:249],v3 offset:6296
v_rcp_f16_sdwa v9,v8 dst_sel:WORD_1 dst_unused:UNUSED_PRESERVE src0_sel:WORD_1
v_lshlrev_b32_sdwa v36,1,v36 dst_sel:DWORD dst_unused:UNUSED_PAD src0_sel:DWORD src1_sel:WORD_1
v_mfma_f32_16x16x32_bf16 v[224:227],a[228:231],v[28:31],v[224:227]
ds_read_u16 v36,v36
ds_read_b64_tr_b16 a[250:251],v3 offset:7832
v_mfma_f32_16x16x32_bf16 v[228:231],a[228:231],v[24:27],v[228:231]
ds_read_b64_tr_b16 a[118:119],v12 offset:1024
v_lshlrev_b32_sdwa v38,1,v38 dst_sel:DWORD dst_unused:UNUSED_PAD src0_sel:DWORD src1_sel:WORD_1
v_mfma_f32_16x16x32_bf16 v[204:207],a[236:239],v[24:27],v[204:207]
ds_read_u16_d16_hi v38,v38
v_fma_mix_f32 v57,v57,v9,0 op_sel_hi:[0,1,0]
v_mfma_f32_16x16x32_bf16 v[200:203],a[236:239],v[28:31],v[200:203]
ds_read_b64_tr_b16 a[120:121],v12 offset:6160
ds_read_b64_tr_b16 a[122:123],v12 offset:7184
v_lshlrev_b32_sdwa v37,1,v37 dst_sel:DWORD dst_unused:UNUSED_PAD src0_sel:DWORD src1_sel:WORD_1
v_fma_mix_f32 v59,v59,v9,0 op_sel:[0,1,0] op_sel_hi:[0,1,0]
v_lshlrev_b32_sdwa v39,1,v39 dst_sel:DWORD dst_unused:UNUSED_PAD src0_sel:DWORD src1_sel:WORD_1
v_mfma_f32_16x16x32_bf16 v[24:27],a[204:207],a[48:51],v[20:23]
s_nop 0
ds_read_u16 v37,v37
ds_read_u16_d16_hi v39,v39
v_lshlrev_b32_sdwa v40,1,v40 dst_sel:DWORD dst_unused:UNUSED_PAD src0_sel:DWORD src1_sel:WORD_1
v_mfma_f32_16x16x32_bf16 v[24:27],a[208:211],a[64:67],v[24:27]
s_waitcnt lgkmcnt(13)
ds_read_u16 v40,v40
ds_read_b64_tr_b16 a[216:217],v1 offset:6160
v_lshl_add_u64 v[60:61],v[60:61],0,v[62:63]
v_mfma_f32_16x16x32_bf16 v[24:27],a[212:215],a[80:83],v[24:27]
ds_read_b64_tr_b16 a[218:219],v1 offset:7184
ds_read_b64_tr_b16 a[124:125],v12 offset:8208
v_lshl_add_u64 v[62:63],v[32:33],0,v[34:35]
v_lshlrev_b32_sdwa v42,1,v42 dst_sel:DWORD dst_unused:UNUSED_PAD src0_sel:DWORD src1_sel:WORD_1
v_mfma_f32_16x16x32_bf16 v[28:31],a[200:203],a[80:83],v[16:19]
ds_read_u16_d16_hi v42,v42
v_lshlrev_b32_sdwa v41,1,v41 dst_sel:DWORD dst_unused:UNUSED_PAD src0_sel:DWORD src1_sel:WORD_1
v_mfma_f32_16x16x32_bf16 v[28:31],a[196:199],a[64:67],v[28:31]
ds_read_u16 v41,v41
v_lshl_add_u64 v[32:33],v[52:53],0,v[54:55]
v_mfma_f32_16x16x32_bf16 v[28:31],a[192:195],a[48:51],v[28:31]
ds_read_b64_tr_b16 a[126:127],v12 offset:9232
v_lshlrev_b32_sdwa v43,1,v43 dst_sel:DWORD dst_unused:UNUSED_PAD src0_sel:DWORD src1_sel:WORD_1
v_mfma_f32_16x16x32_bf16 v[52:55],a[192:195],a[52:55],v[16:19]
s_waitcnt lgkmcnt(12)
ds_read_u16_d16_hi v43,v43
v_or_b32_e64 v34,v56,v58
v_mfma_f32_16x16x32_bf16 v[52:55],a[196:199],a[68:71],v[52:55]
ds_read_b64_tr_b16 a[220:221],v1 offset:8208
v_cvt_pk_bf16_f32 v35,v57,v59
v_lshlrev_b32_sdwa v44,1,v44 dst_sel:DWORD dst_unused:UNUSED_PAD src0_sel:DWORD src1_sel:WORD_1
v_mfma_f32_16x16x32_bf16 v[52:55],a[200:203],a[84:87],v[52:55]
ds_read_u16 v44,v44
v_lshlrev_b32_sdwa v46,1,v46 dst_sel:DWORD dst_unused:UNUSED_PAD src0_sel:DWORD src1_sel:WORD_1
v_mfma_f32_16x16x32_bf16 v[56:59],a[212:215],a[84:87],v[20:23]
ds_read_u16_d16_hi v46,v46
v_lshlrev_b32_sdwa v45,1,v45 dst_sel:DWORD dst_unused:UNUSED_PAD src0_sel:DWORD src1_sel:WORD_1
v_mfma_f32_16x16x32_bf16 v[56:59],a[208:211],a[68:71],v[56:59]
ds_read_u16 v45,v45
v_lshlrev_b32_sdwa v47,1,v47 dst_sel:DWORD dst_unused:UNUSED_PAD src0_sel:DWORD src1_sel:WORD_1
v_mfma_f32_16x16x32_bf16 v[56:59],a[204:207],a[52:55],v[56:59]
ds_read_u16_d16_hi v47,v47
v_lshlrev_b32_sdwa v48,1,v48 dst_sel:DWORD dst_unused:UNUSED_PAD src0_sel:DWORD src1_sel:WORD_1
v_mfma_f32_16x16x32_bf16 v[136:139],a[244:247],v[60:63],v[136:139]
ds_read_u16 v48,v48
v_lshlrev_b32_sdwa v50,1,v50 dst_sel:DWORD dst_unused:UNUSED_PAD src0_sel:DWORD src1_sel:WORD_1
v_mfma_f32_16x16x32_bf16 v[140:143],a[244:247],v[32:35],v[140:143]
ds_read_u16_d16_hi v50,v50
v_mfma_f32_16x16x32_bf16 v[68:71],a[252:255],v[32:35],v[68:71]
ds_read_b64_tr_b16 a[222:223],v1 offset:9232
v_lshlrev_b32_sdwa v49,1,v49 dst_sel:DWORD dst_unused:UNUSED_PAD src0_sel:DWORD src1_sel:WORD_1
v_mfma_f32_16x16x32_bf16 v[64:67],a[252:255],v[60:63],v[64:67]
ds_read_u16 v49,v49
ds_read_b64_tr_b16 a[128:129],v12 offset:10256
v_mfma_f32_16x16x32_bf16 v[144:147],a[232:235],v[60:63],v[144:147]
ds_read_b64_tr_b16 a[130:131],v12 offset:11280
v_lshlrev_b32_sdwa v51,1,v51 dst_sel:DWORD dst_unused:UNUSED_PAD src0_sel:DWORD src1_sel:WORD_1
v_mfma_f32_16x16x32_bf16 v[148:151],a[232:235],v[32:35],v[148:151]
ds_read_u16_d16_hi v51,v51
v_lshlrev_b32_sdwa v24,1,v24 dst_sel:DWORD dst_unused:UNUSED_PAD src0_sel:DWORD src1_sel:WORD_1
v_mfma_f32_16x16x32_bf16 v[84:87],a[240:243],v[32:35],v[84:87]
ds_read_u16 v24,v24
v_mfma_f32_16x16x32_bf16 v[80:83],a[240:243],v[60:63],v[80:83]
ds_read_b64_tr_b16 a[132:133],v11 offset:10256
v_lshlrev_b32_sdwa v26,1,v26 dst_sel:DWORD dst_unused:UNUSED_PAD src0_sel:DWORD src1_sel:WORD_1
v_mfma_f32_16x16x32_bf16 v[152:155],a[224:227],v[60:63],v[152:155]
ds_read_u16_d16_hi v26,v26
v_lshlrev_b32_sdwa v25,1,v25 dst_sel:DWORD dst_unused:UNUSED_PAD src0_sel:DWORD src1_sel:WORD_1
v_mfma_f32_16x16x32_bf16 v[156:159],a[224:227],v[32:35],v[156:159]
ds_read_u16 v25,v25
v_mfma_f32_16x16x32_bf16 v[100:103],a[248:251],v[32:35],v[100:103]
ds_read_b64_tr_b16 a[134:135],v11 offset:11280
v_lshlrev_b32_sdwa v27,1,v27 dst_sel:DWORD dst_unused:UNUSED_PAD src0_sel:DWORD src1_sel:WORD_1
v_mfma_f32_16x16x32_bf16 v[96:99],a[248:251],v[60:63],v[96:99]
ds_read_u16_d16_hi v27,v27
v_mfma_f32_16x16x32_bf16 v[32:35],a[204:207],a[56:59],v[20:23]
ds_read_b64_tr_b16 a[136:137],v11 offset:8208
v_lshlrev_b32_sdwa v28,1,v28 dst_sel:DWORD dst_unused:UNUSED_PAD src0_sel:DWORD src1_sel:WORD_1
v_mfma_f32_16x16x32_bf16 v[32:35],a[208:211],a[72:75],v[32:35]
s_waitcnt lgkmcnt(8)
ds_read_u16 v28,v28
v_lshl_add_u64 v[36:37],v[36:37],0,v[38:39]
v_fma_mixlo_f16 v8,v53,s99,0
v_mfma_f32_16x16x32_bf16 v[32:35],a[212:215],a[88:91],v[32:35]
ds_read_b64_tr_b16 a[138:139],v11 offset:9232
v_lshl_add_u64 v[38:39],v[40:41],0,v[42:43]
v_lshlrev_b32_sdwa v30,1,v30 dst_sel:DWORD dst_unused:UNUSED_PAD src0_sel:DWORD src1_sel:WORD_1
v_mfma_f32_16x16x32_bf16 v[60:63],a[200:203],a[88:91],v[16:19]
ds_read_u16_d16_hi v30,v30
v_fma_mixhi_f16 v8,v55,s99,0
v_mfma_f32_16x16x32_bf16 v[60:63],a[196:199],a[72:75],v[60:63]
ds_read_b64_tr_b16 a[140:141],v11 offset:6160
v_lshl_add_u64 v[40:41],v[44:45],0,v[46:47]
v_lshlrev_b32_sdwa v29,1,v29 dst_sel:DWORD dst_unused:UNUSED_PAD src0_sel:DWORD src1_sel:WORD_1
v_pk_fma_f16 v9,v8,v8,s54
v_mfma_f32_16x16x32_bf16 v[60:63],a[192:195],a[56:59],v[60:63]
ds_read_u16 v29,v29
v_pk_mul_f16 v9,v8,v9
v_mfma_f32_16x16x32_bf16 v[44:47],a[192:195],a[60:63],v[16:19]
ds_read_b128 v[16:19],v4 offset:1280
v_lshlrev_b32_sdwa v31,1,v31 dst_sel:DWORD dst_unused:UNUSED_PAD src0_sel:DWORD src1_sel:WORD_1
v_mfma_f32_16x16x32_bf16 v[44:47],a[196:199],a[76:79],v[44:47]
s_waitcnt lgkmcnt(8)
ds_read_b64_tr_b16 a[192:193],v1 offset:10256
ds_read_b64_tr_b16 a[194:195],v1 offset:11280
v_lshl_add_u64 v[42:43],v[48:49],0,v[50:51]
v_mfma_f32_16x16x32_bf16 v[44:47],a[200:203],a[92:95],v[44:47]
ds_read_u16_d16_hi v31,v31
v_exp_f16 v8,v9
v_lshlrev_b32_sdwa v56,1,v56 dst_sel:DWORD dst_unused:UNUSED_PAD src0_sel:DWORD src1_sel:WORD_1
v_mfma_f32_16x16x32_bf16 v[48:51],a[212:215],a[92:95],v[20:23]
ds_read_b128 v[20:23],v4 offset:1312
ds_read_b64_tr_b16 a[196:197],v2 offset:10256
v_mfma_f32_16x16x32_bf16 v[48:51],a[208:211],a[76:79],v[48:51]
ds_read_b64_tr_b16 a[198:199],v2 offset:11280
ds_read_b64_tr_b16 a[200:201],v2 offset:8208
v_lshlrev_b32_sdwa v58,1,v58 dst_sel:DWORD dst_unused:UNUSED_PAD src0_sel:DWORD src1_sel:WORD_1
v_mfma_f32_16x16x32_bf16 v[48:51],a[204:207],a[60:63],v[48:51]
ds_read_b64_tr_b16 a[202:203],v2 offset:9232
v_exp_f16_sdwa v8,v9 dst_sel:WORD_1 dst_unused:UNUSED_PRESERVE src0_sel:WORD_1
v_mfma_f32_16x16x32_bf16 v[112:115],a[244:247],v[36:39],v[112:115]
ds_read_u16 v56,v56
ds_read_u16_d16_hi v58,v58
v_lshlrev_b32_sdwa v57,1,v57 dst_sel:DWORD dst_unused:UNUSED_PAD src0_sel:DWORD src1_sel:WORD_1
v_mfma_f32_16x16x32_bf16 v[116:119],a[244:247],v[40:43],v[116:119]
ds_read_u16 v57,v57
v_lshlrev_b32_sdwa v59,1,v59 dst_sel:DWORD dst_unused:UNUSED_PAD src0_sel:DWORD src1_sel:WORD_1
v_mfma_f32_16x16x32_bf16 v[76:79],a[252:255],v[40:43],v[76:79]
ds_read_u16_d16_hi v59,v59
v_pk_fma_f16 v8,v8,s55,s55
v_mfma_f32_16x16x32_bf16 v[72:75],a[252:255],v[36:39],v[72:75]
ds_read_b64_tr_b16 a[212:213],v2 offset:6160
v_rcp_f16_e64 v9,v8
v_lshlrev_b32_sdwa v52,1,v52 dst_sel:DWORD dst_unused:UNUSED_PAD src0_sel:DWORD src1_sel:WORD_1
v_mfma_f32_16x16x32_bf16 v[120:123],a[232:235],v[36:39],v[120:123]
ds_read_b64_tr_b16 a[214:215],v2 offset:7184
ds_read_u16 v52,v52
v_mfma_f32_16x16x32_bf16 v[124:127],a[232:235],v[40:43],v[124:127]
ds_read_b64_tr_b16 a[142:143],v11 offset:7184
v_lshlrev_b32_sdwa v54,1,v54 dst_sel:DWORD dst_unused:UNUSED_PAD src0_sel:DWORD src1_sel:WORD_1
v_mfma_f32_16x16x32_bf16 v[92:95],a[240:243],v[40:43],v[92:95]
ds_read_u16_d16_hi v54,v54
v_lshlrev_b32_sdwa v32,1,v32 dst_sel:DWORD dst_unused:UNUSED_PAD src0_sel:DWORD src1_sel:WORD_1
v_mfma_f32_16x16x32_bf16 v[88:91],a[240:243],v[36:39],v[88:91]
v_rcp_f16_sdwa v9,v8 dst_sel:WORD_1 dst_unused:UNUSED_PRESERVE src0_sel:WORD_1
v_mfma_f32_16x16x32_bf16 v[128:131],a[224:227],v[36:39],v[128:131]
ds_read_u16 v32,v32
v_lshlrev_b32_sdwa v34,1,v34 dst_sel:DWORD dst_unused:UNUSED_PAD src0_sel:DWORD src1_sel:WORD_1
v_mfma_f32_16x16x32_bf16 v[132:135],a[224:227],v[40:43],v[132:135]
ds_read_u16_d16_hi v34,v34
v_fma_mix_f32 v53,v53,v9,0 op_sel_hi:[0,1,0]
v_mfma_f32_16x16x32_bf16 v[108:111],a[248:251],v[40:43],v[108:111]
ds_read_b64_tr_b16 a[144:145],v11 offset:12320
v_lshlrev_b32_sdwa v33,1,v33 dst_sel:DWORD dst_unused:UNUSED_PAD src0_sel:DWORD src1_sel:WORD_1
v_mfma_f32_16x16x32_bf16 v[104:107],a[248:251],v[36:39],v[104:107]
s_waitcnt lgkmcnt(8)
ds_read_u16 v33,v33
v_fma_mix_f32 v55,v55,v9,0 op_sel:[0,1,0] op_sel_hi:[0,1,0]
v_lshlrev_b32_sdwa v35,1,v35 dst_sel:DWORD dst_unused:UNUSED_PAD src0_sel:DWORD src1_sel:WORD_1
v_mfma_f32_16x16x32_bf16 v[40:43],a[216:219],a[0:3],v[16:19]
ds_read_u16_d16_hi v35,v35
v_lshlrev_b32_sdwa v60,1,v60 dst_sel:DWORD dst_unused:UNUSED_PAD src0_sel:DWORD src1_sel:WORD_1
v_mfma_f32_16x16x32_bf16 v[40:43],a[220:223],a[16:19],v[40:43]
ds_read_u16 v60,v60
v_lshl_add_u64 v[24:25],v[24:25],0,v[26:27]
v_mfma_f32_16x16x32_bf16 v[40:43],a[192:195],a[32:35],v[40:43]
ds_read_b64_tr_b16 a[146:147],v11 offset:13344
v_lshl_add_u64 v[26:27],v[28:29],0,v[30:31]
v_lshlrev_b32_sdwa v62,1,v62 dst_sel:DWORD dst_unused:UNUSED_PAD src0_sel:DWORD src1_sel:WORD_1
v_mfma_f32_16x16x32_bf16 v[36:39],a[196:199],a[32:35],v[20:23]
ds_read_u16_d16_hi v62,v62
v_lshlrev_b32_sdwa v61,1,v61 dst_sel:DWORD dst_unused:UNUSED_PAD src0_sel:DWORD src1_sel:WORD_1
v_mfma_f32_16x16x32_bf16 v[36:39],a[200:203],a[16:19],v[36:39]
s_waitcnt lgkmcnt(8)
ds_read_u16 v61,v61
v_lshl_add_u64 v[28:29],v[56:57],0,v[58:59]
v_mfma_f32_16x16x32_bf16 v[36:39],a[212:215],a[0:3],v[36:39]
ds_read_b64_tr_b16 a[148:149],v11 offset:14368
v_lshlrev_b32_sdwa v63,1,v63 dst_sel:DWORD dst_unused:UNUSED_PAD src0_sel:DWORD src1_sel:WORD_1
v_mfma_f32_16x16x32_bf16 v[56:59],a[212:215],a[4:7],v[20:23]
ds_read_u16_d16_hi v63,v63
v_or_b32 v30,v52,v54
v_mfma_f32_16x16x32_bf16 v[56:59],a[200:203],a[20:23],v[56:59]
ds_read_b64_tr_b16 a[150:151],v11 offset:15392
v_cvt_pk_bf16_f32 v31,v53,v55
v_lshlrev_b32_sdwa v48,1,v48 dst_sel:DWORD dst_unused:UNUSED_PAD src0_sel:DWORD src1_sel:WORD_1
v_mfma_f32_16x16x32_bf16 v[56:59],a[196:199],a[36:39],v[56:59]
ds_read_u16 v48,v48
v_lshlrev_b32_sdwa v50,1,v50 dst_sel:DWORD dst_unused:UNUSED_PAD src0_sel:DWORD src1_sel:WORD_1
v_mfma_f32_16x16x32_bf16 v[52:55],a[192:195],a[36:39],v[16:19]
ds_read_u16_d16_hi v50,v50
v_lshlrev_b32_sdwa v49,1,v49 dst_sel:DWORD dst_unused:UNUSED_PAD src0_sel:DWORD src1_sel:WORD_1
v_mfma_f32_16x16x32_bf16 v[52:55],a[220:223],a[20:23],v[52:55]
ds_read_u16 v49,v49
v_lshlrev_b32_sdwa v51,1,v51 dst_sel:DWORD dst_unused:UNUSED_PAD src0_sel:DWORD src1_sel:WORD_1
v_mfma_f32_16x16x32_bf16 v[52:55],a[216:219],a[4:7],v[52:55]
ds_read_u16_d16_hi v51,v51
v_lshlrev_b32_sdwa v44,1,v44 dst_sel:DWORD dst_unused:UNUSED_PAD src0_sel:DWORD src1_sel:WORD_1
v_mfma_f32_16x16x32_bf16 v[232:235],a[244:247],v[24:27],v[232:235]
ds_read_u16 v44,v44
ds_read_b64_tr_b16 a[152:153],v11 offset:16416
v_mfma_f32_16x16x32_bf16 v[236:239],a[244:247],v[28:31],v[236:239]
ds_read_b64_tr_b16 a[154:155],v11 offset:17440
v_lshlrev_b32_sdwa v46,1,v46 dst_sel:DWORD dst_unused:UNUSED_PAD src0_sel:DWORD src1_sel:WORD_1
v_mfma_f32_16x16x32_bf16 v[164:167],a[252:255],v[28:31],v[164:167]
ds_read_u16_d16_hi v46,v46
ds_read_b64_tr_b16 a[228:229],v3 offset:12320
v_mfma_f32_16x16x32_bf16 v[160:163],a[252:255],v[24:27],v[160:163]
ds_read_b64_tr_b16 a[230:231],v3 offset:13856
ds_read_b64_tr_b16 a[236:237],v3 offset:12328
v_mfma_f32_16x16x32_bf16 v[240:243],a[232:235],v[24:27],v[240:243]
ds_read_b64_tr_b16 a[238:239],v3 offset:13864
v_lshlrev_b32_sdwa v45,1,v45 dst_sel:DWORD dst_unused:UNUSED_PAD src0_sel:DWORD src1_sel:WORD_1
v_mfma_f32_16x16x32_bf16 v[244:247],a[232:235],v[28:31],v[244:247]
ds_read_u16 v45,v45
v_lshlrev_b32_sdwa v47,1,v47 dst_sel:DWORD dst_unused:UNUSED_PAD src0_sel:DWORD src1_sel:WORD_1
v_mfma_f32_16x16x32_bf16 v[180:183],a[240:243],v[28:31],v[180:183]
ds_read_u16_d16_hi v47,v47
v_lshlrev_b32_sdwa v40,1,v40 dst_sel:DWORD dst_unused:UNUSED_PAD src0_sel:DWORD src1_sel:WORD_1
v_mfma_f32_16x16x32_bf16 v[176:179],a[240:243],v[24:27],v[176:179]
ds_read_u16 v40,v40
v_lshlrev_b32_sdwa v42,1,v42 dst_sel:DWORD dst_unused:UNUSED_PAD src0_sel:DWORD src1_sel:WORD_1
v_mfma_f32_16x16x32_bf16 v[248:251],a[224:227],v[24:27],v[248:251]
ds_read_u16_d16_hi v42,v42
v_lshlrev_b32_sdwa v41,1,v41 dst_sel:DWORD dst_unused:UNUSED_PAD src0_sel:DWORD src1_sel:WORD_1
v_mfma_f32_16x16x32_bf16 v[252:255],a[224:227],v[28:31],v[252:255]
ds_read_u16 v41,v41
v_mfma_f32_16x16x32_bf16 v[196:199],a[248:251],v[28:31],v[196:199]
ds_read_b64_tr_b16 a[156:157],v12 offset:16416
v_lshlrev_b32_sdwa v43,1,v43 dst_sel:DWORD dst_unused:UNUSED_PAD src0_sel:DWORD src1_sel:WORD_1
v_mfma_f32_16x16x32_bf16 v[192:195],a[248:251],v[24:27],v[192:195]
ds_read_u16_d16_hi v43,v43
v_mfma_f32_16x16x32_bf16 v[28:31],a[216:219],a[8:11],v[16:19]
ds_read_b64_tr_b16 a[158:159],v12 offset:17440
v_lshlrev_b32_sdwa v36,1,v36 dst_sel:DWORD dst_unused:UNUSED_PAD src0_sel:DWORD src1_sel:WORD_1
v_mfma_f32_16x16x32_bf16 v[28:31],a[220:223],a[24:27],v[28:31]
s_waitcnt lgkmcnt(8)
ds_read_u16 v36,v36
v_lshl_add_u64 v[32:33],v[32:33],0,v[34:35]
v_fma_mixlo_f16 v8,v57,s99,0
v_mfma_f32_16x16x32_bf16 v[28:31],a[192:195],a[40:43],v[28:31]
ds_read_b64_tr_b16 a[160:161],v12 offset:14368
v_lshl_add_u64 v[34:35],v[60:61],0,v[62:63]
v_lshlrev_b32_sdwa v38,1,v38 dst_sel:DWORD dst_unused:UNUSED_PAD src0_sel:DWORD src1_sel:WORD_1
v_mfma_f32_16x16x32_bf16 v[24:27],a[196:199],a[40:43],v[20:23]
ds_read_u16_d16_hi v38,v38
v_fma_mixhi_f16 v8,v59,s99,0
v_mfma_f32_16x16x32_bf16 v[24:27],a[200:203],a[24:27],v[24:27]
ds_read_b64_tr_b16 a[162:163],v12 offset:15392
v_lshl_add_u64 v[60:61],v[48:49],0,v[50:51]
v_lshlrev_b32_sdwa v37,1,v37 dst_sel:DWORD dst_unused:UNUSED_PAD src0_sel:DWORD src1_sel:WORD_1
v_pk_fma_f16 v9,v8,v8,s54
v_mfma_f32_16x16x32_bf16 v[24:27],a[212:215],a[8:11],v[24:27]
ds_read_u16 v37,v37
v_pk_mul_f16 v9,v8,v9
v_mfma_f32_16x16x32_bf16 v[48:51],a[212:215],a[12:15],v[20:23]
ds_read_b64_tr_b16 a[164:165],v12 offset:12320
v_lshlrev_b32_sdwa v39,1,v39 dst_sel:DWORD dst_unused:UNUSED_PAD src0_sel:DWORD src1_sel:WORD_1
v_mfma_f32_16x16x32_bf16 v[48:51],a[200:203],a[28:31],v[48:51]
s_waitcnt lgkmcnt(8)
ds_read_u16_d16_hi v39,v39
v_lshl_add_u64 v[62:63],v[44:45],0,v[46:47]
v_mfma_f32_16x16x32_bf16 v[48:51],a[196:199],a[44:47],v[48:51]
v_exp_f16 v8,v9
v_lshlrev_b32_sdwa v52,1,v52 dst_sel:DWORD dst_unused:UNUSED_PAD src0_sel:DWORD src1_sel:WORD_1
v_mfma_f32_16x16x32_bf16 v[44:47],a[192:195],a[44:47],v[16:19]
ds_read_u16 v52,v52
v_lshlrev_b32_sdwa v54,1,v54 dst_sel:DWORD dst_unused:UNUSED_PAD src0_sel:DWORD src1_sel:WORD_1
v_mfma_f32_16x16x32_bf16 v[44:47],a[220:223],a[28:31],v[44:47]
ds_read_u16_d16_hi v54,v54
v_lshlrev_b32_sdwa v53,1,v53 dst_sel:DWORD dst_unused:UNUSED_PAD src0_sel:DWORD src1_sel:WORD_1
v_mfma_f32_16x16x32_bf16 v[44:47],a[216:219],a[12:15],v[44:47]
v_exp_f16_sdwa v8,v9 dst_sel:WORD_1 dst_unused:UNUSED_PRESERVE src0_sel:WORD_1
v_mfma_f32_16x16x32_bf16 v[208:211],a[244:247],v[32:35],v[208:211]
ds_read_u16 v53,v53
v_lshlrev_b32_sdwa v55,1,v55 dst_sel:DWORD dst_unused:UNUSED_PAD src0_sel:DWORD src1_sel:WORD_1
v_mfma_f32_16x16x32_bf16 v[212:215],a[244:247],v[60:63],v[212:215]
ds_read_u16_d16_hi v55,v55
v_pk_fma_f16 v8,v8,s55,s55
v_mfma_f32_16x16x32_bf16 v[172:175],a[252:255],v[60:63],v[172:175]
ds_read_b64_tr_b16 a[244:245],v3 offset:12384
v_lshlrev_b32_sdwa v56,1,v56 dst_sel:DWORD dst_unused:UNUSED_PAD src0_sel:DWORD src1_sel:WORD_1
v_mfma_f32_16x16x32_bf16 v[168:171],a[252:255],v[32:35],v[168:171]
ds_read_u16 v56,v56
v_rcp_f16_e64 v9,v8
v_mfma_f32_16x16x32_bf16 v[216:219],a[232:235],v[32:35],v[216:219]
ds_read_b64_tr_b16 a[246:247],v3 offset:13920
ds_read_b64_tr_b16 a[252:253],v3 offset:12392
v_mfma_f32_16x16x32_bf16 v[220:223],a[232:235],v[60:63],v[220:223]
ds_read_b64_tr_b16 a[254:255],v3 offset:13928
ds_read_b64_tr_b16 a[232:233],v3 offset:12448
v_lshlrev_b32_sdwa v58,1,v58 dst_sel:DWORD dst_unused:UNUSED_PAD src0_sel:DWORD src1_sel:WORD_1
v_mfma_f32_16x16x32_bf16 v[188:191],a[240:243],v[60:63],v[188:191]
ds_read_u16_d16_hi v58,v58
ds_read_b64_tr_b16 a[234:235],v3 offset:13984
v_mfma_f32_16x16x32_bf16 v[184:187],a[240:243],v[32:35],v[184:187]
ds_read_b64_tr_b16 a[240:241],v3 offset:12456
v_rcp_f16_sdwa v9,v8 dst_sel:WORD_1 dst_unused:UNUSED_PRESERVE src0_sel:WORD_1
v_lshlrev_b32_sdwa v28,1,v28 dst_sel:DWORD dst_unused:UNUSED_PAD src0_sel:DWORD src1_sel:WORD_1
v_mfma_f32_16x16x32_bf16 v[224:227],a[224:227],v[32:35],v[224:227]
ds_read_u16 v28,v28
ds_read_b64_tr_b16 a[242:243],v3 offset:13992
v_mfma_f32_16x16x32_bf16 v[228:231],a[224:227],v[60:63],v[228:231]
ds_read_b64_tr_b16 a[166:167],v12 offset:13344
v_lshlrev_b32_sdwa v30,1,v30 dst_sel:DWORD dst_unused:UNUSED_PAD src0_sel:DWORD src1_sel:WORD_1
v_mfma_f32_16x16x32_bf16 v[204:207],a[248:251],v[60:63],v[204:207]
ds_read_u16_d16_hi v30,v30
v_fma_mix_f32 v57,v57,v9,0 op_sel_hi:[0,1,0]
v_mfma_f32_16x16x32_bf16 v[200:203],a[248:251],v[32:35],v[200:203]
ds_read_b64_tr_b16 a[168:169],v12 offset:18480
ds_read_b64_tr_b16 a[170:171],v12 offset:19504
v_lshlrev_b32_sdwa v29,1,v29 dst_sel:DWORD dst_unused:UNUSED_PAD src0_sel:DWORD src1_sel:WORD_1
v_fma_mix_f32 v59,v59,v9,0 op_sel:[0,1,0] op_sel_hi:[0,1,0]
v_lshlrev_b32_sdwa v31,1,v31 dst_sel:DWORD dst_unused:UNUSED_PAD src0_sel:DWORD src1_sel:WORD_1
v_mfma_f32_16x16x32_bf16 v[60:63],a[216:219],a[48:51],v[16:19]
s_nop 0
ds_read_u16 v29,v29
ds_read_u16_d16_hi v31,v31
v_lshlrev_b32_sdwa v24,1,v24 dst_sel:DWORD dst_unused:UNUSED_PAD src0_sel:DWORD src1_sel:WORD_1
v_mfma_f32_16x16x32_bf16 v[60:63],a[220:223],a[64:67],v[60:63]
s_waitcnt lgkmcnt(13)
ds_read_u16 v24,v24
ds_read_b64_tr_b16 a[172:173],v12 offset:20528
v_lshl_add_u64 v[40:41],v[40:41],0,v[42:43]
v_mfma_f32_16x16x32_bf16 v[60:63],a[192:195],a[80:83],v[60:63]
ds_read_b64_tr_b16 a[174:175],v12 offset:21552
ds_read_b64_tr_b16 a[208:209],v1 offset:12320
v_lshl_add_u64 v[42:43],v[36:37],0,v[38:39]
v_lshlrev_b32_sdwa v26,1,v26 dst_sel:DWORD dst_unused:UNUSED_PAD src0_sel:DWORD src1_sel:WORD_1
v_mfma_f32_16x16x32_bf16 v[32:35],a[196:199],a[80:83],v[20:23]
ds_read_u16_d16_hi v26,v26
v_lshlrev_b32_sdwa v25,1,v25 dst_sel:DWORD dst_unused:UNUSED_PAD src0_sel:DWORD src1_sel:WORD_1
v_mfma_f32_16x16x32_bf16 v[32:35],a[200:203],a[64:67],v[32:35]
ds_read_u16 v25,v25
v_lshl_add_u64 v[36:37],v[52:53],0,v[54:55]
v_mfma_f32_16x16x32_bf16 v[32:35],a[212:215],a[48:51],v[32:35]
ds_read_b64_tr_b16 a[210:211],v1 offset:13344
v_lshlrev_b32_sdwa v27,1,v27 dst_sel:DWORD dst_unused:UNUSED_PAD src0_sel:DWORD src1_sel:WORD_1
v_mfma_f32_16x16x32_bf16 v[52:55],a[212:215],a[52:55],v[20:23]
s_waitcnt lgkmcnt(12)
ds_read_u16_d16_hi v27,v27
v_or_b32_e64 v38,v56,v58
v_mfma_f32_16x16x32_bf16 v[52:55],a[200:203],a[68:71],v[52:55]
ds_read_b64_tr_b16 a[204:205],v1 offset:14368
v_cvt_pk_bf16_f32 v39,v57,v59
v_lshlrev_b32_sdwa v44,1,v44 dst_sel:DWORD dst_unused:UNUSED_PAD src0_sel:DWORD src1_sel:WORD_1
v_mfma_f32_16x16x32_bf16 v[52:55],a[196:199],a[84:87],v[52:55]
ds_read_u16 v44,v44
v_lshlrev_b32_sdwa v46,1,v46 dst_sel:DWORD dst_unused:UNUSED_PAD src0_sel:DWORD src1_sel:WORD_1
v_mfma_f32_16x16x32_bf16 v[56:59],a[192:195],a[84:87],v[16:19]
ds_read_u16_d16_hi v46,v46
v_lshlrev_b32_sdwa v45,1,v45 dst_sel:DWORD dst_unused:UNUSED_PAD src0_sel:DWORD src1_sel:WORD_1
v_mfma_f32_16x16x32_bf16 v[56:59],a[220:223],a[68:71],v[56:59]
ds_read_u16 v45,v45
v_lshlrev_b32_sdwa v47,1,v47 dst_sel:DWORD dst_unused:UNUSED_PAD src0_sel:DWORD src1_sel:WORD_1
v_mfma_f32_16x16x32_bf16 v[56:59],a[216:219],a[52:55],v[56:59]
ds_read_u16_d16_hi v47,v47
v_lshlrev_b32_sdwa v48,1,v48 dst_sel:DWORD dst_unused:UNUSED_PAD src0_sel:DWORD src1_sel:WORD_1
v_mfma_f32_16x16x32_bf16 v[136:139],a[228:231],v[40:43],v[136:139]
ds_read_u16 v48,v48
v_lshlrev_b32_sdwa v50,1,v50 dst_sel:DWORD dst_unused:UNUSED_PAD src0_sel:DWORD src1_sel:WORD_1
v_mfma_f32_16x16x32_bf16 v[140:143],a[228:231],v[36:39],v[140:143]
ds_read_u16_d16_hi v50,v50
v_mfma_f32_16x16x32_bf16 v[68:71],a[236:239],v[36:39],v[68:71]
ds_read_b64_tr_b16 a[206:207],v1 offset:15392
v_lshlrev_b32_sdwa v49,1,v49 dst_sel:DWORD dst_unused:UNUSED_PAD src0_sel:DWORD src1_sel:WORD_1
v_mfma_f32_16x16x32_bf16 v[64:67],a[236:239],v[40:43],v[64:67]
ds_read_u16 v49,v49
ds_read_b64_tr_b16 a[176:177],v12 offset:22576
v_mfma_f32_16x16x32_bf16 v[144:147],a[244:247],v[40:43],v[144:147]
ds_read_b64_tr_b16 a[178:179],v12 offset:23600
v_lshlrev_b32_sdwa v51,1,v51 dst_sel:DWORD dst_unused:UNUSED_PAD src0_sel:DWORD src1_sel:WORD_1
v_mfma_f32_16x16x32_bf16 v[148:151],a[244:247],v[36:39],v[148:151]
ds_read_u16_d16_hi v51,v51
v_lshlrev_b32_sdwa v60,1,v60 dst_sel:DWORD dst_unused:UNUSED_PAD src0_sel:DWORD src1_sel:WORD_1
v_mfma_f32_16x16x32_bf16 v[84:87],a[252:255],v[36:39],v[84:87]
ds_read_u16 v60,v60
v_mfma_f32_16x16x32_bf16 v[80:83],a[252:255],v[40:43],v[80:83]
ds_read_b64_tr_b16 a[180:181],v11 offset:22576
v_lshlrev_b32_sdwa v62,1,v62 dst_sel:DWORD dst_unused:UNUSED_PAD src0_sel:DWORD src1_sel:WORD_1
v_mfma_f32_16x16x32_bf16 v[152:155],a[232:235],v[40:43],v[152:155]
ds_read_u16_d16_hi v62,v62
v_lshlrev_b32_sdwa v61,1,v61 dst_sel:DWORD dst_unused:UNUSED_PAD src0_sel:DWORD src1_sel:WORD_1
v_mfma_f32_16x16x32_bf16 v[156:159],a[232:235],v[36:39],v[156:159]
ds_read_u16 v61,v61
v_mfma_f32_16x16x32_bf16 v[100:103],a[240:243],v[36:39],v[100:103]
ds_read_b64_tr_b16 a[182:183],v11 offset:23600
v_lshlrev_b32_sdwa v63,1,v63 dst_sel:DWORD dst_unused:UNUSED_PAD src0_sel:DWORD src1_sel:WORD_1
v_mfma_f32_16x16x32_bf16 v[96:99],a[240:243],v[40:43],v[96:99]
ds_read_u16_d16_hi v63,v63
v_mfma_f32_16x16x32_bf16 v[36:39],a[216:219],a[56:59],v[16:19]
ds_read_b64_tr_b16 a[184:185],v11 offset:20528
v_lshlrev_b32_sdwa v32,1,v32 dst_sel:DWORD dst_unused:UNUSED_PAD src0_sel:DWORD src1_sel:WORD_1
v_mfma_f32_16x16x32_bf16 v[36:39],a[220:223],a[72:75],v[36:39]
s_waitcnt lgkmcnt(8)
ds_read_u16 v32,v32
v_lshl_add_u64 v[28:29],v[28:29],0,v[30:31]
v_fma_mixlo_f16 v8,v53,s99,0
v_mfma_f32_16x16x32_bf16 v[36:39],a[192:195],a[88:91],v[36:39]
ds_read_b64_tr_b16 a[186:187],v11 offset:21552
v_lshl_add_u64 v[30:31],v[24:25],0,v[26:27]
v_lshlrev_b32_sdwa v34,1,v34 dst_sel:DWORD dst_unused:UNUSED_PAD src0_sel:DWORD src1_sel:WORD_1
v_mfma_f32_16x16x32_bf16 v[40:43],a[196:199],a[88:91],v[20:23]
ds_read_u16_d16_hi v34,v34
v_fma_mixhi_f16 v8,v55,s99,0
v_mfma_f32_16x16x32_bf16 v[40:43],a[200:203],a[72:75],v[40:43]
ds_read_b64_tr_b16 a[188:189],v11 offset:18480
v_lshl_add_u64 v[24:25],v[44:45],0,v[46:47]
v_lshlrev_b32_sdwa v33,1,v33 dst_sel:DWORD dst_unused:UNUSED_PAD src0_sel:DWORD src1_sel:WORD_1
v_pk_fma_f16 v9,v8,v8,s54
v_mfma_f32_16x16x32_bf16 v[40:43],a[212:215],a[56:59],v[40:43]
ds_read_u16 v33,v33
v_pk_mul_f16 v9,v8,v9
v_mfma_f32_16x16x32_bf16 v[44:47],a[212:215],a[60:63],v[20:23]
ds_read_b128 v[20:23],v4 offset:1408
v_lshlrev_b32_sdwa v35,1,v35 dst_sel:DWORD dst_unused:UNUSED_PAD src0_sel:DWORD src1_sel:WORD_1
v_mfma_f32_16x16x32_bf16 v[44:47],a[200:203],a[76:79],v[44:47]
s_waitcnt lgkmcnt(8)
ds_read_b64_tr_b16 a[212:213],v1 offset:16416
ds_read_b64_tr_b16 a[214:215],v1 offset:17440
v_lshl_add_u64 v[26:27],v[48:49],0,v[50:51]
v_mfma_f32_16x16x32_bf16 v[44:47],a[196:199],a[92:95],v[44:47]
ds_read_u16_d16_hi v35,v35
v_exp_f16 v8,v9
v_lshlrev_b32_sdwa v56,1,v56 dst_sel:DWORD dst_unused:UNUSED_PAD src0_sel:DWORD src1_sel:WORD_1
v_mfma_f32_16x16x32_bf16 v[48:51],a[192:195],a[92:95],v[16:19]
ds_read_b128 v[16:19],v4 offset:1440
ds_read_b64_tr_b16 a[200:201],v2 offset:16416
v_mfma_f32_16x16x32_bf16 v[48:51],a[220:223],a[76:79],v[48:51]
ds_read_b64_tr_b16 a[202:203],v2 offset:17440
ds_read_b64_tr_b16 a[196:197],v2 offset:14368
v_lshlrev_b32_sdwa v58,1,v58 dst_sel:DWORD dst_unused:UNUSED_PAD src0_sel:DWORD src1_sel:WORD_1
v_mfma_f32_16x16x32_bf16 v[48:51],a[216:219],a[60:63],v[48:51]
ds_read_b64_tr_b16 a[198:199],v2 offset:15392
v_exp_f16_sdwa v8,v9 dst_sel:WORD_1 dst_unused:UNUSED_PRESERVE src0_sel:WORD_1
v_mfma_f32_16x16x32_bf16 v[112:115],a[228:231],v[28:31],v[112:115]
ds_read_u16 v56,v56
ds_read_u16_d16_hi v58,v58
v_lshlrev_b32_sdwa v57,1,v57 dst_sel:DWORD dst_unused:UNUSED_PAD src0_sel:DWORD src1_sel:WORD_1
v_mfma_f32_16x16x32_bf16 v[116:119],a[228:231],v[24:27],v[116:119]
ds_read_u16 v57,v57
v_lshlrev_b32_sdwa v59,1,v59 dst_sel:DWORD dst_unused:UNUSED_PAD src0_sel:DWORD src1_sel:WORD_1
v_mfma_f32_16x16x32_bf16 v[76:79],a[236:239],v[24:27],v[76:79]
ds_read_u16_d16_hi v59,v59
v_pk_fma_f16 v8,v8,s55,s55
v_mfma_f32_16x16x32_bf16 v[72:75],a[236:239],v[28:31],v[72:75]
v_rcp_f16_e64 v9,v8
v_lshlrev_b32_sdwa v52,1,v52 dst_sel:DWORD dst_unused:UNUSED_PAD src0_sel:DWORD src1_sel:WORD_1
v_mfma_f32_16x16x32_bf16 v[120:123],a[244:247],v[28:31],v[120:123]
ds_read_b64_tr_b16 a[192:193],v2 offset:12320
v_lshlrev_b32_sdwa v54,1,v54 dst_sel:DWORD dst_unused:UNUSED_PAD src0_sel:DWORD src1_sel:WORD_1
v_mfma_f32_16x16x32_bf16 v[124:127],a[244:247],v[24:27],v[124:127]
ds_read_b64_tr_b16 a[194:195],v2 offset:13344
v_lshlrev_b32_sdwa v36,1,v36 dst_sel:DWORD dst_unused:UNUSED_PAD src0_sel:DWORD src1_sel:WORD_1
v_mfma_f32_16x16x32_bf16 v[92:95],a[252:255],v[24:27],v[92:95]
ds_read_u16 v52,v52
v_lshlrev_b32_sdwa v38,1,v38 dst_sel:DWORD dst_unused:UNUSED_PAD src0_sel:DWORD src1_sel:WORD_1
v_mfma_f32_16x16x32_bf16 v[88:91],a[252:255],v[28:31],v[88:91]
ds_read_u16_d16_hi v54,v54
v_rcp_f16_sdwa v9,v8 dst_sel:WORD_1 dst_unused:UNUSED_PRESERVE src0_sel:WORD_1
v_mfma_f32_16x16x32_bf16 v[128:131],a[232:235],v[28:31],v[128:131]
ds_read_u16 v36,v36
v_fma_mix_f32 v53,v53,v9,0 op_sel_hi:[0,1,0]
v_mfma_f32_16x16x32_bf16 v[132:135],a[232:235],v[24:27],v[132:135]
ds_read_u16_d16_hi v38,v38
v_lshlrev_b32_sdwa v37,1,v37 dst_sel:DWORD dst_unused:UNUSED_PAD src0_sel:DWORD src1_sel:WORD_1
v_mfma_f32_16x16x32_bf16 v[108:111],a[240:243],v[24:27],v[108:111]
TP9647_%=:
s_cbranch_execz TR1_%=
ds_read_u16 v37,v37
v_fma_mix_f32 v55,v55,v9,0 op_sel:[0,1,0] op_sel_hi:[0,1,0]
v_mfma_f32_16x16x32_bf16 v[104:107],a[240:243],v[28:31],v[104:107]
s_waitcnt lgkmcnt(7)
ds_read_b64_tr_b16 a[190:191],v11 offset:19504
v_lshlrev_b32_sdwa v39,1,v39 dst_sel:DWORD dst_unused:UNUSED_PAD src0_sel:DWORD src1_sel:WORD_1
v_mfma_f32_16x16x32_bf16 v[24:27],a[208:211],a[0:3],v[20:23]
ds_read_u16_d16_hi v39,v39
v_lshlrev_b32_sdwa v40,1,v40 dst_sel:DWORD dst_unused:UNUSED_PAD src0_sel:DWORD src1_sel:WORD_1
v_mfma_f32_16x16x32_bf16 v[24:27],a[204:207],a[16:19],v[24:27]
ds_read_u16 v40,v40
v_lshl_add_u64 v[60:61],v[60:61],0,v[62:63]
v_mfma_f32_16x16x32_bf16 v[24:27],a[212:215],a[32:35],v[24:27]
v_lshl_add_u64 v[62:63],v[32:33],0,v[34:35]
v_lshlrev_b32_sdwa v42,1,v42 dst_sel:DWORD dst_unused:UNUSED_PAD src0_sel:DWORD src1_sel:WORD_1
v_mfma_f32_16x16x32_bf16 v[28:31],a[200:203],a[32:35],v[16:19]
ds_read_u16_d16_hi v42,v42
v_lshlrev_b32_sdwa v41,1,v41 dst_sel:DWORD dst_unused:UNUSED_PAD src0_sel:DWORD src1_sel:WORD_1
v_mfma_f32_16x16x32_bf16 v[28:31],a[196:199],a[16:19],v[28:31]
s_waitcnt lgkmcnt(7)
ds_read_u16 v41,v41
v_lshl_add_u64 v[32:33],v[56:57],0,v[58:59]
v_mfma_f32_16x16x32_bf16 v[28:31],a[192:195],a[0:3],v[28:31]
v_lshlrev_b32_sdwa v43,1,v43 dst_sel:DWORD dst_unused:UNUSED_PAD src0_sel:DWORD src1_sel:WORD_1
v_lshlrev_b32_sdwa v48,1,v48 dst_sel:DWORD dst_unused:UNUSED_PAD src0_sel:DWORD src1_sel:WORD_1
v_mfma_f32_16x16x32_bf16 v[56:59],a[192:195],a[4:7],v[16:19]
ds_read_u16_d16_hi v43,v43
v_or_b32_e64 v34,v52,v54
v_mfma_f32_16x16x32_bf16 v[56:59],a[196:199],a[20:23],v[56:59]
s_waitcnt vmcnt(0)
ds_read_u16 v48,v48
v_cvt_pk_bf16_f32 v35,v53,v55
v_mfma_f32_16x16x32_bf16 v[56:59],a[200:203],a[36:39],v[56:59]
s_barrier
v_lshlrev_b32_sdwa v50,1,v50 dst_sel:DWORD dst_unused:UNUSED_PAD src0_sel:DWORD src1_sel:WORD_1
v_lshlrev_b32_sdwa v49,1,v49 dst_sel:DWORD dst_unused:UNUSED_PAD src0_sel:DWORD src1_sel:WORD_1
v_mfma_f32_16x16x32_bf16 v[52:55],a[212:215],a[36:39],v[20:23]
ds_read_u16_d16_hi v50,v50
v_lshlrev_b32_sdwa v51,1,v51 dst_sel:DWORD dst_unused:UNUSED_PAD src0_sel:DWORD src1_sel:WORD_1
v_mfma_f32_16x16x32_bf16 v[52:55],a[204:207],a[20:23],v[52:55]
ds_read_u16 v49,v49
v_mfma_f32_16x16x32_bf16 v[52:55],a[208:211],a[4:7],v[52:55]
ds_read_u16_d16_hi v51,v51
v_lshlrev_b32_sdwa v44,1,v44 dst_sel:DWORD dst_unused:UNUSED_PAD src0_sel:DWORD src1_sel:WORD_1
v_mfma_f32_16x16x32_bf16 v[232:235],a[228:231],v[60:63],v[232:235]
ds_read_u16 v44,v44
v_lshlrev_b32_sdwa v46,1,v46 dst_sel:DWORD dst_unused:UNUSED_PAD src0_sel:DWORD src1_sel:WORD_1
v_mfma_f32_16x16x32_bf16 v[236:239],a[228:231],v[32:35],v[236:239]
ds_read_u16_d16_hi v46,v46
v_mfma_f32_16x16x32_bf16 v[164:167],a[236:239],v[32:35],v[164:167]
ds_read_b64_tr_b16 a[224:225],v3 offset:18480
v_lshlrev_b32_sdwa v45,1,v45 dst_sel:DWORD dst_unused:UNUSED_PAD src0_sel:DWORD src1_sel:WORD_1
v_mfma_f32_16x16x32_bf16 v[160:163],a[236:239],v[60:63],v[160:163]
ds_read_u16 v45,v45
v_mfma_f32_16x16x32_bf16 v[240:243],a[244:247],v[60:63],v[240:243]
ds_read_b64_tr_b16 a[226:227],v3 offset:20016
v_lshlrev_b32_sdwa v47,1,v47 dst_sel:DWORD dst_unused:UNUSED_PAD src0_sel:DWORD src1_sel:WORD_1
v_mfma_f32_16x16x32_bf16 v[244:247],a[244:247],v[32:35],v[244:247]
ds_read_u16_d16_hi v47,v47
v_lshlrev_b32_sdwa v24,1,v24 dst_sel:DWORD dst_unused:UNUSED_PAD src0_sel:DWORD src1_sel:WORD_1
v_mfma_f32_16x16x32_bf16 v[180:183],a[252:255],v[32:35],v[180:183]
ds_read_u16 v24,v24
v_mfma_f32_16x16x32_bf16 v[176:179],a[252:255],v[60:63],v[176:179]
ds_read_b64_tr_b16 a[248:249],v3 offset:18488
v_lshlrev_b32_sdwa v26,1,v26 dst_sel:DWORD dst_unused:UNUSED_PAD src0_sel:DWORD src1_sel:WORD_1
v_mfma_f32_16x16x32_bf16 v[248:251],a[232:235],v[60:63],v[248:251]
ds_read_u16_d16_hi v26,v26
v_lshlrev_b32_sdwa v25,1,v25 dst_sel:DWORD dst_unused:UNUSED_PAD src0_sel:DWORD src1_sel:WORD_1
v_mfma_f32_16x16x32_bf16 v[252:255],a[232:235],v[32:35],v[252:255]
ds_read_u16 v25,v25
v_mfma_f32_16x16x32_bf16 v[196:199],a[240:243],v[32:35],v[196:199]
ds_read_b64_tr_b16 a[250:251],v3 offset:20024
v_lshlrev_b32_sdwa v27,1,v27 dst_sel:DWORD dst_unused:UNUSED_PAD src0_sel:DWORD src1_sel:WORD_1
v_mfma_f32_16x16x32_bf16 v[192:195],a[240:243],v[60:63],v[192:195]
ds_read_u16_d16_hi v27,v27
v_mfma_f32_16x16x32_bf16 v[32:35],a[208:211],a[8:11],v[20:23]
ds_read_b64_tr_b16 a[220:221],v1 offset:18480
v_lshlrev_b32_sdwa v28,1,v28 dst_sel:DWORD dst_unused:UNUSED_PAD src0_sel:DWORD src1_sel:WORD_1
v_mfma_f32_16x16x32_bf16 v[32:35],a[204:207],a[24:27],v[32:35]
s_waitcnt lgkmcnt(8)
ds_read_u16 v28,v28
v_lshl_add_u64 v[36:37],v[36:37],0,v[38:39]
v_fma_mixlo_f16 v8,v57,s99,0
v_mfma_f32_16x16x32_bf16 v[32:35],a[212:215],a[40:43],v[32:35]
ds_read_b64_tr_b16 a[222:223],v1 offset:19504
v_lshl_add_u64 v[38:39],v[40:41],0,v[42:43]
v_lshlrev_b32_sdwa v30,1,v30 dst_sel:DWORD dst_unused:UNUSED_PAD src0_sel:DWORD src1_sel:WORD_1
v_mfma_f32_16x16x32_bf16 v[60:63],a[200:203],a[40:43],v[16:19]
ds_read_u16_d16_hi v30,v30
v_fma_mixhi_f16 v8,v59,s99,0
v_mfma_f32_16x16x32_bf16 v[60:63],a[196:199],a[24:27],v[60:63]
ds_read_b64_tr_b16 a[216:217],v1 offset:20528
v_lshl_add_u64 v[40:41],v[48:49],0,v[50:51]
v_lshlrev_b32_sdwa v29,1,v29 dst_sel:DWORD dst_unused:UNUSED_PAD src0_sel:DWORD src1_sel:WORD_1
v_pk_fma_f16 v9,v8,v8,s54
v_mfma_f32_16x16x32_bf16 v[60:63],a[192:195],a[8:11],v[60:63]
ds_read_u16 v29,v29
v_pk_mul_f16 v9,v8,v9
v_mfma_f32_16x16x32_bf16 v[48:51],a[192:195],a[12:15],v[16:19]
ds_read_b64_tr_b16 a[218:219],v1 offset:21552
v_lshlrev_b32_sdwa v31,1,v31 dst_sel:DWORD dst_unused:UNUSED_PAD src0_sel:DWORD src1_sel:WORD_1
v_mfma_f32_16x16x32_bf16 v[48:51],a[196:199],a[28:31],v[48:51]
s_waitcnt lgkmcnt(8)
ds_read_u16_d16_hi v31,v31
v_lshl_add_u64 v[42:43],v[44:45],0,v[46:47]
v_mfma_f32_16x16x32_bf16 v[48:51],a[200:203],a[44:47],v[48:51]
v_exp_f16 v8,v9
v_lshlrev_b32_sdwa v52,1,v52 dst_sel:DWORD dst_unused:UNUSED_PAD src0_sel:DWORD src1_sel:WORD_1
v_mfma_f32_16x16x32_bf16 v[44:47],a[212:215],a[44:47],v[20:23]
ds_read_u16 v52,v52
v_lshlrev_b32_sdwa v54,1,v54 dst_sel:DWORD dst_unused:UNUSED_PAD src0_sel:DWORD src1_sel:WORD_1
v_mfma_f32_16x16x32_bf16 v[44:47],a[204:207],a[28:31],v[44:47]
ds_read_u16_d16_hi v54,v54
v_lshlrev_b32_sdwa v53,1,v53 dst_sel:DWORD dst_unused:UNUSED_PAD src0_sel:DWORD src1_sel:WORD_1
v_mfma_f32_16x16x32_bf16 v[44:47],a[208:211],a[12:15],v[44:47]
ds_read_b64_tr_b16 a[0:1],v3 offset:38992
ds_read_b64_tr_b16 a[2:3],v3 offset:40528
v_exp_f16_sdwa v8,v9 dst_sel:WORD_1 dst_unused:UNUSED_PRESERVE src0_sel:WORD_1
v_mfma_f32_16x16x32_bf16 v[208:211],a[228:231],v[36:39],v[208:211]
ds_read_b64_tr_b16 a[4:5],v3 offset:45152
ds_read_b64_tr_b16 a[6:7],v3 offset:46688
v_mfma_f32_16x16x32_bf16 v[212:215],a[228:231],v[40:43],v[212:215]
ds_read_u16 v53,v53
ds_read_b64_tr_b16 a[8:9],v3 offset:45160
v_lshlrev_b32_sdwa v55,1,v55 dst_sel:DWORD dst_unused:UNUSED_PAD src0_sel:DWORD src1_sel:WORD_1
v_mfma_f32_16x16x32_bf16 v[172:175],a[236:239],v[40:43],v[172:175]
ds_read_u16_d16_hi v55,v55
ds_read_b64_tr_b16 a[10:11],v3 offset:46696
v_pk_fma_f16 v8,v8,s55,s55
v_mfma_f32_16x16x32_bf16 v[168:171],a[236:239],v[36:39],v[168:171]
ds_read_b64_tr_b16 a[12:13],v3 offset:39000
v_rcp_f16_e64 v9,v8
v_lshlrev_b32_sdwa v56,1,v56 dst_sel:DWORD dst_unused:UNUSED_PAD src0_sel:DWORD src1_sel:WORD_1
v_mfma_f32_16x16x32_bf16 v[216:219],a[244:247],v[36:39],v[216:219]
ds_read_u16 v56,v56
ds_read_b64_tr_b16 a[14:15],v3 offset:40536
v_mfma_f32_16x16x32_bf16 v[220:223],a[244:247],v[40:43],v[220:223]
ds_read_b64_tr_b16 a[228:229],v3 offset:18544
ds_read_b64_tr_b16 a[230:231],v3 offset:20080
v_lshlrev_b32_sdwa v58,1,v58 dst_sel:DWORD dst_unused:UNUSED_PAD src0_sel:DWORD src1_sel:WORD_1
v_mfma_f32_16x16x32_bf16 v[188:191],a[252:255],v[40:43],v[188:191]
ds_read_u16_d16_hi v58,v58
ds_read_b64_tr_b16 a[236:237],v3 offset:18552
v_mfma_f32_16x16x32_bf16 v[184:187],a[252:255],v[36:39],v[184:187]
ds_read_b64_tr_b16 a[238:239],v3 offset:20088
v_rcp_f16_sdwa v9,v8 dst_sel:WORD_1 dst_unused:UNUSED_PRESERVE src0_sel:WORD_1
v_lshlrev_b32_sdwa v32,1,v32 dst_sel:DWORD dst_unused:UNUSED_PAD src0_sel:DWORD src1_sel:WORD_1
v_mfma_f32_16x16x32_bf16 v[224:227],a[232:235],v[36:39],v[224:227]
ds_read_u16 v32,v32
ds_read_b64_tr_b16 a[244:245],v3 offset:18608
v_mfma_f32_16x16x32_bf16 v[228:231],a[232:235],v[40:43],v[228:231]
ds_read_b64_tr_b16 a[246:247],v3 offset:20144
ds_read_b64_tr_b16 a[252:253],v3 offset:18616
v_lshlrev_b32_sdwa v34,1,v34 dst_sel:DWORD dst_unused:UNUSED_PAD src0_sel:DWORD src1_sel:WORD_1
v_mfma_f32_16x16x32_bf16 v[204:207],a[240:243],v[40:43],v[204:207]
ds_read_u16_d16_hi v34,v34
ds_read_b64_tr_b16 a[254:255],v3 offset:20152
v_fma_mix_f32 v57,v57,v9,0 op_sel_hi:[0,1,0]
v_mfma_f32_16x16x32_bf16 v[200:203],a[240:243],v[36:39],v[200:203]
ds_read_b64_tr_b16 a[16:17],v3 offset:39056
ds_read_b64_tr_b16 a[18:19],v3 offset:40592
v_lshlrev_b32_sdwa v33,1,v33 dst_sel:DWORD dst_unused:UNUSED_PAD src0_sel:DWORD src1_sel:WORD_1
v_fma_mix_f32 v59,v59,v9,0 op_sel:[0,1,0] op_sel_hi:[0,1,0]
v_lshlrev_b32_sdwa v35,1,v35 dst_sel:DWORD dst_unused:UNUSED_PAD src0_sel:DWORD src1_sel:WORD_1
v_mfma_f32_16x16x32_bf16 v[40:43],a[208:211],a[48:51],v[20:23]
s_nop 0
ds_read_u16 v33,v33
ds_read_u16_d16_hi v35,v35
v_lshlrev_b32_sdwa v60,1,v60 dst_sel:DWORD dst_unused:UNUSED_PAD src0_sel:DWORD src1_sel:WORD_1
v_mfma_f32_16x16x32_bf16 v[40:43],a[204:207],a[64:67],v[40:43]
s_waitcnt lgkmcnt(15)
ds_read_u16 v60,v60
ds_read_b64_tr_b16 a[20:21],v3 offset:45216
v_lshl_add_u64 v[24:25],v[24:25],0,v[26:27]
v_mfma_f32_16x16x32_bf16 v[40:43],a[212:215],a[80:83],v[40:43]
ds_read_b64_tr_b16 a[22:23],v3 offset:46752
ds_read_b64_tr_b16 a[24:25],v3 offset:45224
v_lshl_add_u64 v[26:27],v[28:29],0,v[30:31]
v_lshlrev_b32_sdwa v62,1,v62 dst_sel:DWORD dst_unused:UNUSED_PAD src0_sel:DWORD src1_sel:WORD_1
v_mfma_f32_16x16x32_bf16 v[36:39],a[200:203],a[80:83],v[16:19]
ds_read_u16_d16_hi v62,v62
v_lshlrev_b32_sdwa v61,1,v61 dst_sel:DWORD dst_unused:UNUSED_PAD src0_sel:DWORD src1_sel:WORD_1
v_mfma_f32_16x16x32_bf16 v[36:39],a[196:199],a[64:67],v[36:39]
ds_read_u16 v61,v61
v_lshl_add_u64 v[28:29],v[52:53],0,v[54:55]
v_mfma_f32_16x16x32_bf16 v[36:39],a[192:195],a[48:51],v[36:39]
ds_read_b64_tr_b16 a[26:27],v3 offset:46760
v_lshlrev_b32_sdwa v63,1,v63 dst_sel:DWORD dst_unused:UNUSED_PAD src0_sel:DWORD src1_sel:WORD_1
v_mfma_f32_16x16x32_bf16 v[52:55],a[192:195],a[52:55],v[16:19]
s_waitcnt lgkmcnt(13)
ds_read_u16_d16_hi v63,v63
v_or_b32_e64 v30,v56,v58
v_mfma_f32_16x16x32_bf16 v[52:55],a[196:199],a[68:71],v[52:55]
ds_read_b64_tr_b16 a[28:29],v3 offset:39064
v_cvt_pk_bf16_f32 v31,v57,v59
v_lshlrev_b32_sdwa v44,1,v44 dst_sel:DWORD dst_unused:UNUSED_PAD src0_sel:DWORD src1_sel:WORD_1
v_mfma_f32_16x16x32_bf16 v[52:55],a[200:203],a[84:87],v[52:55]
ds_read_u16 v44,v44
v_lshlrev_b32_sdwa v46,1,v46 dst_sel:DWORD dst_unused:UNUSED_PAD src0_sel:DWORD src1_sel:WORD_1
v_mfma_f32_16x16x32_bf16 v[56:59],a[212:215],a[84:87],v[20:23]
ds_read_u16_d16_hi v46,v46
v_lshlrev_b32_sdwa v45,1,v45 dst_sel:DWORD dst_unused:UNUSED_PAD src0_sel:DWORD src1_sel:WORD_1
v_mfma_f32_16x16x32_bf16 v[56:59],a[204:207],a[68:71],v[56:59]
ds_read_u16 v45,v45
v_lshlrev_b32_sdwa v47,1,v47 dst_sel:DWORD dst_unused:UNUSED_PAD src0_sel:DWORD src1_sel:WORD_1
v_mfma_f32_16x16x32_bf16 v[56:59],a[208:211],a[52:55],v[56:59]
ds_read_u16_d16_hi v47,v47
v_lshlrev_b32_sdwa v48,1,v48 dst_sel:DWORD dst_unused:UNUSED_PAD src0_sel:DWORD src1_sel:WORD_1
v_mfma_f32_16x16x32_bf16 v[136:139],a[224:227],v[24:27],v[136:139]
ds_read_u16 v48,v48
v_lshlrev_b32_sdwa v50,1,v50 dst_sel:DWORD dst_unused:UNUSED_PAD src0_sel:DWORD src1_sel:WORD_1
v_mfma_f32_16x16x32_bf16 v[140:143],a[224:227],v[28:31],v[140:143]
ds_read_u16_d16_hi v50,v50
v_mfma_f32_16x16x32_bf16 v[68:71],a[248:251],v[28:31],v[68:71]
ds_read_b64_tr_b16 a[30:31],v3 offset:40600
v_lshlrev_b32_sdwa v49,1,v49 dst_sel:DWORD dst_unused:UNUSED_PAD src0_sel:DWORD src1_sel:WORD_1
v_mfma_f32_16x16x32_bf16 v[64:67],a[248:251],v[24:27],v[64:67]
ds_read_u16 v49,v49
ds_read_b64_tr_b16 a[32:33],v3 offset:39120
v_mfma_f32_16x16x32_bf16 v[144:147],a[228:231],v[24:27],v[144:147]
ds_read_b64_tr_b16 a[34:35],v3 offset:40656
v_lshlrev_b32_sdwa v51,1,v51 dst_sel:DWORD dst_unused:UNUSED_PAD src0_sel:DWORD src1_sel:WORD_1
v_mfma_f32_16x16x32_bf16 v[148:151],a[228:231],v[28:31],v[148:151]
ds_read_u16_d16_hi v51,v51
v_lshlrev_b32_sdwa v40,1,v40 dst_sel:DWORD dst_unused:UNUSED_PAD src0_sel:DWORD src1_sel:WORD_1
v_mfma_f32_16x16x32_bf16 v[84:87],a[236:239],v[28:31],v[84:87]
ds_read_u16 v40,v40
v_mfma_f32_16x16x32_bf16 v[80:83],a[236:239],v[24:27],v[80:83]
ds_read_b64_tr_b16 a[36:37],v3 offset:45280
v_lshlrev_b32_sdwa v42,1,v42 dst_sel:DWORD dst_unused:UNUSED_PAD src0_sel:DWORD src1_sel:WORD_1
v_mfma_f32_16x16x32_bf16 v[152:155],a[244:247],v[24:27],v[152:155]
ds_read_u16_d16_hi v42,v42
v_lshlrev_b32_sdwa v41,1,v41 dst_sel:DWORD dst_unused:UNUSED_PAD src0_sel:DWORD src1_sel:WORD_1
v_mfma_f32_16x16x32_bf16 v[156:159],a[244:247],v[28:31],v[156:159]
s_waitcnt lgkmcnt(9)
ds_read_u16 v41,v41
v_mfma_f32_16x16x32_bf16 v[100:103],a[252:255],v[28:31],v[100:103]
ds_read_b64_tr_b16 a[38:39],v3 offset:46816
v_lshlrev_b32_sdwa v43,1,v43 dst_sel:DWORD dst_unused:UNUSED_PAD src0_sel:DWORD src1_sel:WORD_1
v_mfma_f32_16x16x32_bf16 v[96:99],a[252:255],v[24:27],v[96:99]
ds_read_u16_d16_hi v43,v43
v_mfma_f32_16x16x32_bf16 v[28:31],a[208:211],a[56:59],v[20:23]
ds_read_b64_tr_b16 a[40:41],v3 offset:45288
v_mul_f32_dpp v136,v14,v136 row_newbcast:0 row_mask:0xf bank_mask:0xf
s_add_u32 s8,s61,3145728
v_lshlrev_b32_sdwa v36,1,v36 dst_sel:DWORD dst_unused:UNUSED_PAD src0_sel:DWORD src1_sel:WORD_1
v_mfma_f32_16x16x32_bf16 v[28:31],a[204:207],a[72:75],v[28:31]
ds_read_u16 v36,v36
v_lshl_add_u64 v[32:33],v[32:33],0,v[34:35]
v_mul_f32_dpp v137,v14,v137 row_newbcast:1 row_mask:0xf bank_mask:0xf
v_mul_f32_dpp v138,v14,v138 row_newbcast:2 row_mask:0xf bank_mask:0xf
v_mfma_f32_16x16x32_bf16 v[28:31],a[212:215],a[88:91],v[28:31]
ds_read_b64_tr_b16 a[42:43],v3 offset:46824
v_lshl_add_u64 v[34:35],v[60:61],0,v[62:63]
v_mul_f32_dpp v139,v14,v139 row_newbcast:3 row_mask:0xf bank_mask:0xf
v_lshlrev_b32_sdwa v38,1,v38 dst_sel:DWORD dst_unused:UNUSED_PAD src0_sel:DWORD src1_sel:WORD_1
v_mfma_f32_16x16x32_bf16 v[24:27],a[200:203],a[88:91],v[16:19]
ds_read_u16_d16_hi v38,v38
v_mul_f32_dpp v64,v14,v64 row_newbcast:4 row_mask:0xf bank_mask:0xf
v_mul_f32_dpp v65,v14,v65 row_newbcast:5 row_mask:0xf bank_mask:0xf
v_mfma_f32_16x16x32_bf16 v[24:27],a[196:199],a[72:75],v[24:27]
ds_read_b64_tr_b16 a[44:45],v3 offset:39128
v_lshl_add_u64 v[60:61],v[44:45],0,v[46:47]
v_mul_f32_dpp v66,v14,v66 row_newbcast:6 row_mask:0xf bank_mask:0xf
v_lshlrev_b32_sdwa v37,1,v37 dst_sel:DWORD dst_unused:UNUSED_PAD src0_sel:DWORD src1_sel:WORD_1
v_mfma_f32_16x16x32_bf16 v[24:27],a[192:195],a[56:59],v[24:27]
ds_read_u16 v37,v37
v_mul_f32_dpp v67,v14,v67 row_newbcast:7 row_mask:0xf bank_mask:0xf
v_lshlrev_b32_sdwa v39,1,v39 dst_sel:DWORD dst_unused:UNUSED_PAD src0_sel:DWORD src1_sel:WORD_1
v_cvt_pk_bf16_f32 v136,v136,v137
v_mfma_f32_16x16x32_bf16 v[44:47],a[192:195],a[60:63],v[16:19]
s_nop 0
ds_read_b128 v[16:19],v4 offset:0
v_cvt_pk_bf16_f32 v137,v138,v139
v_mfma_f32_16x16x32_bf16 v[44:47],a[196:199],a[76:79],v[44:47]
s_waitcnt lgkmcnt(8)
ds_read_u16_d16_hi v39,v39
ds_read_b64_tr_b16 a[192:193],v1 offset:22576
v_lshl_add_u64 v[62:63],v[48:49],0,v[50:51]
v_cvt_pk_bf16_f32 v138,v64,v65
v_lshlrev_b32_sdwa v56,1,v56 dst_sel:DWORD dst_unused:UNUSED_PAD src0_sel:DWORD src1_sel:WORD_1
v_cvt_pk_bf16_f32 v139,v66,v67
v_mfma_f32_16x16x32_bf16 v[44:47],a[200:203],a[92:95],v[44:47]
buffer_store_dwordx4 v[136:139],v7,s[40:43],s8 offen offset:0 sc1
ds_read_b64_tr_b16 a[194:195],v1 offset:23600
v_mul_f32_dpp v144,v14,v144 row_newbcast:8 row_mask:0xf bank_mask:0xf
v_lshlrev_b32_sdwa v58,1,v58 dst_sel:DWORD dst_unused:UNUSED_PAD src0_sel:DWORD src1_sel:WORD_1
v_mfma_f32_16x16x32_bf16 v[48:51],a[212:215],a[92:95],v[20:23]
ds_read_b128 v[20:23],v4 offset:32
ds_read_b64_tr_b16 a[196:197],v2 offset:22576
v_mul_f32_dpp v145,v14,v145 row_newbcast:9 row_mask:0xf bank_mask:0xf
v_mul_f32_dpp v146,v14,v146 row_newbcast:10 row_mask:0xf bank_mask:0xf
v_mfma_f32_16x16x32_bf16 v[48:51],a[204:207],a[76:79],v[48:51]
ds_read_b64_tr_b16 a[198:199],v2 offset:23600
ds_read_u16 v56,v56
v_mul_f32_dpp v147,v14,v147 row_newbcast:11 row_mask:0xf bank_mask:0xf
v_lshlrev_b32_sdwa v57,1,v57 dst_sel:DWORD dst_unused:UNUSED_PAD src0_sel:DWORD src1_sel:WORD_1
v_mfma_f32_16x16x32_bf16 v[48:51],a[208:211],a[60:63],v[48:51]
ds_read_b64_tr_b16 a[200:201],v2 offset:20528
ds_read_b64_tr_b16 a[202:203],v2 offset:21552
v_mul_f32_dpp v80,v14,v80 row_newbcast:12 row_mask:0xf bank_mask:0xf
v_mul_f32_dpp v81,v14,v81 row_newbcast:13 row_mask:0xf bank_mask:0xf
v_mfma_f32_16x16x32_bf16 v[112:115],a[224:227],v[32:35],v[112:115]
ds_read_u16_d16_hi v58,v58
ds_read_u16 v57,v57
v_mul_f32_dpp v82,v14,v82 row_newbcast:14 row_mask:0xf bank_mask:0xf
v_lshlrev_b32_sdwa v59,1,v59 dst_sel:DWORD dst_unused:UNUSED_PAD src0_sel:DWORD src1_sel:WORD_1
v_mfma_f32_16x16x32_bf16 v[116:119],a[224:227],v[60:63],v[116:119]
ds_read_u16_d16_hi v59,v59
v_mul_f32_dpp v83,v14,v83 row_newbcast:15 row_mask:0xf bank_mask:0xf
v_lshlrev_b32_sdwa v52,1,v52 dst_sel:DWORD dst_unused:UNUSED_PAD src0_sel:DWORD src1_sel:WORD_1
v_cvt_pk_bf16_f32 v144,v144,v145
v_mfma_f32_16x16x32_bf16 v[76:79],a[248:251],v[60:63],v[76:79]
ds_read_b64_tr_b16 a[212:213],v2 offset:18480
v_cvt_pk_bf16_f32 v145,v146,v147
v_mfma_f32_16x16x32_bf16 v[72:75],a[248:251],v[32:35],v[72:75]
ds_read_b64_tr_b16 a[214:215],v2 offset:19504
v_cvt_pk_bf16_f32 v146,v80,v81
v_lshlrev_b32_sdwa v54,1,v54 dst_sel:DWORD dst_unused:UNUSED_PAD src0_sel:DWORD src1_sel:WORD_1
v_cvt_pk_bf16_f32 v147,v82,v83
v_mfma_f32_16x16x32_bf16 v[120:123],a[228:231],v[32:35],v[120:123]
ds_read_u16 v52,v52
v_mul_f32_dpp v152,v15,v152 row_newbcast:0 row_mask:0xf bank_mask:0xf
v_lshlrev_b32_sdwa v53,1,v53 dst_sel:DWORD dst_unused:UNUSED_PAD src0_sel:DWORD src1_sel:WORD_1
v_mfma_f32_16x16x32_bf16 v[124:127],a[228:231],v[60:63],v[124:127]
ds_read_u16_d16_hi v54,v54
v_mul_f32_dpp v153,v15,v153 row_newbcast:1 row_mask:0xf bank_mask:0xf
v_mul_f32_dpp v154,v15,v154 row_newbcast:2 row_mask:0xf bank_mask:0xf
v_mfma_f32_16x16x32_bf16 v[92:95],a[236:239],v[60:63],v[92:95]
ds_read_u16 v53,v53
v_mul_f32_dpp v155,v15,v155 row_newbcast:3 row_mask:0xf bank_mask:0xf
v_lshlrev_b32_sdwa v55,1,v55 dst_sel:DWORD dst_unused:UNUSED_PAD src0_sel:DWORD src1_sel:WORD_1
v_mfma_f32_16x16x32_bf16 v[88:91],a[236:239],v[32:35],v[88:91]
buffer_store_dwordx4 v[144:147],v7,s[40:43],s8 offen offset:64 sc1
v_mul_f32_dpp v96,v15,v96 row_newbcast:4 row_mask:0xf bank_mask:0xf
v_mul_f32_dpp v97,v15,v97 row_newbcast:5 row_mask:0xf bank_mask:0xf
v_mfma_f32_16x16x32_bf16 v[128:131],a[244:247],v[32:35],v[128:131]
ds_read_u16_d16_hi v55,v55
v_mul_f32_dpp v98,v15,v98 row_newbcast:6 row_mask:0xf bank_mask:0xf
v_lshlrev_b32_sdwa v28,1,v28 dst_sel:DWORD dst_unused:UNUSED_PAD src0_sel:DWORD src1_sel:WORD_1
v_mfma_f32_16x16x32_bf16 v[132:135],a[244:247],v[60:63],v[132:135]
ds_read_u16 v28,v28
v_mul_f32_dpp v99,v15,v99 row_newbcast:7 row_mask:0xf bank_mask:0xf
v_lshlrev_b32_sdwa v30,1,v30 dst_sel:DWORD dst_unused:UNUSED_PAD src0_sel:DWORD src1_sel:WORD_1
v_cvt_pk_bf16_f32 v152,v152,v153
v_mfma_f32_16x16x32_bf16 v[108:111],a[252:255],v[60:63],v[108:111]
s_nop 0
ds_read_u16_d16_hi v30,v30
v_cvt_pk_bf16_f32 v153,v154,v155
v_mfma_f32_16x16x32_bf16 v[104:107],a[252:255],v[32:35],v[104:107]
s_waitcnt lgkmcnt(7)
ds_read_b64_tr_b16 a[46:47],v3 offset:40664
v_cvt_pk_bf16_f32 v154,v96,v97
v_lshlrev_b32_sdwa v29,1,v29 dst_sel:DWORD dst_unused:UNUSED_PAD src0_sel:DWORD src1_sel:WORD_1
v_cvt_pk_bf16_f32 v155,v98,v99
v_lshlrev_b32_sdwa v31,1,v31 dst_sel:DWORD dst_unused:UNUSED_PAD src0_sel:DWORD src1_sel:WORD_1
v_mfma_f32_16x16x32_bf16 v[60:63],a[220:223],a[48:51],v[16:19]
ds_read_u16 v29,v29
v_mul_f32_dpp v140,v14,v140 row_newbcast:0 row_mask:0xf bank_mask:0xf
v_lshlrev_b32_sdwa v24,1,v24 dst_sel:DWORD dst_unused:UNUSED_PAD src0_sel:DWORD src1_sel:WORD_1
v_mfma_f32_16x16x32_bf16 v[60:63],a[216:219],a[64:67],v[60:63]
ds_read_u16_d16_hi v31,v31
v_lshl_add_u64 v[40:41],v[40:41],0,v[42:43]
v_mul_f32_dpp v141,v14,v141 row_newbcast:1 row_mask:0xf bank_mask:0xf
v_mul_f32_dpp v142,v14,v142 row_newbcast:2 row_mask:0xf bank_mask:0xf
v_mfma_f32_16x16x32_bf16 v[60:63],a[192:195],a[80:83],v[60:63]
ds_read_u16 v24,v24
v_lshl_add_u64 v[42:43],v[36:37],0,v[38:39]
v_mul_f32_dpp v143,v14,v143 row_newbcast:3 row_mask:0xf bank_mask:0xf
v_lshlrev_b32_sdwa v26,1,v26 dst_sel:DWORD dst_unused:UNUSED_PAD src0_sel:DWORD src1_sel:WORD_1
v_mfma_f32_16x16x32_bf16 v[32:35],a[196:199],a[80:83],v[20:23]
ds_read_u16_d16_hi v26,v26
v_mul_f32_dpp v68,v14,v68 row_newbcast:4 row_mask:0xf bank_mask:0xf
v_mul_f32_dpp v69,v14,v69 row_newbcast:5 row_mask:0xf bank_mask:0xf
v_mfma_f32_16x16x32_bf16 v[32:35],a[200:203],a[64:67],v[32:35]
s_waitcnt lgkmcnt(8)
v_lshl_add_u64 v[36:37],v[56:57],0,v[58:59]
v_mul_f32_dpp v70,v14,v70 row_newbcast:6 row_mask:0xf bank_mask:0xf
v_lshlrev_b32_sdwa v25,1,v25 dst_sel:DWORD dst_unused:UNUSED_PAD src0_sel:DWORD src1_sel:WORD_1
v_mfma_f32_16x16x32_bf16 v[32:35],a[212:215],a[48:51],v[32:35]
buffer_store_dwordx4 v[152:155],v7,s[40:43],s8 offen offset:128 sc1
v_mul_f32_dpp v71,v14,v71 row_newbcast:7 row_mask:0xf bank_mask:0xf
v_lshlrev_b32_sdwa v27,1,v27 dst_sel:DWORD dst_unused:UNUSED_PAD src0_sel:DWORD src1_sel:WORD_1
v_cvt_pk_bf16_f32 v140,v140,v141
v_mfma_f32_16x16x32_bf16 v[56:59],a[212:215],a[52:55],v[20:23]
ds_read_u16 v25,v25
v_cvt_pk_bf16_f32 v141,v142,v143
v_mfma_f32_16x16x32_bf16 v[56:59],a[200:203],a[68:71],v[56:59]
s_waitcnt lgkmcnt(6)
ds_read_u16_d16_hi v27,v27
v_lshl_add_u64 v[38:39],v[52:53],0,v[54:55]
v_cvt_pk_bf16_f32 v142,v68,v69
v_lshlrev_b32_sdwa v48,1,v48 dst_sel:DWORD dst_unused:UNUSED_PAD src0_sel:DWORD src1_sel:WORD_1
v_cvt_pk_bf16_f32 v143,v70,v71
v_mfma_f32_16x16x32_bf16 v[56:59],a[196:199],a[84:87],v[56:59]
ds_read_u16 v48,v48
v_mul_f32_dpp v148,v14,v148 row_newbcast:8 row_mask:0xf bank_mask:0xf
v_lshlrev_b32_sdwa v50,1,v50 dst_sel:DWORD dst_unused:UNUSED_PAD src0_sel:DWORD src1_sel:WORD_1
v_mfma_f32_16x16x32_bf16 v[52:55],a[192:195],a[84:87],v[16:19]
ds_read_u16_d16_hi v50,v50
v_mul_f32_dpp v149,v14,v149 row_newbcast:9 row_mask:0xf bank_mask:0xf
v_mul_f32_dpp v150,v14,v150 row_newbcast:10 row_mask:0xf bank_mask:0xf
v_mfma_f32_16x16x32_bf16 v[52:55],a[216:219],a[68:71],v[52:55]
ds_read_b64_tr_b16 a[232:233],v3 offset:32832
v_mul_f32_dpp v151,v14,v151 row_newbcast:11 row_mask:0xf bank_mask:0xf
v_lshlrev_b32_sdwa v49,1,v49 dst_sel:DWORD dst_unused:UNUSED_PAD src0_sel:DWORD src1_sel:WORD_1
v_mfma_f32_16x16x32_bf16 v[52:55],a[220:223],a[52:55],v[52:55]
ds_read_u16 v49,v49
v_mul_f32_dpp v84,v14,v84 row_newbcast:12 row_mask:0xf bank_mask:0xf
v_mul_f32_dpp v85,v14,v85 row_newbcast:13 row_mask:0xf bank_mask:0xf
v_mfma_f32_16x16x32_bf16 v[232:235],a[224:227],v[40:43],v[232:235]
ds_read_b64_tr_b16 a[234:235],v3 offset:34368
v_mul_f32_dpp v86,v14,v86 row_newbcast:14 row_mask:0xf bank_mask:0xf
v_lshlrev_b32_sdwa v51,1,v51 dst_sel:DWORD dst_unused:UNUSED_PAD src0_sel:DWORD src1_sel:WORD_1
v_mfma_f32_16x16x32_bf16 v[236:239],a[224:227],v[36:39],v[236:239]
ds_read_u16_d16_hi v51,v51
v_mul_f32_dpp v87,v14,v87 row_newbcast:15 row_mask:0xf bank_mask:0xf
v_lshlrev_b32_sdwa v44,1,v44 dst_sel:DWORD dst_unused:UNUSED_PAD src0_sel:DWORD src1_sel:WORD_1
v_cvt_pk_bf16_f32 v148,v148,v149
v_mfma_f32_16x16x32_bf16 v[164:167],a[248:251],v[36:39],v[164:167]
ds_read_u16 v44,v44
v_cvt_pk_bf16_f32 v149,v150,v151
v_mfma_f32_16x16x32_bf16 v[160:163],a[248:251],v[40:43],v[160:163]
ds_read_b64_tr_b16 a[240:241],v3 offset:32840
v_cvt_pk_bf16_f32 v150,v84,v85
v_lshlrev_b32_sdwa v46,1,v46 dst_sel:DWORD dst_unused:UNUSED_PAD src0_sel:DWORD src1_sel:WORD_1
v_cvt_pk_bf16_f32 v151,v86,v87
v_mfma_f32_16x16x32_bf16 v[240:243],a[228:231],v[40:43],v[240:243]
ds_read_u16_d16_hi v46,v46
v_mul_f32_dpp v156,v15,v156 row_newbcast:0 row_mask:0xf bank_mask:0xf
v_lshlrev_b32_sdwa v45,1,v45 dst_sel:DWORD dst_unused:UNUSED_PAD src0_sel:DWORD src1_sel:WORD_1
v_mfma_f32_16x16x32_bf16 v[244:247],a[228:231],v[36:39],v[244:247]
buffer_store_dwordx4 v[140:143],v7,s[40:43],s8 offen offset:192 sc1
v_mul_f32_dpp v157,v15,v157 row_newbcast:1 row_mask:0xf bank_mask:0xf
v_mul_f32_dpp v158,v15,v158 row_newbcast:2 row_mask:0xf bank_mask:0xf
v_mfma_f32_16x16x32_bf16 v[180:183],a[236:239],v[36:39],v[180:183]
ds_read_u16 v45,v45
v_mul_f32_dpp v159,v15,v159 row_newbcast:3 row_mask:0xf bank_mask:0xf
v_lshlrev_b32_sdwa v47,1,v47 dst_sel:DWORD dst_unused:UNUSED_PAD src0_sel:DWORD src1_sel:WORD_1
v_mfma_f32_16x16x32_bf16 v[176:179],a[236:239],v[40:43],v[176:179]
ds_read_u16_d16_hi v47,v47
v_mul_f32_dpp v100,v15,v100 row_newbcast:4 row_mask:0xf bank_mask:0xf
v_mul_f32_dpp v101,v15,v101 row_newbcast:5 row_mask:0xf bank_mask:0xf
v_mfma_f32_16x16x32_bf16 v[248:251],a[244:247],v[40:43],v[248:251]
ds_read_b64_tr_b16 a[242:243],v3 offset:34376
v_mul_f32_dpp v102,v15,v102 row_newbcast:6 row_mask:0xf bank_mask:0xf
v_lshlrev_b32_sdwa v60,1,v60 dst_sel:DWORD dst_unused:UNUSED_PAD src0_sel:DWORD src1_sel:WORD_1
v_mfma_f32_16x16x32_bf16 v[252:255],a[244:247],v[36:39],v[252:255]
ds_read_u16 v60,v60
v_mul_f32_dpp v103,v15,v103 row_newbcast:7 row_mask:0xf bank_mask:0xf
v_lshlrev_b32_sdwa v62,1,v62 dst_sel:DWORD dst_unused:UNUSED_PAD src0_sel:DWORD src1_sel:WORD_1
v_cvt_pk_bf16_f32 v156,v156,v157
v_mfma_f32_16x16x32_bf16 v[196:199],a[252:255],v[36:39],v[196:199]
ds_read_u16_d16_hi v62,v62
v_cvt_pk_bf16_f32 v157,v158,v159
v_mfma_f32_16x16x32_bf16 v[192:195],a[252:255],v[40:43],v[192:195]
v_cvt_pk_bf16_f32 v158,v100,v101
v_lshlrev_b32_sdwa v61,1,v61 dst_sel:DWORD dst_unused:UNUSED_PAD src0_sel:DWORD src1_sel:WORD_1
v_cvt_pk_bf16_f32 v159,v102,v103
v_lshlrev_b32_sdwa v63,1,v63 dst_sel:DWORD dst_unused:UNUSED_PAD src0_sel:DWORD src1_sel:WORD_1
v_mfma_f32_16x16x32_bf16 v[36:39],a[220:223],a[56:59],v[16:19]
s_nop 0
ds_read_u16 v61,v61
ds_read_u16_d16_hi v63,v63
v_mul_f32_dpp v112,v14,v112 row_newbcast:0 row_mask:0xf bank_mask:0xf
s_add_u32 s9,s61,3244032
v_lshlrev_b32_sdwa v32,1,v32 dst_sel:DWORD dst_unused:UNUSED_PAD src0_sel:DWORD src1_sel:WORD_1
v_mfma_f32_16x16x32_bf16 v[36:39],a[216:219],a[72:75],v[36:39]
s_waitcnt lgkmcnt(7)
ds_read_b128 v[64:67],v4 offset:128
ds_read_b128 v[68:71],v4 offset:160
v_lshl_add_u64 v[28:29],v[28:29],0,v[30:31]
v_mul_f32_dpp v113,v14,v113 row_newbcast:1 row_mask:0xf bank_mask:0xf
v_mul_f32_dpp v114,v14,v114 row_newbcast:2 row_mask:0xf bank_mask:0xf
v_mfma_f32_16x16x32_bf16 v[36:39],a[192:195],a[88:91],v[36:39]
ds_read_u16 v32,v32
ds_read_b128 v[80:83],v4 offset:256
v_lshl_add_u64 v[30:31],v[24:25],0,v[26:27]
v_mul_f32_dpp v115,v14,v115 row_newbcast:3 row_mask:0xf bank_mask:0xf
v_lshlrev_b32_sdwa v34,1,v34 dst_sel:DWORD dst_unused:UNUSED_PAD src0_sel:DWORD src1_sel:WORD_1
v_mfma_f32_16x16x32_bf16 v[40:43],a[196:199],a[88:91],v[20:23]
buffer_store_dwordx4 v[148:151],v7,s[40:43],s8 offen offset:256 sc1
ds_read_u16_d16_hi v34,v34
v_mul_f32_dpp v72,v14,v72 row_newbcast:4 row_mask:0xf bank_mask:0xf
v_mul_f32_dpp v73,v14,v73 row_newbcast:5 row_mask:0xf bank_mask:0xf
v_mfma_f32_16x16x32_bf16 v[40:43],a[200:203],a[72:75],v[40:43]
ds_read_b128 v[84:87],v4 offset:288
v_lshl_add_u64 v[24:25],v[48:49],0,v[50:51]
v_mul_f32_dpp v74,v14,v74 row_newbcast:6 row_mask:0xf bank_mask:0xf
v_lshlrev_b32_sdwa v33,1,v33 dst_sel:DWORD dst_unused:UNUSED_PAD src0_sel:DWORD src1_sel:WORD_1
v_mfma_f32_16x16x32_bf16 v[40:43],a[212:215],a[56:59],v[40:43]
ds_read_u16 v33,v33
v_mul_f32_dpp v75,v14,v75 row_newbcast:7 row_mask:0xf bank_mask:0xf
v_lshlrev_b32_sdwa v35,1,v35 dst_sel:DWORD dst_unused:UNUSED_PAD src0_sel:DWORD src1_sel:WORD_1
v_cvt_pk_bf16_f32 v112,v112,v113
v_mfma_f32_16x16x32_bf16 v[48:51],a[212:215],a[60:63],v[20:23]
s_nop 0
ds_read_b64_tr_b16 a[212:213],v3 offset:51312
ds_read_b64_tr_b16 a[214:215],v3 offset:52848
v_cvt_pk_bf16_f32 v113,v114,v115
v_mfma_f32_16x16x32_bf16 v[48:51],a[200:203],a[76:79],v[48:51]
s_waitcnt lgkmcnt(11)
ds_read_b64_tr_b16 a[200:201],v3 offset:57472
ds_read_b64_tr_b16 a[202:203],v3 offset:59008
v_lshl_add_u64 v[26:27],v[44:45],0,v[46:47]
v_cvt_pk_bf16_f32 v114,v72,v73
v_lshlrev_b32_sdwa v52,1,v52 dst_sel:DWORD dst_unused:UNUSED_PAD src0_sel:DWORD src1_sel:WORD_1
v_cvt_pk_bf16_f32 v115,v74,v75
v_mfma_f32_16x16x32_bf16 v[48:51],a[196:199],a[92:95],v[48:51]
ds_read_u16_d16_hi v35,v35
ds_read_u16 v52,v52
v_mul_f32_dpp v120,v14,v120 row_newbcast:8 row_mask:0xf bank_mask:0xf
v_lshlrev_b32_sdwa v54,1,v54 dst_sel:DWORD dst_unused:UNUSED_PAD src0_sel:DWORD src1_sel:WORD_1
v_mfma_f32_16x16x32_bf16 v[44:47],a[192:195],a[92:95],v[16:19]
ds_read_u16_d16_hi v54,v54
ds_read_b64_tr_b16 a[196:197],v3 offset:57480
v_mul_f32_dpp v121,v14,v121 row_newbcast:9 row_mask:0xf bank_mask:0xf
v_mul_f32_dpp v122,v14,v122 row_newbcast:10 row_mask:0xf bank_mask:0xf
v_mfma_f32_16x16x32_bf16 v[44:47],a[216:219],a[76:79],v[44:47]
ds_read_b64_tr_b16 a[198:199],v3 offset:59016
ds_read_b64_tr_b16 a[192:193],v3 offset:51320
v_mul_f32_dpp v123,v14,v123 row_newbcast:11 row_mask:0xf bank_mask:0xf
v_lshlrev_b32_sdwa v53,1,v53 dst_sel:DWORD dst_unused:UNUSED_PAD src0_sel:DWORD src1_sel:WORD_1
v_mfma_f32_16x16x32_bf16 v[44:47],a[220:223],a[60:63],v[44:47]
TP10171_%=:
s_cbranch_execz TR1_%=
ds_read_u16 v53,v53
ds_read_b64_tr_b16 a[194:195],v3 offset:52856
v_mul_f32_dpp v88,v14,v88 row_newbcast:12 row_mask:0xf bank_mask:0xf
v_mul_f32_dpp v89,v14,v89 row_newbcast:13 row_mask:0xf bank_mask:0xf
v_mfma_f32_16x16x32_bf16 v[208:211],a[224:227],v[28:31],v[208:211]
s_nop 0
ds_read_b128 v[96:99],v4 offset:384
ds_read_b128 v[100:103],v4 offset:416
v_mul_f32_dpp v90,v14,v90 row_newbcast:14 row_mask:0xf bank_mask:0xf
v_lshlrev_b32_sdwa v55,1,v55 dst_sel:DWORD dst_unused:UNUSED_PAD src0_sel:DWORD src1_sel:WORD_1
v_mfma_f32_16x16x32_bf16 v[212:215],a[224:227],v[24:27],v[212:215]
buffer_store_dwordx4 v[156:159],v7,s[40:43],s8 offen offset:320 sc1
ds_read_u16_d16_hi v55,v55
v_mul_f32_dpp v91,v14,v91 row_newbcast:15 row_mask:0xf bank_mask:0xf
v_lshlrev_b32_sdwa v56,1,v56 dst_sel:DWORD dst_unused:UNUSED_PAD src0_sel:DWORD src1_sel:WORD_1
v_cvt_pk_bf16_f32 v120,v120,v121
v_mfma_f32_16x16x32_bf16 v[172:175],a[248:251],v[24:27],v[172:175]
ds_read_u16 v56,v56
ds_read_b64_tr_b16 a[224:225],v3 offset:32896
v_cvt_pk_bf16_f32 v121,v122,v123
v_mfma_f32_16x16x32_bf16 v[168:171],a[248:251],v[28:31],v[168:171]
ds_read_b64_tr_b16 a[226:227],v3 offset:34432
ds_read_b64_tr_b16 a[248:249],v3 offset:32904
v_cvt_pk_bf16_f32 v122,v88,v89
v_lshlrev_b32_sdwa v58,1,v58 dst_sel:DWORD dst_unused:UNUSED_PAD src0_sel:DWORD src1_sel:WORD_1
v_cvt_pk_bf16_f32 v123,v90,v91
v_mfma_f32_16x16x32_bf16 v[216:219],a[228:231],v[28:31],v[216:219]
ds_read_u16_d16_hi v58,v58
ds_read_b64_tr_b16 a[250:251],v3 offset:34440
v_mul_f32_dpp v128,v15,v128 row_newbcast:0 row_mask:0xf bank_mask:0xf
v_lshlrev_b32_sdwa v57,1,v57 dst_sel:DWORD dst_unused:UNUSED_PAD src0_sel:DWORD src1_sel:WORD_1
v_mfma_f32_16x16x32_bf16 v[220:223],a[228:231],v[24:27],v[220:223]
ds_read_u16 v57,v57
ds_read_b64_tr_b16 a[228:229],v3 offset:32960
v_mul_f32_dpp v129,v15,v129 row_newbcast:1 row_mask:0xf bank_mask:0xf
v_mul_f32_dpp v130,v15,v130 row_newbcast:2 row_mask:0xf bank_mask:0xf
v_mfma_f32_16x16x32_bf16 v[188:191],a[236:239],v[24:27],v[188:191]
ds_read_b64_tr_b16 a[230:231],v3 offset:34496
ds_read_b64_tr_b16 a[216:217],v3 offset:51376
v_mul_f32_dpp v131,v15,v131 row_newbcast:3 row_mask:0xf bank_mask:0xf
v_lshlrev_b32_sdwa v59,1,v59 dst_sel:DWORD dst_unused:UNUSED_PAD src0_sel:DWORD src1_sel:WORD_1
v_mfma_f32_16x16x32_bf16 v[184:187],a[236:239],v[28:31],v[184:187]
ds_read_u16_d16_hi v59,v59
ds_read_b64_tr_b16 a[236:237],v3 offset:32968
v_mul_f32_dpp v104,v15,v104 row_newbcast:4 row_mask:0xf bank_mask:0xf
v_mul_f32_dpp v105,v15,v105 row_newbcast:5 row_mask:0xf bank_mask:0xf
v_mfma_f32_16x16x32_bf16 v[224:227],a[244:247],v[28:31],v[224:227]
ds_read_b64_tr_b16 a[238:239],v3 offset:34504
ds_read_b64_tr_b16 a[218:219],v3 offset:52912
v_mul_f32_dpp v106,v15,v106 row_newbcast:6 row_mask:0xf bank_mask:0xf
v_lshlrev_b32_sdwa v36,1,v36 dst_sel:DWORD dst_unused:UNUSED_PAD src0_sel:DWORD src1_sel:WORD_1
v_mfma_f32_16x16x32_bf16 v[228:231],a[244:247],v[24:27],v[228:231]
ds_read_u16 v36,v36
ds_read_b128 v[20:23],v4 offset:512
v_mul_f32_dpp v107,v15,v107 row_newbcast:7 row_mask:0xf bank_mask:0xf
v_lshlrev_b32_sdwa v38,1,v38 dst_sel:DWORD dst_unused:UNUSED_PAD src0_sel:DWORD src1_sel:WORD_1
v_cvt_pk_bf16_f32 v128,v128,v129
v_mfma_f32_16x16x32_bf16 v[204:207],a[252:255],v[24:27],v[204:207]
s_nop 0
buffer_store_dwordx4 v[112:115],v7,s[40:43],s9 offen offset:0 sc1
ds_read_u16_d16_hi v38,v38
v_cvt_pk_bf16_f32 v129,v130,v131
v_mfma_f32_16x16x32_bf16 v[200:203],a[252:255],v[28:31],v[200:203]
s_waitcnt lgkmcnt(15)
ds_read_b128 v[16:19],v4 offset:544
v_cvt_pk_bf16_f32 v130,v104,v105
v_lshlrev_b32_sdwa v37,1,v37 dst_sel:DWORD dst_unused:UNUSED_PAD src0_sel:DWORD src1_sel:WORD_1
v_cvt_pk_bf16_f32 v131,v106,v107
v_lshlrev_b32_sdwa v39,1,v39 dst_sel:DWORD dst_unused:UNUSED_PAD src0_sel:DWORD src1_sel:WORD_1
v_mfma_f32_16x16x32_bf16 v[24:27],a[96:99],a[48:51],v[64:67]
ds_read_u16 v37,v37
ds_read_u16_d16_hi v39,v39
v_mul_f32_dpp v116,v14,v116 row_newbcast:0 row_mask:0xf bank_mask:0xf
v_lshlrev_b32_sdwa v40,1,v40 dst_sel:DWORD dst_unused:UNUSED_PAD src0_sel:DWORD src1_sel:WORD_1
v_mfma_f32_16x16x32_bf16 v[24:27],a[100:103],a[64:67],v[24:27]
ds_read_u16 v40,v40
ds_read_b64_tr_b16 a[220:221],v3 offset:57536
v_lshl_add_u64 v[60:61],v[60:61],0,v[62:63]
v_mul_f32_dpp v117,v14,v117 row_newbcast:1 row_mask:0xf bank_mask:0xf
v_mul_f32_dpp v118,v14,v118 row_newbcast:2 row_mask:0xf bank_mask:0xf
v_mfma_f32_16x16x32_bf16 v[24:27],a[104:107],a[80:83],v[24:27]
ds_read_b64_tr_b16 a[222:223],v3 offset:59072
ds_read_b64_tr_b16 a[204:205],v3 offset:57544
v_lshl_add_u64 v[62:63],v[32:33],0,v[34:35]
v_mul_f32_dpp v119,v14,v119 row_newbcast:3 row_mask:0xf bank_mask:0xf
v_lshlrev_b32_sdwa v42,1,v42 dst_sel:DWORD dst_unused:UNUSED_PAD src0_sel:DWORD src1_sel:WORD_1
v_mfma_f32_16x16x32_bf16 v[28:31],a[108:111],a[80:83],v[68:71]
ds_read_u16_d16_hi v42,v42
v_mul_f32_dpp v76,v14,v76 row_newbcast:4 row_mask:0xf bank_mask:0xf
v_mul_f32_dpp v77,v14,v77 row_newbcast:5 row_mask:0xf bank_mask:0xf
v_mfma_f32_16x16x32_bf16 v[28:31],a[112:115],a[64:67],v[28:31]
ds_read_b64_tr_b16 a[206:207],v3 offset:59080
v_lshl_add_u64 v[32:33],v[52:53],0,v[54:55]
v_mul_f32_dpp v78,v14,v78 row_newbcast:6 row_mask:0xf bank_mask:0xf
v_lshlrev_b32_sdwa v41,1,v41 dst_sel:DWORD dst_unused:UNUSED_PAD src0_sel:DWORD src1_sel:WORD_1
v_mfma_f32_16x16x32_bf16 v[28:31],a[116:119],a[48:51],v[28:31]
ds_read_u16 v41,v41
v_mul_f32_dpp v79,v14,v79 row_newbcast:7 row_mask:0xf bank_mask:0xf
v_lshlrev_b32_sdwa v43,1,v43 dst_sel:DWORD dst_unused:UNUSED_PAD src0_sel:DWORD src1_sel:WORD_1
v_cvt_pk_bf16_f32 v116,v116,v117
v_mfma_f32_16x16x32_bf16 v[52:55],a[120:123],a[48:51],v[84:87]
s_nop 0
ds_read_u16_d16_hi v43,v43
v_cvt_pk_bf16_f32 v117,v118,v119
v_mfma_f32_16x16x32_bf16 v[52:55],a[124:127],a[64:67],v[52:55]
s_waitcnt lgkmcnt(11)
ds_read_b64_tr_b16 a[208:209],v3 offset:51384
v_lshl_add_u64 v[34:35],v[56:57],0,v[58:59]
v_cvt_pk_bf16_f32 v118,v76,v77
v_lshlrev_b32_sdwa v44,1,v44 dst_sel:DWORD dst_unused:UNUSED_PAD src0_sel:DWORD src1_sel:WORD_1
v_cvt_pk_bf16_f32 v119,v78,v79
v_mfma_f32_16x16x32_bf16 v[52:55],a[128:131],a[80:83],v[52:55]
buffer_store_dwordx4 v[120:123],v7,s[40:43],s9 offen offset:64 sc1
ds_read_u16 v44,v44
v_mul_f32_dpp v124,v14,v124 row_newbcast:8 row_mask:0xf bank_mask:0xf
v_lshlrev_b32_sdwa v46,1,v46 dst_sel:DWORD dst_unused:UNUSED_PAD src0_sel:DWORD src1_sel:WORD_1
v_mfma_f32_16x16x32_bf16 v[56:59],a[132:135],a[80:83],v[80:83]
ds_read_u16_d16_hi v46,v46
v_mul_f32_dpp v125,v14,v125 row_newbcast:9 row_mask:0xf bank_mask:0xf
v_mul_f32_dpp v126,v14,v126 row_newbcast:10 row_mask:0xf bank_mask:0xf
v_mfma_f32_16x16x32_bf16 v[56:59],a[136:139],a[64:67],v[56:59]
ds_read_b64_tr_b16 a[210:211],v3 offset:52920
v_mul_f32_dpp v127,v14,v127 row_newbcast:11 row_mask:0xf bank_mask:0xf
v_lshlrev_b32_sdwa v45,1,v45 dst_sel:DWORD dst_unused:UNUSED_PAD src0_sel:DWORD src1_sel:WORD_1
v_mfma_f32_16x16x32_bf16 v[56:59],a[140:143],a[48:51],v[56:59]
ds_read_u16 v45,v45
v_mul_f32_dpp v92,v14,v92 row_newbcast:12 row_mask:0xf bank_mask:0xf
v_mul_f32_dpp v93,v14,v93 row_newbcast:13 row_mask:0xf bank_mask:0xf
v_mfma_f32_16x16x32_bf16 v[232:235],a[232:235],v[60:63],v[232:235]
ds_read_b64_tr_b16 a[244:245],v3 offset:51440
v_mul_f32_dpp v94,v14,v94 row_newbcast:14 row_mask:0xf bank_mask:0xf
v_lshlrev_b32_sdwa v47,1,v47 dst_sel:DWORD dst_unused:UNUSED_PAD src0_sel:DWORD src1_sel:WORD_1
v_mfma_f32_16x16x32_bf16 v[236:239],a[232:235],v[32:35],v[236:239]
ds_read_u16_d16_hi v47,v47
v_mul_f32_dpp v95,v14,v95 row_newbcast:15 row_mask:0xf bank_mask:0xf
v_lshlrev_b32_sdwa v48,1,v48 dst_sel:DWORD dst_unused:UNUSED_PAD src0_sel:DWORD src1_sel:WORD_1
v_cvt_pk_bf16_f32 v124,v124,v125
v_mfma_f32_16x16x32_bf16 v[164:167],a[240:243],v[32:35],v[164:167]
ds_read_u16 v48,v48
v_cvt_pk_bf16_f32 v125,v126,v127
v_mfma_f32_16x16x32_bf16 v[160:163],a[240:243],v[60:63],v[160:163]
ds_read_b64_tr_b16 a[246:247],v3 offset:52976
v_cvt_pk_bf16_f32 v126,v92,v93
v_lshlrev_b32_sdwa v50,1,v50 dst_sel:DWORD dst_unused:UNUSED_PAD src0_sel:DWORD src1_sel:WORD_1
v_cvt_pk_bf16_f32 v127,v94,v95
v_mfma_f32_16x16x32_bf16 v[240:243],a[224:227],v[60:63],v[240:243]
ds_read_u16_d16_hi v50,v50
v_mul_f32_dpp v132,v15,v132 row_newbcast:0 row_mask:0xf bank_mask:0xf
v_lshlrev_b32_sdwa v49,1,v49 dst_sel:DWORD dst_unused:UNUSED_PAD src0_sel:DWORD src1_sel:WORD_1
v_mfma_f32_16x16x32_bf16 v[244:247],a[224:227],v[32:35],v[244:247]
ds_read_u16 v49,v49
v_mul_f32_dpp v133,v15,v133 row_newbcast:1 row_mask:0xf bank_mask:0xf
v_mul_f32_dpp v134,v15,v134 row_newbcast:2 row_mask:0xf bank_mask:0xf
v_mfma_f32_16x16x32_bf16 v[180:183],a[248:251],v[32:35],v[180:183]
ds_read_b64_tr_b16 a[252:253],v3 offset:57600
v_mul_f32_dpp v135,v15,v135 row_newbcast:3 row_mask:0xf bank_mask:0xf
v_lshlrev_b32_sdwa v51,1,v51 dst_sel:DWORD dst_unused:UNUSED_PAD src0_sel:DWORD src1_sel:WORD_1
v_mfma_f32_16x16x32_bf16 v[176:179],a[248:251],v[60:63],v[176:179]
buffer_store_dwordx4 v[128:131],v7,s[40:43],s9 offen offset:128 sc1
v_mul_f32_dpp v108,v15,v108 row_newbcast:4 row_mask:0xf bank_mask:0xf
v_mul_f32_dpp v109,v15,v109 row_newbcast:5 row_mask:0xf bank_mask:0xf
v_mfma_f32_16x16x32_bf16 v[248:251],a[228:231],v[60:63],v[248:251]
ds_read_u16_d16_hi v51,v51
v_mul_f32_dpp v110,v15,v110 row_newbcast:6 row_mask:0xf bank_mask:0xf
v_lshlrev_b32_sdwa v24,1,v24 dst_sel:DWORD dst_unused:UNUSED_PAD src0_sel:DWORD src1_sel:WORD_1
v_mfma_f32_16x16x32_bf16 v[252:255],a[228:231],v[32:35],v[252:255]
ds_read_u16 v24,v24
v_mul_f32_dpp v111,v15,v111 row_newbcast:7 row_mask:0xf bank_mask:0xf
v_lshlrev_b32_sdwa v26,1,v26 dst_sel:DWORD dst_unused:UNUSED_PAD src0_sel:DWORD src1_sel:WORD_1
v_cvt_pk_bf16_f32 v132,v132,v133
v_mfma_f32_16x16x32_bf16 v[196:199],a[236:239],v[32:35],v[196:199]
ds_read_u16_d16_hi v26,v26
v_cvt_pk_bf16_f32 v133,v134,v135
v_mfma_f32_16x16x32_bf16 v[192:195],a[236:239],v[60:63],v[192:195]
ds_read_b64_tr_b16 a[254:255],v3 offset:59136
v_cvt_pk_bf16_f32 v134,v108,v109
v_lshlrev_b32_sdwa v25,1,v25 dst_sel:DWORD dst_unused:UNUSED_PAD src0_sel:DWORD src1_sel:WORD_1
v_cvt_pk_bf16_f32 v135,v110,v111
v_lshlrev_b32_sdwa v27,1,v27 dst_sel:DWORD dst_unused:UNUSED_PAD src0_sel:DWORD src1_sel:WORD_1
v_mfma_f32_16x16x32_bf16 v[32:35],a[144:147],a[48:51],v[96:99]
ds_read_u16 v25,v25
v_lshlrev_b32_sdwa v28,1,v28 dst_sel:DWORD dst_unused:UNUSED_PAD src0_sel:DWORD src1_sel:WORD_1
v_mfma_f32_16x16x32_bf16 v[32:35],a[148:151],a[64:67],v[32:35]
s_waitcnt lgkmcnt(7)
ds_read_u16_d16_hi v27,v27
v_lshl_add_u64 v[36:37],v[36:37],0,v[38:39]
v_mfma_f32_16x16x32_bf16 v[32:35],a[152:155],a[80:83],v[32:35]
ds_read_u16 v28,v28
v_lshl_add_u64 v[38:39],v[40:41],0,v[42:43]
v_lshlrev_b32_sdwa v30,1,v30 dst_sel:DWORD dst_unused:UNUSED_PAD src0_sel:DWORD src1_sel:WORD_1
v_mfma_f32_16x16x32_bf16 v[60:63],a[156:159],a[80:83],v[100:103]
ds_read_u16_d16_hi v30,v30
v_lshlrev_b32_sdwa v29,1,v29 dst_sel:DWORD dst_unused:UNUSED_PAD src0_sel:DWORD src1_sel:WORD_1
v_mfma_f32_16x16x32_bf16 v[60:63],a[160:163],a[64:67],v[60:63]
ds_read_u16 v29,v29
v_lshl_add_u64 v[40:41],v[44:45],0,v[46:47]
v_mfma_f32_16x16x32_bf16 v[60:63],a[164:167],a[48:51],v[60:63]
v_lshlrev_b32_sdwa v31,1,v31 dst_sel:DWORD dst_unused:UNUSED_PAD src0_sel:DWORD src1_sel:WORD_1
v_lshlrev_b32_sdwa v56,1,v56 dst_sel:DWORD dst_unused:UNUSED_PAD src0_sel:DWORD src1_sel:WORD_1
v_mfma_f32_16x16x32_bf16 v[44:47],a[168:171],a[48:51],v[16:19]
ds_read_u16_d16_hi v31,v31
v_lshlrev_b32_sdwa v58,1,v58 dst_sel:DWORD dst_unused:UNUSED_PAD src0_sel:DWORD src1_sel:WORD_1
v_mfma_f32_16x16x32_bf16 v[44:47],a[172:175],a[64:67],v[44:47]
s_waitcnt lgkmcnt(7)
ds_read_u16 v56,v56
v_lshl_add_u64 v[42:43],v[48:49],0,v[50:51]
v_mfma_f32_16x16x32_bf16 v[44:47],a[176:179],a[80:83],v[44:47]
buffer_store_dwordx4 v[116:119],v7,s[40:43],s9 offen offset:192 sc1
v_lshlrev_b32_sdwa v57,1,v57 dst_sel:DWORD dst_unused:UNUSED_PAD src0_sel:DWORD src1_sel:WORD_1
v_mfma_f32_16x16x32_bf16 v[48:51],a[180:183],a[80:83],v[20:23]
ds_read_u16_d16_hi v58,v58
v_lshlrev_b32_sdwa v59,1,v59 dst_sel:DWORD dst_unused:UNUSED_PAD src0_sel:DWORD src1_sel:WORD_1
v_mfma_f32_16x16x32_bf16 v[48:51],a[184:187],a[64:67],v[48:51]
ds_read_u16 v57,v57
v_mfma_f32_16x16x32_bf16 v[48:51],a[188:191],a[48:51],v[48:51]
ds_read_u16_d16_hi v59,v59
v_lshlrev_b32_sdwa v52,1,v52 dst_sel:DWORD dst_unused:UNUSED_PAD src0_sel:DWORD src1_sel:WORD_1
v_mfma_f32_16x16x32_bf16 v[208:211],a[232:235],v[36:39],v[208:211]
ds_read_u16 v52,v52
v_lshlrev_b32_sdwa v54,1,v54 dst_sel:DWORD dst_unused:UNUSED_PAD src0_sel:DWORD src1_sel:WORD_1
v_mfma_f32_16x16x32_bf16 v[212:215],a[232:235],v[40:43],v[212:215]
ds_read_u16_d16_hi v54,v54
v_mfma_f32_16x16x32_bf16 v[172:175],a[240:243],v[40:43],v[172:175]
ds_read_b64_tr_b16 a[232:233],v3 offset:57608
v_lshlrev_b32_sdwa v53,1,v53 dst_sel:DWORD dst_unused:UNUSED_PAD src0_sel:DWORD src1_sel:WORD_1
v_mfma_f32_16x16x32_bf16 v[168:171],a[240:243],v[36:39],v[168:171]
ds_read_u16 v53,v53
v_mfma_f32_16x16x32_bf16 v[216:219],a[224:227],v[36:39],v[216:219]
ds_read_b64_tr_b16 a[234:235],v3 offset:59144
v_lshlrev_b32_sdwa v55,1,v55 dst_sel:DWORD dst_unused:UNUSED_PAD src0_sel:DWORD src1_sel:WORD_1
v_mfma_f32_16x16x32_bf16 v[220:223],a[224:227],v[40:43],v[220:223]
ds_read_u16_d16_hi v55,v55
v_lshlrev_b32_sdwa v32,1,v32 dst_sel:DWORD dst_unused:UNUSED_PAD src0_sel:DWORD src1_sel:WORD_1
v_mfma_f32_16x16x32_bf16 v[188:191],a[248:251],v[40:43],v[188:191]
ds_read_u16 v32,v32
v_mfma_f32_16x16x32_bf16 v[184:187],a[248:251],v[36:39],v[184:187]
ds_read_b64_tr_b16 a[240:241],v3 offset:51448
v_lshlrev_b32_sdwa v34,1,v34 dst_sel:DWORD dst_unused:UNUSED_PAD src0_sel:DWORD src1_sel:WORD_1
v_mfma_f32_16x16x32_bf16 v[224:227],a[228:231],v[36:39],v[224:227]
ds_read_u16_d16_hi v34,v34
v_lshlrev_b32_sdwa v33,1,v33 dst_sel:DWORD dst_unused:UNUSED_PAD src0_sel:DWORD src1_sel:WORD_1
v_mfma_f32_16x16x32_bf16 v[228:231],a[228:231],v[40:43],v[228:231]
ds_read_u16 v33,v33
v_mfma_f32_16x16x32_bf16 v[204:207],a[236:239],v[40:43],v[204:207]
ds_read_b64_tr_b16 a[242:243],v3 offset:52984
v_lshlrev_b32_sdwa v35,1,v35 dst_sel:DWORD dst_unused:UNUSED_PAD src0_sel:DWORD src1_sel:WORD_1
v_mfma_f32_16x16x32_bf16 v[200:203],a[236:239],v[36:39],v[200:203]
buffer_store_dwordx4 v[124:127],v7,s[40:43],s9 offen offset:256 sc1
v_mfma_f32_16x16x32_bf16 v[40:43],a[96:99],a[52:55],v[64:67]
ds_read_u16_d16_hi v35,v35
v_lshlrev_b32_sdwa v60,1,v60 dst_sel:DWORD dst_unused:UNUSED_PAD src0_sel:DWORD src1_sel:WORD_1
v_mfma_f32_16x16x32_bf16 v[40:43],a[100:103],a[68:71],v[40:43]
s_waitcnt lgkmcnt(7)
ds_read_u16 v60,v60
v_lshl_add_u64 v[24:25],v[24:25],0,v[26:27]
v_mfma_f32_16x16x32_bf16 v[40:43],a[104:107],a[84:87],v[40:43]
v_lshl_add_u64 v[26:27],v[28:29],0,v[30:31]
v_lshlrev_b32_sdwa v62,1,v62 dst_sel:DWORD dst_unused:UNUSED_PAD src0_sel:DWORD src1_sel:WORD_1
v_mfma_f32_16x16x32_bf16 v[36:39],a[108:111],a[84:87],v[68:71]
ds_read_u16_d16_hi v62,v62
v_lshlrev_b32_sdwa v61,1,v61 dst_sel:DWORD dst_unused:UNUSED_PAD src0_sel:DWORD src1_sel:WORD_1
v_mfma_f32_16x16x32_bf16 v[36:39],a[112:115],a[68:71],v[36:39]
ds_read_u16 v61,v61
v_lshl_add_u64 v[28:29],v[56:57],0,v[58:59]
v_mfma_f32_16x16x32_bf16 v[36:39],a[116:119],a[52:55],v[36:39]
v_lshlrev_b32_sdwa v63,1,v63 dst_sel:DWORD dst_unused:UNUSED_PAD src0_sel:DWORD src1_sel:WORD_1
v_lshlrev_b32_sdwa v48,1,v48 dst_sel:DWORD dst_unused:UNUSED_PAD src0_sel:DWORD src1_sel:WORD_1
v_mfma_f32_16x16x32_bf16 v[56:59],a[120:123],a[52:55],v[84:87]
ds_read_u16_d16_hi v63,v63
v_lshlrev_b32_sdwa v50,1,v50 dst_sel:DWORD dst_unused:UNUSED_PAD src0_sel:DWORD src1_sel:WORD_1
v_mfma_f32_16x16x32_bf16 v[56:59],a[124:127],a[68:71],v[56:59]
s_waitcnt lgkmcnt(5)
ds_read_u16 v48,v48
v_lshl_add_u64 v[30:31],v[52:53],0,v[54:55]
v_mfma_f32_16x16x32_bf16 v[56:59],a[128:131],a[84:87],v[56:59]
ds_read_u16_d16_hi v50,v50
v_lshlrev_b32_sdwa v49,1,v49 dst_sel:DWORD dst_unused:UNUSED_PAD src0_sel:DWORD src1_sel:WORD_1
v_mfma_f32_16x16x32_bf16 v[52:55],a[132:135],a[84:87],v[80:83]
ds_read_u16 v49,v49
v_lshlrev_b32_sdwa v51,1,v51 dst_sel:DWORD dst_unused:UNUSED_PAD src0_sel:DWORD src1_sel:WORD_1
v_mfma_f32_16x16x32_bf16 v[52:55],a[136:139],a[68:71],v[52:55]
ds_read_u16_d16_hi v51,v51
v_mfma_f32_16x16x32_bf16 v[52:55],a[140:143],a[52:55],v[52:55]
v_lshlrev_b32_sdwa v44,1,v44 dst_sel:DWORD dst_unused:UNUSED_PAD src0_sel:DWORD src1_sel:WORD_1
v_mfma_f32_16x16x32_bf16 v[232:235],a[0:3],v[24:27],v[232:235]
ds_read_u16 v44,v44
v_lshlrev_b32_sdwa v46,1,v46 dst_sel:DWORD dst_unused:UNUSED_PAD src0_sel:DWORD src1_sel:WORD_1
v_mfma_f32_16x16x32_bf16 v[232:235],a[4:7],v[28:31],v[232:235]
ds_read_u16_d16_hi v46,v46
v_mfma_f32_16x16x32_bf16 v[160:163],a[8:11],v[28:31],v[160:163]
v_lshlrev_b32_sdwa v45,1,v45 dst_sel:DWORD dst_unused:UNUSED_PAD src0_sel:DWORD src1_sel:WORD_1
v_mfma_f32_16x16x32_bf16 v[160:163],a[12:15],v[24:27],v[160:163]
buffer_store_dwordx4 v[132:135],v7,s[40:43],s9 offen offset:320 sc1
v_mfma_f32_16x16x32_bf16 v[240:243],a[16:19],v[24:27],v[240:243]
ds_read_u16 v45,v45
v_lshlrev_b32_sdwa v47,1,v47 dst_sel:DWORD dst_unused:UNUSED_PAD src0_sel:DWORD src1_sel:WORD_1
v_mfma_f32_16x16x32_bf16 v[240:243],a[20:23],v[28:31],v[240:243]
ds_read_u16_d16_hi v47,v47
v_lshlrev_b32_sdwa v40,1,v40 dst_sel:DWORD dst_unused:UNUSED_PAD src0_sel:DWORD src1_sel:WORD_1
v_mfma_f32_16x16x32_bf16 v[176:179],a[24:27],v[28:31],v[176:179]
ds_read_u16 v40,v40
v_mfma_f32_16x16x32_bf16 v[176:179],a[28:31],v[24:27],v[176:179]
v_lshlrev_b32_sdwa v42,1,v42 dst_sel:DWORD dst_unused:UNUSED_PAD src0_sel:DWORD src1_sel:WORD_1
v_mfma_f32_16x16x32_bf16 v[248:251],a[32:35],v[24:27],v[248:251]
ds_read_u16_d16_hi v42,v42
v_lshlrev_b32_sdwa v41,1,v41 dst_sel:DWORD dst_unused:UNUSED_PAD src0_sel:DWORD src1_sel:WORD_1
v_mfma_f32_16x16x32_bf16 v[248:251],a[36:39],v[28:31],v[248:251]
ds_read_u16 v41,v41
v_mfma_f32_16x16x32_bf16 v[192:195],a[40:43],v[28:31],v[192:195]
v_lshlrev_b32_sdwa v43,1,v43 dst_sel:DWORD dst_unused:UNUSED_PAD src0_sel:DWORD src1_sel:WORD_1
v_mfma_f32_16x16x32_bf16 v[192:195],a[44:47],v[24:27],v[192:195]
ds_read_u16_d16_hi v43,v43
v_mfma_f32_16x16x32_bf16 v[28:31],a[144:147],a[52:55],v[96:99]
v_lshlrev_b32_sdwa v36,1,v36 dst_sel:DWORD dst_unused:UNUSED_PAD src0_sel:DWORD src1_sel:WORD_1
v_lshlrev_b32_sdwa v38,1,v38 dst_sel:DWORD dst_unused:UNUSED_PAD src0_sel:DWORD src1_sel:WORD_1
v_mfma_f32_16x16x32_bf16 v[28:31],a[148:151],a[68:71],v[28:31]
s_waitcnt lgkmcnt(5)
ds_read_u16 v36,v36
v_lshl_add_u64 v[32:33],v[32:33],0,v[34:35]
v_mfma_f32_16x16x32_bf16 v[28:31],a[152:155],a[84:87],v[28:31]
ds_read_u16_d16_hi v38,v38
v_lshl_add_u64 v[34:35],v[60:61],0,v[62:63]
v_mfma_f32_16x16x32_bf16 v[24:27],a[156:159],a[84:87],v[100:103]
v_lshl_add_u64 v[60:61],v[48:49],0,v[50:51]
v_lshlrev_b32_sdwa v37,1,v37 dst_sel:DWORD dst_unused:UNUSED_PAD src0_sel:DWORD src1_sel:WORD_1
v_mfma_f32_16x16x32_bf16 v[24:27],a[160:163],a[68:71],v[24:27]
s_waitcnt lgkmcnt(5)
ds_read_u16 v37,v37
v_lshl_add_u64 v[62:63],v[44:45],0,v[46:47]
v_mfma_f32_16x16x32_bf16 v[24:27],a[164:167],a[52:55],v[24:27]
v_lshlrev_b32_sdwa v39,1,v39 dst_sel:DWORD dst_unused:UNUSED_PAD src0_sel:DWORD src1_sel:WORD_1
v_lshlrev_b32_sdwa v52,1,v52 dst_sel:DWORD dst_unused:UNUSED_PAD src0_sel:DWORD src1_sel:WORD_1
v_mfma_f32_16x16x32_bf16 v[232:235],a[212:215],v[32:35],v[232:235]
ds_read_u16_d16_hi v39,v39
v_lshlrev_b32_sdwa v54,1,v54 dst_sel:DWORD dst_unused:UNUSED_PAD src0_sel:DWORD src1_sel:WORD_1
v_mfma_f32_16x16x32_bf16 v[232:235],a[200:203],v[60:63],v[232:235]
ds_read_u16 v52,v52
v_mfma_f32_16x16x32_bf16 v[160:163],a[196:199],v[60:63],v[160:163]
ds_read_u16_d16_hi v54,v54
v_lshlrev_b32_sdwa v53,1,v53 dst_sel:DWORD dst_unused:UNUSED_PAD src0_sel:DWORD src1_sel:WORD_1
v_mfma_f32_16x16x32_bf16 v[160:163],a[192:195],v[32:35],v[160:163]
ds_read_u16 v53,v53
v_lshlrev_b32_sdwa v55,1,v55 dst_sel:DWORD dst_unused:UNUSED_PAD src0_sel:DWORD src1_sel:WORD_1
v_mfma_f32_16x16x32_bf16 v[240:243],a[216:219],v[32:35],v[240:243]
ds_read_u16_d16_hi v55,v55
v_mfma_f32_16x16x32_bf16 v[240:243],a[220:223],v[60:63],v[240:243]
v_mul_f32_dpp v232,v14,v232 row_newbcast:0 row_mask:0xf bank_mask:0xf
s_add_u32 s10,s61,4718592
v_lshlrev_b32_sdwa v56,1,v56 dst_sel:DWORD dst_unused:UNUSED_PAD src0_sel:DWORD src1_sel:WORD_1
v_mfma_f32_16x16x32_bf16 v[176:179],a[204:207],v[60:63],v[176:179]
ds_read_u16 v56,v56
v_mul_f32_dpp v233,v14,v233 row_newbcast:1 row_mask:0xf bank_mask:0xf
v_mfma_f32_16x16x32_bf16 v[176:179],a[208:211],v[32:35],v[176:179]
v_mul_f32_dpp v234,v14,v234 row_newbcast:2 row_mask:0xf bank_mask:0xf
v_lshlrev_b32_sdwa v58,1,v58 dst_sel:DWORD dst_unused:UNUSED_PAD src0_sel:DWORD src1_sel:WORD_1
v_mfma_f32_16x16x32_bf16 v[248:251],a[244:247],v[32:35],v[248:251]
ds_read_u16_d16_hi v58,v58
v_mul_f32_dpp v235,v14,v235 row_newbcast:3 row_mask:0xf bank_mask:0xf
v_mfma_f32_16x16x32_bf16 v[248:251],a[252:255],v[60:63],v[248:251]
v_mul_f32_dpp v160,v14,v160 row_newbcast:4 row_mask:0xf bank_mask:0xf
v_lshlrev_b32_sdwa v57,1,v57 dst_sel:DWORD dst_unused:UNUSED_PAD src0_sel:DWORD src1_sel:WORD_1
v_mfma_f32_16x16x32_bf16 v[192:195],a[232:235],v[60:63],v[192:195]
ds_read_u16 v57,v57
v_mul_f32_dpp v161,v14,v161 row_newbcast:5 row_mask:0xf bank_mask:0xf
v_mfma_f32_16x16x32_bf16 v[192:195],a[240:243],v[32:35],v[192:195]
v_mul_f32_dpp v162,v14,v162 row_newbcast:6 row_mask:0xf bank_mask:0xf
v_lshlrev_b32_sdwa v59,1,v59 dst_sel:DWORD dst_unused:UNUSED_PAD src0_sel:DWORD src1_sel:WORD_1
v_mfma_f32_16x16x32_bf16 v[48:51],a[168:171],a[52:55],v[16:19]
ds_read_u16_d16_hi v59,v59
v_mul_f32_dpp v163,v14,v163 row_newbcast:7 row_mask:0xf bank_mask:0xf
v_mfma_f32_16x16x32_bf16 v[48:51],a[172:175],a[68:71],v[48:51]
v_cvt_pk_bf16_f32 v232,v232,v233
v_lshlrev_b32_sdwa v28,1,v28 dst_sel:DWORD dst_unused:UNUSED_PAD src0_sel:DWORD src1_sel:WORD_1
v_mfma_f32_16x16x32_bf16 v[48:51],a[176:179],a[84:87],v[48:51]
ds_read_u16 v28,v28
v_cvt_pk_bf16_f32 v233,v234,v235
v_mfma_f32_16x16x32_bf16 v[44:47],a[180:183],a[84:87],v[20:23]
v_cvt_pk_bf16_f32 v234,v160,v161
v_lshlrev_b32_sdwa v30,1,v30 dst_sel:DWORD dst_unused:UNUSED_PAD src0_sel:DWORD src1_sel:WORD_1
v_mfma_f32_16x16x32_bf16 v[44:47],a[184:187],a[68:71],v[44:47]
ds_read_u16_d16_hi v30,v30
v_cvt_pk_bf16_f32 v235,v162,v163
v_mfma_f32_16x16x32_bf16 v[44:47],a[188:191],a[52:55],v[44:47]
buffer_store_dwordx4 v[232:235],v7,s[40:43],s10 offen offset:0 sc1
v_mul_f32_dpp v240,v14,v240 row_newbcast:8 row_mask:0xf bank_mask:0xf
v_lshlrev_b32_sdwa v29,1,v29 dst_sel:DWORD dst_unused:UNUSED_PAD src0_sel:DWORD src1_sel:WORD_1
v_lshlrev_b32_sdwa v31,1,v31 dst_sel:DWORD dst_unused:UNUSED_PAD src0_sel:DWORD src1_sel:WORD_1
v_mfma_f32_16x16x32_bf16 v[60:63],a[96:99],a[56:59],v[64:67]
s_nop 0
ds_read_u16 v29,v29
v_lshlrev_b32_sdwa v24,1,v24 dst_sel:DWORD dst_unused:UNUSED_PAD src0_sel:DWORD src1_sel:WORD_1
v_mfma_f32_16x16x32_bf16 v[60:63],a[100:103],a[72:75],v[60:63]
s_waitcnt lgkmcnt(4)
ds_read_u16_d16_hi v31,v31
v_lshl_add_u64 v[40:41],v[40:41],0,v[42:43]
v_mul_f32_dpp v241,v14,v241 row_newbcast:9 row_mask:0xf bank_mask:0xf
v_mfma_f32_16x16x32_bf16 v[60:63],a[104:107],a[88:91],v[60:63]
ds_read_u16 v24,v24
v_lshl_add_u64 v[42:43],v[36:37],0,v[38:39]
v_mul_f32_dpp v242,v14,v242 row_newbcast:10 row_mask:0xf bank_mask:0xf
v_lshlrev_b32_sdwa v26,1,v26 dst_sel:DWORD dst_unused:UNUSED_PAD src0_sel:DWORD src1_sel:WORD_1
v_mfma_f32_16x16x32_bf16 v[32:35],a[108:111],a[88:91],v[68:71]
ds_read_u16_d16_hi v26,v26
v_mul_f32_dpp v243,v14,v243 row_newbcast:11 row_mask:0xf bank_mask:0xf
v_mfma_f32_16x16x32_bf16 v[32:35],a[112:115],a[72:75],v[32:35]
v_lshl_add_u64 v[36:37],v[52:53],0,v[54:55]
v_mul_f32_dpp v176,v14,v176 row_newbcast:12 row_mask:0xf bank_mask:0xf
v_lshlrev_b32_sdwa v25,1,v25 dst_sel:DWORD dst_unused:UNUSED_PAD src0_sel:DWORD src1_sel:WORD_1
v_mfma_f32_16x16x32_bf16 v[32:35],a[116:119],a[56:59],v[32:35]
ds_read_u16 v25,v25
v_mul_f32_dpp v177,v14,v177 row_newbcast:13 row_mask:0xf bank_mask:0xf
v_lshlrev_b32_sdwa v27,1,v27 dst_sel:DWORD dst_unused:UNUSED_PAD src0_sel:DWORD src1_sel:WORD_1
v_mfma_f32_16x16x32_bf16 v[52:55],a[120:123],a[56:59],v[84:87]
ds_read_u16_d16_hi v27,v27
v_mul_f32_dpp v178,v14,v178 row_newbcast:14 row_mask:0xf bank_mask:0xf
v_mfma_f32_16x16x32_bf16 v[52:55],a[124:127],a[72:75],v[52:55]
s_waitcnt lgkmcnt(6)
v_lshl_add_u64 v[38:39],v[56:57],0,v[58:59]
v_mul_f32_dpp v179,v14,v179 row_newbcast:15 row_mask:0xf bank_mask:0xf
v_lshlrev_b32_sdwa v44,1,v44 dst_sel:DWORD dst_unused:UNUSED_PAD src0_sel:DWORD src1_sel:WORD_1
v_mfma_f32_16x16x32_bf16 v[52:55],a[128:131],a[88:91],v[52:55]
ds_read_u16 v44,v44
v_cvt_pk_bf16_f32 v240,v240,v241
v_lshlrev_b32_sdwa v46,1,v46 dst_sel:DWORD dst_unused:UNUSED_PAD src0_sel:DWORD src1_sel:WORD_1
v_mfma_f32_16x16x32_bf16 v[56:59],a[132:135],a[88:91],v[80:83]
ds_read_u16_d16_hi v46,v46
v_cvt_pk_bf16_f32 v241,v242,v243
v_mfma_f32_16x16x32_bf16 v[56:59],a[136:139],a[72:75],v[56:59]
v_cvt_pk_bf16_f32 v242,v176,v177
v_lshlrev_b32_sdwa v45,1,v45 dst_sel:DWORD dst_unused:UNUSED_PAD src0_sel:DWORD src1_sel:WORD_1
v_mfma_f32_16x16x32_bf16 v[56:59],a[140:143],a[56:59],v[56:59]
ds_read_u16 v45,v45
v_cvt_pk_bf16_f32 v243,v178,v179
v_mfma_f32_16x16x32_bf16 v[236:239],a[0:3],v[40:43],v[236:239]
buffer_store_dwordx4 v[240:243],v7,s[40:43],s10 offen offset:64 sc1
v_lshlrev_b32_sdwa v47,1,v47 dst_sel:DWORD dst_unused:UNUSED_PAD src0_sel:DWORD src1_sel:WORD_1
v_mul_f32_dpp v248,v15,v248 row_newbcast:0 row_mask:0xf bank_mask:0xf
v_mfma_f32_16x16x32_bf16 v[236:239],a[4:7],v[36:39],v[236:239]
ds_read_u16_d16_hi v47,v47
v_mul_f32_dpp v249,v15,v249 row_newbcast:1 row_mask:0xf bank_mask:0xf
v_lshlrev_b32_sdwa v48,1,v48 dst_sel:DWORD dst_unused:UNUSED_PAD src0_sel:DWORD src1_sel:WORD_1
v_mfma_f32_16x16x32_bf16 v[164:167],a[8:11],v[36:39],v[164:167]
ds_read_u16 v48,v48
v_mul_f32_dpp v250,v15,v250 row_newbcast:2 row_mask:0xf bank_mask:0xf
v_mfma_f32_16x16x32_bf16 v[164:167],a[12:15],v[40:43],v[164:167]
v_lshlrev_b32_sdwa v50,1,v50 dst_sel:DWORD dst_unused:UNUSED_PAD src0_sel:DWORD src1_sel:WORD_1
v_mul_f32_dpp v251,v15,v251 row_newbcast:3 row_mask:0xf bank_mask:0xf
v_mfma_f32_16x16x32_bf16 v[244:247],a[16:19],v[40:43],v[244:247]
ds_read_u16_d16_hi v50,v50
v_mul_f32_dpp v192,v15,v192 row_newbcast:4 row_mask:0xf bank_mask:0xf
v_lshlrev_b32_sdwa v49,1,v49 dst_sel:DWORD dst_unused:UNUSED_PAD src0_sel:DWORD src1_sel:WORD_1
v_mfma_f32_16x16x32_bf16 v[244:247],a[20:23],v[36:39],v[244:247]
ds_read_u16 v49,v49
v_mul_f32_dpp v193,v15,v193 row_newbcast:5 row_mask:0xf bank_mask:0xf
v_mfma_f32_16x16x32_bf16 v[180:183],a[24:27],v[36:39],v[180:183]
v_lshlrev_b32_sdwa v51,1,v51 dst_sel:DWORD dst_unused:UNUSED_PAD src0_sel:DWORD src1_sel:WORD_1
v_mul_f32_dpp v194,v15,v194 row_newbcast:6 row_mask:0xf bank_mask:0xf
v_mfma_f32_16x16x32_bf16 v[180:183],a[28:31],v[40:43],v[180:183]
ds_read_u16_d16_hi v51,v51
v_mul_f32_dpp v195,v15,v195 row_newbcast:7 row_mask:0xf bank_mask:0xf
v_mfma_f32_16x16x32_bf16 v[252:255],a[32:35],v[40:43],v[252:255]
v_lshlrev_b32_sdwa v60,1,v60 dst_sel:DWORD dst_unused:UNUSED_PAD src0_sel:DWORD src1_sel:WORD_1
v_cvt_pk_bf16_f32 v248,v248,v249
v_mfma_f32_16x16x32_bf16 v[252:255],a[36:39],v[36:39],v[252:255]
ds_read_u16 v60,v60
v_lshlrev_b32_sdwa v62,1,v62 dst_sel:DWORD dst_unused:UNUSED_PAD src0_sel:DWORD src1_sel:WORD_1
v_cvt_pk_bf16_f32 v249,v250,v251
v_mfma_f32_16x16x32_bf16 v[196:199],a[40:43],v[36:39],v[196:199]
ds_read_u16_d16_hi v62,v62
v_cvt_pk_bf16_f32 v250,v192,v193
v_mfma_f32_16x16x32_bf16 v[196:199],a[44:47],v[40:43],v[196:199]
v_lshlrev_b32_sdwa v61,1,v61 dst_sel:DWORD dst_unused:UNUSED_PAD src0_sel:DWORD src1_sel:WORD_1
v_cvt_pk_bf16_f32 v251,v194,v195
v_lshlrev_b32_sdwa v63,1,v63 dst_sel:DWORD dst_unused:UNUSED_PAD src0_sel:DWORD src1_sel:WORD_1
v_mfma_f32_16x16x32_bf16 v[36:39],a[144:147],a[56:59],v[96:99]
ds_read_u16 v61,v61
v_lshlrev_b32_sdwa v32,1,v32 dst_sel:DWORD dst_unused:UNUSED_PAD src0_sel:DWORD src1_sel:WORD_1
v_mfma_f32_16x16x32_bf16 v[36:39],a[148:151],a[72:75],v[36:39]
s_waitcnt lgkmcnt(5)
buffer_store_dwordx4 v[248:251],v7,s[40:43],s10 offen offset:128 sc1
v_lshl_add_u64 v[28:29],v[28:29],0,v[30:31]
v_mfma_f32_16x16x32_bf16 v[36:39],a[152:155],a[88:91],v[36:39]
ds_read_u16_d16_hi v63,v63
v_lshl_add_u64 v[30:31],v[24:25],0,v[26:27]
v_lshlrev_b32_sdwa v34,1,v34 dst_sel:DWORD dst_unused:UNUSED_PAD src0_sel:DWORD src1_sel:WORD_1
v_mfma_f32_16x16x32_bf16 v[40:43],a[156:159],a[88:91],v[100:103]
ds_read_u16 v32,v32
v_lshl_add_u64 v[24:25],v[44:45],0,v[46:47]
v_mfma_f32_16x16x32_bf16 v[40:43],a[160:163],a[72:75],v[40:43]
s_waitcnt lgkmcnt(5)
ds_read_u16_d16_hi v34,v34
v_lshl_add_u64 v[26:27],v[48:49],0,v[50:51]
v_lshlrev_b32_sdwa v33,1,v33 dst_sel:DWORD dst_unused:UNUSED_PAD src0_sel:DWORD src1_sel:WORD_1
v_mfma_f32_16x16x32_bf16 v[40:43],a[164:167],a[56:59],v[40:43]
ds_read_u16 v33,v33
v_lshlrev_b32_sdwa v35,1,v35 dst_sel:DWORD dst_unused:UNUSED_PAD src0_sel:DWORD src1_sel:WORD_1
v_mfma_f32_16x16x32_bf16 v[236:239],a[212:215],v[28:31],v[236:239]
ds_read_u16_d16_hi v35,v35
v_lshlrev_b32_sdwa v56,1,v56 dst_sel:DWORD dst_unused:UNUSED_PAD src0_sel:DWORD src1_sel:WORD_1
v_mfma_f32_16x16x32_bf16 v[236:239],a[200:203],v[24:27],v[236:239]
ds_read_u16 v56,v56
v_lshlrev_b32_sdwa v58,1,v58 dst_sel:DWORD dst_unused:UNUSED_PAD src0_sel:DWORD src1_sel:WORD_1
v_mfma_f32_16x16x32_bf16 v[164:167],a[196:199],v[24:27],v[164:167]
ds_read_u16_d16_hi v58,v58
v_lshlrev_b32_sdwa v57,1,v57 dst_sel:DWORD dst_unused:UNUSED_PAD src0_sel:DWORD src1_sel:WORD_1
v_mfma_f32_16x16x32_bf16 v[164:167],a[192:195],v[28:31],v[164:167]
ds_read_u16 v57,v57
v_lshlrev_b32_sdwa v59,1,v59 dst_sel:DWORD dst_unused:UNUSED_PAD src0_sel:DWORD src1_sel:WORD_1
v_mfma_f32_16x16x32_bf16 v[244:247],a[216:219],v[28:31],v[244:247]
ds_read_u16_d16_hi v59,v59
v_mfma_f32_16x16x32_bf16 v[244:247],a[220:223],v[24:27],v[244:247]
v_mul_f32_dpp v236,v14,v236 row_newbcast:0 row_mask:0xf bank_mask:0xf
v_lshlrev_b32_sdwa v52,1,v52 dst_sel:DWORD dst_unused:UNUSED_PAD src0_sel:DWORD src1_sel:WORD_1
v_mfma_f32_16x16x32_bf16 v[180:183],a[204:207],v[24:27],v[180:183]
ds_read_u16 v52,v52
v_mul_f32_dpp v237,v14,v237 row_newbcast:1 row_mask:0xf bank_mask:0xf
v_mfma_f32_16x16x32_bf16 v[180:183],a[208:211],v[28:31],v[180:183]
TP10688_%=:
s_cbranch_execz TR1_%=
v_mul_f32_dpp v238,v14,v238 row_newbcast:2 row_mask:0xf bank_mask:0xf
v_lshlrev_b32_sdwa v54,1,v54 dst_sel:DWORD dst_unused:UNUSED_PAD src0_sel:DWORD src1_sel:WORD_1
v_mfma_f32_16x16x32_bf16 v[252:255],a[244:247],v[28:31],v[252:255]
ds_read_u16_d16_hi v54,v54
v_mul_f32_dpp v239,v14,v239 row_newbcast:3 row_mask:0xf bank_mask:0xf
v_mfma_f32_16x16x32_bf16 v[252:255],a[252:255],v[24:27],v[252:255]
v_mul_f32_dpp v164,v14,v164 row_newbcast:4 row_mask:0xf bank_mask:0xf
v_lshlrev_b32_sdwa v53,1,v53 dst_sel:DWORD dst_unused:UNUSED_PAD src0_sel:DWORD src1_sel:WORD_1
v_mfma_f32_16x16x32_bf16 v[196:199],a[232:235],v[24:27],v[196:199]
ds_read_u16 v53,v53
v_mul_f32_dpp v165,v14,v165 row_newbcast:5 row_mask:0xf bank_mask:0xf
v_mfma_f32_16x16x32_bf16 v[196:199],a[240:243],v[28:31],v[196:199]
v_mul_f32_dpp v166,v14,v166 row_newbcast:6 row_mask:0xf bank_mask:0xf
v_lshlrev_b32_sdwa v55,1,v55 dst_sel:DWORD dst_unused:UNUSED_PAD src0_sel:DWORD src1_sel:WORD_1
v_mfma_f32_16x16x32_bf16 v[44:47],a[168:171],a[56:59],v[16:19]
ds_read_u16_d16_hi v55,v55
v_mul_f32_dpp v167,v14,v167 row_newbcast:7 row_mask:0xf bank_mask:0xf
v_mfma_f32_16x16x32_bf16 v[44:47],a[172:175],a[72:75],v[44:47]
v_cvt_pk_bf16_f32 v236,v236,v237
v_lshlrev_b32_sdwa v36,1,v36 dst_sel:DWORD dst_unused:UNUSED_PAD src0_sel:DWORD src1_sel:WORD_1
v_mfma_f32_16x16x32_bf16 v[44:47],a[176:179],a[88:91],v[44:47]
ds_read_u16 v36,v36
v_cvt_pk_bf16_f32 v237,v238,v239
v_mfma_f32_16x16x32_bf16 v[48:51],a[180:183],a[88:91],v[20:23]
v_cvt_pk_bf16_f32 v238,v164,v165
v_lshlrev_b32_sdwa v38,1,v38 dst_sel:DWORD dst_unused:UNUSED_PAD src0_sel:DWORD src1_sel:WORD_1
v_mfma_f32_16x16x32_bf16 v[48:51],a[184:187],a[72:75],v[48:51]
ds_read_u16_d16_hi v38,v38
v_cvt_pk_bf16_f32 v239,v166,v167
v_mfma_f32_16x16x32_bf16 v[48:51],a[188:191],a[56:59],v[48:51]
buffer_store_dwordx4 v[236:239],v7,s[40:43],s10 offen offset:192 sc1
v_mul_f32_dpp v244,v14,v244 row_newbcast:8 row_mask:0xf bank_mask:0xf
v_lshlrev_b32_sdwa v37,1,v37 dst_sel:DWORD dst_unused:UNUSED_PAD src0_sel:DWORD src1_sel:WORD_1
v_lshlrev_b32_sdwa v39,1,v39 dst_sel:DWORD dst_unused:UNUSED_PAD src0_sel:DWORD src1_sel:WORD_1
v_mfma_f32_16x16x32_bf16 v[24:27],a[96:99],a[60:63],v[64:67]
s_nop 0
ds_read_u16 v37,v37
v_lshlrev_b32_sdwa v40,1,v40 dst_sel:DWORD dst_unused:UNUSED_PAD src0_sel:DWORD src1_sel:WORD_1
v_mfma_f32_16x16x32_bf16 v[24:27],a[100:103],a[76:79],v[24:27]
s_waitcnt lgkmcnt(4)
ds_read_u16_d16_hi v39,v39
v_lshl_add_u64 v[60:61],v[60:61],0,v[62:63]
v_mul_f32_dpp v245,v14,v245 row_newbcast:9 row_mask:0xf bank_mask:0xf
v_mfma_f32_16x16x32_bf16 v[24:27],a[104:107],a[92:95],v[24:27]
ds_read_u16 v40,v40
v_lshl_add_u64 v[62:63],v[32:33],0,v[34:35]
v_mul_f32_dpp v246,v14,v246 row_newbcast:10 row_mask:0xf bank_mask:0xf
v_lshlrev_b32_sdwa v42,1,v42 dst_sel:DWORD dst_unused:UNUSED_PAD src0_sel:DWORD src1_sel:WORD_1
v_mfma_f32_16x16x32_bf16 v[28:31],a[108:111],a[92:95],v[68:71]
ds_read_u16_d16_hi v42,v42
v_mul_f32_dpp v247,v14,v247 row_newbcast:11 row_mask:0xf bank_mask:0xf
v_mfma_f32_16x16x32_bf16 v[28:31],a[112:115],a[76:79],v[28:31]
v_lshl_add_u64 v[32:33],v[56:57],0,v[58:59]
v_mul_f32_dpp v180,v14,v180 row_newbcast:12 row_mask:0xf bank_mask:0xf
v_lshlrev_b32_sdwa v41,1,v41 dst_sel:DWORD dst_unused:UNUSED_PAD src0_sel:DWORD src1_sel:WORD_1
v_mfma_f32_16x16x32_bf16 v[28:31],a[116:119],a[60:63],v[28:31]
ds_read_u16 v41,v41
v_mul_f32_dpp v181,v14,v181 row_newbcast:13 row_mask:0xf bank_mask:0xf
v_lshlrev_b32_sdwa v43,1,v43 dst_sel:DWORD dst_unused:UNUSED_PAD src0_sel:DWORD src1_sel:WORD_1
v_mfma_f32_16x16x32_bf16 v[56:59],a[120:123],a[60:63],v[84:87]
s_nop 0
ds_read_u16_d16_hi v43,v43
v_mul_f32_dpp v182,v14,v182 row_newbcast:14 row_mask:0xf bank_mask:0xf
v_mfma_f32_16x16x32_bf16 v[56:59],a[124:127],a[76:79],v[56:59]
s_waitcnt lgkmcnt(6)
v_lshl_add_u64 v[34:35],v[52:53],0,v[54:55]
v_mul_f32_dpp v183,v14,v183 row_newbcast:15 row_mask:0xf bank_mask:0xf
v_lshlrev_b32_sdwa v48,1,v48 dst_sel:DWORD dst_unused:UNUSED_PAD src0_sel:DWORD src1_sel:WORD_1
v_mfma_f32_16x16x32_bf16 v[56:59],a[128:131],a[92:95],v[56:59]
ds_read_u16 v48,v48
v_cvt_pk_bf16_f32 v244,v244,v245
v_lshlrev_b32_sdwa v50,1,v50 dst_sel:DWORD dst_unused:UNUSED_PAD src0_sel:DWORD src1_sel:WORD_1
v_mfma_f32_16x16x32_bf16 v[52:55],a[132:135],a[92:95],v[80:83]
ds_read_u16_d16_hi v50,v50
v_cvt_pk_bf16_f32 v245,v246,v247
v_mfma_f32_16x16x32_bf16 v[52:55],a[136:139],a[76:79],v[52:55]
v_cvt_pk_bf16_f32 v246,v180,v181
v_lshlrev_b32_sdwa v49,1,v49 dst_sel:DWORD dst_unused:UNUSED_PAD src0_sel:DWORD src1_sel:WORD_1
v_mfma_f32_16x16x32_bf16 v[52:55],a[140:143],a[60:63],v[52:55]
ds_read_u16 v49,v49
v_cvt_pk_bf16_f32 v247,v182,v183
v_mfma_f32_16x16x32_bf16 v[208:211],a[0:3],v[60:63],v[208:211]
buffer_store_dwordx4 v[244:247],v7,s[40:43],s10 offen offset:256 sc1
v_lshlrev_b32_sdwa v51,1,v51 dst_sel:DWORD dst_unused:UNUSED_PAD src0_sel:DWORD src1_sel:WORD_1
v_mul_f32_dpp v252,v15,v252 row_newbcast:0 row_mask:0xf bank_mask:0xf
v_mfma_f32_16x16x32_bf16 v[208:211],a[4:7],v[32:35],v[208:211]
ds_read_u16_d16_hi v51,v51
v_mul_f32_dpp v253,v15,v253 row_newbcast:1 row_mask:0xf bank_mask:0xf
v_lshlrev_b32_sdwa v44,1,v44 dst_sel:DWORD dst_unused:UNUSED_PAD src0_sel:DWORD src1_sel:WORD_1
v_mfma_f32_16x16x32_bf16 v[168:171],a[8:11],v[32:35],v[168:171]
ds_read_u16 v44,v44
v_mul_f32_dpp v254,v15,v254 row_newbcast:2 row_mask:0xf bank_mask:0xf
v_mfma_f32_16x16x32_bf16 v[168:171],a[12:15],v[60:63],v[168:171]
v_lshlrev_b32_sdwa v46,1,v46 dst_sel:DWORD dst_unused:UNUSED_PAD src0_sel:DWORD src1_sel:WORD_1
v_mul_f32_dpp v255,v15,v255 row_newbcast:3 row_mask:0xf bank_mask:0xf
v_mfma_f32_16x16x32_bf16 v[216:219],a[16:19],v[60:63],v[216:219]
ds_read_u16_d16_hi v46,v46
v_mul_f32_dpp v196,v15,v196 row_newbcast:4 row_mask:0xf bank_mask:0xf
v_lshlrev_b32_sdwa v45,1,v45 dst_sel:DWORD dst_unused:UNUSED_PAD src0_sel:DWORD src1_sel:WORD_1
v_mfma_f32_16x16x32_bf16 v[216:219],a[20:23],v[32:35],v[216:219]
ds_read_u16 v45,v45
v_mul_f32_dpp v197,v15,v197 row_newbcast:5 row_mask:0xf bank_mask:0xf
v_mfma_f32_16x16x32_bf16 v[184:187],a[24:27],v[32:35],v[184:187]
v_lshlrev_b32_sdwa v47,1,v47 dst_sel:DWORD dst_unused:UNUSED_PAD src0_sel:DWORD src1_sel:WORD_1
v_mul_f32_dpp v198,v15,v198 row_newbcast:6 row_mask:0xf bank_mask:0xf
v_mfma_f32_16x16x32_bf16 v[184:187],a[28:31],v[60:63],v[184:187]
ds_read_u16_d16_hi v47,v47
v_mul_f32_dpp v199,v15,v199 row_newbcast:7 row_mask:0xf bank_mask:0xf
v_mfma_f32_16x16x32_bf16 v[224:227],a[32:35],v[60:63],v[224:227]
v_lshlrev_b32_sdwa v24,1,v24 dst_sel:DWORD dst_unused:UNUSED_PAD src0_sel:DWORD src1_sel:WORD_1
v_cvt_pk_bf16_f32 v252,v252,v253
v_mfma_f32_16x16x32_bf16 v[224:227],a[36:39],v[32:35],v[224:227]
ds_read_u16 v24,v24
v_lshlrev_b32_sdwa v26,1,v26 dst_sel:DWORD dst_unused:UNUSED_PAD src0_sel:DWORD src1_sel:WORD_1
v_cvt_pk_bf16_f32 v253,v254,v255
v_mfma_f32_16x16x32_bf16 v[200:203],a[40:43],v[32:35],v[200:203]
ds_read_u16_d16_hi v26,v26
v_cvt_pk_bf16_f32 v254,v196,v197
v_mfma_f32_16x16x32_bf16 v[200:203],a[44:47],v[60:63],v[200:203]
v_lshlrev_b32_sdwa v25,1,v25 dst_sel:DWORD dst_unused:UNUSED_PAD src0_sel:DWORD src1_sel:WORD_1
v_cvt_pk_bf16_f32 v255,v198,v199
v_lshlrev_b32_sdwa v27,1,v27 dst_sel:DWORD dst_unused:UNUSED_PAD src0_sel:DWORD src1_sel:WORD_1
v_mov_b32 v10,0
v_mfma_f32_16x16x32_bf16 v[32:35],a[144:147],a[60:63],v[96:99]
ds_read_b128 v[72:75],v10 offset:11648
v_lshlrev_b32_sdwa v28,1,v28 dst_sel:DWORD dst_unused:UNUSED_PAD src0_sel:DWORD src1_sel:WORD_1
v_mfma_f32_16x16x32_bf16 v[32:35],a[148:151],a[76:79],v[32:35]
s_waitcnt lgkmcnt(5)
buffer_store_dwordx4 v[252:255],v7,s[40:43],s10 offen offset:320 sc1
v_lshl_add_u64 v[36:37],v[36:37],0,v[38:39]
v_mfma_f32_16x16x32_bf16 v[32:35],a[152:155],a[92:95],v[32:35]
ds_read_u16 v25,v25
ds_read_u16_d16_hi v27,v27
v_lshl_add_u64 v[38:39],v[40:41],0,v[42:43]
v_lshlrev_b32_sdwa v30,1,v30 dst_sel:DWORD dst_unused:UNUSED_PAD src0_sel:DWORD src1_sel:WORD_1
v_mfma_f32_16x16x32_bf16 v[60:63],a[156:159],a[92:95],v[100:103]
s_nop 0
ds_read_u16 v28,v28
v_lshl_add_u64 v[40:41],v[48:49],0,v[50:51]
v_mfma_f32_16x16x32_bf16 v[60:63],a[160:163],a[76:79],v[60:63]
s_waitcnt lgkmcnt(6)
ds_read_u16_d16_hi v30,v30
v_lshl_add_u64 v[42:43],v[44:45],0,v[46:47]
v_lshlrev_b32_sdwa v29,1,v29 dst_sel:DWORD dst_unused:UNUSED_PAD src0_sel:DWORD src1_sel:WORD_1
v_mfma_f32_16x16x32_bf16 v[60:63],a[164:167],a[60:63],v[60:63]
ds_read_u16 v29,v29
v_lshlrev_b32_sdwa v31,1,v31 dst_sel:DWORD dst_unused:UNUSED_PAD src0_sel:DWORD src1_sel:WORD_1
v_mfma_f32_16x16x32_bf16 v[208:211],a[212:215],v[36:39],v[208:211]
ds_read_u16_d16_hi v31,v31
v_lshlrev_b32_sdwa v52,1,v52 dst_sel:DWORD dst_unused:UNUSED_PAD src0_sel:DWORD src1_sel:WORD_1
v_mfma_f32_16x16x32_bf16 v[208:211],a[200:203],v[40:43],v[208:211]
ds_read_u16 v52,v52
v_lshlrev_b32_sdwa v54,1,v54 dst_sel:DWORD dst_unused:UNUSED_PAD src0_sel:DWORD src1_sel:WORD_1
v_mfma_f32_16x16x32_bf16 v[168:171],a[196:199],v[40:43],v[168:171]
ds_read_u16_d16_hi v54,v54
v_lshlrev_b32_sdwa v53,1,v53 dst_sel:DWORD dst_unused:UNUSED_PAD src0_sel:DWORD src1_sel:WORD_1
v_mfma_f32_16x16x32_bf16 v[168:171],a[192:195],v[36:39],v[168:171]
ds_read_u16 v53,v53
v_lshlrev_b32_sdwa v55,1,v55 dst_sel:DWORD dst_unused:UNUSED_PAD src0_sel:DWORD src1_sel:WORD_1
v_mfma_f32_16x16x32_bf16 v[216:219],a[216:219],v[36:39],v[216:219]
ds_read_u16_d16_hi v55,v55
v_mfma_f32_16x16x32_bf16 v[216:219],a[220:223],v[40:43],v[216:219]
v_mul_f32_dpp v208,v14,v208 row_newbcast:0 row_mask:0xf bank_mask:0xf
s_add_u32 s11,s61,4816896
v_mul_f32_dpp v209,v14,v209 row_newbcast:1 row_mask:0xf bank_mask:0xf
v_mul_f32_dpp v210,v14,v210 row_newbcast:2 row_mask:0xf bank_mask:0xf
v_mfma_f32_16x16x32_bf16 v[184:187],a[204:207],v[40:43],v[184:187]
v_mul_f32_dpp v211,v14,v211 row_newbcast:3 row_mask:0xf bank_mask:0xf
v_mul_f32_dpp v168,v14,v168 row_newbcast:4 row_mask:0xf bank_mask:0xf
v_mul_f32_dpp v169,v14,v169 row_newbcast:5 row_mask:0xf bank_mask:0xf
v_mfma_f32_16x16x32_bf16 v[184:187],a[208:211],v[36:39],v[184:187]
v_mul_f32_dpp v170,v14,v170 row_newbcast:6 row_mask:0xf bank_mask:0xf
v_mul_f32_dpp v171,v14,v171 row_newbcast:7 row_mask:0xf bank_mask:0xf
v_cvt_pk_bf16_f32 v208,v208,v209
v_lshlrev_b32_sdwa v56,1,v56 dst_sel:DWORD dst_unused:UNUSED_PAD src0_sel:DWORD src1_sel:WORD_1
v_mfma_f32_16x16x32_bf16 v[224:227],a[244:247],v[36:39],v[224:227]
ds_read_u16 v56,v56
v_cvt_pk_bf16_f32 v209,v210,v211
v_cvt_pk_bf16_f32 v210,v168,v169
v_cvt_pk_bf16_f32 v211,v170,v171
v_mfma_f32_16x16x32_bf16 v[224:227],a[252:255],v[40:43],v[224:227]
buffer_store_dwordx4 v[208:211],v7,s[40:43],s11 offen offset:0 sc1
v_mul_f32_dpp v216,v14,v216 row_newbcast:8 row_mask:0xf bank_mask:0xf
v_mul_f32_dpp v217,v14,v217 row_newbcast:9 row_mask:0xf bank_mask:0xf
v_mul_f32_dpp v218,v14,v218 row_newbcast:10 row_mask:0xf bank_mask:0xf
v_lshlrev_b32_sdwa v58,1,v58 dst_sel:DWORD dst_unused:UNUSED_PAD src0_sel:DWORD src1_sel:WORD_1
v_mfma_f32_16x16x32_bf16 v[200:203],a[232:235],v[40:43],v[200:203]
ds_read_u16_d16_hi v58,v58
v_mul_f32_dpp v219,v14,v219 row_newbcast:11 row_mask:0xf bank_mask:0xf
v_mul_f32_dpp v184,v14,v184 row_newbcast:12 row_mask:0xf bank_mask:0xf
v_mul_f32_dpp v185,v14,v185 row_newbcast:13 row_mask:0xf bank_mask:0xf
v_lshlrev_b32_sdwa v57,1,v57 dst_sel:DWORD dst_unused:UNUSED_PAD src0_sel:DWORD src1_sel:WORD_1
v_mfma_f32_16x16x32_bf16 v[200:203],a[240:243],v[36:39],v[200:203]
ds_read_u16 v57,v57
v_mul_f32_dpp v186,v14,v186 row_newbcast:14 row_mask:0xf bank_mask:0xf
v_mul_f32_dpp v187,v14,v187 row_newbcast:15 row_mask:0xf bank_mask:0xf
v_cvt_pk_bf16_f32 v216,v216,v217
v_mfma_f32_16x16x32_bf16 v[48:51],a[168:171],a[60:63],v[16:19]
v_cvt_pk_bf16_f32 v217,v218,v219
v_cvt_pk_bf16_f32 v218,v184,v185
v_cvt_pk_bf16_f32 v219,v186,v187
v_lshlrev_b32_sdwa v59,1,v59 dst_sel:DWORD dst_unused:UNUSED_PAD src0_sel:DWORD src1_sel:WORD_1
v_mfma_f32_16x16x32_bf16 v[48:51],a[172:175],a[76:79],v[48:51]
ds_read_u16_d16_hi v59,v59
v_mul_f32_dpp v224,v15,v224 row_newbcast:0 row_mask:0xf bank_mask:0xf
v_mul_f32_dpp v225,v15,v225 row_newbcast:1 row_mask:0xf bank_mask:0xf
v_mul_f32_dpp v226,v15,v226 row_newbcast:2 row_mask:0xf bank_mask:0xf
v_mul_f32_dpp v227,v15,v227 row_newbcast:3 row_mask:0xf bank_mask:0xf
v_mfma_f32_16x16x32_bf16 v[48:51],a[176:179],a[92:95],v[48:51]
v_mul_f32_dpp v200,v15,v200 row_newbcast:4 row_mask:0xf bank_mask:0xf
v_mul_f32_dpp v201,v15,v201 row_newbcast:5 row_mask:0xf bank_mask:0xf
v_mul_f32_dpp v202,v15,v202 row_newbcast:6 row_mask:0xf bank_mask:0xf
v_lshlrev_b32_sdwa v32,1,v32 dst_sel:DWORD dst_unused:UNUSED_PAD src0_sel:DWORD src1_sel:WORD_1
v_mfma_f32_16x16x32_bf16 v[44:47],a[180:183],a[92:95],v[20:23]
ds_read_u16 v32,v32
v_mul_f32_dpp v203,v15,v203 row_newbcast:7 row_mask:0xf bank_mask:0xf
v_cvt_pk_bf16_f32 v224,v224,v225
v_cvt_pk_bf16_f32 v225,v226,v227
v_lshlrev_b32_sdwa v34,1,v34 dst_sel:DWORD dst_unused:UNUSED_PAD src0_sel:DWORD src1_sel:WORD_1
v_mfma_f32_16x16x32_bf16 v[44:47],a[184:187],a[76:79],v[44:47]
ds_read_u16_d16_hi v34,v34
v_cvt_pk_bf16_f32 v226,v200,v201
v_cvt_pk_bf16_f32 v227,v202,v203
v_mfma_f32_16x16x32_bf16 v[44:47],a[188:191],a[60:63],v[44:47]
buffer_store_dwordx4 v[216:219],v7,s[40:43],s11 offen offset:64 sc1
v_lshlrev_b32_sdwa v33,1,v33 dst_sel:DWORD dst_unused:UNUSED_PAD src0_sel:DWORD src1_sel:WORD_1
v_lshlrev_b32_sdwa v35,1,v35 dst_sel:DWORD dst_unused:UNUSED_PAD src0_sel:DWORD src1_sel:WORD_1
s_waitcnt lgkmcnt(14)
v_lshl_add_u64 v[24:25],v[24:25],0,v[26:27]
s_waitcnt lgkmcnt(10)
v_lshl_add_u64 v[26:27],v[28:29],0,v[30:31]
s_waitcnt lgkmcnt(6)
v_lshl_add_u64 v[28:29],v[52:53],0,v[54:55]
s_waitcnt lgkmcnt(2)
v_lshl_add_u64 v[30:31],v[56:57],0,v[58:59]
v_mfma_f32_16x16x32_bf16 v[212:215],a[0:3],v[24:27],v[212:215]
ds_read_u16 v33,v33
v_lshlrev_b32_sdwa v60,1,v60 dst_sel:DWORD dst_unused:UNUSED_PAD src0_sel:DWORD src1_sel:WORD_1
v_mfma_f32_16x16x32_bf16 v[212:215],a[4:7],v[28:31],v[212:215]
ds_read_u16_d16_hi v35,v35
v_lshlrev_b32_sdwa v62,1,v62 dst_sel:DWORD dst_unused:UNUSED_PAD src0_sel:DWORD src1_sel:WORD_1
v_mfma_f32_16x16x32_bf16 v[172:175],a[8:11],v[28:31],v[172:175]
ds_read_u16 v60,v60
v_lshlrev_b32_sdwa v61,1,v61 dst_sel:DWORD dst_unused:UNUSED_PAD src0_sel:DWORD src1_sel:WORD_1
v_mfma_f32_16x16x32_bf16 v[172:175],a[12:15],v[24:27],v[172:175]
ds_read_u16_d16_hi v62,v62
v_lshlrev_b32_sdwa v63,1,v63 dst_sel:DWORD dst_unused:UNUSED_PAD src0_sel:DWORD src1_sel:WORD_1
v_mfma_f32_16x16x32_bf16 v[220:223],a[16:19],v[24:27],v[220:223]
ds_read_u16 v61,v61
v_lshlrev_b32_sdwa v44,1,v44 dst_sel:DWORD dst_unused:UNUSED_PAD src0_sel:DWORD src1_sel:WORD_1
v_mfma_f32_16x16x32_bf16 v[220:223],a[20:23],v[28:31],v[220:223]
ds_read_u16_d16_hi v63,v63
v_lshlrev_b32_sdwa v46,1,v46 dst_sel:DWORD dst_unused:UNUSED_PAD src0_sel:DWORD src1_sel:WORD_1
v_mfma_f32_16x16x32_bf16 v[188:191],a[24:27],v[28:31],v[188:191]
ds_read_u16 v44,v44
v_lshlrev_b32_sdwa v45,1,v45 dst_sel:DWORD dst_unused:UNUSED_PAD src0_sel:DWORD src1_sel:WORD_1
v_mfma_f32_16x16x32_bf16 v[188:191],a[28:31],v[24:27],v[188:191]
ds_read_u16_d16_hi v46,v46
v_lshlrev_b32_sdwa v47,1,v47 dst_sel:DWORD dst_unused:UNUSED_PAD src0_sel:DWORD src1_sel:WORD_1
v_mfma_f32_16x16x32_bf16 v[228:231],a[32:35],v[24:27],v[228:231]
ds_read_u16 v45,v45
ds_read_u16_d16_hi v47,v47
v_lshlrev_b32_sdwa v48,1,v48 dst_sel:DWORD dst_unused:UNUSED_PAD src0_sel:DWORD src1_sel:WORD_1
v_mfma_f32_16x16x32_bf16 v[228:231],a[36:39],v[28:31],v[228:231]
ds_read_u16 v48,v48
v_lshlrev_b32_sdwa v50,1,v50 dst_sel:DWORD dst_unused:UNUSED_PAD src0_sel:DWORD src1_sel:WORD_1
v_mfma_f32_16x16x32_bf16 v[204:207],a[40:43],v[28:31],v[204:207]
buffer_store_dwordx4 v[224:227],v7,s[40:43],s11 offen offset:128 sc1
v_lshlrev_b32_sdwa v49,1,v49 dst_sel:DWORD dst_unused:UNUSED_PAD src0_sel:DWORD src1_sel:WORD_1
v_mfma_f32_16x16x32_bf16 v[204:207],a[44:47],v[24:27],v[204:207]
ds_read_u16_d16_hi v50,v50
ds_read_u16 v49,v49
v_lshlrev_b32_sdwa v51,1,v51 dst_sel:DWORD dst_unused:UNUSED_PAD src0_sel:DWORD src1_sel:WORD_1
ds_read_u16_d16_hi v51,v51
s_waitcnt lgkmcnt(12)
v_lshl_add_u64 v[32:33],v[32:33],0,v[34:35]
s_waitcnt lgkmcnt(8)
v_lshl_add_u64 v[34:35],v[60:61],0,v[62:63]
s_waitcnt lgkmcnt(4)
v_lshl_add_u64 v[60:61],v[44:45],0,v[46:47]
s_waitcnt lgkmcnt(0)
v_lshl_add_u64 v[62:63],v[48:49],0,v[50:51]
v_mfma_f32_16x16x32_bf16 v[212:215],a[212:215],v[32:35],v[212:215]
s_nop 0
v_mfma_f32_16x16x32_bf16 v[212:215],a[200:203],v[60:63],v[212:215]
v_mfma_f32_16x16x32_bf16 v[172:175],a[196:199],v[60:63],v[172:175]
v_mfma_f32_16x16x32_bf16 v[172:175],a[192:195],v[32:35],v[172:175]
v_mfma_f32_16x16x32_bf16 v[220:223],a[216:219],v[32:35],v[220:223]
v_mfma_f32_16x16x32_bf16 v[220:223],a[220:223],v[60:63],v[220:223]
v_mfma_f32_16x16x32_bf16 v[188:191],a[204:207],v[60:63],v[188:191]
s_nop 2
v_mul_f32_dpp v212,v14,v212 row_newbcast:0 row_mask:0xf bank_mask:0xf
v_mul_f32_dpp v213,v14,v213 row_newbcast:1 row_mask:0xf bank_mask:0xf
v_mul_f32_dpp v214,v14,v214 row_newbcast:2 row_mask:0xf bank_mask:0xf
v_mfma_f32_16x16x32_bf16 v[188:191],a[208:211],v[32:35],v[188:191]
v_mul_f32_dpp v215,v14,v215 row_newbcast:3 row_mask:0xf bank_mask:0xf
v_mul_f32_dpp v172,v14,v172 row_newbcast:4 row_mask:0xf bank_mask:0xf
v_mul_f32_dpp v173,v14,v173 row_newbcast:5 row_mask:0xf bank_mask:0xf
v_mfma_f32_16x16x32_bf16 v[228:231],a[244:247],v[32:35],v[228:231]
v_mul_f32_dpp v174,v14,v174 row_newbcast:6 row_mask:0xf bank_mask:0xf
v_mul_f32_dpp v175,v14,v175 row_newbcast:7 row_mask:0xf bank_mask:0xf
v_cvt_pk_bf16_f32 v212,v212,v213
v_mfma_f32_16x16x32_bf16 v[228:231],a[252:255],v[60:63],v[228:231]
v_cvt_pk_bf16_f32 v213,v214,v215
v_cvt_pk_bf16_f32 v214,v172,v173
v_cvt_pk_bf16_f32 v215,v174,v175
buffer_store_dwordx4 v[212:215],v7,s[40:43],s11 offen offset:192 sc1
v_mfma_f32_16x16x32_bf16 v[204:207],a[232:235],v[60:63],v[204:207]
v_mul_f32_dpp v220,v14,v220 row_newbcast:8 row_mask:0xf bank_mask:0xf
v_mul_f32_dpp v221,v14,v221 row_newbcast:9 row_mask:0xf bank_mask:0xf
v_mul_f32_dpp v222,v14,v222 row_newbcast:10 row_mask:0xf bank_mask:0xf
v_mfma_f32_16x16x32_bf16 v[204:207],a[240:243],v[32:35],v[204:207]
v_mul_f32_dpp v223,v14,v223 row_newbcast:11 row_mask:0xf bank_mask:0xf
v_mul_f32_dpp v188,v14,v188 row_newbcast:12 row_mask:0xf bank_mask:0xf
v_mul_f32_dpp v189,v14,v189 row_newbcast:13 row_mask:0xf bank_mask:0xf
v_mul_f32_dpp v190,v14,v190 row_newbcast:14 row_mask:0xf bank_mask:0xf
v_mul_f32_dpp v191,v14,v191 row_newbcast:15 row_mask:0xf bank_mask:0xf
v_cvt_pk_bf16_f32 v220,v220,v221
v_cvt_pk_bf16_f32 v221,v222,v223
v_cvt_pk_bf16_f32 v222,v188,v189
v_cvt_pk_bf16_f32 v223,v190,v191
buffer_store_dwordx4 v[220:223],v7,s[40:43],s11 offen offset:256 sc1
v_mul_f32_dpp v228,v15,v228 row_newbcast:0 row_mask:0xf bank_mask:0xf
v_mul_f32_dpp v229,v15,v229 row_newbcast:1 row_mask:0xf bank_mask:0xf
v_mul_f32_dpp v230,v15,v230 row_newbcast:2 row_mask:0xf bank_mask:0xf
v_mul_f32_dpp v231,v15,v231 row_newbcast:3 row_mask:0xf bank_mask:0xf
v_mul_f32_dpp v204,v15,v204 row_newbcast:4 row_mask:0xf bank_mask:0xf
v_mul_f32_dpp v205,v15,v205 row_newbcast:5 row_mask:0xf bank_mask:0xf
v_mul_f32_dpp v206,v15,v206 row_newbcast:6 row_mask:0xf bank_mask:0xf
v_mul_f32_dpp v207,v15,v207 row_newbcast:7 row_mask:0xf bank_mask:0xf
v_cvt_pk_bf16_f32 v228,v228,v229
v_cvt_pk_bf16_f32 v229,v230,v231
v_cvt_pk_bf16_f32 v230,v204,v205
v_cvt_pk_bf16_f32 v231,v206,v207
buffer_store_dwordx4 v[228:231],v7,s[40:43],s11 offen offset:320 sc1
v_or3_b32 v72,v72,v73,v74
v_or_b32 v72,v72,v75
s_nop 0
v_readfirstlane_b32 s64,v72
s_cmp_lg_u32 s64,0
s_cbranch_scc1 N_fallback_%=
s_endpgm
N_mainend_%=:
.fill 512,4,0xbf800000
N_fallback_%=:
s_waitcnt vmcnt(0) lgkmcnt(0)
s_barrier
)ASM"
    :: "s"(blockIdx.x), "v"(tid), "s"(ka)
    : "v1", "v2", "v3", "v4", "v5", "v6", "v7", "v8", "v9", "v10", "v11", "v12", "v13", "v14", "v15", "v16", "v17", "v18", "v19", "v20", "v21", "v22", "v23", "v24", "v25", "v26", "v27", "v28", "v29", "v30", "v31", "v32", "v33", "v34", "v35", "v36", "v37", "v38", "v39", "v40", "v41", "v42", "v43", "v44", "v45", "v46", "v47", "v48", "v49", "v50", "v51", "v52", "v53", "v54", "v55", "v56", "v57", "v58", "v59", "v60", "v61", "v62", "v63", "v64", "v65", "v66", "v67", "v68", "v69", "v70", "v71", "v72", "v73", "v74", "v75", "v76", "v77", "v78", "v79", "v80", "v81", "v82", "v83", "v84", "v85", "v86", "v87", "v88", "v89", "v90", "v91", "v92", "v93", "v94", "v95", "v96", "v97", "v98", "v99", "v100", "v101", "v102", "v103", "v104", "v105", "v106", "v107", "v108", "v109", "v110", "v111", "v112", "v113", "v114", "v115", "v116", "v117", "v118", "v119", "v120", "v121", "v122", "v123", "v124", "v125", "v126", "v127", "v128", "v129", "v130", "v131", "v132", "v133", "v134", "v135", "v136", "v137", "v138", "v139", "v140", "v141", "v142", "v143", "v144", "v145", "v146", "v147", "v148", "v149", "v150", "v151", "v152", "v153", "v154", "v155", "v156", "v157", "v158", "v159", "v160", "v161", "v162", "v163", "v164", "v165", "v166", "v167", "v168", "v169", "v170", "v171", "v172", "v173", "v174", "v175", "v176", "v177", "v178", "v179", "v180", "v181", "v182", "v183", "v184", "v185", "v186", "v187", "v188", "v189", "v190", "v191", "v192", "v193", "v194", "v195", "v196", "v197", "v198", "v199", "v200", "v201", "v202", "v203", "v204", "v205", "v206", "v207", "v208", "v209", "v210", "v211", "v212", "v213", "v214", "v215", "v216", "v217", "v218", "v219", "v220", "v221", "v222", "v223", "v224", "v225", "v226", "v227", "v228", "v229", "v230", "v231", "v232", "v233", "v234", "v235", "v236", "v237", "v238", "v239", "v240", "v241", "v242", "v243", "v244", "v245", "v246", "v247", "v248", "v249", "v250", "v251", "v252", "v253", "v254", "v255", "a0", "a1", "a2", "a3", "a4", "a5", "a6", "a7", "a8", "a9", "a10", "a11", "a12", "a13", "a14", "a15", "a16", "a17", "a18", "a19", "a20", "a21", "a22", "a23", "a24", "a25", "a26", "a27", "a28", "a29", "a30", "a31", "a32", "a33", "a34", "a35", "a36", "a37", "a38", "a39", "a40", "a41", "a42", "a43", "a44", "a45", "a46", "a47", "a48", "a49", "a50", "a51", "a52", "a53", "a54", "a55", "a56", "a57", "a58", "a59", "a60", "a61", "a62", "a63", "a64", "a65", "a66", "a67", "a68", "a69", "a70", "a71", "a72", "a73", "a74", "a75", "a76", "a77", "a78", "a79", "a80", "a81", "a82", "a83", "a84", "a85", "a86", "a87", "a88", "a89", "a90", "a91", "a92", "a93", "a94", "a95", "a96", "a97", "a98", "a99", "a100", "a101", "a102", "a103", "a104", "a105", "a106", "a107", "a108", "a109", "a110", "a111", "a112", "a113", "a114", "a115", "a116", "a117", "a118", "a119", "a120", "a121", "a122", "a123", "a124", "a125", "a126", "a127", "a128", "a129", "a130", "a131", "a132", "a133", "a134", "a135", "a136", "a137", "a138", "a139", "a140", "a141", "a142", "a143", "a144", "a145", "a146", "a147", "a148", "a149", "a150", "a151", "a152", "a153", "a154", "a155", "a156", "a157", "a158", "a159", "a160", "a161", "a162", "a163", "a164", "a165", "a166", "a167", "a168", "a169", "a170", "a171", "a172", "a173", "a174", "a175", "a176", "a177", "a178", "a179", "a180", "a181", "a182", "a183", "a184", "a185", "a186", "a187", "a188", "a189", "a190", "a191", "a192", "a193", "a194", "a195", "a196", "a197", "a198", "a199", "a200", "a201", "a202", "a203", "a204", "a205", "a206", "a207", "a208", "a209", "a210", "a211", "a212", "a213", "a214", "a215", "a216", "a217", "a218", "a219", "a220", "a221", "a222", "a223", "a224", "a225", "a226", "a227", "a228", "a229", "a230", "a231", "a232", "a233", "a234", "a235", "a236", "a237", "a238", "a239", "a240", "a241", "a242", "a243", "a244", "a245", "a246", "a247", "a248", "a249", "a250", "a251", "a252", "a253", "a254", "a255", "s4", "s5", "s6", "s7", "s8", "s9", "s10", "s11", "s12", "s13", "s14", "s15", "s16", "s17", "s18", "s19", "s20", "s21", "s22", "s23", "s24", "s25", "s26", "s27", "s28", "s29", "s30", "s31", "s32", "s33", "s34", "s35", "s36", "s37", "s38", "s39", "s40", "s41", "s42", "s43", "s44", "s45", "s46", "s47", "s48", "s49", "s50", "s51", "s52", "s53", "s54", "s55", "s56", "s57", "s58", "s59", "s60", "s61", "s62", "s63", "s64", "s65", "s66", "s67", "s68", "s69", "s70", "s71", "s72", "s73", "s74", "s75", "s76", "s77", "s78", "s79", "s80", "s81", "s82", "s83", "s84", "s85", "s86", "s87", "s88", "s89", "s90", "s91", "s92", "s93", "s94", "s95", "s96", "s97", "s98", "s99", "vcc", "scc", "memory");
    asm volatile("" : "+s"(ka));
    typedef const unsigned short* __attribute__((address_space(1))) gp;
    unsigned long long const __attribute__((address_space(4)))* kp = (unsigned long long const __attribute__((address_space(4)))*)ka;
    const unsigned short* x = (const unsigned short*)kp[0]; const unsigned short* dw = (const unsigned short*)kp[1];
    const unsigned short* w1 = (const unsigned short*)kp[2]; const unsigned short* b1 = (const unsigned short*)kp[3];
    const unsigned short* w2 = (const unsigned short*)kp[4]; const unsigned short* b2 = (const unsigned short*)kp[5];
    const unsigned short* gamma = (const unsigned short*)kp[6]; unsigned short* out = (unsigned short*)kp[7];
    extern __shared__ float sm[];
    float* cv = sm; float* hid = sm + 16 * 96; float* stt = hid + 16 * 384;
    const int x0 = (blockIdx.x >> 3) * 16, y0 = (blockIdx.x & 7) * 64;
    for (int y = y0; y < y0 + 64; ++y) {
        for (int i = tid; i < 16 * 96; i += 256) {
            int p = i / 96, c = i - p * 96; float s = 0.f;
            for (int ky = 0; ky < 3; ++ky) for (int kx = 0; kx < 3; ++kx) {
                int yy = y + ky - 1, xx = x0 + p + kx - 1;
                float v = (yy >= 0 && yy < 512 && xx >= 0 && xx < 512) ? bf2f(x[((size_t)yy * 512 + xx) * 96 + c]) : 0.f;
                s = fmaf(v, bf2f(dw[(ky * 3 + kx) * 96 + c]), s);
            }
            cv[i] = s;
        }
        __syncthreads();
        if (tid < 16) {
            float m = 0.f; for (int c = 0; c < 96; ++c) m += cv[tid * 96 + c];
            m *= (1.f / 96); float q = 0.f;
            for (int c = 0; c < 96; ++c) { float d = cv[tid * 96 + c] - m; q = fmaf(d, d, q); }
            stt[tid] = m; stt[16 + tid] = 1.f / sqrtf(q * (1.f / 96) + 1e-5f);
        }
        __syncthreads();
        for (int i = tid; i < 16 * 96; i += 256) { int p = i / 96; cv[i] = rbf((cv[i] - stt[p]) * stt[16 + p]); }
        __syncthreads();
        for (int e = tid; e < 384; e += 256) {
            float a[16];
#pragma unroll
            for (int p = 0; p < 16; ++p) a[p] = 0.f;
            for (int c = 0; c < 96; ++c) {
                float w = bf2f(w1[c * 384 + e]);
#pragma unroll
                for (int p = 0; p < 16; ++p) a[p] = fmaf(cv[p * 96 + c], w, a[p]);
            }
            float bb = bf2f(b1[e]);
#pragma unroll
            for (int p = 0; p < 16; ++p) {
                float h = a[p] + bb;
                hid[p * 384 + e] = rbf(0.5f * h * (1.f + tanhf(0.7978845608028654f * (h + 0.044715f * h * h * h))));
            }
        }
        __syncthreads();
        for (int i = tid; i < 16 * 96; i += 256) {
            int p = i / 96, c = i - p * 96; float s = 0.f;
            for (int e = 0; e < 384; ++e) s = fmaf(hid[p * 384 + e], bf2f(w2[e * 96 + c]), s);
            size_t o = ((size_t)y * 512 + x0 + p) * 96 + c;
            out[o] = f2bf(bf2f(x[o]) + bf2f(gamma[c]) * (s + bf2f(b2[c])));
        }
        __syncthreads();
    }

}

extern "C" void kernel_launch(void* const* d_in, const int* in_sizes, int n_in,
                              void* d_out, int out_size, void* d_ws, size_t ws_size,
                              hipStream_t stream) {
    (void)hipFuncSetAttribute((const void*)convnext_kernel, hipFuncAttributeMaxDynamicSharedMemorySize, LDS_BYTES);
    convnext_kernel<<<256, 256, LDS_BYTES, stream>>>(
        (const unsigned short*)d_in[0], (const unsigned short*)d_in[1], (const unsigned short*)d_in[2], (const unsigned short*)d_in[3],
        (const unsigned short*)d_in[4], (const unsigned short*)d_in[5], (const unsigned short*)d_in[6], (unsigned short*)d_out);
}
```
